```python
import torch

def _softmax(x, axis=-1):
    return torch.softmax(x, dim=axis)

def _gelu(x):
    return torch.nn.functional.gelu(x, approximate='tanh')

def _silu(x):
    return torch.nn.functional.silu(x)

def _sigmoid(x):
    return torch.sigmoid(x)

def _layernorm(x, axis=-1, eps=1e-05):
    m = x.mean(dim=axis, keepdim=True)
    v = x.var(dim=axis, keepdim=True, unbiased=False)
    return (x - m) / torch.sqrt(v + eps)

def _scatter_add(tgt, idx, src, axis=0):
    return tgt.clone().index_add_(axis, idx, src)

def _segment_sum(src, seg, n):
    out = torch.zeros((n,) + src.shape[1:], dtype=src.dtype)
    return out.index_add_(0, seg, src)

def _pad(x, pw):
    flat = tuple((v for pair in reversed(pw) for v in pair))
    return torch.nn.functional.pad(x, flat)
kernel_name = 'interleave_layers__memory_aug_recurrent_mem_tr__linear_kernel_hedgehog__prompt'

def setup_inputs():

    _w = lambda a: (lambda t: t.to(torch.bfloat16) if t.is_floating_point() else t)(torch.as_tensor(a))

    def _A_setup():
        B, S, Dm = (1, 2048, 640)
        H, Dh = (32, 192)
        M, seg = (8, 32)
        seg = min(seg, S)
        S = S // seg * seg
        return {'x': _w((lambda *s: torch.randn(*s, dtype=torch.bfloat16) * 0.05)(B, S, Dm)), 'mem0': _w((lambda *s: torch.randn(*s, dtype=torch.bfloat16) * 0.8)(B, M, Dm)), 'Wq': _w((lambda *s: torch.randn(*s, dtype=torch.bfloat16) * 1.6)(Dm, H, Dh) * Dm ** (-0.5)), 'Wk': _w((lambda *s: torch.randn(*s, dtype=torch.bfloat16) * 1.6)(Dm, H, Dh) * Dm ** (-0.5)), 'Wv': _w((lambda *s: torch.randn(*s, dtype=torch.bfloat16) * 0.8)(Dm, H, Dh) * Dm ** (-0.5)), 'Wo': _w((lambda *s: torch.randn(*s, dtype=torch.bfloat16) * 0.8)(H, Dh, Dm) * (H * Dh) ** (-0.5)), 'seg_len': seg}
    _A_in = _A_setup()

    def _B_setup():
        B, S, D, H = (1, 2048, 640, 32)
        Dh = D // H
        return {'x': _w((lambda *s: torch.randn(*s, dtype=torch.bfloat16) * 0.05)(B, S, D)), 'wqkv': _w((lambda *s: torch.randn(*s, dtype=torch.bfloat16) * 0.4)(D, 3 * D)), 'w_phi': _w((lambda *s: torch.randn(*s, dtype=torch.bfloat16) * 0.4)(H, Dh, Dh) * Dh ** (-0.5)), 'wo': _w((lambda *s: torch.randn(*s, dtype=torch.bfloat16) * 0.05)(D, D))}
    _B_in = _B_setup()
    return {'A_x': _A_in['x'], 'A_mem0': _A_in['mem0'], 'A_Wq': _A_in['Wq'], 'A_Wk': _A_in['Wk'], 'A_Wv': _A_in['Wv'], 'A_Wo': _A_in['Wo'], 'A_seg_len': _A_in['seg_len'], 'B_x': _B_in['x'], 'B_wqkv': _B_in['wqkv'], 'B_w_phi': _B_in['w_phi'], 'B_wo': _B_in['wo']}

def reference(A_x, A_mem0, A_Wq, A_Wk, A_Wv, A_Wo, A_seg_len, B_x, B_wqkv, B_w_phi, B_wo):

    def _A_ref(x, mem0, Wq, Wk, Wv, Wo, seg_len):
        B, S, Dm = x.shape
        H, Dh = (Wq.shape[1], Wq.shape[2])
        M = mem0.shape[1]
        scale = Dh ** (-0.5)
        n_seg = S // seg_len
        L = M + seg_len
        idx = torch.arange(L)
        mask = (idx[:, None] < M) | (idx[:, None] >= idx[None, :])
        mem = mem0
        outs = []
        for s in range(n_seg):
            xs = x[:, s * seg_len:(s + 1) * seg_len, :].clone()
            h = torch.concatenate([mem, xs], axis=1)
            q = torch.einsum('btd,dhe->bhte', h, Wq)
            k = torch.einsum('btd,dhe->bhte', h, Wk)
            v = torch.einsum('btd,dhe->bhte', h, Wv)
            scores = torch.einsum('bhqe,bhke->bhqk', q, k) * scale
            scores = torch.where(mask[None, None], scores, -1000000000.0)
            p = _softmax(scores, axis=-1)
            o = torch.einsum('bhqk,bhke->bhqe', p, v)
            o = torch.einsum('bhte,hed->btd', o, Wo)
            mem = o[:, :M, :].clone()
            outs.append(o[:, M:, :])
        return torch.concatenate(outs, axis=1)

    def _B_ref(x, wqkv, w_phi, wo):
        B, S, D = x.shape
        H = 32
        Dh = D // H
        qkv = (lambda x, *d: x.permute(*d))((x @ wqkv).reshape(B, S, 3, H, Dh), 2, 0, 3, 1, 4)
        q, k, v = (qkv[0], qkv[1], qkv[2])
        pq = torch.einsum('bhsd,hdf->bhsf', q, w_phi)
        pk = torch.einsum('bhsd,hdf->bhsf', k, w_phi)
        phi_q = torch.concatenate([_softmax(pq, axis=-1), _softmax(-pq, axis=-1)], axis=-1)
        phi_k = torch.concatenate([_softmax(pk, axis=-1), _softmax(-pk, axis=-1)], axis=-1)
        kv = torch.einsum('bhsf,bhsd->bhfd', phi_k, v)
        z = torch.einsum('bhsf->bhf', phi_k)
        num = torch.einsum('bhqf,bhfd->bhqd', phi_q, kv)
        den = torch.einsum('bhqf,bhf->bhq', phi_q, z)[..., None] + 1e-06
        o = num / den
        return (lambda x, *d: x.permute(*d))(o, 0, 2, 1, 3).reshape(B, S, D) @ wo
    x = A_x
    x = x + _A_ref(_layernorm(x, axis=-1), A_mem0, A_Wq, A_Wk, A_Wv, A_Wo, A_seg_len)
    x = x + _B_ref(_layernorm(x, axis=-1), B_wqkv, B_w_phi, B_wo)
    x = x + _A_ref(_layernorm(x, axis=-1), A_mem0, A_Wq, A_Wk, A_Wv, A_Wo, A_seg_len)
    x = x + _B_ref(_layernorm(x, axis=-1), B_wqkv, B_w_phi, B_wo)
    return x
```

```cpp
#include <hip/hip_runtime.h>
#include <hip/hip_bf16.h>
#include <stdint.h>

typedef __bf16 bf2 __attribute__((ext_vector_type(2)));
typedef __bf16 bf8 __attribute__((ext_vector_type(8)));
typedef __bf16 bf16v __attribute__((ext_vector_type(16)));
typedef float f4 __attribute__((ext_vector_type(4)));
typedef unsigned u2 __attribute__((ext_vector_type(2)));
typedef unsigned u4 __attribute__((ext_vector_type(4)));
typedef unsigned u8v __attribute__((ext_vector_type(8)));
typedef short s4 __attribute__((ext_vector_type(4)));
typedef unsigned short u16;
#define LAS __attribute__((address_space(3)))
#define DEV __device__ __forceinline__
typedef __amdgpu_buffer_rsrc_t rsrc_t;

constexpr unsigned SENT = 0xFFFFFFFFu;
constexpr int SC1 = 16;
constexpr unsigned SPIN_CAP = 1u << 16;

constexpr unsigned RING = 4u;
constexpr unsigned MCOPY = 4u;
constexpr unsigned PSLOT = 49152u;
constexpr unsigned MSLOT = 32768u;
constexpr unsigned OFF_PRING = 0u;
constexpr unsigned OFF_MRING = 786432u;
constexpr unsigned OFF_SCHED = 1310720u;
constexpr unsigned RING_BYTES = 1318912u;
constexpr unsigned OFF_PROBE = 2031616u;
constexpr unsigned OFF_FLAGS = 2097152u;
constexpr unsigned FL_CLAIM = 0u;
constexpr unsigned FL_ABORT = 1024u;
constexpr unsigned FL_KX = 2048u;
constexpr unsigned FL_VX = 6144u;
constexpr unsigned FL_UX = 10240u;
constexpr unsigned FL_KM = 14336u;
constexpr unsigned FL_PT = 18432u;
constexpr unsigned FL_VO = 22528u;
constexpr unsigned FL_CNT = 32768u;
constexpr unsigned FL_END = 34816u;
constexpr unsigned FL_NEAR = 65408u;
constexpr unsigned OFF_WT = 2162688u;
constexpr unsigned OFF_WOT = 25755648u;
constexpr unsigned OFF_BT = 33619968u;
constexpr unsigned OFF_XF = 59834368u;
constexpr unsigned OFF_KX = 62455808u;
constexpr unsigned OFF_VX = 87621632u;
constexpr unsigned OFF_UX = 112787456u;
constexpr unsigned OFF_VO = 196673536u;
constexpr unsigned OFF_KM = 217645056u;
constexpr unsigned OFF_PTX = 223936512u;
constexpr unsigned OFF_PTM = 228130816u;
constexpr unsigned OFF_XA = 229179392u;
constexpr unsigned OFF_XB = 234422272u;
constexpr unsigned OFF_TMP = 239665152u;
constexpr unsigned OFF_PROF = 259588096u;
constexpr unsigned WS_BYTES = 260636672u;
constexpr unsigned OOB = 2147483632u;
constexpr unsigned NSEG = 64u;

DEV unsigned ringphys(unsigned o) { return ((o >> 13) << 14) | (o & 8191); }
DEV unsigned pk2(float a, float b) { bf2 v = {(__bf16)a, (__bf16)b}; return __builtin_bit_cast(unsigned, v); }
DEV float asf(unsigned v) { return __builtin_bit_cast(float, v); }
DEV float bf2f(u16 v) { return __builtin_bit_cast(float, (unsigned)v << 16); }
DEV f4 mfma(u4 a, u4 b, f4 c) { return __builtin_amdgcn_mfma_f32_16x16x32_bf16(__builtin_bit_cast(bf8, a), __builtin_bit_cast(bf8, b), c, 0, 0, 0); }
DEV f4 smfmac(u4 a, u8v b, f4 c, int idx) { return __builtin_amdgcn_smfmac_f32_16x16x64_bf16(__builtin_bit_cast(bf8, a), __builtin_bit_cast(bf16v, b), c, idx, 0, 0); }
DEV u4 ld16(rsrc_t r, unsigned v, unsigned s) { return __builtin_amdgcn_raw_buffer_load_b128(r, v, s, SC1); }
DEV u4 ld16p(rsrc_t r, unsigned v, unsigned s) { return __builtin_amdgcn_raw_buffer_load_b128(r, v, s, 0); }
DEV u2 ld8(rsrc_t r, unsigned v, unsigned s) { return __builtin_amdgcn_raw_buffer_load_b64(r, v, s, SC1); }
DEV unsigned ld4(rsrc_t r, unsigned v, unsigned s) { return __builtin_amdgcn_raw_buffer_load_b32(r, v, s, SC1); }
DEV void st16(u4 d, rsrc_t r, unsigned v, unsigned s) { __builtin_amdgcn_raw_buffer_store_b128(d, r, v, s, SC1); }
DEV void st8(u2 d, rsrc_t r, unsigned v, unsigned s) { __builtin_amdgcn_raw_buffer_store_b64(d, r, v, s, SC1); }
DEV void st4(unsigned d, rsrc_t r, unsigned v, unsigned s) { __builtin_amdgcn_raw_buffer_store_b32(d, r, v, s, SC1); }
DEV u8v cat(u4 a, u4 b) { return __builtin_shufflevector(a, b, 0, 1, 2, 3, 4, 5, 6, 7); }
DEV unsigned umax3(unsigned a, unsigned b, unsigned c) { return max(max(a, b), c); }
DEV unsigned umax4(u4 v, unsigned m) { return umax3(umax3(v.x, v.y, v.z), v.w, m); }
DEV void memfence() { asm volatile("" ::: "memory"); }
DEV void drain() { asm volatile("s_waitcnt vmcnt(0)" ::: "memory"); }
DEV void bar() { asm volatile("s_waitcnt lgkmcnt(0)\n\ts_barrier" ::: "memory"); }
DEV unsigned xcc_id() { return (unsigned)__builtin_amdgcn_s_getreg(20 | (3 << 11)) & 0xfu; }
DEV float fold32(float x, float y) {
    auto r = __builtin_amdgcn_permlane32_swap(__builtin_bit_cast(unsigned, x), __builtin_bit_cast(unsigned, y), false, false);
    return asf(r[0]) + asf(r[1]);
}
DEV u2 trread(LAS char* p) { return __builtin_bit_cast(u2, __builtin_amdgcn_ds_read_tr16_b64_v4i16((LAS s4*)p)); }
template <int CTRL, int RM, int BM> DEV float dppf(float old, float v) {
    return __builtin_bit_cast(float, __builtin_amdgcn_update_dpp(__builtin_bit_cast(int, old), __builtin_bit_cast(int, v), CTRL, RM, BM, false));
}
DEV float xmax16(float v) { auto r = __builtin_amdgcn_permlane16_swap(__builtin_bit_cast(unsigned, v), __builtin_bit_cast(unsigned, v), false, false); return fmaxf(asf(r[0]), asf(r[1])); }
DEV float xmax32(float v) { auto r = __builtin_amdgcn_permlane32_swap(__builtin_bit_cast(unsigned, v), __builtin_bit_cast(unsigned, v), false, false); return fmaxf(asf(r[0]), asf(r[1])); }
DEV float xadd16(float v) { auto r = __builtin_amdgcn_permlane16_swap(__builtin_bit_cast(unsigned, v), __builtin_bit_cast(unsigned, v), false, false); return asf(r[0]) + asf(r[1]); }
DEV float xadd32(float v) { auto r = __builtin_amdgcn_permlane32_swap(__builtin_bit_cast(unsigned, v), __builtin_bit_cast(unsigned, v), false, false); return asf(r[0]) + asf(r[1]); }

DEV bool give_up(unsigned& spins, rsrc_t r) {
    ++spins;
    if (spins > SPIN_CAP) { st4(1u, r, 0, OFF_FLAGS + FL_ABORT); return true; }
    if ((spins & 63u) == 0u) return __builtin_amdgcn_readfirstlane(ld4(r, 0, OFF_FLAGS + FL_ABORT)) != 0u;
    return false;
}
DEV void wait_flags(rsrc_t r, unsigned voff, unsigned& val, unsigned need, bool& dead) {
    unsigned spins = 0;
    while (!dead && !__all(val >= need)) {
        val = ld4(r, voff, OFF_FLAGS);
        memfence();
        if (__all(val >= need)) break;
        __builtin_amdgcn_s_sleep(2);
        if (give_up(spins, r)) dead = true;
    }
}

DEV int dperm(int g, int j) { return 16 * (g >> 1) + 8 * ((j >> 1) & 1) + 4 * (g & 1) + 2 * (j >> 2) + (j & 1); }

__global__ void __launch_bounds__(256) prep_w(const u16* __restrict__ Wq, const u16* __restrict__ Wk, const u16* __restrict__ Wv, const u16* __restrict__ Wo, char* ws) {
    __shared__ __attribute__((aligned(16))) u16 tile[32 * 648];
    const int tid = threadIdx.x, b = blockIdx.x;
    if (b < 960) {
        const int mat = b / 320, h = (b % 320) / 10, t = b % 10;
        const u16* W = mat == 0 ? Wq : (mat == 1 ? Wk : Wv);
        for (int c = tid; c < 64 * 24; c += 256) { const int row = c / 24, ch = c % 24; *(u4*)(tile + row * 200 + ch * 8) = *(const u4*)(W + (size_t)(64 * t + row) * 6144 + h * 192 + ch * 8); }
        __syncthreads();
        u4* out = (u4*)(ws + OFF_WT);
        for (int f = tid; f < 12 * 128; f += 256) {
            const int et = f >> 7, u = (f >> 6) & 1, lane = f & 63, g = lane >> 4, n = lane & 15;
            unsigned w[4];
            for (int q = 0; q < 4; ++q) w[q] = tile[(32 * u + dperm(g, 2 * q)) * 200 + 16 * et + n] | ((unsigned)tile[(32 * u + dperm(g, 2 * q + 1)) * 200 + 16 * et + n] << 16);
            out[((size_t)(((mat * 32 + h) * 12 + et) * 10 + t) * 2 + u) * 64 + lane] = (u4){w[0], w[1], w[2], w[3]};
        }
    } else {
        const int h = (b - 960) / 6, ks = (b - 960) % 6;
        for (int c = tid; c < 32 * 80; c += 256) { const int row = c / 80, ch = c % 80; *(u4*)(tile + row * 648 + ch * 8) = *(const u4*)(Wo + (size_t)(h * 192 + 32 * ks + row) * 640 + ch * 8); }
        __syncthreads();
        u4* out = (u4*)(ws + OFF_WOT);
        for (int f = tid; f < 40 * 64; f += 256) {
            const int ct = f >> 6, lane = f & 63, g = lane >> 4, n = lane & 15;
            unsigned w[4];
            for (int q = 0; q < 4; ++q) { const int e = 16 * (q >> 1) + 4 * g + 2 * (q & 1); w[q] = tile[e * 648 + 16 * ct + n] | ((unsigned)tile[(e + 1) * 648 + 16 * ct + n] << 16); }
            out[(size_t)((h * 40 + ct) * 6 + ks) * 64 + lane] = (u4){w[0], w[1], w[2], w[3]};
        }
    }
}

__global__ void __launch_bounds__(256) gemm_b(const u16* __restrict__ Wv, char* ws) {
    const int h = blockIdx.x / 20, U = blockIdx.x % 20, tid = threadIdx.x, w = tid >> 6, lane = tid & 63, g = lane >> 4, m = lane & 15;
    u4 a[2][6];
    for (int x = 0; x < 2; ++x) {
        const int d = 32 * U + dperm(m >> 2, 4 * x + (m & 3));
        const u16* src = Wv + (size_t)d * 6144 + h * 192 + 4 * g;
        for (int ks = 0; ks < 6; ++ks) { const u2 lo = *(const u2*)(src + 32 * ks), hi = *(const u2*)(src + 32 * ks + 16); a[x][ks] = (u4){lo.x, lo.y, hi.x, hi.y}; }
    }
    const u4* wot = (const u4*)(ws + OFF_WOT);
    u4* out = (u4*)(ws + OFF_BT);
    for (int c = 0; c < 10; ++c) {
        const int ct = 10 * w + c;
        f4 acc0 = {0, 0, 0, 0}, acc1 = {0, 0, 0, 0};
        for (int ks = 0; ks < 6; ++ks) { const u4 bb = wot[(size_t)((h * 40 + ct) * 6 + ks) * 64 + lane]; acc0 = mfma(a[0][ks], bb, acc0); acc1 = mfma(a[1][ks], bb, acc1); }
        const int slice = 2 * ct + (m >> 3);
        out[((size_t)(slice * 16 + (h >> 1)) * 20 + U) * 64 + g * 16 + (h & 1) * 8 + (m & 7)] = (u4){pk2(acc0[0], acc0[1]), pk2(acc0[2], acc0[3]), pk2(acc1[0], acc1[1]), pk2(acc1[2], acc1[3])};
    }
}

template <typename T>
__global__ void __launch_bounds__(256) ln_xf(const T* __restrict__ x, float* __restrict__ xcopy, u16* __restrict__ xnplain, const u16* __restrict__ mem0, char* ws) {
    __shared__ __attribute__((aligned(16))) u16 xn[16 * 648];
    const int tid = threadIdx.x, w = tid >> 6, lane = tid & 63, b = blockIdx.x;
    rsrc_t r = __builtin_amdgcn_make_buffer_rsrc(ws, 0, WS_BYTES, 0x00020000);
    if (b >= 128) {
        if (mem0 == nullptr) return;
        const int q = b - 128;
        if (q < 64) {
            for (unsigned o = (q * 256 + tid) * 16; o < RING_BYTES; o += 64 * 256 * 16) {
                u4 v = {SENT, SENT, SENT, SENT};
                if (o >= OFF_MRING && o < OFF_MRING + MCOPY * MSLOT) {
                    const unsigned p = (o - OFF_MRING) % MSLOT, lo = ((p >> 14) << 13) | (p & 8191);
                    if (!(p & 8192) && lo < 10240) v = *(const u4*)(mem0 + ((lo >> 4) & 7) * 640 + (lo >> 7) * 8);
                }
                st16(v, r, o, 0);
            }
        } else if (q == 64) {
            for (unsigned o = tid * 16; o < FL_END; o += 4096) st16((u4){0, 0, 0, 0}, r, o, OFF_FLAGS);
        } else if (w == 0) {
            unsigned best[2] = {1u << 30, 1u << 30};
            for (int rep = 0; rep < 8; ++rep) for (int c = 0; c < 2; ++c) {
                drain();
                const unsigned long long t0 = __builtin_amdgcn_s_memtime();
                asm volatile("s_waitcnt lgkmcnt(0)" ::: "memory");
                st16((u4){0, 0, 0, 0}, r, lane < 8 ? lane * 16 : OOB, OFF_PROBE + c * 8192 + (rep & 1) * 4096);
                drain();
                const unsigned d = (unsigned)(__builtin_amdgcn_s_memtime() - t0);
                best[c] = min(best[c], d);
            }
            st4((xcc_id() >> 2) ^ (best[0] > best[1] ? 1u : 0u), r, lane == 0 ? 0 : OOB, OFF_FLAGS + FL_NEAR);
        }
        return;
    }
    for (int q = 0; q < 4; ++q) {
        const int m = w * 4 + q, tok = b * 16 + m;
        float v[10], sum = 0.f;
        for (int j = 0; j < 10; ++j) { if constexpr (sizeof(T) == 2) v[j] = bf2f(x[(size_t)tok * 640 + j * 64 + lane]); else v[j] = x[(size_t)tok * 640 + j * 64 + lane]; sum += v[j]; }
        for (int o = 32; o; o >>= 1) sum += __shfl_xor(sum, o);
        const float mean = sum * (1.f / 640.f);
        float var = 0.f;
        for (int j = 0; j < 10; ++j) { const float d = v[j] - mean; var += d * d; }
        for (int o = 32; o; o >>= 1) var += __shfl_xor(var, o);
        const float rs = rsqrtf(var * (1.f / 640.f) + 1e-5f);
        for (int j = 0; j < 10; ++j) {
            const u16 y = (u16)pk2((v[j] - mean) * rs, 0.f);
            xn[m * 648 + j * 64 + lane] = y;
            if (xcopy) xcopy[(size_t)tok * 640 + j * 64 + lane] = v[j];
            if (xnplain) xnplain[(size_t)tok * 640 + j * 64 + lane] = y;
        }
    }
    __syncthreads();
    u4* out = (u4*)(ws + OFF_XF);
    for (int f = tid; f < 20 * 64; f += 256) {
        const int kk = f >> 6, l = f & 63, g = l >> 4, m = l & 15;
        const unsigned* row = (const unsigned*)(xn + m * 648 + 32 * kk + 16 * (g >> 1) + 4 * (g & 1));
        out[(size_t)(b * 20 + kk) * 64 + l] = (u4){row[0], row[4], row[1], row[5]};
    }
}

constexpr int LDS_BYTES = 152 * 1024;
struct Tune { int p[8]; };
#define ROLE_CLOBBERS "v0", "v1", "v2", "v3", "v4", "v5", "v6", "v7", "v8", "v9", "v10", "v11", "v12", "v13", "v14", "v15", "v16", "v17", "v18", "v19", "v20", "v21", "v22", "v23", "v24", "v25", "v26", "v27", "v28", "v29", "v30", "v31", "v32", "v33", "v34", "v35", "v36", "v37", "v38", "v39", "v40", "v41", "v42", "v43", "v44", "v45", "v46", "v47", "v48", "v49", "v50", "v51", "v52", "v53", "v54", "v55", "v56", "v57", "v58", "v59", "v60", "v61", "v62", "v63", "v64", "v65", "v66", "v67", "v68", "v69", "v70", "v71", "v72", "v73", "v74", "v75", "v76", "v77", "v78", "v79", "v80", "v81", "v82", "v83", "v84", "v85", "v86", "v87", "v88", "v89", "v90", "v91", "v92", "v93", "v94", "v95", "v96", "v97", "v98", "v99", "v100", "v101", "v102", "v103", "v104", "v105", "v106", "v107", "v108", "v109", "v110", "v111", "v112", "v113", "v114", "v115", "v116", "v117", "v118", "v119", "v120", "v121", "v122", "v123", "v124", "v125", "v126", "v127", "v128", "v129", "v130", "v131", "v132", "v133", "v134", "v135", "v136", "v137", "v138", "v139", "v140", "v141", "v142", "v143", "v144", "v145", "v146", "v147", "v148", "v149", "v150", "v151", "v152", "v153", "v154", "v155", "v156", "v157", "v158", "v159", "v160", "v161", "v162", "v163", "v164", "v165", "v166", "v167", "v168", "v169", "v170", "v171", "v172", "v173", "v174", "v175", "v176", "v177", "v178", "v179", "v180", "v181", "v182", "v183", "v184", "v185", "v186", "v187", "v188", "v189", "v190", "v191", "v192", "v193", "v194", "v195", "v196", "v197", "v198", "v199", "v200", "v201", "v202", "v203", "v204", "v205", "v206", "v207", "v208", "v209", "v210", "v211", "v212", "v213", "v214", "v215", "v216", "v217", "v218", "v219", "v220", "v221", "v222", "v223", "v224", "v225", "v226", "v227", "v228", "v229", "v230", "v231", "v232", "v233", "v234", "v235", "v236", "v237", "v238", "v239", "v240", "v241", "v242", "v243", "v244", "v245", "v246", "v247", "v248", "v249", "v250", "v251", "v252", "v253", "v254", "v255", "a0", "a1", "a2", "a3", "a4", "a5", "a6", "a7", "a8", "a9", "a10", "a11", "a12", "a13", "a14", "a15", "a16", "a17", "a18", "a19", "a20", "a21", "a22", "a23", "a24", "a25", "a26", "a27", "a28", "a29", "a30", "a31", "a32", "a33", "a34", "a35", "a36", "a37", "a38", "a39", "a40", "a41", "a42", "a43", "a44", "a45", "a46", "a47", "a48", "a49", "a50", "a51", "a52", "a53", "a54", "a55", "a56", "a57", "a58", "a59", "a60", "a61", "a62", "a63", "a64", "a65", "a66", "a67", "a68", "a69", "a70", "a71", "a72", "a73", "a74", "a75", "a76", "a77", "a78", "a79", "a80", "a81", "a82", "a83", "a84", "a85", "a86", "a87", "a88", "a89", "a90", "a91", "a92", "a93", "a94", "a95", "a96", "a97", "a98", "a99", "a100", "a101", "a102", "a103", "a104", "a105", "a106", "a107", "a108", "a109", "a110", "a111", "a112", "a113", "a114", "a115", "a116", "a117", "a118", "a119", "a120", "a121", "a122", "a123", "a124", "a125", "a126", "a127", "a128", "a129", "a130", "a131", "a132", "a133", "a134", "a135", "a136", "a137", "a138", "a139", "a140", "a141", "a142", "a143", "a144", "a145", "a146", "a147", "a148", "a149", "a150", "a151", "a152", "a153", "a154", "a155", "a156", "a157", "a158", "a159", "a160", "a161", "a162", "a163", "a164", "a165", "a166", "a167", "a168", "a169", "a170", "a171", "a172", "a173", "a174", "a175", "a176", "a177", "a178", "a179", "a180", "a181", "a182", "a183", "a184", "a185", "a186", "a187", "a188", "a189", "a190", "a191", "a192", "a193", "a194", "a195", "a196", "a197", "a198", "a199", "a200", "a201", "a202", "a203", "a204", "a205", "a206", "a207", "a208", "a209", "a210", "a211", "a212", "a213", "a214", "a215", "a216", "a217", "a218", "a219", "a220", "a221", "a222", "a223", "a224", "a225", "a226", "a227", "a228", "a229", "a230", "a231", "a232", "a233", "a234", "a235", "a236", "a237", "a238", "a239", "a240", "a241", "a242", "a243", "a244", "a245", "a246", "a247", "a248", "a249", "a250", "a251", "a252", "a253", "a254", "a255", "s18", "s19", "s20", "s21", "s22", "s23", "s24", "s25", "s26", "s27", "s28", "s29", "s30", "s31", "s34", "s35", "s36", "s37", "s38", "s39", "s40", "s41", "s42", "s43", "s44", "s45", "s46", "s47", "s48", "s49", "s50", "s51", "s52", "s53", "s54", "s55", "s56", "s57", "s58", "s59", "s60", "s61", "s62", "s63", "s64", "s65", "s66", "s67", "s68", "s69", "s70", "s71", "s72", "s73", "s74", "s75", "s76", "s77", "s78", "s79", "s80", "s81", "s82", "s83", "s84", "s85", "s86", "s87", "s88", "s89", "s90", "s91", "s92", "s93", "s94", "s95", "s96", "s97", "s98", "s99", "vcc", "scc", "memory"
#define ROLE_INPUTS "s"(lo), "s"(hi), "s"(WS_BYTES), "s"(0x00020000u), "s"(id), "s"(w), "s"(aux), "s"(tn.p[0]), "s"(tn.p[1]), "s"(tn.p[2]), "s"(tn.p[3]), "s"(tn.p[4]), "s"(tn.p[5]), "s"(tn.p[6]), "s"(tn.p[7])
DEV void role_head(unsigned lo, unsigned hi, int id, int w, int aux, const Tune& tn) {
    asm volatile(
    "s_mov_b32 s40, %0\n"
    "s_mov_b32 s41, %1\n"
    "s_mov_b32 s42, %2\n"
    "s_mov_b32 s43, %3\n"
    "s_mov_b32 s44, %4\n"
    "s_mov_b32 s45, %5\n"
    "s_mov_b32 s46, %6\n"
    "s_mov_b32 s66, %7\n"
    "s_mov_b32 s67, %8\n"
    "s_mov_b32 s68, %9\n"
    "s_mov_b32 s69, %10\n"
    "s_mov_b32 s70, %11\n"
    "s_mov_b32 s71, %12\n"
    "s_mov_b32 s72, %13\n"
    "s_mov_b32 s73, %14\n"
    "s_mov_b64 exec, -1\n"
    "v_mbcnt_lo_u32_b32 v15, -1, 0\n"
    "v_mbcnt_hi_u32_b32 v15, -1, v15\n"
    "v_lshlrev_b32 v0, 4, v15\n"
    "s_mov_b32 s58, 2097152\n"
    "s_mov_b32 s92, 0\n"
    "s_lshl_b32 s48, s45, 15\n"
    "v_add_u32 v2, s48, v0\n"
    "s_lshl_b32 s50, s44, 2\n"
    "s_add_u32 s50, s50, s45\n"
    "s_mul_i32 s50, s50, 3\n"
    "s_mul_i32 s51, s50, 6554\n"
    "s_lshr_b32 s51, s51, 16\n"
    "s_mul_i32 s51, s51, 10\n"
    "s_sub_u32 s50, s50, s51\n"
    "s_lshl_b32 s20, s50, 10\n"
    "s_add_u32 s50, s50, 1\n"
    "s_cmp_eq_u32 s50, 10\n"
    "s_cselect_b32 s50, 0, s50\n"
    "s_lshl_b32 s21, s50, 10\n"
    "s_add_u32 s50, s50, 1\n"
    "s_cmp_eq_u32 s50, 10\n"
    "s_cselect_b32 s50, 0, s50\n"
    "s_lshl_b32 s22, s50, 10\n"
    "s_add_u32 s50, s50, 1\n"
    "s_cmp_eq_u32 s50, 10\n"
    "s_cselect_b32 s50, 0, s50\n"
    "s_lshl_b32 s23, s50, 10\n"
    "s_add_u32 s50, s50, 1\n"
    "s_cmp_eq_u32 s50, 10\n"
    "s_cselect_b32 s50, 0, s50\n"
    "s_lshl_b32 s24, s50, 10\n"
    "s_add_u32 s50, s50, 1\n"
    "s_cmp_eq_u32 s50, 10\n"
    "s_cselect_b32 s50, 0, s50\n"
    "s_lshl_b32 s25, s50, 10\n"
    "s_add_u32 s50, s50, 1\n"
    "s_cmp_eq_u32 s50, 10\n"
    "s_cselect_b32 s50, 0, s50\n"
    "s_lshl_b32 s26, s50, 10\n"
    "s_add_u32 s50, s50, 1\n"
    "s_cmp_eq_u32 s50, 10\n"
    "s_cselect_b32 s50, 0, s50\n"
    "s_lshl_b32 s27, s50, 10\n"
    "s_add_u32 s50, s50, 1\n"
    "s_cmp_eq_u32 s50, 10\n"
    "s_cselect_b32 s50, 0, s50\n"
    "s_lshl_b32 s28, s50, 10\n"
    "s_add_u32 s50, s50, 1\n"
    "s_cmp_eq_u32 s50, 10\n"
    "s_cselect_b32 s50, 0, s50\n"
    "s_lshl_b32 s29, s50, 10\n"
    "s_add_u32 s50, s50, 1\n"
    "s_cmp_eq_u32 s50, 10\n"
    "s_cselect_b32 s50, 0, s50\n"
    "s_mul_i32 s48, s44, 12\n"
    "s_mul_i32 s49, s45, 3\n"
    "s_add_u32 s48, s48, s49\n"
    "s_add_u32 s48, s48, 0\n"
    "s_mul_i32 s48, s48, 20480\n"
    "s_add_u32 s48, s48, 2162688\n"
    "s_lshl1_add_u32 s49, s20, s48\n"
    "buffer_load_dwordx4 a[0:3], v0, s[40:43], s49 offen\n"
    "buffer_load_dwordx4 a[4:7], v0, s[40:43], s49 offen offset:1024\n"
    "s_lshl1_add_u32 s49, s21, s48\n"
    "buffer_load_dwordx4 a[24:27], v0, s[40:43], s49 offen\n"
    "buffer_load_dwordx4 a[28:31], v0, s[40:43], s49 offen offset:1024\n"
    "s_lshl1_add_u32 s49, s22, s48\n"
    "buffer_load_dwordx4 a[40:43], v0, s[40:43], s49 offen\n"
    "buffer_load_dwordx4 a[44:47], v0, s[40:43], s49 offen offset:1024\n"
    "s_lshl1_add_u32 s49, s23, s48\n"
    "buffer_load_dwordx4 a[64:67], v0, s[40:43], s49 offen\n"
    "buffer_load_dwordx4 a[68:71], v0, s[40:43], s49 offen offset:1024\n"
    "s_lshl1_add_u32 s49, s24, s48\n"
    "buffer_load_dwordx4 a[88:91], v0, s[40:43], s49 offen\n"
    "buffer_load_dwordx4 a[92:95], v0, s[40:43], s49 offen offset:1024\n"
    "s_lshl1_add_u32 s49, s25, s48\n"
    "buffer_load_dwordx4 a[104:107], v0, s[40:43], s49 offen\n"
    "buffer_load_dwordx4 a[108:111], v0, s[40:43], s49 offen offset:1024\n"
    "s_lshl1_add_u32 s49, s26, s48\n"
    "buffer_load_dwordx4 a[128:131], v0, s[40:43], s49 offen\n"
    "buffer_load_dwordx4 a[132:135], v0, s[40:43], s49 offen offset:1024\n"
    "s_lshl1_add_u32 s49, s27, s48\n"
    "buffer_load_dwordx4 a[152:155], v0, s[40:43], s49 offen\n"
    "buffer_load_dwordx4 a[156:159], v0, s[40:43], s49 offen offset:1024\n"
    "s_lshl1_add_u32 s49, s28, s48\n"
    "buffer_load_dwordx4 a[168:171], v0, s[40:43], s49 offen\n"
    "buffer_load_dwordx4 a[172:175], v0, s[40:43], s49 offen offset:1024\n"
    "s_lshl1_add_u32 s49, s29, s48\n"
    "buffer_load_dwordx4 a[192:195], v0, s[40:43], s49 offen\n"
    "buffer_load_dwordx4 a[196:199], v0, s[40:43], s49 offen offset:1024\n"
    "s_mul_i32 s48, s44, 12\n"
    "s_mul_i32 s49, s45, 3\n"
    "s_add_u32 s48, s48, s49\n"
    "s_add_u32 s48, s48, 384\n"
    "s_mul_i32 s48, s48, 20480\n"
    "s_add_u32 s48, s48, 2162688\n"
    "s_lshl1_add_u32 s49, s20, s48\n"
    "buffer_load_dwordx4 a[16:19], v0, s[40:43], s49 offen\n"
    "buffer_load_dwordx4 a[20:23], v0, s[40:43], s49 offen offset:1024\n"
    "s_lshl1_add_u32 s49, s21, s48\n"
    "buffer_load_dwordx4 a[32:35], v0, s[40:43], s49 offen\n"
    "buffer_load_dwordx4 a[36:39], v0, s[40:43], s49 offen offset:1024\n"
    "s_lshl1_add_u32 s49, s22, s48\n"
    "buffer_load_dwordx4 a[56:59], v0, s[40:43], s49 offen\n"
    "buffer_load_dwordx4 a[60:63], v0, s[40:43], s49 offen offset:1024\n"
    "s_lshl1_add_u32 s49, s23, s48\n"
    "buffer_load_dwordx4 a[80:83], v0, s[40:43], s49 offen\n"
    "buffer_load_dwordx4 a[84:87], v0, s[40:43], s49 offen offset:1024\n"
    "s_lshl1_add_u32 s49, s24, s48\n"
    "buffer_load_dwordx4 a[96:99], v0, s[40:43], s49 offen\n"
    "buffer_load_dwordx4 a[100:103], v0, s[40:43], s49 offen offset:1024\n"
    "s_lshl1_add_u32 s49, s25, s48\n"
    "buffer_load_dwordx4 a[120:123], v0, s[40:43], s49 offen\n"
    "buffer_load_dwordx4 a[124:127], v0, s[40:43], s49 offen offset:1024\n"
    "s_lshl1_add_u32 s49, s26, s48\n"
    "buffer_load_dwordx4 a[144:147], v0, s[40:43], s49 offen\n"
    "buffer_load_dwordx4 a[148:151], v0, s[40:43], s49 offen offset:1024\n"
    "s_lshl1_add_u32 s49, s27, s48\n"
    "buffer_load_dwordx4 a[160:163], v0, s[40:43], s49 offen\n"
    "buffer_load_dwordx4 a[164:167], v0, s[40:43], s49 offen offset:1024\n"
    "s_lshl1_add_u32 s49, s28, s48\n"
    "buffer_load_dwordx4 a[184:187], v0, s[40:43], s49 offen\n"
    "buffer_load_dwordx4 a[188:191], v0, s[40:43], s49 offen offset:1024\n"
    "s_lshl1_add_u32 s49, s29, s48\n"
    "buffer_load_dwordx4 a[208:211], v0, s[40:43], s49 offen\n"
    "buffer_load_dwordx4 a[212:215], v0, s[40:43], s49 offen offset:1024\n"
    "s_mul_i32 s48, s44, 12\n"
    "s_mul_i32 s49, s45, 3\n"
    "s_add_u32 s48, s48, s49\n"
    "s_add_u32 s48, s48, 1\n"
    "s_mul_i32 s48, s48, 20480\n"
    "s_add_u32 s48, s48, 2162688\n"
    "s_lshl1_add_u32 s49, s20, s48\n"
    "buffer_load_dwordx4 a[8:11], v0, s[40:43], s49 offen\n"
    "buffer_load_dwordx4 a[12:15], v0, s[40:43], s49 offen offset:1024\n"
    "s_lshl1_add_u32 s49, s21, s48\n"
    "buffer_load_dwordx4 v[32:35], v0, s[40:43], s49 offen\n"
    "buffer_load_dwordx4 v[36:39], v0, s[40:43], s49 offen offset:1024\n"
    "s_lshl1_add_u32 s49, s22, s48\n"
    "buffer_load_dwordx4 a[48:51], v0, s[40:43], s49 offen\n"
    "buffer_load_dwordx4 a[52:55], v0, s[40:43], s49 offen offset:1024\n"
    "s_lshl1_add_u32 s49, s23, s48\n"
    "buffer_load_dwordx4 a[72:75], v0, s[40:43], s49 offen\n"
    "buffer_load_dwordx4 a[76:79], v0, s[40:43], s49 offen offset:1024\n"
    "s_lshl1_add_u32 s49, s24, s48\n"
    "buffer_load_dwordx4 v[64:67], v0, s[40:43], s49 offen\n"
    "buffer_load_dwordx4 v[68:71], v0, s[40:43], s49 offen offset:1024\n"
    "s_lshl1_add_u32 s49, s25, s48\n"
    "buffer_load_dwordx4 a[112:115], v0, s[40:43], s49 offen\n"
    "buffer_load_dwordx4 a[116:119], v0, s[40:43], s49 offen offset:1024\n"
    "s_lshl1_add_u32 s49, s26, s48\n"
    "buffer_load_dwordx4 a[136:139], v0, s[40:43], s49 offen\n"
    "buffer_load_dwordx4 a[140:143], v0, s[40:43], s49 offen offset:1024\n"
    "s_lshl1_add_u32 s49, s27, s48\n"
    "buffer_load_dwordx4 v[96:99], v0, s[40:43], s49 offen\n"
    "buffer_load_dwordx4 v[100:103], v0, s[40:43], s49 offen offset:1024\n"
    "s_lshl1_add_u32 s49, s28, s48\n"
    "buffer_load_dwordx4 a[176:179], v0, s[40:43], s49 offen\n"
    "buffer_load_dwordx4 a[180:183], v0, s[40:43], s49 offen offset:1024\n"
    "s_lshl1_add_u32 s49, s29, s48\n"
    "buffer_load_dwordx4 a[200:203], v0, s[40:43], s49 offen\n"
    "buffer_load_dwordx4 a[204:207], v0, s[40:43], s49 offen offset:1024\n"
    "s_mul_i32 s48, s44, 12\n"
    "s_mul_i32 s49, s45, 3\n"
    "s_add_u32 s48, s48, s49\n"
    "s_add_u32 s48, s48, 385\n"
    "s_mul_i32 s48, s48, 20480\n"
    "s_add_u32 s48, s48, 2162688\n"
    "s_lshl1_add_u32 s49, s20, s48\n"
    "buffer_load_dwordx4 v[16:19], v0, s[40:43], s49 offen\n"
    "buffer_load_dwordx4 v[20:23], v0, s[40:43], s49 offen offset:1024\n"
    "s_lshl1_add_u32 s49, s21, s48\n"
    "buffer_load_dwordx4 v[24:27], v0, s[40:43], s49 offen\n"
    "s_waitcnt vmcnt(61)\n"
    "buffer_load_dwordx4 v[28:31], v0, s[40:43], s49 offen offset:1024\n"
    "s_lshl1_add_u32 s49, s22, s48\n"
    "buffer_load_dwordx4 v[40:43], v0, s[40:43], s49 offen\n"
    "s_waitcnt vmcnt(61)\n"
    "buffer_load_dwordx4 v[44:47], v0, s[40:43], s49 offen offset:1024\n"
    "s_lshl1_add_u32 s49, s23, s48\n"
    "buffer_load_dwordx4 v[48:51], v0, s[40:43], s49 offen\n"
    "s_waitcnt vmcnt(61)\n"
    "buffer_load_dwordx4 v[52:55], v0, s[40:43], s49 offen offset:1024\n"
    "s_lshl1_add_u32 s49, s24, s48\n"
    "buffer_load_dwordx4 v[56:59], v0, s[40:43], s49 offen\n"
    "s_waitcnt vmcnt(61)\n"
    "buffer_load_dwordx4 v[60:63], v0, s[40:43], s49 offen offset:1024\n"
    "s_lshl1_add_u32 s49, s25, s48\n"
    "buffer_load_dwordx4 v[72:75], v0, s[40:43], s49 offen\n"
    "s_waitcnt vmcnt(61)\n"
    "buffer_load_dwordx4 v[76:79], v0, s[40:43], s49 offen offset:1024\n"
    "s_lshl1_add_u32 s49, s26, s48\n"
    "buffer_load_dwordx4 v[80:83], v0, s[40:43], s49 offen\n"
    "s_waitcnt vmcnt(61)\n"
    "buffer_load_dwordx4 v[84:87], v0, s[40:43], s49 offen offset:1024\n"
    "s_lshl1_add_u32 s49, s27, s48\n"
    "buffer_load_dwordx4 v[88:91], v0, s[40:43], s49 offen\n"
    "s_waitcnt vmcnt(61)\n"
    "buffer_load_dwordx4 v[92:95], v0, s[40:43], s49 offen offset:1024\n"
    "s_lshl1_add_u32 s49, s28, s48\n"
    "buffer_load_dwordx4 v[104:107], v0, s[40:43], s49 offen\n"
    "s_waitcnt vmcnt(61)\n"
    "buffer_load_dwordx4 v[108:111], v0, s[40:43], s49 offen offset:1024\n"
    "s_lshl1_add_u32 s49, s29, s48\n"
    "buffer_load_dwordx4 v[112:115], v0, s[40:43], s49 offen\n"
    "s_waitcnt vmcnt(61)\n"
    "buffer_load_dwordx4 v[116:119], v0, s[40:43], s49 offen offset:1024\n"
    "s_mul_i32 s48, s44, 12\n"
    "s_mul_i32 s49, s45, 3\n"
    "s_add_u32 s48, s48, s49\n"
    "s_add_u32 s48, s48, 2\n"
    "s_mul_i32 s48, s48, 20480\n"
    "s_add_u32 s48, s48, 2162688\n"
    "s_lshl1_add_u32 s49, s20, s48\n"
    "buffer_load_dwordx4 a[216:219], v0, s[40:43], s49 offen\n"
    "s_waitcnt vmcnt(61)\n"
    "buffer_load_dwordx4 a[220:223], v0, s[40:43], s49 offen offset:1024\n"
    "s_lshl1_add_u32 s49, s21, s48\n"
    "buffer_load_dwordx4 a[224:227], v0, s[40:43], s49 offen\n"
    "s_waitcnt vmcnt(61)\n"
    "buffer_load_dwordx4 a[228:231], v0, s[40:43], s49 offen offset:1024\n"
    "s_lshl1_add_u32 s49, s22, s48\n"
    "buffer_load_dwordx4 a[232:235], v0, s[40:43], s49 offen\n"
    "s_waitcnt vmcnt(61)\n"
    "buffer_load_dwordx4 a[236:239], v0, s[40:43], s49 offen offset:1024\n"
    "s_lshl1_add_u32 s49, s23, s48\n"
    "buffer_load_dwordx4 a[240:243], v0, s[40:43], s49 offen\n"
    "s_waitcnt vmcnt(61)\n"
    "buffer_load_dwordx4 a[244:247], v0, s[40:43], s49 offen offset:1024\n"
    "s_lshl1_add_u32 s49, s24, s48\n"
    "buffer_load_dwordx4 v[160:163], v0, s[40:43], s49 offen\n"
    "s_waitcnt vmcnt(61)\n"
    "buffer_load_dwordx4 v[164:167], v0, s[40:43], s49 offen offset:1024\n"
    "s_lshl1_add_u32 s49, s25, s48\n"
    "buffer_load_dwordx4 v[176:179], v0, s[40:43], s49 offen\n"
    "s_waitcnt vmcnt(61)\n"
    "buffer_load_dwordx4 v[180:183], v0, s[40:43], s49 offen offset:1024\n"
    "s_lshl1_add_u32 s49, s26, s48\n"
    "buffer_load_dwordx4 v[192:195], v0, s[40:43], s49 offen\n"
    "s_waitcnt vmcnt(61)\n"
    "buffer_load_dwordx4 v[196:199], v0, s[40:43], s49 offen offset:1024\n"
    "s_lshl1_add_u32 s49, s27, s48\n"
    "buffer_load_dwordx4 v[208:211], v0, s[40:43], s49 offen\n"
    "s_waitcnt vmcnt(61)\n"
    "buffer_load_dwordx4 v[212:215], v0, s[40:43], s49 offen offset:1024\n"
    "s_lshl1_add_u32 s49, s28, s48\n"
    "buffer_load_dwordx4 v[224:227], v0, s[40:43], s49 offen\n"
    "s_waitcnt vmcnt(61)\n"
    "buffer_load_dwordx4 v[228:231], v0, s[40:43], s49 offen offset:1024\n"
    "s_lshl1_add_u32 s49, s29, s48\n"
    "buffer_load_dwordx4 v[240:243], v0, s[40:43], s49 offen\n"
    "s_waitcnt vmcnt(61)\n"
    "buffer_load_dwordx4 v[244:247], v0, s[40:43], s49 offen offset:1024\n"
    "s_mul_i32 s48, s44, 12\n"
    "s_mul_i32 s49, s45, 3\n"
    "s_add_u32 s48, s48, s49\n"
    "s_add_u32 s48, s48, 386\n"
    "s_mul_i32 s48, s48, 20480\n"
    "s_add_u32 s48, s48, 2162688\n"
    "s_lshl1_add_u32 s49, s20, s48\n"
    "buffer_load_dwordx4 v[120:123], v0, s[40:43], s49 offen\n"
    "s_waitcnt vmcnt(61)\n"
    "buffer_load_dwordx4 v[124:127], v0, s[40:43], s49 offen offset:1024\n"
    "s_lshl1_add_u32 s49, s21, s48\n"
    "buffer_load_dwordx4 v[128:131], v0, s[40:43], s49 offen\n"
    "s_waitcnt vmcnt(61)\n"
    "buffer_load_dwordx4 v[132:135], v0, s[40:43], s49 offen offset:1024\n"
    "s_lshl1_add_u32 s49, s22, s48\n"
    "buffer_load_dwordx4 v[136:139], v0, s[40:43], s49 offen\n"
    "s_waitcnt vmcnt(61)\n"
    "buffer_load_dwordx4 v[140:143], v0, s[40:43], s49 offen offset:1024\n"
    "s_lshl1_add_u32 s49, s23, s48\n"
    "buffer_load_dwordx4 a[248:251], v0, s[40:43], s49 offen\n"
    "s_waitcnt vmcnt(61)\n"
    "buffer_load_dwordx4 a[252:255], v0, s[40:43], s49 offen offset:1024\n"
    "s_lshl1_add_u32 s49, s24, s48\n"
    "buffer_load_dwordx4 v[168:171], v0, s[40:43], s49 offen\n"
    "s_waitcnt vmcnt(61)\n"
    "buffer_load_dwordx4 v[172:175], v0, s[40:43], s49 offen offset:1024\n"
    "s_lshl1_add_u32 s49, s25, s48\n"
    "buffer_load_dwordx4 v[184:187], v0, s[40:43], s49 offen\n"
    "s_waitcnt vmcnt(61)\n"
    "buffer_load_dwordx4 v[188:191], v0, s[40:43], s49 offen offset:1024\n"
    "s_lshl1_add_u32 s49, s26, s48\n"
    "buffer_load_dwordx4 v[200:203], v0, s[40:43], s49 offen\n"
    "s_waitcnt vmcnt(61)\n"
    "buffer_load_dwordx4 v[204:207], v0, s[40:43], s49 offen offset:1024\n"
    "s_lshl1_add_u32 s49, s27, s48\n"
    "buffer_load_dwordx4 v[216:219], v0, s[40:43], s49 offen\n"
    "s_waitcnt vmcnt(61)\n"
    "buffer_load_dwordx4 v[220:223], v0, s[40:43], s49 offen offset:1024\n"
    "s_lshl1_add_u32 s49, s28, s48\n"
    "buffer_load_dwordx4 v[232:235], v0, s[40:43], s49 offen\n"
    "s_waitcnt vmcnt(61)\n"
    "buffer_load_dwordx4 v[236:239], v0, s[40:43], s49 offen offset:1024\n"
    "s_lshl1_add_u32 s49, s29, s48\n"
    "buffer_load_dwordx4 v[248:251], v0, s[40:43], s49 offen\n"
    "s_waitcnt vmcnt(61)\n"
    "buffer_load_dwordx4 v[252:255], v0, s[40:43], s49 offen offset:1024\n"
    "s_and_b32 s49, s20, 0x2000\n"
    "s_add_u32 s20, s20, s49\n"
    "s_and_b32 s49, s21, 0x2000\n"
    "s_add_u32 s21, s21, s49\n"
    "s_and_b32 s49, s22, 0x2000\n"
    "s_add_u32 s22, s22, s49\n"
    "s_and_b32 s49, s23, 0x2000\n"
    "s_add_u32 s23, s23, s49\n"
    "s_and_b32 s49, s24, 0x2000\n"
    "s_add_u32 s24, s24, s49\n"
    "s_and_b32 s49, s25, 0x2000\n"
    "s_add_u32 s25, s25, s49\n"
    "s_and_b32 s49, s26, 0x2000\n"
    "s_add_u32 s26, s26, s49\n"
    "s_and_b32 s49, s27, 0x2000\n"
    "s_add_u32 s27, s27, s49\n"
    "s_and_b32 s49, s28, 0x2000\n"
    "s_add_u32 s28, s28, s49\n"
    "s_and_b32 s49, s29, 0x2000\n"
    "s_add_u32 s29, s29, s49\n"
    "s_waitcnt vmcnt(59)\n"
    "ds_write_b128 v2, v[16:19] offset:0\n"
    "s_waitcnt vmcnt(58)\n"
    "ds_write_b128 v2, v[20:23] offset:1024\n"
    "s_waitcnt vmcnt(57)\n"
    "ds_write_b128 v2, v[24:27] offset:2048\n"
    "s_waitcnt vmcnt(56)\n"
    "ds_write_b128 v2, v[28:31] offset:3072\n"
    "ds_write_b128 v2, v[32:35] offset:4096\n"
    "ds_write_b128 v2, v[36:39] offset:5120\n"
    "s_waitcnt vmcnt(55)\n"
    "ds_write_b128 v2, v[40:43] offset:6144\n"
    "s_waitcnt vmcnt(54)\n"
    "ds_write_b128 v2, v[44:47] offset:7168\n"
    "s_waitcnt vmcnt(53)\n"
    "ds_write_b128 v2, v[48:51] offset:8192\n"
    "s_waitcnt vmcnt(52)\n"
    "ds_write_b128 v2, v[52:55] offset:9216\n"
    "s_waitcnt vmcnt(51)\n"
    "ds_write_b128 v2, v[56:59] offset:10240\n"
    "s_waitcnt vmcnt(50)\n"
    "ds_write_b128 v2, v[60:63] offset:11264\n"
    "ds_write_b128 v2, v[64:67] offset:12288\n"
    "ds_write_b128 v2, v[68:71] offset:13312\n"
    "s_waitcnt vmcnt(49)\n"
    "ds_write_b128 v2, v[72:75] offset:14336\n"
    "s_waitcnt vmcnt(48)\n"
    "ds_write_b128 v2, v[76:79] offset:15360\n"
    "s_waitcnt lgkmcnt(14)\n"
    "s_waitcnt vmcnt(47)\n"
    "ds_write_b128 v2, v[80:83] offset:16384\n"
    "s_waitcnt vmcnt(46)\n"
    "ds_write_b128 v2, v[84:87] offset:17408\n"
    "s_waitcnt lgkmcnt(14)\n"
    "s_waitcnt vmcnt(45)\n"
    "ds_write_b128 v2, v[88:91] offset:18432\n"
    "s_waitcnt vmcnt(44)\n"
    "ds_write_b128 v2, v[92:95] offset:19456\n"
    "s_waitcnt lgkmcnt(14)\n"
    "ds_write_b128 v2, v[96:99] offset:20480\n"
    "ds_write_b128 v2, v[100:103] offset:21504\n"
    "s_waitcnt lgkmcnt(14)\n"
    "s_waitcnt vmcnt(43)\n"
    "ds_write_b128 v2, v[104:107] offset:22528\n"
    "s_waitcnt vmcnt(42)\n"
    "ds_write_b128 v2, v[108:111] offset:23552\n"
    "s_waitcnt lgkmcnt(14)\n"
    "s_waitcnt vmcnt(41)\n"
    "ds_write_b128 v2, v[112:115] offset:24576\n"
    "s_waitcnt vmcnt(40)\n"
    "ds_write_b128 v2, v[116:119] offset:25600\n"
    "s_waitcnt lgkmcnt(14)\n"
    "s_waitcnt vmcnt(19)\n"
    "ds_write_b128 v2, v[120:123] offset:26624\n"
    "s_waitcnt vmcnt(18)\n"
    "ds_write_b128 v2, v[124:127] offset:27648\n"
    "s_waitcnt lgkmcnt(14)\n"
    "s_waitcnt vmcnt(17)\n"
    "ds_write_b128 v2, v[128:131] offset:28672\n"
    "s_waitcnt vmcnt(16)\n"
    "ds_write_b128 v2, v[132:135] offset:29696\n"
    "s_waitcnt lgkmcnt(14)\n"
    "s_waitcnt vmcnt(15)\n"
    "ds_write_b128 v2, v[136:139] offset:30720\n"
    "s_waitcnt vmcnt(14)\n"
    "ds_write_b128 v2, v[140:143] offset:31744\n"
    "s_waitcnt lgkmcnt(14)\n"
    "s_waitcnt vmcnt(0) lgkmcnt(0)\n"
    "v_lshrrev_b32 v152, 4, v15\n"
    "v_and_b32 v153, 7, v15\n"
    "v_bfe_u32 v154, v15, 3, 1\n"
    "v_cmp_eq_u32 vcc, 1, v154\n"
    "v_mov_b32 v13, 0x4444\n"
    "v_mov_b32 v1, 0xeeee\n"
    "v_cndmask_b32 v1, v13, v1, vcc\n"
    "v_mov_b32 v13, 0xff800000\n"
    "v_cndmask_b32 v8, 0, v13, vcc\n"
    "v_cndmask_b32 v9, 0, v13, vcc\n"
    "v_cndmask_b32 v10, 0, v13, vcc\n"
    "v_cndmask_b32 v11, 0, v13, vcc\n"
    "v_bfe_u32 v13, v15, 2, 2\n"
    "v_xor_b32 v13, v13, v152\n"
    "v_lshlrev_b32 v13, 3, v13\n"
    "v_and_b32 v3, 15, v15\n"
    "v_lshl_add_u32 v3, v3, 5, v13\n"
    "s_mul_i32 s48, s45, 1536\n"
    "s_add_u32 s48, s48, 143360\n"
    "v_add_u32 v3, s48, v3\n"
    "v_xor_b32 v4, v15, v152\n"
    "v_lshlrev_b32 v4, 3, v4\n"
    "v_add_u32 v4, 143360, v4\n"
    "v_add_u32 v5, 131072, v0\n"
    "s_lshr_b32 s48, s44, 1\n"
    "s_lshl_b32 s48, s48, 10\n"
    "s_and_b32 s49, s44, 1\n"
    "s_lshl_b32 s49, s49, 7\n"
    "s_add_u32 s50, s48, s49\n"
    "s_lshl_b32 s51, s44, 7\n"
    "s_add_u32 s51, s51, 16384\n"
    "s_mov_b32 s47, 0\n"
    "s_mov_b32 s61, 6144\n"
    "s_cmp_lg_u32 s45, 0\n"
    "s_cbranch_scc1 hw_wx_%=\n"
    "v_lshlrev_b32 v6, 8, v152\n"
    "v_add_u32 v6, s50, v6\n"
    "v_mov_b32 v7, s51\n"
    "v_cmp_eq_u32 vcc, 0, v154\n"
    "s_nop 1\n"
    "v_cndmask_b32 v6, v7, v6, vcc\n"
    "v_lshl_add_u32 v6, v153, 4, v6\n"
    "v_and_b32 v13, 0xffffe000, v6\n"
    "v_add_u32 v6, v6, v13\n"
    "v_lshrrev_b32 v7, 3, v15\n"
    "v_cmp_eq_u32 s[56:57], 5, v7\n"
    "s_nop 0\n"
    "s_or_b64 vcc, vcc, s[56:57]\n"
    "v_mov_b32 v13, 2147483632\n"
    "v_cndmask_b32 v6, v13, v6, vcc\n"
    "v_mov_b32 v14, 0\n"
    "v_cmp_eq_u32 vcc, 0, v15\n"
    "v_mov_b32 v13, 2147483632\n"
    "s_nop 0\n"
    "v_cndmask_b32 v14, v13, v14, vcc\n"
    "v_or_b32 v7, s44, v15\n"
    "v_cmp_eq_u32 vcc, 0, v7\n"
    "v_mov_b32 v13, 2147483632\n"
    "s_nop 0\n"
    "v_cndmask_b32 v7, v13, v7, vcc\n"
    "s_mov_b32 s75, 6\n"
    "s_mov_b32 s64, 0x3dd53b95\n"
    "s_mov_b32 s65, s64\n"
    "hw_loopa_%=:\n"
    "s_and_b32 s48, s47, 3\n"
    "s_mul_i32 s53, s48, 196608\n"
    "s_lshl_b32 s50, s48, 2\n"
    "s_add_u32 s50, s50, s46\n"
    "s_mul_i32 s50, s50, 32768\n"
    "s_add_u32 s50, s50, 786432\n"
    "v_add_u32 v158, s50, v0\n"
    "s_add_u32 s50, s53, 49152\n"
    "s_add_u32 s51, s53, 98304\n"
    "s_add_u32 s52, s53, 147456\n"
    "v_mov_b32 v56, 0\n"
    "v_mov_b32 v57, 0\n"
    "v_mov_b32 v58, 0\n"
    "v_mov_b32 v59, 0\n"
    "v_mov_b32 v60, 0\n"
    "v_mov_b32 v61, 0\n"
    "v_mov_b32 v62, 0\n"
    "v_mov_b32 v63, 0\n"
    "v_mov_b32 v64, 0\n"
    "v_mov_b32 v65, 0\n"
    "v_mov_b32 v66, 0\n"
    "v_mov_b32 v67, 0\n"
    "v_mov_b32 v68, 0\n"
    "v_mov_b32 v69, 0\n"
    "v_mov_b32 v70, 0\n"
    "v_mov_b32 v71, 0\n"
    "v_mov_b32 v72, 0\n"
    "v_mov_b32 v73, 0\n"
    "v_mov_b32 v74, 0\n"
    "v_mov_b32 v75, 0\n"
    "v_mov_b32 v76, 0\n"
    "v_mov_b32 v77, 0\n"
    "v_mov_b32 v78, 0\n"
    "v_mov_b32 v79, 0\n"
    "ds_read_b128 v[80:83], v2 offset:0\n"
    "ds_read_b128 v[84:87], v2 offset:1024\n"
    "ds_read_b128 v[88:91], v2 offset:2048\n"
    "ds_read_b128 v[92:95], v2 offset:3072\n"
    "ds_read_b128 v[96:99], v2 offset:4096\n"
    "ds_read_b128 v[100:103], v2 offset:5120\n"
    "s_lshl_b32 s63, s47, 7\n"
    "s_add_u32 s63, s63, 1310720\n"
    "s_waitcnt lgkmcnt(0)\n"
    "s_cmp_lg_u32 s47, 0\n"
    "s_cbranch_scc1 hw_timed_%=\n"
    "s_barrier\n"
    "s_memrealtime s[56:57]\n"
    "s_waitcnt lgkmcnt(0)\n"
    "s_add_u32 s62, s56, s68\n"
    "s_add_u32 s59, s62, s67\n"
    "v_mov_b32 v152, s59\n"
    "s_add_u32 s59, s59, s66\n"
    "v_mov_b32 v153, s59\n"
    "s_nop 1\n"
    "s_branch hw_go_%=\n"
    "hw_timed_%=:\n"
    "s_sub_u32 s55, s47, 1\n"
    "s_lshl_b32 s54, s55, 7\n"
    "s_add_u32 s54, s54, 1310720\n"
    "s_mov_b32 s60, 0\n"
    "hw_rs_%=:\n"
    "buffer_load_dwordx2 v[152:153], v14, s[40:43], s54 offen sc1\n"
    "s_waitcnt vmcnt(0)\n"
    "v_readfirstlane_b32 s55, v152\n"
    "v_readfirstlane_b32 s62, v153\n"
    "s_nop 0\n"
    "s_max_u32 s59, s55, s62\n"
    "s_cmp_lg_u32 s59, -1\n"
    "s_nop 1\n"
    "s_cbranch_scc1 hw_rs_ok_%=\n"
    "s_sleep 4\n"
    "s_add_u32 s60, s60, 1\n"
    "s_cmp_gt_u32 s60, 65536\n"
    "s_cbranch_scc1 hw_abort_%=\n"
    "s_and_b32 s59, s60, 63\n"
    "s_cbranch_scc1 hw_rs_%=\n"
    "v_mov_b32 v152, 1024\n"
    "buffer_load_dword v152, v152, s[40:43], s58 offen sc1\n"
    "s_waitcnt vmcnt(0)\n"
    "v_readfirstlane_b32 s59, v152\n"
    "s_nop 0\n"
    "s_cmp_lg_u32 s59, 0\n"
    "s_nop 2\n"
    "s_cbranch_scc1 hw_end_%=\n"
    "s_branch hw_rs_%=\n"
    "hw_rs_ok_%=:\n"
    "s_add_u32 s59, s62, s67\n"
    "v_mov_b32 v152, s59\n"
    "s_add_u32 s59, s59, s66\n"
    "v_mov_b32 v153, s59\n"
    "s_memrealtime s[56:57]\n"
    "s_waitcnt lgkmcnt(0)\n"
    "s_sub_u32 s59, s62, s56\n"
    "s_cmp_gt_u32 s59, 1000\n"
    "s_cbranch_scc1 hw_wu_go_%=\n"
    "hw_wu_%=:\n"
    "s_memrealtime s[56:57]\n"
    "s_waitcnt lgkmcnt(0)\n"
    "s_sub_u32 s59, s62, s56\n"
    "s_cmp_gt_i32 s59, 15\n"
    "s_cbranch_scc1 hw_wu_%=\n"
    "s_memrealtime s[30:31]\n"
    "s_nop 1\n"
    "s_memrealtime s[90:91]\n"
    "s_nop 1\n"
    "s_memrealtime s[34:35]\n"
    "s_nop 1\n"
    "s_memrealtime s[36:37]\n"
    "s_nop 1\n"
    "s_memrealtime s[38:39]\n"
    "s_nop 1\n"
    "s_memrealtime s[94:95]\n"
    "s_nop 1\n"
    "s_memrealtime s[96:97]\n"
    "s_nop 1\n"
    "s_memrealtime s[98:99]\n"
    "s_nop 1\n"
    "s_getpc_b64 s[56:57]\n"
    "hw_wu_pc_%=:\n"
    "s_waitcnt lgkmcnt(0)\n"
    "s_add_u32 s90, s90, s34\n"
    "s_add_u32 s90, s90, s36\n"
    "s_add_u32 s90, s90, s38\n"
    "s_add_u32 s90, s90, s94\n"
    "s_add_u32 s90, s90, s96\n"
    "s_add_u32 s90, s90, s98\n"
    "s_mul_i32 s34, s30, 7\n"
    "s_sub_u32 s90, s90, s34\n"
    "s_mul_i32 s90, s90, 3\n"
    "s_sub_u32 s59, s62, s30\n"
    "s_mul_i32 s59, s59, 23\n"
    "s_sub_u32 s59, s59, s90\n"
    "s_sub_u32 s59, s59, 170\n"
    "s_max_i32 s59, s59, 0\n"
    "s_min_i32 s59, s59, 252\n"
    "s_andn2_b32 s59, s59, 3\n"
    "s_sub_u32 s59, hw_wu_e_%=-hw_wu_pc_%=, s59\n"
    "s_add_u32 s56, s56, s59\n"
    "s_addc_u32 s57, s57, 0\n"
    "s_setpc_b64 s[56:57]\n"
    "s_nop 0\n"
    "s_nop 0\n"
    "s_nop 0\n"
    "s_nop 0\n"
    "s_nop 0\n"
    "s_nop 0\n"
    "s_nop 0\n"
    "s_nop 0\n"
    "s_nop 0\n"
    "s_nop 0\n"
    "s_nop 0\n"
    "s_nop 0\n"
    "s_nop 0\n"
    "s_nop 0\n"
    "s_nop 0\n"
    "s_nop 0\n"
    "s_nop 0\n"
    "s_nop 0\n"
    "s_nop 0\n"
    "s_nop 0\n"
    "s_nop 0\n"
    "s_nop 0\n"
    "s_nop 0\n"
    "s_nop 0\n"
    "s_nop 0\n"
    "s_nop 0\n"
    "s_nop 0\n"
    "s_nop 0\n"
    "s_nop 0\n"
    "s_nop 0\n"
    "s_nop 0\n"
    "s_nop 0\n"
    "s_nop 0\n"
    "s_nop 0\n"
    "s_nop 0\n"
    "s_nop 0\n"
    "s_nop 0\n"
    "s_nop 0\n"
    "s_nop 0\n"
    "s_nop 0\n"
    "s_nop 0\n"
    "s_nop 0\n"
    "s_nop 0\n"
    "s_nop 0\n"
    "s_nop 0\n"
    "s_nop 0\n"
    "s_nop 0\n"
    "s_nop 0\n"
    "s_nop 0\n"
    "s_nop 0\n"
    "s_nop 0\n"
    "s_nop 0\n"
    "s_nop 0\n"
    "s_nop 0\n"
    "s_nop 0\n"
    "s_nop 0\n"
    "s_nop 0\n"
    "s_nop 0\n"
    "s_nop 0\n"
    "s_nop 0\n"
    "s_nop 0\n"
    "s_nop 0\n"
    "s_nop 0\n"
    "hw_wu_e_%=:\n"
    "hw_wu_go_%=:\n"
    "s_barrier\n"
    "hw_go_%=:\n"
    "s_mov_b32 s60, 0\n"
    "hw_retrya_%=:\n"
    "buffer_load_dwordx4 v[16:19], v158, s[40:43], s20 offen sc1\n"
    "ds_read_b128 v[104:107], v5 offset:0\n"
    "s_nop 6\n"
    "buffer_load_dwordx4 v[20:23], v158, s[40:43], s21 offen sc1\n"
    "ds_read_b128 v[108:111], v5 offset:1024\n"
    "s_nop 6\n"
    "buffer_load_dwordx4 v[24:27], v158, s[40:43], s22 offen sc1\n"
    "ds_read_b128 v[112:115], v5 offset:2048\n"
    "s_nop 6\n"
    "buffer_load_dwordx4 v[28:31], v158, s[40:43], s23 offen sc1\n"
    "ds_read_b128 v[116:119], v5 offset:3072\n"
    "s_nop 6\n"
    "buffer_load_dwordx4 v[32:35], v158, s[40:43], s24 offen sc1\n"
    "ds_read_b128 v[120:123], v5 offset:4096\n"
    "s_nop 6\n"
    "buffer_load_dwordx4 v[36:39], v158, s[40:43], s25 offen sc1\n"
    "ds_read_b128 v[124:127], v5 offset:5120\n"
    "s_nop 6\n"
    "buffer_load_dwordx4 v[40:43], v158, s[40:43], s26 offen sc1\n"
    "ds_read_b128 v[128:131], v5 offset:6144\n"
    "s_nop 6\n"
    "buffer_load_dwordx4 v[44:47], v158, s[40:43], s27 offen sc1\n"
    "ds_read_b128 v[132:135], v5 offset:7168\n"
    "s_nop 6\n"
    "buffer_load_dwordx4 v[48:51], v158, s[40:43], s28 offen sc1\n"
    "s_nop 7\n"
    "buffer_load_dwordx4 v[52:55], v158, s[40:43], s29 offen sc1\n"
    "s_waitcnt lgkmcnt(0)\n"
    "s_waitcnt vmcnt(9)\n"
    "v_smfmac_f32_16x16x64_bf16 v[68:71], v[16:19], v[80:87], v1\n"
    "ds_read_b128 v[80:83], v2 offset:6144\n"
    "v_smfmac_f32_16x16x64_bf16 v[56:59], v[16:19], a[0:7], v1\n"
    "ds_read_b128 v[84:87], v2 offset:7168\n"
    "v_smfmac_f32_16x16x64_bf16 v[64:67], v[16:19], a[8:15], v1\n"
    "v_smfmac_f32_16x16x64_bf16 v[60:63], v[16:19], a[16:23], v1\n"
    "s_waitcnt vmcnt(8)\n"
    "v_smfmac_f32_16x16x64_bf16 v[68:71], v[20:23], v[88:95], v1\n"
    "ds_read_b128 v[88:91], v2 offset:8192\n"
    "v_smfmac_f32_16x16x64_bf16 v[56:59], v[20:23], a[24:31], v1\n"
    "ds_read_b128 v[92:95], v2 offset:9216\n"
    "v_smfmac_f32_16x16x64_bf16 v[64:67], v[20:23], v[96:103], v1\n"
    "ds_read_b128 v[96:99], v2 offset:10240\n"
    "v_smfmac_f32_16x16x64_bf16 v[60:63], v[20:23], a[32:39], v1\n"
    "ds_read_b128 v[100:103], v2 offset:11264\n"
    "s_waitcnt vmcnt(7) lgkmcnt(4)\n"
    "v_smfmac_f32_16x16x64_bf16 v[68:71], v[24:27], v[80:87], v1\n"
    "ds_read_b128 v[80:83], v2 offset:12288\n"
    "v_smfmac_f32_16x16x64_bf16 v[56:59], v[24:27], a[40:47], v1\n"
    "ds_read_b128 v[84:87], v2 offset:13312\n"
    "v_smfmac_f32_16x16x64_bf16 v[64:67], v[24:27], a[48:55], v1\n"
    "v_smfmac_f32_16x16x64_bf16 v[60:63], v[24:27], a[56:63], v1\n"
    "v_max3_u32 v12, v16, v20, v24\n"
    "s_waitcnt vmcnt(6) lgkmcnt(4)\n"
    "v_smfmac_f32_16x16x64_bf16 v[68:71], v[28:31], v[88:95], v1\n"
    "ds_read_b128 v[88:91], v2 offset:14336\n"
    "v_smfmac_f32_16x16x64_bf16 v[56:59], v[28:31], a[64:71], v1\n"
    "ds_read_b128 v[92:95], v2 offset:15360\n"
    "v_smfmac_f32_16x16x64_bf16 v[64:67], v[28:31], a[72:79], v1\n"
    "v_cmp_eq_u32 vcc, -1, v12\n"
    "v_smfmac_f32_16x16x64_bf16 v[60:63], v[28:31], a[80:87], v1\n"
    "s_cbranch_vccnz hw_polla_%=\n"
    "s_waitcnt vmcnt(5) lgkmcnt(4)\n"
    "v_smfmac_f32_16x16x64_bf16 v[68:71], v[32:35], v[96:103], v1\n"
    "ds_read_b128 v[96:99], v2 offset:16384\n"
    "v_smfmac_f32_16x16x64_bf16 v[56:59], v[32:35], a[88:95], v1\n"
    "ds_read_b128 v[100:103], v2 offset:17408\n"
    "s_waitcnt lgkmcnt(4)\n"
    "v_smfmac_f32_16x16x64_bf16 v[64:67], v[32:35], v[80:87], v1\n"
    "ds_read_b128 v[80:83], v2 offset:18432\n"
    "v_smfmac_f32_16x16x64_bf16 v[60:63], v[32:35], a[96:103], v1\n"
    "ds_read_b128 v[84:87], v2 offset:19456\n"
    "s_waitcnt vmcnt(4) lgkmcnt(4)\n"
    "v_smfmac_f32_16x16x64_bf16 v[68:71], v[36:39], v[88:95], v1\n"
    "ds_read_b128 v[88:91], v2 offset:20480\n"
    "v_smfmac_f32_16x16x64_bf16 v[56:59], v[36:39], a[104:111], v1\n"
    "ds_read_b128 v[92:95], v2 offset:21504\n"
    "v_smfmac_f32_16x16x64_bf16 v[64:67], v[36:39], a[112:119], v1\n"
    "v_smfmac_f32_16x16x64_bf16 v[60:63], v[36:39], a[120:127], v1\n"
    "s_waitcnt vmcnt(3) lgkmcnt(4)\n"
    "v_smfmac_f32_16x16x64_bf16 v[68:71], v[40:43], v[96:103], v1\n"
    "ds_read_b128 v[96:99], v2 offset:22528\n"
    "v_smfmac_f32_16x16x64_bf16 v[56:59], v[40:43], a[128:135], v1\n"
    "ds_read_b128 v[100:103], v2 offset:23552\n"
    "v_smfmac_f32_16x16x64_bf16 v[64:67], v[40:43], a[136:143], v1\n"
    "v_smfmac_f32_16x16x64_bf16 v[60:63], v[40:43], a[144:151], v1\n"
    "s_waitcnt vmcnt(2) lgkmcnt(4)\n"
    "v_smfmac_f32_16x16x64_bf16 v[68:71], v[44:47], v[80:87], v1\n"
    "ds_read_b128 v[80:83], v2 offset:24576\n"
    "v_smfmac_f32_16x16x64_bf16 v[56:59], v[44:47], a[152:159], v1\n"
    "ds_read_b128 v[84:87], v2 offset:25600\n"
    "s_waitcnt lgkmcnt(4)\n"
    "v_smfmac_f32_16x16x64_bf16 v[64:67], v[44:47], v[88:95], v1\n"
    "ds_read_b128 v[88:91], v2 offset:26624\n"
    "v_smfmac_f32_16x16x64_bf16 v[60:63], v[44:47], a[160:167], v1\n"
    "ds_read_b128 v[92:95], v2 offset:27648\n"
    "s_waitcnt vmcnt(1) lgkmcnt(4)\n"
    "v_smfmac_f32_16x16x64_bf16 v[68:71], v[48:51], v[96:103], v1\n"
    "ds_read_b128 v[96:99], v2 offset:28672\n"
    "v_smfmac_f32_16x16x64_bf16 v[56:59], v[48:51], a[168:175], v1\n"
    "ds_read_b128 v[100:103], v2 offset:29696\n"
    "v_smfmac_f32_16x16x64_bf16 v[64:67], v[48:51], a[176:183], v1\n"
    "v_smfmac_f32_16x16x64_bf16 v[60:63], v[48:51], a[184:191], v1\n"
    "s_waitcnt vmcnt(0) lgkmcnt(4)\n"
    "v_smfmac_f32_16x16x64_bf16 v[68:71], v[52:55], v[80:87], v1\n"
    "ds_read_b128 v[80:83], v2 offset:30720\n"
    "v_smfmac_f32_16x16x64_bf16 v[56:59], v[52:55], a[192:199], v1\n"
    "ds_read_b128 v[84:87], v2 offset:31744\n"
    "v_smfmac_f32_16x16x64_bf16 v[64:67], v[52:55], a[200:207], v1\n"
    "v_smfmac_f32_16x16x64_bf16 v[60:63], v[52:55], a[208:215], v1\n"
    "v_smfmac_f32_16x16x64_bf16 v[72:75], v[16:19], a[216:223], v1\n"
    "v_mov_b32 v13, v13\n"
    "v_mov_b32 v13, v13\n"
    "v_mov_b32 v13, v13\n"
    "v_mov_b32 v13, v13\n"
    "v_mov_b32 v13, v13\n"
    "v_mov_b32 v13, v13\n"
    "v_mov_b32 v13, v13\n"
    "v_permlane32_swap_b32 v56, v60\n"
    "v_permlane32_swap_b32 v57, v61\n"
    "s_waitcnt lgkmcnt(4)\n"
    "v_smfmac_f32_16x16x64_bf16 v[76:79], v[16:19], v[88:95], v1\n"
    "v_permlane32_swap_b32 v58, v62\n"
    "v_permlane32_swap_b32 v59, v63\n"
    "v_smfmac_f32_16x16x64_bf16 v[72:75], v[20:23], a[224:231], v1\n"
    "v_add_f32 v56, v56, v60\n"
    "v_add_f32 v57, v57, v61\n"
    "s_waitcnt lgkmcnt(2)\n"
    "v_smfmac_f32_16x16x64_bf16 v[76:79], v[20:23], v[96:103], v1\n"
    "v_add_f32 v58, v58, v62\n"
    "v_add_f32 v59, v59, v63\n"
    "v_smfmac_f32_16x16x64_bf16 v[72:75], v[24:27], a[232:239], v1\n"
    "v_cmp_u_f32 vcc, v56, v57\n"
    "v_cmp_u_f32 s[56:57], v58, v59\n"
    "s_waitcnt lgkmcnt(0)\n"
    "v_smfmac_f32_16x16x64_bf16 v[76:79], v[24:27], v[80:87], v1\n"
    "s_or_b64 vcc, vcc, s[56:57]\n"
    "s_cbranch_vccnz hw_faila_%=\n"
    "hw_resumea_%=:\n"
    "buffer_store_dwordx2 v[152:153], v7, s[40:43], s63 offen sc1\n"
    "v_cvt_pk_bf16_f32 v60, v56, v57\n"
    "v_smfmac_f32_16x16x64_bf16 v[72:75], v[28:31], a[240:247], v1\n"
    "v_cvt_pk_bf16_f32 v61, v58, v59\n"
    "ds_write_b64 v3, v[60:61] offset:0\n"
    "v_smfmac_f32_16x16x64_bf16 v[76:79], v[28:31], a[248:255], v1\n"
    "v_permlane32_swap_b32 v64, v68\n"
    "v_permlane32_swap_b32 v65, v69\n"
    "v_smfmac_f32_16x16x64_bf16 v[72:75], v[32:35], v[160:167], v1\n"
    "v_permlane32_swap_b32 v66, v70\n"
    "v_permlane32_swap_b32 v67, v71\n"
    "v_smfmac_f32_16x16x64_bf16 v[76:79], v[32:35], v[168:175], v1\n"
    "v_add_f32 v64, v64, v68\n"
    "v_smfmac_f32_16x16x64_bf16 v[72:75], v[36:39], v[176:183], v1\n"
    "v_add_f32 v65, v65, v69\n"
    "v_smfmac_f32_16x16x64_bf16 v[76:79], v[36:39], v[184:191], v1\n"
    "v_add_f32 v66, v66, v70\n"
    "v_smfmac_f32_16x16x64_bf16 v[72:75], v[40:43], v[192:199], v1\n"
    "v_add_f32 v67, v67, v71\n"
    "v_smfmac_f32_16x16x64_bf16 v[76:79], v[40:43], v[200:207], v1\n"
    "v_cvt_pk_bf16_f32 v68, v64, v65\n"
    "v_smfmac_f32_16x16x64_bf16 v[72:75], v[44:47], v[208:215], v1\n"
    "v_cvt_pk_bf16_f32 v69, v66, v67\n"
    "v_smfmac_f32_16x16x64_bf16 v[76:79], v[44:47], v[216:223], v1\n"
    "ds_write_b64 v3, v[68:69] offset:512\n"
    "v_smfmac_f32_16x16x64_bf16 v[72:75], v[48:51], v[224:231], v1\n"
    "ds_read_b128 v[16:19], v5 offset:8192\n"
    "v_smfmac_f32_16x16x64_bf16 v[76:79], v[48:51], v[232:239], v1\n"
    "ds_read_b128 v[20:23], v5 offset:9216\n"
    "v_smfmac_f32_16x16x64_bf16 v[72:75], v[52:55], v[240:247], v1\n"
    "ds_read_b128 v[24:27], v5 offset:10240\n"
    "v_smfmac_f32_16x16x64_bf16 v[76:79], v[52:55], v[248:255], v1\n"
    "ds_read_b128 v[28:31], v5 offset:11264\n"
    "s_waitcnt lgkmcnt(4)\n"
    "s_barrier\n"
    "ds_read_b64_tr_b16 v[136:137], v4 offset:0\n"
    "ds_read_b64_tr_b16 v[138:139], v4 offset:512\n"
    "v_mov_b32 v13, v13\n"
    "ds_read_b64_tr_b16 v[140:141], v4 offset:1536\n"
    "ds_read_b64_tr_b16 v[142:143], v4 offset:2048\n"
    "v_mov_b32 v13, v13\n"
    "ds_read_b64_tr_b16 v[144:145], v4 offset:3072\n"
    "ds_read_b64_tr_b16 v[146:147], v4 offset:3584\n"
    "v_mov_b32 v13, v13\n"
    "ds_read_b64_tr_b16 v[148:149], v4 offset:4608\n"
    "ds_read_b64_tr_b16 v[150:151], v4 offset:5120\n"
    "v_mov_b32 v13, v13\n"
    "v_permlane32_swap_b32 v72, v76\n"
    "v_permlane32_swap_b32 v73, v77\n"
    "v_permlane32_swap_b32 v74, v78\n"
    "v_permlane32_swap_b32 v75, v79\n"
    "v_pk_add_f32 v[72:73], v[72:73], v[76:77]\n"
    "v_pk_add_f32 v[74:75], v[74:75], v[78:79]\n"
    "v_cvt_pk_bf16_f32 v76, v72, v73\n"
    "v_cvt_pk_bf16_f32 v77, v74, v75\n"
    "ds_write_b64 v3, v[76:77] offset:1024\n"
    "s_waitcnt lgkmcnt(7)\n"
    "v_mfma_f32_16x16x32_bf16 v[56:59], v[104:107], v[136:139], v[8:11]\n"
    "v_mfma_f32_16x16x32_bf16 v[60:63], v[108:111], v[136:139], v[8:11]\n"
    "v_mfma_f32_16x16x32_bf16 v[64:67], v[136:139], v[136:139], 0\n"
    "s_waitcnt lgkmcnt(5)\n"
    "v_mfma_f32_16x16x32_bf16 v[56:59], v[112:115], v[140:143], v[56:59]\n"
    "s_waitcnt lgkmcnt(0)\n"
    "s_barrier\n"
    "ds_read_b64_tr_b16 v[32:33], v4 offset:1024\n"
    "ds_read_b64_tr_b16 v[34:35], v4 offset:2560\n"
    "ds_read_b64_tr_b16 v[36:37], v4 offset:4096\n"
    "ds_read_b64_tr_b16 v[38:39], v4 offset:5632\n"
    "v_mfma_f32_16x16x32_bf16 v[60:63], v[116:119], v[140:143], v[60:63]\n"
    "v_mfma_f32_16x16x32_bf16 v[64:67], v[140:143], v[140:143], v[64:67]\n"
    "v_mfma_f32_16x16x32_bf16 v[56:59], v[120:123], v[144:147], v[56:59]\n"
    "v_mfma_f32_16x16x32_bf16 v[60:63], v[124:127], v[144:147], v[60:63]\n"
    "v_mfma_f32_16x16x32_bf16 v[64:67], v[144:147], v[144:147], v[64:67]\n"
    "v_mfma_f32_16x16x32_bf16 v[56:59], v[128:131], v[148:151], v[56:59]\n"
    "v_mfma_f32_16x16x32_bf16 v[60:63], v[132:135], v[148:151], v[60:63]\n"
    "v_mfma_f32_16x16x32_bf16 v[64:67], v[148:151], v[148:151], v[64:67]\n"
    "s_waitcnt lgkmcnt(2)\n"
    "v_mfma_f32_16x16x32_bf16 v[64:67], v[32:35], v[32:35], v[64:67]\n"
    "s_waitcnt lgkmcnt(0)\n"
    "v_mfma_f32_16x16x32_bf16 v[64:67], v[36:39], v[36:39], v[64:67]\n"
    "v_mfma_f32_16x16x32_bf16 v[56:59], v[16:19], v[32:35], v[56:59]\n"
    "v_mfma_f32_16x16x32_bf16 v[56:59], v[24:27], v[36:39], v[56:59]\n"
    "v_mfma_f32_16x16x32_bf16 v[60:63], v[20:23], v[32:35], v[60:63]\n"
    "v_mfma_f32_16x16x32_bf16 v[60:63], v[28:31], v[36:39], v[60:63]\n"
    "v_mov_b32 v13, v13\n"
    "v_mov_b32 v13, v13\n"
    "v_mov_b32 v13, v13\n"
    "v_mov_b32 v13, v13\n"
    "v_permlane16_swap_b32 v64, v72\n"
    "v_permlane16_swap_b32 v65, v73\n"
    "v_permlane16_swap_b32 v66, v74\n"
    "v_permlane16_swap_b32 v67, v75\n"
    "v_mov_b32_dpp v56, v64 row_shr:8 row_mask:0x4 bank_mask:0xc\n"
    "v_mov_b32_dpp v57, v65 row_shr:8 row_mask:0x4 bank_mask:0xc\n"
    "v_mov_b32_dpp v58, v66 row_shr:8 row_mask:0x4 bank_mask:0xc\n"
    "v_mov_b32_dpp v59, v67 row_shr:8 row_mask:0x4 bank_mask:0xc\n"
    "v_mov_b32_dpp v60, v72 row_shr:8 row_mask:0x4 bank_mask:0xc\n"
    "v_mov_b32_dpp v61, v73 row_shr:8 row_mask:0x4 bank_mask:0xc\n"
    "v_mov_b32_dpp v62, v74 row_shr:8 row_mask:0x4 bank_mask:0xc\n"
    "v_mov_b32_dpp v63, v75 row_shr:8 row_mask:0x4 bank_mask:0xc\n"
    "v_max3_f32 v68, v60, v61, v62\n"
    "v_max3_f32 v69, v56, v57, v58\n"
    "v_max3_f32 v68, v68, v63, v59\n"
    "v_max_f32 v68, v68, v69\n"
    "s_nop 1\n"
    "v_max_f32_dpp v68, v68, v68 row_ror:8 row_mask:0xf bank_mask:0xf\n"
    "v_mov_b32 v69, v68\n"
    "s_nop 1\n"
    "v_permlane16_swap_b32 v68, v69\n"
    "v_max_f32 v68, v68, v69\n"
    "v_mov_b32 v69, v68\n"
    "s_nop 1\n"
    "v_permlane32_swap_b32 v68, v69\n"
    "v_max_f32 v68, v68, v69\n"
    "v_mul_f32 v68, 0xbdd53b95, v68\n"
    "v_pk_fma_f32 v[56:57], v[56:57], s[64:65], v[68:69] op_sel_hi:[1,1,0]\n"
    "v_pk_fma_f32 v[58:59], v[58:59], s[64:65], v[68:69] op_sel_hi:[1,1,0]\n"
    "v_pk_fma_f32 v[60:61], v[60:61], s[64:65], v[68:69] op_sel_hi:[1,1,0]\n"
    "v_pk_fma_f32 v[62:63], v[62:63], s[64:65], v[68:69] op_sel_hi:[1,1,0]\n"
    "v_exp_f32 v56, v56\n"
    "v_exp_f32 v57, v57\n"
    "v_exp_f32 v58, v58\n"
    "v_exp_f32 v59, v59\n"
    "v_exp_f32 v60, v60\n"
    "v_exp_f32 v61, v61\n"
    "v_exp_f32 v62, v62\n"
    "v_exp_f32 v63, v63\n"
    "v_pk_add_f32 v[64:65], v[56:57], v[58:59]\n"
    "v_pk_add_f32 v[66:67], v[60:61], v[62:63]\n"
    "v_pk_add_f32 v[64:65], v[64:65], v[66:67]\n"
    "v_add_f32 v68, v64, v65\n"
    "s_nop 1\n"
    "v_add_f32_dpp v68, v68, v68 row_ror:8 row_mask:0xf bank_mask:0xf\n"
    "v_mov_b32 v69, v68\n"
    "s_nop 1\n"
    "v_permlane16_swap_b32 v68, v69\n"
    "v_add_f32 v68, v68, v69\n"
    "v_mov_b32 v69, v68\n"
    "s_nop 1\n"
    "v_permlane32_swap_b32 v68, v69\n"
    "v_add_f32 v68, v68, v69\n"
    "v_rcp_f32 v68, v68\n"
    "s_nop 0\n"
    "v_pk_mul_f32 v[56:57], v[56:57], v[68:69] op_sel_hi:[1,0]\n"
    "v_pk_mul_f32 v[58:59], v[58:59], v[68:69] op_sel_hi:[1,0]\n"
    "v_pk_mul_f32 v[60:61], v[60:61], v[68:69] op_sel_hi:[1,0]\n"
    "v_pk_mul_f32 v[62:63], v[62:63], v[68:69] op_sel_hi:[1,0]\n"
    "v_cvt_pk_bf16_f32 v64, v56, v57\n"
    "v_cvt_pk_bf16_f32 v65, v58, v59\n"
    "v_cvt_pk_bf16_f32 v66, v60, v61\n"
    "v_cvt_pk_bf16_f32 v67, v62, v63\n"
    "buffer_store_dwordx4 v[64:67], v6, s[40:43], s53 offen sc1\n"
    "buffer_store_dwordx4 v[64:67], v6, s[40:43], s50 offen sc1\n"
    "buffer_store_dwordx4 v[64:67], v6, s[40:43], s51 offen sc1\n"
    "buffer_store_dwordx4 v[64:67], v6, s[40:43], s52 offen sc1\n"
    "v_add_u32 v3, s61, v3\n"
    "v_add_u32 v4, s61, v4\n"
    "s_sub_u32 s61, 0, s61\n"
    "s_add_u32 s47, s47, 1\n"
    "s_cmp_lt_u32 s47, 64\n"
    "s_cbranch_scc1 hw_loopa_%=\n"
    "s_branch hw_end_%=\n"
    "hw_faila_%=:\n"
    "v_max_u32 v12, v16, v17\n"
    "v_max3_u32 v12, v18, v19, v12\n"
    "v_max3_u32 v12, v20, v21, v12\n"
    "v_max3_u32 v12, v22, v23, v12\n"
    "v_max3_u32 v12, v24, v25, v12\n"
    "v_max3_u32 v12, v26, v27, v12\n"
    "v_max3_u32 v12, v28, v29, v12\n"
    "v_max3_u32 v12, v30, v31, v12\n"
    "v_max3_u32 v12, v32, v33, v12\n"
    "v_max3_u32 v12, v34, v35, v12\n"
    "v_max3_u32 v12, v36, v37, v12\n"
    "v_max3_u32 v12, v38, v39, v12\n"
    "v_max3_u32 v12, v40, v41, v12\n"
    "v_max3_u32 v12, v42, v43, v12\n"
    "v_max3_u32 v12, v44, v45, v12\n"
    "v_max3_u32 v12, v46, v47, v12\n"
    "v_max3_u32 v12, v48, v49, v12\n"
    "v_max3_u32 v12, v50, v51, v12\n"
    "v_max3_u32 v12, v52, v53, v12\n"
    "v_max3_u32 v12, v54, v55, v12\n"
    "v_cmp_eq_u32 vcc, -1, v12\n"
    "s_nop 4\n"
    "s_cbranch_vccz hw_resumea_%=\n"
    "hw_polla_%=:\n"
    "s_nop 7\n"
    "s_waitcnt lgkmcnt(0)\n"
    "v_mov_b32 v56, 0\n"
    "v_mov_b32 v57, 0\n"
    "v_mov_b32 v58, 0\n"
    "v_mov_b32 v59, 0\n"
    "v_mov_b32 v60, 0\n"
    "v_mov_b32 v61, 0\n"
    "v_mov_b32 v62, 0\n"
    "v_mov_b32 v63, 0\n"
    "v_mov_b32 v64, 0\n"
    "v_mov_b32 v65, 0\n"
    "v_mov_b32 v66, 0\n"
    "v_mov_b32 v67, 0\n"
    "v_mov_b32 v68, 0\n"
    "v_mov_b32 v69, 0\n"
    "v_mov_b32 v70, 0\n"
    "v_mov_b32 v71, 0\n"
    "v_mov_b32 v72, 0\n"
    "v_mov_b32 v73, 0\n"
    "v_mov_b32 v74, 0\n"
    "v_mov_b32 v75, 0\n"
    "v_mov_b32 v76, 0\n"
    "v_mov_b32 v77, 0\n"
    "v_mov_b32 v78, 0\n"
    "v_mov_b32 v79, 0\n"
    "ds_read_b128 v[80:83], v2 offset:0\n"
    "ds_read_b128 v[84:87], v2 offset:1024\n"
    "ds_read_b128 v[88:91], v2 offset:2048\n"
    "ds_read_b128 v[92:95], v2 offset:3072\n"
    "ds_read_b128 v[96:99], v2 offset:4096\n"
    "ds_read_b128 v[100:103], v2 offset:5120\n"
    "v_sub_u32 v12, v158, v0\n"
    "v_lshl_add_u32 v12, v15, 7, v12\n"
    "v_and_b32 v159, 15, v15\n"
    "v_sub_u32 v159, v159, v15\n"
    "v_lshl_add_u32 v159, v159, 7, v12\n"
    "s_movk_i32 s55, 0x4000\n"
    "s_waitcnt vmcnt(0) lgkmcnt(0)\n"
    "s_add_u32 s60, s60, 1\n"
    "s_cmp_gt_u32 s60, 65536\n"
    "s_cbranch_scc1 hw_abort_%=\n"
    "s_and_b32 s59, s60, 63\n"
    "s_cbranch_scc1 hw_againa_%=\n"
    "v_mov_b32 v13, 1024\n"
    "buffer_load_dword v13, v13, s[40:43], s58 offen sc1\n"
    "s_waitcnt vmcnt(0)\n"
    "v_readfirstlane_b32 s59, v13\n"
    "s_nop 0\n"
    "s_cmp_lg_u32 s59, 0\n"
    "s_nop 2\n"
    "s_cbranch_scc1 hw_end_%=\n"
    "s_branch hw_againa_%=\n"
    "hw_againa_%=:\n"
    "buffer_load_dword v16, v12, s[40:43], 0 offen sc1\n"
    "buffer_load_dword v17, v159, s[40:43], s55 offen sc1\n"
    "s_waitcnt vmcnt(0)\n"
    "v_max_u32 v16, v16, v17\n"
    "v_cmp_eq_u32 vcc, -1, v16\n"
    "s_nop 4\n"
    "s_cbranch_vccz hw_relocka_%=\n"
    "s_add_u32 s60, s60, 1\n"
    "s_cmp_gt_u32 s60, 65536\n"
    "s_cbranch_scc1 hw_abort_%=\n"
    "s_and_b32 s59, s60, 63\n"
    "s_cbranch_scc1 hw_againa_%=\n"
    "v_mov_b32 v13, 1024\n"
    "buffer_load_dword v13, v13, s[40:43], s58 offen sc1\n"
    "s_waitcnt vmcnt(0)\n"
    "v_readfirstlane_b32 s59, v13\n"
    "s_nop 0\n"
    "s_cmp_lg_u32 s59, 0\n"
    "s_nop 2\n"
    "s_cbranch_scc1 hw_end_%=\n"
    "s_branch hw_againa_%=\n"
    "hw_relocka_%=:\n"
    "s_memrealtime s[56:57]\n"
    "s_waitcnt lgkmcnt(0)\n"
    "s_cmp_lt_u32 s47, s75\n"
    "s_cbranch_scc1 hw_nobump_%=\n"
    "s_add_u32 s67, s67, s71\n"
    "s_add_u32 s66, s66, s72\n"
    "hw_nobump_%=:\n"
    "s_add_u32 s75, s47, 2\n"
    "s_max_u32 s75, s75, 6\n"
    "s_add_u32 s62, s56, s68\n"
    "s_add_u32 s59, s62, s67\n"
    "v_mov_b32 v152, s59\n"
    "s_add_u32 s59, s59, s66\n"
    "v_mov_b32 v153, s59\n"
    "s_nop 1\n"
    "s_branch hw_retrya_%=\n"
    "hw_wx_%=:\n"
    "v_lshrrev_b32 v6, 3, v15\n"
    "v_lshlrev_b32 v6, 8, v6\n"
    "v_add_u32 v6, s50, v6\n"
    "v_mov_b32 v13, s51\n"
    "v_cmp_gt_u32 vcc, 32, v15\n"
    "s_nop 1\n"
    "v_cndmask_b32 v6, v13, v6, vcc\n"
    "v_lshl_add_u32 v6, v153, 4, v6\n"
    "v_and_b32 v13, 0xffffe000, v6\n"
    "v_add_u32 v6, v6, v13\n"
    "v_cmp_gt_u32 vcc, 40, v15\n"
    "v_mov_b32 v13, 2147483632\n"
    "s_nop 0\n"
    "v_cndmask_b32 v6, v13, v6, vcc\n"
    "v_lshlrev_b32 v7, 7, v152\n"
    "v_lshl_add_u32 v7, v153, 4, v7\n"
    "v_cmp_eq_u32 vcc, 1, v154\n"
    "v_mov_b32 v13, 2147483632\n"
    "s_nop 0\n"
    "v_cndmask_b32 v7, v13, v7, vcc\n"
    "v_mov_b32 v14, 0\n"
    "s_lshl_b32 s48, s44, 7\n"
    "s_add_u32 s49, s48, 2048\n"
    "v_mov_b32 v104, s49\n"
    "s_add_u32 s49, s48, 14336\n"
    "v_mov_b32 v116, s49\n"
    "s_xor_b32 s49, s45, 1\n"
    "v_or_b32 v13, s49, v15\n"
    "v_cmp_eq_u32 vcc, 0, v13\n"
    "v_mov_b32 v13, 2147483632\n"
    "s_nop 0\n"
    "v_cndmask_b32 v116, v13, v116, vcc\n"
    "s_mov_b32 s48, 0\n"
    "s_cmp_eq_u32 s45, 2\n"
    "s_cselect_b32 s48, 2056, s48\n"
    "s_cmp_eq_u32 s45, 3\n"
    "s_cselect_b32 s48, 10240, s48\n"
    "v_add_u32 v105, s48, v5\n"
    "s_mov_b32 s48, 8\n"
    "s_cmp_eq_u32 s45, 2\n"
    "s_cselect_b32 s48, 8200, s48\n"
    "s_cmp_eq_u32 s45, 3\n"
    "s_cselect_b32 s48, 6144, s48\n"
    "v_add_u32 v106, s48, v5\n"
    "s_mov_b32 s48, 8192\n"
    "s_cmp_eq_u32 s45, 2\n"
    "s_cselect_b32 s48, 4096, s48\n"
    "s_cmp_eq_u32 s45, 3\n"
    "s_cselect_b32 s48, 6152, s48\n"
    "v_add_u32 v107, s48, v5\n"
    "s_mov_b32 s48, 2048\n"
    "s_cmp_eq_u32 s45, 2\n"
    "s_cselect_b32 s48, 4104, s48\n"
    "s_cmp_eq_u32 s45, 3\n"
    "s_cselect_b32 s48, 10248, s48\n"
    "v_add_u32 v108, s48, v5\n"
    "v_mov_b32 v112, -1\n"
    "v_mov_b32 v113, -1\n"
    "v_mov_b32 v114, -1\n"
    "v_mov_b32 v115, -1\n"
    "s_mul_i32 s62, s44, 12288\n"
    "s_lshl_b32 s49, s45, 12\n"
    "s_add_u32 s62, s62, s49\n"
    "s_add_u32 s62, s62, 62451712\n"
    "s_mov_b32 s55, 1\n"
    "s_mov_b32 s60, 0\n"
    "v_cmp_gt_u32 vcc, s55, v14\n"
    "s_nop 4\n"
    "s_cbranch_vccz hw_kx0_ok_%=\n"
    "hw_kx0_poll_%=:\n"
    "buffer_load_dword v14, v104, s[40:43], s58 offen sc1\n"
    "s_waitcnt vmcnt(0)\n"
    "v_cmp_gt_u32 vcc, s55, v14\n"
    "s_nop 4\n"
    "s_cbranch_vccz hw_kx0_ok_%=\n"
    "s_sleep 2\n"
    "s_add_u32 s60, s60, 1\n"
    "s_cmp_gt_u32 s60, 65536\n"
    "s_cbranch_scc1 hw_abort_%=\n"
    "s_and_b32 s59, s60, 63\n"
    "s_cbranch_scc1 hw_kx0_poll_%=\n"
    "v_mov_b32 v13, 1024\n"
    "buffer_load_dword v13, v13, s[40:43], s58 offen sc1\n"
    "s_waitcnt vmcnt(0)\n"
    "v_readfirstlane_b32 s59, v13\n"
    "s_nop 0\n"
    "s_cmp_lg_u32 s59, 0\n"
    "s_nop 2\n"
    "s_cbranch_scc1 hw_end_%=\n"
    "s_branch hw_kx0_poll_%=\n"
    "hw_kx0_ok_%=:\n"
    "buffer_load_dwordx4 v[136:139], v0, s[40:43], s62 offen sc1\n"
    "buffer_load_dwordx4 v[140:143], v0, s[40:43], s62 offen offset:1024 sc1\n"
    "buffer_load_dwordx4 v[144:147], v0, s[40:43], s62 offen offset:2048 sc1\n"
    "buffer_load_dwordx4 v[148:151], v0, s[40:43], s62 offen offset:3072 sc1\n"
    "s_waitcnt vmcnt(3)\n"
    "ds_write2st64_b64 v105, v[136:137], v[138:139] offset1:2\n"
    "s_waitcnt vmcnt(2)\n"
    "ds_write2st64_b64 v106, v[140:141], v[142:143] offset1:2\n"
    "s_waitcnt vmcnt(1)\n"
    "ds_write2st64_b64 v107, v[144:145], v[146:147] offset1:2\n"
    "s_waitcnt vmcnt(0)\n"
    "ds_write2st64_b64 v108, v[148:149], v[150:151] offset1:2\n"
    "hw_loopb_%=:\n"
    "s_add_u32 s62, s62, 393216\n"
    "s_add_u32 s55, s47, 2\n"
    "s_min_u32 s55, s55, 64\n"
    "s_mov_b32 s60, 0\n"
    "v_cmp_gt_u32 vcc, s55, v14\n"
    "s_nop 4\n"
    "s_cbranch_vccz hw_kx1_ok_%=\n"
    "hw_kx1_poll_%=:\n"
    "buffer_load_dword v14, v104, s[40:43], s58 offen sc1\n"
    "s_waitcnt vmcnt(0)\n"
    "v_cmp_gt_u32 vcc, s55, v14\n"
    "s_nop 4\n"
    "s_cbranch_vccz hw_kx1_ok_%=\n"
    "s_sleep 2\n"
    "s_add_u32 s60, s60, 1\n"
    "s_cmp_gt_u32 s60, 65536\n"
    "s_cbranch_scc1 hw_abort_%=\n"
    "s_and_b32 s59, s60, 63\n"
    "s_cbranch_scc1 hw_kx1_poll_%=\n"
    "v_mov_b32 v13, 1024\n"
    "buffer_load_dword v13, v13, s[40:43], s58 offen sc1\n"
    "s_waitcnt vmcnt(0)\n"
    "v_readfirstlane_b32 s59, v13\n"
    "s_nop 0\n"
    "s_cmp_lg_u32 s59, 0\n"
    "s_nop 2\n"
    "s_cbranch_scc1 hw_end_%=\n"
    "s_branch hw_kx1_poll_%=\n"
    "hw_kx1_ok_%=:\n"
    "s_and_b32 s48, s47, 3\n"
    "s_mul_i32 s53, s48, 196608\n"
    "s_lshl_b32 s50, s48, 2\n"
    "s_add_u32 s50, s50, s46\n"
    "s_mul_i32 s50, s50, 32768\n"
    "s_add_u32 s50, s50, 786432\n"
    "v_add_u32 v158, s50, v0\n"
    "s_add_u32 s50, s53, 49152\n"
    "s_add_u32 s51, s53, 98304\n"
    "s_add_u32 s52, s53, 147456\n"
    "v_mov_b32 v56, 0\n"
    "v_mov_b32 v57, 0\n"
    "v_mov_b32 v58, 0\n"
    "v_mov_b32 v59, 0\n"
    "v_mov_b32 v60, 0\n"
    "v_mov_b32 v61, 0\n"
    "v_mov_b32 v62, 0\n"
    "v_mov_b32 v63, 0\n"
    "v_mov_b32 v64, 0\n"
    "v_mov_b32 v65, 0\n"
    "v_mov_b32 v66, 0\n"
    "v_mov_b32 v67, 0\n"
    "v_mov_b32 v68, 0\n"
    "v_mov_b32 v69, 0\n"
    "v_mov_b32 v70, 0\n"
    "v_mov_b32 v71, 0\n"
    "v_mov_b32 v72, 0\n"
    "v_mov_b32 v73, 0\n"
    "v_mov_b32 v74, 0\n"
    "v_mov_b32 v75, 0\n"
    "v_mov_b32 v76, 0\n"
    "v_mov_b32 v77, 0\n"
    "v_mov_b32 v78, 0\n"
    "v_mov_b32 v79, 0\n"
    "ds_read_b128 v[80:83], v2 offset:0\n"
    "ds_read_b128 v[84:87], v2 offset:1024\n"
    "ds_read_b128 v[88:91], v2 offset:2048\n"
    "ds_read_b128 v[92:95], v2 offset:3072\n"
    "ds_read_b128 v[96:99], v2 offset:4096\n"
    "ds_read_b128 v[100:103], v2 offset:5120\n"
    "v_mov_b32 v117, s47\n"
    "s_waitcnt lgkmcnt(0)\n"
    "s_barrier\n"
    "s_mov_b32 s60, 0\n"
    "hw_retryb_%=:\n"
    "buffer_load_dwordx4 v[16:19], v158, s[40:43], s20 offen sc1\n"
    "s_nop 7\n"
    "buffer_load_dwordx4 v[20:23], v158, s[40:43], s21 offen sc1\n"
    "s_nop 7\n"
    "buffer_load_dwordx4 v[24:27], v158, s[40:43], s22 offen sc1\n"
    "s_nop 7\n"
    "buffer_load_dwordx4 v[28:31], v158, s[40:43], s23 offen sc1\n"
    "s_nop 7\n"
    "buffer_load_dwordx4 v[32:35], v158, s[40:43], s24 offen sc1\n"
    "s_nop 7\n"
    "buffer_load_dwordx4 v[36:39], v158, s[40:43], s25 offen sc1\n"
    "s_nop 7\n"
    "buffer_load_dwordx4 v[40:43], v158, s[40:43], s26 offen sc1\n"
    "s_nop 7\n"
    "buffer_load_dwordx4 v[44:47], v158, s[40:43], s27 offen sc1\n"
    "s_nop 7\n"
    "buffer_load_dwordx4 v[48:51], v158, s[40:43], s28 offen sc1\n"
    "s_nop 7\n"
    "buffer_load_dwordx4 v[52:55], v158, s[40:43], s29 offen sc1\n"
    "s_waitcnt vmcnt(9)\n"
    "v_smfmac_f32_16x16x64_bf16 v[68:71], v[16:19], v[80:87], v1\n"
    "ds_read_b128 v[80:83], v2 offset:6144\n"
    "v_smfmac_f32_16x16x64_bf16 v[56:59], v[16:19], a[0:7], v1\n"
    "ds_read_b128 v[84:87], v2 offset:7168\n"
    "v_smfmac_f32_16x16x64_bf16 v[64:67], v[16:19], a[8:15], v1\n"
    "v_smfmac_f32_16x16x64_bf16 v[60:63], v[16:19], a[16:23], v1\n"
    "s_waitcnt vmcnt(8)\n"
    "v_smfmac_f32_16x16x64_bf16 v[68:71], v[20:23], v[88:95], v1\n"
    "ds_read_b128 v[88:91], v2 offset:8192\n"
    "v_smfmac_f32_16x16x64_bf16 v[56:59], v[20:23], a[24:31], v1\n"
    "ds_read_b128 v[92:95], v2 offset:9216\n"
    "v_smfmac_f32_16x16x64_bf16 v[64:67], v[20:23], v[96:103], v1\n"
    "ds_read_b128 v[96:99], v2 offset:10240\n"
    "v_smfmac_f32_16x16x64_bf16 v[60:63], v[20:23], a[32:39], v1\n"
    "ds_read_b128 v[100:103], v2 offset:11264\n"
    "s_waitcnt vmcnt(7) lgkmcnt(4)\n"
    "v_smfmac_f32_16x16x64_bf16 v[68:71], v[24:27], v[80:87], v1\n"
    "ds_read_b128 v[80:83], v2 offset:12288\n"
    "v_smfmac_f32_16x16x64_bf16 v[56:59], v[24:27], a[40:47], v1\n"
    "ds_read_b128 v[84:87], v2 offset:13312\n"
    "v_smfmac_f32_16x16x64_bf16 v[64:67], v[24:27], a[48:55], v1\n"
    "v_smfmac_f32_16x16x64_bf16 v[60:63], v[24:27], a[56:63], v1\n"
    "v_max3_u32 v12, v16, v20, v24\n"
    "s_waitcnt vmcnt(6) lgkmcnt(4)\n"
    "v_smfmac_f32_16x16x64_bf16 v[68:71], v[28:31], v[88:95], v1\n"
    "ds_read_b128 v[88:91], v2 offset:14336\n"
    "v_smfmac_f32_16x16x64_bf16 v[56:59], v[28:31], a[64:71], v1\n"
    "ds_read_b128 v[92:95], v2 offset:15360\n"
    "v_smfmac_f32_16x16x64_bf16 v[64:67], v[28:31], a[72:79], v1\n"
    "v_cmp_eq_u32 vcc, -1, v12\n"
    "v_smfmac_f32_16x16x64_bf16 v[60:63], v[28:31], a[80:87], v1\n"
    "s_cbranch_vccnz hw_pollb_%=\n"
    "s_waitcnt vmcnt(5) lgkmcnt(4)\n"
    "v_smfmac_f32_16x16x64_bf16 v[68:71], v[32:35], v[96:103], v1\n"
    "ds_read_b128 v[96:99], v2 offset:16384\n"
    "v_smfmac_f32_16x16x64_bf16 v[56:59], v[32:35], a[88:95], v1\n"
    "ds_read_b128 v[100:103], v2 offset:17408\n"
    "s_waitcnt lgkmcnt(4)\n"
    "v_smfmac_f32_16x16x64_bf16 v[64:67], v[32:35], v[80:87], v1\n"
    "ds_read_b128 v[80:83], v2 offset:18432\n"
    "v_smfmac_f32_16x16x64_bf16 v[60:63], v[32:35], a[96:103], v1\n"
    "ds_read_b128 v[84:87], v2 offset:19456\n"
    "s_waitcnt vmcnt(4) lgkmcnt(4)\n"
    "v_smfmac_f32_16x16x64_bf16 v[68:71], v[36:39], v[88:95], v1\n"
    "ds_read_b128 v[88:91], v2 offset:20480\n"
    "v_smfmac_f32_16x16x64_bf16 v[56:59], v[36:39], a[104:111], v1\n"
    "ds_read_b128 v[92:95], v2 offset:21504\n"
    "v_smfmac_f32_16x16x64_bf16 v[64:67], v[36:39], a[112:119], v1\n"
    "buffer_load_dwordx4 v[136:139], v0, s[40:43], s62 offen offset:0 sc1\n"
    "v_smfmac_f32_16x16x64_bf16 v[60:63], v[36:39], a[120:127], v1\n"
    "buffer_load_dwordx4 v[140:143], v0, s[40:43], s62 offen offset:1024 sc1\n"
    "s_waitcnt vmcnt(5) lgkmcnt(4)\n"
    "v_smfmac_f32_16x16x64_bf16 v[68:71], v[40:43], v[96:103], v1\n"
    "ds_read_b128 v[96:99], v2 offset:22528\n"
    "v_smfmac_f32_16x16x64_bf16 v[56:59], v[40:43], a[128:135], v1\n"
    "ds_read_b128 v[100:103], v2 offset:23552\n"
    "v_smfmac_f32_16x16x64_bf16 v[64:67], v[40:43], a[136:143], v1\n"
    "buffer_load_dwordx4 v[144:147], v0, s[40:43], s62 offen offset:2048 sc1\n"
    "v_smfmac_f32_16x16x64_bf16 v[60:63], v[40:43], a[144:151], v1\n"
    "buffer_load_dwordx4 v[148:151], v0, s[40:43], s62 offen offset:3072 sc1\n"
    "s_waitcnt vmcnt(6) lgkmcnt(4)\n"
    "v_smfmac_f32_16x16x64_bf16 v[68:71], v[44:47], v[80:87], v1\n"
    "ds_read_b128 v[80:83], v2 offset:24576\n"
    "v_smfmac_f32_16x16x64_bf16 v[56:59], v[44:47], a[152:159], v1\n"
    "ds_read_b128 v[84:87], v2 offset:25600\n"
    "s_waitcnt lgkmcnt(4)\n"
    "v_smfmac_f32_16x16x64_bf16 v[64:67], v[44:47], v[88:95], v1\n"
    "ds_read_b128 v[88:91], v2 offset:26624\n"
    "v_smfmac_f32_16x16x64_bf16 v[60:63], v[44:47], a[160:167], v1\n"
    "ds_read_b128 v[92:95], v2 offset:27648\n"
    "s_waitcnt vmcnt(5) lgkmcnt(4)\n"
    "v_smfmac_f32_16x16x64_bf16 v[68:71], v[48:51], v[96:103], v1\n"
    "ds_read_b128 v[96:99], v2 offset:28672\n"
    "v_smfmac_f32_16x16x64_bf16 v[56:59], v[48:51], a[168:175], v1\n"
    "ds_read_b128 v[100:103], v2 offset:29696\n"
    "v_smfmac_f32_16x16x64_bf16 v[64:67], v[48:51], a[176:183], v1\n"
    "buffer_load_dword v14, v104, s[40:43], s58 offen sc1\n"
    "v_smfmac_f32_16x16x64_bf16 v[60:63], v[48:51], a[184:191], v1\n"
    "s_waitcnt vmcnt(5) lgkmcnt(4)\n"
    "v_smfmac_f32_16x16x64_bf16 v[68:71], v[52:55], v[80:87], v1\n"
    "ds_read_b128 v[80:83], v2 offset:30720\n"
    "v_smfmac_f32_16x16x64_bf16 v[56:59], v[52:55], a[192:199], v1\n"
    "ds_read_b128 v[84:87], v2 offset:31744\n"
    "v_smfmac_f32_16x16x64_bf16 v[64:67], v[52:55], a[200:207], v1\n"
    "v_smfmac_f32_16x16x64_bf16 v[60:63], v[52:55], a[208:215], v1\n"
    "v_smfmac_f32_16x16x64_bf16 v[72:75], v[16:19], a[216:223], v1\n"
    "v_mov_b32 v13, v13\n"
    "v_mov_b32 v13, v13\n"
    "v_mov_b32 v13, v13\n"
    "v_mov_b32 v13, v13\n"
    "v_mov_b32 v13, v13\n"
    "v_mov_b32 v13, v13\n"
    "v_mov_b32 v13, v13\n"
    "v_permlane32_swap_b32 v56, v60\n"
    "v_permlane32_swap_b32 v57, v61\n"
    "s_waitcnt lgkmcnt(4)\n"
    "v_smfmac_f32_16x16x64_bf16 v[76:79], v[16:19], v[88:95], v1\n"
    "v_permlane32_swap_b32 v58, v62\n"
    "v_permlane32_swap_b32 v59, v63\n"
    "v_smfmac_f32_16x16x64_bf16 v[72:75], v[20:23], a[224:231], v1\n"
    "v_add_f32 v56, v56, v60\n"
    "v_add_f32 v57, v57, v61\n"
    "s_waitcnt lgkmcnt(2)\n"
    "v_smfmac_f32_16x16x64_bf16 v[76:79], v[20:23], v[96:103], v1\n"
    "v_add_f32 v58, v58, v62\n"
    "v_add_f32 v59, v59, v63\n"
    "v_smfmac_f32_16x16x64_bf16 v[72:75], v[24:27], a[232:239], v1\n"
    "v_cmp_u_f32 vcc, v56, v57\n"
    "v_cmp_u_f32 s[56:57], v58, v59\n"
    "s_waitcnt lgkmcnt(0)\n"
    "v_smfmac_f32_16x16x64_bf16 v[76:79], v[24:27], v[80:87], v1\n"
    "s_or_b64 vcc, vcc, s[56:57]\n"
    "s_cbranch_vccnz hw_failb_%=\n"
    "hw_resumeb_%=:\n"
    "v_cvt_pk_bf16_f32 v60, v56, v57\n"
    "v_smfmac_f32_16x16x64_bf16 v[72:75], v[28:31], a[240:247], v1\n"
    "v_cvt_pk_bf16_f32 v61, v58, v59\n"
    "ds_write_b64 v3, v[60:61] offset:0\n"
    "v_smfmac_f32_16x16x64_bf16 v[76:79], v[28:31], a[248:255], v1\n"
    "v_permlane32_swap_b32 v64, v68\n"
    "v_permlane32_swap_b32 v65, v69\n"
    "v_smfmac_f32_16x16x64_bf16 v[72:75], v[32:35], v[160:167], v1\n"
    "v_permlane32_swap_b32 v66, v70\n"
    "v_permlane32_swap_b32 v67, v71\n"
    "v_smfmac_f32_16x16x64_bf16 v[76:79], v[32:35], v[168:175], v1\n"
    "v_add_f32 v64, v64, v68\n"
    "v_smfmac_f32_16x16x64_bf16 v[72:75], v[36:39], v[176:183], v1\n"
    "v_add_f32 v65, v65, v69\n"
    "v_smfmac_f32_16x16x64_bf16 v[76:79], v[36:39], v[184:191], v1\n"
    "v_add_f32 v66, v66, v70\n"
    "v_smfmac_f32_16x16x64_bf16 v[72:75], v[40:43], v[192:199], v1\n"
    "v_add_f32 v67, v67, v71\n"
    "v_smfmac_f32_16x16x64_bf16 v[76:79], v[40:43], v[200:207], v1\n"
    "v_cvt_pk_bf16_f32 v68, v64, v65\n"
    "v_smfmac_f32_16x16x64_bf16 v[72:75], v[44:47], v[208:215], v1\n"
    "v_cvt_pk_bf16_f32 v69, v66, v67\n"
    "v_smfmac_f32_16x16x64_bf16 v[76:79], v[44:47], v[216:223], v1\n"
    "ds_write_b64 v3, v[68:69] offset:512\n"
    "v_smfmac_f32_16x16x64_bf16 v[72:75], v[48:51], v[224:231], v1\n"
    "v_smfmac_f32_16x16x64_bf16 v[76:79], v[48:51], v[232:239], v1\n"
    "buffer_store_dword v117, v116, s[40:43], s58 offen sc1\n"
    "v_smfmac_f32_16x16x64_bf16 v[72:75], v[52:55], v[240:247], v1\n"
    "v_smfmac_f32_16x16x64_bf16 v[76:79], v[52:55], v[248:255], v1\n"
    "s_waitcnt lgkmcnt(0)\n"
    "s_barrier\n"
    "v_mov_b32 v13, v13\n"
    "v_mov_b32 v13, v13\n"
    "v_mov_b32 v13, v13\n"
    "v_mov_b32 v13, v13\n"
    "v_mov_b32 v13, v13\n"
    "v_mov_b32 v13, v13\n"
    "v_permlane32_swap_b32 v72, v76\n"
    "v_permlane32_swap_b32 v73, v77\n"
    "v_permlane32_swap_b32 v74, v78\n"
    "v_permlane32_swap_b32 v75, v79\n"
    "v_pk_add_f32 v[72:73], v[72:73], v[76:77]\n"
    "v_pk_add_f32 v[74:75], v[74:75], v[78:79]\n"
    "v_cvt_pk_bf16_f32 v76, v72, v73\n"
    "v_cvt_pk_bf16_f32 v77, v74, v75\n"
    "ds_write_b64 v3, v[76:77] offset:1024\n"
    "s_waitcnt lgkmcnt(0)\n"
    "s_barrier\n"
    "s_waitcnt vmcnt(5)\n"
    "ds_write2st64_b64 v105, v[136:137], v[138:139] offset1:2\n"
    "s_waitcnt vmcnt(4)\n"
    "ds_write2st64_b64 v106, v[140:141], v[142:143] offset1:2\n"
    "s_waitcnt vmcnt(3)\n"
    "ds_write2st64_b64 v107, v[144:145], v[146:147] offset1:2\n"
    "s_waitcnt vmcnt(2)\n"
    "ds_write2st64_b64 v108, v[148:149], v[150:151] offset1:2\n"
    "s_waitcnt vmcnt(1)\n"
    "s_cmp_lg_u32 s45, 1\n"
    "s_cbranch_scc1 hw_n1_%=\n"
    "ds_read_b64_tr_b16 v[16:17], v4 offset:0\n"
    "ds_read_b64_tr_b16 v[18:19], v4 offset:512\n"
    "ds_read_b64_tr_b16 v[20:21], v4 offset:1536\n"
    "ds_read_b64_tr_b16 v[22:23], v4 offset:2048\n"
    "ds_read_b64_tr_b16 v[24:25], v4 offset:3072\n"
    "ds_read_b64_tr_b16 v[26:27], v4 offset:3584\n"
    "ds_read_b64_tr_b16 v[28:29], v4 offset:4608\n"
    "ds_read_b64_tr_b16 v[30:31], v4 offset:5120\n"
    "ds_read_b64_tr_b16 v[32:33], v4 offset:1024\n"
    "ds_read_b64_tr_b16 v[34:35], v4 offset:2560\n"
    "ds_read_b64_tr_b16 v[36:37], v4 offset:4096\n"
    "ds_read_b64_tr_b16 v[38:39], v4 offset:5632\n"
    "s_waitcnt lgkmcnt(14)\n"
    "s_lshl_b32 s54, s47, 5\n"
    "s_add_u32 s54, s54, s44\n"
    "s_mul_i32 s54, s54, 3072\n"
    "s_add_u32 s54, s54, 217645056\n"
    "s_waitcnt lgkmcnt(10)\n"
    "buffer_store_dwordx4 v[16:19], v7, s[40:43], s54 offen sc1\n"
    "s_waitcnt lgkmcnt(8)\n"
    "buffer_store_dwordx4 v[20:23], v7, s[40:43], s54 offen offset:512 sc1\n"
    "s_waitcnt lgkmcnt(6)\n"
    "buffer_store_dwordx4 v[24:27], v7, s[40:43], s54 offen offset:1024 sc1\n"
    "s_waitcnt lgkmcnt(4)\n"
    "buffer_store_dwordx4 v[28:31], v7, s[40:43], s54 offen offset:1536 sc1\n"
    "s_waitcnt lgkmcnt(2)\n"
    "buffer_store_dwordx4 v[32:35], v7, s[40:43], s54 offen offset:2048 sc1\n"
    "s_waitcnt lgkmcnt(0)\n"
    "buffer_store_dwordx4 v[36:39], v7, s[40:43], s54 offen offset:2560 sc1\n"
    "s_nop 1\n"
    "hw_n1_%=:\n"
    "s_cmp_lg_u32 s45, 2\n"
    "s_cbranch_scc1 hw_n2_%=\n"
    "s_xor_b32 s48, s48, 2\n"
    "s_mul_i32 s54, s48, 196608\n"
    "buffer_store_dwordx4 v[112:115], v6, s[40:43], s54 offen sc1\n"
    "s_add_u32 s54, s54, 49152\n"
    "buffer_store_dwordx4 v[112:115], v6, s[40:43], s54 offen sc1\n"
    "s_add_u32 s54, s54, 49152\n"
    "buffer_store_dwordx4 v[112:115], v6, s[40:43], s54 offen sc1\n"
    "s_add_u32 s54, s54, 49152\n"
    "buffer_store_dwordx4 v[112:115], v6, s[40:43], s54 offen sc1\n"
    "s_add_u32 s54, s54, 49152\n"
    "s_nop 0\n"
    "hw_n2_%=:\n"
    "s_cmp_eq_u32 s47, 63\n"
    "s_cbranch_scc1 hw_fin_%=\n"
    "v_add_u32 v3, s61, v3\n"
    "v_add_u32 v4, s61, v4\n"
    "s_sub_u32 s61, 0, s61\n"
    "s_add_u32 s47, s47, 1\n"
    "s_branch hw_loopb_%=\n"
    "hw_failb_%=:\n"
    "v_max_u32 v12, v16, v17\n"
    "v_max3_u32 v12, v18, v19, v12\n"
    "v_max3_u32 v12, v20, v21, v12\n"
    "v_max3_u32 v12, v22, v23, v12\n"
    "v_max3_u32 v12, v24, v25, v12\n"
    "v_max3_u32 v12, v26, v27, v12\n"
    "v_max3_u32 v12, v28, v29, v12\n"
    "v_max3_u32 v12, v30, v31, v12\n"
    "v_max3_u32 v12, v32, v33, v12\n"
    "v_max3_u32 v12, v34, v35, v12\n"
    "v_max3_u32 v12, v36, v37, v12\n"
    "v_max3_u32 v12, v38, v39, v12\n"
    "v_max3_u32 v12, v40, v41, v12\n"
    "v_max3_u32 v12, v42, v43, v12\n"
    "v_max3_u32 v12, v44, v45, v12\n"
    "v_max3_u32 v12, v46, v47, v12\n"
    "v_max3_u32 v12, v48, v49, v12\n"
    "v_max3_u32 v12, v50, v51, v12\n"
    "v_max3_u32 v12, v52, v53, v12\n"
    "v_max3_u32 v12, v54, v55, v12\n"
    "v_cmp_eq_u32 vcc, -1, v12\n"
    "s_nop 4\n"
    "s_cbranch_vccz hw_resumeb_%=\n"
    "hw_pollb_%=:\n"
    "s_nop 7\n"
    "s_waitcnt lgkmcnt(0)\n"
    "v_mov_b32 v56, 0\n"
    "v_mov_b32 v57, 0\n"
    "v_mov_b32 v58, 0\n"
    "v_mov_b32 v59, 0\n"
    "v_mov_b32 v60, 0\n"
    "v_mov_b32 v61, 0\n"
    "v_mov_b32 v62, 0\n"
    "v_mov_b32 v63, 0\n"
    "v_mov_b32 v64, 0\n"
    "v_mov_b32 v65, 0\n"
    "v_mov_b32 v66, 0\n"
    "v_mov_b32 v67, 0\n"
    "v_mov_b32 v68, 0\n"
    "v_mov_b32 v69, 0\n"
    "v_mov_b32 v70, 0\n"
    "v_mov_b32 v71, 0\n"
    "v_mov_b32 v72, 0\n"
    "v_mov_b32 v73, 0\n"
    "v_mov_b32 v74, 0\n"
    "v_mov_b32 v75, 0\n"
    "v_mov_b32 v76, 0\n"
    "v_mov_b32 v77, 0\n"
    "v_mov_b32 v78, 0\n"
    "v_mov_b32 v79, 0\n"
    "ds_read_b128 v[80:83], v2 offset:0\n"
    "ds_read_b128 v[84:87], v2 offset:1024\n"
    "ds_read_b128 v[88:91], v2 offset:2048\n"
    "ds_read_b128 v[92:95], v2 offset:3072\n"
    "ds_read_b128 v[96:99], v2 offset:4096\n"
    "ds_read_b128 v[100:103], v2 offset:5120\n"
    "v_sub_u32 v12, v158, v0\n"
    "v_lshl_add_u32 v12, v15, 7, v12\n"
    "v_and_b32 v159, 15, v15\n"
    "v_sub_u32 v159, v159, v15\n"
    "v_lshl_add_u32 v159, v159, 7, v12\n"
    "s_movk_i32 s55, 0x4000\n"
    "s_waitcnt vmcnt(0) lgkmcnt(0)\n"
    "s_add_u32 s60, s60, 1\n"
    "s_cmp_gt_u32 s60, 65536\n"
    "s_cbranch_scc1 hw_abort_%=\n"
    "s_and_b32 s59, s60, 63\n"
    "s_cbranch_scc1 hw_againb_%=\n"
    "v_mov_b32 v13, 1024\n"
    "buffer_load_dword v13, v13, s[40:43], s58 offen sc1\n"
    "s_waitcnt vmcnt(0)\n"
    "v_readfirstlane_b32 s59, v13\n"
    "s_nop 0\n"
    "s_cmp_lg_u32 s59, 0\n"
    "s_nop 2\n"
    "s_cbranch_scc1 hw_end_%=\n"
    "s_branch hw_againb_%=\n"
    "hw_againb_%=:\n"
    "buffer_load_dword v16, v12, s[40:43], 0 offen sc1\n"
    "buffer_load_dword v17, v159, s[40:43], s55 offen sc1\n"
    "s_waitcnt vmcnt(0)\n"
    "v_max_u32 v16, v16, v17\n"
    "v_cmp_eq_u32 vcc, -1, v16\n"
    "s_nop 4\n"
    "s_cbranch_vccz hw_retryb_%=\n"
    "s_add_u32 s60, s60, 1\n"
    "s_cmp_gt_u32 s60, 65536\n"
    "s_cbranch_scc1 hw_abort_%=\n"
    "s_and_b32 s59, s60, 63\n"
    "s_cbranch_scc1 hw_againb_%=\n"
    "v_mov_b32 v13, 1024\n"
    "buffer_load_dword v13, v13, s[40:43], s58 offen sc1\n"
    "s_waitcnt vmcnt(0)\n"
    "v_readfirstlane_b32 s59, v13\n"
    "s_nop 0\n"
    "s_cmp_lg_u32 s59, 0\n"
    "s_nop 2\n"
    "s_cbranch_scc1 hw_end_%=\n"
    "s_branch hw_againb_%=\n"
    "hw_fin_%=:\n"
    "v_mov_b32 v13, 64\n"
    "s_waitcnt vmcnt(0)\n"
    "buffer_store_dword v13, v116, s[40:43], s58 offen sc1\n"
    "s_nop 1\n"
    "s_branch hw_end_%=\n"
    "hw_abort_%=:\n"
    "v_mov_b32 v13, 1\n"
    "v_mov_b32 v14, 1024\n"
    "buffer_store_dword v13, v14, s[40:43], s58 offen sc1\n"
    "hw_end_%=:\n"
    "s_endpgm\n"
    :: ROLE_INPUTS : ROLE_CLOBBERS);
}
DEV void role_col(unsigned lo, unsigned hi, int id, int w, int aux, const Tune& tn) {
    asm volatile(
    "s_mov_b32 s40, %0\n"
    "s_mov_b32 s41, %1\n"
    "s_mov_b32 s42, %2\n"
    "s_mov_b32 s43, %3\n"
    "s_mov_b32 s44, %4\n"
    "s_mov_b32 s45, %5\n"
    "s_mov_b32 s46, %6\n"
    "s_mov_b32 s66, %7\n"
    "s_mov_b32 s67, %8\n"
    "s_mov_b32 s68, %9\n"
    "s_mov_b32 s69, %10\n"
    "s_mov_b32 s70, %11\n"
    "s_mov_b32 s71, %12\n"
    "s_mov_b32 s72, %13\n"
    "s_mov_b32 s73, %14\n"
    "s_mov_b64 exec, -1\n"
    "v_mbcnt_lo_u32_b32 v15, -1, 0\n"
    "v_mbcnt_hi_u32_b32 v15, -1, v15\n"
    "v_lshlrev_b32 v0, 4, v15\n"
    "s_mov_b32 s58, 2097152\n"
    "s_mov_b32 s92, 0\n"
    "v_lshrrev_b32 v12, 4, v15\n"
    "v_and_b32 v13, 7, v15\n"
    "v_and_b32 v10, 8, v15\n"
    "v_cmp_eq_u32 vcc, 0, v10\n"
    "v_mov_b32 v10, 0xeeee\n"
    "v_mov_b32 v1, 0x4444\n"
    "v_cndmask_b32 v1, v10, v1, vcc\n"
    "s_lshl_b32 s48, s44, 7\n"
    "v_lshlrev_b32 v3, 4, v13\n"
    "v_add_u32 v3, s48, v3\n"
    "v_and_b32 v10, 0xffffe000, v3\n"
    "v_add_u32 v3, v3, v10\n"
    "v_lshl_add_u32 v3, v12, 15, v3\n"
    "v_mov_b32 v10, 2147483632\n"
    "v_cndmask_b32 v3, v10, v3, vcc\n"
    "s_mov_b32 s47, 0\n"
    "s_cmp_lg_u32 s45, 0\n"
    "s_cbranch_scc1 cw_wx_%=\n"
    "v_mov_b32 v5, 0\n"
    "v_cmp_eq_u32 vcc, 0, v15\n"
    "v_mov_b32 v10, 2147483632\n"
    "s_nop 0\n"
    "v_cndmask_b32 v5, v10, v5, vcc\n"
    "s_mov_b32 s65, 0\n"
    "v_and_b32 v2, 31, v15\n"
    "v_lshlrev_b32 v2, 7, v2\n"
    "v_mov_b32 v7, 0\n"
    "v_and_b32 v8, 31, v15\n"
    "v_lshlrev_b32 v8, 7, v8\n"
    "v_add_u32 v8, 10240, v8\n"
    "s_lshl_b32 s61, s44, 14\n"
    "s_add_u32 s61, s61, 112787456\n"
    "cw_loopa_%=:\n"
    "s_and_b32 s48, s47, 3\n"
    "s_lshl_b32 s50, s48, 2\n"
    "s_add_u32 s50, s50, s46\n"
    "s_mul_i32 s50, s50, 32768\n"
    "s_add_u32 s50, s50, 786432\n"
    "s_add_u32 s51, s50, 4096\n"
    "s_add_u32 s52, s50, 16384\n"
    "s_add_u32 s64, s65, s70\n"
    "s_add_u32 s55, s47, 1\n"
    "s_mov_b32 s60, 0\n"
    "v_cmp_gt_u32 vcc, s55, v7\n"
    "s_nop 4\n"
    "s_cbranch_vccz cw_ux_ok_%=\n"
    "cw_ux_poll_%=:\n"
    "buffer_load_dword v7, v8, s[40:43], s58 offen sc1\n"
    "s_waitcnt vmcnt(0)\n"
    "v_cmp_gt_u32 vcc, s55, v7\n"
    "s_nop 4\n"
    "s_cbranch_vccz cw_ux_ok_%=\n"
    "s_sleep 2\n"
    "s_add_u32 s60, s60, 1\n"
    "s_cmp_gt_u32 s60, 65536\n"
    "s_cbranch_scc1 cw_abort_%=\n"
    "s_and_b32 s59, s60, 63\n"
    "s_cbranch_scc1 cw_ux_poll_%=\n"
    "v_mov_b32 v10, 1024\n"
    "buffer_load_dword v10, v10, s[40:43], s58 offen sc1\n"
    "s_waitcnt vmcnt(0)\n"
    "v_readfirstlane_b32 s59, v10\n"
    "s_nop 0\n"
    "s_cmp_lg_u32 s59, 0\n"
    "s_nop 2\n"
    "s_cbranch_scc1 cw_end_%=\n"
    "s_branch cw_ux_poll_%=\n"
    "cw_ux_ok_%=:\n"
    "s_memrealtime s[56:57]\n"
    "s_waitcnt lgkmcnt(0)\n"
    "s_sub_u32 s59, s64, s56\n"
    "s_cmp_gt_u32 s59, 1000\n"
    "s_cbranch_scc1 cw_wm_go_%=\n"
    "cw_wm_%=:\n"
    "s_memrealtime s[56:57]\n"
    "s_waitcnt lgkmcnt(0)\n"
    "s_sub_u32 s59, s64, s56\n"
    "s_cmp_gt_i32 s59, 3\n"
    "s_cbranch_scc1 cw_wm_%=\n"
    "cw_wm_f_%=:\n"
    "s_sub_u32 s59, s59, 1\n"
    "s_cmp_ge_i32 s59, 0\n"
    "s_cbranch_scc1 cw_wm_f_%=\n"
    "cw_wm_go_%=:\n"
    "s_mov_b32 s60, 0\n"
    "buffer_load_dwordx4 v[32:35], v0, s[40:43], s50 offen sc1\n"
    "buffer_load_dwordx4 v[36:39], v0, s[40:43], s50 offen offset:1024 sc1\n"
    "buffer_load_dwordx4 v[40:43], v0, s[40:43], s50 offen offset:2048 sc1\n"
    "buffer_load_dwordx4 v[44:47], v0, s[40:43], s50 offen offset:3072 sc1\n"
    "buffer_load_dwordx4 v[48:51], v0, s[40:43], s51 offen sc1\n"
    "buffer_load_dwordx4 v[52:55], v0, s[40:43], s51 offen offset:1024 sc1\n"
    "buffer_load_dwordx4 v[56:59], v0, s[40:43], s51 offen offset:2048 sc1\n"
    "buffer_load_dwordx4 v[60:63], v0, s[40:43], s51 offen offset:3072 sc1\n"
    "buffer_load_dwordx4 v[64:67], v0, s[40:43], s52 offen sc1\n"
    "buffer_load_dwordx4 v[68:71], v0, s[40:43], s52 offen offset:1024 sc1\n"
    "buffer_load_dwordx4 v[112:115], v0, s[40:43], s61 offen sc1\n"
    "buffer_load_dwordx4 v[116:119], v0, s[40:43], s61 offen offset:1024 sc1\n"
    "buffer_load_dwordx4 v[120:123], v0, s[40:43], s61 offen offset:2048 sc1\n"
    "buffer_load_dwordx4 v[124:127], v0, s[40:43], s61 offen offset:3072 sc1\n"
    "s_add_u32 s61, s61, 4096\n"
    "buffer_load_dwordx4 v[128:131], v0, s[40:43], s61 offen sc1\n"
    "buffer_load_dwordx4 v[132:135], v0, s[40:43], s61 offen offset:1024 sc1\n"
    "buffer_load_dwordx4 v[136:139], v0, s[40:43], s61 offen offset:2048 sc1\n"
    "buffer_load_dwordx4 v[140:143], v0, s[40:43], s61 offen offset:3072 sc1\n"
    "s_add_u32 s61, s61, 4096\n"
    "buffer_load_dwordx4 v[144:147], v0, s[40:43], s61 offen sc1\n"
    "buffer_load_dwordx4 v[148:151], v0, s[40:43], s61 offen offset:1024 sc1\n"
    "buffer_load_dwordx4 v[152:155], v0, s[40:43], s61 offen offset:2048 sc1\n"
    "buffer_load_dwordx4 v[156:159], v0, s[40:43], s61 offen offset:3072 sc1\n"
    "s_add_u32 s61, s61, 4096\n"
    "buffer_load_dwordx4 v[160:163], v0, s[40:43], s61 offen sc1\n"
    "buffer_load_dwordx4 v[164:167], v0, s[40:43], s61 offen offset:1024 sc1\n"
    "buffer_load_dwordx4 v[168:171], v0, s[40:43], s61 offen offset:2048 sc1\n"
    "buffer_load_dwordx4 v[172:175], v0, s[40:43], s61 offen offset:3072 sc1\n"
    "s_add_u32 s61, s61, 1298432\n"
    "buffer_load_dword v7, v8, s[40:43], s58 offen sc1\n"
    "cw_mcomp_%=:\n"
    "s_waitcnt vmcnt(26)\n"
    "ds_write_b128 v0, v[32:35] offset:4096\n"
    "s_waitcnt vmcnt(25)\n"
    "ds_write_b128 v0, v[36:39] offset:5120\n"
    "s_waitcnt vmcnt(24)\n"
    "ds_write_b128 v0, v[40:43] offset:6144\n"
    "s_waitcnt vmcnt(23)\n"
    "ds_write_b128 v0, v[44:47] offset:7168\n"
    "s_waitcnt vmcnt(22)\n"
    "ds_write_b128 v0, v[48:51] offset:8192\n"
    "s_waitcnt vmcnt(21)\n"
    "ds_write_b128 v0, v[52:55] offset:9216\n"
    "s_waitcnt vmcnt(20)\n"
    "ds_write_b128 v0, v[56:59] offset:10240\n"
    "s_waitcnt vmcnt(19)\n"
    "ds_write_b128 v0, v[60:63] offset:11264\n"
    "s_waitcnt vmcnt(18)\n"
    "ds_write_b128 v0, v[64:67] offset:12288\n"
    "s_waitcnt vmcnt(17)\n"
    "ds_write_b128 v0, v[68:71] offset:13312\n"
    "v_max_u32 v6, v32, v33\n"
    "v_max3_u32 v6, v34, v35, v6\n"
    "v_max3_u32 v6, v36, v37, v6\n"
    "v_max3_u32 v6, v38, v39, v6\n"
    "v_max3_u32 v6, v40, v41, v6\n"
    "v_max3_u32 v6, v42, v43, v6\n"
    "v_max3_u32 v6, v44, v45, v6\n"
    "v_max3_u32 v6, v46, v47, v6\n"
    "v_max3_u32 v6, v48, v49, v6\n"
    "v_max3_u32 v6, v50, v51, v6\n"
    "v_max3_u32 v6, v52, v53, v6\n"
    "v_max3_u32 v6, v54, v55, v6\n"
    "v_max3_u32 v6, v56, v57, v6\n"
    "v_max3_u32 v6, v58, v59, v6\n"
    "v_max3_u32 v6, v60, v61, v6\n"
    "v_max3_u32 v6, v62, v63, v6\n"
    "v_max3_u32 v6, v64, v65, v6\n"
    "v_max3_u32 v6, v66, v67, v6\n"
    "v_max3_u32 v6, v68, v69, v6\n"
    "v_max3_u32 v6, v70, v71, v6\n"
    "v_cmp_eq_u32 vcc, -1, v6\n"
    "s_nop 4\n"
    "s_cbranch_vccnz cw_mfail_%=\n"
    "s_waitcnt lgkmcnt(0)\n"
    "s_barrier\n"
    "s_cmp_eq_u32 s47, 63\n"
    "s_cbranch_scc1 cw_end_%=\n"
    "s_mul_i32 s50, s48, 4\n"
    "s_and_b32 s51, s46, 3\n"
    "s_add_u32 s50, s50, s51\n"
    "s_mul_i32 s50, s50, 49152\n"
    "s_add_u32 s51, s50, 4096\n"
    "s_add_u32 s52, s50, 16384\n"
    "s_add_u32 s53, s50, 20480\n"
    "s_add_u32 s62, s50, 32768\n"
    "s_add_u32 s49, s47, 1\n"
    "s_and_b32 s49, s49, 3\n"
    "s_mul_i32 s63, s49, 131072\n"
    "s_add_u32 s63, s63, 786432\n"
    "s_waitcnt lgkmcnt(0)\n"
    "s_add_u32 s64, s65, s69\n"
    "s_memrealtime s[56:57]\n"
    "s_waitcnt lgkmcnt(0)\n"
    "s_sub_u32 s59, s64, s56\n"
    "s_cmp_gt_u32 s59, 1000\n"
    "s_cbranch_scc1 cw_ws_go_%=\n"
    "cw_ws_%=:\n"
    "s_memrealtime s[56:57]\n"
    "s_waitcnt lgkmcnt(0)\n"
    "s_sub_u32 s59, s64, s56\n"
    "s_cmp_gt_i32 s59, 3\n"
    "s_cbranch_scc1 cw_ws_%=\n"
    "cw_ws_f_%=:\n"
    "s_sub_u32 s59, s59, 1\n"
    "s_cmp_ge_i32 s59, 0\n"
    "s_cbranch_scc1 cw_ws_f_%=\n"
    "cw_ws_go_%=:\n"
    "s_lshl_b32 s54, s47, 7\n"
    "s_add_u32 s54, s54, 1310720\n"
    "s_mov_b32 s60, 0\n"
    "cw_rs_%=:\n"
    "buffer_load_dwordx2 v[10:11], v5, s[40:43], s54 offen sc1\n"
    "s_waitcnt vmcnt(0)\n"
    "v_readfirstlane_b32 s64, v10\n"
    "v_readfirstlane_b32 s65, v11\n"
    "s_nop 0\n"
    "s_max_u32 s59, s64, s65\n"
    "s_cmp_lg_u32 s59, -1\n"
    "s_nop 1\n"
    "s_cbranch_scc1 cw_rs_ok_%=\n"
    "s_sleep 4\n"
    "s_add_u32 s60, s60, 1\n"
    "s_cmp_gt_u32 s60, 65536\n"
    "s_cbranch_scc1 cw_abort_%=\n"
    "s_and_b32 s59, s60, 63\n"
    "s_cbranch_scc1 cw_rs_%=\n"
    "v_mov_b32 v10, 1024\n"
    "buffer_load_dword v10, v10, s[40:43], s58 offen sc1\n"
    "s_waitcnt vmcnt(0)\n"
    "v_readfirstlane_b32 s59, v10\n"
    "s_nop 0\n"
    "s_cmp_lg_u32 s59, 0\n"
    "s_nop 2\n"
    "s_cbranch_scc1 cw_end_%=\n"
    "s_branch cw_rs_%=\n"
    "cw_rs_ok_%=:\n"
    "s_mov_b32 s60, 0\n"
    "s_memrealtime s[56:57]\n"
    "s_waitcnt lgkmcnt(0)\n"
    "s_sub_u32 s59, s64, s56\n"
    "s_cmp_gt_u32 s59, 1000\n"
    "s_cbranch_scc1 cw_wu_go_%=\n"
    "cw_wu_%=:\n"
    "s_memrealtime s[56:57]\n"
    "s_waitcnt lgkmcnt(0)\n"
    "s_sub_u32 s59, s64, s56\n"
    "s_cmp_gt_i32 s59, 15\n"
    "s_cbranch_scc1 cw_wu_%=\n"
    "s_memrealtime s[30:31]\n"
    "s_nop 1\n"
    "s_memrealtime s[90:91]\n"
    "s_nop 1\n"
    "s_memrealtime s[34:35]\n"
    "s_nop 1\n"
    "s_memrealtime s[36:37]\n"
    "s_nop 1\n"
    "s_memrealtime s[38:39]\n"
    "s_nop 1\n"
    "s_memrealtime s[94:95]\n"
    "s_nop 1\n"
    "s_memrealtime s[96:97]\n"
    "s_nop 1\n"
    "s_memrealtime s[98:99]\n"
    "s_nop 1\n"
    "s_getpc_b64 s[56:57]\n"
    "cw_wu_pc_%=:\n"
    "s_waitcnt lgkmcnt(0)\n"
    "s_add_u32 s90, s90, s34\n"
    "s_add_u32 s90, s90, s36\n"
    "s_add_u32 s90, s90, s38\n"
    "s_add_u32 s90, s90, s94\n"
    "s_add_u32 s90, s90, s96\n"
    "s_add_u32 s90, s90, s98\n"
    "s_mul_i32 s34, s30, 7\n"
    "s_sub_u32 s90, s90, s34\n"
    "s_mul_i32 s90, s90, 3\n"
    "s_sub_u32 s59, s64, s30\n"
    "s_mul_i32 s59, s59, 23\n"
    "s_sub_u32 s59, s59, s90\n"
    "s_sub_u32 s59, s59, 170\n"
    "s_max_i32 s59, s59, 0\n"
    "s_min_i32 s59, s59, 252\n"
    "s_andn2_b32 s59, s59, 3\n"
    "s_sub_u32 s59, cw_wu_e_%=-cw_wu_pc_%=, s59\n"
    "s_add_u32 s56, s56, s59\n"
    "s_addc_u32 s57, s57, 0\n"
    "s_setpc_b64 s[56:57]\n"
    "s_nop 0\n"
    "s_nop 0\n"
    "s_nop 0\n"
    "s_nop 0\n"
    "s_nop 0\n"
    "s_nop 0\n"
    "s_nop 0\n"
    "s_nop 0\n"
    "s_nop 0\n"
    "s_nop 0\n"
    "s_nop 0\n"
    "s_nop 0\n"
    "s_nop 0\n"
    "s_nop 0\n"
    "s_nop 0\n"
    "s_nop 0\n"
    "s_nop 0\n"
    "s_nop 0\n"
    "s_nop 0\n"
    "s_nop 0\n"
    "s_nop 0\n"
    "s_nop 0\n"
    "s_nop 0\n"
    "s_nop 0\n"
    "s_nop 0\n"
    "s_nop 0\n"
    "s_nop 0\n"
    "s_nop 0\n"
    "s_nop 0\n"
    "s_nop 0\n"
    "s_nop 0\n"
    "s_nop 0\n"
    "s_nop 0\n"
    "s_nop 0\n"
    "s_nop 0\n"
    "s_nop 0\n"
    "s_nop 0\n"
    "s_nop 0\n"
    "s_nop 0\n"
    "s_nop 0\n"
    "s_nop 0\n"
    "s_nop 0\n"
    "s_nop 0\n"
    "s_nop 0\n"
    "s_nop 0\n"
    "s_nop 0\n"
    "s_nop 0\n"
    "s_nop 0\n"
    "s_nop 0\n"
    "s_nop 0\n"
    "s_nop 0\n"
    "s_nop 0\n"
    "s_nop 0\n"
    "s_nop 0\n"
    "s_nop 0\n"
    "s_nop 0\n"
    "s_nop 0\n"
    "s_nop 0\n"
    "s_nop 0\n"
    "s_nop 0\n"
    "s_nop 0\n"
    "s_nop 0\n"
    "s_nop 0\n"
    "cw_wu_e_%=:\n"
    "cw_wu_go_%=:\n"
    "buffer_load_dwordx4 v[32:35], v0, s[40:43], s50 offen sc1\n"
    "buffer_load_dwordx4 v[36:39], v0, s[40:43], s50 offen offset:1024 sc1\n"
    "buffer_load_dwordx4 v[40:43], v0, s[40:43], s50 offen offset:2048 sc1\n"
    "buffer_load_dwordx4 v[44:47], v0, s[40:43], s50 offen offset:3072 sc1\n"
    "buffer_load_dwordx4 v[48:51], v0, s[40:43], s51 offen sc1\n"
    "buffer_load_dwordx4 v[52:55], v0, s[40:43], s51 offen offset:1024 sc1\n"
    "buffer_load_dwordx4 v[56:59], v0, s[40:43], s51 offen offset:2048 sc1\n"
    "buffer_load_dwordx4 v[60:63], v0, s[40:43], s51 offen offset:3072 sc1\n"
    "buffer_load_dwordx4 v[64:67], v0, s[40:43], s52 offen sc1\n"
    "buffer_load_dwordx4 v[68:71], v0, s[40:43], s52 offen offset:1024 sc1\n"
    "buffer_load_dwordx4 v[72:75], v0, s[40:43], s52 offen offset:2048 sc1\n"
    "buffer_load_dwordx4 v[76:79], v0, s[40:43], s52 offen offset:3072 sc1\n"
    "buffer_load_dwordx4 v[80:83], v0, s[40:43], s53 offen sc1\n"
    "buffer_load_dwordx4 v[84:87], v0, s[40:43], s53 offen offset:1024 sc1\n"
    "buffer_load_dwordx4 v[88:91], v0, s[40:43], s53 offen offset:2048 sc1\n"
    "buffer_load_dwordx4 v[92:95], v0, s[40:43], s53 offen offset:3072 sc1\n"
    "buffer_load_dwordx4 v[96:99], v0, s[40:43], s62 offen sc1\n"
    "buffer_load_dwordx4 v[100:103], v0, s[40:43], s62 offen offset:1024 sc1\n"
    "buffer_load_dwordx4 v[104:107], v0, s[40:43], s62 offen offset:2048 sc1\n"
    "buffer_load_dwordx4 v[108:111], v0, s[40:43], s62 offen offset:3072 sc1\n"
    "s_barrier\n"
    "ds_read_b128 v[176:179], v0 offset:0\n"
    "ds_read_b128 v[180:183], v0 offset:1024\n"
    "ds_read_b128 v[184:187], v0 offset:2048\n"
    "ds_read_b128 v[188:191], v0 offset:3072\n"
    "cw_pcomp_%=:\n"
    "s_waitcnt vmcnt(19)\n"
    "v_mfma_f32_16x16x32_bf16 v[24:27], v[112:115], v[32:35], 0\n"
    "s_waitcnt vmcnt(18)\n"
    "v_mfma_f32_16x16x32_bf16 v[24:27], v[116:119], v[36:39], v[24:27]\n"
    "s_waitcnt vmcnt(17)\n"
    "v_mfma_f32_16x16x32_bf16 v[24:27], v[120:123], v[40:43], v[24:27]\n"
    "s_waitcnt vmcnt(16)\n"
    "v_mfma_f32_16x16x32_bf16 v[24:27], v[124:127], v[44:47], v[24:27]\n"
    "s_waitcnt vmcnt(15)\n"
    "v_mfma_f32_16x16x32_bf16 v[24:27], v[128:131], v[48:51], v[24:27]\n"
    "s_waitcnt vmcnt(14)\n"
    "v_mfma_f32_16x16x32_bf16 v[24:27], v[132:135], v[52:55], v[24:27]\n"
    "s_waitcnt vmcnt(13)\n"
    "v_mfma_f32_16x16x32_bf16 v[24:27], v[136:139], v[56:59], v[24:27]\n"
    "s_waitcnt vmcnt(12)\n"
    "v_mfma_f32_16x16x32_bf16 v[24:27], v[140:143], v[60:63], v[24:27]\n"
    "s_waitcnt vmcnt(11)\n"
    "v_mfma_f32_16x16x32_bf16 v[24:27], v[144:147], v[64:67], v[24:27]\n"
    "s_waitcnt vmcnt(10)\n"
    "v_mfma_f32_16x16x32_bf16 v[24:27], v[148:151], v[68:71], v[24:27]\n"
    "s_waitcnt vmcnt(9)\n"
    "v_mfma_f32_16x16x32_bf16 v[24:27], v[152:155], v[72:75], v[24:27]\n"
    "s_waitcnt vmcnt(8)\n"
    "v_mfma_f32_16x16x32_bf16 v[24:27], v[156:159], v[76:79], v[24:27]\n"
    "s_waitcnt vmcnt(7)\n"
    "v_mfma_f32_16x16x32_bf16 v[24:27], v[160:163], v[80:83], v[24:27]\n"
    "s_waitcnt vmcnt(6)\n"
    "v_mfma_f32_16x16x32_bf16 v[24:27], v[164:167], v[84:87], v[24:27]\n"
    "s_waitcnt vmcnt(5)\n"
    "v_mfma_f32_16x16x32_bf16 v[24:27], v[168:171], v[88:91], v[24:27]\n"
    "s_waitcnt vmcnt(4)\n"
    "v_mfma_f32_16x16x32_bf16 v[24:27], v[172:175], v[92:95], v[24:27]\n"
    "s_waitcnt vmcnt(3) lgkmcnt(3)\n"
    "v_mfma_f32_16x16x32_bf16 v[24:27], v[176:179], v[96:99], v[24:27]\n"
    "s_waitcnt vmcnt(2) lgkmcnt(2)\n"
    "v_mfma_f32_16x16x32_bf16 v[24:27], v[180:183], v[100:103], v[24:27]\n"
    "s_waitcnt vmcnt(1) lgkmcnt(1)\n"
    "v_mfma_f32_16x16x32_bf16 v[24:27], v[184:187], v[104:107], v[24:27]\n"
    "s_waitcnt vmcnt(0) lgkmcnt(0)\n"
    "v_mfma_f32_16x16x32_bf16 v[24:27], v[188:191], v[108:111], v[24:27]\n"
    "v_mov_b32 v11, v11\n"
    "v_mov_b32 v11, v11\n"
    "v_mov_b32 v11, v11\n"
    "v_mov_b32 v11, v11\n"
    "v_mov_b32 v11, v11\n"
    "v_mov_b32 v11, v11\n"
    "v_mov_b32 v11, v11\n"
    "v_mov_b32 v11, v11\n"
    "v_cmp_u_f32 vcc, v24, v24\n"
    "v_permlane32_swap_b32 v24, v26\n"
    "v_permlane32_swap_b32 v25, v27\n"
    "s_nop 0\n"
    "v_add_f32_dpp v24, v26, v24 row_ror:8 row_mask:0xf bank_mask:0xf\n"
    "v_add_f32_dpp v25, v27, v25 row_ror:8 row_mask:0xf bank_mask:0xf\n"
    "v_cvt_pk_bf16_f32 v28, v24, v25\n"
    "v_cvt_pk_bf16_f32 v30, v24, v25\n"
    "s_nop 1\n"
    "v_permlane16_swap_b32 v28, v30\n"
    "v_mov_b32 v29, v28\n"
    "v_mov_b32 v31, v30\n"
    "s_nop 0\n"
    "v_permlane32_swap_b32 v28, v29\n"
    "v_permlane32_swap_b32 v30, v31\n"
    "s_cbranch_vccnz cw_pfail_%=\n"
    "cw_presume_%=:\n"
    "buffer_store_dwordx4 v[28:31], v3, s[40:43], s63 offen sc1\n"
    "s_barrier\n"
    "s_add_u32 s47, s47, 1\n"
    "s_branch cw_loopa_%=\n"
    "cw_pagain_%=:\n"
    "buffer_load_dwordx4 v[32:35], v0, s[40:43], s50 offen sc1\n"
    "buffer_load_dwordx4 v[36:39], v0, s[40:43], s50 offen offset:1024 sc1\n"
    "buffer_load_dwordx4 v[40:43], v0, s[40:43], s50 offen offset:2048 sc1\n"
    "buffer_load_dwordx4 v[44:47], v0, s[40:43], s50 offen offset:3072 sc1\n"
    "buffer_load_dwordx4 v[48:51], v0, s[40:43], s51 offen sc1\n"
    "buffer_load_dwordx4 v[52:55], v0, s[40:43], s51 offen offset:1024 sc1\n"
    "buffer_load_dwordx4 v[56:59], v0, s[40:43], s51 offen offset:2048 sc1\n"
    "buffer_load_dwordx4 v[60:63], v0, s[40:43], s51 offen offset:3072 sc1\n"
    "buffer_load_dwordx4 v[64:67], v0, s[40:43], s52 offen sc1\n"
    "buffer_load_dwordx4 v[68:71], v0, s[40:43], s52 offen offset:1024 sc1\n"
    "buffer_load_dwordx4 v[72:75], v0, s[40:43], s52 offen offset:2048 sc1\n"
    "buffer_load_dwordx4 v[76:79], v0, s[40:43], s52 offen offset:3072 sc1\n"
    "buffer_load_dwordx4 v[80:83], v0, s[40:43], s53 offen sc1\n"
    "buffer_load_dwordx4 v[84:87], v0, s[40:43], s53 offen offset:1024 sc1\n"
    "buffer_load_dwordx4 v[88:91], v0, s[40:43], s53 offen offset:2048 sc1\n"
    "buffer_load_dwordx4 v[92:95], v0, s[40:43], s53 offen offset:3072 sc1\n"
    "buffer_load_dwordx4 v[96:99], v0, s[40:43], s62 offen sc1\n"
    "buffer_load_dwordx4 v[100:103], v0, s[40:43], s62 offen offset:1024 sc1\n"
    "buffer_load_dwordx4 v[104:107], v0, s[40:43], s62 offen offset:2048 sc1\n"
    "buffer_load_dwordx4 v[108:111], v0, s[40:43], s62 offen offset:3072 sc1\n"
    "s_nop 1\n"
    "s_branch cw_pcomp_%=\n"
    "cw_mfail_%=:\n"
    "s_sleep 6\n"
    "s_add_u32 s60, s60, 1\n"
    "s_cmp_gt_u32 s60, 65536\n"
    "s_cbranch_scc1 cw_abort_%=\n"
    "s_and_b32 s59, s60, 63\n"
    "s_cbranch_scc1 cw_mretry_%=\n"
    "v_mov_b32 v10, 1024\n"
    "buffer_load_dword v10, v10, s[40:43], s58 offen sc1\n"
    "s_waitcnt vmcnt(0)\n"
    "v_readfirstlane_b32 s59, v10\n"
    "s_nop 0\n"
    "s_cmp_lg_u32 s59, 0\n"
    "s_nop 2\n"
    "s_cbranch_scc1 cw_end_%=\n"
    "s_branch cw_mretry_%=\n"
    "cw_mretry_%=:\n"
    "buffer_load_dwordx4 v[32:35], v0, s[40:43], s50 offen sc1\n"
    "buffer_load_dwordx4 v[36:39], v0, s[40:43], s50 offen offset:1024 sc1\n"
    "buffer_load_dwordx4 v[40:43], v0, s[40:43], s50 offen offset:2048 sc1\n"
    "buffer_load_dwordx4 v[44:47], v0, s[40:43], s50 offen offset:3072 sc1\n"
    "buffer_load_dwordx4 v[48:51], v0, s[40:43], s51 offen sc1\n"
    "buffer_load_dwordx4 v[52:55], v0, s[40:43], s51 offen offset:1024 sc1\n"
    "buffer_load_dwordx4 v[56:59], v0, s[40:43], s51 offen offset:2048 sc1\n"
    "buffer_load_dwordx4 v[60:63], v0, s[40:43], s51 offen offset:3072 sc1\n"
    "buffer_load_dwordx4 v[64:67], v0, s[40:43], s52 offen sc1\n"
    "buffer_load_dwordx4 v[68:71], v0, s[40:43], s52 offen offset:1024 sc1\n"
    "s_waitcnt vmcnt(0)\n"
    "s_nop 0\n"
    "s_branch cw_mcomp_%=\n"
    "cw_pfail_%=:\n"
    "v_max_u32 v6, v32, v33\n"
    "v_max3_u32 v6, v34, v35, v6\n"
    "v_max3_u32 v6, v36, v37, v6\n"
    "v_max3_u32 v6, v38, v39, v6\n"
    "v_max3_u32 v6, v40, v41, v6\n"
    "v_max3_u32 v6, v42, v43, v6\n"
    "v_max3_u32 v6, v44, v45, v6\n"
    "v_max3_u32 v6, v46, v47, v6\n"
    "v_max3_u32 v6, v48, v49, v6\n"
    "v_max3_u32 v6, v50, v51, v6\n"
    "v_max3_u32 v6, v52, v53, v6\n"
    "v_max3_u32 v6, v54, v55, v6\n"
    "v_max3_u32 v6, v56, v57, v6\n"
    "v_max3_u32 v6, v58, v59, v6\n"
    "v_max3_u32 v6, v60, v61, v6\n"
    "v_max3_u32 v6, v62, v63, v6\n"
    "v_max3_u32 v6, v64, v65, v6\n"
    "v_max3_u32 v6, v66, v67, v6\n"
    "v_max3_u32 v6, v68, v69, v6\n"
    "v_max3_u32 v6, v70, v71, v6\n"
    "v_max3_u32 v6, v72, v73, v6\n"
    "v_max3_u32 v6, v74, v75, v6\n"
    "v_max3_u32 v6, v76, v77, v6\n"
    "v_max3_u32 v6, v78, v79, v6\n"
    "v_max3_u32 v6, v80, v81, v6\n"
    "v_max3_u32 v6, v82, v83, v6\n"
    "v_max3_u32 v6, v84, v85, v6\n"
    "v_max3_u32 v6, v86, v87, v6\n"
    "v_max3_u32 v6, v88, v89, v6\n"
    "v_max3_u32 v6, v90, v91, v6\n"
    "v_max3_u32 v6, v92, v93, v6\n"
    "v_max3_u32 v6, v94, v95, v6\n"
    "v_max3_u32 v6, v96, v97, v6\n"
    "v_max3_u32 v6, v98, v99, v6\n"
    "v_max3_u32 v6, v100, v101, v6\n"
    "v_max3_u32 v6, v102, v103, v6\n"
    "v_max3_u32 v6, v104, v105, v6\n"
    "v_max3_u32 v6, v106, v107, v6\n"
    "v_max3_u32 v6, v108, v109, v6\n"
    "v_max3_u32 v6, v110, v111, v6\n"
    "v_cmp_eq_u32 vcc, -1, v6\n"
    "s_nop 4\n"
    "s_cbranch_vccz cw_presume_%=\n"
    "s_cmp_lg_u32 s60, 0\n"
    "s_cbranch_scc1 cw_pspin_%=\n"
    "s_mov_b32 s60, 1\n"
    "s_branch cw_pagain_%=\n"
    "cw_ppoll_%=:\n"
    "buffer_load_dword v10, v2, s[40:43], s62 offen sc1\n"
    "s_waitcnt vmcnt(0)\n"
    "v_cmp_eq_u32 vcc, -1, v10\n"
    "s_nop 4\n"
    "s_cbranch_vccz cw_pagain_%=\n"
    "cw_pspin_%=:\n"
    "s_add_u32 s60, s60, 1\n"
    "s_cmp_gt_u32 s60, 65536\n"
    "s_cbranch_scc1 cw_abort_%=\n"
    "s_and_b32 s59, s60, 63\n"
    "s_cbranch_scc1 cw_ppoll_%=\n"
    "v_mov_b32 v10, 1024\n"
    "buffer_load_dword v10, v10, s[40:43], s58 offen sc1\n"
    "s_waitcnt vmcnt(0)\n"
    "v_readfirstlane_b32 s59, v10\n"
    "s_nop 0\n"
    "s_cmp_lg_u32 s59, 0\n"
    "s_nop 2\n"
    "s_cbranch_scc1 cw_end_%=\n"
    "s_branch cw_ppoll_%=\n"
    "cw_wx_%=:\n"
    "s_mul_i32 s61, s45, 6\n"
    "s_sub_u32 s61, s61, 6\n"
    "s_min_u32 s61, s61, 10\n"
    "s_lshl_b32 s48, s44, 4\n"
    "s_add_u32 s48, s48, s61\n"
    "s_mul_i32 s48, s48, 20480\n"
    "s_add_u32 s48, s48, 33619968\n"
    "s_lshl_b32 s49, s45, 15\n"
    "s_add_u32 s49, s49, -18432\n"
    "v_add_u32 v14, s49, v0\n"
    "buffer_load_dwordx4 v[16:19], v0, s[40:43], s48 offen\n"
    "buffer_load_dwordx4 v[20:23], v0, s[40:43], s48 offen offset:1024\n"
    "buffer_load_dwordx4 v[32:35], v0, s[40:43], s48 offen offset:2048\n"
    "buffer_load_dwordx4 v[36:39], v0, s[40:43], s48 offen offset:3072\n"
    "s_add_u32 s48, s48, 4096\n"
    "buffer_load_dwordx4 v[48:51], v0, s[40:43], s48 offen\n"
    "buffer_load_dwordx4 v[52:55], v0, s[40:43], s48 offen offset:1024\n"
    "buffer_load_dwordx4 v[64:67], v0, s[40:43], s48 offen offset:2048\n"
    "buffer_load_dwordx4 v[68:71], v0, s[40:43], s48 offen offset:3072\n"
    "s_add_u32 s48, s48, 4096\n"
    "buffer_load_dwordx4 v[80:83], v0, s[40:43], s48 offen\n"
    "buffer_load_dwordx4 v[84:87], v0, s[40:43], s48 offen offset:1024\n"
    "buffer_load_dwordx4 v[96:99], v0, s[40:43], s48 offen offset:2048\n"
    "buffer_load_dwordx4 v[100:103], v0, s[40:43], s48 offen offset:3072\n"
    "s_add_u32 s48, s48, 4096\n"
    "buffer_load_dwordx4 v[112:115], v0, s[40:43], s48 offen\n"
    "buffer_load_dwordx4 v[116:119], v0, s[40:43], s48 offen offset:1024\n"
    "buffer_load_dwordx4 v[120:123], v0, s[40:43], s48 offen offset:2048\n"
    "buffer_load_dwordx4 v[124:127], v0, s[40:43], s48 offen offset:3072\n"
    "s_add_u32 s48, s48, 4096\n"
    "buffer_load_dwordx4 v[128:131], v0, s[40:43], s48 offen\n"
    "buffer_load_dwordx4 v[132:135], v0, s[40:43], s48 offen offset:1024\n"
    "buffer_load_dwordx4 v[136:139], v0, s[40:43], s48 offen offset:2048\n"
    "buffer_load_dwordx4 v[140:143], v0, s[40:43], s48 offen offset:3072\n"
    "s_add_u32 s48, s48, 4096\n"
    "buffer_load_dwordx4 a[0:3], v0, s[40:43], s48 offen\n"
    "buffer_load_dwordx4 a[4:7], v0, s[40:43], s48 offen offset:1024\n"
    "buffer_load_dwordx4 a[32:35], v0, s[40:43], s48 offen offset:2048\n"
    "buffer_load_dwordx4 a[36:39], v0, s[40:43], s48 offen offset:3072\n"
    "s_add_u32 s48, s48, 4096\n"
    "buffer_load_dwordx4 a[64:67], v0, s[40:43], s48 offen\n"
    "buffer_load_dwordx4 a[68:71], v0, s[40:43], s48 offen offset:1024\n"
    "buffer_load_dwordx4 a[96:99], v0, s[40:43], s48 offen offset:2048\n"
    "buffer_load_dwordx4 a[100:103], v0, s[40:43], s48 offen offset:3072\n"
    "s_add_u32 s48, s48, 4096\n"
    "buffer_load_dwordx4 a[128:131], v0, s[40:43], s48 offen\n"
    "buffer_load_dwordx4 a[132:135], v0, s[40:43], s48 offen offset:1024\n"
    "buffer_load_dwordx4 a[160:163], v0, s[40:43], s48 offen offset:2048\n"
    "buffer_load_dwordx4 a[164:167], v0, s[40:43], s48 offen offset:3072\n"
    "s_add_u32 s48, s48, 4096\n"
    "buffer_load_dwordx4 a[192:195], v0, s[40:43], s48 offen\n"
    "buffer_load_dwordx4 a[196:199], v0, s[40:43], s48 offen offset:1024\n"
    "buffer_load_dwordx4 a[232:235], v0, s[40:43], s48 offen offset:2048\n"
    "buffer_load_dwordx4 a[236:239], v0, s[40:43], s48 offen offset:3072\n"
    "s_add_u32 s48, s48, 4096\n"
    "buffer_load_dwordx4 v[176:179], v0, s[40:43], s48 offen\n"
    "buffer_load_dwordx4 v[180:183], v0, s[40:43], s48 offen offset:1024\n"
    "buffer_load_dwordx4 v[216:219], v0, s[40:43], s48 offen offset:2048\n"
    "buffer_load_dwordx4 v[220:223], v0, s[40:43], s48 offen offset:3072\n"
    "s_add_u32 s48, s48, 4096\n"
    "buffer_load_dwordx4 a[8:11], v0, s[40:43], s48 offen\n"
    "buffer_load_dwordx4 a[12:15], v0, s[40:43], s48 offen offset:1024\n"
    "buffer_load_dwordx4 a[40:43], v0, s[40:43], s48 offen offset:2048\n"
    "buffer_load_dwordx4 a[44:47], v0, s[40:43], s48 offen offset:3072\n"
    "s_add_u32 s48, s48, 4096\n"
    "buffer_load_dwordx4 a[72:75], v0, s[40:43], s48 offen\n"
    "buffer_load_dwordx4 a[76:79], v0, s[40:43], s48 offen offset:1024\n"
    "buffer_load_dwordx4 a[104:107], v0, s[40:43], s48 offen offset:2048\n"
    "buffer_load_dwordx4 a[108:111], v0, s[40:43], s48 offen offset:3072\n"
    "s_add_u32 s48, s48, 4096\n"
    "buffer_load_dwordx4 a[136:139], v0, s[40:43], s48 offen\n"
    "buffer_load_dwordx4 a[140:143], v0, s[40:43], s48 offen offset:1024\n"
    "buffer_load_dwordx4 a[168:171], v0, s[40:43], s48 offen offset:2048\n"
    "buffer_load_dwordx4 a[172:175], v0, s[40:43], s48 offen offset:3072\n"
    "s_add_u32 s48, s48, 4096\n"
    "buffer_load_dwordx4 a[200:203], v0, s[40:43], s48 offen\n"
    "buffer_load_dwordx4 a[204:207], v0, s[40:43], s48 offen offset:1024\n"
    "buffer_load_dwordx4 a[240:243], v0, s[40:43], s48 offen offset:2048\n"
    "buffer_load_dwordx4 a[244:247], v0, s[40:43], s48 offen offset:3072\n"
    "s_add_u32 s48, s48, 4096\n"
    "buffer_load_dwordx4 v[184:187], v0, s[40:43], s48 offen\n"
    "buffer_load_dwordx4 v[188:191], v0, s[40:43], s48 offen offset:1024\n"
    "buffer_load_dwordx4 v[224:227], v0, s[40:43], s48 offen offset:2048\n"
    "buffer_load_dwordx4 v[228:231], v0, s[40:43], s48 offen offset:3072\n"
    "s_add_u32 s48, s48, 4096\n"
    "buffer_load_dwordx4 v[24:27], v0, s[40:43], s48 offen\n"
    "buffer_load_dwordx4 v[28:31], v0, s[40:43], s48 offen offset:1024\n"
    "buffer_load_dwordx4 v[40:43], v0, s[40:43], s48 offen offset:2048\n"
    "s_waitcnt vmcnt(61)\n"
    "buffer_load_dwordx4 v[44:47], v0, s[40:43], s48 offen offset:3072\n"
    "s_add_u32 s48, s48, 4096\n"
    "buffer_load_dwordx4 v[56:59], v0, s[40:43], s48 offen\n"
    "s_waitcnt vmcnt(61)\n"
    "buffer_load_dwordx4 v[60:63], v0, s[40:43], s48 offen offset:1024\n"
    "buffer_load_dwordx4 v[72:75], v0, s[40:43], s48 offen offset:2048\n"
    "s_waitcnt vmcnt(61)\n"
    "buffer_load_dwordx4 v[76:79], v0, s[40:43], s48 offen offset:3072\n"
    "s_add_u32 s48, s48, 4096\n"
    "buffer_load_dwordx4 v[88:91], v0, s[40:43], s48 offen\n"
    "s_waitcnt vmcnt(61)\n"
    "buffer_load_dwordx4 v[92:95], v0, s[40:43], s48 offen offset:1024\n"
    "buffer_load_dwordx4 v[104:107], v0, s[40:43], s48 offen offset:2048\n"
    "s_waitcnt vmcnt(61)\n"
    "buffer_load_dwordx4 v[108:111], v0, s[40:43], s48 offen offset:3072\n"
    "s_add_u32 s48, s48, 4096\n"
    "buffer_load_dwordx4 a[208:211], v0, s[40:43], s48 offen\n"
    "s_waitcnt vmcnt(61)\n"
    "buffer_load_dwordx4 a[212:215], v0, s[40:43], s48 offen offset:1024\n"
    "buffer_load_dwordx4 a[248:251], v0, s[40:43], s48 offen offset:2048\n"
    "s_waitcnt vmcnt(61)\n"
    "buffer_load_dwordx4 a[252:255], v0, s[40:43], s48 offen offset:3072\n"
    "s_add_u32 s48, s48, 4096\n"
    "buffer_load_dwordx4 v[192:195], v0, s[40:43], s48 offen\n"
    "s_waitcnt vmcnt(61)\n"
    "buffer_load_dwordx4 v[196:199], v0, s[40:43], s48 offen offset:1024\n"
    "buffer_load_dwordx4 v[232:235], v0, s[40:43], s48 offen offset:2048\n"
    "s_waitcnt vmcnt(61)\n"
    "buffer_load_dwordx4 v[236:239], v0, s[40:43], s48 offen offset:3072\n"
    "s_add_u32 s48, s48, 4096\n"
    "buffer_load_dwordx4 a[16:19], v0, s[40:43], s48 offen\n"
    "s_waitcnt vmcnt(61)\n"
    "buffer_load_dwordx4 a[20:23], v0, s[40:43], s48 offen offset:1024\n"
    "buffer_load_dwordx4 a[48:51], v0, s[40:43], s48 offen offset:2048\n"
    "s_waitcnt vmcnt(61)\n"
    "buffer_load_dwordx4 a[52:55], v0, s[40:43], s48 offen offset:3072\n"
    "s_add_u32 s48, s48, 4096\n"
    "buffer_load_dwordx4 a[80:83], v0, s[40:43], s48 offen\n"
    "s_waitcnt vmcnt(61)\n"
    "buffer_load_dwordx4 a[84:87], v0, s[40:43], s48 offen offset:1024\n"
    "buffer_load_dwordx4 a[112:115], v0, s[40:43], s48 offen offset:2048\n"
    "s_waitcnt vmcnt(61)\n"
    "buffer_load_dwordx4 a[116:119], v0, s[40:43], s48 offen offset:3072\n"
    "s_add_u32 s48, s48, 4096\n"
    "buffer_load_dwordx4 a[144:147], v0, s[40:43], s48 offen\n"
    "s_waitcnt vmcnt(61)\n"
    "buffer_load_dwordx4 a[148:151], v0, s[40:43], s48 offen offset:1024\n"
    "buffer_load_dwordx4 a[176:179], v0, s[40:43], s48 offen offset:2048\n"
    "s_waitcnt vmcnt(61)\n"
    "buffer_load_dwordx4 a[180:183], v0, s[40:43], s48 offen offset:3072\n"
    "s_add_u32 s48, s48, 4096\n"
    "buffer_load_dwordx4 a[216:219], v0, s[40:43], s48 offen\n"
    "s_waitcnt vmcnt(61)\n"
    "buffer_load_dwordx4 a[220:223], v0, s[40:43], s48 offen offset:1024\n"
    "buffer_load_dwordx4 v[160:163], v0, s[40:43], s48 offen offset:2048\n"
    "s_waitcnt vmcnt(61)\n"
    "buffer_load_dwordx4 v[164:167], v0, s[40:43], s48 offen offset:3072\n"
    "s_add_u32 s48, s48, 4096\n"
    "buffer_load_dwordx4 v[200:203], v0, s[40:43], s48 offen\n"
    "s_waitcnt vmcnt(61)\n"
    "buffer_load_dwordx4 v[204:207], v0, s[40:43], s48 offen offset:1024\n"
    "buffer_load_dwordx4 v[240:243], v0, s[40:43], s48 offen offset:2048\n"
    "s_waitcnt vmcnt(61)\n"
    "buffer_load_dwordx4 v[244:247], v0, s[40:43], s48 offen offset:3072\n"
    "s_add_u32 s48, s48, 4096\n"
    "buffer_load_dwordx4 a[24:27], v0, s[40:43], s48 offen\n"
    "s_waitcnt vmcnt(61)\n"
    "buffer_load_dwordx4 a[28:31], v0, s[40:43], s48 offen offset:1024\n"
    "buffer_load_dwordx4 a[56:59], v0, s[40:43], s48 offen offset:2048\n"
    "s_waitcnt vmcnt(61)\n"
    "buffer_load_dwordx4 a[60:63], v0, s[40:43], s48 offen offset:3072\n"
    "s_add_u32 s48, s48, 4096\n"
    "buffer_load_dwordx4 a[88:91], v0, s[40:43], s48 offen\n"
    "s_waitcnt vmcnt(61)\n"
    "buffer_load_dwordx4 a[92:95], v0, s[40:43], s48 offen offset:1024\n"
    "buffer_load_dwordx4 a[120:123], v0, s[40:43], s48 offen offset:2048\n"
    "s_waitcnt vmcnt(61)\n"
    "buffer_load_dwordx4 a[124:127], v0, s[40:43], s48 offen offset:3072\n"
    "s_add_u32 s48, s48, 4096\n"
    "buffer_load_dwordx4 a[152:155], v0, s[40:43], s48 offen\n"
    "s_waitcnt vmcnt(61)\n"
    "buffer_load_dwordx4 a[156:159], v0, s[40:43], s48 offen offset:1024\n"
    "buffer_load_dwordx4 a[184:187], v0, s[40:43], s48 offen offset:2048\n"
    "s_waitcnt vmcnt(61)\n"
    "buffer_load_dwordx4 a[188:191], v0, s[40:43], s48 offen offset:3072\n"
    "s_add_u32 s48, s48, 4096\n"
    "buffer_load_dwordx4 a[224:227], v0, s[40:43], s48 offen\n"
    "s_waitcnt vmcnt(61)\n"
    "buffer_load_dwordx4 a[228:231], v0, s[40:43], s48 offen offset:1024\n"
    "buffer_load_dwordx4 v[168:171], v0, s[40:43], s48 offen offset:2048\n"
    "s_waitcnt vmcnt(61)\n"
    "buffer_load_dwordx4 v[172:175], v0, s[40:43], s48 offen offset:3072\n"
    "s_add_u32 s48, s48, 4096\n"
    "buffer_load_dwordx4 v[208:211], v0, s[40:43], s48 offen\n"
    "s_waitcnt vmcnt(61)\n"
    "buffer_load_dwordx4 v[212:215], v0, s[40:43], s48 offen offset:1024\n"
    "buffer_load_dwordx4 v[248:251], v0, s[40:43], s48 offen offset:2048\n"
    "s_waitcnt vmcnt(61)\n"
    "buffer_load_dwordx4 v[252:255], v0, s[40:43], s48 offen offset:3072\n"
    "ds_write_b128 v14, v[16:19] offset:0\n"
    "ds_write_b128 v14, v[20:23] offset:1024\n"
    "s_waitcnt vmcnt(59)\n"
    "ds_write_b128 v14, v[24:27] offset:2048\n"
    "s_waitcnt vmcnt(58)\n"
    "ds_write_b128 v14, v[28:31] offset:3072\n"
    "ds_write_b128 v14, v[32:35] offset:4096\n"
    "ds_write_b128 v14, v[36:39] offset:5120\n"
    "s_waitcnt vmcnt(57)\n"
    "ds_write_b128 v14, v[40:43] offset:6144\n"
    "s_waitcnt vmcnt(56)\n"
    "ds_write_b128 v14, v[44:47] offset:7168\n"
    "ds_write_b128 v14, v[48:51] offset:8192\n"
    "ds_write_b128 v14, v[52:55] offset:9216\n"
    "s_waitcnt vmcnt(55)\n"
    "ds_write_b128 v14, v[56:59] offset:10240\n"
    "s_waitcnt vmcnt(54)\n"
    "ds_write_b128 v14, v[60:63] offset:11264\n"
    "ds_write_b128 v14, v[64:67] offset:12288\n"
    "ds_write_b128 v14, v[68:71] offset:13312\n"
    "s_waitcnt vmcnt(53)\n"
    "ds_write_b128 v14, v[72:75] offset:14336\n"
    "s_waitcnt vmcnt(52)\n"
    "ds_write_b128 v14, v[76:79] offset:15360\n"
    "s_waitcnt lgkmcnt(14)\n"
    "ds_write_b128 v14, v[80:83] offset:16384\n"
    "ds_write_b128 v14, v[84:87] offset:17408\n"
    "s_waitcnt lgkmcnt(14)\n"
    "s_waitcnt vmcnt(51)\n"
    "ds_write_b128 v14, v[88:91] offset:18432\n"
    "s_waitcnt vmcnt(50)\n"
    "ds_write_b128 v14, v[92:95] offset:19456\n"
    "s_waitcnt lgkmcnt(14)\n"
    "ds_write_b128 v14, v[96:99] offset:20480\n"
    "ds_write_b128 v14, v[100:103] offset:21504\n"
    "s_waitcnt lgkmcnt(14)\n"
    "s_waitcnt vmcnt(49)\n"
    "ds_write_b128 v14, v[104:107] offset:22528\n"
    "s_waitcnt vmcnt(48)\n"
    "ds_write_b128 v14, v[108:111] offset:23552\n"
    "s_waitcnt lgkmcnt(14)\n"
    "ds_write_b128 v14, v[112:115] offset:24576\n"
    "ds_write_b128 v14, v[116:119] offset:25600\n"
    "s_waitcnt lgkmcnt(14)\n"
    "ds_write_b128 v14, v[120:123] offset:26624\n"
    "ds_write_b128 v14, v[124:127] offset:27648\n"
    "s_waitcnt lgkmcnt(14)\n"
    "ds_write_b128 v14, v[128:131] offset:28672\n"
    "ds_write_b128 v14, v[132:135] offset:29696\n"
    "s_waitcnt lgkmcnt(14)\n"
    "ds_write_b128 v14, v[136:139] offset:30720\n"
    "ds_write_b128 v14, v[140:143] offset:31744\n"
    "s_waitcnt lgkmcnt(14)\n"
    "s_waitcnt vmcnt(0) lgkmcnt(0)\n"
    "v_mov_b32 v134, v14\n"
    "v_mov_b32 v128, -1\n"
    "v_mov_b32 v129, -1\n"
    "v_mov_b32 v130, -1\n"
    "v_mov_b32 v131, -1\n"
    "s_lshl_b32 s48, s44, 7\n"
    "s_add_u32 s48, s48, 22528\n"
    "v_mov_b32 v132, s48\n"
    "v_cmp_eq_u32 vcc, 0, v15\n"
    "v_mov_b32 v10, 2147483632\n"
    "s_nop 0\n"
    "v_cndmask_b32 v132, v10, v132, vcc\n"
    "v_and_b32 v9, 0xf0, v0\n"
    "v_lshrrev_b32 v10, 1, v12\n"
    "v_lshl_add_u32 v9, v10, 8, v9\n"
    "v_and_b32 v10, 1, v12\n"
    "v_lshl_add_u32 v9, v10, 3, v9\n"
    "s_lshl_b32 s48, s61, 8\n"
    "v_add_u32 v9, s48, v9\n"
    "cw_loopb_%=:\n"
    "v_mov_b32 v56, 0\n"
    "v_mov_b32 v57, 0\n"
    "v_mov_b32 v58, 0\n"
    "v_mov_b32 v59, 0\n"
    "v_mov_b32 v60, 0\n"
    "v_mov_b32 v61, 0\n"
    "v_mov_b32 v62, 0\n"
    "v_mov_b32 v63, 0\n"
    "v_mov_b32 v64, 0\n"
    "v_mov_b32 v65, 0\n"
    "v_mov_b32 v66, 0\n"
    "v_mov_b32 v67, 0\n"
    "v_mov_b32 v68, 0\n"
    "v_mov_b32 v69, 0\n"
    "v_mov_b32 v70, 0\n"
    "v_mov_b32 v71, 0\n"
    "v_mov_b32 v72, 0\n"
    "v_mov_b32 v73, 0\n"
    "v_mov_b32 v74, 0\n"
    "v_mov_b32 v75, 0\n"
    "v_mov_b32 v76, 0\n"
    "v_mov_b32 v77, 0\n"
    "v_mov_b32 v78, 0\n"
    "v_mov_b32 v79, 0\n"
    "ds_read_b128 v[80:83], v134 offset:0\n"
    "ds_read_b128 v[84:87], v134 offset:1024\n"
    "ds_read_b128 v[88:91], v134 offset:2048\n"
    "ds_read_b128 v[92:95], v134 offset:3072\n"
    "ds_read_b128 v[96:99], v134 offset:4096\n"
    "ds_read_b128 v[100:103], v134 offset:5120\n"
    "s_waitcnt lgkmcnt(0)\n"
    "s_barrier\n"
    "ds_read_b128 v[16:19], v0 offset:4096\n"
    "ds_read_b128 v[20:23], v0 offset:5120\n"
    "ds_read_b128 v[24:27], v0 offset:6144\n"
    "ds_read_b128 v[28:31], v0 offset:7168\n"
    "ds_read_b128 v[32:35], v0 offset:8192\n"
    "ds_read_b128 v[36:39], v0 offset:9216\n"
    "ds_read_b128 v[40:43], v0 offset:10240\n"
    "ds_read_b128 v[44:47], v0 offset:11264\n"
    "ds_read_b128 v[48:51], v0 offset:12288\n"
    "ds_read_b128 v[52:55], v0 offset:13312\n"
    "s_waitcnt lgkmcnt(9)\n"
    "v_smfmac_f32_16x16x64_bf16 v[56:59], v[16:19], v[80:87], v1\n"
    "ds_read_b128 v[80:83], v134 offset:6144\n"
    "v_smfmac_f32_16x16x64_bf16 v[60:63], v[16:19], a[0:7], v1\n"
    "ds_read_b128 v[84:87], v134 offset:7168\n"
    "v_smfmac_f32_16x16x64_bf16 v[64:67], v[16:19], a[8:15], v1\n"
    "v_smfmac_f32_16x16x64_bf16 v[68:71], v[16:19], v[88:95], v1\n"
    "ds_read_b128 v[88:91], v134 offset:8192\n"
    "v_smfmac_f32_16x16x64_bf16 v[72:75], v[16:19], a[16:23], v1\n"
    "ds_read_b128 v[92:95], v134 offset:9216\n"
    "v_smfmac_f32_16x16x64_bf16 v[76:79], v[16:19], a[24:31], v1\n"
    "s_waitcnt lgkmcnt(12)\n"
    "v_smfmac_f32_16x16x64_bf16 v[56:59], v[20:23], v[96:103], v1\n"
    "ds_read_b128 v[96:99], v134 offset:10240\n"
    "v_smfmac_f32_16x16x64_bf16 v[60:63], v[20:23], a[32:39], v1\n"
    "ds_read_b128 v[100:103], v134 offset:11264\n"
    "v_smfmac_f32_16x16x64_bf16 v[64:67], v[20:23], a[40:47], v1\n"
    "s_waitcnt lgkmcnt(4)\n"
    "v_smfmac_f32_16x16x64_bf16 v[68:71], v[20:23], v[80:87], v1\n"
    "ds_read_b128 v[80:83], v134 offset:12288\n"
    "v_smfmac_f32_16x16x64_bf16 v[72:75], v[20:23], a[48:55], v1\n"
    "ds_read_b128 v[84:87], v134 offset:13312\n"
    "v_smfmac_f32_16x16x64_bf16 v[76:79], v[20:23], a[56:63], v1\n"
    "s_waitcnt lgkmcnt(4)\n"
    "v_smfmac_f32_16x16x64_bf16 v[56:59], v[24:27], v[88:95], v1\n"
    "ds_read_b128 v[88:91], v134 offset:14336\n"
    "v_smfmac_f32_16x16x64_bf16 v[60:63], v[24:27], a[64:71], v1\n"
    "ds_read_b128 v[92:95], v134 offset:15360\n"
    "v_smfmac_f32_16x16x64_bf16 v[64:67], v[24:27], a[72:79], v1\n"
    "s_waitcnt lgkmcnt(4)\n"
    "v_smfmac_f32_16x16x64_bf16 v[68:71], v[24:27], v[96:103], v1\n"
    "ds_read_b128 v[96:99], v134 offset:16384\n"
    "v_smfmac_f32_16x16x64_bf16 v[72:75], v[24:27], a[80:87], v1\n"
    "ds_read_b128 v[100:103], v134 offset:17408\n"
    "v_smfmac_f32_16x16x64_bf16 v[76:79], v[24:27], a[88:95], v1\n"
    "s_waitcnt lgkmcnt(4)\n"
    "v_smfmac_f32_16x16x64_bf16 v[56:59], v[28:31], v[80:87], v1\n"
    "ds_read_b128 v[80:83], v134 offset:18432\n"
    "v_smfmac_f32_16x16x64_bf16 v[60:63], v[28:31], a[96:103], v1\n"
    "ds_read_b128 v[84:87], v134 offset:19456\n"
    "v_smfmac_f32_16x16x64_bf16 v[64:67], v[28:31], a[104:111], v1\n"
    "s_waitcnt lgkmcnt(4)\n"
    "v_smfmac_f32_16x16x64_bf16 v[68:71], v[28:31], v[88:95], v1\n"
    "ds_read_b128 v[88:91], v134 offset:20480\n"
    "v_smfmac_f32_16x16x64_bf16 v[72:75], v[28:31], a[112:119], v1\n"
    "ds_read_b128 v[92:95], v134 offset:21504\n"
    "v_smfmac_f32_16x16x64_bf16 v[76:79], v[28:31], a[120:127], v1\n"
    "s_waitcnt lgkmcnt(4)\n"
    "v_smfmac_f32_16x16x64_bf16 v[56:59], v[32:35], v[96:103], v1\n"
    "ds_read_b128 v[96:99], v134 offset:22528\n"
    "v_smfmac_f32_16x16x64_bf16 v[60:63], v[32:35], a[128:135], v1\n"
    "ds_read_b128 v[100:103], v134 offset:23552\n"
    "v_smfmac_f32_16x16x64_bf16 v[64:67], v[32:35], a[136:143], v1\n"
    "s_waitcnt lgkmcnt(4)\n"
    "v_smfmac_f32_16x16x64_bf16 v[68:71], v[32:35], v[80:87], v1\n"
    "ds_read_b128 v[80:83], v134 offset:24576\n"
    "v_smfmac_f32_16x16x64_bf16 v[72:75], v[32:35], a[144:151], v1\n"
    "ds_read_b128 v[84:87], v134 offset:25600\n"
    "v_smfmac_f32_16x16x64_bf16 v[76:79], v[32:35], a[152:159], v1\n"
    "s_waitcnt lgkmcnt(4)\n"
    "v_smfmac_f32_16x16x64_bf16 v[56:59], v[36:39], v[88:95], v1\n"
    "ds_read_b128 v[88:91], v134 offset:26624\n"
    "v_smfmac_f32_16x16x64_bf16 v[60:63], v[36:39], a[160:167], v1\n"
    "ds_read_b128 v[92:95], v134 offset:27648\n"
    "v_smfmac_f32_16x16x64_bf16 v[64:67], v[36:39], a[168:175], v1\n"
    "s_waitcnt lgkmcnt(4)\n"
    "v_smfmac_f32_16x16x64_bf16 v[68:71], v[36:39], v[96:103], v1\n"
    "ds_read_b128 v[96:99], v134 offset:28672\n"
    "v_smfmac_f32_16x16x64_bf16 v[72:75], v[36:39], a[176:183], v1\n"
    "ds_read_b128 v[100:103], v134 offset:29696\n"
    "v_smfmac_f32_16x16x64_bf16 v[76:79], v[36:39], a[184:191], v1\n"
    "s_waitcnt lgkmcnt(4)\n"
    "v_smfmac_f32_16x16x64_bf16 v[56:59], v[40:43], v[80:87], v1\n"
    "ds_read_b128 v[80:83], v134 offset:30720\n"
    "v_smfmac_f32_16x16x64_bf16 v[60:63], v[40:43], a[192:199], v1\n"
    "ds_read_b128 v[84:87], v134 offset:31744\n"
    "v_smfmac_f32_16x16x64_bf16 v[64:67], v[40:43], a[200:207], v1\n"
    "v_smfmac_f32_16x16x64_bf16 v[68:71], v[40:43], a[208:215], v1\n"
    "v_smfmac_f32_16x16x64_bf16 v[72:75], v[40:43], a[216:223], v1\n"
    "v_smfmac_f32_16x16x64_bf16 v[76:79], v[40:43], a[224:231], v1\n"
    "s_waitcnt lgkmcnt(4)\n"
    "v_smfmac_f32_16x16x64_bf16 v[56:59], v[44:47], v[88:95], v1\n"
    "v_smfmac_f32_16x16x64_bf16 v[60:63], v[44:47], a[232:239], v1\n"
    "v_smfmac_f32_16x16x64_bf16 v[64:67], v[44:47], a[240:247], v1\n"
    "v_smfmac_f32_16x16x64_bf16 v[68:71], v[44:47], a[248:255], v1\n"
    "v_smfmac_f32_16x16x64_bf16 v[72:75], v[44:47], v[160:167], v1\n"
    "v_smfmac_f32_16x16x64_bf16 v[76:79], v[44:47], v[168:175], v1\n"
    "s_waitcnt lgkmcnt(2)\n"
    "v_smfmac_f32_16x16x64_bf16 v[56:59], v[48:51], v[96:103], v1\n"
    "v_smfmac_f32_16x16x64_bf16 v[60:63], v[48:51], v[176:183], v1\n"
    "v_smfmac_f32_16x16x64_bf16 v[64:67], v[48:51], v[184:191], v1\n"
    "v_smfmac_f32_16x16x64_bf16 v[68:71], v[48:51], v[192:199], v1\n"
    "v_smfmac_f32_16x16x64_bf16 v[72:75], v[48:51], v[200:207], v1\n"
    "v_smfmac_f32_16x16x64_bf16 v[76:79], v[48:51], v[208:215], v1\n"
    "s_waitcnt lgkmcnt(0)\n"
    "v_smfmac_f32_16x16x64_bf16 v[56:59], v[52:55], v[80:87], v1\n"
    "v_smfmac_f32_16x16x64_bf16 v[60:63], v[52:55], v[216:223], v1\n"
    "v_smfmac_f32_16x16x64_bf16 v[64:67], v[52:55], v[224:231], v1\n"
    "v_smfmac_f32_16x16x64_bf16 v[68:71], v[52:55], v[232:239], v1\n"
    "v_smfmac_f32_16x16x64_bf16 v[72:75], v[52:55], v[240:247], v1\n"
    "v_smfmac_f32_16x16x64_bf16 v[76:79], v[52:55], v[248:255], v1\n"
    "v_mov_b32 v11, v11\n"
    "v_mov_b32 v11, v11\n"
    "v_mov_b32 v11, v11\n"
    "v_mov_b32 v11, v11\n"
    "v_permlane32_swap_b32 v56, v60\n"
    "v_permlane32_swap_b32 v57, v61\n"
    "v_permlane32_swap_b32 v58, v62\n"
    "v_permlane32_swap_b32 v59, v63\n"
    "v_pk_add_f32 v[56:57], v[56:57], v[60:61]\n"
    "v_pk_add_f32 v[58:59], v[58:59], v[62:63]\n"
    "v_cvt_pk_bf16_f32 v60, v56, v57\n"
    "v_cvt_pk_bf16_f32 v61, v58, v59\n"
    "ds_write_b64 v9, v[60:61] offset:0\n"
    "v_permlane32_swap_b32 v64, v68\n"
    "v_permlane32_swap_b32 v65, v69\n"
    "v_permlane32_swap_b32 v66, v70\n"
    "v_permlane32_swap_b32 v67, v71\n"
    "v_pk_add_f32 v[64:65], v[64:65], v[68:69]\n"
    "v_pk_add_f32 v[66:67], v[66:67], v[70:71]\n"
    "v_cvt_pk_bf16_f32 v68, v64, v65\n"
    "v_cvt_pk_bf16_f32 v69, v66, v67\n"
    "ds_write_b64 v9, v[68:69] offset:512\n"
    "v_permlane32_swap_b32 v72, v76\n"
    "v_permlane32_swap_b32 v73, v77\n"
    "v_permlane32_swap_b32 v74, v78\n"
    "v_permlane32_swap_b32 v75, v79\n"
    "v_pk_add_f32 v[72:73], v[72:73], v[76:77]\n"
    "v_pk_add_f32 v[74:75], v[74:75], v[78:79]\n"
    "v_cvt_pk_bf16_f32 v76, v72, v73\n"
    "v_cvt_pk_bf16_f32 v77, v74, v75\n"
    "ds_write_b64 v9, v[76:77] offset:1024\n"
    "s_cmp_lg_u32 s45, 1\n"
    "s_cbranch_scc1 cw_n1_%=\n"
    "s_add_u32 s49, s47, 3\n"
    "s_and_b32 s49, s49, 3\n"
    "s_mul_i32 s54, s49, 131072\n"
    "s_add_u32 s54, s54, 786432\n"
    "buffer_store_dwordx4 v[128:131], v3, s[40:43], s54 offen sc1\n"
    "s_nop 1\n"
    "cw_n1_%=:\n"
    "s_waitcnt lgkmcnt(0)\n"
    "s_barrier\n"
    "s_cmp_lg_u32 s45, 2\n"
    "s_cbranch_scc1 cw_n2_%=\n"
    "ds_read_b128 v[16:19], v0 offset:0\n"
    "ds_read_b128 v[20:23], v0 offset:1024\n"
    "ds_read_b128 v[24:27], v0 offset:2048\n"
    "ds_read_b128 v[28:31], v0 offset:3072\n"
    "s_waitcnt lgkmcnt(0)\n"
    "cw_n2_%=:\n"
    "s_waitcnt vmcnt(0)\n"
    "s_barrier\n"
    "s_cmp_lg_u32 s45, 2\n"
    "s_cbranch_scc1 cw_n3_%=\n"
    "s_mul_i32 s49, s47, 80\n"
    "s_add_u32 s49, s49, s44\n"
    "s_lshl_b32 s49, s49, 12\n"
    "s_add_u32 s49, s49, 196673536\n"
    "v_mov_b32 v10, s47\n"
    "buffer_store_dword v10, v132, s[40:43], s58 offen sc1\n"
    "buffer_store_dwordx4 v[16:19], v0, s[40:43], s49 offen sc1\n"
    "buffer_store_dwordx4 v[20:23], v0, s[40:43], s49 offen offset:1024 sc1\n"
    "buffer_store_dwordx4 v[24:27], v0, s[40:43], s49 offen offset:2048 sc1\n"
    "buffer_store_dwordx4 v[28:31], v0, s[40:43], s49 offen offset:3072 sc1\n"
    "s_nop 1\n"
    "cw_n3_%=:\n"
    "s_add_u32 s47, s47, 1\n"
    "s_cmp_lt_u32 s47, 64\n"
    "s_cbranch_scc1 cw_loopb_%=\n"
    "s_cmp_lg_u32 s45, 2\n"
    "s_cbranch_scc1 cw_end_%=\n"
    "v_mov_b32 v10, 64\n"
    "s_waitcnt vmcnt(0)\n"
    "buffer_store_dword v10, v132, s[40:43], s58 offen sc1\n"
    "s_nop 1\n"
    "s_branch cw_end_%=\n"
    "s_branch cw_end_%=\n"
    "cw_abort_%=:\n"
    "v_mov_b32 v10, 1\n"
    "v_mov_b32 v11, 1024\n"
    "buffer_store_dword v10, v11, s[40:43], s58 offen sc1\n"
    "cw_end_%=:\n"
    "s_endpgm\n"
    :: ROLE_INPUTS : ROLE_CLOBBERS);
}
DEV void role_projq(unsigned lo, unsigned hi, int id, int w, int aux, const Tune& tn) {
    asm volatile(
    "s_mov_b32 s40, %0\n"
    "s_mov_b32 s41, %1\n"
    "s_mov_b32 s42, %2\n"
    "s_mov_b32 s43, %3\n"
    "s_mov_b32 s44, %4\n"
    "s_mov_b32 s45, %5\n"
    "s_mov_b32 s46, %6\n"
    "s_mov_b32 s66, %7\n"
    "s_mov_b32 s67, %8\n"
    "s_mov_b32 s68, %9\n"
    "s_mov_b32 s69, %10\n"
    "s_mov_b32 s70, %11\n"
    "s_mov_b32 s71, %12\n"
    "s_mov_b32 s72, %13\n"
    "s_mov_b32 s73, %14\n"
    "s_mov_b64 exec, -1\n"
    "v_mbcnt_lo_u32_b32 v15, -1, 0\n"
    "v_mbcnt_hi_u32_b32 v15, -1, v15\n"
    "v_lshlrev_b32 v0, 4, v15\n"
    "s_mov_b32 s58, 2097152\n"
    "s_mov_b32 s92, 0\n"
    "s_lshl_b32 s48, s46, 5\n"
    "s_add_u32 s48, s48, s44\n"
    "s_mul_i32 s48, s48, 12\n"
    "s_mul_i32 s49, s45, 3\n"
    "s_add_u32 s48, s48, s49\n"
    "s_mul_i32 s48, s48, 20480\n"
    "s_add_u32 s48, s48, 2162688\n"
    "s_mul_i32 s62, s45, 10240\n"
    "s_add_u32 s50, s62, 59834368\n"
    "s_mov_b32 s61, s62\n"
    "s_add_u32 m0, s61, 0\n"
    "s_add_u32 s51, s50, 0\n"
    "buffer_load_dwordx4 v0, s[40:43], s51 offen offset:0 lds\n"
    "buffer_load_dwordx4 v0, s[40:43], s51 offen offset:1024 lds\n"
    "buffer_load_dwordx4 v0, s[40:43], s51 offen offset:2048 lds\n"
    "buffer_load_dwordx4 v0, s[40:43], s51 offen offset:3072 lds\n"
    "s_add_u32 m0, s61, 4096\n"
    "s_add_u32 s51, s50, 4096\n"
    "buffer_load_dwordx4 v0, s[40:43], s51 offen offset:0 lds\n"
    "buffer_load_dwordx4 v0, s[40:43], s51 offen offset:1024 lds\n"
    "buffer_load_dwordx4 v0, s[40:43], s51 offen offset:2048 lds\n"
    "buffer_load_dwordx4 v0, s[40:43], s51 offen offset:3072 lds\n"
    "s_add_u32 m0, s61, 8192\n"
    "s_add_u32 s51, s50, 8192\n"
    "buffer_load_dwordx4 v0, s[40:43], s51 offen offset:0 lds\n"
    "buffer_load_dwordx4 v0, s[40:43], s51 offen offset:1024 lds\n"
    "s_add_u32 s50, s50, 40960\n"
    "s_add_u32 s61, s61, 40960\n"
    "s_cmp_ge_u32 s61, 122880\n"
    "s_cselect_b32 s51, 122880, 0\n"
    "s_sub_u32 s61, s61, s51\n"
    "s_add_u32 m0, s61, 0\n"
    "s_add_u32 s51, s50, 0\n"
    "buffer_load_dwordx4 v0, s[40:43], s51 offen offset:0 lds\n"
    "buffer_load_dwordx4 v0, s[40:43], s51 offen offset:1024 lds\n"
    "buffer_load_dwordx4 v0, s[40:43], s51 offen offset:2048 lds\n"
    "buffer_load_dwordx4 v0, s[40:43], s51 offen offset:3072 lds\n"
    "s_add_u32 m0, s61, 4096\n"
    "s_add_u32 s51, s50, 4096\n"
    "buffer_load_dwordx4 v0, s[40:43], s51 offen offset:0 lds\n"
    "buffer_load_dwordx4 v0, s[40:43], s51 offen offset:1024 lds\n"
    "buffer_load_dwordx4 v0, s[40:43], s51 offen offset:2048 lds\n"
    "buffer_load_dwordx4 v0, s[40:43], s51 offen offset:3072 lds\n"
    "s_add_u32 m0, s61, 8192\n"
    "s_add_u32 s51, s50, 8192\n"
    "buffer_load_dwordx4 v0, s[40:43], s51 offen offset:0 lds\n"
    "buffer_load_dwordx4 v0, s[40:43], s51 offen offset:1024 lds\n"
    "s_add_u32 s50, s50, 40960\n"
    "s_add_u32 s61, s61, 40960\n"
    "s_cmp_ge_u32 s61, 122880\n"
    "s_cselect_b32 s51, 122880, 0\n"
    "s_sub_u32 s61, s61, s51\n"
    "buffer_load_dwordx4 a[0:3], v0, s[40:43], s48 offen\n"
    "buffer_load_dwordx4 a[4:7], v0, s[40:43], s48 offen offset:1024\n"
    "buffer_load_dwordx4 a[8:11], v0, s[40:43], s48 offen offset:2048\n"
    "buffer_load_dwordx4 a[12:15], v0, s[40:43], s48 offen offset:3072\n"
    "s_add_u32 s48, s48, 4096\n"
    "buffer_load_dwordx4 a[16:19], v0, s[40:43], s48 offen\n"
    "buffer_load_dwordx4 a[20:23], v0, s[40:43], s48 offen offset:1024\n"
    "buffer_load_dwordx4 a[24:27], v0, s[40:43], s48 offen offset:2048\n"
    "buffer_load_dwordx4 a[28:31], v0, s[40:43], s48 offen offset:3072\n"
    "s_add_u32 s48, s48, 4096\n"
    "buffer_load_dwordx4 a[32:35], v0, s[40:43], s48 offen\n"
    "buffer_load_dwordx4 a[36:39], v0, s[40:43], s48 offen offset:1024\n"
    "buffer_load_dwordx4 a[40:43], v0, s[40:43], s48 offen offset:2048\n"
    "buffer_load_dwordx4 a[44:47], v0, s[40:43], s48 offen offset:3072\n"
    "s_add_u32 s48, s48, 4096\n"
    "buffer_load_dwordx4 a[48:51], v0, s[40:43], s48 offen\n"
    "buffer_load_dwordx4 a[52:55], v0, s[40:43], s48 offen offset:1024\n"
    "buffer_load_dwordx4 a[56:59], v0, s[40:43], s48 offen offset:2048\n"
    "buffer_load_dwordx4 a[60:63], v0, s[40:43], s48 offen offset:3072\n"
    "s_add_u32 s48, s48, 4096\n"
    "buffer_load_dwordx4 a[64:67], v0, s[40:43], s48 offen\n"
    "buffer_load_dwordx4 a[68:71], v0, s[40:43], s48 offen offset:1024\n"
    "buffer_load_dwordx4 a[72:75], v0, s[40:43], s48 offen offset:2048\n"
    "buffer_load_dwordx4 a[76:79], v0, s[40:43], s48 offen offset:3072\n"
    "s_add_u32 s48, s48, 4096\n"
    "buffer_load_dwordx4 a[80:83], v0, s[40:43], s48 offen\n"
    "buffer_load_dwordx4 a[84:87], v0, s[40:43], s48 offen offset:1024\n"
    "buffer_load_dwordx4 a[88:91], v0, s[40:43], s48 offen offset:2048\n"
    "buffer_load_dwordx4 a[92:95], v0, s[40:43], s48 offen offset:3072\n"
    "s_add_u32 s48, s48, 4096\n"
    "buffer_load_dwordx4 a[96:99], v0, s[40:43], s48 offen\n"
    "buffer_load_dwordx4 a[100:103], v0, s[40:43], s48 offen offset:1024\n"
    "buffer_load_dwordx4 a[104:107], v0, s[40:43], s48 offen offset:2048\n"
    "buffer_load_dwordx4 a[108:111], v0, s[40:43], s48 offen offset:3072\n"
    "s_add_u32 s48, s48, 4096\n"
    "buffer_load_dwordx4 a[112:115], v0, s[40:43], s48 offen\n"
    "buffer_load_dwordx4 a[116:119], v0, s[40:43], s48 offen offset:1024\n"
    "buffer_load_dwordx4 a[120:123], v0, s[40:43], s48 offen offset:2048\n"
    "buffer_load_dwordx4 a[124:127], v0, s[40:43], s48 offen offset:3072\n"
    "s_add_u32 s48, s48, 4096\n"
    "buffer_load_dwordx4 a[128:131], v0, s[40:43], s48 offen\n"
    "buffer_load_dwordx4 a[132:135], v0, s[40:43], s48 offen offset:1024\n"
    "buffer_load_dwordx4 a[136:139], v0, s[40:43], s48 offen offset:2048\n"
    "buffer_load_dwordx4 a[140:143], v0, s[40:43], s48 offen offset:3072\n"
    "s_add_u32 s48, s48, 4096\n"
    "buffer_load_dwordx4 a[144:147], v0, s[40:43], s48 offen\n"
    "buffer_load_dwordx4 a[148:151], v0, s[40:43], s48 offen offset:1024\n"
    "buffer_load_dwordx4 a[152:155], v0, s[40:43], s48 offen offset:2048\n"
    "buffer_load_dwordx4 a[156:159], v0, s[40:43], s48 offen offset:3072\n"
    "s_add_u32 s48, s48, 4096\n"
    "buffer_load_dwordx4 a[160:163], v0, s[40:43], s48 offen\n"
    "buffer_load_dwordx4 a[164:167], v0, s[40:43], s48 offen offset:1024\n"
    "buffer_load_dwordx4 a[168:171], v0, s[40:43], s48 offen offset:2048\n"
    "s_waitcnt vmcnt(61)\n"
    "buffer_load_dwordx4 a[172:175], v0, s[40:43], s48 offen offset:3072\n"
    "s_add_u32 s48, s48, 4096\n"
    "buffer_load_dwordx4 a[176:179], v0, s[40:43], s48 offen\n"
    "s_waitcnt vmcnt(61)\n"
    "buffer_load_dwordx4 a[180:183], v0, s[40:43], s48 offen offset:1024\n"
    "buffer_load_dwordx4 a[184:187], v0, s[40:43], s48 offen offset:2048\n"
    "s_waitcnt vmcnt(61)\n"
    "buffer_load_dwordx4 a[188:191], v0, s[40:43], s48 offen offset:3072\n"
    "s_add_u32 s48, s48, 4096\n"
    "buffer_load_dwordx4 a[192:195], v0, s[40:43], s48 offen\n"
    "s_waitcnt vmcnt(61)\n"
    "buffer_load_dwordx4 a[196:199], v0, s[40:43], s48 offen offset:1024\n"
    "buffer_load_dwordx4 a[200:203], v0, s[40:43], s48 offen offset:2048\n"
    "s_waitcnt vmcnt(61)\n"
    "buffer_load_dwordx4 a[204:207], v0, s[40:43], s48 offen offset:3072\n"
    "s_add_u32 s48, s48, 4096\n"
    "buffer_load_dwordx4 a[208:211], v0, s[40:43], s48 offen\n"
    "s_waitcnt vmcnt(61)\n"
    "buffer_load_dwordx4 a[212:215], v0, s[40:43], s48 offen offset:1024\n"
    "buffer_load_dwordx4 a[216:219], v0, s[40:43], s48 offen offset:2048\n"
    "s_waitcnt vmcnt(61)\n"
    "buffer_load_dwordx4 a[220:223], v0, s[40:43], s48 offen offset:3072\n"
    "s_add_u32 s48, s48, 4096\n"
    "buffer_load_dwordx4 a[224:227], v0, s[40:43], s48 offen\n"
    "s_waitcnt vmcnt(61)\n"
    "buffer_load_dwordx4 a[228:231], v0, s[40:43], s48 offen offset:1024\n"
    "buffer_load_dwordx4 a[232:235], v0, s[40:43], s48 offen offset:2048\n"
    "s_waitcnt vmcnt(61)\n"
    "buffer_load_dwordx4 a[236:239], v0, s[40:43], s48 offen offset:3072\n"
    "v_mov_b32 v1, v0\n"
    "s_mov_b32 s64, 0\n"
    "s_lshl_b32 s48, s44, 7\n"
    "s_add_u32 s49, s48, 18432\n"
    "v_mov_b32 v3, s49\n"
    "v_or_b32 v4, s45, v15\n"
    "v_cmp_eq_u32 vcc, 0, v4\n"
    "v_mov_b32 v4, 2147483632\n"
    "s_nop 0\n"
    "v_cndmask_b32 v3, v4, v3, vcc\n"
    "v_lshrrev_b32 v13, 4, v15\n"
    "v_and_b32 v14, 15, v15\n"
    "s_lshl_b32 s49, s45, 11\n"
    "s_add_u32 s49, s49, 122880\n"
    "v_add_u32 v6, s49, v0\n"
    "s_lshr_b32 s49, s45, 1\n"
    "s_lshl_b32 s49, s49, 11\n"
    "s_and_b32 s53, s45, 1\n"
    "s_lshl_b32 s53, s53, 3\n"
    "s_add_u32 s49, s49, s53\n"
    "s_add_u32 s49, s49, 131072\n"
    "v_add_u32 v7, s49, v0\n"
    "s_lshl_b32 s49, s45, 10\n"
    "s_add_u32 s49, s49, 122880\n"
    "v_add_u32 v8, s49, v0\n"
    "v_mov_b32 v9, 0\n"
    "s_add_u32 s49, s48, 14336\n"
    "v_mov_b32 v10, s49\n"
    "v_and_b32 v11, 7, v15\n"
    "v_lshlrev_b32 v11, 4, v11\n"
    "v_lshl_add_u32 v11, v13, 7, v11\n"
    "s_bfe_u32 s49, s44, 0x20001\n"
    "s_lshl_b32 s49, s49, 8\n"
    "v_add_u32 v12, s49, v0\n"
    "v_cmp_gt_u32 vcc, 16, v15\n"
    "v_mov_b32 v4, 2147483632\n"
    "s_nop 0\n"
    "v_cndmask_b32 v12, v4, v12, vcc\n"
    "s_lshl_b32 s49, s45, 4\n"
    "v_add_u32 v14, s49, v14\n"
    "v_lshlrev_b32 v13, 2, v13\n"
    "v_sub_u32 v14, v14, v13\n"
    "v_mov_b32 v4, 0xff800000\n"
    "v_cmp_gt_i32 vcc, 0, v14\n"
    "s_nop 1\n"
    "v_cndmask_b32 v212, 0, v4, vcc\n"
    "v_cmp_gt_i32 vcc, 1, v14\n"
    "s_nop 1\n"
    "v_cndmask_b32 v213, 0, v4, vcc\n"
    "v_cmp_gt_i32 vcc, 2, v14\n"
    "s_nop 1\n"
    "v_cndmask_b32 v214, 0, v4, vcc\n"
    "v_cmp_gt_i32 vcc, 3, v14\n"
    "s_nop 1\n"
    "v_cndmask_b32 v215, 0, v4, vcc\n"
    "v_cmp_gt_i32 vcc, 16, v14\n"
    "s_nop 1\n"
    "v_cndmask_b32 v216, 0, v4, vcc\n"
    "v_cmp_gt_i32 vcc, 17, v14\n"
    "s_nop 1\n"
    "v_cndmask_b32 v217, 0, v4, vcc\n"
    "v_cmp_gt_i32 vcc, 18, v14\n"
    "s_nop 1\n"
    "v_cndmask_b32 v218, 0, v4, vcc\n"
    "v_cmp_gt_i32 vcc, 19, v14\n"
    "s_nop 1\n"
    "v_cndmask_b32 v219, 0, v4, vcc\n"
    "v_cmp_lt_u32 vcc, 31, v15\n"
    "s_nop 1\n"
    "v_cndmask_b32 v220, 0, v4, vcc\n"
    "v_cndmask_b32 v221, 0, v4, vcc\n"
    "v_cndmask_b32 v222, 0, v4, vcc\n"
    "v_cndmask_b32 v223, 0, v4, vcc\n"
    "s_mov_b32 s47, 0\n"
    "s_waitcnt vmcnt(0)\n"
    "s_waitcnt lgkmcnt(0)\n"
    "s_barrier\n"
    "v_add_u32 v2, 8, v6\n"
    "ds_read_b128 v[80:83], v1 offset:0\n"
    "ds_read_b128 v[84:87], v1 offset:20480\n"
    "ds_read_b128 v[88:91], v1 offset:1024\n"
    "ds_read_b128 v[92:95], v1 offset:21504\n"
    "ds_read_b128 v[96:99], v1 offset:2048\n"
    "pq_loop_%=:\n"
    "s_cmp_gt_u32 s45, 1\n"
    "s_cbranch_scc1 pq_na0_%=\n"
    "s_cmp_eq_u32 s47, 0\n"
    "s_cbranch_scc1 pq_na0_%=\n"
    "s_mov_b32 s60, 0\n"
    "v_cmp_gt_u32 vcc, s47, v9\n"
    "s_nop 4\n"
    "s_cbranch_vccz pq_kma0_ok_%=\n"
    "pq_kma0_poll_%=:\n"
    "buffer_load_dword v9, v10, s[40:43], s58 offen sc1\n"
    "s_waitcnt vmcnt(0)\n"
    "v_cmp_gt_u32 vcc, s47, v9\n"
    "s_nop 4\n"
    "s_cbranch_vccz pq_kma0_ok_%=\n"
    "s_sleep 2\n"
    "s_add_u32 s60, s60, 1\n"
    "s_cmp_gt_u32 s60, 65536\n"
    "s_cbranch_scc1 pq_abort_%=\n"
    "s_and_b32 s59, s60, 63\n"
    "s_cbranch_scc1 pq_kma0_poll_%=\n"
    "v_mov_b32 v4, 1024\n"
    "buffer_load_dword v4, v4, s[40:43], s58 offen sc1\n"
    "s_waitcnt vmcnt(0)\n"
    "v_readfirstlane_b32 s59, v4\n"
    "s_nop 0\n"
    "s_cmp_lg_u32 s59, 0\n"
    "s_nop 2\n"
    "s_cbranch_scc1 pq_end_%=\n"
    "s_branch pq_kma0_poll_%=\n"
    "pq_kma0_ok_%=:\n"
    "s_sub_u32 s53, s47, 1\n"
    "s_lshl_b32 s53, s53, 5\n"
    "s_add_u32 s53, s53, s44\n"
    "s_mul_i32 s54, s53, 12288\n"
    "s_add_u32 s54, s54, 62455808\n"
    "s_add_u32 s55, s54, 4096\n"
    "s_add_u32 s56, s54, 8192\n"
    "s_mul_i32 s57, s53, 3072\n"
    "s_add_u32 s57, s57, 217645056\n"
    "buffer_load_dwordx4 v[116:119], v0, s[40:43], s54 offen sc1\n"
    "buffer_load_dwordx4 v[120:123], v0, s[40:43], s54 offen offset:1024 sc1\n"
    "buffer_load_dwordx4 v[124:127], v0, s[40:43], s54 offen offset:3072 sc1\n"
    "buffer_load_dwordx4 v[128:131], v0, s[40:43], s55 offen sc1\n"
    "buffer_load_dwordx4 v[132:135], v0, s[40:43], s55 offen offset:2048 sc1\n"
    "buffer_load_dwordx4 v[136:139], v0, s[40:43], s55 offen offset:3072 sc1\n"
    "buffer_load_dwordx4 v[140:143], v0, s[40:43], s56 offen offset:1024 sc1\n"
    "buffer_load_dwordx4 v[144:147], v0, s[40:43], s56 offen offset:2048 sc1\n"
    "buffer_load_dwordx4 v[148:151], v0, s[40:43], s54 offen offset:2048 sc1\n"
    "buffer_load_dwordx4 v[152:155], v0, s[40:43], s55 offen offset:1024 sc1\n"
    "buffer_load_dwordx4 v[156:159], v0, s[40:43], s56 offen sc1\n"
    "buffer_load_dwordx4 v[160:163], v0, s[40:43], s56 offen offset:3072 sc1\n"
    "buffer_load_dwordx4 v[164:167], v11, s[40:43], s57 offen sc1\n"
    "buffer_load_dwordx4 v[168:171], v11, s[40:43], s57 offen offset:512 sc1\n"
    "buffer_load_dwordx4 v[172:175], v11, s[40:43], s57 offen offset:1024 sc1\n"
    "buffer_load_dwordx4 v[176:179], v11, s[40:43], s57 offen offset:1536 sc1\n"
    "buffer_load_dwordx4 v[180:183], v11, s[40:43], s57 offen offset:2048 sc1\n"
    "buffer_load_dwordx4 v[184:187], v11, s[40:43], s57 offen offset:2560 sc1\n"
    "s_nop 1\n"
    "pq_na0_%=:\n"
    "s_waitcnt lgkmcnt(4)\n"
    "v_mfma_f32_16x16x32_bf16 v[16:19], a[0:3], v[80:83], 0\n"
    "v_mfma_f32_16x16x32_bf16 v[24:27], a[80:83], v[80:83], 0\n"
    "v_mfma_f32_16x16x32_bf16 v[32:35], a[160:163], v[80:83], 0\n"
    "ds_read_b128 v[80:83], v1 offset:22528\n"
    "s_waitcnt lgkmcnt(4)\n"
    "v_mfma_f32_16x16x32_bf16 v[20:23], a[0:3], v[84:87], 0\n"
    "v_mfma_f32_16x16x32_bf16 v[28:31], a[80:83], v[84:87], 0\n"
    "v_mfma_f32_16x16x32_bf16 v[36:39], a[160:163], v[84:87], 0\n"
    "ds_read_b128 v[84:87], v1 offset:3072\n"
    "s_waitcnt lgkmcnt(4)\n"
    "v_mfma_f32_16x16x32_bf16 v[16:19], a[4:7], v[88:91], v[16:19]\n"
    "v_mfma_f32_16x16x32_bf16 v[24:27], a[84:87], v[88:91], v[24:27]\n"
    "v_mfma_f32_16x16x32_bf16 v[32:35], a[164:167], v[88:91], v[32:35]\n"
    "ds_read_b128 v[88:91], v1 offset:23552\n"
    "s_waitcnt lgkmcnt(4)\n"
    "v_mfma_f32_16x16x32_bf16 v[20:23], a[4:7], v[92:95], v[20:23]\n"
    "v_cvt_pk_bf16_f32 v104, v40, v41\n"
    "v_mfma_f32_16x16x32_bf16 v[28:31], a[84:87], v[92:95], v[28:31]\n"
    "v_cvt_pk_bf16_f32 v105, v42, v43\n"
    "v_mfma_f32_16x16x32_bf16 v[36:39], a[164:167], v[92:95], v[36:39]\n"
    "ds_read_b128 v[92:95], v1 offset:4096\n"
    "s_waitcnt lgkmcnt(4)\n"
    "v_mfma_f32_16x16x32_bf16 v[16:19], a[8:11], v[96:99], v[16:19]\n"
    "v_cvt_pk_bf16_f32 v106, v44, v45\n"
    "v_mfma_f32_16x16x32_bf16 v[24:27], a[88:91], v[96:99], v[24:27]\n"
    "v_cvt_pk_bf16_f32 v107, v46, v47\n"
    "v_mfma_f32_16x16x32_bf16 v[32:35], a[168:171], v[96:99], v[32:35]\n"
    "ds_read_b128 v[96:99], v1 offset:24576\n"
    "s_waitcnt lgkmcnt(4)\n"
    "v_mfma_f32_16x16x32_bf16 v[20:23], a[8:11], v[80:83], v[20:23]\n"
    "ds_write2st64_b64 v6, v[104:105], v[106:107] offset0:24 offset1:26\n"
    "v_mfma_f32_16x16x32_bf16 v[28:31], a[88:91], v[80:83], v[28:31]\n"
    "v_cvt_pk_bf16_f32 v108, v48, v49\n"
    "v_mfma_f32_16x16x32_bf16 v[36:39], a[168:171], v[80:83], v[36:39]\n"
    "ds_read_b128 v[80:83], v1 offset:5120\n"
    "s_waitcnt lgkmcnt(5)\n"
    "v_mfma_f32_16x16x32_bf16 v[16:19], a[12:15], v[84:87], v[16:19]\n"
    "v_cvt_pk_bf16_f32 v109, v50, v51\n"
    "v_mfma_f32_16x16x32_bf16 v[24:27], a[92:95], v[84:87], v[24:27]\n"
    "v_cvt_pk_bf16_f32 v110, v52, v53\n"
    "v_mfma_f32_16x16x32_bf16 v[32:35], a[172:175], v[84:87], v[32:35]\n"
    "ds_read_b128 v[84:87], v1 offset:25600\n"
    "s_waitcnt lgkmcnt(5)\n"
    "v_mfma_f32_16x16x32_bf16 v[20:23], a[12:15], v[88:91], v[20:23]\n"
    "v_cvt_pk_bf16_f32 v111, v54, v55\n"
    "v_mfma_f32_16x16x32_bf16 v[28:31], a[92:95], v[88:91], v[28:31]\n"
    "ds_write2st64_b64 v2, v[108:109], v[110:111] offset0:24 offset1:26\n"
    "v_mfma_f32_16x16x32_bf16 v[36:39], a[172:175], v[88:91], v[36:39]\n"
    "ds_read_b128 v[88:91], v1 offset:6144\n"
    "s_waitcnt lgkmcnt(6)\n"
    "v_mfma_f32_16x16x32_bf16 v[16:19], a[16:19], v[92:95], v[16:19]\n"
    "v_cvt_pk_bf16_f32 v112, v56, v57\n"
    "v_mfma_f32_16x16x32_bf16 v[24:27], a[96:99], v[92:95], v[24:27]\n"
    "v_cvt_pk_bf16_f32 v113, v58, v59\n"
    "v_mfma_f32_16x16x32_bf16 v[32:35], a[176:179], v[92:95], v[32:35]\n"
    "ds_read_b128 v[92:95], v1 offset:26624\n"
    "s_waitcnt lgkmcnt(6)\n"
    "v_mfma_f32_16x16x32_bf16 v[20:23], a[16:19], v[96:99], v[20:23]\n"
    "v_cvt_pk_bf16_f32 v114, v60, v61\n"
    "v_mfma_f32_16x16x32_bf16 v[28:31], a[96:99], v[96:99], v[28:31]\n"
    "v_cvt_pk_bf16_f32 v115, v62, v63\n"
    "v_mfma_f32_16x16x32_bf16 v[36:39], a[176:179], v[96:99], v[36:39]\n"
    "ds_read_b128 v[96:99], v1 offset:7168\n"
    "s_waitcnt lgkmcnt(5)\n"
    "v_mfma_f32_16x16x32_bf16 v[16:19], a[20:23], v[80:83], v[16:19]\n"
    "ds_write2st64_b64 v7, v[112:113], v[114:115] offset0:24 offset1:26\n"
    "v_mfma_f32_16x16x32_bf16 v[24:27], a[100:103], v[80:83], v[24:27]\n"
    "v_mfma_f32_16x16x32_bf16 v[32:35], a[180:183], v[80:83], v[32:35]\n"
    "ds_read_b128 v[80:83], v1 offset:27648\n"
    "s_waitcnt lgkmcnt(6)\n"
    "v_mfma_f32_16x16x32_bf16 v[20:23], a[20:23], v[84:87], v[20:23]\n"
    "v_mfma_f32_16x16x32_bf16 v[28:31], a[100:103], v[84:87], v[28:31]\n"
    "v_mfma_f32_16x16x32_bf16 v[36:39], a[180:183], v[84:87], v[36:39]\n"
    "ds_read_b128 v[84:87], v1 offset:8192\n"
    "s_waitcnt lgkmcnt(5)\n"
    "v_mfma_f32_16x16x32_bf16 v[16:19], a[24:27], v[88:91], v[16:19]\n"
    "v_mfma_f32_16x16x32_bf16 v[24:27], a[104:107], v[88:91], v[24:27]\n"
    "v_mfma_f32_16x16x32_bf16 v[32:35], a[184:187], v[88:91], v[32:35]\n"
    "ds_read_b128 v[88:91], v1 offset:28672\n"
    "s_waitcnt lgkmcnt(5)\n"
    "v_mfma_f32_16x16x32_bf16 v[20:23], a[24:27], v[92:95], v[20:23]\n"
    "v_mfma_f32_16x16x32_bf16 v[28:31], a[104:107], v[92:95], v[28:31]\n"
    "v_mfma_f32_16x16x32_bf16 v[36:39], a[184:187], v[92:95], v[36:39]\n"
    "ds_read_b128 v[92:95], v1 offset:9216\n"
    "s_waitcnt lgkmcnt(5)\n"
    "v_mfma_f32_16x16x32_bf16 v[16:19], a[28:31], v[96:99], v[16:19]\n"
    "v_mfma_f32_16x16x32_bf16 v[24:27], a[108:111], v[96:99], v[24:27]\n"
    "v_mfma_f32_16x16x32_bf16 v[32:35], a[188:191], v[96:99], v[32:35]\n"
    "ds_read_b128 v[96:99], v1 offset:29696\n"
    "s_waitcnt lgkmcnt(4)\n"
    "v_mfma_f32_16x16x32_bf16 v[20:23], a[28:31], v[80:83], v[20:23]\n"
    "v_mfma_f32_16x16x32_bf16 v[28:31], a[108:111], v[80:83], v[28:31]\n"
    "v_mfma_f32_16x16x32_bf16 v[36:39], a[188:191], v[80:83], v[36:39]\n"
    "ds_read_b128 v[80:83], v1 offset:10240\n"
    "s_waitcnt lgkmcnt(4)\n"
    "v_mfma_f32_16x16x32_bf16 v[16:19], a[32:35], v[84:87], v[16:19]\n"
    "v_mfma_f32_16x16x32_bf16 v[24:27], a[112:115], v[84:87], v[24:27]\n"
    "v_mfma_f32_16x16x32_bf16 v[32:35], a[192:195], v[84:87], v[32:35]\n"
    "ds_read_b128 v[84:87], v1 offset:30720\n"
    "s_waitcnt lgkmcnt(4)\n"
    "v_mfma_f32_16x16x32_bf16 v[20:23], a[32:35], v[88:91], v[20:23]\n"
    "v_mfma_f32_16x16x32_bf16 v[28:31], a[112:115], v[88:91], v[28:31]\n"
    "v_mfma_f32_16x16x32_bf16 v[36:39], a[192:195], v[88:91], v[36:39]\n"
    "ds_read_b128 v[88:91], v1 offset:11264\n"
    "s_waitcnt lgkmcnt(4)\n"
    "v_mfma_f32_16x16x32_bf16 v[16:19], a[36:39], v[92:95], v[16:19]\n"
    "v_mfma_f32_16x16x32_bf16 v[24:27], a[116:119], v[92:95], v[24:27]\n"
    "v_mfma_f32_16x16x32_bf16 v[32:35], a[196:199], v[92:95], v[32:35]\n"
    "ds_read_b128 v[92:95], v1 offset:31744\n"
    "s_waitcnt lgkmcnt(4)\n"
    "v_mfma_f32_16x16x32_bf16 v[20:23], a[36:39], v[96:99], v[20:23]\n"
    "v_mfma_f32_16x16x32_bf16 v[28:31], a[116:119], v[96:99], v[28:31]\n"
    "v_mfma_f32_16x16x32_bf16 v[36:39], a[196:199], v[96:99], v[36:39]\n"
    "ds_read_b128 v[96:99], v1 offset:12288\n"
    "s_waitcnt lgkmcnt(4)\n"
    "v_mfma_f32_16x16x32_bf16 v[16:19], a[40:43], v[80:83], v[16:19]\n"
    "v_mfma_f32_16x16x32_bf16 v[24:27], a[120:123], v[80:83], v[24:27]\n"
    "v_mfma_f32_16x16x32_bf16 v[32:35], a[200:203], v[80:83], v[32:35]\n"
    "ds_read_b128 v[80:83], v1 offset:32768\n"
    "s_waitcnt lgkmcnt(4)\n"
    "v_mfma_f32_16x16x32_bf16 v[20:23], a[40:43], v[84:87], v[20:23]\n"
    "v_mfma_f32_16x16x32_bf16 v[28:31], a[120:123], v[84:87], v[28:31]\n"
    "v_mfma_f32_16x16x32_bf16 v[36:39], a[200:203], v[84:87], v[36:39]\n"
    "ds_read_b128 v[84:87], v1 offset:13312\n"
    "s_waitcnt lgkmcnt(4)\n"
    "v_mfma_f32_16x16x32_bf16 v[16:19], a[44:47], v[88:91], v[16:19]\n"
    "v_mfma_f32_16x16x32_bf16 v[24:27], a[124:127], v[88:91], v[24:27]\n"
    "v_mfma_f32_16x16x32_bf16 v[32:35], a[204:207], v[88:91], v[32:35]\n"
    "ds_read_b128 v[88:91], v1 offset:33792\n"
    "s_waitcnt lgkmcnt(4)\n"
    "v_mfma_f32_16x16x32_bf16 v[20:23], a[44:47], v[92:95], v[20:23]\n"
    "v_mfma_f32_16x16x32_bf16 v[28:31], a[124:127], v[92:95], v[28:31]\n"
    "v_mfma_f32_16x16x32_bf16 v[36:39], a[204:207], v[92:95], v[36:39]\n"
    "ds_read_b128 v[92:95], v1 offset:14336\n"
    "s_waitcnt lgkmcnt(4)\n"
    "v_mfma_f32_16x16x32_bf16 v[16:19], a[48:51], v[96:99], v[16:19]\n"
    "v_mfma_f32_16x16x32_bf16 v[24:27], a[128:131], v[96:99], v[24:27]\n"
    "v_mfma_f32_16x16x32_bf16 v[32:35], a[208:211], v[96:99], v[32:35]\n"
    "ds_read_b128 v[96:99], v1 offset:34816\n"
    "s_waitcnt lgkmcnt(4)\n"
    "v_mfma_f32_16x16x32_bf16 v[20:23], a[48:51], v[80:83], v[20:23]\n"
    "v_mfma_f32_16x16x32_bf16 v[28:31], a[128:131], v[80:83], v[28:31]\n"
    "v_mfma_f32_16x16x32_bf16 v[36:39], a[208:211], v[80:83], v[36:39]\n"
    "ds_read_b128 v[80:83], v1 offset:15360\n"
    "s_waitcnt lgkmcnt(4)\n"
    "v_mfma_f32_16x16x32_bf16 v[16:19], a[52:55], v[84:87], v[16:19]\n"
    "v_mfma_f32_16x16x32_bf16 v[24:27], a[132:135], v[84:87], v[24:27]\n"
    "v_mfma_f32_16x16x32_bf16 v[32:35], a[212:215], v[84:87], v[32:35]\n"
    "ds_read_b128 v[84:87], v1 offset:35840\n"
    "s_waitcnt lgkmcnt(4)\n"
    "v_mfma_f32_16x16x32_bf16 v[20:23], a[52:55], v[88:91], v[20:23]\n"
    "v_mfma_f32_16x16x32_bf16 v[28:31], a[132:135], v[88:91], v[28:31]\n"
    "v_mfma_f32_16x16x32_bf16 v[36:39], a[212:215], v[88:91], v[36:39]\n"
    "ds_read_b128 v[88:91], v1 offset:16384\n"
    "s_waitcnt lgkmcnt(4)\n"
    "v_mfma_f32_16x16x32_bf16 v[16:19], a[56:59], v[92:95], v[16:19]\n"
    "v_mfma_f32_16x16x32_bf16 v[24:27], a[136:139], v[92:95], v[24:27]\n"
    "v_mfma_f32_16x16x32_bf16 v[32:35], a[216:219], v[92:95], v[32:35]\n"
    "ds_read_b128 v[92:95], v1 offset:36864\n"
    "s_waitcnt lgkmcnt(4)\n"
    "v_mfma_f32_16x16x32_bf16 v[20:23], a[56:59], v[96:99], v[20:23]\n"
    "v_mfma_f32_16x16x32_bf16 v[28:31], a[136:139], v[96:99], v[28:31]\n"
    "v_mfma_f32_16x16x32_bf16 v[36:39], a[216:219], v[96:99], v[36:39]\n"
    "s_waitcnt vmcnt(0) lgkmcnt(4)\n"
    "s_barrier\n"
    "s_sub_u32 s54, s47, 1\n"
    "s_max_i32 s54, s54, 0\n"
    "v_mov_b32 v4, s54\n"
    "buffer_store_dword v4, v3, s[40:43], s58 offen sc1\n"
    "ds_read_b128 v[96:99], v1 offset:17408\n"
    "s_waitcnt lgkmcnt(4)\n"
    "v_mfma_f32_16x16x32_bf16 v[16:19], a[60:63], v[80:83], v[16:19]\n"
    "ds_read_b128 v[188:191], v8 offset:12288\n"
    "s_add_u32 m0, s61, 0\n"
    "s_add_u32 s51, s50, 0\n"
    "buffer_load_dwordx4 v0, s[40:43], s51 offen offset:0 lds\n"
    "v_mfma_f32_16x16x32_bf16 v[24:27], a[140:143], v[80:83], v[24:27]\n"
    "ds_read_b128 v[192:195], v8 offset:14336\n"
    "v_mfma_f32_16x16x32_bf16 v[32:35], a[220:223], v[80:83], v[32:35]\n"
    "ds_read_b128 v[80:83], v1 offset:37888\n"
    "s_waitcnt lgkmcnt(6)\n"
    "v_mfma_f32_16x16x32_bf16 v[20:23], a[60:63], v[84:87], v[20:23]\n"
    "ds_read_b128 v[196:199], v8 offset:16384\n"
    "buffer_load_dwordx4 v0, s[40:43], s51 offen offset:1024 lds\n"
    "v_mfma_f32_16x16x32_bf16 v[28:31], a[140:143], v[84:87], v[28:31]\n"
    "ds_read_b128 v[200:203], v8 offset:18432\n"
    "v_mfma_f32_16x16x32_bf16 v[36:39], a[220:223], v[84:87], v[36:39]\n"
    "ds_read_b128 v[84:87], v1 offset:18432\n"
    "s_waitcnt lgkmcnt(8)\n"
    "v_mfma_f32_16x16x32_bf16 v[16:19], a[64:67], v[88:91], v[16:19]\n"
    "ds_read_b128 v[204:207], v8 offset:20480\n"
    "buffer_load_dwordx4 v0, s[40:43], s51 offen offset:2048 lds\n"
    "v_mfma_f32_16x16x32_bf16 v[24:27], a[144:147], v[88:91], v[24:27]\n"
    "ds_read_b128 v[208:211], v8 offset:22528\n"
    "v_mfma_f32_16x16x32_bf16 v[32:35], a[224:227], v[88:91], v[32:35]\n"
    "ds_read_b128 v[88:91], v1 offset:38912\n"
    "s_waitcnt lgkmcnt(10)\n"
    "v_mfma_f32_16x16x32_bf16 v[20:23], a[64:67], v[92:95], v[20:23]\n"
    "buffer_load_dwordx4 v0, s[40:43], s51 offen offset:3072 lds\n"
    "v_mfma_f32_16x16x32_bf16 v[28:31], a[144:147], v[92:95], v[28:31]\n"
    "v_mfma_f32_16x16x32_bf16 v[36:39], a[224:227], v[92:95], v[36:39]\n"
    "ds_read_b128 v[92:95], v1 offset:19456\n"
    "s_waitcnt lgkmcnt(10)\n"
    "v_mfma_f32_16x16x32_bf16 v[16:19], a[68:71], v[96:99], v[16:19]\n"
    "s_add_u32 m0, s61, 4096\n"
    "s_add_u32 s51, s50, 4096\n"
    "buffer_load_dwordx4 v0, s[40:43], s51 offen offset:0 lds\n"
    "v_mfma_f32_16x16x32_bf16 v[24:27], a[148:151], v[96:99], v[24:27]\n"
    "v_mfma_f32_16x16x32_bf16 v[32:35], a[228:231], v[96:99], v[32:35]\n"
    "ds_read_b128 v[96:99], v1 offset:39936\n"
    "s_waitcnt lgkmcnt(8)\n"
    "v_mfma_f32_16x16x32_bf16 v[20:23], a[68:71], v[80:83], v[20:23]\n"
    "buffer_load_dwordx4 v0, s[40:43], s51 offen offset:1024 lds\n"
    "v_mfma_f32_16x16x32_bf16 v[28:31], a[148:151], v[80:83], v[28:31]\n"
    "v_mfma_f32_16x16x32_bf16 v[36:39], a[228:231], v[80:83], v[36:39]\n"
    "s_add_u32 s64, s64, 40960\n"
    "s_cmp_ge_u32 s64, 122880\n"
    "s_cselect_b32 s64, 0, s64\n"
    "v_add_u32 v1, s64, v0\n"
    "ds_read_b128 v[80:83], v1 offset:0\n"
    "s_waitcnt lgkmcnt(6)\n"
    "v_mfma_f32_16x16x32_bf16 v[16:19], a[72:75], v[84:87], v[16:19]\n"
    "buffer_load_dwordx4 v0, s[40:43], s51 offen offset:2048 lds\n"
    "v_mfma_f32_16x16x32_bf16 v[24:27], a[152:155], v[84:87], v[24:27]\n"
    "v_mfma_f32_16x16x32_bf16 v[32:35], a[232:235], v[84:87], v[32:35]\n"
    "ds_read_b128 v[84:87], v1 offset:20480\n"
    "s_waitcnt lgkmcnt(4)\n"
    "v_mfma_f32_16x16x32_bf16 v[20:23], a[72:75], v[88:91], v[20:23]\n"
    "buffer_load_dwordx4 v0, s[40:43], s51 offen offset:3072 lds\n"
    "v_mfma_f32_16x16x32_bf16 v[28:31], a[152:155], v[88:91], v[28:31]\n"
    "v_mfma_f32_16x16x32_bf16 v[36:39], a[232:235], v[88:91], v[36:39]\n"
    "ds_read_b128 v[88:91], v1 offset:1024\n"
    "s_waitcnt lgkmcnt(4)\n"
    "v_mfma_f32_16x16x32_bf16 v[16:19], a[76:79], v[92:95], v[16:19]\n"
    "s_add_u32 m0, s61, 8192\n"
    "s_add_u32 s51, s50, 8192\n"
    "buffer_load_dwordx4 v0, s[40:43], s51 offen offset:0 lds\n"
    "v_mfma_f32_16x16x32_bf16 v[24:27], a[156:159], v[92:95], v[24:27]\n"
    "v_mfma_f32_16x16x32_bf16 v[32:35], a[236:239], v[92:95], v[32:35]\n"
    "ds_read_b128 v[92:95], v1 offset:21504\n"
    "s_waitcnt lgkmcnt(4)\n"
    "v_mfma_f32_16x16x32_bf16 v[20:23], a[76:79], v[96:99], v[20:23]\n"
    "buffer_load_dwordx4 v0, s[40:43], s51 offen offset:1024 lds\n"
    "s_add_u32 s50, s50, 40960\n"
    "s_add_u32 s61, s61, 40960\n"
    "s_cmp_ge_u32 s61, 122880\n"
    "s_cselect_b32 s51, 122880, 0\n"
    "s_sub_u32 s61, s61, s51\n"
    "v_mfma_f32_16x16x32_bf16 v[28:31], a[156:159], v[96:99], v[28:31]\n"
    "v_mfma_f32_16x16x32_bf16 v[36:39], a[236:239], v[96:99], v[36:39]\n"
    "ds_read_b128 v[96:99], v1 offset:2048\n"
    "s_cmp_gt_u32 s45, 1\n"
    "s_nop 5\n"
    "s_cbranch_scc1 pq_nb0_%=\n"
    "s_cmp_eq_u32 s47, 0\n"
    "s_cbranch_scc1 pq_nb0_%=\n"
    "v_swap_b32 v118, v120\n"
    "v_swap_b32 v119, v121\n"
    "v_swap_b32 v126, v128\n"
    "v_swap_b32 v127, v129\n"
    "v_swap_b32 v134, v136\n"
    "v_swap_b32 v135, v137\n"
    "v_swap_b32 v142, v144\n"
    "v_swap_b32 v143, v145\n"
    "v_swap_b32 v150, v152\n"
    "v_swap_b32 v151, v153\n"
    "v_swap_b32 v158, v160\n"
    "v_swap_b32 v159, v161\n"
    "v_mfma_f32_16x16x32_bf16 v[64:67], v[116:119], v[188:191], v[212:215]\n"
    "v_mfma_f32_16x16x32_bf16 v[68:71], v[120:123], v[188:191], v[216:219]\n"
    "v_mfma_f32_16x16x32_bf16 v[72:75], v[164:167], v[188:191], v[220:223]\n"
    "v_mfma_f32_16x16x32_bf16 v[64:67], v[124:127], v[192:195], v[64:67]\n"
    "v_mfma_f32_16x16x32_bf16 v[68:71], v[128:131], v[192:195], v[68:71]\n"
    "v_mfma_f32_16x16x32_bf16 v[72:75], v[168:171], v[192:195], v[72:75]\n"
    "v_mfma_f32_16x16x32_bf16 v[64:67], v[132:135], v[196:199], v[64:67]\n"
    "v_mfma_f32_16x16x32_bf16 v[68:71], v[136:139], v[196:199], v[68:71]\n"
    "v_mfma_f32_16x16x32_bf16 v[72:75], v[172:175], v[196:199], v[72:75]\n"
    "v_mfma_f32_16x16x32_bf16 v[64:67], v[140:143], v[200:203], v[64:67]\n"
    "v_mfma_f32_16x16x32_bf16 v[68:71], v[144:147], v[200:203], v[68:71]\n"
    "v_mfma_f32_16x16x32_bf16 v[72:75], v[176:179], v[200:203], v[72:75]\n"
    "v_mfma_f32_16x16x32_bf16 v[64:67], v[148:151], v[204:207], v[64:67]\n"
    "v_mfma_f32_16x16x32_bf16 v[68:71], v[152:155], v[204:207], v[68:71]\n"
    "v_mfma_f32_16x16x32_bf16 v[72:75], v[180:183], v[204:207], v[72:75]\n"
    "v_mfma_f32_16x16x32_bf16 v[64:67], v[156:159], v[208:211], v[64:67]\n"
    "v_mfma_f32_16x16x32_bf16 v[68:71], v[160:163], v[208:211], v[68:71]\n"
    "v_mfma_f32_16x16x32_bf16 v[72:75], v[184:187], v[208:211], v[72:75]\n"
    "s_nop 5\n"
    "v_max3_f32 v76, v64, v65, v66\n"
    "v_max3_f32 v76, v76, v67, v68\n"
    "v_max3_f32 v76, v76, v69, v70\n"
    "v_max3_f32 v76, v76, v71, v72\n"
    "v_max3_f32 v76, v76, v73, v74\n"
    "v_max_f32 v76, v76, v75\n"
    "v_mov_b32 v77, v76\n"
    "s_nop 1\n"
    "v_permlane16_swap_b32 v76, v77\n"
    "v_max_f32 v76, v76, v77\n"
    "v_mov_b32 v77, v76\n"
    "s_nop 1\n"
    "v_permlane32_swap_b32 v76, v77\n"
    "v_max_f32 v76, v76, v77\n"
    "v_mul_f32 v76, 0xbdd53b95, v76\n"
    "v_fmamk_f32 v64, v64, 0x3dd53b95, v76\n"
    "v_fmamk_f32 v65, v65, 0x3dd53b95, v76\n"
    "v_fmamk_f32 v66, v66, 0x3dd53b95, v76\n"
    "v_fmamk_f32 v67, v67, 0x3dd53b95, v76\n"
    "v_fmamk_f32 v68, v68, 0x3dd53b95, v76\n"
    "v_fmamk_f32 v69, v69, 0x3dd53b95, v76\n"
    "v_fmamk_f32 v70, v70, 0x3dd53b95, v76\n"
    "v_fmamk_f32 v71, v71, 0x3dd53b95, v76\n"
    "v_fmamk_f32 v72, v72, 0x3dd53b95, v76\n"
    "v_fmamk_f32 v73, v73, 0x3dd53b95, v76\n"
    "v_fmamk_f32 v74, v74, 0x3dd53b95, v76\n"
    "v_fmamk_f32 v75, v75, 0x3dd53b95, v76\n"
    "v_exp_f32 v64, v64\n"
    "v_exp_f32 v65, v65\n"
    "v_exp_f32 v66, v66\n"
    "v_exp_f32 v67, v67\n"
    "v_exp_f32 v68, v68\n"
    "v_exp_f32 v69, v69\n"
    "v_exp_f32 v70, v70\n"
    "v_exp_f32 v71, v71\n"
    "v_exp_f32 v72, v72\n"
    "v_exp_f32 v73, v73\n"
    "v_exp_f32 v74, v74\n"
    "v_exp_f32 v75, v75\n"
    "v_add_f32 v76, v64, v65\n"
    "v_add_f32 v76, v76, v66\n"
    "v_add_f32 v76, v76, v67\n"
    "v_add_f32 v76, v76, v68\n"
    "v_add_f32 v76, v76, v69\n"
    "v_add_f32 v76, v76, v70\n"
    "v_add_f32 v76, v76, v71\n"
    "v_add_f32 v76, v76, v72\n"
    "v_add_f32 v76, v76, v73\n"
    "v_add_f32 v76, v76, v74\n"
    "v_add_f32 v76, v76, v75\n"
    "v_mov_b32 v77, v76\n"
    "s_nop 1\n"
    "v_permlane16_swap_b32 v76, v77\n"
    "v_add_f32 v76, v76, v77\n"
    "v_mov_b32 v77, v76\n"
    "s_nop 1\n"
    "v_permlane32_swap_b32 v76, v77\n"
    "v_add_f32 v76, v76, v77\n"
    "v_rcp_f32 v76, v76\n"
    "s_nop 0\n"
    "v_mul_f32 v64, v64, v76\n"
    "v_mul_f32 v65, v65, v76\n"
    "v_mul_f32 v66, v66, v76\n"
    "v_mul_f32 v67, v67, v76\n"
    "v_mul_f32 v68, v68, v76\n"
    "v_mul_f32 v69, v69, v76\n"
    "v_mul_f32 v70, v70, v76\n"
    "v_mul_f32 v71, v71, v76\n"
    "v_mul_f32 v72, v72, v76\n"
    "v_mul_f32 v73, v73, v76\n"
    "v_mul_f32 v74, v74, v76\n"
    "v_mul_f32 v75, v75, v76\n"
    "v_cvt_pk_bf16_f32 v104, v64, v65\n"
    "v_cvt_pk_bf16_f32 v105, v66, v67\n"
    "v_cvt_pk_bf16_f32 v106, v68, v69\n"
    "v_cvt_pk_bf16_f32 v107, v70, v71\n"
    "v_cvt_pk_bf16_f32 v108, v72, v73\n"
    "v_cvt_pk_bf16_f32 v109, v74, v75\n"
    "v_mov_b32 v110, v108\n"
    "v_mov_b32 v111, v109\n"
    "s_nop 0\n"
    "v_permlane16_swap_b32 v108, v110\n"
    "v_permlane16_swap_b32 v109, v111\n"
    "s_lshl_b32 s54, s53, 1\n"
    "s_add_u32 s54, s54, s45\n"
    "s_lshl_b32 s54, s54, 10\n"
    "s_add_u32 s54, s54, 223936512\n"
    "buffer_store_dwordx4 v[104:107], v0, s[40:43], s54 offen sc1\n"
    "s_sub_u32 s54, s47, 1\n"
    "s_lshl_b32 s54, s54, 1\n"
    "s_add_u32 s54, s54, s45\n"
    "s_lshl_b32 s54, s54, 3\n"
    "s_lshr_b32 s55, s44, 3\n"
    "s_lshl_b32 s55, s55, 1\n"
    "s_add_u32 s54, s54, s55\n"
    "s_and_b32 s55, s44, 1\n"
    "s_add_u32 s54, s54, s55\n"
    "s_lshl_b32 s54, s54, 10\n"
    "s_add_u32 s54, s54, 228130816\n"
    "buffer_store_dwordx4 v[108:111], v12, s[40:43], s54 offen sc1\n"
    "s_nop 1\n"
    "pq_nb0_%=:\n"
    "s_add_u32 s47, s47, 1\n"
    "s_cmp_gt_u32 s45, 1\n"
    "s_cbranch_scc1 pq_na1_%=\n"
    "s_cmp_eq_u32 s47, 0\n"
    "s_cbranch_scc1 pq_na1_%=\n"
    "s_mov_b32 s60, 0\n"
    "v_cmp_gt_u32 vcc, s47, v9\n"
    "s_nop 4\n"
    "s_cbranch_vccz pq_kma1_ok_%=\n"
    "pq_kma1_poll_%=:\n"
    "buffer_load_dword v9, v10, s[40:43], s58 offen sc1\n"
    "s_waitcnt vmcnt(0)\n"
    "v_cmp_gt_u32 vcc, s47, v9\n"
    "s_nop 4\n"
    "s_cbranch_vccz pq_kma1_ok_%=\n"
    "s_sleep 2\n"
    "s_add_u32 s60, s60, 1\n"
    "s_cmp_gt_u32 s60, 65536\n"
    "s_cbranch_scc1 pq_abort_%=\n"
    "s_and_b32 s59, s60, 63\n"
    "s_cbranch_scc1 pq_kma1_poll_%=\n"
    "v_mov_b32 v4, 1024\n"
    "buffer_load_dword v4, v4, s[40:43], s58 offen sc1\n"
    "s_waitcnt vmcnt(0)\n"
    "v_readfirstlane_b32 s59, v4\n"
    "s_nop 0\n"
    "s_cmp_lg_u32 s59, 0\n"
    "s_nop 2\n"
    "s_cbranch_scc1 pq_end_%=\n"
    "s_branch pq_kma1_poll_%=\n"
    "pq_kma1_ok_%=:\n"
    "s_sub_u32 s53, s47, 1\n"
    "s_lshl_b32 s53, s53, 5\n"
    "s_add_u32 s53, s53, s44\n"
    "s_mul_i32 s54, s53, 12288\n"
    "s_add_u32 s54, s54, 62455808\n"
    "s_add_u32 s55, s54, 4096\n"
    "s_add_u32 s56, s54, 8192\n"
    "s_mul_i32 s57, s53, 3072\n"
    "s_add_u32 s57, s57, 217645056\n"
    "buffer_load_dwordx4 v[116:119], v0, s[40:43], s54 offen sc1\n"
    "buffer_load_dwordx4 v[120:123], v0, s[40:43], s54 offen offset:1024 sc1\n"
    "buffer_load_dwordx4 v[124:127], v0, s[40:43], s54 offen offset:3072 sc1\n"
    "buffer_load_dwordx4 v[128:131], v0, s[40:43], s55 offen sc1\n"
    "buffer_load_dwordx4 v[132:135], v0, s[40:43], s55 offen offset:2048 sc1\n"
    "buffer_load_dwordx4 v[136:139], v0, s[40:43], s55 offen offset:3072 sc1\n"
    "buffer_load_dwordx4 v[140:143], v0, s[40:43], s56 offen offset:1024 sc1\n"
    "buffer_load_dwordx4 v[144:147], v0, s[40:43], s56 offen offset:2048 sc1\n"
    "buffer_load_dwordx4 v[148:151], v0, s[40:43], s54 offen offset:2048 sc1\n"
    "buffer_load_dwordx4 v[152:155], v0, s[40:43], s55 offen offset:1024 sc1\n"
    "buffer_load_dwordx4 v[156:159], v0, s[40:43], s56 offen sc1\n"
    "buffer_load_dwordx4 v[160:163], v0, s[40:43], s56 offen offset:3072 sc1\n"
    "buffer_load_dwordx4 v[164:167], v11, s[40:43], s57 offen sc1\n"
    "buffer_load_dwordx4 v[168:171], v11, s[40:43], s57 offen offset:512 sc1\n"
    "buffer_load_dwordx4 v[172:175], v11, s[40:43], s57 offen offset:1024 sc1\n"
    "buffer_load_dwordx4 v[176:179], v11, s[40:43], s57 offen offset:1536 sc1\n"
    "buffer_load_dwordx4 v[180:183], v11, s[40:43], s57 offen offset:2048 sc1\n"
    "buffer_load_dwordx4 v[184:187], v11, s[40:43], s57 offen offset:2560 sc1\n"
    "s_nop 1\n"
    "pq_na1_%=:\n"
    "s_waitcnt lgkmcnt(4)\n"
    "v_mfma_f32_16x16x32_bf16 v[40:43], a[0:3], v[80:83], 0\n"
    "v_mfma_f32_16x16x32_bf16 v[48:51], a[80:83], v[80:83], 0\n"
    "v_mfma_f32_16x16x32_bf16 v[56:59], a[160:163], v[80:83], 0\n"
    "ds_read_b128 v[80:83], v1 offset:22528\n"
    "s_waitcnt lgkmcnt(4)\n"
    "v_mfma_f32_16x16x32_bf16 v[44:47], a[0:3], v[84:87], 0\n"
    "v_mfma_f32_16x16x32_bf16 v[52:55], a[80:83], v[84:87], 0\n"
    "v_mfma_f32_16x16x32_bf16 v[60:63], a[160:163], v[84:87], 0\n"
    "ds_read_b128 v[84:87], v1 offset:3072\n"
    "s_waitcnt lgkmcnt(4)\n"
    "v_mfma_f32_16x16x32_bf16 v[40:43], a[4:7], v[88:91], v[40:43]\n"
    "v_mfma_f32_16x16x32_bf16 v[48:51], a[84:87], v[88:91], v[48:51]\n"
    "v_mfma_f32_16x16x32_bf16 v[56:59], a[164:167], v[88:91], v[56:59]\n"
    "ds_read_b128 v[88:91], v1 offset:23552\n"
    "s_waitcnt lgkmcnt(4)\n"
    "v_mfma_f32_16x16x32_bf16 v[44:47], a[4:7], v[92:95], v[44:47]\n"
    "v_cvt_pk_bf16_f32 v104, v16, v17\n"
    "v_mfma_f32_16x16x32_bf16 v[52:55], a[84:87], v[92:95], v[52:55]\n"
    "v_cvt_pk_bf16_f32 v105, v18, v19\n"
    "v_mfma_f32_16x16x32_bf16 v[60:63], a[164:167], v[92:95], v[60:63]\n"
    "ds_read_b128 v[92:95], v1 offset:4096\n"
    "s_waitcnt lgkmcnt(4)\n"
    "v_mfma_f32_16x16x32_bf16 v[40:43], a[8:11], v[96:99], v[40:43]\n"
    "v_cvt_pk_bf16_f32 v106, v20, v21\n"
    "v_mfma_f32_16x16x32_bf16 v[48:51], a[88:91], v[96:99], v[48:51]\n"
    "v_cvt_pk_bf16_f32 v107, v22, v23\n"
    "v_mfma_f32_16x16x32_bf16 v[56:59], a[168:171], v[96:99], v[56:59]\n"
    "ds_read_b128 v[96:99], v1 offset:24576\n"
    "s_waitcnt lgkmcnt(4)\n"
    "v_mfma_f32_16x16x32_bf16 v[44:47], a[8:11], v[80:83], v[44:47]\n"
    "ds_write2st64_b64 v6, v[104:105], v[106:107] offset0:0 offset1:2\n"
    "v_mfma_f32_16x16x32_bf16 v[52:55], a[88:91], v[80:83], v[52:55]\n"
    "v_cvt_pk_bf16_f32 v108, v24, v25\n"
    "v_mfma_f32_16x16x32_bf16 v[60:63], a[168:171], v[80:83], v[60:63]\n"
    "ds_read_b128 v[80:83], v1 offset:5120\n"
    "s_waitcnt lgkmcnt(5)\n"
    "v_mfma_f32_16x16x32_bf16 v[40:43], a[12:15], v[84:87], v[40:43]\n"
    "v_cvt_pk_bf16_f32 v109, v26, v27\n"
    "v_mfma_f32_16x16x32_bf16 v[48:51], a[92:95], v[84:87], v[48:51]\n"
    "v_cvt_pk_bf16_f32 v110, v28, v29\n"
    "v_mfma_f32_16x16x32_bf16 v[56:59], a[172:175], v[84:87], v[56:59]\n"
    "ds_read_b128 v[84:87], v1 offset:25600\n"
    "s_waitcnt lgkmcnt(5)\n"
    "v_mfma_f32_16x16x32_bf16 v[44:47], a[12:15], v[88:91], v[44:47]\n"
    "v_cvt_pk_bf16_f32 v111, v30, v31\n"
    "v_mfma_f32_16x16x32_bf16 v[52:55], a[92:95], v[88:91], v[52:55]\n"
    "ds_write2st64_b64 v2, v[108:109], v[110:111] offset0:0 offset1:2\n"
    "v_mfma_f32_16x16x32_bf16 v[60:63], a[172:175], v[88:91], v[60:63]\n"
    "ds_read_b128 v[88:91], v1 offset:6144\n"
    "s_waitcnt lgkmcnt(6)\n"
    "v_mfma_f32_16x16x32_bf16 v[40:43], a[16:19], v[92:95], v[40:43]\n"
    "v_cvt_pk_bf16_f32 v112, v32, v33\n"
    "v_mfma_f32_16x16x32_bf16 v[48:51], a[96:99], v[92:95], v[48:51]\n"
    "v_cvt_pk_bf16_f32 v113, v34, v35\n"
    "v_mfma_f32_16x16x32_bf16 v[56:59], a[176:179], v[92:95], v[56:59]\n"
    "ds_read_b128 v[92:95], v1 offset:26624\n"
    "s_waitcnt lgkmcnt(6)\n"
    "v_mfma_f32_16x16x32_bf16 v[44:47], a[16:19], v[96:99], v[44:47]\n"
    "v_cvt_pk_bf16_f32 v114, v36, v37\n"
    "v_mfma_f32_16x16x32_bf16 v[52:55], a[96:99], v[96:99], v[52:55]\n"
    "v_cvt_pk_bf16_f32 v115, v38, v39\n"
    "v_mfma_f32_16x16x32_bf16 v[60:63], a[176:179], v[96:99], v[60:63]\n"
    "ds_read_b128 v[96:99], v1 offset:7168\n"
    "s_waitcnt lgkmcnt(5)\n"
    "v_mfma_f32_16x16x32_bf16 v[40:43], a[20:23], v[80:83], v[40:43]\n"
    "ds_write2st64_b64 v7, v[112:113], v[114:115] offset0:0 offset1:2\n"
    "v_mfma_f32_16x16x32_bf16 v[48:51], a[100:103], v[80:83], v[48:51]\n"
    "v_mfma_f32_16x16x32_bf16 v[56:59], a[180:183], v[80:83], v[56:59]\n"
    "ds_read_b128 v[80:83], v1 offset:27648\n"
    "s_waitcnt lgkmcnt(6)\n"
    "v_mfma_f32_16x16x32_bf16 v[44:47], a[20:23], v[84:87], v[44:47]\n"
    "v_mfma_f32_16x16x32_bf16 v[52:55], a[100:103], v[84:87], v[52:55]\n"
    "v_mfma_f32_16x16x32_bf16 v[60:63], a[180:183], v[84:87], v[60:63]\n"
    "ds_read_b128 v[84:87], v1 offset:8192\n"
    "s_waitcnt lgkmcnt(5)\n"
    "v_mfma_f32_16x16x32_bf16 v[40:43], a[24:27], v[88:91], v[40:43]\n"
    "v_mfma_f32_16x16x32_bf16 v[48:51], a[104:107], v[88:91], v[48:51]\n"
    "v_mfma_f32_16x16x32_bf16 v[56:59], a[184:187], v[88:91], v[56:59]\n"
    "ds_read_b128 v[88:91], v1 offset:28672\n"
    "s_waitcnt lgkmcnt(5)\n"
    "v_mfma_f32_16x16x32_bf16 v[44:47], a[24:27], v[92:95], v[44:47]\n"
    "v_mfma_f32_16x16x32_bf16 v[52:55], a[104:107], v[92:95], v[52:55]\n"
    "v_mfma_f32_16x16x32_bf16 v[60:63], a[184:187], v[92:95], v[60:63]\n"
    "ds_read_b128 v[92:95], v1 offset:9216\n"
    "s_waitcnt lgkmcnt(5)\n"
    "v_mfma_f32_16x16x32_bf16 v[40:43], a[28:31], v[96:99], v[40:43]\n"
    "v_mfma_f32_16x16x32_bf16 v[48:51], a[108:111], v[96:99], v[48:51]\n"
    "v_mfma_f32_16x16x32_bf16 v[56:59], a[188:191], v[96:99], v[56:59]\n"
    "ds_read_b128 v[96:99], v1 offset:29696\n"
    "s_waitcnt lgkmcnt(4)\n"
    "v_mfma_f32_16x16x32_bf16 v[44:47], a[28:31], v[80:83], v[44:47]\n"
    "v_mfma_f32_16x16x32_bf16 v[52:55], a[108:111], v[80:83], v[52:55]\n"
    "v_mfma_f32_16x16x32_bf16 v[60:63], a[188:191], v[80:83], v[60:63]\n"
    "ds_read_b128 v[80:83], v1 offset:10240\n"
    "s_waitcnt lgkmcnt(4)\n"
    "v_mfma_f32_16x16x32_bf16 v[40:43], a[32:35], v[84:87], v[40:43]\n"
    "v_mfma_f32_16x16x32_bf16 v[48:51], a[112:115], v[84:87], v[48:51]\n"
    "v_mfma_f32_16x16x32_bf16 v[56:59], a[192:195], v[84:87], v[56:59]\n"
    "ds_read_b128 v[84:87], v1 offset:30720\n"
    "s_waitcnt lgkmcnt(4)\n"
    "v_mfma_f32_16x16x32_bf16 v[44:47], a[32:35], v[88:91], v[44:47]\n"
    "v_mfma_f32_16x16x32_bf16 v[52:55], a[112:115], v[88:91], v[52:55]\n"
    "v_mfma_f32_16x16x32_bf16 v[60:63], a[192:195], v[88:91], v[60:63]\n"
    "ds_read_b128 v[88:91], v1 offset:11264\n"
    "s_waitcnt lgkmcnt(4)\n"
    "v_mfma_f32_16x16x32_bf16 v[40:43], a[36:39], v[92:95], v[40:43]\n"
    "v_mfma_f32_16x16x32_bf16 v[48:51], a[116:119], v[92:95], v[48:51]\n"
    "v_mfma_f32_16x16x32_bf16 v[56:59], a[196:199], v[92:95], v[56:59]\n"
    "ds_read_b128 v[92:95], v1 offset:31744\n"
    "s_waitcnt lgkmcnt(4)\n"
    "v_mfma_f32_16x16x32_bf16 v[44:47], a[36:39], v[96:99], v[44:47]\n"
    "v_mfma_f32_16x16x32_bf16 v[52:55], a[116:119], v[96:99], v[52:55]\n"
    "v_mfma_f32_16x16x32_bf16 v[60:63], a[196:199], v[96:99], v[60:63]\n"
    "ds_read_b128 v[96:99], v1 offset:12288\n"
    "s_waitcnt lgkmcnt(4)\n"
    "v_mfma_f32_16x16x32_bf16 v[40:43], a[40:43], v[80:83], v[40:43]\n"
    "v_mfma_f32_16x16x32_bf16 v[48:51], a[120:123], v[80:83], v[48:51]\n"
    "v_mfma_f32_16x16x32_bf16 v[56:59], a[200:203], v[80:83], v[56:59]\n"
    "ds_read_b128 v[80:83], v1 offset:32768\n"
    "s_waitcnt lgkmcnt(4)\n"
    "v_mfma_f32_16x16x32_bf16 v[44:47], a[40:43], v[84:87], v[44:47]\n"
    "v_mfma_f32_16x16x32_bf16 v[52:55], a[120:123], v[84:87], v[52:55]\n"
    "v_mfma_f32_16x16x32_bf16 v[60:63], a[200:203], v[84:87], v[60:63]\n"
    "ds_read_b128 v[84:87], v1 offset:13312\n"
    "s_waitcnt lgkmcnt(4)\n"
    "v_mfma_f32_16x16x32_bf16 v[40:43], a[44:47], v[88:91], v[40:43]\n"
    "v_mfma_f32_16x16x32_bf16 v[48:51], a[124:127], v[88:91], v[48:51]\n"
    "v_mfma_f32_16x16x32_bf16 v[56:59], a[204:207], v[88:91], v[56:59]\n"
    "ds_read_b128 v[88:91], v1 offset:33792\n"
    "s_waitcnt lgkmcnt(4)\n"
    "v_mfma_f32_16x16x32_bf16 v[44:47], a[44:47], v[92:95], v[44:47]\n"
    "v_mfma_f32_16x16x32_bf16 v[52:55], a[124:127], v[92:95], v[52:55]\n"
    "v_mfma_f32_16x16x32_bf16 v[60:63], a[204:207], v[92:95], v[60:63]\n"
    "ds_read_b128 v[92:95], v1 offset:14336\n"
    "s_waitcnt lgkmcnt(4)\n"
    "v_mfma_f32_16x16x32_bf16 v[40:43], a[48:51], v[96:99], v[40:43]\n"
    "v_mfma_f32_16x16x32_bf16 v[48:51], a[128:131], v[96:99], v[48:51]\n"
    "v_mfma_f32_16x16x32_bf16 v[56:59], a[208:211], v[96:99], v[56:59]\n"
    "ds_read_b128 v[96:99], v1 offset:34816\n"
    "s_waitcnt lgkmcnt(4)\n"
    "v_mfma_f32_16x16x32_bf16 v[44:47], a[48:51], v[80:83], v[44:47]\n"
    "v_mfma_f32_16x16x32_bf16 v[52:55], a[128:131], v[80:83], v[52:55]\n"
    "v_mfma_f32_16x16x32_bf16 v[60:63], a[208:211], v[80:83], v[60:63]\n"
    "ds_read_b128 v[80:83], v1 offset:15360\n"
    "s_waitcnt lgkmcnt(4)\n"
    "v_mfma_f32_16x16x32_bf16 v[40:43], a[52:55], v[84:87], v[40:43]\n"
    "v_mfma_f32_16x16x32_bf16 v[48:51], a[132:135], v[84:87], v[48:51]\n"
    "v_mfma_f32_16x16x32_bf16 v[56:59], a[212:215], v[84:87], v[56:59]\n"
    "ds_read_b128 v[84:87], v1 offset:35840\n"
    "s_waitcnt lgkmcnt(4)\n"
    "v_mfma_f32_16x16x32_bf16 v[44:47], a[52:55], v[88:91], v[44:47]\n"
    "v_mfma_f32_16x16x32_bf16 v[52:55], a[132:135], v[88:91], v[52:55]\n"
    "v_mfma_f32_16x16x32_bf16 v[60:63], a[212:215], v[88:91], v[60:63]\n"
    "ds_read_b128 v[88:91], v1 offset:16384\n"
    "s_waitcnt lgkmcnt(4)\n"
    "v_mfma_f32_16x16x32_bf16 v[40:43], a[56:59], v[92:95], v[40:43]\n"
    "v_mfma_f32_16x16x32_bf16 v[48:51], a[136:139], v[92:95], v[48:51]\n"
    "v_mfma_f32_16x16x32_bf16 v[56:59], a[216:219], v[92:95], v[56:59]\n"
    "ds_read_b128 v[92:95], v1 offset:36864\n"
    "s_waitcnt lgkmcnt(4)\n"
    "v_mfma_f32_16x16x32_bf16 v[44:47], a[56:59], v[96:99], v[44:47]\n"
    "v_mfma_f32_16x16x32_bf16 v[52:55], a[136:139], v[96:99], v[52:55]\n"
    "v_mfma_f32_16x16x32_bf16 v[60:63], a[216:219], v[96:99], v[60:63]\n"
    "s_waitcnt vmcnt(0) lgkmcnt(4)\n"
    "s_barrier\n"
    "s_sub_u32 s54, s47, 1\n"
    "s_max_i32 s54, s54, 0\n"
    "v_mov_b32 v4, s54\n"
    "buffer_store_dword v4, v3, s[40:43], s58 offen sc1\n"
    "ds_read_b128 v[96:99], v1 offset:17408\n"
    "s_waitcnt lgkmcnt(4)\n"
    "v_mfma_f32_16x16x32_bf16 v[40:43], a[60:63], v[80:83], v[40:43]\n"
    "ds_read_b128 v[188:191], v8 offset:0\n"
    "s_add_u32 m0, s61, 0\n"
    "s_add_u32 s51, s50, 0\n"
    "buffer_load_dwordx4 v0, s[40:43], s51 offen offset:0 lds\n"
    "v_mfma_f32_16x16x32_bf16 v[48:51], a[140:143], v[80:83], v[48:51]\n"
    "ds_read_b128 v[192:195], v8 offset:2048\n"
    "v_mfma_f32_16x16x32_bf16 v[56:59], a[220:223], v[80:83], v[56:59]\n"
    "ds_read_b128 v[80:83], v1 offset:37888\n"
    "s_waitcnt lgkmcnt(6)\n"
    "v_mfma_f32_16x16x32_bf16 v[44:47], a[60:63], v[84:87], v[44:47]\n"
    "ds_read_b128 v[196:199], v8 offset:4096\n"
    "buffer_load_dwordx4 v0, s[40:43], s51 offen offset:1024 lds\n"
    "v_mfma_f32_16x16x32_bf16 v[52:55], a[140:143], v[84:87], v[52:55]\n"
    "ds_read_b128 v[200:203], v8 offset:6144\n"
    "v_mfma_f32_16x16x32_bf16 v[60:63], a[220:223], v[84:87], v[60:63]\n"
    "ds_read_b128 v[84:87], v1 offset:18432\n"
    "s_waitcnt lgkmcnt(8)\n"
    "v_mfma_f32_16x16x32_bf16 v[40:43], a[64:67], v[88:91], v[40:43]\n"
    "ds_read_b128 v[204:207], v8 offset:8192\n"
    "buffer_load_dwordx4 v0, s[40:43], s51 offen offset:2048 lds\n"
    "v_mfma_f32_16x16x32_bf16 v[48:51], a[144:147], v[88:91], v[48:51]\n"
    "ds_read_b128 v[208:211], v8 offset:10240\n"
    "v_mfma_f32_16x16x32_bf16 v[56:59], a[224:227], v[88:91], v[56:59]\n"
    "ds_read_b128 v[88:91], v1 offset:38912\n"
    "s_waitcnt lgkmcnt(10)\n"
    "v_mfma_f32_16x16x32_bf16 v[44:47], a[64:67], v[92:95], v[44:47]\n"
    "buffer_load_dwordx4 v0, s[40:43], s51 offen offset:3072 lds\n"
    "v_mfma_f32_16x16x32_bf16 v[52:55], a[144:147], v[92:95], v[52:55]\n"
    "v_mfma_f32_16x16x32_bf16 v[60:63], a[224:227], v[92:95], v[60:63]\n"
    "ds_read_b128 v[92:95], v1 offset:19456\n"
    "s_waitcnt lgkmcnt(10)\n"
    "v_mfma_f32_16x16x32_bf16 v[40:43], a[68:71], v[96:99], v[40:43]\n"
    "s_add_u32 m0, s61, 4096\n"
    "s_add_u32 s51, s50, 4096\n"
    "buffer_load_dwordx4 v0, s[40:43], s51 offen offset:0 lds\n"
    "v_mfma_f32_16x16x32_bf16 v[48:51], a[148:151], v[96:99], v[48:51]\n"
    "v_mfma_f32_16x16x32_bf16 v[56:59], a[228:231], v[96:99], v[56:59]\n"
    "ds_read_b128 v[96:99], v1 offset:39936\n"
    "s_waitcnt lgkmcnt(8)\n"
    "v_mfma_f32_16x16x32_bf16 v[44:47], a[68:71], v[80:83], v[44:47]\n"
    "buffer_load_dwordx4 v0, s[40:43], s51 offen offset:1024 lds\n"
    "v_mfma_f32_16x16x32_bf16 v[52:55], a[148:151], v[80:83], v[52:55]\n"
    "v_mfma_f32_16x16x32_bf16 v[60:63], a[228:231], v[80:83], v[60:63]\n"
    "s_add_u32 s64, s64, 40960\n"
    "s_cmp_ge_u32 s64, 122880\n"
    "s_cselect_b32 s64, 0, s64\n"
    "v_add_u32 v1, s64, v0\n"
    "ds_read_b128 v[80:83], v1 offset:0\n"
    "s_waitcnt lgkmcnt(6)\n"
    "v_mfma_f32_16x16x32_bf16 v[40:43], a[72:75], v[84:87], v[40:43]\n"
    "buffer_load_dwordx4 v0, s[40:43], s51 offen offset:2048 lds\n"
    "v_mfma_f32_16x16x32_bf16 v[48:51], a[152:155], v[84:87], v[48:51]\n"
    "v_mfma_f32_16x16x32_bf16 v[56:59], a[232:235], v[84:87], v[56:59]\n"
    "ds_read_b128 v[84:87], v1 offset:20480\n"
    "s_waitcnt lgkmcnt(4)\n"
    "v_mfma_f32_16x16x32_bf16 v[44:47], a[72:75], v[88:91], v[44:47]\n"
    "buffer_load_dwordx4 v0, s[40:43], s51 offen offset:3072 lds\n"
    "v_mfma_f32_16x16x32_bf16 v[52:55], a[152:155], v[88:91], v[52:55]\n"
    "v_mfma_f32_16x16x32_bf16 v[60:63], a[232:235], v[88:91], v[60:63]\n"
    "ds_read_b128 v[88:91], v1 offset:1024\n"
    "s_waitcnt lgkmcnt(4)\n"
    "v_mfma_f32_16x16x32_bf16 v[40:43], a[76:79], v[92:95], v[40:43]\n"
    "s_add_u32 m0, s61, 8192\n"
    "s_add_u32 s51, s50, 8192\n"
    "buffer_load_dwordx4 v0, s[40:43], s51 offen offset:0 lds\n"
    "v_mfma_f32_16x16x32_bf16 v[48:51], a[156:159], v[92:95], v[48:51]\n"
    "v_mfma_f32_16x16x32_bf16 v[56:59], a[236:239], v[92:95], v[56:59]\n"
    "ds_read_b128 v[92:95], v1 offset:21504\n"
    "s_waitcnt lgkmcnt(4)\n"
    "v_mfma_f32_16x16x32_bf16 v[44:47], a[76:79], v[96:99], v[44:47]\n"
    "buffer_load_dwordx4 v0, s[40:43], s51 offen offset:1024 lds\n"
    "s_add_u32 s50, s50, 40960\n"
    "s_add_u32 s61, s61, 40960\n"
    "s_cmp_ge_u32 s61, 122880\n"
    "s_cselect_b32 s51, 122880, 0\n"
    "s_sub_u32 s61, s61, s51\n"
    "v_mfma_f32_16x16x32_bf16 v[52:55], a[156:159], v[96:99], v[52:55]\n"
    "v_mfma_f32_16x16x32_bf16 v[60:63], a[236:239], v[96:99], v[60:63]\n"
    "ds_read_b128 v[96:99], v1 offset:2048\n"
    "s_cmp_gt_u32 s45, 1\n"
    "s_nop 5\n"
    "s_cbranch_scc1 pq_nb1_%=\n"
    "s_cmp_eq_u32 s47, 0\n"
    "s_cbranch_scc1 pq_nb1_%=\n"
    "v_swap_b32 v118, v120\n"
    "v_swap_b32 v119, v121\n"
    "v_swap_b32 v126, v128\n"
    "v_swap_b32 v127, v129\n"
    "v_swap_b32 v134, v136\n"
    "v_swap_b32 v135, v137\n"
    "v_swap_b32 v142, v144\n"
    "v_swap_b32 v143, v145\n"
    "v_swap_b32 v150, v152\n"
    "v_swap_b32 v151, v153\n"
    "v_swap_b32 v158, v160\n"
    "v_swap_b32 v159, v161\n"
    "v_mfma_f32_16x16x32_bf16 v[64:67], v[116:119], v[188:191], v[212:215]\n"
    "v_mfma_f32_16x16x32_bf16 v[68:71], v[120:123], v[188:191], v[216:219]\n"
    "v_mfma_f32_16x16x32_bf16 v[72:75], v[164:167], v[188:191], v[220:223]\n"
    "v_mfma_f32_16x16x32_bf16 v[64:67], v[124:127], v[192:195], v[64:67]\n"
    "v_mfma_f32_16x16x32_bf16 v[68:71], v[128:131], v[192:195], v[68:71]\n"
    "v_mfma_f32_16x16x32_bf16 v[72:75], v[168:171], v[192:195], v[72:75]\n"
    "v_mfma_f32_16x16x32_bf16 v[64:67], v[132:135], v[196:199], v[64:67]\n"
    "v_mfma_f32_16x16x32_bf16 v[68:71], v[136:139], v[196:199], v[68:71]\n"
    "v_mfma_f32_16x16x32_bf16 v[72:75], v[172:175], v[196:199], v[72:75]\n"
    "v_mfma_f32_16x16x32_bf16 v[64:67], v[140:143], v[200:203], v[64:67]\n"
    "v_mfma_f32_16x16x32_bf16 v[68:71], v[144:147], v[200:203], v[68:71]\n"
    "v_mfma_f32_16x16x32_bf16 v[72:75], v[176:179], v[200:203], v[72:75]\n"
    "v_mfma_f32_16x16x32_bf16 v[64:67], v[148:151], v[204:207], v[64:67]\n"
    "v_mfma_f32_16x16x32_bf16 v[68:71], v[152:155], v[204:207], v[68:71]\n"
    "v_mfma_f32_16x16x32_bf16 v[72:75], v[180:183], v[204:207], v[72:75]\n"
    "v_mfma_f32_16x16x32_bf16 v[64:67], v[156:159], v[208:211], v[64:67]\n"
    "v_mfma_f32_16x16x32_bf16 v[68:71], v[160:163], v[208:211], v[68:71]\n"
    "v_mfma_f32_16x16x32_bf16 v[72:75], v[184:187], v[208:211], v[72:75]\n"
    "s_nop 5\n"
    "v_max3_f32 v76, v64, v65, v66\n"
    "v_max3_f32 v76, v76, v67, v68\n"
    "v_max3_f32 v76, v76, v69, v70\n"
    "v_max3_f32 v76, v76, v71, v72\n"
    "v_max3_f32 v76, v76, v73, v74\n"
    "v_max_f32 v76, v76, v75\n"
    "v_mov_b32 v77, v76\n"
    "s_nop 1\n"
    "v_permlane16_swap_b32 v76, v77\n"
    "v_max_f32 v76, v76, v77\n"
    "v_mov_b32 v77, v76\n"
    "s_nop 1\n"
    "v_permlane32_swap_b32 v76, v77\n"
    "v_max_f32 v76, v76, v77\n"
    "v_mul_f32 v76, 0xbdd53b95, v76\n"
    "v_fmamk_f32 v64, v64, 0x3dd53b95, v76\n"
    "v_fmamk_f32 v65, v65, 0x3dd53b95, v76\n"
    "v_fmamk_f32 v66, v66, 0x3dd53b95, v76\n"
    "v_fmamk_f32 v67, v67, 0x3dd53b95, v76\n"
    "v_fmamk_f32 v68, v68, 0x3dd53b95, v76\n"
    "v_fmamk_f32 v69, v69, 0x3dd53b95, v76\n"
    "v_fmamk_f32 v70, v70, 0x3dd53b95, v76\n"
    "v_fmamk_f32 v71, v71, 0x3dd53b95, v76\n"
    "v_fmamk_f32 v72, v72, 0x3dd53b95, v76\n"
    "v_fmamk_f32 v73, v73, 0x3dd53b95, v76\n"
    "v_fmamk_f32 v74, v74, 0x3dd53b95, v76\n"
    "v_fmamk_f32 v75, v75, 0x3dd53b95, v76\n"
    "v_exp_f32 v64, v64\n"
    "v_exp_f32 v65, v65\n"
    "v_exp_f32 v66, v66\n"
    "v_exp_f32 v67, v67\n"
    "v_exp_f32 v68, v68\n"
    "v_exp_f32 v69, v69\n"
    "v_exp_f32 v70, v70\n"
    "v_exp_f32 v71, v71\n"
    "v_exp_f32 v72, v72\n"
    "v_exp_f32 v73, v73\n"
    "v_exp_f32 v74, v74\n"
    "v_exp_f32 v75, v75\n"
    "v_add_f32 v76, v64, v65\n"
    "v_add_f32 v76, v76, v66\n"
    "v_add_f32 v76, v76, v67\n"
    "v_add_f32 v76, v76, v68\n"
    "v_add_f32 v76, v76, v69\n"
    "v_add_f32 v76, v76, v70\n"
    "v_add_f32 v76, v76, v71\n"
    "v_add_f32 v76, v76, v72\n"
    "v_add_f32 v76, v76, v73\n"
    "v_add_f32 v76, v76, v74\n"
    "v_add_f32 v76, v76, v75\n"
    "v_mov_b32 v77, v76\n"
    "s_nop 1\n"
    "v_permlane16_swap_b32 v76, v77\n"
    "v_add_f32 v76, v76, v77\n"
    "v_mov_b32 v77, v76\n"
    "s_nop 1\n"
    "v_permlane32_swap_b32 v76, v77\n"
    "v_add_f32 v76, v76, v77\n"
    "v_rcp_f32 v76, v76\n"
    "s_nop 0\n"
    "v_mul_f32 v64, v64, v76\n"
    "v_mul_f32 v65, v65, v76\n"
    "v_mul_f32 v66, v66, v76\n"
    "v_mul_f32 v67, v67, v76\n"
    "v_mul_f32 v68, v68, v76\n"
    "v_mul_f32 v69, v69, v76\n"
    "v_mul_f32 v70, v70, v76\n"
    "v_mul_f32 v71, v71, v76\n"
    "v_mul_f32 v72, v72, v76\n"
    "v_mul_f32 v73, v73, v76\n"
    "v_mul_f32 v74, v74, v76\n"
    "v_mul_f32 v75, v75, v76\n"
    "v_cvt_pk_bf16_f32 v104, v64, v65\n"
    "v_cvt_pk_bf16_f32 v105, v66, v67\n"
    "v_cvt_pk_bf16_f32 v106, v68, v69\n"
    "v_cvt_pk_bf16_f32 v107, v70, v71\n"
    "v_cvt_pk_bf16_f32 v108, v72, v73\n"
    "v_cvt_pk_bf16_f32 v109, v74, v75\n"
    "v_mov_b32 v110, v108\n"
    "v_mov_b32 v111, v109\n"
    "s_nop 0\n"
    "v_permlane16_swap_b32 v108, v110\n"
    "v_permlane16_swap_b32 v109, v111\n"
    "s_lshl_b32 s54, s53, 1\n"
    "s_add_u32 s54, s54, s45\n"
    "s_lshl_b32 s54, s54, 10\n"
    "s_add_u32 s54, s54, 223936512\n"
    "buffer_store_dwordx4 v[104:107], v0, s[40:43], s54 offen sc1\n"
    "s_sub_u32 s54, s47, 1\n"
    "s_lshl_b32 s54, s54, 1\n"
    "s_add_u32 s54, s54, s45\n"
    "s_lshl_b32 s54, s54, 3\n"
    "s_lshr_b32 s55, s44, 3\n"
    "s_lshl_b32 s55, s55, 1\n"
    "s_add_u32 s54, s54, s55\n"
    "s_and_b32 s55, s44, 1\n"
    "s_add_u32 s54, s54, s55\n"
    "s_lshl_b32 s54, s54, 10\n"
    "s_add_u32 s54, s54, 228130816\n"
    "buffer_store_dwordx4 v[108:111], v12, s[40:43], s54 offen sc1\n"
    "s_nop 1\n"
    "pq_nb1_%=:\n"
    "s_add_u32 s47, s47, 1\n"
    "s_cmp_lt_u32 s47, 64\n"
    "s_cbranch_scc1 pq_loop_%=\n"
    "s_waitcnt lgkmcnt(0)\n"
    "v_cvt_pk_bf16_f32 v104, v40, v41\n"
    "v_cvt_pk_bf16_f32 v105, v42, v43\n"
    "v_cvt_pk_bf16_f32 v106, v44, v45\n"
    "v_cvt_pk_bf16_f32 v107, v46, v47\n"
    "ds_write2st64_b64 v6, v[104:105], v[106:107] offset0:24 offset1:26\n"
    "v_cvt_pk_bf16_f32 v108, v48, v49\n"
    "v_cvt_pk_bf16_f32 v109, v50, v51\n"
    "v_cvt_pk_bf16_f32 v110, v52, v53\n"
    "v_cvt_pk_bf16_f32 v111, v54, v55\n"
    "ds_write2st64_b64 v2, v[108:109], v[110:111] offset0:24 offset1:26\n"
    "v_cvt_pk_bf16_f32 v112, v56, v57\n"
    "v_cvt_pk_bf16_f32 v113, v58, v59\n"
    "v_cvt_pk_bf16_f32 v114, v60, v61\n"
    "v_cvt_pk_bf16_f32 v115, v62, v63\n"
    "ds_write2st64_b64 v7, v[112:113], v[114:115] offset0:24 offset1:26\n"
    "s_waitcnt lgkmcnt(0)\n"
    "s_barrier\n"
    "s_cmp_gt_u32 s45, 1\n"
    "s_cbranch_scc1 pq_nc_%=\n"
    "s_mov_b32 s60, 0\n"
    "v_cmp_gt_u32 vcc, s47, v9\n"
    "s_nop 4\n"
    "s_cbranch_vccz pq_kme_ok_%=\n"
    "pq_kme_poll_%=:\n"
    "buffer_load_dword v9, v10, s[40:43], s58 offen sc1\n"
    "s_waitcnt vmcnt(0)\n"
    "v_cmp_gt_u32 vcc, s47, v9\n"
    "s_nop 4\n"
    "s_cbranch_vccz pq_kme_ok_%=\n"
    "s_sleep 2\n"
    "s_add_u32 s60, s60, 1\n"
    "s_cmp_gt_u32 s60, 65536\n"
    "s_cbranch_scc1 pq_abort_%=\n"
    "s_and_b32 s59, s60, 63\n"
    "s_cbranch_scc1 pq_kme_poll_%=\n"
    "v_mov_b32 v4, 1024\n"
    "buffer_load_dword v4, v4, s[40:43], s58 offen sc1\n"
    "s_waitcnt vmcnt(0)\n"
    "v_readfirstlane_b32 s59, v4\n"
    "s_nop 0\n"
    "s_cmp_lg_u32 s59, 0\n"
    "s_nop 2\n"
    "s_cbranch_scc1 pq_end_%=\n"
    "s_branch pq_kme_poll_%=\n"
    "pq_kme_ok_%=:\n"
    "s_sub_u32 s53, s47, 1\n"
    "s_lshl_b32 s53, s53, 5\n"
    "s_add_u32 s53, s53, s44\n"
    "s_mul_i32 s54, s53, 12288\n"
    "s_add_u32 s54, s54, 62455808\n"
    "s_add_u32 s55, s54, 4096\n"
    "s_add_u32 s56, s54, 8192\n"
    "s_mul_i32 s57, s53, 3072\n"
    "s_add_u32 s57, s57, 217645056\n"
    "buffer_load_dwordx4 v[116:119], v0, s[40:43], s54 offen sc1\n"
    "buffer_load_dwordx4 v[120:123], v0, s[40:43], s54 offen offset:1024 sc1\n"
    "buffer_load_dwordx4 v[124:127], v0, s[40:43], s54 offen offset:3072 sc1\n"
    "buffer_load_dwordx4 v[128:131], v0, s[40:43], s55 offen sc1\n"
    "buffer_load_dwordx4 v[132:135], v0, s[40:43], s55 offen offset:2048 sc1\n"
    "buffer_load_dwordx4 v[136:139], v0, s[40:43], s55 offen offset:3072 sc1\n"
    "buffer_load_dwordx4 v[140:143], v0, s[40:43], s56 offen offset:1024 sc1\n"
    "buffer_load_dwordx4 v[144:147], v0, s[40:43], s56 offen offset:2048 sc1\n"
    "buffer_load_dwordx4 v[148:151], v0, s[40:43], s54 offen offset:2048 sc1\n"
    "buffer_load_dwordx4 v[152:155], v0, s[40:43], s55 offen offset:1024 sc1\n"
    "buffer_load_dwordx4 v[156:159], v0, s[40:43], s56 offen sc1\n"
    "buffer_load_dwordx4 v[160:163], v0, s[40:43], s56 offen offset:3072 sc1\n"
    "buffer_load_dwordx4 v[164:167], v11, s[40:43], s57 offen sc1\n"
    "buffer_load_dwordx4 v[168:171], v11, s[40:43], s57 offen offset:512 sc1\n"
    "buffer_load_dwordx4 v[172:175], v11, s[40:43], s57 offen offset:1024 sc1\n"
    "buffer_load_dwordx4 v[176:179], v11, s[40:43], s57 offen offset:1536 sc1\n"
    "buffer_load_dwordx4 v[180:183], v11, s[40:43], s57 offen offset:2048 sc1\n"
    "buffer_load_dwordx4 v[184:187], v11, s[40:43], s57 offen offset:2560 sc1\n"
    "ds_read_b128 v[188:191], v8 offset:12288\n"
    "ds_read_b128 v[192:195], v8 offset:14336\n"
    "ds_read_b128 v[196:199], v8 offset:16384\n"
    "ds_read_b128 v[200:203], v8 offset:18432\n"
    "ds_read_b128 v[204:207], v8 offset:20480\n"
    "ds_read_b128 v[208:211], v8 offset:22528\n"
    "s_waitcnt vmcnt(16)\n"
    "v_swap_b32 v118, v120\n"
    "v_swap_b32 v119, v121\n"
    "s_waitcnt vmcnt(14)\n"
    "v_swap_b32 v126, v128\n"
    "v_swap_b32 v127, v129\n"
    "s_waitcnt vmcnt(12)\n"
    "v_swap_b32 v134, v136\n"
    "v_swap_b32 v135, v137\n"
    "s_waitcnt vmcnt(10)\n"
    "v_swap_b32 v142, v144\n"
    "v_swap_b32 v143, v145\n"
    "s_waitcnt vmcnt(8)\n"
    "v_swap_b32 v150, v152\n"
    "v_swap_b32 v151, v153\n"
    "s_waitcnt vmcnt(6)\n"
    "v_swap_b32 v158, v160\n"
    "v_swap_b32 v159, v161\n"
    "s_waitcnt lgkmcnt(5)\n"
    "v_mfma_f32_16x16x32_bf16 v[64:67], v[116:119], v[188:191], v[212:215]\n"
    "v_mfma_f32_16x16x32_bf16 v[68:71], v[120:123], v[188:191], v[216:219]\n"
    "s_waitcnt vmcnt(5)\n"
    "v_mfma_f32_16x16x32_bf16 v[72:75], v[164:167], v[188:191], v[220:223]\n"
    "s_waitcnt lgkmcnt(4)\n"
    "v_mfma_f32_16x16x32_bf16 v[64:67], v[124:127], v[192:195], v[64:67]\n"
    "v_mfma_f32_16x16x32_bf16 v[68:71], v[128:131], v[192:195], v[68:71]\n"
    "s_waitcnt vmcnt(4)\n"
    "v_mfma_f32_16x16x32_bf16 v[72:75], v[168:171], v[192:195], v[72:75]\n"
    "s_waitcnt lgkmcnt(3)\n"
    "v_mfma_f32_16x16x32_bf16 v[64:67], v[132:135], v[196:199], v[64:67]\n"
    "v_mfma_f32_16x16x32_bf16 v[68:71], v[136:139], v[196:199], v[68:71]\n"
    "s_waitcnt vmcnt(3)\n"
    "v_mfma_f32_16x16x32_bf16 v[72:75], v[172:175], v[196:199], v[72:75]\n"
    "s_waitcnt lgkmcnt(2)\n"
    "v_mfma_f32_16x16x32_bf16 v[64:67], v[140:143], v[200:203], v[64:67]\n"
    "v_mfma_f32_16x16x32_bf16 v[68:71], v[144:147], v[200:203], v[68:71]\n"
    "s_waitcnt vmcnt(2)\n"
    "v_mfma_f32_16x16x32_bf16 v[72:75], v[176:179], v[200:203], v[72:75]\n"
    "s_waitcnt lgkmcnt(1)\n"
    "v_mfma_f32_16x16x32_bf16 v[64:67], v[148:151], v[204:207], v[64:67]\n"
    "v_mfma_f32_16x16x32_bf16 v[68:71], v[152:155], v[204:207], v[68:71]\n"
    "s_waitcnt vmcnt(1)\n"
    "v_mfma_f32_16x16x32_bf16 v[72:75], v[180:183], v[204:207], v[72:75]\n"
    "s_waitcnt lgkmcnt(0)\n"
    "v_mfma_f32_16x16x32_bf16 v[64:67], v[156:159], v[208:211], v[64:67]\n"
    "v_mfma_f32_16x16x32_bf16 v[68:71], v[160:163], v[208:211], v[68:71]\n"
    "s_waitcnt vmcnt(0)\n"
    "v_mfma_f32_16x16x32_bf16 v[72:75], v[184:187], v[208:211], v[72:75]\n"
    "s_nop 4\n"
    "v_max3_f32 v76, v64, v65, v66\n"
    "v_max3_f32 v76, v76, v67, v68\n"
    "v_max3_f32 v76, v76, v69, v70\n"
    "v_max3_f32 v76, v76, v71, v72\n"
    "v_max3_f32 v76, v76, v73, v74\n"
    "v_max_f32 v76, v76, v75\n"
    "v_mov_b32 v77, v76\n"
    "s_nop 1\n"
    "v_permlane16_swap_b32 v76, v77\n"
    "v_max_f32 v76, v76, v77\n"
    "v_mov_b32 v77, v76\n"
    "s_nop 1\n"
    "v_permlane32_swap_b32 v76, v77\n"
    "v_max_f32 v76, v76, v77\n"
    "v_mul_f32 v76, 0xbdd53b95, v76\n"
    "v_fmamk_f32 v64, v64, 0x3dd53b95, v76\n"
    "v_fmamk_f32 v65, v65, 0x3dd53b95, v76\n"
    "v_fmamk_f32 v66, v66, 0x3dd53b95, v76\n"
    "v_fmamk_f32 v67, v67, 0x3dd53b95, v76\n"
    "v_fmamk_f32 v68, v68, 0x3dd53b95, v76\n"
    "v_fmamk_f32 v69, v69, 0x3dd53b95, v76\n"
    "v_fmamk_f32 v70, v70, 0x3dd53b95, v76\n"
    "v_fmamk_f32 v71, v71, 0x3dd53b95, v76\n"
    "v_fmamk_f32 v72, v72, 0x3dd53b95, v76\n"
    "v_fmamk_f32 v73, v73, 0x3dd53b95, v76\n"
    "v_fmamk_f32 v74, v74, 0x3dd53b95, v76\n"
    "v_fmamk_f32 v75, v75, 0x3dd53b95, v76\n"
    "v_exp_f32 v64, v64\n"
    "v_exp_f32 v65, v65\n"
    "v_exp_f32 v66, v66\n"
    "v_exp_f32 v67, v67\n"
    "v_exp_f32 v68, v68\n"
    "v_exp_f32 v69, v69\n"
    "v_exp_f32 v70, v70\n"
    "v_exp_f32 v71, v71\n"
    "v_exp_f32 v72, v72\n"
    "v_exp_f32 v73, v73\n"
    "v_exp_f32 v74, v74\n"
    "v_exp_f32 v75, v75\n"
    "v_add_f32 v76, v64, v65\n"
    "v_add_f32 v76, v76, v66\n"
    "v_add_f32 v76, v76, v67\n"
    "v_add_f32 v76, v76, v68\n"
    "v_add_f32 v76, v76, v69\n"
    "v_add_f32 v76, v76, v70\n"
    "v_add_f32 v76, v76, v71\n"
    "v_add_f32 v76, v76, v72\n"
    "v_add_f32 v76, v76, v73\n"
    "v_add_f32 v76, v76, v74\n"
    "v_add_f32 v76, v76, v75\n"
    "v_mov_b32 v77, v76\n"
    "s_nop 1\n"
    "v_permlane16_swap_b32 v76, v77\n"
    "v_add_f32 v76, v76, v77\n"
    "v_mov_b32 v77, v76\n"
    "s_nop 1\n"
    "v_permlane32_swap_b32 v76, v77\n"
    "v_add_f32 v76, v76, v77\n"
    "v_rcp_f32 v76, v76\n"
    "s_nop 0\n"
    "v_mul_f32 v64, v64, v76\n"
    "v_mul_f32 v65, v65, v76\n"
    "v_mul_f32 v66, v66, v76\n"
    "v_mul_f32 v67, v67, v76\n"
    "v_mul_f32 v68, v68, v76\n"
    "v_mul_f32 v69, v69, v76\n"
    "v_mul_f32 v70, v70, v76\n"
    "v_mul_f32 v71, v71, v76\n"
    "v_mul_f32 v72, v72, v76\n"
    "v_mul_f32 v73, v73, v76\n"
    "v_mul_f32 v74, v74, v76\n"
    "v_mul_f32 v75, v75, v76\n"
    "v_cvt_pk_bf16_f32 v104, v64, v65\n"
    "v_cvt_pk_bf16_f32 v105, v66, v67\n"
    "v_cvt_pk_bf16_f32 v106, v68, v69\n"
    "v_cvt_pk_bf16_f32 v107, v70, v71\n"
    "v_cvt_pk_bf16_f32 v108, v72, v73\n"
    "v_cvt_pk_bf16_f32 v109, v74, v75\n"
    "v_mov_b32 v110, v108\n"
    "v_mov_b32 v111, v109\n"
    "s_nop 0\n"
    "v_permlane16_swap_b32 v108, v110\n"
    "v_permlane16_swap_b32 v109, v111\n"
    "s_lshl_b32 s54, s53, 1\n"
    "s_add_u32 s54, s54, s45\n"
    "s_lshl_b32 s54, s54, 10\n"
    "s_add_u32 s54, s54, 223936512\n"
    "buffer_store_dwordx4 v[104:107], v0, s[40:43], s54 offen sc1\n"
    "s_sub_u32 s54, s47, 1\n"
    "s_lshl_b32 s54, s54, 1\n"
    "s_add_u32 s54, s54, s45\n"
    "s_lshl_b32 s54, s54, 3\n"
    "s_lshr_b32 s55, s44, 3\n"
    "s_lshl_b32 s55, s55, 1\n"
    "s_add_u32 s54, s54, s55\n"
    "s_and_b32 s55, s44, 1\n"
    "s_add_u32 s54, s54, s55\n"
    "s_lshl_b32 s54, s54, 10\n"
    "s_add_u32 s54, s54, 228130816\n"
    "buffer_store_dwordx4 v[108:111], v12, s[40:43], s54 offen sc1\n"
    "s_nop 1\n"
    "pq_nc_%=:\n"
    "s_waitcnt vmcnt(0)\n"
    "s_waitcnt lgkmcnt(0)\n"
    "s_barrier\n"
    "v_mov_b32 v4, 64\n"
    "buffer_store_dword v4, v3, s[40:43], s58 offen sc1\n"
    "s_nop 1\n"
    "s_branch pq_end_%=\n"
    "pq_abort_%=:\n"
    "v_mov_b32 v4, 1\n"
    "v_mov_b32 v5, 1024\n"
    "buffer_store_dword v4, v5, s[40:43], s58 offen sc1\n"
    "pq_end_%=:\n"
    "s_endpgm\n"
    :: ROLE_INPUTS : ROLE_CLOBBERS);
}
DEV void role_projkv(unsigned lo, unsigned hi, int id, int w, int aux, const Tune& tn) {
    asm volatile(
    "s_mov_b32 s40, %0\n"
    "s_mov_b32 s41, %1\n"
    "s_mov_b32 s42, %2\n"
    "s_mov_b32 s43, %3\n"
    "s_mov_b32 s44, %4\n"
    "s_mov_b32 s45, %5\n"
    "s_mov_b32 s46, %6\n"
    "s_mov_b32 s66, %7\n"
    "s_mov_b32 s67, %8\n"
    "s_mov_b32 s68, %9\n"
    "s_mov_b32 s69, %10\n"
    "s_mov_b32 s70, %11\n"
    "s_mov_b32 s71, %12\n"
    "s_mov_b32 s72, %13\n"
    "s_mov_b32 s73, %14\n"
    "s_mov_b64 exec, -1\n"
    "v_mbcnt_lo_u32_b32 v15, -1, 0\n"
    "v_mbcnt_hi_u32_b32 v15, -1, v15\n"
    "v_lshlrev_b32 v0, 4, v15\n"
    "s_mov_b32 s58, 2097152\n"
    "s_mov_b32 s92, 0\n"
    "s_lshl_b32 s48, s46, 5\n"
    "s_add_u32 s48, s48, s44\n"
    "s_mul_i32 s48, s48, 12\n"
    "s_mul_i32 s49, s45, 3\n"
    "s_add_u32 s48, s48, s49\n"
    "s_mul_i32 s48, s48, 20480\n"
    "s_add_u32 s48, s48, 2162688\n"
    "s_mul_i32 s62, s45, 10240\n"
    "s_add_u32 s50, s62, 59834368\n"
    "s_mov_b32 s61, s62\n"
    "s_add_u32 m0, s61, 0\n"
    "s_add_u32 s51, s50, 0\n"
    "buffer_load_dwordx4 v0, s[40:43], s51 offen offset:0 lds\n"
    "buffer_load_dwordx4 v0, s[40:43], s51 offen offset:1024 lds\n"
    "buffer_load_dwordx4 v0, s[40:43], s51 offen offset:2048 lds\n"
    "buffer_load_dwordx4 v0, s[40:43], s51 offen offset:3072 lds\n"
    "s_add_u32 m0, s61, 4096\n"
    "s_add_u32 s51, s50, 4096\n"
    "buffer_load_dwordx4 v0, s[40:43], s51 offen offset:0 lds\n"
    "buffer_load_dwordx4 v0, s[40:43], s51 offen offset:1024 lds\n"
    "buffer_load_dwordx4 v0, s[40:43], s51 offen offset:2048 lds\n"
    "buffer_load_dwordx4 v0, s[40:43], s51 offen offset:3072 lds\n"
    "s_add_u32 m0, s61, 8192\n"
    "s_add_u32 s51, s50, 8192\n"
    "buffer_load_dwordx4 v0, s[40:43], s51 offen offset:0 lds\n"
    "buffer_load_dwordx4 v0, s[40:43], s51 offen offset:1024 lds\n"
    "s_add_u32 s50, s50, 40960\n"
    "s_add_u32 s61, s61, 40960\n"
    "s_cmp_ge_u32 s61, 122880\n"
    "s_cselect_b32 s51, 122880, 0\n"
    "s_sub_u32 s61, s61, s51\n"
    "s_add_u32 m0, s61, 0\n"
    "s_add_u32 s51, s50, 0\n"
    "buffer_load_dwordx4 v0, s[40:43], s51 offen offset:0 lds\n"
    "buffer_load_dwordx4 v0, s[40:43], s51 offen offset:1024 lds\n"
    "buffer_load_dwordx4 v0, s[40:43], s51 offen offset:2048 lds\n"
    "buffer_load_dwordx4 v0, s[40:43], s51 offen offset:3072 lds\n"
    "s_add_u32 m0, s61, 4096\n"
    "s_add_u32 s51, s50, 4096\n"
    "buffer_load_dwordx4 v0, s[40:43], s51 offen offset:0 lds\n"
    "buffer_load_dwordx4 v0, s[40:43], s51 offen offset:1024 lds\n"
    "buffer_load_dwordx4 v0, s[40:43], s51 offen offset:2048 lds\n"
    "buffer_load_dwordx4 v0, s[40:43], s51 offen offset:3072 lds\n"
    "s_add_u32 m0, s61, 8192\n"
    "s_add_u32 s51, s50, 8192\n"
    "buffer_load_dwordx4 v0, s[40:43], s51 offen offset:0 lds\n"
    "buffer_load_dwordx4 v0, s[40:43], s51 offen offset:1024 lds\n"
    "s_add_u32 s50, s50, 40960\n"
    "s_add_u32 s61, s61, 40960\n"
    "s_cmp_ge_u32 s61, 122880\n"
    "s_cselect_b32 s51, 122880, 0\n"
    "s_sub_u32 s61, s61, s51\n"
    "buffer_load_dwordx4 a[0:3], v0, s[40:43], s48 offen\n"
    "buffer_load_dwordx4 a[4:7], v0, s[40:43], s48 offen offset:1024\n"
    "buffer_load_dwordx4 a[8:11], v0, s[40:43], s48 offen offset:2048\n"
    "buffer_load_dwordx4 a[12:15], v0, s[40:43], s48 offen offset:3072\n"
    "s_add_u32 s48, s48, 4096\n"
    "buffer_load_dwordx4 a[16:19], v0, s[40:43], s48 offen\n"
    "buffer_load_dwordx4 a[20:23], v0, s[40:43], s48 offen offset:1024\n"
    "buffer_load_dwordx4 a[24:27], v0, s[40:43], s48 offen offset:2048\n"
    "buffer_load_dwordx4 a[28:31], v0, s[40:43], s48 offen offset:3072\n"
    "s_add_u32 s48, s48, 4096\n"
    "buffer_load_dwordx4 a[32:35], v0, s[40:43], s48 offen\n"
    "buffer_load_dwordx4 a[36:39], v0, s[40:43], s48 offen offset:1024\n"
    "buffer_load_dwordx4 a[40:43], v0, s[40:43], s48 offen offset:2048\n"
    "buffer_load_dwordx4 a[44:47], v0, s[40:43], s48 offen offset:3072\n"
    "s_add_u32 s48, s48, 4096\n"
    "buffer_load_dwordx4 a[48:51], v0, s[40:43], s48 offen\n"
    "buffer_load_dwordx4 a[52:55], v0, s[40:43], s48 offen offset:1024\n"
    "buffer_load_dwordx4 a[56:59], v0, s[40:43], s48 offen offset:2048\n"
    "buffer_load_dwordx4 a[60:63], v0, s[40:43], s48 offen offset:3072\n"
    "s_add_u32 s48, s48, 4096\n"
    "buffer_load_dwordx4 a[64:67], v0, s[40:43], s48 offen\n"
    "buffer_load_dwordx4 a[68:71], v0, s[40:43], s48 offen offset:1024\n"
    "buffer_load_dwordx4 a[72:75], v0, s[40:43], s48 offen offset:2048\n"
    "buffer_load_dwordx4 a[76:79], v0, s[40:43], s48 offen offset:3072\n"
    "s_add_u32 s48, s48, 4096\n"
    "buffer_load_dwordx4 a[80:83], v0, s[40:43], s48 offen\n"
    "buffer_load_dwordx4 a[84:87], v0, s[40:43], s48 offen offset:1024\n"
    "buffer_load_dwordx4 a[88:91], v0, s[40:43], s48 offen offset:2048\n"
    "buffer_load_dwordx4 a[92:95], v0, s[40:43], s48 offen offset:3072\n"
    "s_add_u32 s48, s48, 4096\n"
    "buffer_load_dwordx4 a[96:99], v0, s[40:43], s48 offen\n"
    "buffer_load_dwordx4 a[100:103], v0, s[40:43], s48 offen offset:1024\n"
    "buffer_load_dwordx4 a[104:107], v0, s[40:43], s48 offen offset:2048\n"
    "buffer_load_dwordx4 a[108:111], v0, s[40:43], s48 offen offset:3072\n"
    "s_add_u32 s48, s48, 4096\n"
    "buffer_load_dwordx4 a[112:115], v0, s[40:43], s48 offen\n"
    "buffer_load_dwordx4 a[116:119], v0, s[40:43], s48 offen offset:1024\n"
    "buffer_load_dwordx4 a[120:123], v0, s[40:43], s48 offen offset:2048\n"
    "buffer_load_dwordx4 a[124:127], v0, s[40:43], s48 offen offset:3072\n"
    "s_add_u32 s48, s48, 4096\n"
    "buffer_load_dwordx4 a[128:131], v0, s[40:43], s48 offen\n"
    "buffer_load_dwordx4 a[132:135], v0, s[40:43], s48 offen offset:1024\n"
    "buffer_load_dwordx4 a[136:139], v0, s[40:43], s48 offen offset:2048\n"
    "buffer_load_dwordx4 a[140:143], v0, s[40:43], s48 offen offset:3072\n"
    "s_add_u32 s48, s48, 4096\n"
    "buffer_load_dwordx4 a[144:147], v0, s[40:43], s48 offen\n"
    "buffer_load_dwordx4 a[148:151], v0, s[40:43], s48 offen offset:1024\n"
    "buffer_load_dwordx4 a[152:155], v0, s[40:43], s48 offen offset:2048\n"
    "buffer_load_dwordx4 a[156:159], v0, s[40:43], s48 offen offset:3072\n"
    "s_add_u32 s48, s48, 4096\n"
    "buffer_load_dwordx4 a[160:163], v0, s[40:43], s48 offen\n"
    "buffer_load_dwordx4 a[164:167], v0, s[40:43], s48 offen offset:1024\n"
    "buffer_load_dwordx4 a[168:171], v0, s[40:43], s48 offen offset:2048\n"
    "s_waitcnt vmcnt(61)\n"
    "buffer_load_dwordx4 a[172:175], v0, s[40:43], s48 offen offset:3072\n"
    "s_add_u32 s48, s48, 4096\n"
    "buffer_load_dwordx4 a[176:179], v0, s[40:43], s48 offen\n"
    "s_waitcnt vmcnt(61)\n"
    "buffer_load_dwordx4 a[180:183], v0, s[40:43], s48 offen offset:1024\n"
    "buffer_load_dwordx4 a[184:187], v0, s[40:43], s48 offen offset:2048\n"
    "s_waitcnt vmcnt(61)\n"
    "buffer_load_dwordx4 a[188:191], v0, s[40:43], s48 offen offset:3072\n"
    "s_add_u32 s48, s48, 4096\n"
    "buffer_load_dwordx4 a[192:195], v0, s[40:43], s48 offen\n"
    "s_waitcnt vmcnt(61)\n"
    "buffer_load_dwordx4 a[196:199], v0, s[40:43], s48 offen offset:1024\n"
    "buffer_load_dwordx4 a[200:203], v0, s[40:43], s48 offen offset:2048\n"
    "s_waitcnt vmcnt(61)\n"
    "buffer_load_dwordx4 a[204:207], v0, s[40:43], s48 offen offset:3072\n"
    "s_add_u32 s48, s48, 4096\n"
    "buffer_load_dwordx4 a[208:211], v0, s[40:43], s48 offen\n"
    "s_waitcnt vmcnt(61)\n"
    "buffer_load_dwordx4 a[212:215], v0, s[40:43], s48 offen offset:1024\n"
    "buffer_load_dwordx4 a[216:219], v0, s[40:43], s48 offen offset:2048\n"
    "s_waitcnt vmcnt(61)\n"
    "buffer_load_dwordx4 a[220:223], v0, s[40:43], s48 offen offset:3072\n"
    "s_add_u32 s48, s48, 4096\n"
    "buffer_load_dwordx4 a[224:227], v0, s[40:43], s48 offen\n"
    "s_waitcnt vmcnt(61)\n"
    "buffer_load_dwordx4 a[228:231], v0, s[40:43], s48 offen offset:1024\n"
    "buffer_load_dwordx4 a[232:235], v0, s[40:43], s48 offen offset:2048\n"
    "s_waitcnt vmcnt(61)\n"
    "buffer_load_dwordx4 a[236:239], v0, s[40:43], s48 offen offset:3072\n"
    "v_mov_b32 v1, v0\n"
    "s_mov_b32 s64, 0\n"
    "s_lshl_b32 s48, s44, 7\n"
    "s_mov_b32 s49, 6144\n"
    "s_mov_b32 s63, 87621632\n"
    "s_cmp_eq_u32 s46, 1\n"
    "s_cselect_b32 s49, 2048, s49\n"
    "s_cselect_b32 s63, 62455808, s63\n"
    "s_add_u32 s49, s49, s48\n"
    "s_mul_i32 s53, s44, 12288\n"
    "s_add_u32 s63, s63, s53\n"
    "s_mul_i32 s53, s45, 3072\n"
    "s_add_u32 s63, s63, s53\n"
    "v_mov_b32 v3, s49\n"
    "v_or_b32 v4, s45, v15\n"
    "v_cmp_eq_u32 vcc, 0, v4\n"
    "v_mov_b32 v4, 2147483632\n"
    "s_nop 0\n"
    "v_cndmask_b32 v3, v4, v3, vcc\n"
    "s_mov_b32 s47, 0\n"
    "s_waitcnt vmcnt(0)\n"
    "s_waitcnt lgkmcnt(0)\n"
    "s_barrier\n"
    "s_mov_b32 s65, 0x40000000\n"
    "ds_read_b128 v[80:83], v1 offset:0\n"
    "ds_read_b128 v[84:87], v1 offset:20480\n"
    "ds_read_b128 v[88:91], v1 offset:1024\n"
    "ds_read_b128 v[92:95], v1 offset:21504\n"
    "ds_read_b128 v[96:99], v1 offset:2048\n"
    "pk_loop_%=:\n"
    "s_waitcnt lgkmcnt(4)\n"
    "v_mfma_f32_16x16x32_bf16 v[16:19], a[0:3], v[80:83], 0\n"
    "v_mfma_f32_16x16x32_bf16 v[24:27], a[80:83], v[80:83], 0\n"
    "v_mfma_f32_16x16x32_bf16 v[32:35], a[160:163], v[80:83], 0\n"
    "ds_read_b128 v[80:83], v1 offset:22528\n"
    "s_waitcnt lgkmcnt(4)\n"
    "v_mfma_f32_16x16x32_bf16 v[20:23], a[0:3], v[84:87], 0\n"
    "v_mfma_f32_16x16x32_bf16 v[28:31], a[80:83], v[84:87], 0\n"
    "v_mfma_f32_16x16x32_bf16 v[36:39], a[160:163], v[84:87], 0\n"
    "ds_read_b128 v[84:87], v1 offset:3072\n"
    "s_waitcnt lgkmcnt(4)\n"
    "v_mfma_f32_16x16x32_bf16 v[16:19], a[4:7], v[88:91], v[16:19]\n"
    "v_mfma_f32_16x16x32_bf16 v[24:27], a[84:87], v[88:91], v[24:27]\n"
    "v_mfma_f32_16x16x32_bf16 v[32:35], a[164:167], v[88:91], v[32:35]\n"
    "ds_read_b128 v[88:91], v1 offset:23552\n"
    "s_waitcnt lgkmcnt(4)\n"
    "v_mfma_f32_16x16x32_bf16 v[20:23], a[4:7], v[92:95], v[20:23]\n"
    "v_cvt_pk_bf16_f32 v104, v40, v41\n"
    "v_mfma_f32_16x16x32_bf16 v[28:31], a[84:87], v[92:95], v[28:31]\n"
    "v_cvt_pk_bf16_f32 v105, v42, v43\n"
    "v_mfma_f32_16x16x32_bf16 v[36:39], a[164:167], v[92:95], v[36:39]\n"
    "ds_read_b128 v[92:95], v1 offset:4096\n"
    "s_waitcnt lgkmcnt(4)\n"
    "v_mfma_f32_16x16x32_bf16 v[16:19], a[8:11], v[96:99], v[16:19]\n"
    "v_cvt_pk_bf16_f32 v106, v44, v45\n"
    "v_mfma_f32_16x16x32_bf16 v[24:27], a[88:91], v[96:99], v[24:27]\n"
    "v_cvt_pk_bf16_f32 v107, v46, v47\n"
    "v_mfma_f32_16x16x32_bf16 v[32:35], a[168:171], v[96:99], v[32:35]\n"
    "ds_read_b128 v[96:99], v1 offset:24576\n"
    "s_waitcnt lgkmcnt(4)\n"
    "v_mfma_f32_16x16x32_bf16 v[20:23], a[8:11], v[80:83], v[20:23]\n"
    "buffer_store_dwordx4 v[104:107], v0, s[40:43], s65 offen offset:0 sc1\n"
    "v_mfma_f32_16x16x32_bf16 v[28:31], a[88:91], v[80:83], v[28:31]\n"
    "v_cvt_pk_bf16_f32 v108, v48, v49\n"
    "v_mfma_f32_16x16x32_bf16 v[36:39], a[168:171], v[80:83], v[36:39]\n"
    "ds_read_b128 v[80:83], v1 offset:5120\n"
    "s_waitcnt lgkmcnt(4)\n"
    "v_mfma_f32_16x16x32_bf16 v[16:19], a[12:15], v[84:87], v[16:19]\n"
    "v_cvt_pk_bf16_f32 v109, v50, v51\n"
    "v_mfma_f32_16x16x32_bf16 v[24:27], a[92:95], v[84:87], v[24:27]\n"
    "v_cvt_pk_bf16_f32 v110, v52, v53\n"
    "v_mfma_f32_16x16x32_bf16 v[32:35], a[172:175], v[84:87], v[32:35]\n"
    "ds_read_b128 v[84:87], v1 offset:25600\n"
    "s_waitcnt lgkmcnt(4)\n"
    "v_mfma_f32_16x16x32_bf16 v[20:23], a[12:15], v[88:91], v[20:23]\n"
    "v_cvt_pk_bf16_f32 v111, v54, v55\n"
    "v_mfma_f32_16x16x32_bf16 v[28:31], a[92:95], v[88:91], v[28:31]\n"
    "buffer_store_dwordx4 v[108:111], v0, s[40:43], s65 offen offset:1024 sc1\n"
    "v_mfma_f32_16x16x32_bf16 v[36:39], a[172:175], v[88:91], v[36:39]\n"
    "ds_read_b128 v[88:91], v1 offset:6144\n"
    "s_waitcnt lgkmcnt(4)\n"
    "v_mfma_f32_16x16x32_bf16 v[16:19], a[16:19], v[92:95], v[16:19]\n"
    "v_cvt_pk_bf16_f32 v112, v56, v57\n"
    "v_mfma_f32_16x16x32_bf16 v[24:27], a[96:99], v[92:95], v[24:27]\n"
    "v_cvt_pk_bf16_f32 v113, v58, v59\n"
    "v_mfma_f32_16x16x32_bf16 v[32:35], a[176:179], v[92:95], v[32:35]\n"
    "ds_read_b128 v[92:95], v1 offset:26624\n"
    "s_waitcnt lgkmcnt(4)\n"
    "v_mfma_f32_16x16x32_bf16 v[20:23], a[16:19], v[96:99], v[20:23]\n"
    "v_cvt_pk_bf16_f32 v114, v60, v61\n"
    "v_mfma_f32_16x16x32_bf16 v[28:31], a[96:99], v[96:99], v[28:31]\n"
    "v_cvt_pk_bf16_f32 v115, v62, v63\n"
    "v_mfma_f32_16x16x32_bf16 v[36:39], a[176:179], v[96:99], v[36:39]\n"
    "ds_read_b128 v[96:99], v1 offset:7168\n"
    "s_waitcnt lgkmcnt(4)\n"
    "v_mfma_f32_16x16x32_bf16 v[16:19], a[20:23], v[80:83], v[16:19]\n"
    "buffer_store_dwordx4 v[112:115], v0, s[40:43], s65 offen offset:2048 sc1\n"
    "v_mfma_f32_16x16x32_bf16 v[24:27], a[100:103], v[80:83], v[24:27]\n"
    "v_mfma_f32_16x16x32_bf16 v[32:35], a[180:183], v[80:83], v[32:35]\n"
    "ds_read_b128 v[80:83], v1 offset:27648\n"
    "s_waitcnt lgkmcnt(4)\n"
    "v_mfma_f32_16x16x32_bf16 v[20:23], a[20:23], v[84:87], v[20:23]\n"
    "v_mfma_f32_16x16x32_bf16 v[28:31], a[100:103], v[84:87], v[28:31]\n"
    "v_mfma_f32_16x16x32_bf16 v[36:39], a[180:183], v[84:87], v[36:39]\n"
    "ds_read_b128 v[84:87], v1 offset:8192\n"
    "s_waitcnt lgkmcnt(4)\n"
    "v_mfma_f32_16x16x32_bf16 v[16:19], a[24:27], v[88:91], v[16:19]\n"
    "v_mfma_f32_16x16x32_bf16 v[24:27], a[104:107], v[88:91], v[24:27]\n"
    "v_mfma_f32_16x16x32_bf16 v[32:35], a[184:187], v[88:91], v[32:35]\n"
    "ds_read_b128 v[88:91], v1 offset:28672\n"
    "s_waitcnt lgkmcnt(4)\n"
    "v_mfma_f32_16x16x32_bf16 v[20:23], a[24:27], v[92:95], v[20:23]\n"
    "v_mfma_f32_16x16x32_bf16 v[28:31], a[104:107], v[92:95], v[28:31]\n"
    "v_mfma_f32_16x16x32_bf16 v[36:39], a[184:187], v[92:95], v[36:39]\n"
    "ds_read_b128 v[92:95], v1 offset:9216\n"
    "s_waitcnt lgkmcnt(4)\n"
    "v_mfma_f32_16x16x32_bf16 v[16:19], a[28:31], v[96:99], v[16:19]\n"
    "v_mfma_f32_16x16x32_bf16 v[24:27], a[108:111], v[96:99], v[24:27]\n"
    "v_mfma_f32_16x16x32_bf16 v[32:35], a[188:191], v[96:99], v[32:35]\n"
    "ds_read_b128 v[96:99], v1 offset:29696\n"
    "s_waitcnt lgkmcnt(4)\n"
    "v_mfma_f32_16x16x32_bf16 v[20:23], a[28:31], v[80:83], v[20:23]\n"
    "v_mfma_f32_16x16x32_bf16 v[28:31], a[108:111], v[80:83], v[28:31]\n"
    "v_mfma_f32_16x16x32_bf16 v[36:39], a[188:191], v[80:83], v[36:39]\n"
    "ds_read_b128 v[80:83], v1 offset:10240\n"
    "s_waitcnt lgkmcnt(4)\n"
    "v_mfma_f32_16x16x32_bf16 v[16:19], a[32:35], v[84:87], v[16:19]\n"
    "v_mfma_f32_16x16x32_bf16 v[24:27], a[112:115], v[84:87], v[24:27]\n"
    "v_mfma_f32_16x16x32_bf16 v[32:35], a[192:195], v[84:87], v[32:35]\n"
    "ds_read_b128 v[84:87], v1 offset:30720\n"
    "s_waitcnt lgkmcnt(4)\n"
    "v_mfma_f32_16x16x32_bf16 v[20:23], a[32:35], v[88:91], v[20:23]\n"
    "v_mfma_f32_16x16x32_bf16 v[28:31], a[112:115], v[88:91], v[28:31]\n"
    "v_mfma_f32_16x16x32_bf16 v[36:39], a[192:195], v[88:91], v[36:39]\n"
    "ds_read_b128 v[88:91], v1 offset:11264\n"
    "s_waitcnt lgkmcnt(4)\n"
    "v_mfma_f32_16x16x32_bf16 v[16:19], a[36:39], v[92:95], v[16:19]\n"
    "v_mfma_f32_16x16x32_bf16 v[24:27], a[116:119], v[92:95], v[24:27]\n"
    "v_mfma_f32_16x16x32_bf16 v[32:35], a[196:199], v[92:95], v[32:35]\n"
    "ds_read_b128 v[92:95], v1 offset:31744\n"
    "s_waitcnt lgkmcnt(4)\n"
    "v_mfma_f32_16x16x32_bf16 v[20:23], a[36:39], v[96:99], v[20:23]\n"
    "v_mfma_f32_16x16x32_bf16 v[28:31], a[116:119], v[96:99], v[28:31]\n"
    "v_mfma_f32_16x16x32_bf16 v[36:39], a[196:199], v[96:99], v[36:39]\n"
    "ds_read_b128 v[96:99], v1 offset:12288\n"
    "s_waitcnt lgkmcnt(4)\n"
    "v_mfma_f32_16x16x32_bf16 v[16:19], a[40:43], v[80:83], v[16:19]\n"
    "v_mfma_f32_16x16x32_bf16 v[24:27], a[120:123], v[80:83], v[24:27]\n"
    "v_mfma_f32_16x16x32_bf16 v[32:35], a[200:203], v[80:83], v[32:35]\n"
    "ds_read_b128 v[80:83], v1 offset:32768\n"
    "s_waitcnt lgkmcnt(4)\n"
    "v_mfma_f32_16x16x32_bf16 v[20:23], a[40:43], v[84:87], v[20:23]\n"
    "v_mfma_f32_16x16x32_bf16 v[28:31], a[120:123], v[84:87], v[28:31]\n"
    "v_mfma_f32_16x16x32_bf16 v[36:39], a[200:203], v[84:87], v[36:39]\n"
    "ds_read_b128 v[84:87], v1 offset:13312\n"
    "s_waitcnt lgkmcnt(4)\n"
    "v_mfma_f32_16x16x32_bf16 v[16:19], a[44:47], v[88:91], v[16:19]\n"
    "v_mfma_f32_16x16x32_bf16 v[24:27], a[124:127], v[88:91], v[24:27]\n"
    "v_mfma_f32_16x16x32_bf16 v[32:35], a[204:207], v[88:91], v[32:35]\n"
    "ds_read_b128 v[88:91], v1 offset:33792\n"
    "s_waitcnt lgkmcnt(4)\n"
    "v_mfma_f32_16x16x32_bf16 v[20:23], a[44:47], v[92:95], v[20:23]\n"
    "v_mfma_f32_16x16x32_bf16 v[28:31], a[124:127], v[92:95], v[28:31]\n"
    "v_mfma_f32_16x16x32_bf16 v[36:39], a[204:207], v[92:95], v[36:39]\n"
    "ds_read_b128 v[92:95], v1 offset:14336\n"
    "s_waitcnt lgkmcnt(4)\n"
    "v_mfma_f32_16x16x32_bf16 v[16:19], a[48:51], v[96:99], v[16:19]\n"
    "v_mfma_f32_16x16x32_bf16 v[24:27], a[128:131], v[96:99], v[24:27]\n"
    "v_mfma_f32_16x16x32_bf16 v[32:35], a[208:211], v[96:99], v[32:35]\n"
    "ds_read_b128 v[96:99], v1 offset:34816\n"
    "s_waitcnt lgkmcnt(4)\n"
    "v_mfma_f32_16x16x32_bf16 v[20:23], a[48:51], v[80:83], v[20:23]\n"
    "v_mfma_f32_16x16x32_bf16 v[28:31], a[128:131], v[80:83], v[28:31]\n"
    "v_mfma_f32_16x16x32_bf16 v[36:39], a[208:211], v[80:83], v[36:39]\n"
    "ds_read_b128 v[80:83], v1 offset:15360\n"
    "s_waitcnt lgkmcnt(4)\n"
    "v_mfma_f32_16x16x32_bf16 v[16:19], a[52:55], v[84:87], v[16:19]\n"
    "v_mfma_f32_16x16x32_bf16 v[24:27], a[132:135], v[84:87], v[24:27]\n"
    "v_mfma_f32_16x16x32_bf16 v[32:35], a[212:215], v[84:87], v[32:35]\n"
    "ds_read_b128 v[84:87], v1 offset:35840\n"
    "s_waitcnt lgkmcnt(4)\n"
    "v_mfma_f32_16x16x32_bf16 v[20:23], a[52:55], v[88:91], v[20:23]\n"
    "v_mfma_f32_16x16x32_bf16 v[28:31], a[132:135], v[88:91], v[28:31]\n"
    "v_mfma_f32_16x16x32_bf16 v[36:39], a[212:215], v[88:91], v[36:39]\n"
    "ds_read_b128 v[88:91], v1 offset:16384\n"
    "s_waitcnt lgkmcnt(4)\n"
    "v_mfma_f32_16x16x32_bf16 v[16:19], a[56:59], v[92:95], v[16:19]\n"
    "v_mfma_f32_16x16x32_bf16 v[24:27], a[136:139], v[92:95], v[24:27]\n"
    "v_mfma_f32_16x16x32_bf16 v[32:35], a[216:219], v[92:95], v[32:35]\n"
    "ds_read_b128 v[92:95], v1 offset:36864\n"
    "s_waitcnt lgkmcnt(4)\n"
    "v_mfma_f32_16x16x32_bf16 v[20:23], a[56:59], v[96:99], v[20:23]\n"
    "v_mfma_f32_16x16x32_bf16 v[28:31], a[136:139], v[96:99], v[28:31]\n"
    "v_mfma_f32_16x16x32_bf16 v[36:39], a[216:219], v[96:99], v[36:39]\n"
    "s_waitcnt vmcnt(0)\n"
    "s_barrier\n"
    "v_mov_b32 v4, s47\n"
    "buffer_store_dword v4, v3, s[40:43], s58 offen sc1\n"
    "ds_read_b128 v[96:99], v1 offset:17408\n"
    "s_waitcnt lgkmcnt(4)\n"
    "v_mfma_f32_16x16x32_bf16 v[16:19], a[60:63], v[80:83], v[16:19]\n"
    "s_add_u32 m0, s61, 0\n"
    "s_add_u32 s51, s50, 0\n"
    "buffer_load_dwordx4 v0, s[40:43], s51 offen offset:0 lds\n"
    "v_mfma_f32_16x16x32_bf16 v[24:27], a[140:143], v[80:83], v[24:27]\n"
    "v_mfma_f32_16x16x32_bf16 v[32:35], a[220:223], v[80:83], v[32:35]\n"
    "ds_read_b128 v[80:83], v1 offset:37888\n"
    "s_waitcnt lgkmcnt(4)\n"
    "v_mfma_f32_16x16x32_bf16 v[20:23], a[60:63], v[84:87], v[20:23]\n"
    "buffer_load_dwordx4 v0, s[40:43], s51 offen offset:1024 lds\n"
    "v_mfma_f32_16x16x32_bf16 v[28:31], a[140:143], v[84:87], v[28:31]\n"
    "v_mfma_f32_16x16x32_bf16 v[36:39], a[220:223], v[84:87], v[36:39]\n"
    "ds_read_b128 v[84:87], v1 offset:18432\n"
    "s_waitcnt lgkmcnt(4)\n"
    "v_mfma_f32_16x16x32_bf16 v[16:19], a[64:67], v[88:91], v[16:19]\n"
    "buffer_load_dwordx4 v0, s[40:43], s51 offen offset:2048 lds\n"
    "v_mfma_f32_16x16x32_bf16 v[24:27], a[144:147], v[88:91], v[24:27]\n"
    "v_mfma_f32_16x16x32_bf16 v[32:35], a[224:227], v[88:91], v[32:35]\n"
    "ds_read_b128 v[88:91], v1 offset:38912\n"
    "s_waitcnt lgkmcnt(4)\n"
    "v_mfma_f32_16x16x32_bf16 v[20:23], a[64:67], v[92:95], v[20:23]\n"
    "buffer_load_dwordx4 v0, s[40:43], s51 offen offset:3072 lds\n"
    "v_mfma_f32_16x16x32_bf16 v[28:31], a[144:147], v[92:95], v[28:31]\n"
    "v_mfma_f32_16x16x32_bf16 v[36:39], a[224:227], v[92:95], v[36:39]\n"
    "ds_read_b128 v[92:95], v1 offset:19456\n"
    "s_waitcnt lgkmcnt(4)\n"
    "v_mfma_f32_16x16x32_bf16 v[16:19], a[68:71], v[96:99], v[16:19]\n"
    "s_add_u32 m0, s61, 4096\n"
    "s_add_u32 s51, s50, 4096\n"
    "buffer_load_dwordx4 v0, s[40:43], s51 offen offset:0 lds\n"
    "v_mfma_f32_16x16x32_bf16 v[24:27], a[148:151], v[96:99], v[24:27]\n"
    "v_mfma_f32_16x16x32_bf16 v[32:35], a[228:231], v[96:99], v[32:35]\n"
    "ds_read_b128 v[96:99], v1 offset:39936\n"
    "s_waitcnt lgkmcnt(4)\n"
    "v_mfma_f32_16x16x32_bf16 v[20:23], a[68:71], v[80:83], v[20:23]\n"
    "buffer_load_dwordx4 v0, s[40:43], s51 offen offset:1024 lds\n"
    "v_mfma_f32_16x16x32_bf16 v[28:31], a[148:151], v[80:83], v[28:31]\n"
    "v_mfma_f32_16x16x32_bf16 v[36:39], a[228:231], v[80:83], v[36:39]\n"
    "s_add_u32 s64, s64, 40960\n"
    "s_cmp_ge_u32 s64, 122880\n"
    "s_cselect_b32 s64, 0, s64\n"
    "v_add_u32 v1, s64, v0\n"
    "ds_read_b128 v[80:83], v1 offset:0\n"
    "s_waitcnt lgkmcnt(4)\n"
    "v_mfma_f32_16x16x32_bf16 v[16:19], a[72:75], v[84:87], v[16:19]\n"
    "buffer_load_dwordx4 v0, s[40:43], s51 offen offset:2048 lds\n"
    "v_mfma_f32_16x16x32_bf16 v[24:27], a[152:155], v[84:87], v[24:27]\n"
    "v_mfma_f32_16x16x32_bf16 v[32:35], a[232:235], v[84:87], v[32:35]\n"
    "ds_read_b128 v[84:87], v1 offset:20480\n"
    "s_waitcnt lgkmcnt(4)\n"
    "v_mfma_f32_16x16x32_bf16 v[20:23], a[72:75], v[88:91], v[20:23]\n"
    "buffer_load_dwordx4 v0, s[40:43], s51 offen offset:3072 lds\n"
    "v_mfma_f32_16x16x32_bf16 v[28:31], a[152:155], v[88:91], v[28:31]\n"
    "v_mfma_f32_16x16x32_bf16 v[36:39], a[232:235], v[88:91], v[36:39]\n"
    "ds_read_b128 v[88:91], v1 offset:1024\n"
    "s_waitcnt lgkmcnt(4)\n"
    "v_mfma_f32_16x16x32_bf16 v[16:19], a[76:79], v[92:95], v[16:19]\n"
    "s_add_u32 m0, s61, 8192\n"
    "s_add_u32 s51, s50, 8192\n"
    "buffer_load_dwordx4 v0, s[40:43], s51 offen offset:0 lds\n"
    "v_mfma_f32_16x16x32_bf16 v[24:27], a[156:159], v[92:95], v[24:27]\n"
    "v_mfma_f32_16x16x32_bf16 v[32:35], a[236:239], v[92:95], v[32:35]\n"
    "ds_read_b128 v[92:95], v1 offset:21504\n"
    "s_waitcnt lgkmcnt(4)\n"
    "v_mfma_f32_16x16x32_bf16 v[20:23], a[76:79], v[96:99], v[20:23]\n"
    "buffer_load_dwordx4 v0, s[40:43], s51 offen offset:1024 lds\n"
    "s_add_u32 s50, s50, 40960\n"
    "s_add_u32 s61, s61, 40960\n"
    "s_cmp_ge_u32 s61, 122880\n"
    "s_cselect_b32 s51, 122880, 0\n"
    "s_sub_u32 s61, s61, s51\n"
    "v_mfma_f32_16x16x32_bf16 v[28:31], a[156:159], v[96:99], v[28:31]\n"
    "v_mfma_f32_16x16x32_bf16 v[36:39], a[236:239], v[96:99], v[36:39]\n"
    "ds_read_b128 v[96:99], v1 offset:2048\n"
    "s_mov_b32 s65, s63\n"
    "s_add_u32 s63, s63, 393216\n"
    "s_add_u32 s47, s47, 1\n"
    "s_waitcnt lgkmcnt(4)\n"
    "v_mfma_f32_16x16x32_bf16 v[40:43], a[0:3], v[80:83], 0\n"
    "v_mfma_f32_16x16x32_bf16 v[48:51], a[80:83], v[80:83], 0\n"
    "v_mfma_f32_16x16x32_bf16 v[56:59], a[160:163], v[80:83], 0\n"
    "ds_read_b128 v[80:83], v1 offset:22528\n"
    "s_waitcnt lgkmcnt(4)\n"
    "v_mfma_f32_16x16x32_bf16 v[44:47], a[0:3], v[84:87], 0\n"
    "v_mfma_f32_16x16x32_bf16 v[52:55], a[80:83], v[84:87], 0\n"
    "v_mfma_f32_16x16x32_bf16 v[60:63], a[160:163], v[84:87], 0\n"
    "ds_read_b128 v[84:87], v1 offset:3072\n"
    "s_waitcnt lgkmcnt(4)\n"
    "v_mfma_f32_16x16x32_bf16 v[40:43], a[4:7], v[88:91], v[40:43]\n"
    "v_mfma_f32_16x16x32_bf16 v[48:51], a[84:87], v[88:91], v[48:51]\n"
    "v_mfma_f32_16x16x32_bf16 v[56:59], a[164:167], v[88:91], v[56:59]\n"
    "ds_read_b128 v[88:91], v1 offset:23552\n"
    "s_waitcnt lgkmcnt(4)\n"
    "v_mfma_f32_16x16x32_bf16 v[44:47], a[4:7], v[92:95], v[44:47]\n"
    "v_cvt_pk_bf16_f32 v104, v16, v17\n"
    "v_mfma_f32_16x16x32_bf16 v[52:55], a[84:87], v[92:95], v[52:55]\n"
    "v_cvt_pk_bf16_f32 v105, v18, v19\n"
    "v_mfma_f32_16x16x32_bf16 v[60:63], a[164:167], v[92:95], v[60:63]\n"
    "ds_read_b128 v[92:95], v1 offset:4096\n"
    "s_waitcnt lgkmcnt(4)\n"
    "v_mfma_f32_16x16x32_bf16 v[40:43], a[8:11], v[96:99], v[40:43]\n"
    "v_cvt_pk_bf16_f32 v106, v20, v21\n"
    "v_mfma_f32_16x16x32_bf16 v[48:51], a[88:91], v[96:99], v[48:51]\n"
    "v_cvt_pk_bf16_f32 v107, v22, v23\n"
    "v_mfma_f32_16x16x32_bf16 v[56:59], a[168:171], v[96:99], v[56:59]\n"
    "ds_read_b128 v[96:99], v1 offset:24576\n"
    "s_waitcnt lgkmcnt(4)\n"
    "v_mfma_f32_16x16x32_bf16 v[44:47], a[8:11], v[80:83], v[44:47]\n"
    "buffer_store_dwordx4 v[104:107], v0, s[40:43], s65 offen offset:0 sc1\n"
    "v_mfma_f32_16x16x32_bf16 v[52:55], a[88:91], v[80:83], v[52:55]\n"
    "v_cvt_pk_bf16_f32 v108, v24, v25\n"
    "v_mfma_f32_16x16x32_bf16 v[60:63], a[168:171], v[80:83], v[60:63]\n"
    "ds_read_b128 v[80:83], v1 offset:5120\n"
    "s_waitcnt lgkmcnt(4)\n"
    "v_mfma_f32_16x16x32_bf16 v[40:43], a[12:15], v[84:87], v[40:43]\n"
    "v_cvt_pk_bf16_f32 v109, v26, v27\n"
    "v_mfma_f32_16x16x32_bf16 v[48:51], a[92:95], v[84:87], v[48:51]\n"
    "v_cvt_pk_bf16_f32 v110, v28, v29\n"
    "v_mfma_f32_16x16x32_bf16 v[56:59], a[172:175], v[84:87], v[56:59]\n"
    "ds_read_b128 v[84:87], v1 offset:25600\n"
    "s_waitcnt lgkmcnt(4)\n"
    "v_mfma_f32_16x16x32_bf16 v[44:47], a[12:15], v[88:91], v[44:47]\n"
    "v_cvt_pk_bf16_f32 v111, v30, v31\n"
    "v_mfma_f32_16x16x32_bf16 v[52:55], a[92:95], v[88:91], v[52:55]\n"
    "buffer_store_dwordx4 v[108:111], v0, s[40:43], s65 offen offset:1024 sc1\n"
    "v_mfma_f32_16x16x32_bf16 v[60:63], a[172:175], v[88:91], v[60:63]\n"
    "ds_read_b128 v[88:91], v1 offset:6144\n"
    "s_waitcnt lgkmcnt(4)\n"
    "v_mfma_f32_16x16x32_bf16 v[40:43], a[16:19], v[92:95], v[40:43]\n"
    "v_cvt_pk_bf16_f32 v112, v32, v33\n"
    "v_mfma_f32_16x16x32_bf16 v[48:51], a[96:99], v[92:95], v[48:51]\n"
    "v_cvt_pk_bf16_f32 v113, v34, v35\n"
    "v_mfma_f32_16x16x32_bf16 v[56:59], a[176:179], v[92:95], v[56:59]\n"
    "ds_read_b128 v[92:95], v1 offset:26624\n"
    "s_waitcnt lgkmcnt(4)\n"
    "v_mfma_f32_16x16x32_bf16 v[44:47], a[16:19], v[96:99], v[44:47]\n"
    "v_cvt_pk_bf16_f32 v114, v36, v37\n"
    "v_mfma_f32_16x16x32_bf16 v[52:55], a[96:99], v[96:99], v[52:55]\n"
    "v_cvt_pk_bf16_f32 v115, v38, v39\n"
    "v_mfma_f32_16x16x32_bf16 v[60:63], a[176:179], v[96:99], v[60:63]\n"
    "ds_read_b128 v[96:99], v1 offset:7168\n"
    "s_waitcnt lgkmcnt(4)\n"
    "v_mfma_f32_16x16x32_bf16 v[40:43], a[20:23], v[80:83], v[40:43]\n"
    "buffer_store_dwordx4 v[112:115], v0, s[40:43], s65 offen offset:2048 sc1\n"
    "v_mfma_f32_16x16x32_bf16 v[48:51], a[100:103], v[80:83], v[48:51]\n"
    "v_mfma_f32_16x16x32_bf16 v[56:59], a[180:183], v[80:83], v[56:59]\n"
    "ds_read_b128 v[80:83], v1 offset:27648\n"
    "s_waitcnt lgkmcnt(4)\n"
    "v_mfma_f32_16x16x32_bf16 v[44:47], a[20:23], v[84:87], v[44:47]\n"
    "v_mfma_f32_16x16x32_bf16 v[52:55], a[100:103], v[84:87], v[52:55]\n"
    "v_mfma_f32_16x16x32_bf16 v[60:63], a[180:183], v[84:87], v[60:63]\n"
    "ds_read_b128 v[84:87], v1 offset:8192\n"
    "s_waitcnt lgkmcnt(4)\n"
    "v_mfma_f32_16x16x32_bf16 v[40:43], a[24:27], v[88:91], v[40:43]\n"
    "v_mfma_f32_16x16x32_bf16 v[48:51], a[104:107], v[88:91], v[48:51]\n"
    "v_mfma_f32_16x16x32_bf16 v[56:59], a[184:187], v[88:91], v[56:59]\n"
    "ds_read_b128 v[88:91], v1 offset:28672\n"
    "s_waitcnt lgkmcnt(4)\n"
    "v_mfma_f32_16x16x32_bf16 v[44:47], a[24:27], v[92:95], v[44:47]\n"
    "v_mfma_f32_16x16x32_bf16 v[52:55], a[104:107], v[92:95], v[52:55]\n"
    "v_mfma_f32_16x16x32_bf16 v[60:63], a[184:187], v[92:95], v[60:63]\n"
    "ds_read_b128 v[92:95], v1 offset:9216\n"
    "s_waitcnt lgkmcnt(4)\n"
    "v_mfma_f32_16x16x32_bf16 v[40:43], a[28:31], v[96:99], v[40:43]\n"
    "v_mfma_f32_16x16x32_bf16 v[48:51], a[108:111], v[96:99], v[48:51]\n"
    "v_mfma_f32_16x16x32_bf16 v[56:59], a[188:191], v[96:99], v[56:59]\n"
    "ds_read_b128 v[96:99], v1 offset:29696\n"
    "s_waitcnt lgkmcnt(4)\n"
    "v_mfma_f32_16x16x32_bf16 v[44:47], a[28:31], v[80:83], v[44:47]\n"
    "v_mfma_f32_16x16x32_bf16 v[52:55], a[108:111], v[80:83], v[52:55]\n"
    "v_mfma_f32_16x16x32_bf16 v[60:63], a[188:191], v[80:83], v[60:63]\n"
    "ds_read_b128 v[80:83], v1 offset:10240\n"
    "s_waitcnt lgkmcnt(4)\n"
    "v_mfma_f32_16x16x32_bf16 v[40:43], a[32:35], v[84:87], v[40:43]\n"
    "v_mfma_f32_16x16x32_bf16 v[48:51], a[112:115], v[84:87], v[48:51]\n"
    "v_mfma_f32_16x16x32_bf16 v[56:59], a[192:195], v[84:87], v[56:59]\n"
    "ds_read_b128 v[84:87], v1 offset:30720\n"
    "s_waitcnt lgkmcnt(4)\n"
    "v_mfma_f32_16x16x32_bf16 v[44:47], a[32:35], v[88:91], v[44:47]\n"
    "v_mfma_f32_16x16x32_bf16 v[52:55], a[112:115], v[88:91], v[52:55]\n"
    "v_mfma_f32_16x16x32_bf16 v[60:63], a[192:195], v[88:91], v[60:63]\n"
    "ds_read_b128 v[88:91], v1 offset:11264\n"
    "s_waitcnt lgkmcnt(4)\n"
    "v_mfma_f32_16x16x32_bf16 v[40:43], a[36:39], v[92:95], v[40:43]\n"
    "v_mfma_f32_16x16x32_bf16 v[48:51], a[116:119], v[92:95], v[48:51]\n"
    "v_mfma_f32_16x16x32_bf16 v[56:59], a[196:199], v[92:95], v[56:59]\n"
    "ds_read_b128 v[92:95], v1 offset:31744\n"
    "s_waitcnt lgkmcnt(4)\n"
    "v_mfma_f32_16x16x32_bf16 v[44:47], a[36:39], v[96:99], v[44:47]\n"
    "v_mfma_f32_16x16x32_bf16 v[52:55], a[116:119], v[96:99], v[52:55]\n"
    "v_mfma_f32_16x16x32_bf16 v[60:63], a[196:199], v[96:99], v[60:63]\n"
    "ds_read_b128 v[96:99], v1 offset:12288\n"
    "s_waitcnt lgkmcnt(4)\n"
    "v_mfma_f32_16x16x32_bf16 v[40:43], a[40:43], v[80:83], v[40:43]\n"
    "v_mfma_f32_16x16x32_bf16 v[48:51], a[120:123], v[80:83], v[48:51]\n"
    "v_mfma_f32_16x16x32_bf16 v[56:59], a[200:203], v[80:83], v[56:59]\n"
    "ds_read_b128 v[80:83], v1 offset:32768\n"
    "s_waitcnt lgkmcnt(4)\n"
    "v_mfma_f32_16x16x32_bf16 v[44:47], a[40:43], v[84:87], v[44:47]\n"
    "v_mfma_f32_16x16x32_bf16 v[52:55], a[120:123], v[84:87], v[52:55]\n"
    "v_mfma_f32_16x16x32_bf16 v[60:63], a[200:203], v[84:87], v[60:63]\n"
    "ds_read_b128 v[84:87], v1 offset:13312\n"
    "s_waitcnt lgkmcnt(4)\n"
    "v_mfma_f32_16x16x32_bf16 v[40:43], a[44:47], v[88:91], v[40:43]\n"
    "v_mfma_f32_16x16x32_bf16 v[48:51], a[124:127], v[88:91], v[48:51]\n"
    "v_mfma_f32_16x16x32_bf16 v[56:59], a[204:207], v[88:91], v[56:59]\n"
    "ds_read_b128 v[88:91], v1 offset:33792\n"
    "s_waitcnt lgkmcnt(4)\n"
    "v_mfma_f32_16x16x32_bf16 v[44:47], a[44:47], v[92:95], v[44:47]\n"
    "v_mfma_f32_16x16x32_bf16 v[52:55], a[124:127], v[92:95], v[52:55]\n"
    "v_mfma_f32_16x16x32_bf16 v[60:63], a[204:207], v[92:95], v[60:63]\n"
    "ds_read_b128 v[92:95], v1 offset:14336\n"
    "s_waitcnt lgkmcnt(4)\n"
    "v_mfma_f32_16x16x32_bf16 v[40:43], a[48:51], v[96:99], v[40:43]\n"
    "v_mfma_f32_16x16x32_bf16 v[48:51], a[128:131], v[96:99], v[48:51]\n"
    "v_mfma_f32_16x16x32_bf16 v[56:59], a[208:211], v[96:99], v[56:59]\n"
    "ds_read_b128 v[96:99], v1 offset:34816\n"
    "s_waitcnt lgkmcnt(4)\n"
    "v_mfma_f32_16x16x32_bf16 v[44:47], a[48:51], v[80:83], v[44:47]\n"
    "v_mfma_f32_16x16x32_bf16 v[52:55], a[128:131], v[80:83], v[52:55]\n"
    "v_mfma_f32_16x16x32_bf16 v[60:63], a[208:211], v[80:83], v[60:63]\n"
    "ds_read_b128 v[80:83], v1 offset:15360\n"
    "s_waitcnt lgkmcnt(4)\n"
    "v_mfma_f32_16x16x32_bf16 v[40:43], a[52:55], v[84:87], v[40:43]\n"
    "v_mfma_f32_16x16x32_bf16 v[48:51], a[132:135], v[84:87], v[48:51]\n"
    "v_mfma_f32_16x16x32_bf16 v[56:59], a[212:215], v[84:87], v[56:59]\n"
    "ds_read_b128 v[84:87], v1 offset:35840\n"
    "s_waitcnt lgkmcnt(4)\n"
    "v_mfma_f32_16x16x32_bf16 v[44:47], a[52:55], v[88:91], v[44:47]\n"
    "v_mfma_f32_16x16x32_bf16 v[52:55], a[132:135], v[88:91], v[52:55]\n"
    "v_mfma_f32_16x16x32_bf16 v[60:63], a[212:215], v[88:91], v[60:63]\n"
    "ds_read_b128 v[88:91], v1 offset:16384\n"
    "s_waitcnt lgkmcnt(4)\n"
    "v_mfma_f32_16x16x32_bf16 v[40:43], a[56:59], v[92:95], v[40:43]\n"
    "v_mfma_f32_16x16x32_bf16 v[48:51], a[136:139], v[92:95], v[48:51]\n"
    "v_mfma_f32_16x16x32_bf16 v[56:59], a[216:219], v[92:95], v[56:59]\n"
    "ds_read_b128 v[92:95], v1 offset:36864\n"
    "s_waitcnt lgkmcnt(4)\n"
    "v_mfma_f32_16x16x32_bf16 v[44:47], a[56:59], v[96:99], v[44:47]\n"
    "v_mfma_f32_16x16x32_bf16 v[52:55], a[136:139], v[96:99], v[52:55]\n"
    "v_mfma_f32_16x16x32_bf16 v[60:63], a[216:219], v[96:99], v[60:63]\n"
    "s_waitcnt vmcnt(0)\n"
    "s_barrier\n"
    "v_mov_b32 v4, s47\n"
    "buffer_store_dword v4, v3, s[40:43], s58 offen sc1\n"
    "ds_read_b128 v[96:99], v1 offset:17408\n"
    "s_waitcnt lgkmcnt(4)\n"
    "v_mfma_f32_16x16x32_bf16 v[40:43], a[60:63], v[80:83], v[40:43]\n"
    "s_add_u32 m0, s61, 0\n"
    "s_add_u32 s51, s50, 0\n"
    "buffer_load_dwordx4 v0, s[40:43], s51 offen offset:0 lds\n"
    "v_mfma_f32_16x16x32_bf16 v[48:51], a[140:143], v[80:83], v[48:51]\n"
    "v_mfma_f32_16x16x32_bf16 v[56:59], a[220:223], v[80:83], v[56:59]\n"
    "ds_read_b128 v[80:83], v1 offset:37888\n"
    "s_waitcnt lgkmcnt(4)\n"
    "v_mfma_f32_16x16x32_bf16 v[44:47], a[60:63], v[84:87], v[44:47]\n"
    "buffer_load_dwordx4 v0, s[40:43], s51 offen offset:1024 lds\n"
    "v_mfma_f32_16x16x32_bf16 v[52:55], a[140:143], v[84:87], v[52:55]\n"
    "v_mfma_f32_16x16x32_bf16 v[60:63], a[220:223], v[84:87], v[60:63]\n"
    "ds_read_b128 v[84:87], v1 offset:18432\n"
    "s_waitcnt lgkmcnt(4)\n"
    "v_mfma_f32_16x16x32_bf16 v[40:43], a[64:67], v[88:91], v[40:43]\n"
    "buffer_load_dwordx4 v0, s[40:43], s51 offen offset:2048 lds\n"
    "v_mfma_f32_16x16x32_bf16 v[48:51], a[144:147], v[88:91], v[48:51]\n"
    "v_mfma_f32_16x16x32_bf16 v[56:59], a[224:227], v[88:91], v[56:59]\n"
    "ds_read_b128 v[88:91], v1 offset:38912\n"
    "s_waitcnt lgkmcnt(4)\n"
    "v_mfma_f32_16x16x32_bf16 v[44:47], a[64:67], v[92:95], v[44:47]\n"
    "buffer_load_dwordx4 v0, s[40:43], s51 offen offset:3072 lds\n"
    "v_mfma_f32_16x16x32_bf16 v[52:55], a[144:147], v[92:95], v[52:55]\n"
    "v_mfma_f32_16x16x32_bf16 v[60:63], a[224:227], v[92:95], v[60:63]\n"
    "ds_read_b128 v[92:95], v1 offset:19456\n"
    "s_waitcnt lgkmcnt(4)\n"
    "v_mfma_f32_16x16x32_bf16 v[40:43], a[68:71], v[96:99], v[40:43]\n"
    "s_add_u32 m0, s61, 4096\n"
    "s_add_u32 s51, s50, 4096\n"
    "buffer_load_dwordx4 v0, s[40:43], s51 offen offset:0 lds\n"
    "v_mfma_f32_16x16x32_bf16 v[48:51], a[148:151], v[96:99], v[48:51]\n"
    "v_mfma_f32_16x16x32_bf16 v[56:59], a[228:231], v[96:99], v[56:59]\n"
    "ds_read_b128 v[96:99], v1 offset:39936\n"
    "s_waitcnt lgkmcnt(4)\n"
    "v_mfma_f32_16x16x32_bf16 v[44:47], a[68:71], v[80:83], v[44:47]\n"
    "buffer_load_dwordx4 v0, s[40:43], s51 offen offset:1024 lds\n"
    "v_mfma_f32_16x16x32_bf16 v[52:55], a[148:151], v[80:83], v[52:55]\n"
    "v_mfma_f32_16x16x32_bf16 v[60:63], a[228:231], v[80:83], v[60:63]\n"
    "s_add_u32 s64, s64, 40960\n"
    "s_cmp_ge_u32 s64, 122880\n"
    "s_cselect_b32 s64, 0, s64\n"
    "v_add_u32 v1, s64, v0\n"
    "ds_read_b128 v[80:83], v1 offset:0\n"
    "s_waitcnt lgkmcnt(4)\n"
    "v_mfma_f32_16x16x32_bf16 v[40:43], a[72:75], v[84:87], v[40:43]\n"
    "buffer_load_dwordx4 v0, s[40:43], s51 offen offset:2048 lds\n"
    "v_mfma_f32_16x16x32_bf16 v[48:51], a[152:155], v[84:87], v[48:51]\n"
    "v_mfma_f32_16x16x32_bf16 v[56:59], a[232:235], v[84:87], v[56:59]\n"
    "ds_read_b128 v[84:87], v1 offset:20480\n"
    "s_waitcnt lgkmcnt(4)\n"
    "v_mfma_f32_16x16x32_bf16 v[44:47], a[72:75], v[88:91], v[44:47]\n"
    "buffer_load_dwordx4 v0, s[40:43], s51 offen offset:3072 lds\n"
    "v_mfma_f32_16x16x32_bf16 v[52:55], a[152:155], v[88:91], v[52:55]\n"
    "v_mfma_f32_16x16x32_bf16 v[60:63], a[232:235], v[88:91], v[60:63]\n"
    "ds_read_b128 v[88:91], v1 offset:1024\n"
    "s_waitcnt lgkmcnt(4)\n"
    "v_mfma_f32_16x16x32_bf16 v[40:43], a[76:79], v[92:95], v[40:43]\n"
    "s_add_u32 m0, s61, 8192\n"
    "s_add_u32 s51, s50, 8192\n"
    "buffer_load_dwordx4 v0, s[40:43], s51 offen offset:0 lds\n"
    "v_mfma_f32_16x16x32_bf16 v[48:51], a[156:159], v[92:95], v[48:51]\n"
    "v_mfma_f32_16x16x32_bf16 v[56:59], a[236:239], v[92:95], v[56:59]\n"
    "ds_read_b128 v[92:95], v1 offset:21504\n"
    "s_waitcnt lgkmcnt(4)\n"
    "v_mfma_f32_16x16x32_bf16 v[44:47], a[76:79], v[96:99], v[44:47]\n"
    "buffer_load_dwordx4 v0, s[40:43], s51 offen offset:1024 lds\n"
    "s_add_u32 s50, s50, 40960\n"
    "s_add_u32 s61, s61, 40960\n"
    "s_cmp_ge_u32 s61, 122880\n"
    "s_cselect_b32 s51, 122880, 0\n"
    "s_sub_u32 s61, s61, s51\n"
    "v_mfma_f32_16x16x32_bf16 v[52:55], a[156:159], v[96:99], v[52:55]\n"
    "v_mfma_f32_16x16x32_bf16 v[60:63], a[236:239], v[96:99], v[60:63]\n"
    "ds_read_b128 v[96:99], v1 offset:2048\n"
    "s_mov_b32 s65, s63\n"
    "s_add_u32 s63, s63, 393216\n"
    "s_add_u32 s47, s47, 1\n"
    "s_cmp_lt_u32 s47, 64\n"
    "s_cbranch_scc1 pk_loop_%=\n"
    "s_waitcnt lgkmcnt(0)\n"
    "v_cvt_pk_bf16_f32 v104, v40, v41\n"
    "v_cvt_pk_bf16_f32 v105, v42, v43\n"
    "v_cvt_pk_bf16_f32 v106, v44, v45\n"
    "v_cvt_pk_bf16_f32 v107, v46, v47\n"
    "buffer_store_dwordx4 v[104:107], v0, s[40:43], s65 offen offset:0 sc1\n"
    "v_cvt_pk_bf16_f32 v108, v48, v49\n"
    "v_cvt_pk_bf16_f32 v109, v50, v51\n"
    "v_cvt_pk_bf16_f32 v110, v52, v53\n"
    "v_cvt_pk_bf16_f32 v111, v54, v55\n"
    "buffer_store_dwordx4 v[108:111], v0, s[40:43], s65 offen offset:1024 sc1\n"
    "v_cvt_pk_bf16_f32 v112, v56, v57\n"
    "v_cvt_pk_bf16_f32 v113, v58, v59\n"
    "v_cvt_pk_bf16_f32 v114, v60, v61\n"
    "v_cvt_pk_bf16_f32 v115, v62, v63\n"
    "buffer_store_dwordx4 v[112:115], v0, s[40:43], s65 offen offset:2048 sc1\n"
    "s_waitcnt vmcnt(0)\n"
    "s_waitcnt lgkmcnt(0)\n"
    "s_barrier\n"
    "v_mov_b32 v4, 64\n"
    "buffer_store_dword v4, v3, s[40:43], s58 offen sc1\n"
    "s_nop 1\n"
    "s_branch pk_end_%=\n"
    "pk_abort_%=:\n"
    "v_mov_b32 v4, 1\n"
    "v_mov_b32 v5, 1024\n"
    "buffer_store_dword v4, v5, s[40:43], s58 offen sc1\n"
    "pk_end_%=:\n"
    "s_endpgm\n"
    :: ROLE_INPUTS : ROLE_CLOBBERS);
}
DEV void role_out(unsigned lo, unsigned hi, int id, int w, int aux, const Tune& tn) {
    asm volatile(
    "s_mov_b32 s40, %0\n"
    "s_mov_b32 s41, %1\n"
    "s_mov_b32 s42, %2\n"
    "s_mov_b32 s43, %3\n"
    "s_mov_b32 s44, %4\n"
    "s_mov_b32 s45, %5\n"
    "s_mov_b32 s46, %6\n"
    "s_mov_b32 s66, %7\n"
    "s_mov_b32 s67, %8\n"
    "s_mov_b32 s68, %9\n"
    "s_mov_b32 s69, %10\n"
    "s_mov_b32 s70, %11\n"
    "s_mov_b32 s71, %12\n"
    "s_mov_b32 s72, %13\n"
    "s_mov_b32 s73, %14\n"
    "s_mov_b64 exec, -1\n"
    "v_mbcnt_lo_u32_b32 v15, -1, 0\n"
    "v_mbcnt_hi_u32_b32 v15, -1, v15\n"
    "v_lshlrev_b32 v0, 4, v15\n"
    "s_mov_b32 s58, 2097152\n"
    "s_mov_b32 s92, 0\n"
    "s_mul_i32 s48, s44, 40\n"
    "s_mul_i32 s49, s45, 10\n"
    "s_add_u32 s48, s48, s49\n"
    "s_mul_i32 s48, s48, 6144\n"
    "s_add_u32 s48, s48, 25755648\n"
    "buffer_load_dwordx4 a[0:3], v0, s[40:43], s48 offen\n"
    "buffer_load_dwordx4 a[4:7], v0, s[40:43], s48 offen offset:1024\n"
    "buffer_load_dwordx4 a[8:11], v0, s[40:43], s48 offen offset:2048\n"
    "buffer_load_dwordx4 a[12:15], v0, s[40:43], s48 offen offset:3072\n"
    "s_add_u32 s48, s48, 4096\n"
    "buffer_load_dwordx4 a[16:19], v0, s[40:43], s48 offen\n"
    "buffer_load_dwordx4 a[20:23], v0, s[40:43], s48 offen offset:1024\n"
    "buffer_load_dwordx4 a[24:27], v0, s[40:43], s48 offen offset:2048\n"
    "buffer_load_dwordx4 a[28:31], v0, s[40:43], s48 offen offset:3072\n"
    "s_add_u32 s48, s48, 4096\n"
    "buffer_load_dwordx4 a[32:35], v0, s[40:43], s48 offen\n"
    "buffer_load_dwordx4 a[36:39], v0, s[40:43], s48 offen offset:1024\n"
    "buffer_load_dwordx4 a[40:43], v0, s[40:43], s48 offen offset:2048\n"
    "buffer_load_dwordx4 a[44:47], v0, s[40:43], s48 offen offset:3072\n"
    "s_add_u32 s48, s48, 4096\n"
    "buffer_load_dwordx4 a[48:51], v0, s[40:43], s48 offen\n"
    "buffer_load_dwordx4 a[52:55], v0, s[40:43], s48 offen offset:1024\n"
    "buffer_load_dwordx4 a[56:59], v0, s[40:43], s48 offen offset:2048\n"
    "buffer_load_dwordx4 a[60:63], v0, s[40:43], s48 offen offset:3072\n"
    "s_add_u32 s48, s48, 4096\n"
    "buffer_load_dwordx4 a[64:67], v0, s[40:43], s48 offen\n"
    "buffer_load_dwordx4 a[68:71], v0, s[40:43], s48 offen offset:1024\n"
    "buffer_load_dwordx4 a[72:75], v0, s[40:43], s48 offen offset:2048\n"
    "buffer_load_dwordx4 a[76:79], v0, s[40:43], s48 offen offset:3072\n"
    "s_add_u32 s48, s48, 4096\n"
    "buffer_load_dwordx4 a[80:83], v0, s[40:43], s48 offen\n"
    "buffer_load_dwordx4 a[84:87], v0, s[40:43], s48 offen offset:1024\n"
    "buffer_load_dwordx4 a[88:91], v0, s[40:43], s48 offen offset:2048\n"
    "buffer_load_dwordx4 a[92:95], v0, s[40:43], s48 offen offset:3072\n"
    "s_add_u32 s48, s48, 4096\n"
    "buffer_load_dwordx4 a[96:99], v0, s[40:43], s48 offen\n"
    "buffer_load_dwordx4 a[100:103], v0, s[40:43], s48 offen offset:1024\n"
    "buffer_load_dwordx4 a[104:107], v0, s[40:43], s48 offen offset:2048\n"
    "buffer_load_dwordx4 a[108:111], v0, s[40:43], s48 offen offset:3072\n"
    "s_add_u32 s48, s48, 4096\n"
    "buffer_load_dwordx4 a[112:115], v0, s[40:43], s48 offen\n"
    "buffer_load_dwordx4 a[116:119], v0, s[40:43], s48 offen offset:1024\n"
    "buffer_load_dwordx4 a[120:123], v0, s[40:43], s48 offen offset:2048\n"
    "buffer_load_dwordx4 a[124:127], v0, s[40:43], s48 offen offset:3072\n"
    "s_add_u32 s48, s48, 4096\n"
    "buffer_load_dwordx4 a[128:131], v0, s[40:43], s48 offen\n"
    "buffer_load_dwordx4 a[132:135], v0, s[40:43], s48 offen offset:1024\n"
    "buffer_load_dwordx4 a[136:139], v0, s[40:43], s48 offen offset:2048\n"
    "buffer_load_dwordx4 a[140:143], v0, s[40:43], s48 offen offset:3072\n"
    "s_add_u32 s48, s48, 4096\n"
    "buffer_load_dwordx4 a[144:147], v0, s[40:43], s48 offen\n"
    "buffer_load_dwordx4 a[148:151], v0, s[40:43], s48 offen offset:1024\n"
    "buffer_load_dwordx4 a[152:155], v0, s[40:43], s48 offen offset:2048\n"
    "buffer_load_dwordx4 a[156:159], v0, s[40:43], s48 offen offset:3072\n"
    "s_add_u32 s48, s48, 4096\n"
    "buffer_load_dwordx4 a[160:163], v0, s[40:43], s48 offen\n"
    "buffer_load_dwordx4 a[164:167], v0, s[40:43], s48 offen offset:1024\n"
    "buffer_load_dwordx4 a[168:171], v0, s[40:43], s48 offen offset:2048\n"
    "buffer_load_dwordx4 a[172:175], v0, s[40:43], s48 offen offset:3072\n"
    "s_add_u32 s48, s48, 4096\n"
    "buffer_load_dwordx4 a[176:179], v0, s[40:43], s48 offen\n"
    "buffer_load_dwordx4 a[180:183], v0, s[40:43], s48 offen offset:1024\n"
    "buffer_load_dwordx4 a[184:187], v0, s[40:43], s48 offen offset:2048\n"
    "buffer_load_dwordx4 a[188:191], v0, s[40:43], s48 offen offset:3072\n"
    "s_add_u32 s48, s48, 4096\n"
    "buffer_load_dwordx4 a[192:195], v0, s[40:43], s48 offen\n"
    "buffer_load_dwordx4 a[196:199], v0, s[40:43], s48 offen offset:1024\n"
    "buffer_load_dwordx4 a[200:203], v0, s[40:43], s48 offen offset:2048\n"
    "buffer_load_dwordx4 a[204:207], v0, s[40:43], s48 offen offset:3072\n"
    "s_add_u32 s48, s48, 4096\n"
    "buffer_load_dwordx4 a[208:211], v0, s[40:43], s48 offen\n"
    "buffer_load_dwordx4 a[212:215], v0, s[40:43], s48 offen offset:1024\n"
    "buffer_load_dwordx4 a[216:219], v0, s[40:43], s48 offen offset:2048\n"
    "buffer_load_dwordx4 a[220:223], v0, s[40:43], s48 offen offset:3072\n"
    "s_add_u32 s48, s48, 4096\n"
    "buffer_load_dwordx4 a[224:227], v0, s[40:43], s48 offen\n"
    "buffer_load_dwordx4 a[228:231], v0, s[40:43], s48 offen offset:1024\n"
    "buffer_load_dwordx4 a[232:235], v0, s[40:43], s48 offen offset:2048\n"
    "buffer_load_dwordx4 a[236:239], v0, s[40:43], s48 offen offset:3072\n"
    "v_and_b32 v1, 0x370, v0\n"
    "v_bfe_u32 v4, v15, 3, 1\n"
    "v_lshl_add_u32 v1, v4, 14, v1\n"
    "s_lshr_b32 s48, s44, 1\n"
    "s_lshl_b32 s48, s48, 10\n"
    "s_and_b32 s49, s44, 1\n"
    "s_lshl_b32 s49, s49, 7\n"
    "s_add_u32 s48, s48, s49\n"
    "v_add_u32 v1, s48, v1\n"
    "s_mul_i32 s63, s45, 327680\n"
    "s_add_u32 s63, s63, 112787456\n"
    "s_mul_i32 s48, s45, 3\n"
    "s_add_u32 s48, s48, 0\n"
    "s_and_b32 s49, s48, 1\n"
    "s_lshr_b32 s48, s48, 1\n"
    "s_lshl_b32 s48, s48, 11\n"
    "s_lshl3_add_u32 s48, s49, s48\n"
    "v_add_u32 v8, s48, v0\n"
    "s_mul_i32 s48, s45, 3\n"
    "s_add_u32 s48, s48, 1\n"
    "s_and_b32 s49, s48, 1\n"
    "s_lshr_b32 s48, s48, 1\n"
    "s_lshl_b32 s48, s48, 11\n"
    "s_lshl3_add_u32 s48, s49, s48\n"
    "v_add_u32 v9, s48, v0\n"
    "s_mul_i32 s48, s45, 3\n"
    "s_add_u32 s48, s48, 2\n"
    "s_and_b32 s49, s48, 1\n"
    "s_lshr_b32 s48, s48, 1\n"
    "s_lshl_b32 s48, s48, 11\n"
    "s_lshl3_add_u32 s48, s49, s48\n"
    "v_add_u32 v10, s48, v0\n"
    "s_mul_i32 s49, s45, 3072\n"
    "s_mul_i32 s50, s44, 12288\n"
    "s_add_u32 s50, s50, 87621632\n"
    "s_add_u32 s50, s50, s49\n"
    "s_lshl_b32 s48, s44, 7\n"
    "s_add_u32 s49, s48, 6144\n"
    "v_mov_b32 v7, s49\n"
    "v_mov_b32 v6, 0\n"
    "s_add_u32 s49, s48, 10240\n"
    "v_mov_b32 v2, s49\n"
    "v_or_b32 v4, s45, v15\n"
    "v_cmp_eq_u32 vcc, 0, v4\n"
    "v_mov_b32 v4, 2147483632\n"
    "s_nop 0\n"
    "v_cndmask_b32 v2, v4, v2, vcc\n"
    "s_mov_b32 s47, 0\n"
    "s_waitcnt vmcnt(0)\n"
    "s_mov_b32 s55, 1\n"
    "s_mov_b32 s64, 0\n"
    "s_mov_b32 s60, 0\n"
    "v_cmp_gt_u32 vcc, s55, v6\n"
    "s_nop 4\n"
    "s_cbranch_vccz po_v0_ok_%=\n"
    "po_v0_poll_%=:\n"
    "buffer_load_dword v6, v7, s[40:43], s58 offen sc1\n"
    "s_waitcnt vmcnt(0)\n"
    "v_cmp_gt_u32 vcc, s55, v6\n"
    "s_nop 4\n"
    "s_cbranch_vccz po_v0_ok_%=\n"
    "s_sleep 2\n"
    "s_add_u32 s60, s60, 1\n"
    "s_cmp_gt_u32 s60, 65536\n"
    "s_cbranch_scc1 po_abort_%=\n"
    "s_and_b32 s59, s60, 63\n"
    "s_cbranch_scc1 po_v0_poll_%=\n"
    "v_mov_b32 v4, 1024\n"
    "buffer_load_dword v4, v4, s[40:43], s58 offen sc1\n"
    "s_waitcnt vmcnt(0)\n"
    "v_readfirstlane_b32 s59, v4\n"
    "s_nop 0\n"
    "s_cmp_lg_u32 s59, 0\n"
    "s_nop 2\n"
    "s_cbranch_scc1 po_end_%=\n"
    "s_branch po_v0_poll_%=\n"
    "po_v0_ok_%=:\n"
    "buffer_load_dwordx4 v[144:147], v0, s[40:43], s50 offen offset:0 sc1\n"
    "buffer_load_dwordx4 v[148:151], v0, s[40:43], s50 offen offset:1024 sc1\n"
    "buffer_load_dwordx4 v[152:155], v0, s[40:43], s50 offen offset:2048 sc1\n"
    "s_waitcnt vmcnt(2)\n"
    "ds_write2st64_b64 v8, v[144:145], v[146:147] offset0:0 offset1:2\n"
    "s_waitcnt vmcnt(1)\n"
    "ds_write2st64_b64 v9, v[148:149], v[150:151] offset0:0 offset1:2\n"
    "s_waitcnt vmcnt(0)\n"
    "ds_write2st64_b64 v10, v[152:153], v[154:155] offset0:0 offset1:2\n"
    "s_waitcnt lgkmcnt(0)\n"
    "s_barrier\n"
    "ds_read_b128 v[16:19], v0 offset:0\n"
    "ds_read_b128 v[20:23], v0 offset:1024\n"
    "ds_read_b128 v[24:27], v0 offset:2048\n"
    "ds_read_b128 v[28:31], v0 offset:3072\n"
    "ds_read_b128 v[32:35], v0 offset:4096\n"
    "ds_read_b128 v[36:39], v0 offset:5120\n"
    "ds_read_b128 v[40:43], v0 offset:6144\n"
    "ds_read_b128 v[44:47], v0 offset:7168\n"
    "ds_read_b128 v[48:51], v0 offset:8192\n"
    "ds_read_b128 v[52:55], v0 offset:9216\n"
    "ds_read_b128 v[56:59], v0 offset:10240\n"
    "ds_read_b128 v[60:63], v0 offset:11264\n"
    "s_waitcnt lgkmcnt(0)\n"
    "s_mov_b32 s53, 0x40000000\n"
    "po_loop_%=:\n"
    "s_add_u32 s55, s47, 2\n"
    "s_min_u32 s55, s55, 64\n"
    "s_add_u32 s50, s50, 393216\n"
    "v_cmp_gt_u32 vcc, s55, v6\n"
    "s_nop 0\n"
    "s_cmp_eq_u64 vcc, 0\n"
    "s_cselect_b32 s56, s50, 0x40000000\n"
    "v_mfma_f32_16x16x32_bf16 v[112:115], v[16:19], a[0:3], 0\n"
    "buffer_load_dwordx4 v[144:147], v0, s[40:43], s56 offen offset:0 sc1\n"
    "v_mfma_f32_16x16x32_bf16 v[116:119], v[20:23], a[0:3], 0\n"
    "buffer_load_dwordx4 v[148:151], v0, s[40:43], s56 offen offset:1024 sc1\n"
    "v_mfma_f32_16x16x32_bf16 v[112:115], v[24:27], a[4:7], v[112:115]\n"
    "buffer_load_dwordx4 v[152:155], v0, s[40:43], s56 offen offset:2048 sc1\n"
    "v_mfma_f32_16x16x32_bf16 v[116:119], v[28:31], a[4:7], v[116:119]\n"
    "buffer_load_dword v6, v7, s[40:43], s58 offen sc1\n"
    "v_mfma_f32_16x16x32_bf16 v[112:115], v[32:35], a[8:11], v[112:115]\n"
    "v_mfma_f32_16x16x32_bf16 v[116:119], v[36:39], a[8:11], v[116:119]\n"
    "v_mfma_f32_16x16x32_bf16 v[112:115], v[40:43], a[12:15], v[112:115]\n"
    "v_cvt_pk_bf16_f32 v140, v120, v121\n"
    "v_mfma_f32_16x16x32_bf16 v[116:119], v[44:47], a[12:15], v[116:119]\n"
    "v_cvt_pk_bf16_f32 v141, v122, v123\n"
    "v_mfma_f32_16x16x32_bf16 v[112:115], v[48:51], a[16:19], v[112:115]\n"
    "v_cvt_pk_bf16_f32 v142, v124, v125\n"
    "v_mfma_f32_16x16x32_bf16 v[116:119], v[52:55], a[16:19], v[116:119]\n"
    "v_cvt_pk_bf16_f32 v143, v126, v127\n"
    "v_mfma_f32_16x16x32_bf16 v[112:115], v[56:59], a[20:23], v[112:115]\n"
    "buffer_store_dwordx4 v[140:143], v1, s[40:43], s53 offen sc1\n"
    "v_mfma_f32_16x16x32_bf16 v[116:119], v[60:63], a[20:23], v[116:119]\n"
    "s_mov_b32 s53, s63\n"
    "v_mfma_f32_16x16x32_bf16 v[120:123], v[16:19], a[24:27], 0\n"
    "v_mfma_f32_16x16x32_bf16 v[124:127], v[20:23], a[24:27], 0\n"
    "v_mfma_f32_16x16x32_bf16 v[120:123], v[24:27], a[28:31], v[120:123]\n"
    "v_mfma_f32_16x16x32_bf16 v[124:127], v[28:31], a[28:31], v[124:127]\n"
    "v_mfma_f32_16x16x32_bf16 v[120:123], v[32:35], a[32:35], v[120:123]\n"
    "v_mfma_f32_16x16x32_bf16 v[124:127], v[36:39], a[32:35], v[124:127]\n"
    "v_mfma_f32_16x16x32_bf16 v[120:123], v[40:43], a[36:39], v[120:123]\n"
    "v_cvt_pk_bf16_f32 v136, v112, v113\n"
    "v_mfma_f32_16x16x32_bf16 v[124:127], v[44:47], a[36:39], v[124:127]\n"
    "v_cvt_pk_bf16_f32 v137, v114, v115\n"
    "v_mfma_f32_16x16x32_bf16 v[120:123], v[48:51], a[40:43], v[120:123]\n"
    "v_cvt_pk_bf16_f32 v138, v116, v117\n"
    "v_mfma_f32_16x16x32_bf16 v[124:127], v[52:55], a[40:43], v[124:127]\n"
    "v_cvt_pk_bf16_f32 v139, v118, v119\n"
    "v_mfma_f32_16x16x32_bf16 v[120:123], v[56:59], a[44:47], v[120:123]\n"
    "buffer_store_dwordx4 v[136:139], v1, s[40:43], s53 offen sc1\n"
    "v_mfma_f32_16x16x32_bf16 v[124:127], v[60:63], a[44:47], v[124:127]\n"
    "s_add_u32 s53, s53, 32768\n"
    "v_mfma_f32_16x16x32_bf16 v[112:115], v[16:19], a[48:51], 0\n"
    "v_mfma_f32_16x16x32_bf16 v[116:119], v[20:23], a[48:51], 0\n"
    "v_mfma_f32_16x16x32_bf16 v[112:115], v[24:27], a[52:55], v[112:115]\n"
    "v_mfma_f32_16x16x32_bf16 v[116:119], v[28:31], a[52:55], v[116:119]\n"
    "v_mfma_f32_16x16x32_bf16 v[112:115], v[32:35], a[56:59], v[112:115]\n"
    "v_mfma_f32_16x16x32_bf16 v[116:119], v[36:39], a[56:59], v[116:119]\n"
    "v_mfma_f32_16x16x32_bf16 v[112:115], v[40:43], a[60:63], v[112:115]\n"
    "v_cvt_pk_bf16_f32 v140, v120, v121\n"
    "v_mfma_f32_16x16x32_bf16 v[116:119], v[44:47], a[60:63], v[116:119]\n"
    "v_cvt_pk_bf16_f32 v141, v122, v123\n"
    "v_mfma_f32_16x16x32_bf16 v[112:115], v[48:51], a[64:67], v[112:115]\n"
    "v_cvt_pk_bf16_f32 v142, v124, v125\n"
    "v_mfma_f32_16x16x32_bf16 v[116:119], v[52:55], a[64:67], v[116:119]\n"
    "v_cvt_pk_bf16_f32 v143, v126, v127\n"
    "v_mfma_f32_16x16x32_bf16 v[112:115], v[56:59], a[68:71], v[112:115]\n"
    "buffer_store_dwordx4 v[140:143], v1, s[40:43], s53 offen sc1\n"
    "v_mfma_f32_16x16x32_bf16 v[116:119], v[60:63], a[68:71], v[116:119]\n"
    "s_add_u32 s53, s53, 32768\n"
    "v_mfma_f32_16x16x32_bf16 v[120:123], v[16:19], a[72:75], 0\n"
    "v_mfma_f32_16x16x32_bf16 v[124:127], v[20:23], a[72:75], 0\n"
    "v_mfma_f32_16x16x32_bf16 v[120:123], v[24:27], a[76:79], v[120:123]\n"
    "v_mfma_f32_16x16x32_bf16 v[124:127], v[28:31], a[76:79], v[124:127]\n"
    "v_mfma_f32_16x16x32_bf16 v[120:123], v[32:35], a[80:83], v[120:123]\n"
    "v_mfma_f32_16x16x32_bf16 v[124:127], v[36:39], a[80:83], v[124:127]\n"
    "v_mfma_f32_16x16x32_bf16 v[120:123], v[40:43], a[84:87], v[120:123]\n"
    "v_cvt_pk_bf16_f32 v136, v112, v113\n"
    "v_mfma_f32_16x16x32_bf16 v[124:127], v[44:47], a[84:87], v[124:127]\n"
    "v_cvt_pk_bf16_f32 v137, v114, v115\n"
    "v_mfma_f32_16x16x32_bf16 v[120:123], v[48:51], a[88:91], v[120:123]\n"
    "v_cvt_pk_bf16_f32 v138, v116, v117\n"
    "v_mfma_f32_16x16x32_bf16 v[124:127], v[52:55], a[88:91], v[124:127]\n"
    "v_cvt_pk_bf16_f32 v139, v118, v119\n"
    "v_mfma_f32_16x16x32_bf16 v[120:123], v[56:59], a[92:95], v[120:123]\n"
    "buffer_store_dwordx4 v[136:139], v1, s[40:43], s53 offen sc1\n"
    "v_mfma_f32_16x16x32_bf16 v[124:127], v[60:63], a[92:95], v[124:127]\n"
    "s_add_u32 s53, s53, 32768\n"
    "v_mfma_f32_16x16x32_bf16 v[112:115], v[16:19], a[96:99], 0\n"
    "v_mfma_f32_16x16x32_bf16 v[116:119], v[20:23], a[96:99], 0\n"
    "v_mfma_f32_16x16x32_bf16 v[112:115], v[24:27], a[100:103], v[112:115]\n"
    "v_mfma_f32_16x16x32_bf16 v[116:119], v[28:31], a[100:103], v[116:119]\n"
    "v_mfma_f32_16x16x32_bf16 v[112:115], v[32:35], a[104:107], v[112:115]\n"
    "v_mfma_f32_16x16x32_bf16 v[116:119], v[36:39], a[104:107], v[116:119]\n"
    "v_mfma_f32_16x16x32_bf16 v[112:115], v[40:43], a[108:111], v[112:115]\n"
    "v_cvt_pk_bf16_f32 v140, v120, v121\n"
    "v_mfma_f32_16x16x32_bf16 v[116:119], v[44:47], a[108:111], v[116:119]\n"
    "v_cvt_pk_bf16_f32 v141, v122, v123\n"
    "v_mfma_f32_16x16x32_bf16 v[112:115], v[48:51], a[112:115], v[112:115]\n"
    "v_cvt_pk_bf16_f32 v142, v124, v125\n"
    "v_mfma_f32_16x16x32_bf16 v[116:119], v[52:55], a[112:115], v[116:119]\n"
    "v_cvt_pk_bf16_f32 v143, v126, v127\n"
    "v_mfma_f32_16x16x32_bf16 v[112:115], v[56:59], a[116:119], v[112:115]\n"
    "buffer_store_dwordx4 v[140:143], v1, s[40:43], s53 offen sc1\n"
    "v_mfma_f32_16x16x32_bf16 v[116:119], v[60:63], a[116:119], v[116:119]\n"
    "s_add_u32 s53, s53, 32768\n"
    "v_mfma_f32_16x16x32_bf16 v[120:123], v[16:19], a[120:123], 0\n"
    "v_mfma_f32_16x16x32_bf16 v[124:127], v[20:23], a[120:123], 0\n"
    "v_mfma_f32_16x16x32_bf16 v[120:123], v[24:27], a[124:127], v[120:123]\n"
    "v_mfma_f32_16x16x32_bf16 v[124:127], v[28:31], a[124:127], v[124:127]\n"
    "v_mfma_f32_16x16x32_bf16 v[120:123], v[32:35], a[128:131], v[120:123]\n"
    "v_mfma_f32_16x16x32_bf16 v[124:127], v[36:39], a[128:131], v[124:127]\n"
    "v_mfma_f32_16x16x32_bf16 v[120:123], v[40:43], a[132:135], v[120:123]\n"
    "v_cvt_pk_bf16_f32 v136, v112, v113\n"
    "v_mfma_f32_16x16x32_bf16 v[124:127], v[44:47], a[132:135], v[124:127]\n"
    "v_cvt_pk_bf16_f32 v137, v114, v115\n"
    "v_mfma_f32_16x16x32_bf16 v[120:123], v[48:51], a[136:139], v[120:123]\n"
    "v_cvt_pk_bf16_f32 v138, v116, v117\n"
    "v_mfma_f32_16x16x32_bf16 v[124:127], v[52:55], a[136:139], v[124:127]\n"
    "v_cvt_pk_bf16_f32 v139, v118, v119\n"
    "v_mfma_f32_16x16x32_bf16 v[120:123], v[56:59], a[140:143], v[120:123]\n"
    "buffer_store_dwordx4 v[136:139], v1, s[40:43], s53 offen sc1\n"
    "v_mfma_f32_16x16x32_bf16 v[124:127], v[60:63], a[140:143], v[124:127]\n"
    "s_add_u32 s53, s53, 32768\n"
    "v_mfma_f32_16x16x32_bf16 v[112:115], v[16:19], a[144:147], 0\n"
    "v_mfma_f32_16x16x32_bf16 v[116:119], v[20:23], a[144:147], 0\n"
    "v_mfma_f32_16x16x32_bf16 v[112:115], v[24:27], a[148:151], v[112:115]\n"
    "v_mfma_f32_16x16x32_bf16 v[116:119], v[28:31], a[148:151], v[116:119]\n"
    "v_mfma_f32_16x16x32_bf16 v[112:115], v[32:35], a[152:155], v[112:115]\n"
    "v_mfma_f32_16x16x32_bf16 v[116:119], v[36:39], a[152:155], v[116:119]\n"
    "v_mfma_f32_16x16x32_bf16 v[112:115], v[40:43], a[156:159], v[112:115]\n"
    "v_cvt_pk_bf16_f32 v140, v120, v121\n"
    "v_mfma_f32_16x16x32_bf16 v[116:119], v[44:47], a[156:159], v[116:119]\n"
    "v_cvt_pk_bf16_f32 v141, v122, v123\n"
    "v_mfma_f32_16x16x32_bf16 v[112:115], v[48:51], a[160:163], v[112:115]\n"
    "v_cvt_pk_bf16_f32 v142, v124, v125\n"
    "v_mfma_f32_16x16x32_bf16 v[116:119], v[52:55], a[160:163], v[116:119]\n"
    "v_cvt_pk_bf16_f32 v143, v126, v127\n"
    "v_mfma_f32_16x16x32_bf16 v[112:115], v[56:59], a[164:167], v[112:115]\n"
    "buffer_store_dwordx4 v[140:143], v1, s[40:43], s53 offen sc1\n"
    "v_mfma_f32_16x16x32_bf16 v[116:119], v[60:63], a[164:167], v[116:119]\n"
    "s_add_u32 s53, s53, 32768\n"
    "s_waitcnt vmcnt(6)\n"
    "s_cmp_eq_u32 s56, s50\n"
    "s_cbranch_scc1 po_have0_%=\n"
    "s_mov_b32 s60, 0\n"
    "v_cmp_gt_u32 vcc, s55, v6\n"
    "s_nop 4\n"
    "s_cbranch_vccz po_v10_ok_%=\n"
    "po_v10_poll_%=:\n"
    "buffer_load_dword v6, v7, s[40:43], s58 offen sc1\n"
    "s_waitcnt vmcnt(0)\n"
    "v_cmp_gt_u32 vcc, s55, v6\n"
    "s_nop 4\n"
    "s_cbranch_vccz po_v10_ok_%=\n"
    "s_sleep 2\n"
    "s_add_u32 s60, s60, 1\n"
    "s_cmp_gt_u32 s60, 65536\n"
    "s_cbranch_scc1 po_abort_%=\n"
    "s_and_b32 s59, s60, 63\n"
    "s_cbranch_scc1 po_v10_poll_%=\n"
    "v_mov_b32 v4, 1024\n"
    "buffer_load_dword v4, v4, s[40:43], s58 offen sc1\n"
    "s_waitcnt vmcnt(0)\n"
    "v_readfirstlane_b32 s59, v4\n"
    "s_nop 0\n"
    "s_cmp_lg_u32 s59, 0\n"
    "s_nop 2\n"
    "s_cbranch_scc1 po_end_%=\n"
    "s_branch po_v10_poll_%=\n"
    "po_v10_ok_%=:\n"
    "buffer_load_dwordx4 v[144:147], v0, s[40:43], s50 offen offset:0 sc1\n"
    "buffer_load_dwordx4 v[148:151], v0, s[40:43], s50 offen offset:1024 sc1\n"
    "buffer_load_dwordx4 v[152:155], v0, s[40:43], s50 offen offset:2048 sc1\n"
    "s_waitcnt vmcnt(0)\n"
    "po_have0_%=:\n"
    "v_mfma_f32_16x16x32_bf16 v[120:123], v[16:19], a[168:171], 0\n"
    "ds_write2st64_b64 v8, v[144:145], v[146:147] offset0:24 offset1:26\n"
    "v_mfma_f32_16x16x32_bf16 v[124:127], v[20:23], a[168:171], 0\n"
    "ds_write2st64_b64 v9, v[148:149], v[150:151] offset0:24 offset1:26\n"
    "v_mfma_f32_16x16x32_bf16 v[120:123], v[24:27], a[172:175], v[120:123]\n"
    "ds_write2st64_b64 v10, v[152:153], v[154:155] offset0:24 offset1:26\n"
    "v_mfma_f32_16x16x32_bf16 v[124:127], v[28:31], a[172:175], v[124:127]\n"
    "v_mfma_f32_16x16x32_bf16 v[120:123], v[32:35], a[176:179], v[120:123]\n"
    "v_mfma_f32_16x16x32_bf16 v[124:127], v[36:39], a[176:179], v[124:127]\n"
    "v_mfma_f32_16x16x32_bf16 v[120:123], v[40:43], a[180:183], v[120:123]\n"
    "v_cvt_pk_bf16_f32 v136, v112, v113\n"
    "v_mfma_f32_16x16x32_bf16 v[124:127], v[44:47], a[180:183], v[124:127]\n"
    "v_cvt_pk_bf16_f32 v137, v114, v115\n"
    "v_mfma_f32_16x16x32_bf16 v[120:123], v[48:51], a[184:187], v[120:123]\n"
    "v_cvt_pk_bf16_f32 v138, v116, v117\n"
    "v_mfma_f32_16x16x32_bf16 v[124:127], v[52:55], a[184:187], v[124:127]\n"
    "v_cvt_pk_bf16_f32 v139, v118, v119\n"
    "v_mfma_f32_16x16x32_bf16 v[120:123], v[56:59], a[188:191], v[120:123]\n"
    "buffer_store_dwordx4 v[136:139], v1, s[40:43], s53 offen sc1\n"
    "v_mfma_f32_16x16x32_bf16 v[124:127], v[60:63], a[188:191], v[124:127]\n"
    "s_add_u32 s53, s53, 32768\n"
    "s_waitcnt lgkmcnt(0)\n"
    "s_barrier\n"
    "s_max_u32 s64, s64, s47\n"
    "v_mov_b32 v4, s64\n"
    "buffer_store_dword v4, v2, s[40:43], s58 offen sc1\n"
    "v_mfma_f32_16x16x32_bf16 v[112:115], v[16:19], a[192:195], 0\n"
    "ds_read_b128 v[64:67], v0 offset:12288\n"
    "v_mfma_f32_16x16x32_bf16 v[116:119], v[20:23], a[192:195], 0\n"
    "ds_read_b128 v[68:71], v0 offset:13312\n"
    "v_mfma_f32_16x16x32_bf16 v[112:115], v[24:27], a[196:199], v[112:115]\n"
    "ds_read_b128 v[72:75], v0 offset:14336\n"
    "v_mfma_f32_16x16x32_bf16 v[116:119], v[28:31], a[196:199], v[116:119]\n"
    "ds_read_b128 v[76:79], v0 offset:15360\n"
    "v_mfma_f32_16x16x32_bf16 v[112:115], v[32:35], a[200:203], v[112:115]\n"
    "ds_read_b128 v[80:83], v0 offset:16384\n"
    "v_mfma_f32_16x16x32_bf16 v[116:119], v[36:39], a[200:203], v[116:119]\n"
    "ds_read_b128 v[84:87], v0 offset:17408\n"
    "v_mfma_f32_16x16x32_bf16 v[112:115], v[40:43], a[204:207], v[112:115]\n"
    "v_cvt_pk_bf16_f32 v140, v120, v121\n"
    "v_mfma_f32_16x16x32_bf16 v[116:119], v[44:47], a[204:207], v[116:119]\n"
    "v_cvt_pk_bf16_f32 v141, v122, v123\n"
    "v_mfma_f32_16x16x32_bf16 v[112:115], v[48:51], a[208:211], v[112:115]\n"
    "v_cvt_pk_bf16_f32 v142, v124, v125\n"
    "v_mfma_f32_16x16x32_bf16 v[116:119], v[52:55], a[208:211], v[116:119]\n"
    "v_cvt_pk_bf16_f32 v143, v126, v127\n"
    "v_mfma_f32_16x16x32_bf16 v[112:115], v[56:59], a[212:215], v[112:115]\n"
    "buffer_store_dwordx4 v[140:143], v1, s[40:43], s53 offen sc1\n"
    "v_mfma_f32_16x16x32_bf16 v[116:119], v[60:63], a[212:215], v[116:119]\n"
    "s_add_u32 s53, s53, 32768\n"
    "v_mfma_f32_16x16x32_bf16 v[120:123], v[16:19], a[216:219], 0\n"
    "ds_read_b128 v[88:91], v0 offset:18432\n"
    "v_mfma_f32_16x16x32_bf16 v[124:127], v[20:23], a[216:219], 0\n"
    "ds_read_b128 v[92:95], v0 offset:19456\n"
    "v_mfma_f32_16x16x32_bf16 v[120:123], v[24:27], a[220:223], v[120:123]\n"
    "ds_read_b128 v[96:99], v0 offset:20480\n"
    "v_mfma_f32_16x16x32_bf16 v[124:127], v[28:31], a[220:223], v[124:127]\n"
    "ds_read_b128 v[100:103], v0 offset:21504\n"
    "v_mfma_f32_16x16x32_bf16 v[120:123], v[32:35], a[224:227], v[120:123]\n"
    "ds_read_b128 v[104:107], v0 offset:22528\n"
    "v_mfma_f32_16x16x32_bf16 v[124:127], v[36:39], a[224:227], v[124:127]\n"
    "ds_read_b128 v[108:111], v0 offset:23552\n"
    "v_mfma_f32_16x16x32_bf16 v[120:123], v[40:43], a[228:231], v[120:123]\n"
    "v_cvt_pk_bf16_f32 v136, v112, v113\n"
    "v_mfma_f32_16x16x32_bf16 v[124:127], v[44:47], a[228:231], v[124:127]\n"
    "v_cvt_pk_bf16_f32 v137, v114, v115\n"
    "v_mfma_f32_16x16x32_bf16 v[120:123], v[48:51], a[232:235], v[120:123]\n"
    "v_cvt_pk_bf16_f32 v138, v116, v117\n"
    "v_mfma_f32_16x16x32_bf16 v[124:127], v[52:55], a[232:235], v[124:127]\n"
    "v_cvt_pk_bf16_f32 v139, v118, v119\n"
    "v_mfma_f32_16x16x32_bf16 v[120:123], v[56:59], a[236:239], v[120:123]\n"
    "buffer_store_dwordx4 v[136:139], v1, s[40:43], s53 offen sc1\n"
    "v_mfma_f32_16x16x32_bf16 v[124:127], v[60:63], a[236:239], v[124:127]\n"
    "s_add_u32 s53, s53, 32768\n"
    "s_waitcnt lgkmcnt(0)\n"
    "s_add_u32 s63, s63, 1310720\n"
    "s_sub_u32 s59, s47, 2\n"
    "s_cmp_lt_u32 s59, 61\n"
    "s_cbranch_scc1 po_late0_%=\n"
    "v_cvt_pk_bf16_f32 v140, v120, v121\n"
    "v_cvt_pk_bf16_f32 v141, v122, v123\n"
    "v_cvt_pk_bf16_f32 v142, v124, v125\n"
    "v_cvt_pk_bf16_f32 v143, v126, v127\n"
    "buffer_store_dwordx4 v[140:143], v1, s[40:43], s53 offen sc1\n"
    "s_add_u32 s64, s47, 1\n"
    "v_mov_b32 v4, s64\n"
    "s_waitcnt vmcnt(0)\n"
    "s_barrier\n"
    "buffer_store_dword v4, v2, s[40:43], s58 offen sc1\n"
    "s_cmp_eq_u32 s47, 63\n"
    "s_nop 0\n"
    "s_cbranch_scc1 po_end_%=\n"
    "po_late0_%=:\n"
    "s_add_u32 s47, s47, 1\n"
    "s_add_u32 s55, s47, 2\n"
    "s_min_u32 s55, s55, 64\n"
    "s_add_u32 s50, s50, 393216\n"
    "v_cmp_gt_u32 vcc, s55, v6\n"
    "s_nop 0\n"
    "s_cmp_eq_u64 vcc, 0\n"
    "s_cselect_b32 s56, s50, 0x40000000\n"
    "v_mfma_f32_16x16x32_bf16 v[112:115], v[64:67], a[0:3], 0\n"
    "buffer_load_dwordx4 v[144:147], v0, s[40:43], s56 offen offset:0 sc1\n"
    "v_mfma_f32_16x16x32_bf16 v[116:119], v[68:71], a[0:3], 0\n"
    "buffer_load_dwordx4 v[148:151], v0, s[40:43], s56 offen offset:1024 sc1\n"
    "v_mfma_f32_16x16x32_bf16 v[112:115], v[72:75], a[4:7], v[112:115]\n"
    "buffer_load_dwordx4 v[152:155], v0, s[40:43], s56 offen offset:2048 sc1\n"
    "v_mfma_f32_16x16x32_bf16 v[116:119], v[76:79], a[4:7], v[116:119]\n"
    "buffer_load_dword v6, v7, s[40:43], s58 offen sc1\n"
    "v_mfma_f32_16x16x32_bf16 v[112:115], v[80:83], a[8:11], v[112:115]\n"
    "v_mfma_f32_16x16x32_bf16 v[116:119], v[84:87], a[8:11], v[116:119]\n"
    "v_mfma_f32_16x16x32_bf16 v[112:115], v[88:91], a[12:15], v[112:115]\n"
    "v_cvt_pk_bf16_f32 v140, v120, v121\n"
    "v_mfma_f32_16x16x32_bf16 v[116:119], v[92:95], a[12:15], v[116:119]\n"
    "v_cvt_pk_bf16_f32 v141, v122, v123\n"
    "v_mfma_f32_16x16x32_bf16 v[112:115], v[96:99], a[16:19], v[112:115]\n"
    "v_cvt_pk_bf16_f32 v142, v124, v125\n"
    "v_mfma_f32_16x16x32_bf16 v[116:119], v[100:103], a[16:19], v[116:119]\n"
    "v_cvt_pk_bf16_f32 v143, v126, v127\n"
    "v_mfma_f32_16x16x32_bf16 v[112:115], v[104:107], a[20:23], v[112:115]\n"
    "buffer_store_dwordx4 v[140:143], v1, s[40:43], s53 offen sc1\n"
    "v_mfma_f32_16x16x32_bf16 v[116:119], v[108:111], a[20:23], v[116:119]\n"
    "s_mov_b32 s53, s63\n"
    "v_mfma_f32_16x16x32_bf16 v[120:123], v[64:67], a[24:27], 0\n"
    "v_mfma_f32_16x16x32_bf16 v[124:127], v[68:71], a[24:27], 0\n"
    "v_mfma_f32_16x16x32_bf16 v[120:123], v[72:75], a[28:31], v[120:123]\n"
    "v_mfma_f32_16x16x32_bf16 v[124:127], v[76:79], a[28:31], v[124:127]\n"
    "v_mfma_f32_16x16x32_bf16 v[120:123], v[80:83], a[32:35], v[120:123]\n"
    "v_mfma_f32_16x16x32_bf16 v[124:127], v[84:87], a[32:35], v[124:127]\n"
    "v_mfma_f32_16x16x32_bf16 v[120:123], v[88:91], a[36:39], v[120:123]\n"
    "v_cvt_pk_bf16_f32 v136, v112, v113\n"
    "v_mfma_f32_16x16x32_bf16 v[124:127], v[92:95], a[36:39], v[124:127]\n"
    "v_cvt_pk_bf16_f32 v137, v114, v115\n"
    "v_mfma_f32_16x16x32_bf16 v[120:123], v[96:99], a[40:43], v[120:123]\n"
    "v_cvt_pk_bf16_f32 v138, v116, v117\n"
    "v_mfma_f32_16x16x32_bf16 v[124:127], v[100:103], a[40:43], v[124:127]\n"
    "v_cvt_pk_bf16_f32 v139, v118, v119\n"
    "v_mfma_f32_16x16x32_bf16 v[120:123], v[104:107], a[44:47], v[120:123]\n"
    "buffer_store_dwordx4 v[136:139], v1, s[40:43], s53 offen sc1\n"
    "v_mfma_f32_16x16x32_bf16 v[124:127], v[108:111], a[44:47], v[124:127]\n"
    "s_add_u32 s53, s53, 32768\n"
    "v_mfma_f32_16x16x32_bf16 v[112:115], v[64:67], a[48:51], 0\n"
    "v_mfma_f32_16x16x32_bf16 v[116:119], v[68:71], a[48:51], 0\n"
    "v_mfma_f32_16x16x32_bf16 v[112:115], v[72:75], a[52:55], v[112:115]\n"
    "v_mfma_f32_16x16x32_bf16 v[116:119], v[76:79], a[52:55], v[116:119]\n"
    "v_mfma_f32_16x16x32_bf16 v[112:115], v[80:83], a[56:59], v[112:115]\n"
    "v_mfma_f32_16x16x32_bf16 v[116:119], v[84:87], a[56:59], v[116:119]\n"
    "v_mfma_f32_16x16x32_bf16 v[112:115], v[88:91], a[60:63], v[112:115]\n"
    "v_cvt_pk_bf16_f32 v140, v120, v121\n"
    "v_mfma_f32_16x16x32_bf16 v[116:119], v[92:95], a[60:63], v[116:119]\n"
    "v_cvt_pk_bf16_f32 v141, v122, v123\n"
    "v_mfma_f32_16x16x32_bf16 v[112:115], v[96:99], a[64:67], v[112:115]\n"
    "v_cvt_pk_bf16_f32 v142, v124, v125\n"
    "v_mfma_f32_16x16x32_bf16 v[116:119], v[100:103], a[64:67], v[116:119]\n"
    "v_cvt_pk_bf16_f32 v143, v126, v127\n"
    "v_mfma_f32_16x16x32_bf16 v[112:115], v[104:107], a[68:71], v[112:115]\n"
    "buffer_store_dwordx4 v[140:143], v1, s[40:43], s53 offen sc1\n"
    "v_mfma_f32_16x16x32_bf16 v[116:119], v[108:111], a[68:71], v[116:119]\n"
    "s_add_u32 s53, s53, 32768\n"
    "v_mfma_f32_16x16x32_bf16 v[120:123], v[64:67], a[72:75], 0\n"
    "v_mfma_f32_16x16x32_bf16 v[124:127], v[68:71], a[72:75], 0\n"
    "v_mfma_f32_16x16x32_bf16 v[120:123], v[72:75], a[76:79], v[120:123]\n"
    "v_mfma_f32_16x16x32_bf16 v[124:127], v[76:79], a[76:79], v[124:127]\n"
    "v_mfma_f32_16x16x32_bf16 v[120:123], v[80:83], a[80:83], v[120:123]\n"
    "v_mfma_f32_16x16x32_bf16 v[124:127], v[84:87], a[80:83], v[124:127]\n"
    "v_mfma_f32_16x16x32_bf16 v[120:123], v[88:91], a[84:87], v[120:123]\n"
    "v_cvt_pk_bf16_f32 v136, v112, v113\n"
    "v_mfma_f32_16x16x32_bf16 v[124:127], v[92:95], a[84:87], v[124:127]\n"
    "v_cvt_pk_bf16_f32 v137, v114, v115\n"
    "v_mfma_f32_16x16x32_bf16 v[120:123], v[96:99], a[88:91], v[120:123]\n"
    "v_cvt_pk_bf16_f32 v138, v116, v117\n"
    "v_mfma_f32_16x16x32_bf16 v[124:127], v[100:103], a[88:91], v[124:127]\n"
    "v_cvt_pk_bf16_f32 v139, v118, v119\n"
    "v_mfma_f32_16x16x32_bf16 v[120:123], v[104:107], a[92:95], v[120:123]\n"
    "buffer_store_dwordx4 v[136:139], v1, s[40:43], s53 offen sc1\n"
    "v_mfma_f32_16x16x32_bf16 v[124:127], v[108:111], a[92:95], v[124:127]\n"
    "s_add_u32 s53, s53, 32768\n"
    "v_mfma_f32_16x16x32_bf16 v[112:115], v[64:67], a[96:99], 0\n"
    "v_mfma_f32_16x16x32_bf16 v[116:119], v[68:71], a[96:99], 0\n"
    "v_mfma_f32_16x16x32_bf16 v[112:115], v[72:75], a[100:103], v[112:115]\n"
    "v_mfma_f32_16x16x32_bf16 v[116:119], v[76:79], a[100:103], v[116:119]\n"
    "v_mfma_f32_16x16x32_bf16 v[112:115], v[80:83], a[104:107], v[112:115]\n"
    "v_mfma_f32_16x16x32_bf16 v[116:119], v[84:87], a[104:107], v[116:119]\n"
    "v_mfma_f32_16x16x32_bf16 v[112:115], v[88:91], a[108:111], v[112:115]\n"
    "v_cvt_pk_bf16_f32 v140, v120, v121\n"
    "v_mfma_f32_16x16x32_bf16 v[116:119], v[92:95], a[108:111], v[116:119]\n"
    "v_cvt_pk_bf16_f32 v141, v122, v123\n"
    "v_mfma_f32_16x16x32_bf16 v[112:115], v[96:99], a[112:115], v[112:115]\n"
    "v_cvt_pk_bf16_f32 v142, v124, v125\n"
    "v_mfma_f32_16x16x32_bf16 v[116:119], v[100:103], a[112:115], v[116:119]\n"
    "v_cvt_pk_bf16_f32 v143, v126, v127\n"
    "v_mfma_f32_16x16x32_bf16 v[112:115], v[104:107], a[116:119], v[112:115]\n"
    "buffer_store_dwordx4 v[140:143], v1, s[40:43], s53 offen sc1\n"
    "v_mfma_f32_16x16x32_bf16 v[116:119], v[108:111], a[116:119], v[116:119]\n"
    "s_add_u32 s53, s53, 32768\n"
    "v_mfma_f32_16x16x32_bf16 v[120:123], v[64:67], a[120:123], 0\n"
    "v_mfma_f32_16x16x32_bf16 v[124:127], v[68:71], a[120:123], 0\n"
    "v_mfma_f32_16x16x32_bf16 v[120:123], v[72:75], a[124:127], v[120:123]\n"
    "v_mfma_f32_16x16x32_bf16 v[124:127], v[76:79], a[124:127], v[124:127]\n"
    "v_mfma_f32_16x16x32_bf16 v[120:123], v[80:83], a[128:131], v[120:123]\n"
    "v_mfma_f32_16x16x32_bf16 v[124:127], v[84:87], a[128:131], v[124:127]\n"
    "v_mfma_f32_16x16x32_bf16 v[120:123], v[88:91], a[132:135], v[120:123]\n"
    "v_cvt_pk_bf16_f32 v136, v112, v113\n"
    "v_mfma_f32_16x16x32_bf16 v[124:127], v[92:95], a[132:135], v[124:127]\n"
    "v_cvt_pk_bf16_f32 v137, v114, v115\n"
    "v_mfma_f32_16x16x32_bf16 v[120:123], v[96:99], a[136:139], v[120:123]\n"
    "v_cvt_pk_bf16_f32 v138, v116, v117\n"
    "v_mfma_f32_16x16x32_bf16 v[124:127], v[100:103], a[136:139], v[124:127]\n"
    "v_cvt_pk_bf16_f32 v139, v118, v119\n"
    "v_mfma_f32_16x16x32_bf16 v[120:123], v[104:107], a[140:143], v[120:123]\n"
    "buffer_store_dwordx4 v[136:139], v1, s[40:43], s53 offen sc1\n"
    "v_mfma_f32_16x16x32_bf16 v[124:127], v[108:111], a[140:143], v[124:127]\n"
    "s_add_u32 s53, s53, 32768\n"
    "v_mfma_f32_16x16x32_bf16 v[112:115], v[64:67], a[144:147], 0\n"
    "v_mfma_f32_16x16x32_bf16 v[116:119], v[68:71], a[144:147], 0\n"
    "v_mfma_f32_16x16x32_bf16 v[112:115], v[72:75], a[148:151], v[112:115]\n"
    "v_mfma_f32_16x16x32_bf16 v[116:119], v[76:79], a[148:151], v[116:119]\n"
    "v_mfma_f32_16x16x32_bf16 v[112:115], v[80:83], a[152:155], v[112:115]\n"
    "v_mfma_f32_16x16x32_bf16 v[116:119], v[84:87], a[152:155], v[116:119]\n"
    "v_mfma_f32_16x16x32_bf16 v[112:115], v[88:91], a[156:159], v[112:115]\n"
    "v_cvt_pk_bf16_f32 v140, v120, v121\n"
    "v_mfma_f32_16x16x32_bf16 v[116:119], v[92:95], a[156:159], v[116:119]\n"
    "v_cvt_pk_bf16_f32 v141, v122, v123\n"
    "v_mfma_f32_16x16x32_bf16 v[112:115], v[96:99], a[160:163], v[112:115]\n"
    "v_cvt_pk_bf16_f32 v142, v124, v125\n"
    "v_mfma_f32_16x16x32_bf16 v[116:119], v[100:103], a[160:163], v[116:119]\n"
    "v_cvt_pk_bf16_f32 v143, v126, v127\n"
    "v_mfma_f32_16x16x32_bf16 v[112:115], v[104:107], a[164:167], v[112:115]\n"
    "buffer_store_dwordx4 v[140:143], v1, s[40:43], s53 offen sc1\n"
    "v_mfma_f32_16x16x32_bf16 v[116:119], v[108:111], a[164:167], v[116:119]\n"
    "s_add_u32 s53, s53, 32768\n"
    "s_waitcnt vmcnt(6)\n"
    "s_cmp_eq_u32 s56, s50\n"
    "s_cbranch_scc1 po_have1_%=\n"
    "s_mov_b32 s60, 0\n"
    "v_cmp_gt_u32 vcc, s55, v6\n"
    "s_nop 4\n"
    "s_cbranch_vccz po_v11_ok_%=\n"
    "po_v11_poll_%=:\n"
    "buffer_load_dword v6, v7, s[40:43], s58 offen sc1\n"
    "s_waitcnt vmcnt(0)\n"
    "v_cmp_gt_u32 vcc, s55, v6\n"
    "s_nop 4\n"
    "s_cbranch_vccz po_v11_ok_%=\n"
    "s_sleep 2\n"
    "s_add_u32 s60, s60, 1\n"
    "s_cmp_gt_u32 s60, 65536\n"
    "s_cbranch_scc1 po_abort_%=\n"
    "s_and_b32 s59, s60, 63\n"
    "s_cbranch_scc1 po_v11_poll_%=\n"
    "v_mov_b32 v4, 1024\n"
    "buffer_load_dword v4, v4, s[40:43], s58 offen sc1\n"
    "s_waitcnt vmcnt(0)\n"
    "v_readfirstlane_b32 s59, v4\n"
    "s_nop 0\n"
    "s_cmp_lg_u32 s59, 0\n"
    "s_nop 2\n"
    "s_cbranch_scc1 po_end_%=\n"
    "s_branch po_v11_poll_%=\n"
    "po_v11_ok_%=:\n"
    "buffer_load_dwordx4 v[144:147], v0, s[40:43], s50 offen offset:0 sc1\n"
    "buffer_load_dwordx4 v[148:151], v0, s[40:43], s50 offen offset:1024 sc1\n"
    "buffer_load_dwordx4 v[152:155], v0, s[40:43], s50 offen offset:2048 sc1\n"
    "s_waitcnt vmcnt(0)\n"
    "po_have1_%=:\n"
    "v_mfma_f32_16x16x32_bf16 v[120:123], v[64:67], a[168:171], 0\n"
    "ds_write2st64_b64 v8, v[144:145], v[146:147] offset0:0 offset1:2\n"
    "v_mfma_f32_16x16x32_bf16 v[124:127], v[68:71], a[168:171], 0\n"
    "ds_write2st64_b64 v9, v[148:149], v[150:151] offset0:0 offset1:2\n"
    "v_mfma_f32_16x16x32_bf16 v[120:123], v[72:75], a[172:175], v[120:123]\n"
    "ds_write2st64_b64 v10, v[152:153], v[154:155] offset0:0 offset1:2\n"
    "v_mfma_f32_16x16x32_bf16 v[124:127], v[76:79], a[172:175], v[124:127]\n"
    "v_mfma_f32_16x16x32_bf16 v[120:123], v[80:83], a[176:179], v[120:123]\n"
    "v_mfma_f32_16x16x32_bf16 v[124:127], v[84:87], a[176:179], v[124:127]\n"
    "v_mfma_f32_16x16x32_bf16 v[120:123], v[88:91], a[180:183], v[120:123]\n"
    "v_cvt_pk_bf16_f32 v136, v112, v113\n"
    "v_mfma_f32_16x16x32_bf16 v[124:127], v[92:95], a[180:183], v[124:127]\n"
    "v_cvt_pk_bf16_f32 v137, v114, v115\n"
    "v_mfma_f32_16x16x32_bf16 v[120:123], v[96:99], a[184:187], v[120:123]\n"
    "v_cvt_pk_bf16_f32 v138, v116, v117\n"
    "v_mfma_f32_16x16x32_bf16 v[124:127], v[100:103], a[184:187], v[124:127]\n"
    "v_cvt_pk_bf16_f32 v139, v118, v119\n"
    "v_mfma_f32_16x16x32_bf16 v[120:123], v[104:107], a[188:191], v[120:123]\n"
    "buffer_store_dwordx4 v[136:139], v1, s[40:43], s53 offen sc1\n"
    "v_mfma_f32_16x16x32_bf16 v[124:127], v[108:111], a[188:191], v[124:127]\n"
    "s_add_u32 s53, s53, 32768\n"
    "s_waitcnt lgkmcnt(0)\n"
    "s_barrier\n"
    "s_max_u32 s64, s64, s47\n"
    "v_mov_b32 v4, s64\n"
    "buffer_store_dword v4, v2, s[40:43], s58 offen sc1\n"
    "v_mfma_f32_16x16x32_bf16 v[112:115], v[64:67], a[192:195], 0\n"
    "ds_read_b128 v[16:19], v0 offset:0\n"
    "v_mfma_f32_16x16x32_bf16 v[116:119], v[68:71], a[192:195], 0\n"
    "ds_read_b128 v[20:23], v0 offset:1024\n"
    "v_mfma_f32_16x16x32_bf16 v[112:115], v[72:75], a[196:199], v[112:115]\n"
    "ds_read_b128 v[24:27], v0 offset:2048\n"
    "v_mfma_f32_16x16x32_bf16 v[116:119], v[76:79], a[196:199], v[116:119]\n"
    "ds_read_b128 v[28:31], v0 offset:3072\n"
    "v_mfma_f32_16x16x32_bf16 v[112:115], v[80:83], a[200:203], v[112:115]\n"
    "ds_read_b128 v[32:35], v0 offset:4096\n"
    "v_mfma_f32_16x16x32_bf16 v[116:119], v[84:87], a[200:203], v[116:119]\n"
    "ds_read_b128 v[36:39], v0 offset:5120\n"
    "v_mfma_f32_16x16x32_bf16 v[112:115], v[88:91], a[204:207], v[112:115]\n"
    "v_cvt_pk_bf16_f32 v140, v120, v121\n"
    "v_mfma_f32_16x16x32_bf16 v[116:119], v[92:95], a[204:207], v[116:119]\n"
    "v_cvt_pk_bf16_f32 v141, v122, v123\n"
    "v_mfma_f32_16x16x32_bf16 v[112:115], v[96:99], a[208:211], v[112:115]\n"
    "v_cvt_pk_bf16_f32 v142, v124, v125\n"
    "v_mfma_f32_16x16x32_bf16 v[116:119], v[100:103], a[208:211], v[116:119]\n"
    "v_cvt_pk_bf16_f32 v143, v126, v127\n"
    "v_mfma_f32_16x16x32_bf16 v[112:115], v[104:107], a[212:215], v[112:115]\n"
    "buffer_store_dwordx4 v[140:143], v1, s[40:43], s53 offen sc1\n"
    "v_mfma_f32_16x16x32_bf16 v[116:119], v[108:111], a[212:215], v[116:119]\n"
    "s_add_u32 s53, s53, 32768\n"
    "v_mfma_f32_16x16x32_bf16 v[120:123], v[64:67], a[216:219], 0\n"
    "ds_read_b128 v[40:43], v0 offset:6144\n"
    "v_mfma_f32_16x16x32_bf16 v[124:127], v[68:71], a[216:219], 0\n"
    "ds_read_b128 v[44:47], v0 offset:7168\n"
    "v_mfma_f32_16x16x32_bf16 v[120:123], v[72:75], a[220:223], v[120:123]\n"
    "ds_read_b128 v[48:51], v0 offset:8192\n"
    "v_mfma_f32_16x16x32_bf16 v[124:127], v[76:79], a[220:223], v[124:127]\n"
    "ds_read_b128 v[52:55], v0 offset:9216\n"
    "v_mfma_f32_16x16x32_bf16 v[120:123], v[80:83], a[224:227], v[120:123]\n"
    "ds_read_b128 v[56:59], v0 offset:10240\n"
    "v_mfma_f32_16x16x32_bf16 v[124:127], v[84:87], a[224:227], v[124:127]\n"
    "ds_read_b128 v[60:63], v0 offset:11264\n"
    "v_mfma_f32_16x16x32_bf16 v[120:123], v[88:91], a[228:231], v[120:123]\n"
    "v_cvt_pk_bf16_f32 v136, v112, v113\n"
    "v_mfma_f32_16x16x32_bf16 v[124:127], v[92:95], a[228:231], v[124:127]\n"
    "v_cvt_pk_bf16_f32 v137, v114, v115\n"
    "v_mfma_f32_16x16x32_bf16 v[120:123], v[96:99], a[232:235], v[120:123]\n"
    "v_cvt_pk_bf16_f32 v138, v116, v117\n"
    "v_mfma_f32_16x16x32_bf16 v[124:127], v[100:103], a[232:235], v[124:127]\n"
    "v_cvt_pk_bf16_f32 v139, v118, v119\n"
    "v_mfma_f32_16x16x32_bf16 v[120:123], v[104:107], a[236:239], v[120:123]\n"
    "buffer_store_dwordx4 v[136:139], v1, s[40:43], s53 offen sc1\n"
    "v_mfma_f32_16x16x32_bf16 v[124:127], v[108:111], a[236:239], v[124:127]\n"
    "s_add_u32 s53, s53, 32768\n"
    "s_waitcnt lgkmcnt(0)\n"
    "s_add_u32 s63, s63, 1310720\n"
    "s_sub_u32 s59, s47, 2\n"
    "s_cmp_lt_u32 s59, 61\n"
    "s_cbranch_scc1 po_late1_%=\n"
    "v_cvt_pk_bf16_f32 v140, v120, v121\n"
    "v_cvt_pk_bf16_f32 v141, v122, v123\n"
    "v_cvt_pk_bf16_f32 v142, v124, v125\n"
    "v_cvt_pk_bf16_f32 v143, v126, v127\n"
    "buffer_store_dwordx4 v[140:143], v1, s[40:43], s53 offen sc1\n"
    "s_add_u32 s64, s47, 1\n"
    "v_mov_b32 v4, s64\n"
    "s_waitcnt vmcnt(0)\n"
    "s_barrier\n"
    "buffer_store_dword v4, v2, s[40:43], s58 offen sc1\n"
    "s_cmp_eq_u32 s47, 63\n"
    "s_nop 0\n"
    "s_cbranch_scc1 po_end_%=\n"
    "po_late1_%=:\n"
    "s_add_u32 s47, s47, 1\n"
    "s_branch po_loop_%=\n"
    "s_branch po_end_%=\n"
    "po_abort_%=:\n"
    "v_mov_b32 v4, 1\n"
    "v_mov_b32 v5, 1024\n"
    "buffer_store_dword v4, v5, s[40:43], s58 offen sc1\n"
    "po_end_%=:\n"
    "s_endpgm\n"
    :: ROLE_INPUTS : ROLE_CLOBBERS);
}

__global__ void __launch_bounds__(256, 1) layer_a(char* ws, Tune tn) {
    extern __shared__ __attribute__((aligned(16))) char smem[];
    LAS char* lds = (LAS char*)smem;
    rsrc_t r = __builtin_amdgcn_make_buffer_rsrc(ws, 0, WS_BYTES, 0x00020000);
    const int tid = threadIdx.x, lane = tid & 63, w = __builtin_amdgcn_readfirstlane(tid >> 6);
    const unsigned xcc = xcc_id();
    if (tid == 0) {
        const unsigned nearg = ld4(r, 0, OFF_FLAGS + FL_NEAR) & 1u;
        const unsigned pos = ((((xcc >> 2) ^ nearg) & 1u) << 2) | (xcc & 3u);
        unsigned role = pos * 32 + ((blockIdx.x >> 3) & 31);
        unsigned* claim = (unsigned*)(ws + OFF_FLAGS + FL_CLAIM);
        for (int n = 0; n < 256; ++n, role = (role + 1) & 255)
            if (__hip_atomic_exchange(claim + role, 1u, __ATOMIC_RELAXED, __HIP_MEMORY_SCOPE_AGENT) == 0u) break;
        *(LAS unsigned*)lds = role;
    }
    __syncthreads();
    const int role = __builtin_amdgcn_readfirstlane(*(LAS unsigned*)lds);
    __syncthreads();
    const int pos = role >> 5, idx = role & 31;
    const unsigned lo = (unsigned)(uintptr_t)ws, hi = (unsigned)((uintptr_t)ws >> 32) & 0xffffu;
    if (pos < 4) {
        if (idx < 8) role_head(lo, hi, pos * 8 + idx, w, xcc & 3, tn);
        else if (idx < 28) role_col(lo, hi, pos * 20 + idx - 8, w, xcc & 3, tn);
    } else {
        const int h = (pos - 4) * 8 + (idx & 7), kind = idx >> 3;
        if (kind == 0) role_projq(lo, hi, h, w, 0, tn);
        else if (kind < 3) role_projkv(lo, hi, h, w, kind, tn);
        else role_out(lo, hi, h, w, 0, tn);
    }
}

__global__ void __launch_bounds__(256) tok_out(const char* __restrict__ ws, const float* __restrict__ xin, float* __restrict__ xout) {
    __shared__ float red[4][32][84];
    const int s = blockIdx.x >> 3, cg = blockIdx.x & 7, tid = threadIdx.x, w = tid >> 6, lane = tid & 63, g = lane >> 4, m = lane & 15;
    f4 acc[5][2];
    for (int c = 0; c < 5; ++c) { acc[c][0] = (f4){0, 0, 0, 0}; acc[c][1] = acc[c][0]; }
#pragma unroll 2
    for (int hh = 0; hh < 10; ++hh) {
        u4 p[2], u[5];
        for (int tt = 0; tt < 2; ++tt)
            p[tt] = hh < 8 ? *(const u4*)(ws + OFF_PTX + (size_t)((s * 32 + 8 * w + hh) * 2 + tt) * 1024 + lane * 16) : *(const u4*)(ws + OFF_PTM + (size_t)((s * 2 + tt) * 8 + w * 2 + hh - 8) * 1024 + lane * 16);
        for (int c = 0; c < 5; ++c) {
            const int slice = 2 * (5 * cg + c) + (m >> 3);
            u[c] = hh < 8 ? *(const u4*)(ws + OFF_UX + (size_t)(s * 80 + slice) * 16384 + (4 * w + (hh >> 1)) * 1024 + (g * 16 + (hh & 1) * 8 + (m & 7)) * 16)
                          : *(const u4*)(ws + OFF_VO + (size_t)(s * 80 + slice) * 4096 + w * 1024 + (g * 16 + (hh - 8) * 8 + (m & 7)) * 16);
        }
        for (int c = 0; c < 5; ++c) for (int tt = 0; tt < 2; ++tt) acc[c][tt] = mfma(u[c], p[tt], acc[c][tt]);
    }
    for (int c = 0; c < 5; ++c) for (int tt = 0; tt < 2; ++tt) *(f4*)&red[w][16 * tt + m][16 * c + 4 * g] = acc[c][tt];
    __syncthreads();
    for (int e = tid; e < 32 * 20; e += 256) {
        const int tok = e / 20, c4 = (e % 20) * 4;
        const size_t o = (size_t)(s * 32 + tok) * 640 + cg * 80 + c4;
        *(f4*)(xout + o) = *(const f4*)(xin + o) + *(f4*)&red[0][tok][c4] + *(f4*)&red[1][tok][c4] + *(f4*)&red[2][tok][c4] + *(f4*)&red[3][tok][c4];
    }
}

__global__ void __launch_bounds__(256) b_qkv(const u16* __restrict__ xn, const u16* __restrict__ W, float* __restrict__ qkv) {
    __shared__ float xs[16][640];
    const int t0 = blockIdx.x * 16, n = blockIdx.y * 256 + threadIdx.x;
    for (int e = threadIdx.x; e < 16 * 640; e += 256) xs[e / 640][e % 640] = bf2f(xn[(size_t)t0 * 640 + e]);
    __syncthreads();
    if (n >= 1920) return;
    float acc[16];
    for (int q = 0; q < 16; ++q) acc[q] = 0.f;
    for (int d = 0; d < 640; ++d) { const float wv = bf2f(W[(size_t)d * 1920 + n]); for (int q = 0; q < 16; ++q) acc[q] += xs[q][d] * wv; }
    for (int q = 0; q < 16; ++q) qkv[(size_t)(t0 + q) * 1920 + n] = acc[q];
}
DEV void hedge_phi(const float* q, const u16* wp, float* phi) {
    float p[20], mx = -__builtin_inff(), mn = __builtin_inff();
    for (int f = 0; f < 20; ++f) { float a = 0.f; for (int d = 0; d < 20; ++d) a += q[d] * bf2f(wp[d * 20 + f]); p[f] = a; mx = fmaxf(mx, a); mn = fminf(mn, a); }
    float s0 = 0.f, s1 = 0.f;
    for (int f = 0; f < 20; ++f) { phi[f] = __expf(p[f] - mx); s0 += phi[f]; phi[20 + f] = __expf(mn - p[f]); s1 += phi[20 + f]; }
    for (int f = 0; f < 20; ++f) { phi[f] /= s0; phi[20 + f] /= s1; }
}
__global__ void __launch_bounds__(256) b_kv(const float* __restrict__ qkv, const u16* __restrict__ wphi, float* __restrict__ part) {
    __shared__ float ph[256][41], vv[256][21];
    const int h = blockIdx.x, ch = blockIdx.y, tok = ch * 256 + threadIdx.x;
    float q[20], phi[40];
    for (int d = 0; d < 20; ++d) { q[d] = qkv[(size_t)tok * 1920 + 640 + h * 20 + d]; vv[threadIdx.x][d] = qkv[(size_t)tok * 1920 + 1280 + h * 20 + d]; }
    vv[threadIdx.x][20] = 1.f;
    hedge_phi(q, wphi + h * 400, phi);
    for (int f = 0; f < 40; ++f) ph[threadIdx.x][f] = phi[f];
    __syncthreads();
    for (int e = threadIdx.x; e < 840; e += 256) { const int f = e / 21, d = e % 21; float a = 0.f; for (int t = 0; t < 256; ++t) a += ph[t][f] * vv[t][d]; part[(size_t)(ch * 32 + h) * 840 + e] = a; }
}
__global__ void __launch_bounds__(256) b_o(const float* __restrict__ qkv, const u16* __restrict__ wphi, const float* __restrict__ part, u16* __restrict__ o) {
    __shared__ float kv[840];
    const int h = blockIdx.x, tok = blockIdx.y * 256 + threadIdx.x;
    for (int e = threadIdx.x; e < 840; e += 256) { float a = 0.f; for (int c = 0; c < 8; ++c) a += part[(size_t)(c * 32 + h) * 840 + e]; kv[e] = a; }
    __syncthreads();
    float q[20], phi[40];
    for (int d = 0; d < 20; ++d) q[d] = qkv[(size_t)tok * 1920 + h * 20 + d];
    hedge_phi(q, wphi + h * 400, phi);
    float den = 1e-6f;
    for (int f = 0; f < 40; ++f) den += phi[f] * kv[f * 21 + 20];
    for (int d = 0; d < 20; ++d) { float a = 0.f; for (int f = 0; f < 40; ++f) a += phi[f] * kv[f * 21 + d]; o[(size_t)tok * 640 + h * 20 + d] = (u16)pk2(a / den, 0.f); }
}
__global__ void __launch_bounds__(256) b_wo(const u16* __restrict__ o, const u16* __restrict__ W, const float* __restrict__ xin, float* __restrict__ xout, u16* __restrict__ out16) {
    __shared__ float xs[16][640];
    const int t0 = blockIdx.x * 16, n = blockIdx.y * 256 + threadIdx.x;
    for (int e = threadIdx.x; e < 16 * 640; e += 256) xs[e / 640][e % 640] = bf2f(o[(size_t)t0 * 640 + e]);
    __syncthreads();
    if (n >= 640) return;
    float acc[16];
    for (int q = 0; q < 16; ++q) acc[q] = 0.f;
    for (int d = 0; d < 640; ++d) { const float wv = bf2f(W[(size_t)d * 640 + n]); for (int q = 0; q < 16; ++q) acc[q] += xs[q][d] * wv; }
    for (int q = 0; q < 16; ++q) { const size_t e = (size_t)(t0 + q) * 640 + n; const float y = xin[e] + acc[q]; if (out16) out16[e] = (u16)pk2(y, 0.f); else xout[e] = y; }
}

extern "C" void kernel_launch(void* const* d_in, const int* in_sizes, int n_in, void* d_out, int out_size, void* d_ws, size_t ws_size, hipStream_t stream) {
    const u16* x = (const u16*)d_in[0];
    const u16* mem0 = (const u16*)d_in[1];
    const u16 *Wq = (const u16*)d_in[2], *Wk = (const u16*)d_in[3], *Wv = (const u16*)d_in[4], *Wo = (const u16*)d_in[5];
    const u16 *wqkv = (const u16*)d_in[8], *wphi = (const u16*)d_in[9], *wo = (const u16*)d_in[10];
    char* ws = (char*)(((uintptr_t)d_ws + 0x1FFFFF) & ~(uintptr_t)0x1FFFFF);
    float *xa = (float*)(ws + OFF_XA), *xb = (float*)(ws + OFF_XB);
    float* qkv = (float*)(ws + OFF_TMP);
    u16* xnp = (u16*)(ws + OFF_KX);
    u16* ob = (u16*)(ws + OFF_VX);
    float* part = (float*)(ws + OFF_TMP + (16u << 20));
    (void)hipFuncSetAttribute((const void*)layer_a, hipFuncAttributeMaxDynamicSharedMemorySize, LDS_BYTES);
    Tune tn = {{52,124,15,66,18,2,1,0}};
    prep_w<<<960 + 192, 256, 0, stream>>>(Wq, Wk, Wv, Wo, ws);
    gemm_b<<<640, 256, 0, stream>>>(Wv, ws);
    for (int l = 0; l < 2; ++l) {
        if (l == 0) ln_xf<u16><<<128 + 66, 256, 0, stream>>>(x, xa, nullptr, mem0, ws);
        else ln_xf<float><<<128 + 66, 256, 0, stream>>>(xa, nullptr, nullptr, mem0, ws);
        layer_a<<<256, 256, LDS_BYTES, stream>>>(ws, tn);
        tok_out<<<512, 256, 0, stream>>>(ws, xa, xb);
        ln_xf<float><<<128, 256, 0, stream>>>(xb, nullptr, xnp, nullptr, ws);
        b_qkv<<<dim3(128, 8), 256, 0, stream>>>(xnp, wqkv, qkv);
        b_kv<<<dim3(32, 8), 256, 0, stream>>>(qkv, wphi, part);
        b_o<<<dim3(32, 8), 256, 0, stream>>>(qkv, wphi, part, ob);
        b_wo<<<dim3(128, 3), 256, 0, stream>>>(ob, wo, xb, xa, l == 1 ? (u16*)d_out : nullptr);
    }
}
```

```cpp
#include <hip/hip_runtime.h>
#include <hip/hip_bf16.h>
#include <stdint.h>

typedef __bf16 bf2 __attribute__((ext_vector_type(2)));
typedef __bf16 bf8 __attribute__((ext_vector_type(8)));
typedef __bf16 bf16v __attribute__((ext_vector_type(16)));
typedef float f4 __attribute__((ext_vector_type(4)));
typedef unsigned u2 __attribute__((ext_vector_type(2)));
typedef unsigned u4 __attribute__((ext_vector_type(4)));
typedef unsigned u8v __attribute__((ext_vector_type(8)));
typedef short s4 __attribute__((ext_vector_type(4)));
typedef unsigned short u16;
#define LAS __attribute__((address_space(3)))
#define DEV __device__ __forceinline__
typedef __amdgpu_buffer_rsrc_t rsrc_t;

constexpr unsigned SENT = 0xFFFFFFFFu;
constexpr int SC1 = 16;
constexpr unsigned SPIN_CAP = 1u << 16;

constexpr unsigned RING = 4u;
constexpr unsigned MCOPY = 4u;
constexpr unsigned PSLOT = 49152u;
constexpr unsigned MSLOT = 32768u;
constexpr unsigned OFF_PRING = 0u;
constexpr unsigned OFF_MRING = 786432u;
constexpr unsigned OFF_SCHED = 1310720u;
constexpr unsigned RING_BYTES = 1318912u;
constexpr unsigned OFF_PROBE = 2031616u;
constexpr unsigned OFF_FLAGS = 2097152u;
constexpr unsigned FL_CLAIM = 0u;
constexpr unsigned FL_ABORT = 1024u;
constexpr unsigned FL_KX = 2048u;
constexpr unsigned FL_VX = 6144u;
constexpr unsigned FL_UX = 10240u;
constexpr unsigned FL_KM = 14336u;
constexpr unsigned FL_PT = 18432u;
constexpr unsigned FL_VO = 22528u;
constexpr unsigned FL_CNT = 32768u;
constexpr unsigned FL_END = 34816u;
constexpr unsigned FL_NEAR = 65408u;
constexpr unsigned OFF_WT = 2162688u;
constexpr unsigned OFF_WOT = 25755648u;
constexpr unsigned OFF_BT = 33619968u;
constexpr unsigned OFF_XF = 59834368u;
constexpr unsigned OFF_KX = 62455808u;
constexpr unsigned OFF_VX = 87621632u;
constexpr unsigned OFF_UX = 112787456u;
constexpr unsigned OFF_VO = 196673536u;
constexpr unsigned OFF_KM = 217645056u;
constexpr unsigned OFF_PTX = 223936512u;
constexpr unsigned OFF_PTM = 228130816u;
constexpr unsigned OFF_XA = 229179392u;
constexpr unsigned OFF_XB = 234422272u;
constexpr unsigned OFF_XR = 239665152u;
constexpr unsigned OFF_WB = 242286592u;
constexpr unsigned OFF_CS = 244744192u;
constexpr unsigned OFF_WO2 = 244752384u;
constexpr unsigned OFF_PHI = 245571584u;
constexpr unsigned OFF_SLAB = 250814464u;
constexpr unsigned OFF_KVC = 257105920u;
constexpr unsigned OFF_PROF = 257171456u;
constexpr unsigned WS_BYTES = 258220032u;
constexpr unsigned OOB = 2147483632u;
constexpr unsigned NSEG = 64u;

DEV unsigned ringphys(unsigned o) { return ((o >> 13) << 14) | (o & 8191); }
DEV unsigned pk2(float a, float b) { bf2 v = {(__bf16)a, (__bf16)b}; return __builtin_bit_cast(unsigned, v); }
DEV float asf(unsigned v) { return __builtin_bit_cast(float, v); }
DEV float bf2f(u16 v) { return __builtin_bit_cast(float, (unsigned)v << 16); }
DEV f4 mfma(u4 a, u4 b, f4 c) { return __builtin_amdgcn_mfma_f32_16x16x32_bf16(__builtin_bit_cast(bf8, a), __builtin_bit_cast(bf8, b), c, 0, 0, 0); }
DEV f4 smfmac(u4 a, u8v b, f4 c, int idx) { return __builtin_amdgcn_smfmac_f32_16x16x64_bf16(__builtin_bit_cast(bf8, a), __builtin_bit_cast(bf16v, b), c, idx, 0, 0); }
DEV u4 ld16(rsrc_t r, unsigned v, unsigned s) { return __builtin_amdgcn_raw_buffer_load_b128(r, v, s, SC1); }
DEV u4 ld16p(rsrc_t r, unsigned v, unsigned s) { return __builtin_amdgcn_raw_buffer_load_b128(r, v, s, 0); }
DEV u2 ld8(rsrc_t r, unsigned v, unsigned s) { return __builtin_amdgcn_raw_buffer_load_b64(r, v, s, SC1); }
DEV unsigned ld4(rsrc_t r, unsigned v, unsigned s) { return __builtin_amdgcn_raw_buffer_load_b32(r, v, s, SC1); }
DEV void st16(u4 d, rsrc_t r, unsigned v, unsigned s) { __builtin_amdgcn_raw_buffer_store_b128(d, r, v, s, SC1); asm volatile("s_nop 1" :: "v"(d) : "memory"); }
DEV void st8(u2 d, rsrc_t r, unsigned v, unsigned s) { __builtin_amdgcn_raw_buffer_store_b64(d, r, v, s, SC1); }
DEV void st4(unsigned d, rsrc_t r, unsigned v, unsigned s) { __builtin_amdgcn_raw_buffer_store_b32(d, r, v, s, SC1); }
DEV u8v cat(u4 a, u4 b) { return __builtin_shufflevector(a, b, 0, 1, 2, 3, 4, 5, 6, 7); }
DEV unsigned umax3(unsigned a, unsigned b, unsigned c) { return max(max(a, b), c); }
DEV unsigned umax4(u4 v, unsigned m) { return umax3(umax3(v.x, v.y, v.z), v.w, m); }
DEV void memfence() { asm volatile("" ::: "memory"); __builtin_amdgcn_sched_barrier(0); }
DEV void drain() { asm volatile("s_waitcnt vmcnt(0)" ::: "memory"); }
DEV void bar() { asm volatile("s_waitcnt lgkmcnt(0)\n\ts_barrier" ::: "memory"); }
DEV unsigned xcc_id() { return (unsigned)__builtin_amdgcn_s_getreg(20 | (3 << 11)) & 0xfu; }
DEV float fold32(float x, float y) {
    auto r = __builtin_amdgcn_permlane32_swap(__builtin_bit_cast(unsigned, x), __builtin_bit_cast(unsigned, y), false, false);
    return asf(r[0]) + asf(r[1]);
}
DEV u2 trread(LAS char* p) { return __builtin_bit_cast(u2, __builtin_amdgcn_ds_read_tr16_b64_v4i16((LAS s4*)p)); }
template <int CTRL, int RM, int BM> DEV float dppf(float old, float v) {
    return __builtin_bit_cast(float, __builtin_amdgcn_update_dpp(__builtin_bit_cast(int, old), __builtin_bit_cast(int, v), CTRL, RM, BM, false));
}
DEV float xmax16(float v) { auto r = __builtin_amdgcn_permlane16_swap(__builtin_bit_cast(unsigned, v), __builtin_bit_cast(unsigned, v), false, false); return fmaxf(asf(r[0]), asf(r[1])); }
DEV float xmax32(float v) { auto r = __builtin_amdgcn_permlane32_swap(__builtin_bit_cast(unsigned, v), __builtin_bit_cast(unsigned, v), false, false); return fmaxf(asf(r[0]), asf(r[1])); }
DEV float xadd16(float v) { auto r = __builtin_amdgcn_permlane16_swap(__builtin_bit_cast(unsigned, v), __builtin_bit_cast(unsigned, v), false, false); return asf(r[0]) + asf(r[1]); }
DEV float xadd32(float v) { auto r = __builtin_amdgcn_permlane32_swap(__builtin_bit_cast(unsigned, v), __builtin_bit_cast(unsigned, v), false, false); return asf(r[0]) + asf(r[1]); }

DEV bool give_up(unsigned& spins, rsrc_t r) {
    ++spins;
    if (spins > SPIN_CAP) { st4(1u, r, 0, OFF_FLAGS + FL_ABORT); return true; }
    if ((spins & 63u) == 0u) return __builtin_amdgcn_readfirstlane(ld4(r, 0, OFF_FLAGS + FL_ABORT)) != 0u;
    return false;
}
DEV void wait_flags(rsrc_t r, unsigned voff, unsigned& val, unsigned need, bool& dead) {
    unsigned spins = 0;
    while (!dead && !__all(val >= need)) {
        val = ld4(r, voff, OFF_FLAGS);
        memfence();
        if (__all(val >= need)) break;
        __builtin_amdgcn_s_sleep(2);
        if (give_up(spins, r)) dead = true;
    }
}

DEV int dperm(int g, int j) { return 16 * (g >> 1) + 8 * ((j >> 1) & 1) + 4 * (g & 1) + 2 * (j >> 2) + (j & 1); }

__global__ void __launch_bounds__(256) prep_w(const u16* __restrict__ Wq, const u16* __restrict__ Wk, const u16* __restrict__ Wv, const u16* __restrict__ Wo, char* ws) {
    __shared__ __attribute__((aligned(16))) u16 tile[32 * 648];
    const int tid = threadIdx.x, b = blockIdx.x;
    if (b < 960) {
        const int mat = b / 320, h = (b % 320) / 10, t = b % 10;
        const u16* W = mat == 0 ? Wq : (mat == 1 ? Wk : Wv);
        for (int c = tid; c < 64 * 24; c += 256) { const int row = c / 24, ch = c % 24; *(u4*)(tile + row * 200 + ch * 8) = *(const u4*)(W + (size_t)(64 * t + row) * 6144 + h * 192 + ch * 8); }
        __syncthreads();
        u4* out = (u4*)(ws + OFF_WT);
        for (int f = tid; f < 12 * 128; f += 256) {
            const int et = f >> 7, u = (f >> 6) & 1, lane = f & 63, g = lane >> 4, n = lane & 15;
            unsigned w[4];
            for (int q = 0; q < 4; ++q) w[q] = tile[(32 * u + dperm(g, 2 * q)) * 200 + 16 * et + n] | ((unsigned)tile[(32 * u + dperm(g, 2 * q + 1)) * 200 + 16 * et + n] << 16);
            out[((size_t)(((mat * 32 + h) * 12 + et) * 10 + t) * 2 + u) * 64 + lane] = (u4){w[0], w[1], w[2], w[3]};
        }
    } else {
        const int h = (b - 960) / 6, ks = (b - 960) % 6;
        for (int c = tid; c < 32 * 80; c += 256) { const int row = c / 80, ch = c % 80; *(u4*)(tile + row * 648 + ch * 8) = *(const u4*)(Wo + (size_t)(h * 192 + 32 * ks + row) * 640 + ch * 8); }
        __syncthreads();
        u4* out = (u4*)(ws + OFF_WOT);
        for (int f = tid; f < 40 * 64; f += 256) {
            const int ct = f >> 6, lane = f & 63, g = lane >> 4, n = lane & 15;
            unsigned w[4];
            for (int q = 0; q < 4; ++q) { const int e = 16 * (q >> 1) + 4 * g + 2 * (q & 1); w[q] = tile[e * 648 + 16 * ct + n] | ((unsigned)tile[(e + 1) * 648 + 16 * ct + n] << 16); }
            out[(size_t)((h * 40 + ct) * 6 + ks) * 64 + lane] = (u4){w[0], w[1], w[2], w[3]};
        }
    }
}

__global__ void __launch_bounds__(256) gemm_b(const u16* __restrict__ Wv, char* ws) {
    const int h = blockIdx.x / 20, U = blockIdx.x % 20, tid = threadIdx.x, w = tid >> 6, lane = tid & 63, g = lane >> 4, m = lane & 15;
    u4 a[2][6];
    for (int x = 0; x < 2; ++x) {
        const int d = 32 * U + dperm(m >> 2, 4 * x + (m & 3));
        const u16* src = Wv + (size_t)d * 6144 + h * 192 + 4 * g;
        for (int ks = 0; ks < 6; ++ks) { const u2 lo = *(const u2*)(src + 32 * ks), hi = *(const u2*)(src + 32 * ks + 16); a[x][ks] = (u4){lo.x, lo.y, hi.x, hi.y}; }
    }
    const u4* wot = (const u4*)(ws + OFF_WOT);
    u4* out = (u4*)(ws + OFF_BT);
    for (int c = 0; c < 10; ++c) {
        const int ct = 10 * w + c;
        f4 acc0 = {0, 0, 0, 0}, acc1 = {0, 0, 0, 0};
        for (int ks = 0; ks < 6; ++ks) { const u4 bb = wot[(size_t)((h * 40 + ct) * 6 + ks) * 64 + lane]; acc0 = mfma(a[0][ks], bb, acc0); acc1 = mfma(a[1][ks], bb, acc1); }
        const int slice = 2 * ct + (m >> 3);
        out[((size_t)(slice * 16 + (h >> 1)) * 20 + U) * 64 + g * 16 + (h & 1) * 8 + (m & 7)] = (u4){pk2(acc0[0], acc0[1]), pk2(acc0[2], acc0[3]), pk2(acc1[0], acc1[1]), pk2(acc1[2], acc1[3])};
    }
}

__global__ void __launch_bounds__(256) ln_xf(const u16* __restrict__ x, const u16* __restrict__ mem0, char* ws, int first) {
    __shared__ __attribute__((aligned(16))) u16 xn[16 * 648];
    const int tid = threadIdx.x, w = tid >> 6, lane = tid & 63, b = blockIdx.x + first;
    rsrc_t r = __builtin_amdgcn_make_buffer_rsrc(ws, 0, WS_BYTES, 0x00020000);
    if (b >= 128) {
        const int q = b - 128;
        if (q < 64) {
            for (unsigned o = (q * 256 + tid) * 16; o < RING_BYTES; o += 64 * 256 * 16) {
                u4 v = {SENT, SENT, SENT, SENT};
                if (o >= OFF_MRING && o < OFF_MRING + MCOPY * MSLOT) {
                    const unsigned p = (o - OFF_MRING) % MSLOT, lo = ((p >> 14) << 13) | (p & 8191);
                    if (!(p & 8192) && lo < 10240) v = *(const u4*)(mem0 + ((lo >> 4) & 7) * 640 + (lo >> 7) * 8);
                }
                st16(v, r, o, 0);
            }
        } else if (q == 64) {
            for (unsigned o = tid * 16; o < FL_END; o += 4096) st16((u4){0, 0, 0, 0}, r, o, OFF_FLAGS);
        } else if (w == 0) {
            unsigned best[2] = {1u << 30, 1u << 30};
            for (int rep = 0; rep < 8; ++rep) for (int c = 0; c < 2; ++c) {
                drain();
                const unsigned long long t0 = __builtin_amdgcn_s_memtime();
                asm volatile("s_waitcnt lgkmcnt(0)" ::: "memory");
                st16((u4){0, 0, 0, 0}, r, lane < 8 ? lane * 16 : OOB, OFF_PROBE + c * 8192 + (rep & 1) * 4096);
                drain();
                const unsigned d = (unsigned)(__builtin_amdgcn_s_memtime() - t0);
                best[c] = min(best[c], d);
            }
            st4((xcc_id() >> 2) ^ (best[0] > best[1] ? 1u : 0u), r, lane == 0 ? 0 : OOB, OFF_FLAGS + FL_NEAR);
        }
        return;
    }
    for (int q = 0; q < 4; ++q) {
        const int m = w * 4 + q, tok = b * 16 + m;
        float v[10], sum = 0.f;
        for (int j = 0; j < 10; ++j) { v[j] = bf2f(x[(size_t)tok * 640 + j * 64 + lane]); sum += v[j]; }
        for (int o = 32; o; o >>= 1) sum += __shfl_xor(sum, o);
        const float mean = sum * (1.f / 640.f);
        float var = 0.f;
        for (int j = 0; j < 10; ++j) { const float d = v[j] - mean; var += d * d; }
        for (int o = 32; o; o >>= 1) var += __shfl_xor(var, o);
        const float rs = rsqrtf(var * (1.f / 640.f) + 1e-5f);
        for (int j = 0; j < 10; ++j) {
            const u16 y = (u16)pk2((v[j] - mean) * rs, 0.f);
            xn[m * 648 + j * 64 + lane] = y;
            *(float*)(ws + OFF_XA + ((size_t)(b * 160 + j * 16 + (lane >> 2)) * 16 + m) * 16 + (lane & 3) * 4) = v[j];
        }
    }
    __syncthreads();
    u4* out = (u4*)(ws + OFF_XF);
    for (int f = tid; f < 20 * 64; f += 256) {
        const int kk = f >> 6, l = f & 63, g = l >> 4, m = l & 15;
        const unsigned* row = (const unsigned*)(xn + m * 648 + 32 * kk + 16 * (g >> 1) + 4 * (g & 1));
        out[(size_t)(b * 20 + kk) * 64 + l] = (u4){row[0], row[4], row[1], row[5]};
    }
}

constexpr int LDS_BYTES = 152 * 1024;
struct Tune { int p[8]; };
#define ROLE_CLOBBERS "v0", "v1", "v2", "v3", "v4", "v5", "v6", "v7", "v8", "v9", "v10", "v11", "v12", "v13", "v14", "v15", "v16", "v17", "v18", "v19", "v20", "v21", "v22", "v23", "v24", "v25", "v26", "v27", "v28", "v29", "v30", "v31", "v32", "v33", "v34", "v35", "v36", "v37", "v38", "v39", "v40", "v41", "v42", "v43", "v44", "v45", "v46", "v47", "v48", "v49", "v50", "v51", "v52", "v53", "v54", "v55", "v56", "v57", "v58", "v59", "v60", "v61", "v62", "v63", "v64", "v65", "v66", "v67", "v68", "v69", "v70", "v71", "v72", "v73", "v74", "v75", "v76", "v77", "v78", "v79", "v80", "v81", "v82", "v83", "v84", "v85", "v86", "v87", "v88", "v89", "v90", "v91", "v92", "v93", "v94", "v95", "v96", "v97", "v98", "v99", "v100", "v101", "v102", "v103", "v104", "v105", "v106", "v107", "v108", "v109", "v110", "v111", "v112", "v113", "v114", "v115", "v116", "v117", "v118", "v119", "v120", "v121", "v122", "v123", "v124", "v125", "v126", "v127", "v128", "v129", "v130", "v131", "v132", "v133", "v134", "v135", "v136", "v137", "v138", "v139", "v140", "v141", "v142", "v143", "v144", "v145", "v146", "v147", "v148", "v149", "v150", "v151", "v152", "v153", "v154", "v155", "v156", "v157", "v158", "v159", "v160", "v161", "v162", "v163", "v164", "v165", "v166", "v167", "v168", "v169", "v170", "v171", "v172", "v173", "v174", "v175", "v176", "v177", "v178", "v179", "v180", "v181", "v182", "v183", "v184", "v185", "v186", "v187", "v188", "v189", "v190", "v191", "v192", "v193", "v194", "v195", "v196", "v197", "v198", "v199", "v200", "v201", "v202", "v203", "v204", "v205", "v206", "v207", "v208", "v209", "v210", "v211", "v212", "v213", "v214", "v215", "v216", "v217", "v218", "v219", "v220", "v221", "v222", "v223", "v224", "v225", "v226", "v227", "v228", "v229", "v230", "v231", "v232", "v233", "v234", "v235", "v236", "v237", "v238", "v239", "v240", "v241", "v242", "v243", "v244", "v245", "v246", "v247", "v248", "v249", "v250", "v251", "v252", "v253", "v254", "v255", "a0", "a1", "a2", "a3", "a4", "a5", "a6", "a7", "a8", "a9", "a10", "a11", "a12", "a13", "a14", "a15", "a16", "a17", "a18", "a19", "a20", "a21", "a22", "a23", "a24", "a25", "a26", "a27", "a28", "a29", "a30", "a31", "a32", "a33", "a34", "a35", "a36", "a37", "a38", "a39", "a40", "a41", "a42", "a43", "a44", "a45", "a46", "a47", "a48", "a49", "a50", "a51", "a52", "a53", "a54", "a55", "a56", "a57", "a58", "a59", "a60", "a61", "a62", "a63", "a64", "a65", "a66", "a67", "a68", "a69", "a70", "a71", "a72", "a73", "a74", "a75", "a76", "a77", "a78", "a79", "a80", "a81", "a82", "a83", "a84", "a85", "a86", "a87", "a88", "a89", "a90", "a91", "a92", "a93", "a94", "a95", "a96", "a97", "a98", "a99", "a100", "a101", "a102", "a103", "a104", "a105", "a106", "a107", "a108", "a109", "a110", "a111", "a112", "a113", "a114", "a115", "a116", "a117", "a118", "a119", "a120", "a121", "a122", "a123", "a124", "a125", "a126", "a127", "a128", "a129", "a130", "a131", "a132", "a133", "a134", "a135", "a136", "a137", "a138", "a139", "a140", "a141", "a142", "a143", "a144", "a145", "a146", "a147", "a148", "a149", "a150", "a151", "a152", "a153", "a154", "a155", "a156", "a157", "a158", "a159", "a160", "a161", "a162", "a163", "a164", "a165", "a166", "a167", "a168", "a169", "a170", "a171", "a172", "a173", "a174", "a175", "a176", "a177", "a178", "a179", "a180", "a181", "a182", "a183", "a184", "a185", "a186", "a187", "a188", "a189", "a190", "a191", "a192", "a193", "a194", "a195", "a196", "a197", "a198", "a199", "a200", "a201", "a202", "a203", "a204", "a205", "a206", "a207", "a208", "a209", "a210", "a211", "a212", "a213", "a214", "a215", "a216", "a217", "a218", "a219", "a220", "a221", "a222", "a223", "a224", "a225", "a226", "a227", "a228", "a229", "a230", "a231", "a232", "a233", "a234", "a235", "a236", "a237", "a238", "a239", "a240", "a241", "a242", "a243", "a244", "a245", "a246", "a247", "a248", "a249", "a250", "a251", "a252", "a253", "a254", "a255", "s18", "s19", "s20", "s21", "s22", "s23", "s24", "s25", "s26", "s27", "s28", "s29", "s30", "s31", "s34", "s35", "s36", "s37", "s38", "s39", "s40", "s41", "s42", "s43", "s44", "s45", "s46", "s47", "s48", "s49", "s50", "s51", "s52", "s53", "s54", "s55", "s56", "s57", "s58", "s59", "s60", "s61", "s62", "s63", "s64", "s65", "s66", "s67", "s68", "s69", "s70", "s71", "s72", "s73", "s74", "s75", "s76", "s77", "s78", "s79", "s80", "s81", "s82", "s83", "s84", "s85", "s86", "s87", "s88", "s89", "s90", "s91", "s92", "s93", "s94", "s95", "s96", "s97", "s98", "s99", "vcc", "scc", "memory"
#define ROLE_INPUTS "s"(lo), "s"(hi), "s"(WS_BYTES), "s"(0x00020000u), "s"(id), "s"(w), "s"(aux), "s"(tn.p[0]), "s"(tn.p[1]), "s"(tn.p[2]), "s"(tn.p[3]), "s"(tn.p[4]), "s"(tn.p[5]), "s"(tn.p[6]), "s"(tn.p[7])
DEV void role_head(unsigned lo, unsigned hi, int id, int w, int aux, const Tune& tn) {
    asm volatile(
    "s_mov_b32 s40, %0\n"
    "s_mov_b32 s41, %1\n"
    "s_mov_b32 s42, %2\n"
    "s_mov_b32 s43, %3\n"
    "s_mov_b32 s44, %4\n"
    "s_mov_b32 s45, %5\n"
    "s_mov_b32 s46, %6\n"
    "s_mov_b32 s66, %7\n"
    "s_mov_b32 s67, %8\n"
    "s_mov_b32 s68, %9\n"
    "s_mov_b32 s69, %10\n"
    "s_mov_b32 s70, %11\n"
    "s_mov_b32 s71, %12\n"
    "s_mov_b32 s72, %13\n"
    "s_mov_b32 s73, %14\n"
    "s_mov_b64 exec, -1\n"
    "v_mbcnt_lo_u32_b32 v15, -1, 0\n"
    "v_mbcnt_hi_u32_b32 v15, -1, v15\n"
    "v_lshlrev_b32 v0, 4, v15\n"
    "s_mov_b32 s58, 2097152\n"
    "s_mov_b32 s92, 0\n"
    "s_lshl_b32 s48, s45, 15\n"
    "v_add_u32 v2, s48, v0\n"
    "s_lshl_b32 s50, s44, 2\n"
    "s_add_u32 s50, s50, s45\n"
    "s_mul_i32 s50, s50, 3\n"
    "s_mul_i32 s51, s50, 6554\n"
    "s_lshr_b32 s51, s51, 16\n"
    "s_mul_i32 s51, s51, 10\n"
    "s_sub_u32 s50, s50, s51\n"
    "s_lshl_b32 s20, s50, 10\n"
    "s_add_u32 s50, s50, 1\n"
    "s_cmp_eq_u32 s50, 10\n"
    "s_cselect_b32 s50, 0, s50\n"
    "s_lshl_b32 s21, s50, 10\n"
    "s_add_u32 s50, s50, 1\n"
    "s_cmp_eq_u32 s50, 10\n"
    "s_cselect_b32 s50, 0, s50\n"
    "s_lshl_b32 s22, s50, 10\n"
    "s_add_u32 s50, s50, 1\n"
    "s_cmp_eq_u32 s50, 10\n"
    "s_cselect_b32 s50, 0, s50\n"
    "s_lshl_b32 s23, s50, 10\n"
    "s_add_u32 s50, s50, 1\n"
    "s_cmp_eq_u32 s50, 10\n"
    "s_cselect_b32 s50, 0, s50\n"
    "s_lshl_b32 s24, s50, 10\n"
    "s_add_u32 s50, s50, 1\n"
    "s_cmp_eq_u32 s50, 10\n"
    "s_cselect_b32 s50, 0, s50\n"
    "s_lshl_b32 s25, s50, 10\n"
    "s_add_u32 s50, s50, 1\n"
    "s_cmp_eq_u32 s50, 10\n"
    "s_cselect_b32 s50, 0, s50\n"
    "s_lshl_b32 s26, s50, 10\n"
    "s_add_u32 s50, s50, 1\n"
    "s_cmp_eq_u32 s50, 10\n"
    "s_cselect_b32 s50, 0, s50\n"
    "s_lshl_b32 s27, s50, 10\n"
    "s_add_u32 s50, s50, 1\n"
    "s_cmp_eq_u32 s50, 10\n"
    "s_cselect_b32 s50, 0, s50\n"
    "s_lshl_b32 s28, s50, 10\n"
    "s_add_u32 s50, s50, 1\n"
    "s_cmp_eq_u32 s50, 10\n"
    "s_cselect_b32 s50, 0, s50\n"
    "s_lshl_b32 s29, s50, 10\n"
    "s_add_u32 s50, s50, 1\n"
    "s_cmp_eq_u32 s50, 10\n"
    "s_cselect_b32 s50, 0, s50\n"
    "s_mul_i32 s48, s44, 12\n"
    "s_mul_i32 s49, s45, 3\n"
    "s_add_u32 s48, s48, s49\n"
    "s_add_u32 s48, s48, 0\n"
    "s_mul_i32 s48, s48, 20480\n"
    "s_add_u32 s48, s48, 2162688\n"
    "s_lshl1_add_u32 s49, s20, s48\n"
    "buffer_load_dwordx4 a[0:3], v0, s[40:43], s49 offen\n"
    "buffer_load_dwordx4 a[4:7], v0, s[40:43], s49 offen offset:1024\n"
    "s_lshl1_add_u32 s49, s21, s48\n"
    "buffer_load_dwordx4 a[24:27], v0, s[40:43], s49 offen\n"
    "buffer_load_dwordx4 a[28:31], v0, s[40:43], s49 offen offset:1024\n"
    "s_lshl1_add_u32 s49, s22, s48\n"
    "buffer_load_dwordx4 a[40:43], v0, s[40:43], s49 offen\n"
    "buffer_load_dwordx4 a[44:47], v0, s[40:43], s49 offen offset:1024\n"
    "s_lshl1_add_u32 s49, s23, s48\n"
    "buffer_load_dwordx4 a[64:67], v0, s[40:43], s49 offen\n"
    "buffer_load_dwordx4 a[68:71], v0, s[40:43], s49 offen offset:1024\n"
    "s_lshl1_add_u32 s49, s24, s48\n"
    "buffer_load_dwordx4 a[88:91], v0, s[40:43], s49 offen\n"
    "buffer_load_dwordx4 a[92:95], v0, s[40:43], s49 offen offset:1024\n"
    "s_lshl1_add_u32 s49, s25, s48\n"
    "buffer_load_dwordx4 a[104:107], v0, s[40:43], s49 offen\n"
    "buffer_load_dwordx4 a[108:111], v0, s[40:43], s49 offen offset:1024\n"
    "s_lshl1_add_u32 s49, s26, s48\n"
    "buffer_load_dwordx4 a[128:131], v0, s[40:43], s49 offen\n"
    "buffer_load_dwordx4 a[132:135], v0, s[40:43], s49 offen offset:1024\n"
    "s_lshl1_add_u32 s49, s27, s48\n"
    "buffer_load_dwordx4 a[152:155], v0, s[40:43], s49 offen\n"
    "buffer_load_dwordx4 a[156:159], v0, s[40:43], s49 offen offset:1024\n"
    "s_lshl1_add_u32 s49, s28, s48\n"
    "buffer_load_dwordx4 a[168:171], v0, s[40:43], s49 offen\n"
    "buffer_load_dwordx4 a[172:175], v0, s[40:43], s49 offen offset:1024\n"
    "s_lshl1_add_u32 s49, s29, s48\n"
    "buffer_load_dwordx4 a[192:195], v0, s[40:43], s49 offen\n"
    "buffer_load_dwordx4 a[196:199], v0, s[40:43], s49 offen offset:1024\n"
    "s_mul_i32 s48, s44, 12\n"
    "s_mul_i32 s49, s45, 3\n"
    "s_add_u32 s48, s48, s49\n"
    "s_add_u32 s48, s48, 384\n"
    "s_mul_i32 s48, s48, 20480\n"
    "s_add_u32 s48, s48, 2162688\n"
    "s_lshl1_add_u32 s49, s20, s48\n"
    "buffer_load_dwordx4 a[16:19], v0, s[40:43], s49 offen\n"
    "buffer_load_dwordx4 a[20:23], v0, s[40:43], s49 offen offset:1024\n"
    "s_lshl1_add_u32 s49, s21, s48\n"
    "buffer_load_dwordx4 a[32:35], v0, s[40:43], s49 offen\n"
    "buffer_load_dwordx4 a[36:39], v0, s[40:43], s49 offen offset:1024\n"
    "s_lshl1_add_u32 s49, s22, s48\n"
    "buffer_load_dwordx4 a[56:59], v0, s[40:43], s49 offen\n"
    "buffer_load_dwordx4 a[60:63], v0, s[40:43], s49 offen offset:1024\n"
    "s_lshl1_add_u32 s49, s23, s48\n"
    "buffer_load_dwordx4 a[80:83], v0, s[40:43], s49 offen\n"
    "buffer_load_dwordx4 a[84:87], v0, s[40:43], s49 offen offset:1024\n"
    "s_lshl1_add_u32 s49, s24, s48\n"
    "buffer_load_dwordx4 a[96:99], v0, s[40:43], s49 offen\n"
    "buffer_load_dwordx4 a[100:103], v0, s[40:43], s49 offen offset:1024\n"
    "s_lshl1_add_u32 s49, s25, s48\n"
    "buffer_load_dwordx4 a[120:123], v0, s[40:43], s49 offen\n"
    "buffer_load_dwordx4 a[124:127], v0, s[40:43], s49 offen offset:1024\n"
    "s_lshl1_add_u32 s49, s26, s48\n"
    "buffer_load_dwordx4 a[144:147], v0, s[40:43], s49 offen\n"
    "buffer_load_dwordx4 a[148:151], v0, s[40:43], s49 offen offset:1024\n"
    "s_lshl1_add_u32 s49, s27, s48\n"
    "buffer_load_dwordx4 a[160:163], v0, s[40:43], s49 offen\n"
    "buffer_load_dwordx4 a[164:167], v0, s[40:43], s49 offen offset:1024\n"
    "s_lshl1_add_u32 s49, s28, s48\n"
    "buffer_load_dwordx4 a[184:187], v0, s[40:43], s49 offen\n"
    "buffer_load_dwordx4 a[188:191], v0, s[40:43], s49 offen offset:1024\n"
    "s_lshl1_add_u32 s49, s29, s48\n"
    "buffer_load_dwordx4 a[208:211], v0, s[40:43], s49 offen\n"
    "buffer_load_dwordx4 a[212:215], v0, s[40:43], s49 offen offset:1024\n"
    "s_mul_i32 s48, s44, 12\n"
    "s_mul_i32 s49, s45, 3\n"
    "s_add_u32 s48, s48, s49\n"
    "s_add_u32 s48, s48, 1\n"
    "s_mul_i32 s48, s48, 20480\n"
    "s_add_u32 s48, s48, 2162688\n"
    "s_lshl1_add_u32 s49, s20, s48\n"
    "buffer_load_dwordx4 a[8:11], v0, s[40:43], s49 offen\n"
    "buffer_load_dwordx4 a[12:15], v0, s[40:43], s49 offen offset:1024\n"
    "s_lshl1_add_u32 s49, s21, s48\n"
    "buffer_load_dwordx4 v[32:35], v0, s[40:43], s49 offen\n"
    "buffer_load_dwordx4 v[36:39], v0, s[40:43], s49 offen offset:1024\n"
    "s_lshl1_add_u32 s49, s22, s48\n"
    "buffer_load_dwordx4 a[48:51], v0, s[40:43], s49 offen\n"
    "buffer_load_dwordx4 a[52:55], v0, s[40:43], s49 offen offset:1024\n"
    "s_lshl1_add_u32 s49, s23, s48\n"
    "buffer_load_dwordx4 a[72:75], v0, s[40:43], s49 offen\n"
    "buffer_load_dwordx4 a[76:79], v0, s[40:43], s49 offen offset:1024\n"
    "s_lshl1_add_u32 s49, s24, s48\n"
    "buffer_load_dwordx4 v[64:67], v0, s[40:43], s49 offen\n"
    "buffer_load_dwordx4 v[68:71], v0, s[40:43], s49 offen offset:1024\n"
    "s_lshl1_add_u32 s49, s25, s48\n"
    "buffer_load_dwordx4 a[112:115], v0, s[40:43], s49 offen\n"
    "buffer_load_dwordx4 a[116:119], v0, s[40:43], s49 offen offset:1024\n"
    "s_lshl1_add_u32 s49, s26, s48\n"
    "buffer_load_dwordx4 a[136:139], v0, s[40:43], s49 offen\n"
    "buffer_load_dwordx4 a[140:143], v0, s[40:43], s49 offen offset:1024\n"
    "s_lshl1_add_u32 s49, s27, s48\n"
    "buffer_load_dwordx4 v[96:99], v0, s[40:43], s49 offen\n"
    "buffer_load_dwordx4 v[100:103], v0, s[40:43], s49 offen offset:1024\n"
    "s_lshl1_add_u32 s49, s28, s48\n"
    "buffer_load_dwordx4 a[176:179], v0, s[40:43], s49 offen\n"
    "buffer_load_dwordx4 a[180:183], v0, s[40:43], s49 offen offset:1024\n"
    "s_lshl1_add_u32 s49, s29, s48\n"
    "buffer_load_dwordx4 a[200:203], v0, s[40:43], s49 offen\n"
    "buffer_load_dwordx4 a[204:207], v0, s[40:43], s49 offen offset:1024\n"
    "s_mul_i32 s48, s44, 12\n"
    "s_mul_i32 s49, s45, 3\n"
    "s_add_u32 s48, s48, s49\n"
    "s_add_u32 s48, s48, 385\n"
    "s_mul_i32 s48, s48, 20480\n"
    "s_add_u32 s48, s48, 2162688\n"
    "s_lshl1_add_u32 s49, s20, s48\n"
    "buffer_load_dwordx4 v[16:19], v0, s[40:43], s49 offen\n"
    "buffer_load_dwordx4 v[20:23], v0, s[40:43], s49 offen offset:1024\n"
    "s_lshl1_add_u32 s49, s21, s48\n"
    "buffer_load_dwordx4 v[24:27], v0, s[40:43], s49 offen\n"
    "s_waitcnt vmcnt(61)\n"
    "buffer_load_dwordx4 v[28:31], v0, s[40:43], s49 offen offset:1024\n"
    "s_lshl1_add_u32 s49, s22, s48\n"
    "buffer_load_dwordx4 v[40:43], v0, s[40:43], s49 offen\n"
    "s_waitcnt vmcnt(61)\n"
    "buffer_load_dwordx4 v[44:47], v0, s[40:43], s49 offen offset:1024\n"
    "s_lshl1_add_u32 s49, s23, s48\n"
    "buffer_load_dwordx4 v[48:51], v0, s[40:43], s49 offen\n"
    "s_waitcnt vmcnt(61)\n"
    "buffer_load_dwordx4 v[52:55], v0, s[40:43], s49 offen offset:1024\n"
    "s_lshl1_add_u32 s49, s24, s48\n"
    "buffer_load_dwordx4 v[56:59], v0, s[40:43], s49 offen\n"
    "s_waitcnt vmcnt(61)\n"
    "buffer_load_dwordx4 v[60:63], v0, s[40:43], s49 offen offset:1024\n"
    "s_lshl1_add_u32 s49, s25, s48\n"
    "buffer_load_dwordx4 v[72:75], v0, s[40:43], s49 offen\n"
    "s_waitcnt vmcnt(61)\n"
    "buffer_load_dwordx4 v[76:79], v0, s[40:43], s49 offen offset:1024\n"
    "s_lshl1_add_u32 s49, s26, s48\n"
    "buffer_load_dwordx4 v[80:83], v0, s[40:43], s49 offen\n"
    "s_waitcnt vmcnt(61)\n"
    "buffer_load_dwordx4 v[84:87], v0, s[40:43], s49 offen offset:1024\n"
    "s_lshl1_add_u32 s49, s27, s48\n"
    "buffer_load_dwordx4 v[88:91], v0, s[40:43], s49 offen\n"
    "s_waitcnt vmcnt(61)\n"
    "buffer_load_dwordx4 v[92:95], v0, s[40:43], s49 offen offset:1024\n"
    "s_lshl1_add_u32 s49, s28, s48\n"
    "buffer_load_dwordx4 v[104:107], v0, s[40:43], s49 offen\n"
    "s_waitcnt vmcnt(61)\n"
    "buffer_load_dwordx4 v[108:111], v0, s[40:43], s49 offen offset:1024\n"
    "s_lshl1_add_u32 s49, s29, s48\n"
    "buffer_load_dwordx4 v[112:115], v0, s[40:43], s49 offen\n"
    "s_waitcnt vmcnt(61)\n"
    "buffer_load_dwordx4 v[116:119], v0, s[40:43], s49 offen offset:1024\n"
    "s_mul_i32 s48, s44, 12\n"
    "s_mul_i32 s49, s45, 3\n"
    "s_add_u32 s48, s48, s49\n"
    "s_add_u32 s48, s48, 2\n"
    "s_mul_i32 s48, s48, 20480\n"
    "s_add_u32 s48, s48, 2162688\n"
    "s_lshl1_add_u32 s49, s20, s48\n"
    "buffer_load_dwordx4 a[216:219], v0, s[40:43], s49 offen\n"
    "s_waitcnt vmcnt(61)\n"
    "buffer_load_dwordx4 a[220:223], v0, s[40:43], s49 offen offset:1024\n"
    "s_lshl1_add_u32 s49, s21, s48\n"
    "buffer_load_dwordx4 a[224:227], v0, s[40:43], s49 offen\n"
    "s_waitcnt vmcnt(61)\n"
    "buffer_load_dwordx4 a[228:231], v0, s[40:43], s49 offen offset:1024\n"
    "s_lshl1_add_u32 s49, s22, s48\n"
    "buffer_load_dwordx4 a[232:235], v0, s[40:43], s49 offen\n"
    "s_waitcnt vmcnt(61)\n"
    "buffer_load_dwordx4 a[236:239], v0, s[40:43], s49 offen offset:1024\n"
    "s_lshl1_add_u32 s49, s23, s48\n"
    "buffer_load_dwordx4 a[240:243], v0, s[40:43], s49 offen\n"
    "s_waitcnt vmcnt(61)\n"
    "buffer_load_dwordx4 a[244:247], v0, s[40:43], s49 offen offset:1024\n"
    "s_lshl1_add_u32 s49, s24, s48\n"
    "buffer_load_dwordx4 v[160:163], v0, s[40:43], s49 offen\n"
    "s_waitcnt vmcnt(61)\n"
    "buffer_load_dwordx4 v[164:167], v0, s[40:43], s49 offen offset:1024\n"
    "s_lshl1_add_u32 s49, s25, s48\n"
    "buffer_load_dwordx4 v[176:179], v0, s[40:43], s49 offen\n"
    "s_waitcnt vmcnt(61)\n"
    "buffer_load_dwordx4 v[180:183], v0, s[40:43], s49 offen offset:1024\n"
    "s_lshl1_add_u32 s49, s26, s48\n"
    "buffer_load_dwordx4 v[192:195], v0, s[40:43], s49 offen\n"
    "s_waitcnt vmcnt(61)\n"
    "buffer_load_dwordx4 v[196:199], v0, s[40:43], s49 offen offset:1024\n"
    "s_lshl1_add_u32 s49, s27, s48\n"
    "buffer_load_dwordx4 v[208:211], v0, s[40:43], s49 offen\n"
    "s_waitcnt vmcnt(61)\n"
    "buffer_load_dwordx4 v[212:215], v0, s[40:43], s49 offen offset:1024\n"
    "s_lshl1_add_u32 s49, s28, s48\n"
    "buffer_load_dwordx4 v[224:227], v0, s[40:43], s49 offen\n"
    "s_waitcnt vmcnt(61)\n"
    "buffer_load_dwordx4 v[228:231], v0, s[40:43], s49 offen offset:1024\n"
    "s_lshl1_add_u32 s49, s29, s48\n"
    "buffer_load_dwordx4 v[240:243], v0, s[40:43], s49 offen\n"
    "s_waitcnt vmcnt(61)\n"
    "buffer_load_dwordx4 v[244:247], v0, s[40:43], s49 offen offset:1024\n"
    "s_mul_i32 s48, s44, 12\n"
    "s_mul_i32 s49, s45, 3\n"
    "s_add_u32 s48, s48, s49\n"
    "s_add_u32 s48, s48, 386\n"
    "s_mul_i32 s48, s48, 20480\n"
    "s_add_u32 s48, s48, 2162688\n"
    "s_lshl1_add_u32 s49, s20, s48\n"
    "buffer_load_dwordx4 v[120:123], v0, s[40:43], s49 offen\n"
    "s_waitcnt vmcnt(61)\n"
    "buffer_load_dwordx4 v[124:127], v0, s[40:43], s49 offen offset:1024\n"
    "s_lshl1_add_u32 s49, s21, s48\n"
    "buffer_load_dwordx4 v[128:131], v0, s[40:43], s49 offen\n"
    "s_waitcnt vmcnt(61)\n"
    "buffer_load_dwordx4 v[132:135], v0, s[40:43], s49 offen offset:1024\n"
    "s_lshl1_add_u32 s49, s22, s48\n"
    "buffer_load_dwordx4 v[136:139], v0, s[40:43], s49 offen\n"
    "s_waitcnt vmcnt(61)\n"
    "buffer_load_dwordx4 v[140:143], v0, s[40:43], s49 offen offset:1024\n"
    "s_lshl1_add_u32 s49, s23, s48\n"
    "buffer_load_dwordx4 a[248:251], v0, s[40:43], s49 offen\n"
    "s_waitcnt vmcnt(61)\n"
    "buffer_load_dwordx4 a[252:255], v0, s[40:43], s49 offen offset:1024\n"
    "s_lshl1_add_u32 s49, s24, s48\n"
    "buffer_load_dwordx4 v[168:171], v0, s[40:43], s49 offen\n"
    "s_waitcnt vmcnt(61)\n"
    "buffer_load_dwordx4 v[172:175], v0, s[40:43], s49 offen offset:1024\n"
    "s_lshl1_add_u32 s49, s25, s48\n"
    "buffer_load_dwordx4 v[184:187], v0, s[40:43], s49 offen\n"
    "s_waitcnt vmcnt(61)\n"
    "buffer_load_dwordx4 v[188:191], v0, s[40:43], s49 offen offset:1024\n"
    "s_lshl1_add_u32 s49, s26, s48\n"
    "buffer_load_dwordx4 v[200:203], v0, s[40:43], s49 offen\n"
    "s_waitcnt vmcnt(61)\n"
    "buffer_load_dwordx4 v[204:207], v0, s[40:43], s49 offen offset:1024\n"
    "s_lshl1_add_u32 s49, s27, s48\n"
    "buffer_load_dwordx4 v[216:219], v0, s[40:43], s49 offen\n"
    "s_waitcnt vmcnt(61)\n"
    "buffer_load_dwordx4 v[220:223], v0, s[40:43], s49 offen offset:1024\n"
    "s_lshl1_add_u32 s49, s28, s48\n"
    "buffer_load_dwordx4 v[232:235], v0, s[40:43], s49 offen\n"
    "s_waitcnt vmcnt(61)\n"
    "buffer_load_dwordx4 v[236:239], v0, s[40:43], s49 offen offset:1024\n"
    "s_lshl1_add_u32 s49, s29, s48\n"
    "buffer_load_dwordx4 v[248:251], v0, s[40:43], s49 offen\n"
    "s_waitcnt vmcnt(61)\n"
    "buffer_load_dwordx4 v[252:255], v0, s[40:43], s49 offen offset:1024\n"
    "s_and_b32 s49, s20, 0x2000\n"
    "s_add_u32 s20, s20, s49\n"
    "s_and_b32 s49, s21, 0x2000\n"
    "s_add_u32 s21, s21, s49\n"
    "s_and_b32 s49, s22, 0x2000\n"
    "s_add_u32 s22, s22, s49\n"
    "s_and_b32 s49, s23, 0x2000\n"
    "s_add_u32 s23, s23, s49\n"
    "s_and_b32 s49, s24, 0x2000\n"
    "s_add_u32 s24, s24, s49\n"
    "s_and_b32 s49, s25, 0x2000\n"
    "s_add_u32 s25, s25, s49\n"
    "s_and_b32 s49, s26, 0x2000\n"
    "s_add_u32 s26, s26, s49\n"
    "s_and_b32 s49, s27, 0x2000\n"
    "s_add_u32 s27, s27, s49\n"
    "s_and_b32 s49, s28, 0x2000\n"
    "s_add_u32 s28, s28, s49\n"
    "s_and_b32 s49, s29, 0x2000\n"
    "s_add_u32 s29, s29, s49\n"
    "s_waitcnt vmcnt(59)\n"
    "ds_write_b128 v2, v[16:19] offset:0\n"
    "s_waitcnt vmcnt(58)\n"
    "ds_write_b128 v2, v[20:23] offset:1024\n"
    "s_waitcnt vmcnt(57)\n"
    "ds_write_b128 v2, v[24:27] offset:2048\n"
    "s_waitcnt vmcnt(56)\n"
    "ds_write_b128 v2, v[28:31] offset:3072\n"
    "ds_write_b128 v2, v[32:35] offset:4096\n"
    "ds_write_b128 v2, v[36:39] offset:5120\n"
    "s_waitcnt vmcnt(55)\n"
    "ds_write_b128 v2, v[40:43] offset:6144\n"
    "s_waitcnt vmcnt(54)\n"
    "ds_write_b128 v2, v[44:47] offset:7168\n"
    "s_waitcnt vmcnt(53)\n"
    "ds_write_b128 v2, v[48:51] offset:8192\n"
    "s_waitcnt vmcnt(52)\n"
    "ds_write_b128 v2, v[52:55] offset:9216\n"
    "s_waitcnt vmcnt(51)\n"
    "ds_write_b128 v2, v[56:59] offset:10240\n"
    "s_waitcnt vmcnt(50)\n"
    "ds_write_b128 v2, v[60:63] offset:11264\n"
    "ds_write_b128 v2, v[64:67] offset:12288\n"
    "ds_write_b128 v2, v[68:71] offset:13312\n"
    "s_waitcnt vmcnt(49)\n"
    "ds_write_b128 v2, v[72:75] offset:14336\n"
    "s_waitcnt vmcnt(48)\n"
    "ds_write_b128 v2, v[76:79] offset:15360\n"
    "s_waitcnt lgkmcnt(14)\n"
    "s_waitcnt vmcnt(47)\n"
    "ds_write_b128 v2, v[80:83] offset:16384\n"
    "s_waitcnt vmcnt(46)\n"
    "ds_write_b128 v2, v[84:87] offset:17408\n"
    "s_waitcnt lgkmcnt(14)\n"
    "s_waitcnt vmcnt(45)\n"
    "ds_write_b128 v2, v[88:91] offset:18432\n"
    "s_waitcnt vmcnt(44)\n"
    "ds_write_b128 v2, v[92:95] offset:19456\n"
    "s_waitcnt lgkmcnt(14)\n"
    "ds_write_b128 v2, v[96:99] offset:20480\n"
    "ds_write_b128 v2, v[100:103] offset:21504\n"
    "s_waitcnt lgkmcnt(14)\n"
    "s_waitcnt vmcnt(43)\n"
    "ds_write_b128 v2, v[104:107] offset:22528\n"
    "s_waitcnt vmcnt(42)\n"
    "ds_write_b128 v2, v[108:111] offset:23552\n"
    "s_waitcnt lgkmcnt(14)\n"
    "s_waitcnt vmcnt(41)\n"
    "ds_write_b128 v2, v[112:115] offset:24576\n"
    "s_waitcnt vmcnt(40)\n"
    "ds_write_b128 v2, v[116:119] offset:25600\n"
    "s_waitcnt lgkmcnt(14)\n"
    "s_waitcnt vmcnt(19)\n"
    "ds_write_b128 v2, v[120:123] offset:26624\n"
    "s_waitcnt vmcnt(18)\n"
    "ds_write_b128 v2, v[124:127] offset:27648\n"
    "s_waitcnt lgkmcnt(14)\n"
    "s_waitcnt vmcnt(17)\n"
    "ds_write_b128 v2, v[128:131] offset:28672\n"
    "s_waitcnt vmcnt(16)\n"
    "ds_write_b128 v2, v[132:135] offset:29696\n"
    "s_waitcnt lgkmcnt(14)\n"
    "s_waitcnt vmcnt(15)\n"
    "ds_write_b128 v2, v[136:139] offset:30720\n"
    "s_waitcnt vmcnt(14)\n"
    "ds_write_b128 v2, v[140:143] offset:31744\n"
    "s_waitcnt lgkmcnt(14)\n"
    "s_waitcnt vmcnt(0) lgkmcnt(0)\n"
    "v_lshrrev_b32 v152, 4, v15\n"
    "v_and_b32 v153, 7, v15\n"
    "v_bfe_u32 v154, v15, 3, 1\n"
    "v_cmp_eq_u32 vcc, 1, v154\n"
    "v_mov_b32 v13, 0x4444\n"
    "v_mov_b32 v1, 0xeeee\n"
    "v_cndmask_b32 v1, v13, v1, vcc\n"
    "v_mov_b32 v13, 0xff800000\n"
    "v_cndmask_b32 v8, 0, v13, vcc\n"
    "v_cndmask_b32 v9, 0, v13, vcc\n"
    "v_cndmask_b32 v10, 0, v13, vcc\n"
    "v_cndmask_b32 v11, 0, v13, vcc\n"
    "v_bfe_u32 v13, v15, 2, 2\n"
    "v_xor_b32 v13, v13, v152\n"
    "v_lshlrev_b32 v13, 3, v13\n"
    "v_and_b32 v3, 15, v15\n"
    "v_lshl_add_u32 v3, v3, 5, v13\n"
    "s_mul_i32 s48, s45, 1536\n"
    "s_add_u32 s48, s48, 143360\n"
    "v_add_u32 v3, s48, v3\n"
    "v_xor_b32 v4, v15, v152\n"
    "v_lshlrev_b32 v4, 3, v4\n"
    "v_add_u32 v4, 143360, v4\n"
    "v_add_u32 v5, 131072, v0\n"
    "s_lshr_b32 s48, s44, 1\n"
    "s_lshl_b32 s48, s48, 10\n"
    "s_and_b32 s49, s44, 1\n"
    "s_lshl_b32 s49, s49, 7\n"
    "s_add_u32 s50, s48, s49\n"
    "s_lshl_b32 s51, s44, 7\n"
    "s_add_u32 s51, s51, 16384\n"
    "s_mov_b32 s47, 0\n"
    "s_mov_b32 s61, 6144\n"
    "s_cmp_lg_u32 s45, 0\n"
    "s_cbranch_scc1 hw_wx_%=\n"
    "v_lshlrev_b32 v6, 8, v152\n"
    "v_add_u32 v6, s50, v6\n"
    "v_mov_b32 v7, s51\n"
    "v_cmp_eq_u32 vcc, 0, v154\n"
    "s_nop 1\n"
    "v_cndmask_b32 v6, v7, v6, vcc\n"
    "v_lshl_add_u32 v6, v153, 4, v6\n"
    "v_and_b32 v13, 0xffffe000, v6\n"
    "v_add_u32 v6, v6, v13\n"
    "v_lshrrev_b32 v7, 3, v15\n"
    "v_cmp_eq_u32 s[56:57], 5, v7\n"
    "s_nop 0\n"
    "s_or_b64 vcc, vcc, s[56:57]\n"
    "v_mov_b32 v13, 2147483632\n"
    "v_cndmask_b32 v6, v13, v6, vcc\n"
    "v_mov_b32 v14, 0\n"
    "v_cmp_eq_u32 vcc, 0, v15\n"
    "v_mov_b32 v13, 2147483632\n"
    "s_nop 0\n"
    "v_cndmask_b32 v14, v13, v14, vcc\n"
    "v_or_b32 v7, s44, v15\n"
    "v_cmp_eq_u32 vcc, 0, v7\n"
    "v_mov_b32 v13, 2147483632\n"
    "s_nop 0\n"
    "v_cndmask_b32 v7, v13, v7, vcc\n"
    "s_mov_b32 s75, 6\n"
    "s_mov_b32 s64, 0x3dd53b95\n"
    "s_mov_b32 s65, s64\n"
    "hw_loopa_%=:\n"
    "s_and_b32 s48, s47, 3\n"
    "s_mul_i32 s53, s48, 196608\n"
    "s_lshl_b32 s50, s48, 2\n"
    "s_add_u32 s50, s50, s46\n"
    "s_mul_i32 s50, s50, 32768\n"
    "s_add_u32 s50, s50, 786432\n"
    "v_add_u32 v158, s50, v0\n"
    "s_add_u32 s50, s53, 49152\n"
    "s_add_u32 s51, s53, 98304\n"
    "s_add_u32 s52, s53, 147456\n"
    "v_mov_b32 v56, 0\n"
    "v_mov_b32 v57, 0\n"
    "v_mov_b32 v58, 0\n"
    "v_mov_b32 v59, 0\n"
    "v_mov_b32 v60, 0\n"
    "v_mov_b32 v61, 0\n"
    "v_mov_b32 v62, 0\n"
    "v_mov_b32 v63, 0\n"
    "v_mov_b32 v64, 0\n"
    "v_mov_b32 v65, 0\n"
    "v_mov_b32 v66, 0\n"
    "v_mov_b32 v67, 0\n"
    "v_mov_b32 v68, 0\n"
    "v_mov_b32 v69, 0\n"
    "v_mov_b32 v70, 0\n"
    "v_mov_b32 v71, 0\n"
    "v_mov_b32 v72, 0\n"
    "v_mov_b32 v73, 0\n"
    "v_mov_b32 v74, 0\n"
    "v_mov_b32 v75, 0\n"
    "v_mov_b32 v76, 0\n"
    "v_mov_b32 v77, 0\n"
    "v_mov_b32 v78, 0\n"
    "v_mov_b32 v79, 0\n"
    "ds_read_b128 v[80:83], v2 offset:0\n"
    "ds_read_b128 v[84:87], v2 offset:1024\n"
    "ds_read_b128 v[88:91], v2 offset:2048\n"
    "ds_read_b128 v[92:95], v2 offset:3072\n"
    "ds_read_b128 v[96:99], v2 offset:4096\n"
    "ds_read_b128 v[100:103], v2 offset:5120\n"
    "s_lshl_b32 s63, s47, 7\n"
    "s_add_u32 s63, s63, 1310720\n"
    "s_waitcnt lgkmcnt(0)\n"
    "s_cmp_lg_u32 s47, 0\n"
    "s_cbranch_scc1 hw_timed_%=\n"
    "s_barrier\n"
    "s_memrealtime s[56:57]\n"
    "s_waitcnt lgkmcnt(0)\n"
    "s_add_u32 s62, s56, s68\n"
    "s_add_u32 s59, s62, s67\n"
    "v_mov_b32 v152, s59\n"
    "s_add_u32 s59, s59, s66\n"
    "v_mov_b32 v153, s59\n"
    "s_nop 1\n"
    "s_branch hw_go_%=\n"
    "hw_timed_%=:\n"
    "s_sub_u32 s55, s47, 1\n"
    "s_lshl_b32 s54, s55, 7\n"
    "s_add_u32 s54, s54, 1310720\n"
    "s_mov_b32 s60, 0\n"
    "hw_rs_%=:\n"
    "buffer_load_dwordx2 v[152:153], v14, s[40:43], s54 offen sc1\n"
    "s_waitcnt vmcnt(0)\n"
    "v_readfirstlane_b32 s55, v152\n"
    "v_readfirstlane_b32 s62, v153\n"
    "s_nop 0\n"
    "s_max_u32 s59, s55, s62\n"
    "s_cmp_lg_u32 s59, -1\n"
    "s_nop 1\n"
    "s_cbranch_scc1 hw_rs_ok_%=\n"
    "s_sleep 4\n"
    "s_add_u32 s60, s60, 1\n"
    "s_cmp_gt_u32 s60, 65536\n"
    "s_cbranch_scc1 hw_abort_%=\n"
    "s_and_b32 s59, s60, 63\n"
    "s_cbranch_scc1 hw_rs_%=\n"
    "v_mov_b32 v152, 1024\n"
    "buffer_load_dword v152, v152, s[40:43], s58 offen sc1\n"
    "s_waitcnt vmcnt(0)\n"
    "v_readfirstlane_b32 s59, v152\n"
    "s_nop 0\n"
    "s_cmp_lg_u32 s59, 0\n"
    "s_nop 2\n"
    "s_cbranch_scc1 hw_end_%=\n"
    "s_branch hw_rs_%=\n"
    "hw_rs_ok_%=:\n"
    "s_add_u32 s59, s62, s67\n"
    "v_mov_b32 v152, s59\n"
    "s_add_u32 s59, s59, s66\n"
    "v_mov_b32 v153, s59\n"
    "s_memrealtime s[56:57]\n"
    "s_waitcnt lgkmcnt(0)\n"
    "s_sub_u32 s59, s62, s56\n"
    "s_cmp_gt_u32 s59, 1000\n"
    "s_cbranch_scc1 hw_wu_go_%=\n"
    "hw_wu_%=:\n"
    "s_memrealtime s[56:57]\n"
    "s_waitcnt lgkmcnt(0)\n"
    "s_sub_u32 s59, s62, s56\n"
    "s_cmp_gt_i32 s59, 15\n"
    "s_cbranch_scc1 hw_wu_%=\n"
    "s_memrealtime s[30:31]\n"
    "s_nop 1\n"
    "s_memrealtime s[90:91]\n"
    "s_nop 1\n"
    "s_memrealtime s[34:35]\n"
    "s_nop 1\n"
    "s_memrealtime s[36:37]\n"
    "s_nop 1\n"
    "s_memrealtime s[38:39]\n"
    "s_nop 1\n"
    "s_memrealtime s[94:95]\n"
    "s_nop 1\n"
    "s_memrealtime s[96:97]\n"
    "s_nop 1\n"
    "s_memrealtime s[98:99]\n"
    "s_nop 1\n"
    "s_getpc_b64 s[56:57]\n"
    "hw_wu_pc_%=:\n"
    "s_waitcnt lgkmcnt(0)\n"
    "s_add_u32 s90, s90, s34\n"
    "s_add_u32 s90, s90, s36\n"
    "s_add_u32 s90, s90, s38\n"
    "s_add_u32 s90, s90, s94\n"
    "s_add_u32 s90, s90, s96\n"
    "s_add_u32 s90, s90, s98\n"
    "s_mul_i32 s34, s30, 7\n"
    "s_sub_u32 s90, s90, s34\n"
    "s_mul_i32 s90, s90, 3\n"
    "s_sub_u32 s59, s62, s30\n"
    "s_mul_i32 s59, s59, 23\n"
    "s_sub_u32 s59, s59, s90\n"
    "s_sub_u32 s59, s59, 170\n"
    "s_max_i32 s59, s59, 0\n"
    "s_min_i32 s59, s59, 252\n"
    "s_andn2_b32 s59, s59, 3\n"
    "s_sub_u32 s59, hw_wu_e_%=-hw_wu_pc_%=, s59\n"
    "s_add_u32 s56, s56, s59\n"
    "s_addc_u32 s57, s57, 0\n"
    "s_setpc_b64 s[56:57]\n"
    "s_nop 0\n"
    "s_nop 0\n"
    "s_nop 0\n"
    "s_nop 0\n"
    "s_nop 0\n"
    "s_nop 0\n"
    "s_nop 0\n"
    "s_nop 0\n"
    "s_nop 0\n"
    "s_nop 0\n"
    "s_nop 0\n"
    "s_nop 0\n"
    "s_nop 0\n"
    "s_nop 0\n"
    "s_nop 0\n"
    "s_nop 0\n"
    "s_nop 0\n"
    "s_nop 0\n"
    "s_nop 0\n"
    "s_nop 0\n"
    "s_nop 0\n"
    "s_nop 0\n"
    "s_nop 0\n"
    "s_nop 0\n"
    "s_nop 0\n"
    "s_nop 0\n"
    "s_nop 0\n"
    "s_nop 0\n"
    "s_nop 0\n"
    "s_nop 0\n"
    "s_nop 0\n"
    "s_nop 0\n"
    "s_nop 0\n"
    "s_nop 0\n"
    "s_nop 0\n"
    "s_nop 0\n"
    "s_nop 0\n"
    "s_nop 0\n"
    "s_nop 0\n"
    "s_nop 0\n"
    "s_nop 0\n"
    "s_nop 0\n"
    "s_nop 0\n"
    "s_nop 0\n"
    "s_nop 0\n"
    "s_nop 0\n"
    "s_nop 0\n"
    "s_nop 0\n"
    "s_nop 0\n"
    "s_nop 0\n"
    "s_nop 0\n"
    "s_nop 0\n"
    "s_nop 0\n"
    "s_nop 0\n"
    "s_nop 0\n"
    "s_nop 0\n"
    "s_nop 0\n"
    "s_nop 0\n"
    "s_nop 0\n"
    "s_nop 0\n"
    "s_nop 0\n"
    "s_nop 0\n"
    "s_nop 0\n"
    "hw_wu_e_%=:\n"
    "hw_wu_go_%=:\n"
    "s_barrier\n"
    "hw_go_%=:\n"
    "s_mov_b32 s60, 0\n"
    "hw_retrya_%=:\n"
    "buffer_load_dwordx4 v[16:19], v158, s[40:43], s20 offen sc1\n"
    "ds_read_b128 v[104:107], v5 offset:0\n"
    "s_nop 6\n"
    "buffer_load_dwordx4 v[20:23], v158, s[40:43], s21 offen sc1\n"
    "ds_read_b128 v[108:111], v5 offset:1024\n"
    "s_nop 6\n"
    "buffer_load_dwordx4 v[24:27], v158, s[40:43], s22 offen sc1\n"
    "ds_read_b128 v[112:115], v5 offset:2048\n"
    "s_nop 6\n"
    "buffer_load_dwordx4 v[28:31], v158, s[40:43], s23 offen sc1\n"
    "ds_read_b128 v[116:119], v5 offset:3072\n"
    "s_nop 6\n"
    "buffer_load_dwordx4 v[32:35], v158, s[40:43], s24 offen sc1\n"
    "ds_read_b128 v[120:123], v5 offset:4096\n"
    "s_nop 6\n"
    "buffer_load_dwordx4 v[36:39], v158, s[40:43], s25 offen sc1\n"
    "ds_read_b128 v[124:127], v5 offset:5120\n"
    "s_nop 6\n"
    "buffer_load_dwordx4 v[40:43], v158, s[40:43], s26 offen sc1\n"
    "ds_read_b128 v[128:131], v5 offset:6144\n"
    "s_nop 6\n"
    "buffer_load_dwordx4 v[44:47], v158, s[40:43], s27 offen sc1\n"
    "ds_read_b128 v[132:135], v5 offset:7168\n"
    "s_nop 6\n"
    "buffer_load_dwordx4 v[48:51], v158, s[40:43], s28 offen sc1\n"
    "s_nop 7\n"
    "buffer_load_dwordx4 v[52:55], v158, s[40:43], s29 offen sc1\n"
    "s_waitcnt lgkmcnt(0)\n"
    "s_waitcnt vmcnt(9)\n"
    "v_smfmac_f32_16x16x64_bf16 v[68:71], v[16:19], v[80:87], v1\n"
    "ds_read_b128 v[80:83], v2 offset:6144\n"
    "v_smfmac_f32_16x16x64_bf16 v[56:59], v[16:19], a[0:7], v1\n"
    "ds_read_b128 v[84:87], v2 offset:7168\n"
    "v_smfmac_f32_16x16x64_bf16 v[64:67], v[16:19], a[8:15], v1\n"
    "v_smfmac_f32_16x16x64_bf16 v[60:63], v[16:19], a[16:23], v1\n"
    "s_waitcnt vmcnt(8)\n"
    "v_smfmac_f32_16x16x64_bf16 v[68:71], v[20:23], v[88:95], v1\n"
    "ds_read_b128 v[88:91], v2 offset:8192\n"
    "v_smfmac_f32_16x16x64_bf16 v[56:59], v[20:23], a[24:31], v1\n"
    "ds_read_b128 v[92:95], v2 offset:9216\n"
    "v_smfmac_f32_16x16x64_bf16 v[64:67], v[20:23], v[96:103], v1\n"
    "ds_read_b128 v[96:99], v2 offset:10240\n"
    "v_smfmac_f32_16x16x64_bf16 v[60:63], v[20:23], a[32:39], v1\n"
    "ds_read_b128 v[100:103], v2 offset:11264\n"
    "s_waitcnt vmcnt(7) lgkmcnt(4)\n"
    "v_smfmac_f32_16x16x64_bf16 v[68:71], v[24:27], v[80:87], v1\n"
    "ds_read_b128 v[80:83], v2 offset:12288\n"
    "v_smfmac_f32_16x16x64_bf16 v[56:59], v[24:27], a[40:47], v1\n"
    "ds_read_b128 v[84:87], v2 offset:13312\n"
    "v_smfmac_f32_16x16x64_bf16 v[64:67], v[24:27], a[48:55], v1\n"
    "v_smfmac_f32_16x16x64_bf16 v[60:63], v[24:27], a[56:63], v1\n"
    "v_max3_u32 v12, v16, v20, v24\n"
    "s_waitcnt vmcnt(6) lgkmcnt(4)\n"
    "v_smfmac_f32_16x16x64_bf16 v[68:71], v[28:31], v[88:95], v1\n"
    "ds_read_b128 v[88:91], v2 offset:14336\n"
    "v_smfmac_f32_16x16x64_bf16 v[56:59], v[28:31], a[64:71], v1\n"
    "ds_read_b128 v[92:95], v2 offset:15360\n"
    "v_smfmac_f32_16x16x64_bf16 v[64:67], v[28:31], a[72:79], v1\n"
    "v_cmp_eq_u32 vcc, -1, v12\n"
    "v_smfmac_f32_16x16x64_bf16 v[60:63], v[28:31], a[80:87], v1\n"
    "s_cbranch_vccnz hw_polla_%=\n"
    "s_waitcnt vmcnt(5) lgkmcnt(4)\n"
    "v_smfmac_f32_16x16x64_bf16 v[68:71], v[32:35], v[96:103], v1\n"
    "ds_read_b128 v[96:99], v2 offset:16384\n"
    "v_smfmac_f32_16x16x64_bf16 v[56:59], v[32:35], a[88:95], v1\n"
    "ds_read_b128 v[100:103], v2 offset:17408\n"
    "s_waitcnt lgkmcnt(4)\n"
    "v_smfmac_f32_16x16x64_bf16 v[64:67], v[32:35], v[80:87], v1\n"
    "ds_read_b128 v[80:83], v2 offset:18432\n"
    "v_smfmac_f32_16x16x64_bf16 v[60:63], v[32:35], a[96:103], v1\n"
    "ds_read_b128 v[84:87], v2 offset:19456\n"
    "s_waitcnt vmcnt(4) lgkmcnt(4)\n"
    "v_smfmac_f32_16x16x64_bf16 v[68:71], v[36:39], v[88:95], v1\n"
    "ds_read_b128 v[88:91], v2 offset:20480\n"
    "v_smfmac_f32_16x16x64_bf16 v[56:59], v[36:39], a[104:111], v1\n"
    "ds_read_b128 v[92:95], v2 offset:21504\n"
    "v_smfmac_f32_16x16x64_bf16 v[64:67], v[36:39], a[112:119], v1\n"
    "v_smfmac_f32_16x16x64_bf16 v[60:63], v[36:39], a[120:127], v1\n"
    "s_waitcnt vmcnt(3) lgkmcnt(4)\n"
    "v_smfmac_f32_16x16x64_bf16 v[68:71], v[40:43], v[96:103], v1\n"
    "ds_read_b128 v[96:99], v2 offset:22528\n"
    "v_smfmac_f32_16x16x64_bf16 v[56:59], v[40:43], a[128:135], v1\n"
    "ds_read_b128 v[100:103], v2 offset:23552\n"
    "v_smfmac_f32_16x16x64_bf16 v[64:67], v[40:43], a[136:143], v1\n"
    "v_smfmac_f32_16x16x64_bf16 v[60:63], v[40:43], a[144:151], v1\n"
    "s_waitcnt vmcnt(2) lgkmcnt(4)\n"
    "v_smfmac_f32_16x16x64_bf16 v[68:71], v[44:47], v[80:87], v1\n"
    "ds_read_b128 v[80:83], v2 offset:24576\n"
    "v_smfmac_f32_16x16x64_bf16 v[56:59], v[44:47], a[152:159], v1\n"
    "ds_read_b128 v[84:87], v2 offset:25600\n"
    "s_waitcnt lgkmcnt(4)\n"
    "v_smfmac_f32_16x16x64_bf16 v[64:67], v[44:47], v[88:95], v1\n"
    "ds_read_b128 v[88:91], v2 offset:26624\n"
    "v_smfmac_f32_16x16x64_bf16 v[60:63], v[44:47], a[160:167], v1\n"
    "ds_read_b128 v[92:95], v2 offset:27648\n"
    "s_waitcnt vmcnt(1) lgkmcnt(4)\n"
    "v_smfmac_f32_16x16x64_bf16 v[68:71], v[48:51], v[96:103], v1\n"
    "ds_read_b128 v[96:99], v2 offset:28672\n"
    "v_smfmac_f32_16x16x64_bf16 v[56:59], v[48:51], a[168:175], v1\n"
    "ds_read_b128 v[100:103], v2 offset:29696\n"
    "v_smfmac_f32_16x16x64_bf16 v[64:67], v[48:51], a[176:183], v1\n"
    "v_smfmac_f32_16x16x64_bf16 v[60:63], v[48:51], a[184:191], v1\n"
    "s_waitcnt vmcnt(0) lgkmcnt(4)\n"
    "v_smfmac_f32_16x16x64_bf16 v[68:71], v[52:55], v[80:87], v1\n"
    "ds_read_b128 v[80:83], v2 offset:30720\n"
    "v_smfmac_f32_16x16x64_bf16 v[56:59], v[52:55], a[192:199], v1\n"
    "ds_read_b128 v[84:87], v2 offset:31744\n"
    "v_smfmac_f32_16x16x64_bf16 v[64:67], v[52:55], a[200:207], v1\n"
    "v_smfmac_f32_16x16x64_bf16 v[60:63], v[52:55], a[208:215], v1\n"
    "v_smfmac_f32_16x16x64_bf16 v[72:75], v[16:19], a[216:223], v1\n"
    "v_mov_b32 v13, v13\n"
    "v_mov_b32 v13, v13\n"
    "v_mov_b32 v13, v13\n"
    "v_mov_b32 v13, v13\n"
    "v_mov_b32 v13, v13\n"
    "v_mov_b32 v13, v13\n"
    "v_mov_b32 v13, v13\n"
    "v_permlane32_swap_b32 v56, v60\n"
    "v_permlane32_swap_b32 v57, v61\n"
    "s_waitcnt lgkmcnt(4)\n"
    "v_smfmac_f32_16x16x64_bf16 v[76:79], v[16:19], v[88:95], v1\n"
    "v_permlane32_swap_b32 v58, v62\n"
    "v_permlane32_swap_b32 v59, v63\n"
    "v_smfmac_f32_16x16x64_bf16 v[72:75], v[20:23], a[224:231], v1\n"
    "v_add_f32 v56, v56, v60\n"
    "v_add_f32 v57, v57, v61\n"
    "s_waitcnt lgkmcnt(2)\n"
    "v_smfmac_f32_16x16x64_bf16 v[76:79], v[20:23], v[96:103], v1\n"
    "v_add_f32 v58, v58, v62\n"
    "v_add_f32 v59, v59, v63\n"
    "v_smfmac_f32_16x16x64_bf16 v[72:75], v[24:27], a[232:239], v1\n"
    "v_cmp_u_f32 vcc, v56, v57\n"
    "v_cmp_u_f32 s[56:57], v58, v59\n"
    "s_waitcnt lgkmcnt(0)\n"
    "v_smfmac_f32_16x16x64_bf16 v[76:79], v[24:27], v[80:87], v1\n"
    "s_or_b64 vcc, vcc, s[56:57]\n"
    "s_cbranch_vccnz hw_faila_%=\n"
    "hw_resumea_%=:\n"
    "buffer_store_dwordx2 v[152:153], v7, s[40:43], s63 offen sc1\n"
    "v_cvt_pk_bf16_f32 v60, v56, v57\n"
    "v_smfmac_f32_16x16x64_bf16 v[72:75], v[28:31], a[240:247], v1\n"
    "v_cvt_pk_bf16_f32 v61, v58, v59\n"
    "ds_write_b64 v3, v[60:61] offset:0\n"
    "v_smfmac_f32_16x16x64_bf16 v[76:79], v[28:31], a[248:255], v1\n"
    "v_permlane32_swap_b32 v64, v68\n"
    "v_permlane32_swap_b32 v65, v69\n"
    "v_smfmac_f32_16x16x64_bf16 v[72:75], v[32:35], v[160:167], v1\n"
    "v_permlane32_swap_b32 v66, v70\n"
    "v_permlane32_swap_b32 v67, v71\n"
    "v_smfmac_f32_16x16x64_bf16 v[76:79], v[32:35], v[168:175], v1\n"
    "v_add_f32 v64, v64, v68\n"
    "v_smfmac_f32_16x16x64_bf16 v[72:75], v[36:39], v[176:183], v1\n"
    "v_add_f32 v65, v65, v69\n"
    "v_smfmac_f32_16x16x64_bf16 v[76:79], v[36:39], v[184:191], v1\n"
    "v_add_f32 v66, v66, v70\n"
    "v_smfmac_f32_16x16x64_bf16 v[72:75], v[40:43], v[192:199], v1\n"
    "v_add_f32 v67, v67, v71\n"
    "v_smfmac_f32_16x16x64_bf16 v[76:79], v[40:43], v[200:207], v1\n"
    "v_cvt_pk_bf16_f32 v68, v64, v65\n"
    "v_smfmac_f32_16x16x64_bf16 v[72:75], v[44:47], v[208:215], v1\n"
    "v_cvt_pk_bf16_f32 v69, v66, v67\n"
    "v_smfmac_f32_16x16x64_bf16 v[76:79], v[44:47], v[216:223], v1\n"
    "ds_write_b64 v3, v[68:69] offset:512\n"
    "v_smfmac_f32_16x16x64_bf16 v[72:75], v[48:51], v[224:231], v1\n"
    "ds_read_b128 v[16:19], v5 offset:8192\n"
    "v_smfmac_f32_16x16x64_bf16 v[76:79], v[48:51], v[232:239], v1\n"
    "ds_read_b128 v[20:23], v5 offset:9216\n"
    "v_smfmac_f32_16x16x64_bf16 v[72:75], v[52:55], v[240:247], v1\n"
    "ds_read_b128 v[24:27], v5 offset:10240\n"
    "v_smfmac_f32_16x16x64_bf16 v[76:79], v[52:55], v[248:255], v1\n"
    "ds_read_b128 v[28:31], v5 offset:11264\n"
    "s_waitcnt lgkmcnt(4)\n"
    "s_barrier\n"
    "ds_read_b64_tr_b16 v[136:137], v4 offset:0\n"
    "ds_read_b64_tr_b16 v[138:139], v4 offset:512\n"
    "v_mov_b32 v13, v13\n"
    "ds_read_b64_tr_b16 v[140:141], v4 offset:1536\n"
    "ds_read_b64_tr_b16 v[142:143], v4 offset:2048\n"
    "v_mov_b32 v13, v13\n"
    "ds_read_b64_tr_b16 v[144:145], v4 offset:3072\n"
    "ds_read_b64_tr_b16 v[146:147], v4 offset:3584\n"
    "v_mov_b32 v13, v13\n"
    "ds_read_b64_tr_b16 v[148:149], v4 offset:4608\n"
    "ds_read_b64_tr_b16 v[150:151], v4 offset:5120\n"
    "v_mov_b32 v13, v13\n"
    "v_permlane32_swap_b32 v72, v76\n"
    "v_permlane32_swap_b32 v73, v77\n"
    "v_permlane32_swap_b32 v74, v78\n"
    "v_permlane32_swap_b32 v75, v79\n"
    "v_pk_add_f32 v[72:73], v[72:73], v[76:77]\n"
    "v_pk_add_f32 v[74:75], v[74:75], v[78:79]\n"
    "v_cvt_pk_bf16_f32 v76, v72, v73\n"
    "v_cvt_pk_bf16_f32 v77, v74, v75\n"
    "ds_write_b64 v3, v[76:77] offset:1024\n"
    "s_waitcnt lgkmcnt(7)\n"
    "v_mfma_f32_16x16x32_bf16 v[56:59], v[104:107], v[136:139], v[8:11]\n"
    "v_mfma_f32_16x16x32_bf16 v[60:63], v[108:111], v[136:139], v[8:11]\n"
    "v_mfma_f32_16x16x32_bf16 v[64:67], v[136:139], v[136:139], 0\n"
    "s_waitcnt lgkmcnt(5)\n"
    "v_mfma_f32_16x16x32_bf16 v[56:59], v[112:115], v[140:143], v[56:59]\n"
    "s_waitcnt lgkmcnt(0)\n"
    "s_barrier\n"
    "ds_read_b64_tr_b16 v[32:33], v4 offset:1024\n"
    "ds_read_b64_tr_b16 v[34:35], v4 offset:2560\n"
    "ds_read_b64_tr_b16 v[36:37], v4 offset:4096\n"
    "ds_read_b64_tr_b16 v[38:39], v4 offset:5632\n"
    "v_mfma_f32_16x16x32_bf16 v[60:63], v[116:119], v[140:143], v[60:63]\n"
    "v_mfma_f32_16x16x32_bf16 v[64:67], v[140:143], v[140:143], v[64:67]\n"
    "v_mfma_f32_16x16x32_bf16 v[56:59], v[120:123], v[144:147], v[56:59]\n"
    "v_mfma_f32_16x16x32_bf16 v[60:63], v[124:127], v[144:147], v[60:63]\n"
    "v_mfma_f32_16x16x32_bf16 v[64:67], v[144:147], v[144:147], v[64:67]\n"
    "v_mfma_f32_16x16x32_bf16 v[56:59], v[128:131], v[148:151], v[56:59]\n"
    "v_mfma_f32_16x16x32_bf16 v[60:63], v[132:135], v[148:151], v[60:63]\n"
    "v_mfma_f32_16x16x32_bf16 v[64:67], v[148:151], v[148:151], v[64:67]\n"
    "s_waitcnt lgkmcnt(2)\n"
    "v_mfma_f32_16x16x32_bf16 v[64:67], v[32:35], v[32:35], v[64:67]\n"
    "s_waitcnt lgkmcnt(0)\n"
    "v_mfma_f32_16x16x32_bf16 v[64:67], v[36:39], v[36:39], v[64:67]\n"
    "v_mfma_f32_16x16x32_bf16 v[56:59], v[16:19], v[32:35], v[56:59]\n"
    "v_mfma_f32_16x16x32_bf16 v[56:59], v[24:27], v[36:39], v[56:59]\n"
    "v_mfma_f32_16x16x32_bf16 v[60:63], v[20:23], v[32:35], v[60:63]\n"
    "v_mfma_f32_16x16x32_bf16 v[60:63], v[28:31], v[36:39], v[60:63]\n"
    "v_mov_b32 v13, v13\n"
    "v_mov_b32 v13, v13\n"
    "v_mov_b32 v13, v13\n"
    "v_mov_b32 v13, v13\n"
    "v_permlane16_swap_b32 v64, v72\n"
    "v_permlane16_swap_b32 v65, v73\n"
    "v_permlane16_swap_b32 v66, v74\n"
    "v_permlane16_swap_b32 v67, v75\n"
    "v_mov_b32_dpp v56, v64 row_shr:8 row_mask:0x4 bank_mask:0xc\n"
    "v_mov_b32_dpp v57, v65 row_shr:8 row_mask:0x4 bank_mask:0xc\n"
    "v_mov_b32_dpp v58, v66 row_shr:8 row_mask:0x4 bank_mask:0xc\n"
    "v_mov_b32_dpp v59, v67 row_shr:8 row_mask:0x4 bank_mask:0xc\n"
    "v_mov_b32_dpp v60, v72 row_shr:8 row_mask:0x4 bank_mask:0xc\n"
    "v_mov_b32_dpp v61, v73 row_shr:8 row_mask:0x4 bank_mask:0xc\n"
    "v_mov_b32_dpp v62, v74 row_shr:8 row_mask:0x4 bank_mask:0xc\n"
    "v_mov_b32_dpp v63, v75 row_shr:8 row_mask:0x4 bank_mask:0xc\n"
    "v_max3_f32 v68, v60, v61, v62\n"
    "v_max3_f32 v69, v56, v57, v58\n"
    "v_max3_f32 v68, v68, v63, v59\n"
    "v_max_f32 v68, v68, v69\n"
    "s_nop 1\n"
    "v_max_f32_dpp v68, v68, v68 row_ror:8 row_mask:0xf bank_mask:0xf\n"
    "v_mov_b32 v69, v68\n"
    "s_nop 1\n"
    "v_permlane16_swap_b32 v68, v69\n"
    "v_max_f32 v68, v68, v69\n"
    "v_mov_b32 v69, v68\n"
    "s_nop 1\n"
    "v_permlane32_swap_b32 v68, v69\n"
    "v_max_f32 v68, v68, v69\n"
    "v_mul_f32 v68, 0xbdd53b95, v68\n"
    "v_pk_fma_f32 v[56:57], v[56:57], s[64:65], v[68:69] op_sel_hi:[1,1,0]\n"
    "v_pk_fma_f32 v[58:59], v[58:59], s[64:65], v[68:69] op_sel_hi:[1,1,0]\n"
    "v_pk_fma_f32 v[60:61], v[60:61], s[64:65], v[68:69] op_sel_hi:[1,1,0]\n"
    "v_pk_fma_f32 v[62:63], v[62:63], s[64:65], v[68:69] op_sel_hi:[1,1,0]\n"
    "v_exp_f32 v56, v56\n"
    "v_exp_f32 v57, v57\n"
    "v_exp_f32 v58, v58\n"
    "v_exp_f32 v59, v59\n"
    "v_exp_f32 v60, v60\n"
    "v_exp_f32 v61, v61\n"
    "v_exp_f32 v62, v62\n"
    "v_exp_f32 v63, v63\n"
    "v_pk_add_f32 v[64:65], v[56:57], v[58:59]\n"
    "v_pk_add_f32 v[66:67], v[60:61], v[62:63]\n"
    "v_pk_add_f32 v[64:65], v[64:65], v[66:67]\n"
    "v_add_f32 v68, v64, v65\n"
    "s_nop 1\n"
    "v_add_f32_dpp v68, v68, v68 row_ror:8 row_mask:0xf bank_mask:0xf\n"
    "v_mov_b32 v69, v68\n"
    "s_nop 1\n"
    "v_permlane16_swap_b32 v68, v69\n"
    "v_add_f32 v68, v68, v69\n"
    "v_mov_b32 v69, v68\n"
    "s_nop 1\n"
    "v_permlane32_swap_b32 v68, v69\n"
    "v_add_f32 v68, v68, v69\n"
    "v_rcp_f32 v68, v68\n"
    "s_nop 0\n"
    "v_pk_mul_f32 v[56:57], v[56:57], v[68:69] op_sel_hi:[1,0]\n"
    "v_pk_mul_f32 v[58:59], v[58:59], v[68:69] op_sel_hi:[1,0]\n"
    "v_pk_mul_f32 v[60:61], v[60:61], v[68:69] op_sel_hi:[1,0]\n"
    "v_pk_mul_f32 v[62:63], v[62:63], v[68:69] op_sel_hi:[1,0]\n"
    "v_cvt_pk_bf16_f32 v64, v56, v57\n"
    "v_cvt_pk_bf16_f32 v65, v58, v59\n"
    "v_cvt_pk_bf16_f32 v66, v60, v61\n"
    "v_cvt_pk_bf16_f32 v67, v62, v63\n"
    "buffer_store_dwordx4 v[64:67], v6, s[40:43], s53 offen sc1\n"
    "buffer_store_dwordx4 v[64:67], v6, s[40:43], s50 offen sc1\n"
    "buffer_store_dwordx4 v[64:67], v6, s[40:43], s51 offen sc1\n"
    "buffer_store_dwordx4 v[64:67], v6, s[40:43], s52 offen sc1\n"
    "v_add_u32 v3, s61, v3\n"
    "v_add_u32 v4, s61, v4\n"
    "s_sub_u32 s61, 0, s61\n"
    "s_add_u32 s47, s47, 1\n"
    "s_cmp_lt_u32 s47, 64\n"
    "s_cbranch_scc1 hw_loopa_%=\n"
    "s_branch hw_end_%=\n"
    "hw_faila_%=:\n"
    "v_max_u32 v12, v16, v17\n"
    "v_max3_u32 v12, v18, v19, v12\n"
    "v_max3_u32 v12, v20, v21, v12\n"
    "v_max3_u32 v12, v22, v23, v12\n"
    "v_max3_u32 v12, v24, v25, v12\n"
    "v_max3_u32 v12, v26, v27, v12\n"
    "v_max3_u32 v12, v28, v29, v12\n"
    "v_max3_u32 v12, v30, v31, v12\n"
    "v_max3_u32 v12, v32, v33, v12\n"
    "v_max3_u32 v12, v34, v35, v12\n"
    "v_max3_u32 v12, v36, v37, v12\n"
    "v_max3_u32 v12, v38, v39, v12\n"
    "v_max3_u32 v12, v40, v41, v12\n"
    "v_max3_u32 v12, v42, v43, v12\n"
    "v_max3_u32 v12, v44, v45, v12\n"
    "v_max3_u32 v12, v46, v47, v12\n"
    "v_max3_u32 v12, v48, v49, v12\n"
    "v_max3_u32 v12, v50, v51, v12\n"
    "v_max3_u32 v12, v52, v53, v12\n"
    "v_max3_u32 v12, v54, v55, v12\n"
    "v_cmp_eq_u32 vcc, -1, v12\n"
    "s_nop 4\n"
    "s_cbranch_vccz hw_resumea_%=\n"
    "hw_polla_%=:\n"
    "s_nop 7\n"
    "s_waitcnt lgkmcnt(0)\n"
    "v_mov_b32 v56, 0\n"
    "v_mov_b32 v57, 0\n"
    "v_mov_b32 v58, 0\n"
    "v_mov_b32 v59, 0\n"
    "v_mov_b32 v60, 0\n"
    "v_mov_b32 v61, 0\n"
    "v_mov_b32 v62, 0\n"
    "v_mov_b32 v63, 0\n"
    "v_mov_b32 v64, 0\n"
    "v_mov_b32 v65, 0\n"
    "v_mov_b32 v66, 0\n"
    "v_mov_b32 v67, 0\n"
    "v_mov_b32 v68, 0\n"
    "v_mov_b32 v69, 0\n"
    "v_mov_b32 v70, 0\n"
    "v_mov_b32 v71, 0\n"
    "v_mov_b32 v72, 0\n"
    "v_mov_b32 v73, 0\n"
    "v_mov_b32 v74, 0\n"
    "v_mov_b32 v75, 0\n"
    "v_mov_b32 v76, 0\n"
    "v_mov_b32 v77, 0\n"
    "v_mov_b32 v78, 0\n"
    "v_mov_b32 v79, 0\n"
    "ds_read_b128 v[80:83], v2 offset:0\n"
    "ds_read_b128 v[84:87], v2 offset:1024\n"
    "ds_read_b128 v[88:91], v2 offset:2048\n"
    "ds_read_b128 v[92:95], v2 offset:3072\n"
    "ds_read_b128 v[96:99], v2 offset:4096\n"
    "ds_read_b128 v[100:103], v2 offset:5120\n"
    "v_sub_u32 v12, v158, v0\n"
    "v_lshl_add_u32 v12, v15, 7, v12\n"
    "v_and_b32 v159, 15, v15\n"
    "v_sub_u32 v159, v159, v15\n"
    "v_lshl_add_u32 v159, v159, 7, v12\n"
    "s_movk_i32 s55, 0x4000\n"
    "s_waitcnt vmcnt(0) lgkmcnt(0)\n"
    "s_add_u32 s60, s60, 1\n"
    "s_cmp_gt_u32 s60, 65536\n"
    "s_cbranch_scc1 hw_abort_%=\n"
    "s_and_b32 s59, s60, 63\n"
    "s_cbranch_scc1 hw_againa_%=\n"
    "v_mov_b32 v13, 1024\n"
    "buffer_load_dword v13, v13, s[40:43], s58 offen sc1\n"
    "s_waitcnt vmcnt(0)\n"
    "v_readfirstlane_b32 s59, v13\n"
    "s_nop 0\n"
    "s_cmp_lg_u32 s59, 0\n"
    "s_nop 2\n"
    "s_cbranch_scc1 hw_end_%=\n"
    "s_branch hw_againa_%=\n"
    "hw_againa_%=:\n"
    "buffer_load_dword v16, v12, s[40:43], 0 offen sc1\n"
    "buffer_load_dword v17, v159, s[40:43], s55 offen sc1\n"
    "s_waitcnt vmcnt(0)\n"
    "v_max_u32 v16, v16, v17\n"
    "v_cmp_eq_u32 vcc, -1, v16\n"
    "s_nop 4\n"
    "s_cbranch_vccz hw_relocka_%=\n"
    "s_add_u32 s60, s60, 1\n"
    "s_cmp_gt_u32 s60, 65536\n"
    "s_cbranch_scc1 hw_abort_%=\n"
    "s_and_b32 s59, s60, 63\n"
    "s_cbranch_scc1 hw_againa_%=\n"
    "v_mov_b32 v13, 1024\n"
    "buffer_load_dword v13, v13, s[40:43], s58 offen sc1\n"
    "s_waitcnt vmcnt(0)\n"
    "v_readfirstlane_b32 s59, v13\n"
    "s_nop 0\n"
    "s_cmp_lg_u32 s59, 0\n"
    "s_nop 2\n"
    "s_cbranch_scc1 hw_end_%=\n"
    "s_branch hw_againa_%=\n"
    "hw_relocka_%=:\n"
    "s_memrealtime s[56:57]\n"
    "s_waitcnt lgkmcnt(0)\n"
    "s_cmp_lt_u32 s47, s75\n"
    "s_cbranch_scc1 hw_nobump_%=\n"
    "s_add_u32 s67, s67, s71\n"
    "s_add_u32 s66, s66, s72\n"
    "hw_nobump_%=:\n"
    "s_add_u32 s75, s47, 2\n"
    "s_max_u32 s75, s75, 6\n"
    "s_add_u32 s62, s56, s68\n"
    "s_add_u32 s59, s62, s67\n"
    "v_mov_b32 v152, s59\n"
    "s_add_u32 s59, s59, s66\n"
    "v_mov_b32 v153, s59\n"
    "s_nop 1\n"
    "s_branch hw_retrya_%=\n"
    "hw_wx_%=:\n"
    "v_lshrrev_b32 v6, 3, v15\n"
    "v_lshlrev_b32 v6, 8, v6\n"
    "v_add_u32 v6, s50, v6\n"
    "v_mov_b32 v13, s51\n"
    "v_cmp_gt_u32 vcc, 32, v15\n"
    "s_nop 1\n"
    "v_cndmask_b32 v6, v13, v6, vcc\n"
    "v_lshl_add_u32 v6, v153, 4, v6\n"
    "v_and_b32 v13, 0xffffe000, v6\n"
    "v_add_u32 v6, v6, v13\n"
    "v_cmp_gt_u32 vcc, 40, v15\n"
    "v_mov_b32 v13, 2147483632\n"
    "s_nop 0\n"
    "v_cndmask_b32 v6, v13, v6, vcc\n"
    "v_lshlrev_b32 v7, 7, v152\n"
    "v_lshl_add_u32 v7, v153, 4, v7\n"
    "v_cmp_eq_u32 vcc, 1, v154\n"
    "v_mov_b32 v13, 2147483632\n"
    "s_nop 0\n"
    "v_cndmask_b32 v7, v13, v7, vcc\n"
    "v_mov_b32 v14, 0\n"
    "s_lshl_b32 s48, s44, 7\n"
    "s_add_u32 s49, s48, 2048\n"
    "v_mov_b32 v104, s49\n"
    "s_add_u32 s49, s48, 14336\n"
    "v_mov_b32 v116, s49\n"
    "s_xor_b32 s49, s45, 1\n"
    "v_or_b32 v13, s49, v15\n"
    "v_cmp_eq_u32 vcc, 0, v13\n"
    "v_mov_b32 v13, 2147483632\n"
    "s_nop 0\n"
    "v_cndmask_b32 v116, v13, v116, vcc\n"
    "s_mov_b32 s48, 0\n"
    "s_cmp_eq_u32 s45, 2\n"
    "s_cselect_b32 s48, 2056, s48\n"
    "s_cmp_eq_u32 s45, 3\n"
    "s_cselect_b32 s48, 10240, s48\n"
    "v_add_u32 v105, s48, v5\n"
    "s_mov_b32 s48, 8\n"
    "s_cmp_eq_u32 s45, 2\n"
    "s_cselect_b32 s48, 8200, s48\n"
    "s_cmp_eq_u32 s45, 3\n"
    "s_cselect_b32 s48, 6144, s48\n"
    "v_add_u32 v106, s48, v5\n"
    "s_mov_b32 s48, 8192\n"
    "s_cmp_eq_u32 s45, 2\n"
    "s_cselect_b32 s48, 4096, s48\n"
    "s_cmp_eq_u32 s45, 3\n"
    "s_cselect_b32 s48, 6152, s48\n"
    "v_add_u32 v107, s48, v5\n"
    "s_mov_b32 s48, 2048\n"
    "s_cmp_eq_u32 s45, 2\n"
    "s_cselect_b32 s48, 4104, s48\n"
    "s_cmp_eq_u32 s45, 3\n"
    "s_cselect_b32 s48, 10248, s48\n"
    "v_add_u32 v108, s48, v5\n"
    "v_mov_b32 v112, -1\n"
    "v_mov_b32 v113, -1\n"
    "v_mov_b32 v114, -1\n"
    "v_mov_b32 v115, -1\n"
    "s_mul_i32 s62, s44, 12288\n"
    "s_lshl_b32 s49, s45, 12\n"
    "s_add_u32 s62, s62, s49\n"
    "s_add_u32 s62, s62, 62451712\n"
    "s_mov_b32 s55, 1\n"
    "s_mov_b32 s60, 0\n"
    "v_cmp_gt_u32 vcc, s55, v14\n"
    "s_nop 4\n"
    "s_cbranch_vccz hw_kx0_ok_%=\n"
    "hw_kx0_poll_%=:\n"
    "buffer_load_dword v14, v104, s[40:43], s58 offen sc1\n"
    "s_waitcnt vmcnt(0)\n"
    "v_cmp_gt_u32 vcc, s55, v14\n"
    "s_nop 4\n"
    "s_cbranch_vccz hw_kx0_ok_%=\n"
    "s_sleep 2\n"
    "s_add_u32 s60, s60, 1\n"
    "s_cmp_gt_u32 s60, 65536\n"
    "s_cbranch_scc1 hw_abort_%=\n"
    "s_and_b32 s59, s60, 63\n"
    "s_cbranch_scc1 hw_kx0_poll_%=\n"
    "v_mov_b32 v13, 1024\n"
    "buffer_load_dword v13, v13, s[40:43], s58 offen sc1\n"
    "s_waitcnt vmcnt(0)\n"
    "v_readfirstlane_b32 s59, v13\n"
    "s_nop 0\n"
    "s_cmp_lg_u32 s59, 0\n"
    "s_nop 2\n"
    "s_cbranch_scc1 hw_end_%=\n"
    "s_branch hw_kx0_poll_%=\n"
    "hw_kx0_ok_%=:\n"
    "buffer_load_dwordx4 v[136:139], v0, s[40:43], s62 offen sc1\n"
    "buffer_load_dwordx4 v[140:143], v0, s[40:43], s62 offen offset:1024 sc1\n"
    "buffer_load_dwordx4 v[144:147], v0, s[40:43], s62 offen offset:2048 sc1\n"
    "buffer_load_dwordx4 v[148:151], v0, s[40:43], s62 offen offset:3072 sc1\n"
    "s_waitcnt vmcnt(3)\n"
    "ds_write2st64_b64 v105, v[136:137], v[138:139] offset1:2\n"
    "s_waitcnt vmcnt(2)\n"
    "ds_write2st64_b64 v106, v[140:141], v[142:143] offset1:2\n"
    "s_waitcnt vmcnt(1)\n"
    "ds_write2st64_b64 v107, v[144:145], v[146:147] offset1:2\n"
    "s_waitcnt vmcnt(0)\n"
    "ds_write2st64_b64 v108, v[148:149], v[150:151] offset1:2\n"
    "hw_loopb_%=:\n"
    "s_add_u32 s62, s62, 393216\n"
    "s_add_u32 s55, s47, 2\n"
    "s_min_u32 s55, s55, 64\n"
    "s_mov_b32 s60, 0\n"
    "v_cmp_gt_u32 vcc, s55, v14\n"
    "s_nop 4\n"
    "s_cbranch_vccz hw_kx1_ok_%=\n"
    "hw_kx1_poll_%=:\n"
    "buffer_load_dword v14, v104, s[40:43], s58 offen sc1\n"
    "s_waitcnt vmcnt(0)\n"
    "v_cmp_gt_u32 vcc, s55, v14\n"
    "s_nop 4\n"
    "s_cbranch_vccz hw_kx1_ok_%=\n"
    "s_sleep 2\n"
    "s_add_u32 s60, s60, 1\n"
    "s_cmp_gt_u32 s60, 65536\n"
    "s_cbranch_scc1 hw_abort_%=\n"
    "s_and_b32 s59, s60, 63\n"
    "s_cbranch_scc1 hw_kx1_poll_%=\n"
    "v_mov_b32 v13, 1024\n"
    "buffer_load_dword v13, v13, s[40:43], s58 offen sc1\n"
    "s_waitcnt vmcnt(0)\n"
    "v_readfirstlane_b32 s59, v13\n"
    "s_nop 0\n"
    "s_cmp_lg_u32 s59, 0\n"
    "s_nop 2\n"
    "s_cbranch_scc1 hw_end_%=\n"
    "s_branch hw_kx1_poll_%=\n"
    "hw_kx1_ok_%=:\n"
    "s_and_b32 s48, s47, 3\n"
    "s_mul_i32 s53, s48, 196608\n"
    "s_lshl_b32 s50, s48, 2\n"
    "s_add_u32 s50, s50, s46\n"
    "s_mul_i32 s50, s50, 32768\n"
    "s_add_u32 s50, s50, 786432\n"
    "v_add_u32 v158, s50, v0\n"
    "s_add_u32 s50, s53, 49152\n"
    "s_add_u32 s51, s53, 98304\n"
    "s_add_u32 s52, s53, 147456\n"
    "v_mov_b32 v56, 0\n"
    "v_mov_b32 v57, 0\n"
    "v_mov_b32 v58, 0\n"
    "v_mov_b32 v59, 0\n"
    "v_mov_b32 v60, 0\n"
    "v_mov_b32 v61, 0\n"
    "v_mov_b32 v62, 0\n"
    "v_mov_b32 v63, 0\n"
    "v_mov_b32 v64, 0\n"
    "v_mov_b32 v65, 0\n"
    "v_mov_b32 v66, 0\n"
    "v_mov_b32 v67, 0\n"
    "v_mov_b32 v68, 0\n"
    "v_mov_b32 v69, 0\n"
    "v_mov_b32 v70, 0\n"
    "v_mov_b32 v71, 0\n"
    "v_mov_b32 v72, 0\n"
    "v_mov_b32 v73, 0\n"
    "v_mov_b32 v74, 0\n"
    "v_mov_b32 v75, 0\n"
    "v_mov_b32 v76, 0\n"
    "v_mov_b32 v77, 0\n"
    "v_mov_b32 v78, 0\n"
    "v_mov_b32 v79, 0\n"
    "ds_read_b128 v[80:83], v2 offset:0\n"
    "ds_read_b128 v[84:87], v2 offset:1024\n"
    "ds_read_b128 v[88:91], v2 offset:2048\n"
    "ds_read_b128 v[92:95], v2 offset:3072\n"
    "ds_read_b128 v[96:99], v2 offset:4096\n"
    "ds_read_b128 v[100:103], v2 offset:5120\n"
    "v_mov_b32 v117, s47\n"
    "s_waitcnt lgkmcnt(0)\n"
    "s_barrier\n"
    "s_mov_b32 s60, 0\n"
    "hw_retryb_%=:\n"
    "buffer_load_dwordx4 v[16:19], v158, s[40:43], s20 offen sc1\n"
    "s_nop 7\n"
    "buffer_load_dwordx4 v[20:23], v158, s[40:43], s21 offen sc1\n"
    "s_nop 7\n"
    "buffer_load_dwordx4 v[24:27], v158, s[40:43], s22 offen sc1\n"
    "s_nop 7\n"
    "buffer_load_dwordx4 v[28:31], v158, s[40:43], s23 offen sc1\n"
    "s_nop 7\n"
    "buffer_load_dwordx4 v[32:35], v158, s[40:43], s24 offen sc1\n"
    "s_nop 7\n"
    "buffer_load_dwordx4 v[36:39], v158, s[40:43], s25 offen sc1\n"
    "s_nop 7\n"
    "buffer_load_dwordx4 v[40:43], v158, s[40:43], s26 offen sc1\n"
    "s_nop 7\n"
    "buffer_load_dwordx4 v[44:47], v158, s[40:43], s27 offen sc1\n"
    "s_nop 7\n"
    "buffer_load_dwordx4 v[48:51], v158, s[40:43], s28 offen sc1\n"
    "s_nop 7\n"
    "buffer_load_dwordx4 v[52:55], v158, s[40:43], s29 offen sc1\n"
    "s_waitcnt vmcnt(9)\n"
    "v_smfmac_f32_16x16x64_bf16 v[68:71], v[16:19], v[80:87], v1\n"
    "ds_read_b128 v[80:83], v2 offset:6144\n"
    "v_smfmac_f32_16x16x64_bf16 v[56:59], v[16:19], a[0:7], v1\n"
    "ds_read_b128 v[84:87], v2 offset:7168\n"
    "v_smfmac_f32_16x16x64_bf16 v[64:67], v[16:19], a[8:15], v1\n"
    "v_smfmac_f32_16x16x64_bf16 v[60:63], v[16:19], a[16:23], v1\n"
    "s_waitcnt vmcnt(8)\n"
    "v_smfmac_f32_16x16x64_bf16 v[68:71], v[20:23], v[88:95], v1\n"
    "ds_read_b128 v[88:91], v2 offset:8192\n"
    "v_smfmac_f32_16x16x64_bf16 v[56:59], v[20:23], a[24:31], v1\n"
    "ds_read_b128 v[92:95], v2 offset:9216\n"
    "v_smfmac_f32_16x16x64_bf16 v[64:67], v[20:23], v[96:103], v1\n"
    "ds_read_b128 v[96:99], v2 offset:10240\n"
    "v_smfmac_f32_16x16x64_bf16 v[60:63], v[20:23], a[32:39], v1\n"
    "ds_read_b128 v[100:103], v2 offset:11264\n"
    "s_waitcnt vmcnt(7) lgkmcnt(4)\n"
    "v_smfmac_f32_16x16x64_bf16 v[68:71], v[24:27], v[80:87], v1\n"
    "ds_read_b128 v[80:83], v2 offset:12288\n"
    "v_smfmac_f32_16x16x64_bf16 v[56:59], v[24:27], a[40:47], v1\n"
    "ds_read_b128 v[84:87], v2 offset:13312\n"
    "v_smfmac_f32_16x16x64_bf16 v[64:67], v[24:27], a[48:55], v1\n"
    "v_smfmac_f32_16x16x64_bf16 v[60:63], v[24:27], a[56:63], v1\n"
    "v_max3_u32 v12, v16, v20, v24\n"
    "s_waitcnt vmcnt(6) lgkmcnt(4)\n"
    "v_smfmac_f32_16x16x64_bf16 v[68:71], v[28:31], v[88:95], v1\n"
    "ds_read_b128 v[88:91], v2 offset:14336\n"
    "v_smfmac_f32_16x16x64_bf16 v[56:59], v[28:31], a[64:71], v1\n"
    "ds_read_b128 v[92:95], v2 offset:15360\n"
    "v_smfmac_f32_16x16x64_bf16 v[64:67], v[28:31], a[72:79], v1\n"
    "v_cmp_eq_u32 vcc, -1, v12\n"
    "v_smfmac_f32_16x16x64_bf16 v[60:63], v[28:31], a[80:87], v1\n"
    "s_cbranch_vccnz hw_pollb_%=\n"
    "s_waitcnt vmcnt(5) lgkmcnt(4)\n"
    "v_smfmac_f32_16x16x64_bf16 v[68:71], v[32:35], v[96:103], v1\n"
    "ds_read_b128 v[96:99], v2 offset:16384\n"
    "v_smfmac_f32_16x16x64_bf16 v[56:59], v[32:35], a[88:95], v1\n"
    "ds_read_b128 v[100:103], v2 offset:17408\n"
    "s_waitcnt lgkmcnt(4)\n"
    "v_smfmac_f32_16x16x64_bf16 v[64:67], v[32:35], v[80:87], v1\n"
    "ds_read_b128 v[80:83], v2 offset:18432\n"
    "v_smfmac_f32_16x16x64_bf16 v[60:63], v[32:35], a[96:103], v1\n"
    "ds_read_b128 v[84:87], v2 offset:19456\n"
    "s_waitcnt vmcnt(4) lgkmcnt(4)\n"
    "v_smfmac_f32_16x16x64_bf16 v[68:71], v[36:39], v[88:95], v1\n"
    "ds_read_b128 v[88:91], v2 offset:20480\n"
    "v_smfmac_f32_16x16x64_bf16 v[56:59], v[36:39], a[104:111], v1\n"
    "ds_read_b128 v[92:95], v2 offset:21504\n"
    "v_smfmac_f32_16x16x64_bf16 v[64:67], v[36:39], a[112:119], v1\n"
    "buffer_load_dwordx4 v[136:139], v0, s[40:43], s62 offen offset:0 sc1\n"
    "v_smfmac_f32_16x16x64_bf16 v[60:63], v[36:39], a[120:127], v1\n"
    "buffer_load_dwordx4 v[140:143], v0, s[40:43], s62 offen offset:1024 sc1\n"
    "s_waitcnt vmcnt(5) lgkmcnt(4)\n"
    "v_smfmac_f32_16x16x64_bf16 v[68:71], v[40:43], v[96:103], v1\n"
    "ds_read_b128 v[96:99], v2 offset:22528\n"
    "v_smfmac_f32_16x16x64_bf16 v[56:59], v[40:43], a[128:135], v1\n"
    "ds_read_b128 v[100:103], v2 offset:23552\n"
    "v_smfmac_f32_16x16x64_bf16 v[64:67], v[40:43], a[136:143], v1\n"
    "buffer_load_dwordx4 v[144:147], v0, s[40:43], s62 offen offset:2048 sc1\n"
    "v_smfmac_f32_16x16x64_bf16 v[60:63], v[40:43], a[144:151], v1\n"
    "buffer_load_dwordx4 v[148:151], v0, s[40:43], s62 offen offset:3072 sc1\n"
    "s_waitcnt vmcnt(6) lgkmcnt(4)\n"
    "v_smfmac_f32_16x16x64_bf16 v[68:71], v[44:47], v[80:87], v1\n"
    "ds_read_b128 v[80:83], v2 offset:24576\n"
    "v_smfmac_f32_16x16x64_bf16 v[56:59], v[44:47], a[152:159], v1\n"
    "ds_read_b128 v[84:87], v2 offset:25600\n"
    "s_waitcnt lgkmcnt(4)\n"
    "v_smfmac_f32_16x16x64_bf16 v[64:67], v[44:47], v[88:95], v1\n"
    "ds_read_b128 v[88:91], v2 offset:26624\n"
    "v_smfmac_f32_16x16x64_bf16 v[60:63], v[44:47], a[160:167], v1\n"
    "ds_read_b128 v[92:95], v2 offset:27648\n"
    "s_waitcnt vmcnt(5) lgkmcnt(4)\n"
    "v_smfmac_f32_16x16x64_bf16 v[68:71], v[48:51], v[96:103], v1\n"
    "ds_read_b128 v[96:99], v2 offset:28672\n"
    "v_smfmac_f32_16x16x64_bf16 v[56:59], v[48:51], a[168:175], v1\n"
    "ds_read_b128 v[100:103], v2 offset:29696\n"
    "v_smfmac_f32_16x16x64_bf16 v[64:67], v[48:51], a[176:183], v1\n"
    "buffer_load_dword v14, v104, s[40:43], s58 offen sc1\n"
    "v_smfmac_f32_16x16x64_bf16 v[60:63], v[48:51], a[184:191], v1\n"
    "s_waitcnt vmcnt(5) lgkmcnt(4)\n"
    "v_smfmac_f32_16x16x64_bf16 v[68:71], v[52:55], v[80:87], v1\n"
    "ds_read_b128 v[80:83], v2 offset:30720\n"
    "v_smfmac_f32_16x16x64_bf16 v[56:59], v[52:55], a[192:199], v1\n"
    "ds_read_b128 v[84:87], v2 offset:31744\n"
    "v_smfmac_f32_16x16x64_bf16 v[64:67], v[52:55], a[200:207], v1\n"
    "v_smfmac_f32_16x16x64_bf16 v[60:63], v[52:55], a[208:215], v1\n"
    "v_smfmac_f32_16x16x64_bf16 v[72:75], v[16:19], a[216:223], v1\n"
    "v_mov_b32 v13, v13\n"
    "v_mov_b32 v13, v13\n"
    "v_mov_b32 v13, v13\n"
    "v_mov_b32 v13, v13\n"
    "v_mov_b32 v13, v13\n"
    "v_mov_b32 v13, v13\n"
    "v_mov_b32 v13, v13\n"
    "v_permlane32_swap_b32 v56, v60\n"
    "v_permlane32_swap_b32 v57, v61\n"
    "s_waitcnt lgkmcnt(4)\n"
    "v_smfmac_f32_16x16x64_bf16 v[76:79], v[16:19], v[88:95], v1\n"
    "v_permlane32_swap_b32 v58, v62\n"
    "v_permlane32_swap_b32 v59, v63\n"
    "v_smfmac_f32_16x16x64_bf16 v[72:75], v[20:23], a[224:231], v1\n"
    "v_add_f32 v56, v56, v60\n"
    "v_add_f32 v57, v57, v61\n"
    "s_waitcnt lgkmcnt(2)\n"
    "v_smfmac_f32_16x16x64_bf16 v[76:79], v[20:23], v[96:103], v1\n"
    "v_add_f32 v58, v58, v62\n"
    "v_add_f32 v59, v59, v63\n"
    "v_smfmac_f32_16x16x64_bf16 v[72:75], v[24:27], a[232:239], v1\n"
    "v_cmp_u_f32 vcc, v56, v57\n"
    "v_cmp_u_f32 s[56:57], v58, v59\n"
    "s_waitcnt lgkmcnt(0)\n"
    "v_smfmac_f32_16x16x64_bf16 v[76:79], v[24:27], v[80:87], v1\n"
    "s_or_b64 vcc, vcc, s[56:57]\n"
    "s_cbranch_vccnz hw_failb_%=\n"
    "hw_resumeb_%=:\n"
    "v_cvt_pk_bf16_f32 v60, v56, v57\n"
    "v_smfmac_f32_16x16x64_bf16 v[72:75], v[28:31], a[240:247], v1\n"
    "v_cvt_pk_bf16_f32 v61, v58, v59\n"
    "ds_write_b64 v3, v[60:61] offset:0\n"
    "v_smfmac_f32_16x16x64_bf16 v[76:79], v[28:31], a[248:255], v1\n"
    "v_permlane32_swap_b32 v64, v68\n"
    "v_permlane32_swap_b32 v65, v69\n"
    "v_smfmac_f32_16x16x64_bf16 v[72:75], v[32:35], v[160:167], v1\n"
    "v_permlane32_swap_b32 v66, v70\n"
    "v_permlane32_swap_b32 v67, v71\n"
    "v_smfmac_f32_16x16x64_bf16 v[76:79], v[32:35], v[168:175], v1\n"
    "v_add_f32 v64, v64, v68\n"
    "v_smfmac_f32_16x16x64_bf16 v[72:75], v[36:39], v[176:183], v1\n"
    "v_add_f32 v65, v65, v69\n"
    "v_smfmac_f32_16x16x64_bf16 v[76:79], v[36:39], v[184:191], v1\n"
    "v_add_f32 v66, v66, v70\n"
    "v_smfmac_f32_16x16x64_bf16 v[72:75], v[40:43], v[192:199], v1\n"
    "v_add_f32 v67, v67, v71\n"
    "v_smfmac_f32_16x16x64_bf16 v[76:79], v[40:43], v[200:207], v1\n"
    "v_cvt_pk_bf16_f32 v68, v64, v65\n"
    "v_smfmac_f32_16x16x64_bf16 v[72:75], v[44:47], v[208:215], v1\n"
    "v_cvt_pk_bf16_f32 v69, v66, v67\n"
    "v_smfmac_f32_16x16x64_bf16 v[76:79], v[44:47], v[216:223], v1\n"
    "ds_write_b64 v3, v[68:69] offset:512\n"
    "v_smfmac_f32_16x16x64_bf16 v[72:75], v[48:51], v[224:231], v1\n"
    "v_smfmac_f32_16x16x64_bf16 v[76:79], v[48:51], v[232:239], v1\n"
    "buffer_store_dword v117, v116, s[40:43], s58 offen sc1\n"
    "v_smfmac_f32_16x16x64_bf16 v[72:75], v[52:55], v[240:247], v1\n"
    "v_smfmac_f32_16x16x64_bf16 v[76:79], v[52:55], v[248:255], v1\n"
    "s_waitcnt lgkmcnt(0)\n"
    "s_barrier\n"
    "v_mov_b32 v13, v13\n"
    "v_mov_b32 v13, v13\n"
    "v_mov_b32 v13, v13\n"
    "v_mov_b32 v13, v13\n"
    "v_mov_b32 v13, v13\n"
    "v_mov_b32 v13, v13\n"
    "v_permlane32_swap_b32 v72, v76\n"
    "v_permlane32_swap_b32 v73, v77\n"
    "v_permlane32_swap_b32 v74, v78\n"
    "v_permlane32_swap_b32 v75, v79\n"
    "v_pk_add_f32 v[72:73], v[72:73], v[76:77]\n"
    "v_pk_add_f32 v[74:75], v[74:75], v[78:79]\n"
    "v_cvt_pk_bf16_f32 v76, v72, v73\n"
    "v_cvt_pk_bf16_f32 v77, v74, v75\n"
    "ds_write_b64 v3, v[76:77] offset:1024\n"
    "s_waitcnt lgkmcnt(0)\n"
    "s_barrier\n"
    "s_waitcnt vmcnt(5)\n"
    "ds_write2st64_b64 v105, v[136:137], v[138:139] offset1:2\n"
    "s_waitcnt vmcnt(4)\n"
    "ds_write2st64_b64 v106, v[140:141], v[142:143] offset1:2\n"
    "s_waitcnt vmcnt(3)\n"
    "ds_write2st64_b64 v107, v[144:145], v[146:147] offset1:2\n"
    "s_waitcnt vmcnt(2)\n"
    "ds_write2st64_b64 v108, v[148:149], v[150:151] offset1:2\n"
    "s_waitcnt vmcnt(1)\n"
    "s_cmp_lg_u32 s45, 1\n"
    "s_cbranch_scc1 hw_n1_%=\n"
    "ds_read_b64_tr_b16 v[16:17], v4 offset:0\n"
    "ds_read_b64_tr_b16 v[18:19], v4 offset:512\n"
    "ds_read_b64_tr_b16 v[20:21], v4 offset:1536\n"
    "ds_read_b64_tr_b16 v[22:23], v4 offset:2048\n"
    "ds_read_b64_tr_b16 v[24:25], v4 offset:3072\n"
    "ds_read_b64_tr_b16 v[26:27], v4 offset:3584\n"
    "ds_read_b64_tr_b16 v[28:29], v4 offset:4608\n"
    "ds_read_b64_tr_b16 v[30:31], v4 offset:5120\n"
    "ds_read_b64_tr_b16 v[32:33], v4 offset:1024\n"
    "ds_read_b64_tr_b16 v[34:35], v4 offset:2560\n"
    "ds_read_b64_tr_b16 v[36:37], v4 offset:4096\n"
    "ds_read_b64_tr_b16 v[38:39], v4 offset:5632\n"
    "s_waitcnt lgkmcnt(14)\n"
    "s_lshl_b32 s54, s47, 5\n"
    "s_add_u32 s54, s54, s44\n"
    "s_mul_i32 s54, s54, 3072\n"
    "s_add_u32 s54, s54, 217645056\n"
    "s_waitcnt lgkmcnt(10)\n"
    "buffer_store_dwordx4 v[16:19], v7, s[40:43], s54 offen sc1\n"
    "s_waitcnt lgkmcnt(8)\n"
    "buffer_store_dwordx4 v[20:23], v7, s[40:43], s54 offen offset:512 sc1\n"
    "s_waitcnt lgkmcnt(6)\n"
    "buffer_store_dwordx4 v[24:27], v7, s[40:43], s54 offen offset:1024 sc1\n"
    "s_waitcnt lgkmcnt(4)\n"
    "buffer_store_dwordx4 v[28:31], v7, s[40:43], s54 offen offset:1536 sc1\n"
    "s_waitcnt lgkmcnt(2)\n"
    "buffer_store_dwordx4 v[32:35], v7, s[40:43], s54 offen offset:2048 sc1\n"
    "s_waitcnt lgkmcnt(0)\n"
    "buffer_store_dwordx4 v[36:39], v7, s[40:43], s54 offen offset:2560 sc1\n"
    "s_nop 1\n"
    "hw_n1_%=:\n"
    "s_cmp_lg_u32 s45, 2\n"
    "s_cbranch_scc1 hw_n2_%=\n"
    "s_xor_b32 s48, s48, 2\n"
    "s_mul_i32 s54, s48, 196608\n"
    "buffer_store_dwordx4 v[112:115], v6, s[40:43], s54 offen sc1\n"
    "s_add_u32 s54, s54, 49152\n"
    "buffer_store_dwordx4 v[112:115], v6, s[40:43], s54 offen sc1\n"
    "s_add_u32 s54, s54, 49152\n"
    "buffer_store_dwordx4 v[112:115], v6, s[40:43], s54 offen sc1\n"
    "s_add_u32 s54, s54, 49152\n"
    "buffer_store_dwordx4 v[112:115], v6, s[40:43], s54 offen sc1\n"
    "s_add_u32 s54, s54, 49152\n"
    "s_nop 0\n"
    "hw_n2_%=:\n"
    "s_cmp_eq_u32 s47, 63\n"
    "s_cbranch_scc1 hw_fin_%=\n"
    "v_add_u32 v3, s61, v3\n"
    "v_add_u32 v4, s61, v4\n"
    "s_sub_u32 s61, 0, s61\n"
    "s_add_u32 s47, s47, 1\n"
    "s_branch hw_loopb_%=\n"
    "hw_failb_%=:\n"
    "v_max_u32 v12, v16, v17\n"
    "v_max3_u32 v12, v18, v19, v12\n"
    "v_max3_u32 v12, v20, v21, v12\n"
    "v_max3_u32 v12, v22, v23, v12\n"
    "v_max3_u32 v12, v24, v25, v12\n"
    "v_max3_u32 v12, v26, v27, v12\n"
    "v_max3_u32 v12, v28, v29, v12\n"
    "v_max3_u32 v12, v30, v31, v12\n"
    "v_max3_u32 v12, v32, v33, v12\n"
    "v_max3_u32 v12, v34, v35, v12\n"
    "v_max3_u32 v12, v36, v37, v12\n"
    "v_max3_u32 v12, v38, v39, v12\n"
    "v_max3_u32 v12, v40, v41, v12\n"
    "v_max3_u32 v12, v42, v43, v12\n"
    "v_max3_u32 v12, v44, v45, v12\n"
    "v_max3_u32 v12, v46, v47, v12\n"
    "v_max3_u32 v12, v48, v49, v12\n"
    "v_max3_u32 v12, v50, v51, v12\n"
    "v_max3_u32 v12, v52, v53, v12\n"
    "v_max3_u32 v12, v54, v55, v12\n"
    "v_cmp_eq_u32 vcc, -1, v12\n"
    "s_nop 4\n"
    "s_cbranch_vccz hw_resumeb_%=\n"
    "hw_pollb_%=:\n"
    "s_nop 7\n"
    "s_waitcnt lgkmcnt(0)\n"
    "v_mov_b32 v56, 0\n"
    "v_mov_b32 v57, 0\n"
    "v_mov_b32 v58, 0\n"
    "v_mov_b32 v59, 0\n"
    "v_mov_b32 v60, 0\n"
    "v_mov_b32 v61, 0\n"
    "v_mov_b32 v62, 0\n"
    "v_mov_b32 v63, 0\n"
    "v_mov_b32 v64, 0\n"
    "v_mov_b32 v65, 0\n"
    "v_mov_b32 v66, 0\n"
    "v_mov_b32 v67, 0\n"
    "v_mov_b32 v68, 0\n"
    "v_mov_b32 v69, 0\n"
    "v_mov_b32 v70, 0\n"
    "v_mov_b32 v71, 0\n"
    "v_mov_b32 v72, 0\n"
    "v_mov_b32 v73, 0\n"
    "v_mov_b32 v74, 0\n"
    "v_mov_b32 v75, 0\n"
    "v_mov_b32 v76, 0\n"
    "v_mov_b32 v77, 0\n"
    "v_mov_b32 v78, 0\n"
    "v_mov_b32 v79, 0\n"
    "ds_read_b128 v[80:83], v2 offset:0\n"
    "ds_read_b128 v[84:87], v2 offset:1024\n"
    "ds_read_b128 v[88:91], v2 offset:2048\n"
    "ds_read_b128 v[92:95], v2 offset:3072\n"
    "ds_read_b128 v[96:99], v2 offset:4096\n"
    "ds_read_b128 v[100:103], v2 offset:5120\n"
    "v_sub_u32 v12, v158, v0\n"
    "v_lshl_add_u32 v12, v15, 7, v12\n"
    "v_and_b32 v159, 15, v15\n"
    "v_sub_u32 v159, v159, v15\n"
    "v_lshl_add_u32 v159, v159, 7, v12\n"
    "s_movk_i32 s55, 0x4000\n"
    "s_waitcnt vmcnt(0) lgkmcnt(0)\n"
    "s_add_u32 s60, s60, 1\n"
    "s_cmp_gt_u32 s60, 65536\n"
    "s_cbranch_scc1 hw_abort_%=\n"
    "s_and_b32 s59, s60, 63\n"
    "s_cbranch_scc1 hw_againb_%=\n"
    "v_mov_b32 v13, 1024\n"
    "buffer_load_dword v13, v13, s[40:43], s58 offen sc1\n"
    "s_waitcnt vmcnt(0)\n"
    "v_readfirstlane_b32 s59, v13\n"
    "s_nop 0\n"
    "s_cmp_lg_u32 s59, 0\n"
    "s_nop 2\n"
    "s_cbranch_scc1 hw_end_%=\n"
    "s_branch hw_againb_%=\n"
    "hw_againb_%=:\n"
    "buffer_load_dword v16, v12, s[40:43], 0 offen sc1\n"
    "buffer_load_dword v17, v159, s[40:43], s55 offen sc1\n"
    "s_waitcnt vmcnt(0)\n"
    "v_max_u32 v16, v16, v17\n"
    "v_cmp_eq_u32 vcc, -1, v16\n"
    "s_nop 4\n"
    "s_cbranch_vccz hw_retryb_%=\n"
    "s_add_u32 s60, s60, 1\n"
    "s_cmp_gt_u32 s60, 65536\n"
    "s_cbranch_scc1 hw_abort_%=\n"
    "s_and_b32 s59, s60, 63\n"
    "s_cbranch_scc1 hw_againb_%=\n"
    "v_mov_b32 v13, 1024\n"
    "buffer_load_dword v13, v13, s[40:43], s58 offen sc1\n"
    "s_waitcnt vmcnt(0)\n"
    "v_readfirstlane_b32 s59, v13\n"
    "s_nop 0\n"
    "s_cmp_lg_u32 s59, 0\n"
    "s_nop 2\n"
    "s_cbranch_scc1 hw_end_%=\n"
    "s_branch hw_againb_%=\n"
    "hw_fin_%=:\n"
    "v_mov_b32 v13, 64\n"
    "s_waitcnt vmcnt(0)\n"
    "buffer_store_dword v13, v116, s[40:43], s58 offen sc1\n"
    "s_nop 1\n"
    "s_branch hw_end_%=\n"
    "hw_abort_%=:\n"
    "v_mov_b32 v13, 1\n"
    "v_mov_b32 v14, 1024\n"
    "buffer_store_dword v13, v14, s[40:43], s58 offen sc1\n"
    "hw_end_%=:\n"
    "s_endpgm\n"
    :: ROLE_INPUTS : ROLE_CLOBBERS);
}
DEV void role_col(unsigned lo, unsigned hi, int id, int w, int aux, const Tune& tn) {
    asm volatile(
    "s_mov_b32 s40, %0\n"
    "s_mov_b32 s41, %1\n"
    "s_mov_b32 s42, %2\n"
    "s_mov_b32 s43, %3\n"
    "s_mov_b32 s44, %4\n"
    "s_mov_b32 s45, %5\n"
    "s_mov_b32 s46, %6\n"
    "s_mov_b32 s66, %7\n"
    "s_mov_b32 s67, %8\n"
    "s_mov_b32 s68, %9\n"
    "s_mov_b32 s69, %10\n"
    "s_mov_b32 s70, %11\n"
    "s_mov_b32 s71, %12\n"
    "s_mov_b32 s72, %13\n"
    "s_mov_b32 s73, %14\n"
    "s_mov_b64 exec, -1\n"
    "v_mbcnt_lo_u32_b32 v15, -1, 0\n"
    "v_mbcnt_hi_u32_b32 v15, -1, v15\n"
    "v_lshlrev_b32 v0, 4, v15\n"
    "s_mov_b32 s58, 2097152\n"
    "s_mov_b32 s92, 0\n"
    "v_lshrrev_b32 v12, 4, v15\n"
    "v_and_b32 v13, 7, v15\n"
    "v_and_b32 v10, 8, v15\n"
    "v_cmp_eq_u32 vcc, 0, v10\n"
    "v_mov_b32 v10, 0xeeee\n"
    "v_mov_b32 v1, 0x4444\n"
    "v_cndmask_b32 v1, v10, v1, vcc\n"
    "s_lshl_b32 s48, s44, 7\n"
    "v_lshlrev_b32 v3, 4, v13\n"
    "v_add_u32 v3, s48, v3\n"
    "v_and_b32 v10, 0xffffe000, v3\n"
    "v_add_u32 v3, v3, v10\n"
    "v_lshl_add_u32 v3, v12, 15, v3\n"
    "v_mov_b32 v10, 2147483632\n"
    "v_cndmask_b32 v3, v10, v3, vcc\n"
    "s_mov_b32 s47, 0\n"
    "s_cmp_lg_u32 s45, 0\n"
    "s_cbranch_scc1 cw_wx_%=\n"
    "v_mov_b32 v5, 0\n"
    "v_cmp_eq_u32 vcc, 0, v15\n"
    "v_mov_b32 v10, 2147483632\n"
    "s_nop 0\n"
    "v_cndmask_b32 v5, v10, v5, vcc\n"
    "s_mov_b32 s65, 0\n"
    "v_and_b32 v2, 31, v15\n"
    "v_lshlrev_b32 v2, 7, v2\n"
    "v_mov_b32 v7, 0\n"
    "v_and_b32 v8, 31, v15\n"
    "v_lshlrev_b32 v8, 7, v8\n"
    "v_add_u32 v8, 10240, v8\n"
    "s_lshl_b32 s61, s44, 14\n"
    "s_add_u32 s61, s61, 112787456\n"
    "cw_loopa_%=:\n"
    "s_and_b32 s48, s47, 3\n"
    "s_lshl_b32 s50, s48, 2\n"
    "s_add_u32 s50, s50, s46\n"
    "s_mul_i32 s50, s50, 32768\n"
    "s_add_u32 s50, s50, 786432\n"
    "s_add_u32 s51, s50, 4096\n"
    "s_add_u32 s52, s50, 16384\n"
    "s_add_u32 s64, s65, s70\n"
    "s_add_u32 s55, s47, 1\n"
    "s_mov_b32 s60, 0\n"
    "v_cmp_gt_u32 vcc, s55, v7\n"
    "s_nop 4\n"
    "s_cbranch_vccz cw_ux_ok_%=\n"
    "cw_ux_poll_%=:\n"
    "buffer_load_dword v7, v8, s[40:43], s58 offen sc1\n"
    "s_waitcnt vmcnt(0)\n"
    "v_cmp_gt_u32 vcc, s55, v7\n"
    "s_nop 4\n"
    "s_cbranch_vccz cw_ux_ok_%=\n"
    "s_sleep 2\n"
    "s_add_u32 s60, s60, 1\n"
    "s_cmp_gt_u32 s60, 65536\n"
    "s_cbranch_scc1 cw_abort_%=\n"
    "s_and_b32 s59, s60, 63\n"
    "s_cbranch_scc1 cw_ux_poll_%=\n"
    "v_mov_b32 v10, 1024\n"
    "buffer_load_dword v10, v10, s[40:43], s58 offen sc1\n"
    "s_waitcnt vmcnt(0)\n"
    "v_readfirstlane_b32 s59, v10\n"
    "s_nop 0\n"
    "s_cmp_lg_u32 s59, 0\n"
    "s_nop 2\n"
    "s_cbranch_scc1 cw_end_%=\n"
    "s_branch cw_ux_poll_%=\n"
    "cw_ux_ok_%=:\n"
    "s_memrealtime s[56:57]\n"
    "s_waitcnt lgkmcnt(0)\n"
    "s_sub_u32 s59, s64, s56\n"
    "s_cmp_gt_u32 s59, 1000\n"
    "s_cbranch_scc1 cw_wm_go_%=\n"
    "cw_wm_%=:\n"
    "s_memrealtime s[56:57]\n"
    "s_waitcnt lgkmcnt(0)\n"
    "s_sub_u32 s59, s64, s56\n"
    "s_cmp_gt_i32 s59, 3\n"
    "s_cbranch_scc1 cw_wm_%=\n"
    "cw_wm_f_%=:\n"
    "s_sub_u32 s59, s59, 1\n"
    "s_cmp_ge_i32 s59, 0\n"
    "s_cbranch_scc1 cw_wm_f_%=\n"
    "cw_wm_go_%=:\n"
    "s_mov_b32 s60, 0\n"
    "buffer_load_dwordx4 v[32:35], v0, s[40:43], s50 offen sc1\n"
    "buffer_load_dwordx4 v[36:39], v0, s[40:43], s50 offen offset:1024 sc1\n"
    "buffer_load_dwordx4 v[40:43], v0, s[40:43], s50 offen offset:2048 sc1\n"
    "buffer_load_dwordx4 v[44:47], v0, s[40:43], s50 offen offset:3072 sc1\n"
    "buffer_load_dwordx4 v[48:51], v0, s[40:43], s51 offen sc1\n"
    "buffer_load_dwordx4 v[52:55], v0, s[40:43], s51 offen offset:1024 sc1\n"
    "buffer_load_dwordx4 v[56:59], v0, s[40:43], s51 offen offset:2048 sc1\n"
    "buffer_load_dwordx4 v[60:63], v0, s[40:43], s51 offen offset:3072 sc1\n"
    "buffer_load_dwordx4 v[64:67], v0, s[40:43], s52 offen sc1\n"
    "buffer_load_dwordx4 v[68:71], v0, s[40:43], s52 offen offset:1024 sc1\n"
    "buffer_load_dwordx4 v[112:115], v0, s[40:43], s61 offen sc1\n"
    "buffer_load_dwordx4 v[116:119], v0, s[40:43], s61 offen offset:1024 sc1\n"
    "buffer_load_dwordx4 v[120:123], v0, s[40:43], s61 offen offset:2048 sc1\n"
    "buffer_load_dwordx4 v[124:127], v0, s[40:43], s61 offen offset:3072 sc1\n"
    "s_add_u32 s61, s61, 4096\n"
    "buffer_load_dwordx4 v[128:131], v0, s[40:43], s61 offen sc1\n"
    "buffer_load_dwordx4 v[132:135], v0, s[40:43], s61 offen offset:1024 sc1\n"
    "buffer_load_dwordx4 v[136:139], v0, s[40:43], s61 offen offset:2048 sc1\n"
    "buffer_load_dwordx4 v[140:143], v0, s[40:43], s61 offen offset:3072 sc1\n"
    "s_add_u32 s61, s61, 4096\n"
    "buffer_load_dwordx4 v[144:147], v0, s[40:43], s61 offen sc1\n"
    "buffer_load_dwordx4 v[148:151], v0, s[40:43], s61 offen offset:1024 sc1\n"
    "buffer_load_dwordx4 v[152:155], v0, s[40:43], s61 offen offset:2048 sc1\n"
    "buffer_load_dwordx4 v[156:159], v0, s[40:43], s61 offen offset:3072 sc1\n"
    "s_add_u32 s61, s61, 4096\n"
    "buffer_load_dwordx4 v[160:163], v0, s[40:43], s61 offen sc1\n"
    "buffer_load_dwordx4 v[164:167], v0, s[40:43], s61 offen offset:1024 sc1\n"
    "buffer_load_dwordx4 v[168:171], v0, s[40:43], s61 offen offset:2048 sc1\n"
    "buffer_load_dwordx4 v[172:175], v0, s[40:43], s61 offen offset:3072 sc1\n"
    "s_add_u32 s61, s61, 1298432\n"
    "buffer_load_dword v7, v8, s[40:43], s58 offen sc1\n"
    "cw_mcomp_%=:\n"
    "s_waitcnt vmcnt(26)\n"
    "ds_write_b128 v0, v[32:35] offset:4096\n"
    "s_waitcnt vmcnt(25)\n"
    "ds_write_b128 v0, v[36:39] offset:5120\n"
    "s_waitcnt vmcnt(24)\n"
    "ds_write_b128 v0, v[40:43] offset:6144\n"
    "s_waitcnt vmcnt(23)\n"
    "ds_write_b128 v0, v[44:47] offset:7168\n"
    "s_waitcnt vmcnt(22)\n"
    "ds_write_b128 v0, v[48:51] offset:8192\n"
    "s_waitcnt vmcnt(21)\n"
    "ds_write_b128 v0, v[52:55] offset:9216\n"
    "s_waitcnt vmcnt(20)\n"
    "ds_write_b128 v0, v[56:59] offset:10240\n"
    "s_waitcnt vmcnt(19)\n"
    "ds_write_b128 v0, v[60:63] offset:11264\n"
    "s_waitcnt vmcnt(18)\n"
    "ds_write_b128 v0, v[64:67] offset:12288\n"
    "s_waitcnt vmcnt(17)\n"
    "ds_write_b128 v0, v[68:71] offset:13312\n"
    "v_max_u32 v6, v32, v33\n"
    "v_max3_u32 v6, v34, v35, v6\n"
    "v_max3_u32 v6, v36, v37, v6\n"
    "v_max3_u32 v6, v38, v39, v6\n"
    "v_max3_u32 v6, v40, v41, v6\n"
    "v_max3_u32 v6, v42, v43, v6\n"
    "v_max3_u32 v6, v44, v45, v6\n"
    "v_max3_u32 v6, v46, v47, v6\n"
    "v_max3_u32 v6, v48, v49, v6\n"
    "v_max3_u32 v6, v50, v51, v6\n"
    "v_max3_u32 v6, v52, v53, v6\n"
    "v_max3_u32 v6, v54, v55, v6\n"
    "v_max3_u32 v6, v56, v57, v6\n"
    "v_max3_u32 v6, v58, v59, v6\n"
    "v_max3_u32 v6, v60, v61, v6\n"
    "v_max3_u32 v6, v62, v63, v6\n"
    "v_max3_u32 v6, v64, v65, v6\n"
    "v_max3_u32 v6, v66, v67, v6\n"
    "v_max3_u32 v6, v68, v69, v6\n"
    "v_max3_u32 v6, v70, v71, v6\n"
    "v_cmp_eq_u32 vcc, -1, v6\n"
    "s_nop 4\n"
    "s_cbranch_vccnz cw_mfail_%=\n"
    "s_waitcnt lgkmcnt(0)\n"
    "s_barrier\n"
    "s_cmp_eq_u32 s47, 63\n"
    "s_cbranch_scc1 cw_end_%=\n"
    "s_mul_i32 s50, s48, 4\n"
    "s_and_b32 s51, s46, 3\n"
    "s_add_u32 s50, s50, s51\n"
    "s_mul_i32 s50, s50, 49152\n"
    "s_add_u32 s51, s50, 4096\n"
    "s_add_u32 s52, s50, 16384\n"
    "s_add_u32 s53, s50, 20480\n"
    "s_add_u32 s62, s50, 32768\n"
    "s_add_u32 s49, s47, 1\n"
    "s_and_b32 s49, s49, 3\n"
    "s_mul_i32 s63, s49, 131072\n"
    "s_add_u32 s63, s63, 786432\n"
    "s_waitcnt lgkmcnt(0)\n"
    "s_add_u32 s64, s65, s69\n"
    "s_memrealtime s[56:57]\n"
    "s_waitcnt lgkmcnt(0)\n"
    "s_sub_u32 s59, s64, s56\n"
    "s_cmp_gt_u32 s59, 1000\n"
    "s_cbranch_scc1 cw_ws_go_%=\n"
    "cw_ws_%=:\n"
    "s_memrealtime s[56:57]\n"
    "s_waitcnt lgkmcnt(0)\n"
    "s_sub_u32 s59, s64, s56\n"
    "s_cmp_gt_i32 s59, 3\n"
    "s_cbranch_scc1 cw_ws_%=\n"
    "cw_ws_f_%=:\n"
    "s_sub_u32 s59, s59, 1\n"
    "s_cmp_ge_i32 s59, 0\n"
    "s_cbranch_scc1 cw_ws_f_%=\n"
    "cw_ws_go_%=:\n"
    "s_lshl_b32 s54, s47, 7\n"
    "s_add_u32 s54, s54, 1310720\n"
    "s_mov_b32 s60, 0\n"
    "cw_rs_%=:\n"
    "buffer_load_dwordx2 v[10:11], v5, s[40:43], s54 offen sc1\n"
    "s_waitcnt vmcnt(0)\n"
    "v_readfirstlane_b32 s64, v10\n"
    "v_readfirstlane_b32 s65, v11\n"
    "s_nop 0\n"
    "s_max_u32 s59, s64, s65\n"
    "s_cmp_lg_u32 s59, -1\n"
    "s_nop 1\n"
    "s_cbranch_scc1 cw_rs_ok_%=\n"
    "s_sleep 4\n"
    "s_add_u32 s60, s60, 1\n"
    "s_cmp_gt_u32 s60, 65536\n"
    "s_cbranch_scc1 cw_abort_%=\n"
    "s_and_b32 s59, s60, 63\n"
    "s_cbranch_scc1 cw_rs_%=\n"
    "v_mov_b32 v10, 1024\n"
    "buffer_load_dword v10, v10, s[40:43], s58 offen sc1\n"
    "s_waitcnt vmcnt(0)\n"
    "v_readfirstlane_b32 s59, v10\n"
    "s_nop 0\n"
    "s_cmp_lg_u32 s59, 0\n"
    "s_nop 2\n"
    "s_cbranch_scc1 cw_end_%=\n"
    "s_branch cw_rs_%=\n"
    "cw_rs_ok_%=:\n"
    "s_mov_b32 s60, 0\n"
    "s_memrealtime s[56:57]\n"
    "s_waitcnt lgkmcnt(0)\n"
    "s_sub_u32 s59, s64, s56\n"
    "s_cmp_gt_u32 s59, 1000\n"
    "s_cbranch_scc1 cw_wu_go_%=\n"
    "cw_wu_%=:\n"
    "s_memrealtime s[56:57]\n"
    "s_waitcnt lgkmcnt(0)\n"
    "s_sub_u32 s59, s64, s56\n"
    "s_cmp_gt_i32 s59, 15\n"
    "s_cbranch_scc1 cw_wu_%=\n"
    "s_memrealtime s[30:31]\n"
    "s_nop 1\n"
    "s_memrealtime s[90:91]\n"
    "s_nop 1\n"
    "s_memrealtime s[34:35]\n"
    "s_nop 1\n"
    "s_memrealtime s[36:37]\n"
    "s_nop 1\n"
    "s_memrealtime s[38:39]\n"
    "s_nop 1\n"
    "s_memrealtime s[94:95]\n"
    "s_nop 1\n"
    "s_memrealtime s[96:97]\n"
    "s_nop 1\n"
    "s_memrealtime s[98:99]\n"
    "s_nop 1\n"
    "s_getpc_b64 s[56:57]\n"
    "cw_wu_pc_%=:\n"
    "s_waitcnt lgkmcnt(0)\n"
    "s_add_u32 s90, s90, s34\n"
    "s_add_u32 s90, s90, s36\n"
    "s_add_u32 s90, s90, s38\n"
    "s_add_u32 s90, s90, s94\n"
    "s_add_u32 s90, s90, s96\n"
    "s_add_u32 s90, s90, s98\n"
    "s_mul_i32 s34, s30, 7\n"
    "s_sub_u32 s90, s90, s34\n"
    "s_mul_i32 s90, s90, 3\n"
    "s_sub_u32 s59, s64, s30\n"
    "s_mul_i32 s59, s59, 23\n"
    "s_sub_u32 s59, s59, s90\n"
    "s_sub_u32 s59, s59, 170\n"
    "s_max_i32 s59, s59, 0\n"
    "s_min_i32 s59, s59, 252\n"
    "s_andn2_b32 s59, s59, 3\n"
    "s_sub_u32 s59, cw_wu_e_%=-cw_wu_pc_%=, s59\n"
    "s_add_u32 s56, s56, s59\n"
    "s_addc_u32 s57, s57, 0\n"
    "s_setpc_b64 s[56:57]\n"
    "s_nop 0\n"
    "s_nop 0\n"
    "s_nop 0\n"
    "s_nop 0\n"
    "s_nop 0\n"
    "s_nop 0\n"
    "s_nop 0\n"
    "s_nop 0\n"
    "s_nop 0\n"
    "s_nop 0\n"
    "s_nop 0\n"
    "s_nop 0\n"
    "s_nop 0\n"
    "s_nop 0\n"
    "s_nop 0\n"
    "s_nop 0\n"
    "s_nop 0\n"
    "s_nop 0\n"
    "s_nop 0\n"
    "s_nop 0\n"
    "s_nop 0\n"
    "s_nop 0\n"
    "s_nop 0\n"
    "s_nop 0\n"
    "s_nop 0\n"
    "s_nop 0\n"
    "s_nop 0\n"
    "s_nop 0\n"
    "s_nop 0\n"
    "s_nop 0\n"
    "s_nop 0\n"
    "s_nop 0\n"
    "s_nop 0\n"
    "s_nop 0\n"
    "s_nop 0\n"
    "s_nop 0\n"
    "s_nop 0\n"
    "s_nop 0\n"
    "s_nop 0\n"
    "s_nop 0\n"
    "s_nop 0\n"
    "s_nop 0\n"
    "s_nop 0\n"
    "s_nop 0\n"
    "s_nop 0\n"
    "s_nop 0\n"
    "s_nop 0\n"
    "s_nop 0\n"
    "s_nop 0\n"
    "s_nop 0\n"
    "s_nop 0\n"
    "s_nop 0\n"
    "s_nop 0\n"
    "s_nop 0\n"
    "s_nop 0\n"
    "s_nop 0\n"
    "s_nop 0\n"
    "s_nop 0\n"
    "s_nop 0\n"
    "s_nop 0\n"
    "s_nop 0\n"
    "s_nop 0\n"
    "s_nop 0\n"
    "cw_wu_e_%=:\n"
    "cw_wu_go_%=:\n"
    "buffer_load_dwordx4 v[32:35], v0, s[40:43], s50 offen sc1\n"
    "buffer_load_dwordx4 v[36:39], v0, s[40:43], s50 offen offset:1024 sc1\n"
    "buffer_load_dwordx4 v[40:43], v0, s[40:43], s50 offen offset:2048 sc1\n"
    "buffer_load_dwordx4 v[44:47], v0, s[40:43], s50 offen offset:3072 sc1\n"
    "buffer_load_dwordx4 v[48:51], v0, s[40:43], s51 offen sc1\n"
    "buffer_load_dwordx4 v[52:55], v0, s[40:43], s51 offen offset:1024 sc1\n"
    "buffer_load_dwordx4 v[56:59], v0, s[40:43], s51 offen offset:2048 sc1\n"
    "buffer_load_dwordx4 v[60:63], v0, s[40:43], s51 offen offset:3072 sc1\n"
    "buffer_load_dwordx4 v[64:67], v0, s[40:43], s52 offen sc1\n"
    "buffer_load_dwordx4 v[68:71], v0, s[40:43], s52 offen offset:1024 sc1\n"
    "buffer_load_dwordx4 v[72:75], v0, s[40:43], s52 offen offset:2048 sc1\n"
    "buffer_load_dwordx4 v[76:79], v0, s[40:43], s52 offen offset:3072 sc1\n"
    "buffer_load_dwordx4 v[80:83], v0, s[40:43], s53 offen sc1\n"
    "buffer_load_dwordx4 v[84:87], v0, s[40:43], s53 offen offset:1024 sc1\n"
    "buffer_load_dwordx4 v[88:91], v0, s[40:43], s53 offen offset:2048 sc1\n"
    "buffer_load_dwordx4 v[92:95], v0, s[40:43], s53 offen offset:3072 sc1\n"
    "buffer_load_dwordx4 v[96:99], v0, s[40:43], s62 offen sc1\n"
    "buffer_load_dwordx4 v[100:103], v0, s[40:43], s62 offen offset:1024 sc1\n"
    "buffer_load_dwordx4 v[104:107], v0, s[40:43], s62 offen offset:2048 sc1\n"
    "buffer_load_dwordx4 v[108:111], v0, s[40:43], s62 offen offset:3072 sc1\n"
    "s_barrier\n"
    "ds_read_b128 v[176:179], v0 offset:0\n"
    "ds_read_b128 v[180:183], v0 offset:1024\n"
    "ds_read_b128 v[184:187], v0 offset:2048\n"
    "ds_read_b128 v[188:191], v0 offset:3072\n"
    "cw_pcomp_%=:\n"
    "s_waitcnt vmcnt(19)\n"
    "v_mfma_f32_16x16x32_bf16 v[24:27], v[112:115], v[32:35], 0\n"
    "s_waitcnt vmcnt(18)\n"
    "v_mfma_f32_16x16x32_bf16 v[24:27], v[116:119], v[36:39], v[24:27]\n"
    "s_waitcnt vmcnt(17)\n"
    "v_mfma_f32_16x16x32_bf16 v[24:27], v[120:123], v[40:43], v[24:27]\n"
    "s_waitcnt vmcnt(16)\n"
    "v_mfma_f32_16x16x32_bf16 v[24:27], v[124:127], v[44:47], v[24:27]\n"
    "s_waitcnt vmcnt(15)\n"
    "v_mfma_f32_16x16x32_bf16 v[24:27], v[128:131], v[48:51], v[24:27]\n"
    "s_waitcnt vmcnt(14)\n"
    "v_mfma_f32_16x16x32_bf16 v[24:27], v[132:135], v[52:55], v[24:27]\n"
    "s_waitcnt vmcnt(13)\n"
    "v_mfma_f32_16x16x32_bf16 v[24:27], v[136:139], v[56:59], v[24:27]\n"
    "s_waitcnt vmcnt(12)\n"
    "v_mfma_f32_16x16x32_bf16 v[24:27], v[140:143], v[60:63], v[24:27]\n"
    "s_waitcnt vmcnt(11)\n"
    "v_mfma_f32_16x16x32_bf16 v[24:27], v[144:147], v[64:67], v[24:27]\n"
    "s_waitcnt vmcnt(10)\n"
    "v_mfma_f32_16x16x32_bf16 v[24:27], v[148:151], v[68:71], v[24:27]\n"
    "s_waitcnt vmcnt(9)\n"
    "v_mfma_f32_16x16x32_bf16 v[24:27], v[152:155], v[72:75], v[24:27]\n"
    "s_waitcnt vmcnt(8)\n"
    "v_mfma_f32_16x16x32_bf16 v[24:27], v[156:159], v[76:79], v[24:27]\n"
    "s_waitcnt vmcnt(7)\n"
    "v_mfma_f32_16x16x32_bf16 v[24:27], v[160:163], v[80:83], v[24:27]\n"
    "s_waitcnt vmcnt(6)\n"
    "v_mfma_f32_16x16x32_bf16 v[24:27], v[164:167], v[84:87], v[24:27]\n"
    "s_waitcnt vmcnt(5)\n"
    "v_mfma_f32_16x16x32_bf16 v[24:27], v[168:171], v[88:91], v[24:27]\n"
    "s_waitcnt vmcnt(4)\n"
    "v_mfma_f32_16x16x32_bf16 v[24:27], v[172:175], v[92:95], v[24:27]\n"
    "s_waitcnt vmcnt(3) lgkmcnt(3)\n"
    "v_mfma_f32_16x16x32_bf16 v[24:27], v[176:179], v[96:99], v[24:27]\n"
    "s_waitcnt vmcnt(2) lgkmcnt(2)\n"
    "v_mfma_f32_16x16x32_bf16 v[24:27], v[180:183], v[100:103], v[24:27]\n"
    "s_waitcnt vmcnt(1) lgkmcnt(1)\n"
    "v_mfma_f32_16x16x32_bf16 v[24:27], v[184:187], v[104:107], v[24:27]\n"
    "s_waitcnt vmcnt(0) lgkmcnt(0)\n"
    "v_mfma_f32_16x16x32_bf16 v[24:27], v[188:191], v[108:111], v[24:27]\n"
    "v_mov_b32 v11, v11\n"
    "v_mov_b32 v11, v11\n"
    "v_mov_b32 v11, v11\n"
    "v_mov_b32 v11, v11\n"
    "v_mov_b32 v11, v11\n"
    "v_mov_b32 v11, v11\n"
    "v_mov_b32 v11, v11\n"
    "v_mov_b32 v11, v11\n"
    "v_cmp_u_f32 vcc, v24, v24\n"
    "v_permlane32_swap_b32 v24, v26\n"
    "v_permlane32_swap_b32 v25, v27\n"
    "s_nop 0\n"
    "v_add_f32_dpp v24, v26, v24 row_ror:8 row_mask:0xf bank_mask:0xf\n"
    "v_add_f32_dpp v25, v27, v25 row_ror:8 row_mask:0xf bank_mask:0xf\n"
    "v_cvt_pk_bf16_f32 v28, v24, v25\n"
    "v_cvt_pk_bf16_f32 v30, v24, v25\n"
    "s_nop 1\n"
    "v_permlane16_swap_b32 v28, v30\n"
    "v_mov_b32 v29, v28\n"
    "v_mov_b32 v31, v30\n"
    "s_nop 0\n"
    "v_permlane32_swap_b32 v28, v29\n"
    "v_permlane32_swap_b32 v30, v31\n"
    "s_cbranch_vccnz cw_pfail_%=\n"
    "cw_presume_%=:\n"
    "buffer_store_dwordx4 v[28:31], v3, s[40:43], s63 offen sc1\n"
    "s_barrier\n"
    "s_add_u32 s47, s47, 1\n"
    "s_branch cw_loopa_%=\n"
    "cw_pagain_%=:\n"
    "buffer_load_dwordx4 v[32:35], v0, s[40:43], s50 offen sc1\n"
    "buffer_load_dwordx4 v[36:39], v0, s[40:43], s50 offen offset:1024 sc1\n"
    "buffer_load_dwordx4 v[40:43], v0, s[40:43], s50 offen offset:2048 sc1\n"
    "buffer_load_dwordx4 v[44:47], v0, s[40:43], s50 offen offset:3072 sc1\n"
    "buffer_load_dwordx4 v[48:51], v0, s[40:43], s51 offen sc1\n"
    "buffer_load_dwordx4 v[52:55], v0, s[40:43], s51 offen offset:1024 sc1\n"
    "buffer_load_dwordx4 v[56:59], v0, s[40:43], s51 offen offset:2048 sc1\n"
    "buffer_load_dwordx4 v[60:63], v0, s[40:43], s51 offen offset:3072 sc1\n"
    "buffer_load_dwordx4 v[64:67], v0, s[40:43], s52 offen sc1\n"
    "buffer_load_dwordx4 v[68:71], v0, s[40:43], s52 offen offset:1024 sc1\n"
    "buffer_load_dwordx4 v[72:75], v0, s[40:43], s52 offen offset:2048 sc1\n"
    "buffer_load_dwordx4 v[76:79], v0, s[40:43], s52 offen offset:3072 sc1\n"
    "buffer_load_dwordx4 v[80:83], v0, s[40:43], s53 offen sc1\n"
    "buffer_load_dwordx4 v[84:87], v0, s[40:43], s53 offen offset:1024 sc1\n"
    "buffer_load_dwordx4 v[88:91], v0, s[40:43], s53 offen offset:2048 sc1\n"
    "buffer_load_dwordx4 v[92:95], v0, s[40:43], s53 offen offset:3072 sc1\n"
    "buffer_load_dwordx4 v[96:99], v0, s[40:43], s62 offen sc1\n"
    "buffer_load_dwordx4 v[100:103], v0, s[40:43], s62 offen offset:1024 sc1\n"
    "buffer_load_dwordx4 v[104:107], v0, s[40:43], s62 offen offset:2048 sc1\n"
    "buffer_load_dwordx4 v[108:111], v0, s[40:43], s62 offen offset:3072 sc1\n"
    "s_nop 1\n"
    "s_branch cw_pcomp_%=\n"
    "cw_mfail_%=:\n"
    "s_sleep 6\n"
    "s_add_u32 s60, s60, 1\n"
    "s_cmp_gt_u32 s60, 65536\n"
    "s_cbranch_scc1 cw_abort_%=\n"
    "s_and_b32 s59, s60, 63\n"
    "s_cbranch_scc1 cw_mretry_%=\n"
    "v_mov_b32 v10, 1024\n"
    "buffer_load_dword v10, v10, s[40:43], s58 offen sc1\n"
    "s_waitcnt vmcnt(0)\n"
    "v_readfirstlane_b32 s59, v10\n"
    "s_nop 0\n"
    "s_cmp_lg_u32 s59, 0\n"
    "s_nop 2\n"
    "s_cbranch_scc1 cw_end_%=\n"
    "s_branch cw_mretry_%=\n"
    "cw_mretry_%=:\n"
    "buffer_load_dwordx4 v[32:35], v0, s[40:43], s50 offen sc1\n"
    "buffer_load_dwordx4 v[36:39], v0, s[40:43], s50 offen offset:1024 sc1\n"
    "buffer_load_dwordx4 v[40:43], v0, s[40:43], s50 offen offset:2048 sc1\n"
    "buffer_load_dwordx4 v[44:47], v0, s[40:43], s50 offen offset:3072 sc1\n"
    "buffer_load_dwordx4 v[48:51], v0, s[40:43], s51 offen sc1\n"
    "buffer_load_dwordx4 v[52:55], v0, s[40:43], s51 offen offset:1024 sc1\n"
    "buffer_load_dwordx4 v[56:59], v0, s[40:43], s51 offen offset:2048 sc1\n"
    "buffer_load_dwordx4 v[60:63], v0, s[40:43], s51 offen offset:3072 sc1\n"
    "buffer_load_dwordx4 v[64:67], v0, s[40:43], s52 offen sc1\n"
    "buffer_load_dwordx4 v[68:71], v0, s[40:43], s52 offen offset:1024 sc1\n"
    "s_waitcnt vmcnt(0)\n"
    "s_nop 0\n"
    "s_branch cw_mcomp_%=\n"
    "cw_pfail_%=:\n"
    "v_max_u32 v6, v32, v33\n"
    "v_max3_u32 v6, v34, v35, v6\n"
    "v_max3_u32 v6, v36, v37, v6\n"
    "v_max3_u32 v6, v38, v39, v6\n"
    "v_max3_u32 v6, v40, v41, v6\n"
    "v_max3_u32 v6, v42, v43, v6\n"
    "v_max3_u32 v6, v44, v45, v6\n"
    "v_max3_u32 v6, v46, v47, v6\n"
    "v_max3_u32 v6, v48, v49, v6\n"
    "v_max3_u32 v6, v50, v51, v6\n"
    "v_max3_u32 v6, v52, v53, v6\n"
    "v_max3_u32 v6, v54, v55, v6\n"
    "v_max3_u32 v6, v56, v57, v6\n"
    "v_max3_u32 v6, v58, v59, v6\n"
    "v_max3_u32 v6, v60, v61, v6\n"
    "v_max3_u32 v6, v62, v63, v6\n"
    "v_max3_u32 v6, v64, v65, v6\n"
    "v_max3_u32 v6, v66, v67, v6\n"
    "v_max3_u32 v6, v68, v69, v6\n"
    "v_max3_u32 v6, v70, v71, v6\n"
    "v_max3_u32 v6, v72, v73, v6\n"
    "v_max3_u32 v6, v74, v75, v6\n"
    "v_max3_u32 v6, v76, v77, v6\n"
    "v_max3_u32 v6, v78, v79, v6\n"
    "v_max3_u32 v6, v80, v81, v6\n"
    "v_max3_u32 v6, v82, v83, v6\n"
    "v_max3_u32 v6, v84, v85, v6\n"
    "v_max3_u32 v6, v86, v87, v6\n"
    "v_max3_u32 v6, v88, v89, v6\n"
    "v_max3_u32 v6, v90, v91, v6\n"
    "v_max3_u32 v6, v92, v93, v6\n"
    "v_max3_u32 v6, v94, v95, v6\n"
    "v_max3_u32 v6, v96, v97, v6\n"
    "v_max3_u32 v6, v98, v99, v6\n"
    "v_max3_u32 v6, v100, v101, v6\n"
    "v_max3_u32 v6, v102, v103, v6\n"
    "v_max3_u32 v6, v104, v105, v6\n"
    "v_max3_u32 v6, v106, v107, v6\n"
    "v_max3_u32 v6, v108, v109, v6\n"
    "v_max3_u32 v6, v110, v111, v6\n"
    "v_cmp_eq_u32 vcc, -1, v6\n"
    "s_nop 4\n"
    "s_cbranch_vccz cw_presume_%=\n"
    "s_cmp_lg_u32 s60, 0\n"
    "s_cbranch_scc1 cw_pspin_%=\n"
    "s_mov_b32 s60, 1\n"
    "s_branch cw_pagain_%=\n"
    "cw_ppoll_%=:\n"
    "buffer_load_dword v10, v2, s[40:43], s62 offen sc1\n"
    "s_waitcnt vmcnt(0)\n"
    "v_cmp_eq_u32 vcc, -1, v10\n"
    "s_nop 4\n"
    "s_cbranch_vccz cw_pagain_%=\n"
    "cw_pspin_%=:\n"
    "s_add_u32 s60, s60, 1\n"
    "s_cmp_gt_u32 s60, 65536\n"
    "s_cbranch_scc1 cw_abort_%=\n"
    "s_and_b32 s59, s60, 63\n"
    "s_cbranch_scc1 cw_ppoll_%=\n"
    "v_mov_b32 v10, 1024\n"
    "buffer_load_dword v10, v10, s[40:43], s58 offen sc1\n"
    "s_waitcnt vmcnt(0)\n"
    "v_readfirstlane_b32 s59, v10\n"
    "s_nop 0\n"
    "s_cmp_lg_u32 s59, 0\n"
    "s_nop 2\n"
    "s_cbranch_scc1 cw_end_%=\n"
    "s_branch cw_ppoll_%=\n"
    "cw_wx_%=:\n"
    "s_mul_i32 s61, s45, 6\n"
    "s_sub_u32 s61, s61, 6\n"
    "s_min_u32 s61, s61, 10\n"
    "s_lshl_b32 s48, s44, 4\n"
    "s_add_u32 s48, s48, s61\n"
    "s_mul_i32 s48, s48, 20480\n"
    "s_add_u32 s48, s48, 33619968\n"
    "s_lshl_b32 s49, s45, 15\n"
    "s_add_u32 s49, s49, -18432\n"
    "v_add_u32 v14, s49, v0\n"
    "buffer_load_dwordx4 v[16:19], v0, s[40:43], s48 offen\n"
    "buffer_load_dwordx4 v[20:23], v0, s[40:43], s48 offen offset:1024\n"
    "buffer_load_dwordx4 v[32:35], v0, s[40:43], s48 offen offset:2048\n"
    "buffer_load_dwordx4 v[36:39], v0, s[40:43], s48 offen offset:3072\n"
    "s_add_u32 s48, s48, 4096\n"
    "buffer_load_dwordx4 v[48:51], v0, s[40:43], s48 offen\n"
    "buffer_load_dwordx4 v[52:55], v0, s[40:43], s48 offen offset:1024\n"
    "buffer_load_dwordx4 v[64:67], v0, s[40:43], s48 offen offset:2048\n"
    "buffer_load_dwordx4 v[68:71], v0, s[40:43], s48 offen offset:3072\n"
    "s_add_u32 s48, s48, 4096\n"
    "buffer_load_dwordx4 v[80:83], v0, s[40:43], s48 offen\n"
    "buffer_load_dwordx4 v[84:87], v0, s[40:43], s48 offen offset:1024\n"
    "buffer_load_dwordx4 v[96:99], v0, s[40:43], s48 offen offset:2048\n"
    "buffer_load_dwordx4 v[100:103], v0, s[40:43], s48 offen offset:3072\n"
    "s_add_u32 s48, s48, 4096\n"
    "buffer_load_dwordx4 v[112:115], v0, s[40:43], s48 offen\n"
    "buffer_load_dwordx4 v[116:119], v0, s[40:43], s48 offen offset:1024\n"
    "buffer_load_dwordx4 v[120:123], v0, s[40:43], s48 offen offset:2048\n"
    "buffer_load_dwordx4 v[124:127], v0, s[40:43], s48 offen offset:3072\n"
    "s_add_u32 s48, s48, 4096\n"
    "buffer_load_dwordx4 v[128:131], v0, s[40:43], s48 offen\n"
    "buffer_load_dwordx4 v[132:135], v0, s[40:43], s48 offen offset:1024\n"
    "buffer_load_dwordx4 v[136:139], v0, s[40:43], s48 offen offset:2048\n"
    "buffer_load_dwordx4 v[140:143], v0, s[40:43], s48 offen offset:3072\n"
    "s_add_u32 s48, s48, 4096\n"
    "buffer_load_dwordx4 a[0:3], v0, s[40:43], s48 offen\n"
    "buffer_load_dwordx4 a[4:7], v0, s[40:43], s48 offen offset:1024\n"
    "buffer_load_dwordx4 a[32:35], v0, s[40:43], s48 offen offset:2048\n"
    "buffer_load_dwordx4 a[36:39], v0, s[40:43], s48 offen offset:3072\n"
    "s_add_u32 s48, s48, 4096\n"
    "buffer_load_dwordx4 a[64:67], v0, s[40:43], s48 offen\n"
    "buffer_load_dwordx4 a[68:71], v0, s[40:43], s48 offen offset:1024\n"
    "buffer_load_dwordx4 a[96:99], v0, s[40:43], s48 offen offset:2048\n"
    "buffer_load_dwordx4 a[100:103], v0, s[40:43], s48 offen offset:3072\n"
    "s_add_u32 s48, s48, 4096\n"
    "buffer_load_dwordx4 a[128:131], v0, s[40:43], s48 offen\n"
    "buffer_load_dwordx4 a[132:135], v0, s[40:43], s48 offen offset:1024\n"
    "buffer_load_dwordx4 a[160:163], v0, s[40:43], s48 offen offset:2048\n"
    "buffer_load_dwordx4 a[164:167], v0, s[40:43], s48 offen offset:3072\n"
    "s_add_u32 s48, s48, 4096\n"
    "buffer_load_dwordx4 a[192:195], v0, s[40:43], s48 offen\n"
    "buffer_load_dwordx4 a[196:199], v0, s[40:43], s48 offen offset:1024\n"
    "buffer_load_dwordx4 a[232:235], v0, s[40:43], s48 offen offset:2048\n"
    "buffer_load_dwordx4 a[236:239], v0, s[40:43], s48 offen offset:3072\n"
    "s_add_u32 s48, s48, 4096\n"
    "buffer_load_dwordx4 v[176:179], v0, s[40:43], s48 offen\n"
    "buffer_load_dwordx4 v[180:183], v0, s[40:43], s48 offen offset:1024\n"
    "buffer_load_dwordx4 v[216:219], v0, s[40:43], s48 offen offset:2048\n"
    "buffer_load_dwordx4 v[220:223], v0, s[40:43], s48 offen offset:3072\n"
    "s_add_u32 s48, s48, 4096\n"
    "buffer_load_dwordx4 a[8:11], v0, s[40:43], s48 offen\n"
    "buffer_load_dwordx4 a[12:15], v0, s[40:43], s48 offen offset:1024\n"
    "buffer_load_dwordx4 a[40:43], v0, s[40:43], s48 offen offset:2048\n"
    "buffer_load_dwordx4 a[44:47], v0, s[40:43], s48 offen offset:3072\n"
    "s_add_u32 s48, s48, 4096\n"
    "buffer_load_dwordx4 a[72:75], v0, s[40:43], s48 offen\n"
    "buffer_load_dwordx4 a[76:79], v0, s[40:43], s48 offen offset:1024\n"
    "buffer_load_dwordx4 a[104:107], v0, s[40:43], s48 offen offset:2048\n"
    "buffer_load_dwordx4 a[108:111], v0, s[40:43], s48 offen offset:3072\n"
    "s_add_u32 s48, s48, 4096\n"
    "buffer_load_dwordx4 a[136:139], v0, s[40:43], s48 offen\n"
    "buffer_load_dwordx4 a[140:143], v0, s[40:43], s48 offen offset:1024\n"
    "buffer_load_dwordx4 a[168:171], v0, s[40:43], s48 offen offset:2048\n"
    "buffer_load_dwordx4 a[172:175], v0, s[40:43], s48 offen offset:3072\n"
    "s_add_u32 s48, s48, 4096\n"
    "buffer_load_dwordx4 a[200:203], v0, s[40:43], s48 offen\n"
    "buffer_load_dwordx4 a[204:207], v0, s[40:43], s48 offen offset:1024\n"
    "buffer_load_dwordx4 a[240:243], v0, s[40:43], s48 offen offset:2048\n"
    "buffer_load_dwordx4 a[244:247], v0, s[40:43], s48 offen offset:3072\n"
    "s_add_u32 s48, s48, 4096\n"
    "buffer_load_dwordx4 v[184:187], v0, s[40:43], s48 offen\n"
    "buffer_load_dwordx4 v[188:191], v0, s[40:43], s48 offen offset:1024\n"
    "buffer_load_dwordx4 v[224:227], v0, s[40:43], s48 offen offset:2048\n"
    "buffer_load_dwordx4 v[228:231], v0, s[40:43], s48 offen offset:3072\n"
    "s_add_u32 s48, s48, 4096\n"
    "buffer_load_dwordx4 v[24:27], v0, s[40:43], s48 offen\n"
    "buffer_load_dwordx4 v[28:31], v0, s[40:43], s48 offen offset:1024\n"
    "buffer_load_dwordx4 v[40:43], v0, s[40:43], s48 offen offset:2048\n"
    "s_waitcnt vmcnt(61)\n"
    "buffer_load_dwordx4 v[44:47], v0, s[40:43], s48 offen offset:3072\n"
    "s_add_u32 s48, s48, 4096\n"
    "buffer_load_dwordx4 v[56:59], v0, s[40:43], s48 offen\n"
    "s_waitcnt vmcnt(61)\n"
    "buffer_load_dwordx4 v[60:63], v0, s[40:43], s48 offen offset:1024\n"
    "buffer_load_dwordx4 v[72:75], v0, s[40:43], s48 offen offset:2048\n"
    "s_waitcnt vmcnt(61)\n"
    "buffer_load_dwordx4 v[76:79], v0, s[40:43], s48 offen offset:3072\n"
    "s_add_u32 s48, s48, 4096\n"
    "buffer_load_dwordx4 v[88:91], v0, s[40:43], s48 offen\n"
    "s_waitcnt vmcnt(61)\n"
    "buffer_load_dwordx4 v[92:95], v0, s[40:43], s48 offen offset:1024\n"
    "buffer_load_dwordx4 v[104:107], v0, s[40:43], s48 offen offset:2048\n"
    "s_waitcnt vmcnt(61)\n"
    "buffer_load_dwordx4 v[108:111], v0, s[40:43], s48 offen offset:3072\n"
    "s_add_u32 s48, s48, 4096\n"
    "buffer_load_dwordx4 a[208:211], v0, s[40:43], s48 offen\n"
    "s_waitcnt vmcnt(61)\n"
    "buffer_load_dwordx4 a[212:215], v0, s[40:43], s48 offen offset:1024\n"
    "buffer_load_dwordx4 a[248:251], v0, s[40:43], s48 offen offset:2048\n"
    "s_waitcnt vmcnt(61)\n"
    "buffer_load_dwordx4 a[252:255], v0, s[40:43], s48 offen offset:3072\n"
    "s_add_u32 s48, s48, 4096\n"
    "buffer_load_dwordx4 v[192:195], v0, s[40:43], s48 offen\n"
    "s_waitcnt vmcnt(61)\n"
    "buffer_load_dwordx4 v[196:199], v0, s[40:43], s48 offen offset:1024\n"
    "buffer_load_dwordx4 v[232:235], v0, s[40:43], s48 offen offset:2048\n"
    "s_waitcnt vmcnt(61)\n"
    "buffer_load_dwordx4 v[236:239], v0, s[40:43], s48 offen offset:3072\n"
    "s_add_u32 s48, s48, 4096\n"
    "buffer_load_dwordx4 a[16:19], v0, s[40:43], s48 offen\n"
    "s_waitcnt vmcnt(61)\n"
    "buffer_load_dwordx4 a[20:23], v0, s[40:43], s48 offen offset:1024\n"
    "buffer_load_dwordx4 a[48:51], v0, s[40:43], s48 offen offset:2048\n"
    "s_waitcnt vmcnt(61)\n"
    "buffer_load_dwordx4 a[52:55], v0, s[40:43], s48 offen offset:3072\n"
    "s_add_u32 s48, s48, 4096\n"
    "buffer_load_dwordx4 a[80:83], v0, s[40:43], s48 offen\n"
    "s_waitcnt vmcnt(61)\n"
    "buffer_load_dwordx4 a[84:87], v0, s[40:43], s48 offen offset:1024\n"
    "buffer_load_dwordx4 a[112:115], v0, s[40:43], s48 offen offset:2048\n"
    "s_waitcnt vmcnt(61)\n"
    "buffer_load_dwordx4 a[116:119], v0, s[40:43], s48 offen offset:3072\n"
    "s_add_u32 s48, s48, 4096\n"
    "buffer_load_dwordx4 a[144:147], v0, s[40:43], s48 offen\n"
    "s_waitcnt vmcnt(61)\n"
    "buffer_load_dwordx4 a[148:151], v0, s[40:43], s48 offen offset:1024\n"
    "buffer_load_dwordx4 a[176:179], v0, s[40:43], s48 offen offset:2048\n"
    "s_waitcnt vmcnt(61)\n"
    "buffer_load_dwordx4 a[180:183], v0, s[40:43], s48 offen offset:3072\n"
    "s_add_u32 s48, s48, 4096\n"
    "buffer_load_dwordx4 a[216:219], v0, s[40:43], s48 offen\n"
    "s_waitcnt vmcnt(61)\n"
    "buffer_load_dwordx4 a[220:223], v0, s[40:43], s48 offen offset:1024\n"
    "buffer_load_dwordx4 v[160:163], v0, s[40:43], s48 offen offset:2048\n"
    "s_waitcnt vmcnt(61)\n"
    "buffer_load_dwordx4 v[164:167], v0, s[40:43], s48 offen offset:3072\n"
    "s_add_u32 s48, s48, 4096\n"
    "buffer_load_dwordx4 v[200:203], v0, s[40:43], s48 offen\n"
    "s_waitcnt vmcnt(61)\n"
    "buffer_load_dwordx4 v[204:207], v0, s[40:43], s48 offen offset:1024\n"
    "buffer_load_dwordx4 v[240:243], v0, s[40:43], s48 offen offset:2048\n"
    "s_waitcnt vmcnt(61)\n"
    "buffer_load_dwordx4 v[244:247], v0, s[40:43], s48 offen offset:3072\n"
    "s_add_u32 s48, s48, 4096\n"
    "buffer_load_dwordx4 a[24:27], v0, s[40:43], s48 offen\n"
    "s_waitcnt vmcnt(61)\n"
    "buffer_load_dwordx4 a[28:31], v0, s[40:43], s48 offen offset:1024\n"
    "buffer_load_dwordx4 a[56:59], v0, s[40:43], s48 offen offset:2048\n"
    "s_waitcnt vmcnt(61)\n"
    "buffer_load_dwordx4 a[60:63], v0, s[40:43], s48 offen offset:3072\n"
    "s_add_u32 s48, s48, 4096\n"
    "buffer_load_dwordx4 a[88:91], v0, s[40:43], s48 offen\n"
    "s_waitcnt vmcnt(61)\n"
    "buffer_load_dwordx4 a[92:95], v0, s[40:43], s48 offen offset:1024\n"
    "buffer_load_dwordx4 a[120:123], v0, s[40:43], s48 offen offset:2048\n"
    "s_waitcnt vmcnt(61)\n"
    "buffer_load_dwordx4 a[124:127], v0, s[40:43], s48 offen offset:3072\n"
    "s_add_u32 s48, s48, 4096\n"
    "buffer_load_dwordx4 a[152:155], v0, s[40:43], s48 offen\n"
    "s_waitcnt vmcnt(61)\n"
    "buffer_load_dwordx4 a[156:159], v0, s[40:43], s48 offen offset:1024\n"
    "buffer_load_dwordx4 a[184:187], v0, s[40:43], s48 offen offset:2048\n"
    "s_waitcnt vmcnt(61)\n"
    "buffer_load_dwordx4 a[188:191], v0, s[40:43], s48 offen offset:3072\n"
    "s_add_u32 s48, s48, 4096\n"
    "buffer_load_dwordx4 a[224:227], v0, s[40:43], s48 offen\n"
    "s_waitcnt vmcnt(61)\n"
    "buffer_load_dwordx4 a[228:231], v0, s[40:43], s48 offen offset:1024\n"
    "buffer_load_dwordx4 v[168:171], v0, s[40:43], s48 offen offset:2048\n"
    "s_waitcnt vmcnt(61)\n"
    "buffer_load_dwordx4 v[172:175], v0, s[40:43], s48 offen offset:3072\n"
    "s_add_u32 s48, s48, 4096\n"
    "buffer_load_dwordx4 v[208:211], v0, s[40:43], s48 offen\n"
    "s_waitcnt vmcnt(61)\n"
    "buffer_load_dwordx4 v[212:215], v0, s[40:43], s48 offen offset:1024\n"
    "buffer_load_dwordx4 v[248:251], v0, s[40:43], s48 offen offset:2048\n"
    "s_waitcnt vmcnt(61)\n"
    "buffer_load_dwordx4 v[252:255], v0, s[40:43], s48 offen offset:3072\n"
    "ds_write_b128 v14, v[16:19] offset:0\n"
    "ds_write_b128 v14, v[20:23] offset:1024\n"
    "s_waitcnt vmcnt(59)\n"
    "ds_write_b128 v14, v[24:27] offset:2048\n"
    "s_waitcnt vmcnt(58)\n"
    "ds_write_b128 v14, v[28:31] offset:3072\n"
    "ds_write_b128 v14, v[32:35] offset:4096\n"
    "ds_write_b128 v14, v[36:39] offset:5120\n"
    "s_waitcnt vmcnt(57)\n"
    "ds_write_b128 v14, v[40:43] offset:6144\n"
    "s_waitcnt vmcnt(56)\n"
    "ds_write_b128 v14, v[44:47] offset:7168\n"
    "ds_write_b128 v14, v[48:51] offset:8192\n"
    "ds_write_b128 v14, v[52:55] offset:9216\n"
    "s_waitcnt vmcnt(55)\n"
    "ds_write_b128 v14, v[56:59] offset:10240\n"
    "s_waitcnt vmcnt(54)\n"
    "ds_write_b128 v14, v[60:63] offset:11264\n"
    "ds_write_b128 v14, v[64:67] offset:12288\n"
    "ds_write_b128 v14, v[68:71] offset:13312\n"
    "s_waitcnt vmcnt(53)\n"
    "ds_write_b128 v14, v[72:75] offset:14336\n"
    "s_waitcnt vmcnt(52)\n"
    "ds_write_b128 v14, v[76:79] offset:15360\n"
    "s_waitcnt lgkmcnt(14)\n"
    "ds_write_b128 v14, v[80:83] offset:16384\n"
    "ds_write_b128 v14, v[84:87] offset:17408\n"
    "s_waitcnt lgkmcnt(14)\n"
    "s_waitcnt vmcnt(51)\n"
    "ds_write_b128 v14, v[88:91] offset:18432\n"
    "s_waitcnt vmcnt(50)\n"
    "ds_write_b128 v14, v[92:95] offset:19456\n"
    "s_waitcnt lgkmcnt(14)\n"
    "ds_write_b128 v14, v[96:99] offset:20480\n"
    "ds_write_b128 v14, v[100:103] offset:21504\n"
    "s_waitcnt lgkmcnt(14)\n"
    "s_waitcnt vmcnt(49)\n"
    "ds_write_b128 v14, v[104:107] offset:22528\n"
    "s_waitcnt vmcnt(48)\n"
    "ds_write_b128 v14, v[108:111] offset:23552\n"
    "s_waitcnt lgkmcnt(14)\n"
    "ds_write_b128 v14, v[112:115] offset:24576\n"
    "ds_write_b128 v14, v[116:119] offset:25600\n"
    "s_waitcnt lgkmcnt(14)\n"
    "ds_write_b128 v14, v[120:123] offset:26624\n"
    "ds_write_b128 v14, v[124:127] offset:27648\n"
    "s_waitcnt lgkmcnt(14)\n"
    "ds_write_b128 v14, v[128:131] offset:28672\n"
    "ds_write_b128 v14, v[132:135] offset:29696\n"
    "s_waitcnt lgkmcnt(14)\n"
    "ds_write_b128 v14, v[136:139] offset:30720\n"
    "ds_write_b128 v14, v[140:143] offset:31744\n"
    "s_waitcnt lgkmcnt(14)\n"
    "s_waitcnt vmcnt(0) lgkmcnt(0)\n"
    "v_mov_b32 v134, v14\n"
    "v_mov_b32 v128, -1\n"
    "v_mov_b32 v129, -1\n"
    "v_mov_b32 v130, -1\n"
    "v_mov_b32 v131, -1\n"
    "s_lshl_b32 s48, s44, 7\n"
    "s_add_u32 s48, s48, 22528\n"
    "v_mov_b32 v132, s48\n"
    "v_cmp_eq_u32 vcc, 0, v15\n"
    "v_mov_b32 v10, 2147483632\n"
    "s_nop 0\n"
    "v_cndmask_b32 v132, v10, v132, vcc\n"
    "v_and_b32 v9, 0xf0, v0\n"
    "v_lshrrev_b32 v10, 1, v12\n"
    "v_lshl_add_u32 v9, v10, 8, v9\n"
    "v_and_b32 v10, 1, v12\n"
    "v_lshl_add_u32 v9, v10, 3, v9\n"
    "s_lshl_b32 s48, s61, 8\n"
    "v_add_u32 v9, s48, v9\n"
    "cw_loopb_%=:\n"
    "v_mov_b32 v56, 0\n"
    "v_mov_b32 v57, 0\n"
    "v_mov_b32 v58, 0\n"
    "v_mov_b32 v59, 0\n"
    "v_mov_b32 v60, 0\n"
    "v_mov_b32 v61, 0\n"
    "v_mov_b32 v62, 0\n"
    "v_mov_b32 v63, 0\n"
    "v_mov_b32 v64, 0\n"
    "v_mov_b32 v65, 0\n"
    "v_mov_b32 v66, 0\n"
    "v_mov_b32 v67, 0\n"
    "v_mov_b32 v68, 0\n"
    "v_mov_b32 v69, 0\n"
    "v_mov_b32 v70, 0\n"
    "v_mov_b32 v71, 0\n"
    "v_mov_b32 v72, 0\n"
    "v_mov_b32 v73, 0\n"
    "v_mov_b32 v74, 0\n"
    "v_mov_b32 v75, 0\n"
    "v_mov_b32 v76, 0\n"
    "v_mov_b32 v77, 0\n"
    "v_mov_b32 v78, 0\n"
    "v_mov_b32 v79, 0\n"
    "ds_read_b128 v[80:83], v134 offset:0\n"
    "ds_read_b128 v[84:87], v134 offset:1024\n"
    "ds_read_b128 v[88:91], v134 offset:2048\n"
    "ds_read_b128 v[92:95], v134 offset:3072\n"
    "ds_read_b128 v[96:99], v134 offset:4096\n"
    "ds_read_b128 v[100:103], v134 offset:5120\n"
    "s_waitcnt lgkmcnt(0)\n"
    "s_barrier\n"
    "ds_read_b128 v[16:19], v0 offset:4096\n"
    "ds_read_b128 v[20:23], v0 offset:5120\n"
    "ds_read_b128 v[24:27], v0 offset:6144\n"
    "ds_read_b128 v[28:31], v0 offset:7168\n"
    "ds_read_b128 v[32:35], v0 offset:8192\n"
    "ds_read_b128 v[36:39], v0 offset:9216\n"
    "ds_read_b128 v[40:43], v0 offset:10240\n"
    "ds_read_b128 v[44:47], v0 offset:11264\n"
    "ds_read_b128 v[48:51], v0 offset:12288\n"
    "ds_read_b128 v[52:55], v0 offset:13312\n"
    "s_waitcnt lgkmcnt(9)\n"
    "v_smfmac_f32_16x16x64_bf16 v[56:59], v[16:19], v[80:87], v1\n"
    "ds_read_b128 v[80:83], v134 offset:6144\n"
    "v_smfmac_f32_16x16x64_bf16 v[60:63], v[16:19], a[0:7], v1\n"
    "ds_read_b128 v[84:87], v134 offset:7168\n"
    "v_smfmac_f32_16x16x64_bf16 v[64:67], v[16:19], a[8:15], v1\n"
    "v_smfmac_f32_16x16x64_bf16 v[68:71], v[16:19], v[88:95], v1\n"
    "ds_read_b128 v[88:91], v134 offset:8192\n"
    "v_smfmac_f32_16x16x64_bf16 v[72:75], v[16:19], a[16:23], v1\n"
    "ds_read_b128 v[92:95], v134 offset:9216\n"
    "v_smfmac_f32_16x16x64_bf16 v[76:79], v[16:19], a[24:31], v1\n"
    "s_waitcnt lgkmcnt(12)\n"
    "v_smfmac_f32_16x16x64_bf16 v[56:59], v[20:23], v[96:103], v1\n"
    "ds_read_b128 v[96:99], v134 offset:10240\n"
    "v_smfmac_f32_16x16x64_bf16 v[60:63], v[20:23], a[32:39], v1\n"
    "ds_read_b128 v[100:103], v134 offset:11264\n"
    "v_smfmac_f32_16x16x64_bf16 v[64:67], v[20:23], a[40:47], v1\n"
    "s_waitcnt lgkmcnt(4)\n"
    "v_smfmac_f32_16x16x64_bf16 v[68:71], v[20:23], v[80:87], v1\n"
    "ds_read_b128 v[80:83], v134 offset:12288\n"
    "v_smfmac_f32_16x16x64_bf16 v[72:75], v[20:23], a[48:55], v1\n"
    "ds_read_b128 v[84:87], v134 offset:13312\n"
    "v_smfmac_f32_16x16x64_bf16 v[76:79], v[20:23], a[56:63], v1\n"
    "s_waitcnt lgkmcnt(4)\n"
    "v_smfmac_f32_16x16x64_bf16 v[56:59], v[24:27], v[88:95], v1\n"
    "ds_read_b128 v[88:91], v134 offset:14336\n"
    "v_smfmac_f32_16x16x64_bf16 v[60:63], v[24:27], a[64:71], v1\n"
    "ds_read_b128 v[92:95], v134 offset:15360\n"
    "v_smfmac_f32_16x16x64_bf16 v[64:67], v[24:27], a[72:79], v1\n"
    "s_waitcnt lgkmcnt(4)\n"
    "v_smfmac_f32_16x16x64_bf16 v[68:71], v[24:27], v[96:103], v1\n"
    "ds_read_b128 v[96:99], v134 offset:16384\n"
    "v_smfmac_f32_16x16x64_bf16 v[72:75], v[24:27], a[80:87], v1\n"
    "ds_read_b128 v[100:103], v134 offset:17408\n"
    "v_smfmac_f32_16x16x64_bf16 v[76:79], v[24:27], a[88:95], v1\n"
    "s_waitcnt lgkmcnt(4)\n"
    "v_smfmac_f32_16x16x64_bf16 v[56:59], v[28:31], v[80:87], v1\n"
    "ds_read_b128 v[80:83], v134 offset:18432\n"
    "v_smfmac_f32_16x16x64_bf16 v[60:63], v[28:31], a[96:103], v1\n"
    "ds_read_b128 v[84:87], v134 offset:19456\n"
    "v_smfmac_f32_16x16x64_bf16 v[64:67], v[28:31], a[104:111], v1\n"
    "s_waitcnt lgkmcnt(4)\n"
    "v_smfmac_f32_16x16x64_bf16 v[68:71], v[28:31], v[88:95], v1\n"
    "ds_read_b128 v[88:91], v134 offset:20480\n"
    "v_smfmac_f32_16x16x64_bf16 v[72:75], v[28:31], a[112:119], v1\n"
    "ds_read_b128 v[92:95], v134 offset:21504\n"
    "v_smfmac_f32_16x16x64_bf16 v[76:79], v[28:31], a[120:127], v1\n"
    "s_waitcnt lgkmcnt(4)\n"
    "v_smfmac_f32_16x16x64_bf16 v[56:59], v[32:35], v[96:103], v1\n"
    "ds_read_b128 v[96:99], v134 offset:22528\n"
    "v_smfmac_f32_16x16x64_bf16 v[60:63], v[32:35], a[128:135], v1\n"
    "ds_read_b128 v[100:103], v134 offset:23552\n"
    "v_smfmac_f32_16x16x64_bf16 v[64:67], v[32:35], a[136:143], v1\n"
    "s_waitcnt lgkmcnt(4)\n"
    "v_smfmac_f32_16x16x64_bf16 v[68:71], v[32:35], v[80:87], v1\n"
    "ds_read_b128 v[80:83], v134 offset:24576\n"
    "v_smfmac_f32_16x16x64_bf16 v[72:75], v[32:35], a[144:151], v1\n"
    "ds_read_b128 v[84:87], v134 offset:25600\n"
    "v_smfmac_f32_16x16x64_bf16 v[76:79], v[32:35], a[152:159], v1\n"
    "s_waitcnt lgkmcnt(4)\n"
    "v_smfmac_f32_16x16x64_bf16 v[56:59], v[36:39], v[88:95], v1\n"
    "ds_read_b128 v[88:91], v134 offset:26624\n"
    "v_smfmac_f32_16x16x64_bf16 v[60:63], v[36:39], a[160:167], v1\n"
    "ds_read_b128 v[92:95], v134 offset:27648\n"
    "v_smfmac_f32_16x16x64_bf16 v[64:67], v[36:39], a[168:175], v1\n"
    "s_waitcnt lgkmcnt(4)\n"
    "v_smfmac_f32_16x16x64_bf16 v[68:71], v[36:39], v[96:103], v1\n"
    "ds_read_b128 v[96:99], v134 offset:28672\n"
    "v_smfmac_f32_16x16x64_bf16 v[72:75], v[36:39], a[176:183], v1\n"
    "ds_read_b128 v[100:103], v134 offset:29696\n"
    "v_smfmac_f32_16x16x64_bf16 v[76:79], v[36:39], a[184:191], v1\n"
    "s_waitcnt lgkmcnt(4)\n"
    "v_smfmac_f32_16x16x64_bf16 v[56:59], v[40:43], v[80:87], v1\n"
    "ds_read_b128 v[80:83], v134 offset:30720\n"
    "v_smfmac_f32_16x16x64_bf16 v[60:63], v[40:43], a[192:199], v1\n"
    "ds_read_b128 v[84:87], v134 offset:31744\n"
    "v_smfmac_f32_16x16x64_bf16 v[64:67], v[40:43], a[200:207], v1\n"
    "v_smfmac_f32_16x16x64_bf16 v[68:71], v[40:43], a[208:215], v1\n"
    "v_smfmac_f32_16x16x64_bf16 v[72:75], v[40:43], a[216:223], v1\n"
    "v_smfmac_f32_16x16x64_bf16 v[76:79], v[40:43], a[224:231], v1\n"
    "s_waitcnt lgkmcnt(4)\n"
    "v_smfmac_f32_16x16x64_bf16 v[56:59], v[44:47], v[88:95], v1\n"
    "v_smfmac_f32_16x16x64_bf16 v[60:63], v[44:47], a[232:239], v1\n"
    "v_smfmac_f32_16x16x64_bf16 v[64:67], v[44:47], a[240:247], v1\n"
    "v_smfmac_f32_16x16x64_bf16 v[68:71], v[44:47], a[248:255], v1\n"
    "v_smfmac_f32_16x16x64_bf16 v[72:75], v[44:47], v[160:167], v1\n"
    "v_smfmac_f32_16x16x64_bf16 v[76:79], v[44:47], v[168:175], v1\n"
    "s_waitcnt lgkmcnt(2)\n"
    "v_smfmac_f32_16x16x64_bf16 v[56:59], v[48:51], v[96:103], v1\n"
    "v_smfmac_f32_16x16x64_bf16 v[60:63], v[48:51], v[176:183], v1\n"
    "v_smfmac_f32_16x16x64_bf16 v[64:67], v[48:51], v[184:191], v1\n"
    "v_smfmac_f32_16x16x64_bf16 v[68:71], v[48:51], v[192:199], v1\n"
    "v_smfmac_f32_16x16x64_bf16 v[72:75], v[48:51], v[200:207], v1\n"
    "v_smfmac_f32_16x16x64_bf16 v[76:79], v[48:51], v[208:215], v1\n"
    "s_waitcnt lgkmcnt(0)\n"
    "v_smfmac_f32_16x16x64_bf16 v[56:59], v[52:55], v[80:87], v1\n"
    "v_smfmac_f32_16x16x64_bf16 v[60:63], v[52:55], v[216:223], v1\n"
    "v_smfmac_f32_16x16x64_bf16 v[64:67], v[52:55], v[224:231], v1\n"
    "v_smfmac_f32_16x16x64_bf16 v[68:71], v[52:55], v[232:239], v1\n"
    "v_smfmac_f32_16x16x64_bf16 v[72:75], v[52:55], v[240:247], v1\n"
    "v_smfmac_f32_16x16x64_bf16 v[76:79], v[52:55], v[248:255], v1\n"
    "v_mov_b32 v11, v11\n"
    "v_mov_b32 v11, v11\n"
    "v_mov_b32 v11, v11\n"
    "v_mov_b32 v11, v11\n"
    "v_permlane32_swap_b32 v56, v60\n"
    "v_permlane32_swap_b32 v57, v61\n"
    "v_permlane32_swap_b32 v58, v62\n"
    "v_permlane32_swap_b32 v59, v63\n"
    "v_pk_add_f32 v[56:57], v[56:57], v[60:61]\n"
    "v_pk_add_f32 v[58:59], v[58:59], v[62:63]\n"
    "v_cvt_pk_bf16_f32 v60, v56, v57\n"
    "v_cvt_pk_bf16_f32 v61, v58, v59\n"
    "ds_write_b64 v9, v[60:61] offset:0\n"
    "v_permlane32_swap_b32 v64, v68\n"
    "v_permlane32_swap_b32 v65, v69\n"
    "v_permlane32_swap_b32 v66, v70\n"
    "v_permlane32_swap_b32 v67, v71\n"
    "v_pk_add_f32 v[64:65], v[64:65], v[68:69]\n"
    "v_pk_add_f32 v[66:67], v[66:67], v[70:71]\n"
    "v_cvt_pk_bf16_f32 v68, v64, v65\n"
    "v_cvt_pk_bf16_f32 v69, v66, v67\n"
    "ds_write_b64 v9, v[68:69] offset:512\n"
    "v_permlane32_swap_b32 v72, v76\n"
    "v_permlane32_swap_b32 v73, v77\n"
    "v_permlane32_swap_b32 v74, v78\n"
    "v_permlane32_swap_b32 v75, v79\n"
    "v_pk_add_f32 v[72:73], v[72:73], v[76:77]\n"
    "v_pk_add_f32 v[74:75], v[74:75], v[78:79]\n"
    "v_cvt_pk_bf16_f32 v76, v72, v73\n"
    "v_cvt_pk_bf16_f32 v77, v74, v75\n"
    "ds_write_b64 v9, v[76:77] offset:1024\n"
    "s_cmp_lg_u32 s45, 1\n"
    "s_cbranch_scc1 cw_n1_%=\n"
    "s_add_u32 s49, s47, 3\n"
    "s_and_b32 s49, s49, 3\n"
    "s_mul_i32 s54, s49, 131072\n"
    "s_add_u32 s54, s54, 786432\n"
    "buffer_store_dwordx4 v[128:131], v3, s[40:43], s54 offen sc1\n"
    "s_nop 1\n"
    "cw_n1_%=:\n"
    "s_waitcnt lgkmcnt(0)\n"
    "s_barrier\n"
    "s_cmp_lg_u32 s45, 2\n"
    "s_cbranch_scc1 cw_n2_%=\n"
    "ds_read_b128 v[16:19], v0 offset:0\n"
    "ds_read_b128 v[20:23], v0 offset:1024\n"
    "ds_read_b128 v[24:27], v0 offset:2048\n"
    "ds_read_b128 v[28:31], v0 offset:3072\n"
    "s_waitcnt lgkmcnt(0)\n"
    "cw_n2_%=:\n"
    "s_waitcnt vmcnt(0)\n"
    "s_barrier\n"
    "s_cmp_lg_u32 s45, 2\n"
    "s_cbranch_scc1 cw_n3_%=\n"
    "s_mul_i32 s49, s47, 80\n"
    "s_add_u32 s49, s49, s44\n"
    "s_lshl_b32 s49, s49, 12\n"
    "s_add_u32 s49, s49, 196673536\n"
    "v_mov_b32 v10, s47\n"
    "buffer_store_dword v10, v132, s[40:43], s58 offen sc1\n"
    "buffer_store_dwordx4 v[16:19], v0, s[40:43], s49 offen sc1\n"
    "buffer_store_dwordx4 v[20:23], v0, s[40:43], s49 offen offset:1024 sc1\n"
    "buffer_store_dwordx4 v[24:27], v0, s[40:43], s49 offen offset:2048 sc1\n"
    "buffer_store_dwordx4 v[28:31], v0, s[40:43], s49 offen offset:3072 sc1\n"
    "s_nop 1\n"
    "cw_n3_%=:\n"
    "s_add_u32 s47, s47, 1\n"
    "s_cmp_lt_u32 s47, 64\n"
    "s_cbranch_scc1 cw_loopb_%=\n"
    "s_cmp_lg_u32 s45, 2\n"
    "s_cbranch_scc1 cw_end_%=\n"
    "v_mov_b32 v10, 64\n"
    "s_waitcnt vmcnt(0)\n"
    "buffer_store_dword v10, v132, s[40:43], s58 offen sc1\n"
    "s_nop 1\n"
    "s_branch cw_end_%=\n"
    "s_branch cw_end_%=\n"
    "cw_abort_%=:\n"
    "v_mov_b32 v10, 1\n"
    "v_mov_b32 v11, 1024\n"
    "buffer_store_dword v10, v11, s[40:43], s58 offen sc1\n"
    "cw_end_%=:\n"
    "s_endpgm\n"
    :: ROLE_INPUTS : ROLE_CLOBBERS);
}
DEV void role_projq(unsigned lo, unsigned hi, int id, int w, int aux, const Tune& tn) {
    asm volatile(
    "s_mov_b32 s40, %0\n"
    "s_mov_b32 s41, %1\n"
    "s_mov_b32 s42, %2\n"
    "s_mov_b32 s43, %3\n"
    "s_mov_b32 s44, %4\n"
    "s_mov_b32 s45, %5\n"
    "s_mov_b32 s46, %6\n"
    "s_mov_b32 s66, %7\n"
    "s_mov_b32 s67, %8\n"
    "s_mov_b32 s68, %9\n"
    "s_mov_b32 s69, %10\n"
    "s_mov_b32 s70, %11\n"
    "s_mov_b32 s71, %12\n"
    "s_mov_b32 s72, %13\n"
    "s_mov_b32 s73, %14\n"
    "s_mov_b64 exec, -1\n"
    "v_mbcnt_lo_u32_b32 v15, -1, 0\n"
    "v_mbcnt_hi_u32_b32 v15, -1, v15\n"
    "v_lshlrev_b32 v0, 4, v15\n"
    "s_mov_b32 s58, 2097152\n"
    "s_mov_b32 s92, 0\n"
    "s_lshl_b32 s48, s46, 5\n"
    "s_add_u32 s48, s48, s44\n"
    "s_mul_i32 s48, s48, 12\n"
    "s_mul_i32 s49, s45, 3\n"
    "s_add_u32 s48, s48, s49\n"
    "s_mul_i32 s48, s48, 20480\n"
    "s_add_u32 s48, s48, 2162688\n"
    "s_mul_i32 s62, s45, 10240\n"
    "s_add_u32 s50, s62, 59834368\n"
    "s_mov_b32 s61, s62\n"
    "s_add_u32 m0, s61, 0\n"
    "s_add_u32 s51, s50, 0\n"
    "buffer_load_dwordx4 v0, s[40:43], s51 offen offset:0 lds\n"
    "buffer_load_dwordx4 v0, s[40:43], s51 offen offset:1024 lds\n"
    "buffer_load_dwordx4 v0, s[40:43], s51 offen offset:2048 lds\n"
    "buffer_load_dwordx4 v0, s[40:43], s51 offen offset:3072 lds\n"
    "s_add_u32 m0, s61, 4096\n"
    "s_add_u32 s51, s50, 4096\n"
    "buffer_load_dwordx4 v0, s[40:43], s51 offen offset:0 lds\n"
    "buffer_load_dwordx4 v0, s[40:43], s51 offen offset:1024 lds\n"
    "buffer_load_dwordx4 v0, s[40:43], s51 offen offset:2048 lds\n"
    "buffer_load_dwordx4 v0, s[40:43], s51 offen offset:3072 lds\n"
    "s_add_u32 m0, s61, 8192\n"
    "s_add_u32 s51, s50, 8192\n"
    "buffer_load_dwordx4 v0, s[40:43], s51 offen offset:0 lds\n"
    "buffer_load_dwordx4 v0, s[40:43], s51 offen offset:1024 lds\n"
    "s_add_u32 s50, s50, 40960\n"
    "s_add_u32 s61, s61, 40960\n"
    "s_cmp_ge_u32 s61, 122880\n"
    "s_cselect_b32 s51, 122880, 0\n"
    "s_sub_u32 s61, s61, s51\n"
    "s_add_u32 m0, s61, 0\n"
    "s_add_u32 s51, s50, 0\n"
    "buffer_load_dwordx4 v0, s[40:43], s51 offen offset:0 lds\n"
    "buffer_load_dwordx4 v0, s[40:43], s51 offen offset:1024 lds\n"
    "buffer_load_dwordx4 v0, s[40:43], s51 offen offset:2048 lds\n"
    "buffer_load_dwordx4 v0, s[40:43], s51 offen offset:3072 lds\n"
    "s_add_u32 m0, s61, 4096\n"
    "s_add_u32 s51, s50, 4096\n"
    "buffer_load_dwordx4 v0, s[40:43], s51 offen offset:0 lds\n"
    "buffer_load_dwordx4 v0, s[40:43], s51 offen offset:1024 lds\n"
    "buffer_load_dwordx4 v0, s[40:43], s51 offen offset:2048 lds\n"
    "buffer_load_dwordx4 v0, s[40:43], s51 offen offset:3072 lds\n"
    "s_add_u32 m0, s61, 8192\n"
    "s_add_u32 s51, s50, 8192\n"
    "buffer_load_dwordx4 v0, s[40:43], s51 offen offset:0 lds\n"
    "buffer_load_dwordx4 v0, s[40:43], s51 offen offset:1024 lds\n"
    "s_add_u32 s50, s50, 40960\n"
    "s_add_u32 s61, s61, 40960\n"
    "s_cmp_ge_u32 s61, 122880\n"
    "s_cselect_b32 s51, 122880, 0\n"
    "s_sub_u32 s61, s61, s51\n"
    "buffer_load_dwordx4 a[0:3], v0, s[40:43], s48 offen\n"
    "buffer_load_dwordx4 a[4:7], v0, s[40:43], s48 offen offset:1024\n"
    "buffer_load_dwordx4 a[8:11], v0, s[40:43], s48 offen offset:2048\n"
    "buffer_load_dwordx4 a[12:15], v0, s[40:43], s48 offen offset:3072\n"
    "s_add_u32 s48, s48, 4096\n"
    "buffer_load_dwordx4 a[16:19], v0, s[40:43], s48 offen\n"
    "buffer_load_dwordx4 a[20:23], v0, s[40:43], s48 offen offset:1024\n"
    "buffer_load_dwordx4 a[24:27], v0, s[40:43], s48 offen offset:2048\n"
    "buffer_load_dwordx4 a[28:31], v0, s[40:43], s48 offen offset:3072\n"
    "s_add_u32 s48, s48, 4096\n"
    "buffer_load_dwordx4 a[32:35], v0, s[40:43], s48 offen\n"
    "buffer_load_dwordx4 a[36:39], v0, s[40:43], s48 offen offset:1024\n"
    "buffer_load_dwordx4 a[40:43], v0, s[40:43], s48 offen offset:2048\n"
    "buffer_load_dwordx4 a[44:47], v0, s[40:43], s48 offen offset:3072\n"
    "s_add_u32 s48, s48, 4096\n"
    "buffer_load_dwordx4 a[48:51], v0, s[40:43], s48 offen\n"
    "buffer_load_dwordx4 a[52:55], v0, s[40:43], s48 offen offset:1024\n"
    "buffer_load_dwordx4 a[56:59], v0, s[40:43], s48 offen offset:2048\n"
    "buffer_load_dwordx4 a[60:63], v0, s[40:43], s48 offen offset:3072\n"
    "s_add_u32 s48, s48, 4096\n"
    "buffer_load_dwordx4 a[64:67], v0, s[40:43], s48 offen\n"
    "buffer_load_dwordx4 a[68:71], v0, s[40:43], s48 offen offset:1024\n"
    "buffer_load_dwordx4 a[72:75], v0, s[40:43], s48 offen offset:2048\n"
    "buffer_load_dwordx4 a[76:79], v0, s[40:43], s48 offen offset:3072\n"
    "s_add_u32 s48, s48, 4096\n"
    "buffer_load_dwordx4 a[80:83], v0, s[40:43], s48 offen\n"
    "buffer_load_dwordx4 a[84:87], v0, s[40:43], s48 offen offset:1024\n"
    "buffer_load_dwordx4 a[88:91], v0, s[40:43], s48 offen offset:2048\n"
    "buffer_load_dwordx4 a[92:95], v0, s[40:43], s48 offen offset:3072\n"
    "s_add_u32 s48, s48, 4096\n"
    "buffer_load_dwordx4 a[96:99], v0, s[40:43], s48 offen\n"
    "buffer_load_dwordx4 a[100:103], v0, s[40:43], s48 offen offset:1024\n"
    "buffer_load_dwordx4 a[104:107], v0, s[40:43], s48 offen offset:2048\n"
    "buffer_load_dwordx4 a[108:111], v0, s[40:43], s48 offen offset:3072\n"
    "s_add_u32 s48, s48, 4096\n"
    "buffer_load_dwordx4 a[112:115], v0, s[40:43], s48 offen\n"
    "buffer_load_dwordx4 a[116:119], v0, s[40:43], s48 offen offset:1024\n"
    "buffer_load_dwordx4 a[120:123], v0, s[40:43], s48 offen offset:2048\n"
    "buffer_load_dwordx4 a[124:127], v0, s[40:43], s48 offen offset:3072\n"
    "s_add_u32 s48, s48, 4096\n"
    "buffer_load_dwordx4 a[128:131], v0, s[40:43], s48 offen\n"
    "buffer_load_dwordx4 a[132:135], v0, s[40:43], s48 offen offset:1024\n"
    "buffer_load_dwordx4 a[136:139], v0, s[40:43], s48 offen offset:2048\n"
    "buffer_load_dwordx4 a[140:143], v0, s[40:43], s48 offen offset:3072\n"
    "s_add_u32 s48, s48, 4096\n"
    "buffer_load_dwordx4 a[144:147], v0, s[40:43], s48 offen\n"
    "buffer_load_dwordx4 a[148:151], v0, s[40:43], s48 offen offset:1024\n"
    "buffer_load_dwordx4 a[152:155], v0, s[40:43], s48 offen offset:2048\n"
    "buffer_load_dwordx4 a[156:159], v0, s[40:43], s48 offen offset:3072\n"
    "s_add_u32 s48, s48, 4096\n"
    "buffer_load_dwordx4 a[160:163], v0, s[40:43], s48 offen\n"
    "buffer_load_dwordx4 a[164:167], v0, s[40:43], s48 offen offset:1024\n"
    "buffer_load_dwordx4 a[168:171], v0, s[40:43], s48 offen offset:2048\n"
    "s_waitcnt vmcnt(61)\n"
    "buffer_load_dwordx4 a[172:175], v0, s[40:43], s48 offen offset:3072\n"
    "s_add_u32 s48, s48, 4096\n"
    "buffer_load_dwordx4 a[176:179], v0, s[40:43], s48 offen\n"
    "s_waitcnt vmcnt(61)\n"
    "buffer_load_dwordx4 a[180:183], v0, s[40:43], s48 offen offset:1024\n"
    "buffer_load_dwordx4 a[184:187], v0, s[40:43], s48 offen offset:2048\n"
    "s_waitcnt vmcnt(61)\n"
    "buffer_load_dwordx4 a[188:191], v0, s[40:43], s48 offen offset:3072\n"
    "s_add_u32 s48, s48, 4096\n"
    "buffer_load_dwordx4 a[192:195], v0, s[40:43], s48 offen\n"
    "s_waitcnt vmcnt(61)\n"
    "buffer_load_dwordx4 a[196:199], v0, s[40:43], s48 offen offset:1024\n"
    "buffer_load_dwordx4 a[200:203], v0, s[40:43], s48 offen offset:2048\n"
    "s_waitcnt vmcnt(61)\n"
    "buffer_load_dwordx4 a[204:207], v0, s[40:43], s48 offen offset:3072\n"
    "s_add_u32 s48, s48, 4096\n"
    "buffer_load_dwordx4 a[208:211], v0, s[40:43], s48 offen\n"
    "s_waitcnt vmcnt(61)\n"
    "buffer_load_dwordx4 a[212:215], v0, s[40:43], s48 offen offset:1024\n"
    "buffer_load_dwordx4 a[216:219], v0, s[40:43], s48 offen offset:2048\n"
    "s_waitcnt vmcnt(61)\n"
    "buffer_load_dwordx4 a[220:223], v0, s[40:43], s48 offen offset:3072\n"
    "s_add_u32 s48, s48, 4096\n"
    "buffer_load_dwordx4 a[224:227], v0, s[40:43], s48 offen\n"
    "s_waitcnt vmcnt(61)\n"
    "buffer_load_dwordx4 a[228:231], v0, s[40:43], s48 offen offset:1024\n"
    "buffer_load_dwordx4 a[232:235], v0, s[40:43], s48 offen offset:2048\n"
    "s_waitcnt vmcnt(61)\n"
    "buffer_load_dwordx4 a[236:239], v0, s[40:43], s48 offen offset:3072\n"
    "v_mov_b32 v1, v0\n"
    "s_mov_b32 s64, 0\n"
    "s_lshl_b32 s48, s44, 7\n"
    "s_add_u32 s49, s48, 18432\n"
    "v_mov_b32 v3, s49\n"
    "v_or_b32 v4, s45, v15\n"
    "v_cmp_eq_u32 vcc, 0, v4\n"
    "v_mov_b32 v4, 2147483632\n"
    "s_nop 0\n"
    "v_cndmask_b32 v3, v4, v3, vcc\n"
    "v_lshrrev_b32 v13, 4, v15\n"
    "v_and_b32 v14, 15, v15\n"
    "s_lshl_b32 s49, s45, 11\n"
    "s_add_u32 s49, s49, 122880\n"
    "v_add_u32 v6, s49, v0\n"
    "s_lshr_b32 s49, s45, 1\n"
    "s_lshl_b32 s49, s49, 11\n"
    "s_and_b32 s53, s45, 1\n"
    "s_lshl_b32 s53, s53, 3\n"
    "s_add_u32 s49, s49, s53\n"
    "s_add_u32 s49, s49, 131072\n"
    "v_add_u32 v7, s49, v0\n"
    "s_lshl_b32 s49, s45, 10\n"
    "s_add_u32 s49, s49, 122880\n"
    "v_add_u32 v8, s49, v0\n"
    "v_mov_b32 v9, 0\n"
    "s_add_u32 s49, s48, 14336\n"
    "v_mov_b32 v10, s49\n"
    "v_and_b32 v11, 7, v15\n"
    "v_lshlrev_b32 v11, 4, v11\n"
    "v_lshl_add_u32 v11, v13, 7, v11\n"
    "s_bfe_u32 s49, s44, 0x20001\n"
    "s_lshl_b32 s49, s49, 8\n"
    "v_add_u32 v12, s49, v0\n"
    "v_cmp_gt_u32 vcc, 16, v15\n"
    "v_mov_b32 v4, 2147483632\n"
    "s_nop 0\n"
    "v_cndmask_b32 v12, v4, v12, vcc\n"
    "s_lshl_b32 s49, s45, 4\n"
    "v_add_u32 v14, s49, v14\n"
    "v_lshlrev_b32 v13, 2, v13\n"
    "v_sub_u32 v14, v14, v13\n"
    "v_mov_b32 v4, 0xff800000\n"
    "v_cmp_gt_i32 vcc, 0, v14\n"
    "s_nop 1\n"
    "v_cndmask_b32 v212, 0, v4, vcc\n"
    "v_cmp_gt_i32 vcc, 1, v14\n"
    "s_nop 1\n"
    "v_cndmask_b32 v213, 0, v4, vcc\n"
    "v_cmp_gt_i32 vcc, 2, v14\n"
    "s_nop 1\n"
    "v_cndmask_b32 v214, 0, v4, vcc\n"
    "v_cmp_gt_i32 vcc, 3, v14\n"
    "s_nop 1\n"
    "v_cndmask_b32 v215, 0, v4, vcc\n"
    "v_cmp_gt_i32 vcc, 16, v14\n"
    "s_nop 1\n"
    "v_cndmask_b32 v216, 0, v4, vcc\n"
    "v_cmp_gt_i32 vcc, 17, v14\n"
    "s_nop 1\n"
    "v_cndmask_b32 v217, 0, v4, vcc\n"
    "v_cmp_gt_i32 vcc, 18, v14\n"
    "s_nop 1\n"
    "v_cndmask_b32 v218, 0, v4, vcc\n"
    "v_cmp_gt_i32 vcc, 19, v14\n"
    "s_nop 1\n"
    "v_cndmask_b32 v219, 0, v4, vcc\n"
    "v_cmp_lt_u32 vcc, 31, v15\n"
    "s_nop 1\n"
    "v_cndmask_b32 v220, 0, v4, vcc\n"
    "v_cndmask_b32 v221, 0, v4, vcc\n"
    "v_cndmask_b32 v222, 0, v4, vcc\n"
    "v_cndmask_b32 v223, 0, v4, vcc\n"
    "s_mov_b32 s47, 0\n"
    "s_waitcnt vmcnt(0)\n"
    "s_waitcnt lgkmcnt(0)\n"
    "s_barrier\n"
    "v_add_u32 v2, 8, v6\n"
    "ds_read_b128 v[80:83], v1 offset:0\n"
    "ds_read_b128 v[84:87], v1 offset:20480\n"
    "ds_read_b128 v[88:91], v1 offset:1024\n"
    "ds_read_b128 v[92:95], v1 offset:21504\n"
    "ds_read_b128 v[96:99], v1 offset:2048\n"
    "pq_loop_%=:\n"
    "s_cmp_gt_u32 s45, 1\n"
    "s_cbranch_scc1 pq_na0_%=\n"
    "s_cmp_eq_u32 s47, 0\n"
    "s_cbranch_scc1 pq_na0_%=\n"
    "s_mov_b32 s60, 0\n"
    "v_cmp_gt_u32 vcc, s47, v9\n"
    "s_nop 4\n"
    "s_cbranch_vccz pq_kma0_ok_%=\n"
    "pq_kma0_poll_%=:\n"
    "buffer_load_dword v9, v10, s[40:43], s58 offen sc1\n"
    "s_waitcnt vmcnt(0)\n"
    "v_cmp_gt_u32 vcc, s47, v9\n"
    "s_nop 4\n"
    "s_cbranch_vccz pq_kma0_ok_%=\n"
    "s_sleep 2\n"
    "s_add_u32 s60, s60, 1\n"
    "s_cmp_gt_u32 s60, 65536\n"
    "s_cbranch_scc1 pq_abort_%=\n"
    "s_and_b32 s59, s60, 63\n"
    "s_cbranch_scc1 pq_kma0_poll_%=\n"
    "v_mov_b32 v4, 1024\n"
    "buffer_load_dword v4, v4, s[40:43], s58 offen sc1\n"
    "s_waitcnt vmcnt(0)\n"
    "v_readfirstlane_b32 s59, v4\n"
    "s_nop 0\n"
    "s_cmp_lg_u32 s59, 0\n"
    "s_nop 2\n"
    "s_cbranch_scc1 pq_end_%=\n"
    "s_branch pq_kma0_poll_%=\n"
    "pq_kma0_ok_%=:\n"
    "s_sub_u32 s53, s47, 1\n"
    "s_lshl_b32 s53, s53, 5\n"
    "s_add_u32 s53, s53, s44\n"
    "s_mul_i32 s54, s53, 12288\n"
    "s_add_u32 s54, s54, 62455808\n"
    "s_add_u32 s55, s54, 4096\n"
    "s_add_u32 s56, s54, 8192\n"
    "s_mul_i32 s57, s53, 3072\n"
    "s_add_u32 s57, s57, 217645056\n"
    "buffer_load_dwordx4 v[116:119], v0, s[40:43], s54 offen sc1\n"
    "buffer_load_dwordx4 v[120:123], v0, s[40:43], s54 offen offset:1024 sc1\n"
    "buffer_load_dwordx4 v[124:127], v0, s[40:43], s54 offen offset:3072 sc1\n"
    "buffer_load_dwordx4 v[128:131], v0, s[40:43], s55 offen sc1\n"
    "buffer_load_dwordx4 v[132:135], v0, s[40:43], s55 offen offset:2048 sc1\n"
    "buffer_load_dwordx4 v[136:139], v0, s[40:43], s55 offen offset:3072 sc1\n"
    "buffer_load_dwordx4 v[140:143], v0, s[40:43], s56 offen offset:1024 sc1\n"
    "buffer_load_dwordx4 v[144:147], v0, s[40:43], s56 offen offset:2048 sc1\n"
    "buffer_load_dwordx4 v[148:151], v0, s[40:43], s54 offen offset:2048 sc1\n"
    "buffer_load_dwordx4 v[152:155], v0, s[40:43], s55 offen offset:1024 sc1\n"
    "buffer_load_dwordx4 v[156:159], v0, s[40:43], s56 offen sc1\n"
    "buffer_load_dwordx4 v[160:163], v0, s[40:43], s56 offen offset:3072 sc1\n"
    "buffer_load_dwordx4 v[164:167], v11, s[40:43], s57 offen sc1\n"
    "buffer_load_dwordx4 v[168:171], v11, s[40:43], s57 offen offset:512 sc1\n"
    "buffer_load_dwordx4 v[172:175], v11, s[40:43], s57 offen offset:1024 sc1\n"
    "buffer_load_dwordx4 v[176:179], v11, s[40:43], s57 offen offset:1536 sc1\n"
    "buffer_load_dwordx4 v[180:183], v11, s[40:43], s57 offen offset:2048 sc1\n"
    "buffer_load_dwordx4 v[184:187], v11, s[40:43], s57 offen offset:2560 sc1\n"
    "s_nop 1\n"
    "pq_na0_%=:\n"
    "s_waitcnt lgkmcnt(4)\n"
    "v_mfma_f32_16x16x32_bf16 v[16:19], a[0:3], v[80:83], 0\n"
    "v_mfma_f32_16x16x32_bf16 v[24:27], a[80:83], v[80:83], 0\n"
    "v_mfma_f32_16x16x32_bf16 v[32:35], a[160:163], v[80:83], 0\n"
    "ds_read_b128 v[80:83], v1 offset:22528\n"
    "s_waitcnt lgkmcnt(4)\n"
    "v_mfma_f32_16x16x32_bf16 v[20:23], a[0:3], v[84:87], 0\n"
    "v_mfma_f32_16x16x32_bf16 v[28:31], a[80:83], v[84:87], 0\n"
    "v_mfma_f32_16x16x32_bf16 v[36:39], a[160:163], v[84:87], 0\n"
    "ds_read_b128 v[84:87], v1 offset:3072\n"
    "s_waitcnt lgkmcnt(4)\n"
    "v_mfma_f32_16x16x32_bf16 v[16:19], a[4:7], v[88:91], v[16:19]\n"
    "v_mfma_f32_16x16x32_bf16 v[24:27], a[84:87], v[88:91], v[24:27]\n"
    "v_mfma_f32_16x16x32_bf16 v[32:35], a[164:167], v[88:91], v[32:35]\n"
    "ds_read_b128 v[88:91], v1 offset:23552\n"
    "s_waitcnt lgkmcnt(4)\n"
    "v_mfma_f32_16x16x32_bf16 v[20:23], a[4:7], v[92:95], v[20:23]\n"
    "v_cvt_pk_bf16_f32 v104, v40, v41\n"
    "v_mfma_f32_16x16x32_bf16 v[28:31], a[84:87], v[92:95], v[28:31]\n"
    "v_cvt_pk_bf16_f32 v105, v42, v43\n"
    "v_mfma_f32_16x16x32_bf16 v[36:39], a[164:167], v[92:95], v[36:39]\n"
    "ds_read_b128 v[92:95], v1 offset:4096\n"
    "s_waitcnt lgkmcnt(4)\n"
    "v_mfma_f32_16x16x32_bf16 v[16:19], a[8:11], v[96:99], v[16:19]\n"
    "v_cvt_pk_bf16_f32 v106, v44, v45\n"
    "v_mfma_f32_16x16x32_bf16 v[24:27], a[88:91], v[96:99], v[24:27]\n"
    "v_cvt_pk_bf16_f32 v107, v46, v47\n"
    "v_mfma_f32_16x16x32_bf16 v[32:35], a[168:171], v[96:99], v[32:35]\n"
    "ds_read_b128 v[96:99], v1 offset:24576\n"
    "s_waitcnt lgkmcnt(4)\n"
    "v_mfma_f32_16x16x32_bf16 v[20:23], a[8:11], v[80:83], v[20:23]\n"
    "ds_write2st64_b64 v6, v[104:105], v[106:107] offset0:24 offset1:26\n"
    "v_mfma_f32_16x16x32_bf16 v[28:31], a[88:91], v[80:83], v[28:31]\n"
    "v_cvt_pk_bf16_f32 v108, v48, v49\n"
    "v_mfma_f32_16x16x32_bf16 v[36:39], a[168:171], v[80:83], v[36:39]\n"
    "ds_read_b128 v[80:83], v1 offset:5120\n"
    "s_waitcnt lgkmcnt(5)\n"
    "v_mfma_f32_16x16x32_bf16 v[16:19], a[12:15], v[84:87], v[16:19]\n"
    "v_cvt_pk_bf16_f32 v109, v50, v51\n"
    "v_mfma_f32_16x16x32_bf16 v[24:27], a[92:95], v[84:87], v[24:27]\n"
    "v_cvt_pk_bf16_f32 v110, v52, v53\n"
    "v_mfma_f32_16x16x32_bf16 v[32:35], a[172:175], v[84:87], v[32:35]\n"
    "ds_read_b128 v[84:87], v1 offset:25600\n"
    "s_waitcnt lgkmcnt(5)\n"
    "v_mfma_f32_16x16x32_bf16 v[20:23], a[12:15], v[88:91], v[20:23]\n"
    "v_cvt_pk_bf16_f32 v111, v54, v55\n"
    "v_mfma_f32_16x16x32_bf16 v[28:31], a[92:95], v[88:91], v[28:31]\n"
    "ds_write2st64_b64 v2, v[108:109], v[110:111] offset0:24 offset1:26\n"
    "v_mfma_f32_16x16x32_bf16 v[36:39], a[172:175], v[88:91], v[36:39]\n"
    "ds_read_b128 v[88:91], v1 offset:6144\n"
    "s_waitcnt lgkmcnt(6)\n"
    "v_mfma_f32_16x16x32_bf16 v[16:19], a[16:19], v[92:95], v[16:19]\n"
    "v_cvt_pk_bf16_f32 v112, v56, v57\n"
    "v_mfma_f32_16x16x32_bf16 v[24:27], a[96:99], v[92:95], v[24:27]\n"
    "v_cvt_pk_bf16_f32 v113, v58, v59\n"
    "v_mfma_f32_16x16x32_bf16 v[32:35], a[176:179], v[92:95], v[32:35]\n"
    "ds_read_b128 v[92:95], v1 offset:26624\n"
    "s_waitcnt lgkmcnt(6)\n"
    "v_mfma_f32_16x16x32_bf16 v[20:23], a[16:19], v[96:99], v[20:23]\n"
    "v_cvt_pk_bf16_f32 v114, v60, v61\n"
    "v_mfma_f32_16x16x32_bf16 v[28:31], a[96:99], v[96:99], v[28:31]\n"
    "v_cvt_pk_bf16_f32 v115, v62, v63\n"
    "v_mfma_f32_16x16x32_bf16 v[36:39], a[176:179], v[96:99], v[36:39]\n"
    "ds_read_b128 v[96:99], v1 offset:7168\n"
    "s_waitcnt lgkmcnt(5)\n"
    "v_mfma_f32_16x16x32_bf16 v[16:19], a[20:23], v[80:83], v[16:19]\n"
    "ds_write2st64_b64 v7, v[112:113], v[114:115] offset0:24 offset1:26\n"
    "v_mfma_f32_16x16x32_bf16 v[24:27], a[100:103], v[80:83], v[24:27]\n"
    "v_mfma_f32_16x16x32_bf16 v[32:35], a[180:183], v[80:83], v[32:35]\n"
    "ds_read_b128 v[80:83], v1 offset:27648\n"
    "s_waitcnt lgkmcnt(6)\n"
    "v_mfma_f32_16x16x32_bf16 v[20:23], a[20:23], v[84:87], v[20:23]\n"
    "v_mfma_f32_16x16x32_bf16 v[28:31], a[100:103], v[84:87], v[28:31]\n"
    "v_mfma_f32_16x16x32_bf16 v[36:39], a[180:183], v[84:87], v[36:39]\n"
    "ds_read_b128 v[84:87], v1 offset:8192\n"
    "s_waitcnt lgkmcnt(5)\n"
    "v_mfma_f32_16x16x32_bf16 v[16:19], a[24:27], v[88:91], v[16:19]\n"
    "v_mfma_f32_16x16x32_bf16 v[24:27], a[104:107], v[88:91], v[24:27]\n"
    "v_mfma_f32_16x16x32_bf16 v[32:35], a[184:187], v[88:91], v[32:35]\n"
    "ds_read_b128 v[88:91], v1 offset:28672\n"
    "s_waitcnt lgkmcnt(5)\n"
    "v_mfma_f32_16x16x32_bf16 v[20:23], a[24:27], v[92:95], v[20:23]\n"
    "v_mfma_f32_16x16x32_bf16 v[28:31], a[104:107], v[92:95], v[28:31]\n"
    "v_mfma_f32_16x16x32_bf16 v[36:39], a[184:187], v[92:95], v[36:39]\n"
    "ds_read_b128 v[92:95], v1 offset:9216\n"
    "s_waitcnt lgkmcnt(5)\n"
    "v_mfma_f32_16x16x32_bf16 v[16:19], a[28:31], v[96:99], v[16:19]\n"
    "v_mfma_f32_16x16x32_bf16 v[24:27], a[108:111], v[96:99], v[24:27]\n"
    "v_mfma_f32_16x16x32_bf16 v[32:35], a[188:191], v[96:99], v[32:35]\n"
    "ds_read_b128 v[96:99], v1 offset:29696\n"
    "s_waitcnt lgkmcnt(4)\n"
    "v_mfma_f32_16x16x32_bf16 v[20:23], a[28:31], v[80:83], v[20:23]\n"
    "v_mfma_f32_16x16x32_bf16 v[28:31], a[108:111], v[80:83], v[28:31]\n"
    "v_mfma_f32_16x16x32_bf16 v[36:39], a[188:191], v[80:83], v[36:39]\n"
    "ds_read_b128 v[80:83], v1 offset:10240\n"
    "s_waitcnt lgkmcnt(4)\n"
    "v_mfma_f32_16x16x32_bf16 v[16:19], a[32:35], v[84:87], v[16:19]\n"
    "v_mfma_f32_16x16x32_bf16 v[24:27], a[112:115], v[84:87], v[24:27]\n"
    "v_mfma_f32_16x16x32_bf16 v[32:35], a[192:195], v[84:87], v[32:35]\n"
    "ds_read_b128 v[84:87], v1 offset:30720\n"
    "s_waitcnt lgkmcnt(4)\n"
    "v_mfma_f32_16x16x32_bf16 v[20:23], a[32:35], v[88:91], v[20:23]\n"
    "v_mfma_f32_16x16x32_bf16 v[28:31], a[112:115], v[88:91], v[28:31]\n"
    "v_mfma_f32_16x16x32_bf16 v[36:39], a[192:195], v[88:91], v[36:39]\n"
    "ds_read_b128 v[88:91], v1 offset:11264\n"
    "s_waitcnt lgkmcnt(4)\n"
    "v_mfma_f32_16x16x32_bf16 v[16:19], a[36:39], v[92:95], v[16:19]\n"
    "v_mfma_f32_16x16x32_bf16 v[24:27], a[116:119], v[92:95], v[24:27]\n"
    "v_mfma_f32_16x16x32_bf16 v[32:35], a[196:199], v[92:95], v[32:35]\n"
    "ds_read_b128 v[92:95], v1 offset:31744\n"
    "s_waitcnt lgkmcnt(4)\n"
    "v_mfma_f32_16x16x32_bf16 v[20:23], a[36:39], v[96:99], v[20:23]\n"
    "v_mfma_f32_16x16x32_bf16 v[28:31], a[116:119], v[96:99], v[28:31]\n"
    "v_mfma_f32_16x16x32_bf16 v[36:39], a[196:199], v[96:99], v[36:39]\n"
    "ds_read_b128 v[96:99], v1 offset:12288\n"
    "s_waitcnt lgkmcnt(4)\n"
    "v_mfma_f32_16x16x32_bf16 v[16:19], a[40:43], v[80:83], v[16:19]\n"
    "v_mfma_f32_16x16x32_bf16 v[24:27], a[120:123], v[80:83], v[24:27]\n"
    "v_mfma_f32_16x16x32_bf16 v[32:35], a[200:203], v[80:83], v[32:35]\n"
    "ds_read_b128 v[80:83], v1 offset:32768\n"
    "s_waitcnt lgkmcnt(4)\n"
    "v_mfma_f32_16x16x32_bf16 v[20:23], a[40:43], v[84:87], v[20:23]\n"
    "v_mfma_f32_16x16x32_bf16 v[28:31], a[120:123], v[84:87], v[28:31]\n"
    "v_mfma_f32_16x16x32_bf16 v[36:39], a[200:203], v[84:87], v[36:39]\n"
    "ds_read_b128 v[84:87], v1 offset:13312\n"
    "s_waitcnt lgkmcnt(4)\n"
    "v_mfma_f32_16x16x32_bf16 v[16:19], a[44:47], v[88:91], v[16:19]\n"
    "v_mfma_f32_16x16x32_bf16 v[24:27], a[124:127], v[88:91], v[24:27]\n"
    "v_mfma_f32_16x16x32_bf16 v[32:35], a[204:207], v[88:91], v[32:35]\n"
    "ds_read_b128 v[88:91], v1 offset:33792\n"
    "s_waitcnt lgkmcnt(4)\n"
    "v_mfma_f32_16x16x32_bf16 v[20:23], a[44:47], v[92:95], v[20:23]\n"
    "v_mfma_f32_16x16x32_bf16 v[28:31], a[124:127], v[92:95], v[28:31]\n"
    "v_mfma_f32_16x16x32_bf16 v[36:39], a[204:207], v[92:95], v[36:39]\n"
    "ds_read_b128 v[92:95], v1 offset:14336\n"
    "s_waitcnt lgkmcnt(4)\n"
    "v_mfma_f32_16x16x32_bf16 v[16:19], a[48:51], v[96:99], v[16:19]\n"
    "v_mfma_f32_16x16x32_bf16 v[24:27], a[128:131], v[96:99], v[24:27]\n"
    "v_mfma_f32_16x16x32_bf16 v[32:35], a[208:211], v[96:99], v[32:35]\n"
    "ds_read_b128 v[96:99], v1 offset:34816\n"
    "s_waitcnt lgkmcnt(4)\n"
    "v_mfma_f32_16x16x32_bf16 v[20:23], a[48:51], v[80:83], v[20:23]\n"
    "v_mfma_f32_16x16x32_bf16 v[28:31], a[128:131], v[80:83], v[28:31]\n"
    "v_mfma_f32_16x16x32_bf16 v[36:39], a[208:211], v[80:83], v[36:39]\n"
    "ds_read_b128 v[80:83], v1 offset:15360\n"
    "s_waitcnt lgkmcnt(4)\n"
    "v_mfma_f32_16x16x32_bf16 v[16:19], a[52:55], v[84:87], v[16:19]\n"
    "v_mfma_f32_16x16x32_bf16 v[24:27], a[132:135], v[84:87], v[24:27]\n"
    "v_mfma_f32_16x16x32_bf16 v[32:35], a[212:215], v[84:87], v[32:35]\n"
    "ds_read_b128 v[84:87], v1 offset:35840\n"
    "s_waitcnt lgkmcnt(4)\n"
    "v_mfma_f32_16x16x32_bf16 v[20:23], a[52:55], v[88:91], v[20:23]\n"
    "v_mfma_f32_16x16x32_bf16 v[28:31], a[132:135], v[88:91], v[28:31]\n"
    "v_mfma_f32_16x16x32_bf16 v[36:39], a[212:215], v[88:91], v[36:39]\n"
    "ds_read_b128 v[88:91], v1 offset:16384\n"
    "s_waitcnt lgkmcnt(4)\n"
    "v_mfma_f32_16x16x32_bf16 v[16:19], a[56:59], v[92:95], v[16:19]\n"
    "v_mfma_f32_16x16x32_bf16 v[24:27], a[136:139], v[92:95], v[24:27]\n"
    "v_mfma_f32_16x16x32_bf16 v[32:35], a[216:219], v[92:95], v[32:35]\n"
    "ds_read_b128 v[92:95], v1 offset:36864\n"
    "s_waitcnt lgkmcnt(4)\n"
    "v_mfma_f32_16x16x32_bf16 v[20:23], a[56:59], v[96:99], v[20:23]\n"
    "v_mfma_f32_16x16x32_bf16 v[28:31], a[136:139], v[96:99], v[28:31]\n"
    "v_mfma_f32_16x16x32_bf16 v[36:39], a[216:219], v[96:99], v[36:39]\n"
    "s_waitcnt vmcnt(0) lgkmcnt(4)\n"
    "s_barrier\n"
    "s_sub_u32 s54, s47, 1\n"
    "s_max_i32 s54, s54, 0\n"
    "v_mov_b32 v4, s54\n"
    "buffer_store_dword v4, v3, s[40:43], s58 offen sc1\n"
    "ds_read_b128 v[96:99], v1 offset:17408\n"
    "s_waitcnt lgkmcnt(4)\n"
    "v_mfma_f32_16x16x32_bf16 v[16:19], a[60:63], v[80:83], v[16:19]\n"
    "ds_read_b128 v[188:191], v8 offset:12288\n"
    "s_add_u32 m0, s61, 0\n"
    "s_add_u32 s51, s50, 0\n"
    "buffer_load_dwordx4 v0, s[40:43], s51 offen offset:0 lds\n"
    "v_mfma_f32_16x16x32_bf16 v[24:27], a[140:143], v[80:83], v[24:27]\n"
    "ds_read_b128 v[192:195], v8 offset:14336\n"
    "v_mfma_f32_16x16x32_bf16 v[32:35], a[220:223], v[80:83], v[32:35]\n"
    "ds_read_b128 v[80:83], v1 offset:37888\n"
    "s_waitcnt lgkmcnt(6)\n"
    "v_mfma_f32_16x16x32_bf16 v[20:23], a[60:63], v[84:87], v[20:23]\n"
    "ds_read_b128 v[196:199], v8 offset:16384\n"
    "buffer_load_dwordx4 v0, s[40:43], s51 offen offset:1024 lds\n"
    "v_mfma_f32_16x16x32_bf16 v[28:31], a[140:143], v[84:87], v[28:31]\n"
    "ds_read_b128 v[200:203], v8 offset:18432\n"
    "v_mfma_f32_16x16x32_bf16 v[36:39], a[220:223], v[84:87], v[36:39]\n"
    "ds_read_b128 v[84:87], v1 offset:18432\n"
    "s_waitcnt lgkmcnt(8)\n"
    "v_mfma_f32_16x16x32_bf16 v[16:19], a[64:67], v[88:91], v[16:19]\n"
    "ds_read_b128 v[204:207], v8 offset:20480\n"
    "buffer_load_dwordx4 v0, s[40:43], s51 offen offset:2048 lds\n"
    "v_mfma_f32_16x16x32_bf16 v[24:27], a[144:147], v[88:91], v[24:27]\n"
    "ds_read_b128 v[208:211], v8 offset:22528\n"
    "v_mfma_f32_16x16x32_bf16 v[32:35], a[224:227], v[88:91], v[32:35]\n"
    "ds_read_b128 v[88:91], v1 offset:38912\n"
    "s_waitcnt lgkmcnt(10)\n"
    "v_mfma_f32_16x16x32_bf16 v[20:23], a[64:67], v[92:95], v[20:23]\n"
    "buffer_load_dwordx4 v0, s[40:43], s51 offen offset:3072 lds\n"
    "v_mfma_f32_16x16x32_bf16 v[28:31], a[144:147], v[92:95], v[28:31]\n"
    "v_mfma_f32_16x16x32_bf16 v[36:39], a[224:227], v[92:95], v[36:39]\n"
    "ds_read_b128 v[92:95], v1 offset:19456\n"
    "s_waitcnt lgkmcnt(10)\n"
    "v_mfma_f32_16x16x32_bf16 v[16:19], a[68:71], v[96:99], v[16:19]\n"
    "s_add_u32 m0, s61, 4096\n"
    "s_add_u32 s51, s50, 4096\n"
    "buffer_load_dwordx4 v0, s[40:43], s51 offen offset:0 lds\n"
    "v_mfma_f32_16x16x32_bf16 v[24:27], a[148:151], v[96:99], v[24:27]\n"
    "v_mfma_f32_16x16x32_bf16 v[32:35], a[228:231], v[96:99], v[32:35]\n"
    "ds_read_b128 v[96:99], v1 offset:39936\n"
    "s_waitcnt lgkmcnt(8)\n"
    "v_mfma_f32_16x16x32_bf16 v[20:23], a[68:71], v[80:83], v[20:23]\n"
    "buffer_load_dwordx4 v0, s[40:43], s51 offen offset:1024 lds\n"
    "v_mfma_f32_16x16x32_bf16 v[28:31], a[148:151], v[80:83], v[28:31]\n"
    "v_mfma_f32_16x16x32_bf16 v[36:39], a[228:231], v[80:83], v[36:39]\n"
    "s_add_u32 s64, s64, 40960\n"
    "s_cmp_ge_u32 s64, 122880\n"
    "s_cselect_b32 s64, 0, s64\n"
    "v_add_u32 v1, s64, v0\n"
    "ds_read_b128 v[80:83], v1 offset:0\n"
    "s_waitcnt lgkmcnt(6)\n"
    "v_mfma_f32_16x16x32_bf16 v[16:19], a[72:75], v[84:87], v[16:19]\n"
    "buffer_load_dwordx4 v0, s[40:43], s51 offen offset:2048 lds\n"
    "v_mfma_f32_16x16x32_bf16 v[24:27], a[152:155], v[84:87], v[24:27]\n"
    "v_mfma_f32_16x16x32_bf16 v[32:35], a[232:235], v[84:87], v[32:35]\n"
    "ds_read_b128 v[84:87], v1 offset:20480\n"
    "s_waitcnt lgkmcnt(4)\n"
    "v_mfma_f32_16x16x32_bf16 v[20:23], a[72:75], v[88:91], v[20:23]\n"
    "buffer_load_dwordx4 v0, s[40:43], s51 offen offset:3072 lds\n"
    "v_mfma_f32_16x16x32_bf16 v[28:31], a[152:155], v[88:91], v[28:31]\n"
    "v_mfma_f32_16x16x32_bf16 v[36:39], a[232:235], v[88:91], v[36:39]\n"
    "ds_read_b128 v[88:91], v1 offset:1024\n"
    "s_waitcnt lgkmcnt(4)\n"
    "v_mfma_f32_16x16x32_bf16 v[16:19], a[76:79], v[92:95], v[16:19]\n"
    "s_add_u32 m0, s61, 8192\n"
    "s_add_u32 s51, s50, 8192\n"
    "buffer_load_dwordx4 v0, s[40:43], s51 offen offset:0 lds\n"
    "v_mfma_f32_16x16x32_bf16 v[24:27], a[156:159], v[92:95], v[24:27]\n"
    "v_mfma_f32_16x16x32_bf16 v[32:35], a[236:239], v[92:95], v[32:35]\n"
    "ds_read_b128 v[92:95], v1 offset:21504\n"
    "s_waitcnt lgkmcnt(4)\n"
    "v_mfma_f32_16x16x32_bf16 v[20:23], a[76:79], v[96:99], v[20:23]\n"
    "buffer_load_dwordx4 v0, s[40:43], s51 offen offset:1024 lds\n"
    "s_add_u32 s50, s50, 40960\n"
    "s_add_u32 s61, s61, 40960\n"
    "s_cmp_ge_u32 s61, 122880\n"
    "s_cselect_b32 s51, 122880, 0\n"
    "s_sub_u32 s61, s61, s51\n"
    "v_mfma_f32_16x16x32_bf16 v[28:31], a[156:159], v[96:99], v[28:31]\n"
    "v_mfma_f32_16x16x32_bf16 v[36:39], a[236:239], v[96:99], v[36:39]\n"
    "ds_read_b128 v[96:99], v1 offset:2048\n"
    "s_cmp_gt_u32 s45, 1\n"
    "s_nop 5\n"
    "s_cbranch_scc1 pq_nb0_%=\n"
    "s_cmp_eq_u32 s47, 0\n"
    "s_cbranch_scc1 pq_nb0_%=\n"
    "v_swap_b32 v118, v120\n"
    "v_swap_b32 v119, v121\n"
    "v_swap_b32 v126, v128\n"
    "v_swap_b32 v127, v129\n"
    "v_swap_b32 v134, v136\n"
    "v_swap_b32 v135, v137\n"
    "v_swap_b32 v142, v144\n"
    "v_swap_b32 v143, v145\n"
    "v_swap_b32 v150, v152\n"
    "v_swap_b32 v151, v153\n"
    "v_swap_b32 v158, v160\n"
    "v_swap_b32 v159, v161\n"
    "v_mfma_f32_16x16x32_bf16 v[64:67], v[116:119], v[188:191], v[212:215]\n"
    "v_mfma_f32_16x16x32_bf16 v[68:71], v[120:123], v[188:191], v[216:219]\n"
    "v_mfma_f32_16x16x32_bf16 v[72:75], v[164:167], v[188:191], v[220:223]\n"
    "v_mfma_f32_16x16x32_bf16 v[64:67], v[124:127], v[192:195], v[64:67]\n"
    "v_mfma_f32_16x16x32_bf16 v[68:71], v[128:131], v[192:195], v[68:71]\n"
    "v_mfma_f32_16x16x32_bf16 v[72:75], v[168:171], v[192:195], v[72:75]\n"
    "v_mfma_f32_16x16x32_bf16 v[64:67], v[132:135], v[196:199], v[64:67]\n"
    "v_mfma_f32_16x16x32_bf16 v[68:71], v[136:139], v[196:199], v[68:71]\n"
    "v_mfma_f32_16x16x32_bf16 v[72:75], v[172:175], v[196:199], v[72:75]\n"
    "v_mfma_f32_16x16x32_bf16 v[64:67], v[140:143], v[200:203], v[64:67]\n"
    "v_mfma_f32_16x16x32_bf16 v[68:71], v[144:147], v[200:203], v[68:71]\n"
    "v_mfma_f32_16x16x32_bf16 v[72:75], v[176:179], v[200:203], v[72:75]\n"
    "v_mfma_f32_16x16x32_bf16 v[64:67], v[148:151], v[204:207], v[64:67]\n"
    "v_mfma_f32_16x16x32_bf16 v[68:71], v[152:155], v[204:207], v[68:71]\n"
    "v_mfma_f32_16x16x32_bf16 v[72:75], v[180:183], v[204:207], v[72:75]\n"
    "v_mfma_f32_16x16x32_bf16 v[64:67], v[156:159], v[208:211], v[64:67]\n"
    "v_mfma_f32_16x16x32_bf16 v[68:71], v[160:163], v[208:211], v[68:71]\n"
    "v_mfma_f32_16x16x32_bf16 v[72:75], v[184:187], v[208:211], v[72:75]\n"
    "s_nop 5\n"
    "v_max3_f32 v76, v64, v65, v66\n"
    "v_max3_f32 v76, v76, v67, v68\n"
    "v_max3_f32 v76, v76, v69, v70\n"
    "v_max3_f32 v76, v76, v71, v72\n"
    "v_max3_f32 v76, v76, v73, v74\n"
    "v_max_f32 v76, v76, v75\n"
    "v_mov_b32 v77, v76\n"
    "s_nop 1\n"
    "v_permlane16_swap_b32 v76, v77\n"
    "v_max_f32 v76, v76, v77\n"
    "v_mov_b32 v77, v76\n"
    "s_nop 1\n"
    "v_permlane32_swap_b32 v76, v77\n"
    "v_max_f32 v76, v76, v77\n"
    "v_mul_f32 v76, 0xbdd53b95, v76\n"
    "v_fmamk_f32 v64, v64, 0x3dd53b95, v76\n"
    "v_fmamk_f32 v65, v65, 0x3dd53b95, v76\n"
    "v_fmamk_f32 v66, v66, 0x3dd53b95, v76\n"
    "v_fmamk_f32 v67, v67, 0x3dd53b95, v76\n"
    "v_fmamk_f32 v68, v68, 0x3dd53b95, v76\n"
    "v_fmamk_f32 v69, v69, 0x3dd53b95, v76\n"
    "v_fmamk_f32 v70, v70, 0x3dd53b95, v76\n"
    "v_fmamk_f32 v71, v71, 0x3dd53b95, v76\n"
    "v_fmamk_f32 v72, v72, 0x3dd53b95, v76\n"
    "v_fmamk_f32 v73, v73, 0x3dd53b95, v76\n"
    "v_fmamk_f32 v74, v74, 0x3dd53b95, v76\n"
    "v_fmamk_f32 v75, v75, 0x3dd53b95, v76\n"
    "v_exp_f32 v64, v64\n"
    "v_exp_f32 v65, v65\n"
    "v_exp_f32 v66, v66\n"
    "v_exp_f32 v67, v67\n"
    "v_exp_f32 v68, v68\n"
    "v_exp_f32 v69, v69\n"
    "v_exp_f32 v70, v70\n"
    "v_exp_f32 v71, v71\n"
    "v_exp_f32 v72, v72\n"
    "v_exp_f32 v73, v73\n"
    "v_exp_f32 v74, v74\n"
    "v_exp_f32 v75, v75\n"
    "v_add_f32 v76, v64, v65\n"
    "v_add_f32 v76, v76, v66\n"
    "v_add_f32 v76, v76, v67\n"
    "v_add_f32 v76, v76, v68\n"
    "v_add_f32 v76, v76, v69\n"
    "v_add_f32 v76, v76, v70\n"
    "v_add_f32 v76, v76, v71\n"
    "v_add_f32 v76, v76, v72\n"
    "v_add_f32 v76, v76, v73\n"
    "v_add_f32 v76, v76, v74\n"
    "v_add_f32 v76, v76, v75\n"
    "v_mov_b32 v77, v76\n"
    "s_nop 1\n"
    "v_permlane16_swap_b32 v76, v77\n"
    "v_add_f32 v76, v76, v77\n"
    "v_mov_b32 v77, v76\n"
    "s_nop 1\n"
    "v_permlane32_swap_b32 v76, v77\n"
    "v_add_f32 v76, v76, v77\n"
    "v_rcp_f32 v76, v76\n"
    "s_nop 0\n"
    "v_mul_f32 v64, v64, v76\n"
    "v_mul_f32 v65, v65, v76\n"
    "v_mul_f32 v66, v66, v76\n"
    "v_mul_f32 v67, v67, v76\n"
    "v_mul_f32 v68, v68, v76\n"
    "v_mul_f32 v69, v69, v76\n"
    "v_mul_f32 v70, v70, v76\n"
    "v_mul_f32 v71, v71, v76\n"
    "v_mul_f32 v72, v72, v76\n"
    "v_mul_f32 v73, v73, v76\n"
    "v_mul_f32 v74, v74, v76\n"
    "v_mul_f32 v75, v75, v76\n"
    "v_cvt_pk_bf16_f32 v104, v64, v65\n"
    "v_cvt_pk_bf16_f32 v105, v66, v67\n"
    "v_cvt_pk_bf16_f32 v106, v68, v69\n"
    "v_cvt_pk_bf16_f32 v107, v70, v71\n"
    "v_cvt_pk_bf16_f32 v108, v72, v73\n"
    "v_cvt_pk_bf16_f32 v109, v74, v75\n"
    "v_mov_b32 v110, v108\n"
    "v_mov_b32 v111, v109\n"
    "s_nop 0\n"
    "v_permlane16_swap_b32 v108, v110\n"
    "v_permlane16_swap_b32 v109, v111\n"
    "s_lshl_b32 s54, s53, 1\n"
    "s_add_u32 s54, s54, s45\n"
    "s_lshl_b32 s54, s54, 10\n"
    "s_add_u32 s54, s54, 223936512\n"
    "buffer_store_dwordx4 v[104:107], v0, s[40:43], s54 offen sc1\n"
    "s_sub_u32 s54, s47, 1\n"
    "s_lshl_b32 s54, s54, 1\n"
    "s_add_u32 s54, s54, s45\n"
    "s_lshl_b32 s54, s54, 3\n"
    "s_lshr_b32 s55, s44, 3\n"
    "s_lshl_b32 s55, s55, 1\n"
    "s_add_u32 s54, s54, s55\n"
    "s_and_b32 s55, s44, 1\n"
    "s_add_u32 s54, s54, s55\n"
    "s_lshl_b32 s54, s54, 10\n"
    "s_add_u32 s54, s54, 228130816\n"
    "buffer_store_dwordx4 v[108:111], v12, s[40:43], s54 offen sc1\n"
    "s_nop 1\n"
    "pq_nb0_%=:\n"
    "s_add_u32 s47, s47, 1\n"
    "s_cmp_gt_u32 s45, 1\n"
    "s_cbranch_scc1 pq_na1_%=\n"
    "s_cmp_eq_u32 s47, 0\n"
    "s_cbranch_scc1 pq_na1_%=\n"
    "s_mov_b32 s60, 0\n"
    "v_cmp_gt_u32 vcc, s47, v9\n"
    "s_nop 4\n"
    "s_cbranch_vccz pq_kma1_ok_%=\n"
    "pq_kma1_poll_%=:\n"
    "buffer_load_dword v9, v10, s[40:43], s58 offen sc1\n"
    "s_waitcnt vmcnt(0)\n"
    "v_cmp_gt_u32 vcc, s47, v9\n"
    "s_nop 4\n"
    "s_cbranch_vccz pq_kma1_ok_%=\n"
    "s_sleep 2\n"
    "s_add_u32 s60, s60, 1\n"
    "s_cmp_gt_u32 s60, 65536\n"
    "s_cbranch_scc1 pq_abort_%=\n"
    "s_and_b32 s59, s60, 63\n"
    "s_cbranch_scc1 pq_kma1_poll_%=\n"
    "v_mov_b32 v4, 1024\n"
    "buffer_load_dword v4, v4, s[40:43], s58 offen sc1\n"
    "s_waitcnt vmcnt(0)\n"
    "v_readfirstlane_b32 s59, v4\n"
    "s_nop 0\n"
    "s_cmp_lg_u32 s59, 0\n"
    "s_nop 2\n"
    "s_cbranch_scc1 pq_end_%=\n"
    "s_branch pq_kma1_poll_%=\n"
    "pq_kma1_ok_%=:\n"
    "s_sub_u32 s53, s47, 1\n"
    "s_lshl_b32 s53, s53, 5\n"
    "s_add_u32 s53, s53, s44\n"
    "s_mul_i32 s54, s53, 12288\n"
    "s_add_u32 s54, s54, 62455808\n"
    "s_add_u32 s55, s54, 4096\n"
    "s_add_u32 s56, s54, 8192\n"
    "s_mul_i32 s57, s53, 3072\n"
    "s_add_u32 s57, s57, 217645056\n"
    "buffer_load_dwordx4 v[116:119], v0, s[40:43], s54 offen sc1\n"
    "buffer_load_dwordx4 v[120:123], v0, s[40:43], s54 offen offset:1024 sc1\n"
    "buffer_load_dwordx4 v[124:127], v0, s[40:43], s54 offen offset:3072 sc1\n"
    "buffer_load_dwordx4 v[128:131], v0, s[40:43], s55 offen sc1\n"
    "buffer_load_dwordx4 v[132:135], v0, s[40:43], s55 offen offset:2048 sc1\n"
    "buffer_load_dwordx4 v[136:139], v0, s[40:43], s55 offen offset:3072 sc1\n"
    "buffer_load_dwordx4 v[140:143], v0, s[40:43], s56 offen offset:1024 sc1\n"
    "buffer_load_dwordx4 v[144:147], v0, s[40:43], s56 offen offset:2048 sc1\n"
    "buffer_load_dwordx4 v[148:151], v0, s[40:43], s54 offen offset:2048 sc1\n"
    "buffer_load_dwordx4 v[152:155], v0, s[40:43], s55 offen offset:1024 sc1\n"
    "buffer_load_dwordx4 v[156:159], v0, s[40:43], s56 offen sc1\n"
    "buffer_load_dwordx4 v[160:163], v0, s[40:43], s56 offen offset:3072 sc1\n"
    "buffer_load_dwordx4 v[164:167], v11, s[40:43], s57 offen sc1\n"
    "buffer_load_dwordx4 v[168:171], v11, s[40:43], s57 offen offset:512 sc1\n"
    "buffer_load_dwordx4 v[172:175], v11, s[40:43], s57 offen offset:1024 sc1\n"
    "buffer_load_dwordx4 v[176:179], v11, s[40:43], s57 offen offset:1536 sc1\n"
    "buffer_load_dwordx4 v[180:183], v11, s[40:43], s57 offen offset:2048 sc1\n"
    "buffer_load_dwordx4 v[184:187], v11, s[40:43], s57 offen offset:2560 sc1\n"
    "s_nop 1\n"
    "pq_na1_%=:\n"
    "s_waitcnt lgkmcnt(4)\n"
    "v_mfma_f32_16x16x32_bf16 v[40:43], a[0:3], v[80:83], 0\n"
    "v_mfma_f32_16x16x32_bf16 v[48:51], a[80:83], v[80:83], 0\n"
    "v_mfma_f32_16x16x32_bf16 v[56:59], a[160:163], v[80:83], 0\n"
    "ds_read_b128 v[80:83], v1 offset:22528\n"
    "s_waitcnt lgkmcnt(4)\n"
    "v_mfma_f32_16x16x32_bf16 v[44:47], a[0:3], v[84:87], 0\n"
    "v_mfma_f32_16x16x32_bf16 v[52:55], a[80:83], v[84:87], 0\n"
    "v_mfma_f32_16x16x32_bf16 v[60:63], a[160:163], v[84:87], 0\n"
    "ds_read_b128 v[84:87], v1 offset:3072\n"
    "s_waitcnt lgkmcnt(4)\n"
    "v_mfma_f32_16x16x32_bf16 v[40:43], a[4:7], v[88:91], v[40:43]\n"
    "v_mfma_f32_16x16x32_bf16 v[48:51], a[84:87], v[88:91], v[48:51]\n"
    "v_mfma_f32_16x16x32_bf16 v[56:59], a[164:167], v[88:91], v[56:59]\n"
    "ds_read_b128 v[88:91], v1 offset:23552\n"
    "s_waitcnt lgkmcnt(4)\n"
    "v_mfma_f32_16x16x32_bf16 v[44:47], a[4:7], v[92:95], v[44:47]\n"
    "v_cvt_pk_bf16_f32 v104, v16, v17\n"
    "v_mfma_f32_16x16x32_bf16 v[52:55], a[84:87], v[92:95], v[52:55]\n"
    "v_cvt_pk_bf16_f32 v105, v18, v19\n"
    "v_mfma_f32_16x16x32_bf16 v[60:63], a[164:167], v[92:95], v[60:63]\n"
    "ds_read_b128 v[92:95], v1 offset:4096\n"
    "s_waitcnt lgkmcnt(4)\n"
    "v_mfma_f32_16x16x32_bf16 v[40:43], a[8:11], v[96:99], v[40:43]\n"
    "v_cvt_pk_bf16_f32 v106, v20, v21\n"
    "v_mfma_f32_16x16x32_bf16 v[48:51], a[88:91], v[96:99], v[48:51]\n"
    "v_cvt_pk_bf16_f32 v107, v22, v23\n"
    "v_mfma_f32_16x16x32_bf16 v[56:59], a[168:171], v[96:99], v[56:59]\n"
    "ds_read_b128 v[96:99], v1 offset:24576\n"
    "s_waitcnt lgkmcnt(4)\n"
    "v_mfma_f32_16x16x32_bf16 v[44:47], a[8:11], v[80:83], v[44:47]\n"
    "ds_write2st64_b64 v6, v[104:105], v[106:107] offset0:0 offset1:2\n"
    "v_mfma_f32_16x16x32_bf16 v[52:55], a[88:91], v[80:83], v[52:55]\n"
    "v_cvt_pk_bf16_f32 v108, v24, v25\n"
    "v_mfma_f32_16x16x32_bf16 v[60:63], a[168:171], v[80:83], v[60:63]\n"
    "ds_read_b128 v[80:83], v1 offset:5120\n"
    "s_waitcnt lgkmcnt(5)\n"
    "v_mfma_f32_16x16x32_bf16 v[40:43], a[12:15], v[84:87], v[40:43]\n"
    "v_cvt_pk_bf16_f32 v109, v26, v27\n"
    "v_mfma_f32_16x16x32_bf16 v[48:51], a[92:95], v[84:87], v[48:51]\n"
    "v_cvt_pk_bf16_f32 v110, v28, v29\n"
    "v_mfma_f32_16x16x32_bf16 v[56:59], a[172:175], v[84:87], v[56:59]\n"
    "ds_read_b128 v[84:87], v1 offset:25600\n"
    "s_waitcnt lgkmcnt(5)\n"
    "v_mfma_f32_16x16x32_bf16 v[44:47], a[12:15], v[88:91], v[44:47]\n"
    "v_cvt_pk_bf16_f32 v111, v30, v31\n"
    "v_mfma_f32_16x16x32_bf16 v[52:55], a[92:95], v[88:91], v[52:55]\n"
    "ds_write2st64_b64 v2, v[108:109], v[110:111] offset0:0 offset1:2\n"
    "v_mfma_f32_16x16x32_bf16 v[60:63], a[172:175], v[88:91], v[60:63]\n"
    "ds_read_b128 v[88:91], v1 offset:6144\n"
    "s_waitcnt lgkmcnt(6)\n"
    "v_mfma_f32_16x16x32_bf16 v[40:43], a[16:19], v[92:95], v[40:43]\n"
    "v_cvt_pk_bf16_f32 v112, v32, v33\n"
    "v_mfma_f32_16x16x32_bf16 v[48:51], a[96:99], v[92:95], v[48:51]\n"
    "v_cvt_pk_bf16_f32 v113, v34, v35\n"
    "v_mfma_f32_16x16x32_bf16 v[56:59], a[176:179], v[92:95], v[56:59]\n"
    "ds_read_b128 v[92:95], v1 offset:26624\n"
    "s_waitcnt lgkmcnt(6)\n"
    "v_mfma_f32_16x16x32_bf16 v[44:47], a[16:19], v[96:99], v[44:47]\n"
    "v_cvt_pk_bf16_f32 v114, v36, v37\n"
    "v_mfma_f32_16x16x32_bf16 v[52:55], a[96:99], v[96:99], v[52:55]\n"
    "v_cvt_pk_bf16_f32 v115, v38, v39\n"
    "v_mfma_f32_16x16x32_bf16 v[60:63], a[176:179], v[96:99], v[60:63]\n"
    "ds_read_b128 v[96:99], v1 offset:7168\n"
    "s_waitcnt lgkmcnt(5)\n"
    "v_mfma_f32_16x16x32_bf16 v[40:43], a[20:23], v[80:83], v[40:43]\n"
    "ds_write2st64_b64 v7, v[112:113], v[114:115] offset0:0 offset1:2\n"
    "v_mfma_f32_16x16x32_bf16 v[48:51], a[100:103], v[80:83], v[48:51]\n"
    "v_mfma_f32_16x16x32_bf16 v[56:59], a[180:183], v[80:83], v[56:59]\n"
    "ds_read_b128 v[80:83], v1 offset:27648\n"
    "s_waitcnt lgkmcnt(6)\n"
    "v_mfma_f32_16x16x32_bf16 v[44:47], a[20:23], v[84:87], v[44:47]\n"
    "v_mfma_f32_16x16x32_bf16 v[52:55], a[100:103], v[84:87], v[52:55]\n"
    "v_mfma_f32_16x16x32_bf16 v[60:63], a[180:183], v[84:87], v[60:63]\n"
    "ds_read_b128 v[84:87], v1 offset:8192\n"
    "s_waitcnt lgkmcnt(5)\n"
    "v_mfma_f32_16x16x32_bf16 v[40:43], a[24:27], v[88:91], v[40:43]\n"
    "v_mfma_f32_16x16x32_bf16 v[48:51], a[104:107], v[88:91], v[48:51]\n"
    "v_mfma_f32_16x16x32_bf16 v[56:59], a[184:187], v[88:91], v[56:59]\n"
    "ds_read_b128 v[88:91], v1 offset:28672\n"
    "s_waitcnt lgkmcnt(5)\n"
    "v_mfma_f32_16x16x32_bf16 v[44:47], a[24:27], v[92:95], v[44:47]\n"
    "v_mfma_f32_16x16x32_bf16 v[52:55], a[104:107], v[92:95], v[52:55]\n"
    "v_mfma_f32_16x16x32_bf16 v[60:63], a[184:187], v[92:95], v[60:63]\n"
    "ds_read_b128 v[92:95], v1 offset:9216\n"
    "s_waitcnt lgkmcnt(5)\n"
    "v_mfma_f32_16x16x32_bf16 v[40:43], a[28:31], v[96:99], v[40:43]\n"
    "v_mfma_f32_16x16x32_bf16 v[48:51], a[108:111], v[96:99], v[48:51]\n"
    "v_mfma_f32_16x16x32_bf16 v[56:59], a[188:191], v[96:99], v[56:59]\n"
    "ds_read_b128 v[96:99], v1 offset:29696\n"
    "s_waitcnt lgkmcnt(4)\n"
    "v_mfma_f32_16x16x32_bf16 v[44:47], a[28:31], v[80:83], v[44:47]\n"
    "v_mfma_f32_16x16x32_bf16 v[52:55], a[108:111], v[80:83], v[52:55]\n"
    "v_mfma_f32_16x16x32_bf16 v[60:63], a[188:191], v[80:83], v[60:63]\n"
    "ds_read_b128 v[80:83], v1 offset:10240\n"
    "s_waitcnt lgkmcnt(4)\n"
    "v_mfma_f32_16x16x32_bf16 v[40:43], a[32:35], v[84:87], v[40:43]\n"
    "v_mfma_f32_16x16x32_bf16 v[48:51], a[112:115], v[84:87], v[48:51]\n"
    "v_mfma_f32_16x16x32_bf16 v[56:59], a[192:195], v[84:87], v[56:59]\n"
    "ds_read_b128 v[84:87], v1 offset:30720\n"
    "s_waitcnt lgkmcnt(4)\n"
    "v_mfma_f32_16x16x32_bf16 v[44:47], a[32:35], v[88:91], v[44:47]\n"
    "v_mfma_f32_16x16x32_bf16 v[52:55], a[112:115], v[88:91], v[52:55]\n"
    "v_mfma_f32_16x16x32_bf16 v[60:63], a[192:195], v[88:91], v[60:63]\n"
    "ds_read_b128 v[88:91], v1 offset:11264\n"
    "s_waitcnt lgkmcnt(4)\n"
    "v_mfma_f32_16x16x32_bf16 v[40:43], a[36:39], v[92:95], v[40:43]\n"
    "v_mfma_f32_16x16x32_bf16 v[48:51], a[116:119], v[92:95], v[48:51]\n"
    "v_mfma_f32_16x16x32_bf16 v[56:59], a[196:199], v[92:95], v[56:59]\n"
    "ds_read_b128 v[92:95], v1 offset:31744\n"
    "s_waitcnt lgkmcnt(4)\n"
    "v_mfma_f32_16x16x32_bf16 v[44:47], a[36:39], v[96:99], v[44:47]\n"
    "v_mfma_f32_16x16x32_bf16 v[52:55], a[116:119], v[96:99], v[52:55]\n"
    "v_mfma_f32_16x16x32_bf16 v[60:63], a[196:199], v[96:99], v[60:63]\n"
    "ds_read_b128 v[96:99], v1 offset:12288\n"
    "s_waitcnt lgkmcnt(4)\n"
    "v_mfma_f32_16x16x32_bf16 v[40:43], a[40:43], v[80:83], v[40:43]\n"
    "v_mfma_f32_16x16x32_bf16 v[48:51], a[120:123], v[80:83], v[48:51]\n"
    "v_mfma_f32_16x16x32_bf16 v[56:59], a[200:203], v[80:83], v[56:59]\n"
    "ds_read_b128 v[80:83], v1 offset:32768\n"
    "s_waitcnt lgkmcnt(4)\n"
    "v_mfma_f32_16x16x32_bf16 v[44:47], a[40:43], v[84:87], v[44:47]\n"
    "v_mfma_f32_16x16x32_bf16 v[52:55], a[120:123], v[84:87], v[52:55]\n"
    "v_mfma_f32_16x16x32_bf16 v[60:63], a[200:203], v[84:87], v[60:63]\n"
    "ds_read_b128 v[84:87], v1 offset:13312\n"
    "s_waitcnt lgkmcnt(4)\n"
    "v_mfma_f32_16x16x32_bf16 v[40:43], a[44:47], v[88:91], v[40:43]\n"
    "v_mfma_f32_16x16x32_bf16 v[48:51], a[124:127], v[88:91], v[48:51]\n"
    "v_mfma_f32_16x16x32_bf16 v[56:59], a[204:207], v[88:91], v[56:59]\n"
    "ds_read_b128 v[88:91], v1 offset:33792\n"
    "s_waitcnt lgkmcnt(4)\n"
    "v_mfma_f32_16x16x32_bf16 v[44:47], a[44:47], v[92:95], v[44:47]\n"
    "v_mfma_f32_16x16x32_bf16 v[52:55], a[124:127], v[92:95], v[52:55]\n"
    "v_mfma_f32_16x16x32_bf16 v[60:63], a[204:207], v[92:95], v[60:63]\n"
    "ds_read_b128 v[92:95], v1 offset:14336\n"
    "s_waitcnt lgkmcnt(4)\n"
    "v_mfma_f32_16x16x32_bf16 v[40:43], a[48:51], v[96:99], v[40:43]\n"
    "v_mfma_f32_16x16x32_bf16 v[48:51], a[128:131], v[96:99], v[48:51]\n"
    "v_mfma_f32_16x16x32_bf16 v[56:59], a[208:211], v[96:99], v[56:59]\n"
    "ds_read_b128 v[96:99], v1 offset:34816\n"
    "s_waitcnt lgkmcnt(4)\n"
    "v_mfma_f32_16x16x32_bf16 v[44:47], a[48:51], v[80:83], v[44:47]\n"
    "v_mfma_f32_16x16x32_bf16 v[52:55], a[128:131], v[80:83], v[52:55]\n"
    "v_mfma_f32_16x16x32_bf16 v[60:63], a[208:211], v[80:83], v[60:63]\n"
    "ds_read_b128 v[80:83], v1 offset:15360\n"
    "s_waitcnt lgkmcnt(4)\n"
    "v_mfma_f32_16x16x32_bf16 v[40:43], a[52:55], v[84:87], v[40:43]\n"
    "v_mfma_f32_16x16x32_bf16 v[48:51], a[132:135], v[84:87], v[48:51]\n"
    "v_mfma_f32_16x16x32_bf16 v[56:59], a[212:215], v[84:87], v[56:59]\n"
    "ds_read_b128 v[84:87], v1 offset:35840\n"
    "s_waitcnt lgkmcnt(4)\n"
    "v_mfma_f32_16x16x32_bf16 v[44:47], a[52:55], v[88:91], v[44:47]\n"
    "v_mfma_f32_16x16x32_bf16 v[52:55], a[132:135], v[88:91], v[52:55]\n"
    "v_mfma_f32_16x16x32_bf16 v[60:63], a[212:215], v[88:91], v[60:63]\n"
    "ds_read_b128 v[88:91], v1 offset:16384\n"
    "s_waitcnt lgkmcnt(4)\n"
    "v_mfma_f32_16x16x32_bf16 v[40:43], a[56:59], v[92:95], v[40:43]\n"
    "v_mfma_f32_16x16x32_bf16 v[48:51], a[136:139], v[92:95], v[48:51]\n"
    "v_mfma_f32_16x16x32_bf16 v[56:59], a[216:219], v[92:95], v[56:59]\n"
    "ds_read_b128 v[92:95], v1 offset:36864\n"
    "s_waitcnt lgkmcnt(4)\n"
    "v_mfma_f32_16x16x32_bf16 v[44:47], a[56:59], v[96:99], v[44:47]\n"
    "v_mfma_f32_16x16x32_bf16 v[52:55], a[136:139], v[96:99], v[52:55]\n"
    "v_mfma_f32_16x16x32_bf16 v[60:63], a[216:219], v[96:99], v[60:63]\n"
    "s_waitcnt vmcnt(0) lgkmcnt(4)\n"
    "s_barrier\n"
    "s_sub_u32 s54, s47, 1\n"
    "s_max_i32 s54, s54, 0\n"
    "v_mov_b32 v4, s54\n"
    "buffer_store_dword v4, v3, s[40:43], s58 offen sc1\n"
    "ds_read_b128 v[96:99], v1 offset:17408\n"
    "s_waitcnt lgkmcnt(4)\n"
    "v_mfma_f32_16x16x32_bf16 v[40:43], a[60:63], v[80:83], v[40:43]\n"
    "ds_read_b128 v[188:191], v8 offset:0\n"
    "s_add_u32 m0, s61, 0\n"
    "s_add_u32 s51, s50, 0\n"
    "buffer_load_dwordx4 v0, s[40:43], s51 offen offset:0 lds\n"
    "v_mfma_f32_16x16x32_bf16 v[48:51], a[140:143], v[80:83], v[48:51]\n"
    "ds_read_b128 v[192:195], v8 offset:2048\n"
    "v_mfma_f32_16x16x32_bf16 v[56:59], a[220:223], v[80:83], v[56:59]\n"
    "ds_read_b128 v[80:83], v1 offset:37888\n"
    "s_waitcnt lgkmcnt(6)\n"
    "v_mfma_f32_16x16x32_bf16 v[44:47], a[60:63], v[84:87], v[44:47]\n"
    "ds_read_b128 v[196:199], v8 offset:4096\n"
    "buffer_load_dwordx4 v0, s[40:43], s51 offen offset:1024 lds\n"
    "v_mfma_f32_16x16x32_bf16 v[52:55], a[140:143], v[84:87], v[52:55]\n"
    "ds_read_b128 v[200:203], v8 offset:6144\n"
    "v_mfma_f32_16x16x32_bf16 v[60:63], a[220:223], v[84:87], v[60:63]\n"
    "ds_read_b128 v[84:87], v1 offset:18432\n"
    "s_waitcnt lgkmcnt(8)\n"
    "v_mfma_f32_16x16x32_bf16 v[40:43], a[64:67], v[88:91], v[40:43]\n"
    "ds_read_b128 v[204:207], v8 offset:8192\n"
    "buffer_load_dwordx4 v0, s[40:43], s51 offen offset:2048 lds\n"
    "v_mfma_f32_16x16x32_bf16 v[48:51], a[144:147], v[88:91], v[48:51]\n"
    "ds_read_b128 v[208:211], v8 offset:10240\n"
    "v_mfma_f32_16x16x32_bf16 v[56:59], a[224:227], v[88:91], v[56:59]\n"
    "ds_read_b128 v[88:91], v1 offset:38912\n"
    "s_waitcnt lgkmcnt(10)\n"
    "v_mfma_f32_16x16x32_bf16 v[44:47], a[64:67], v[92:95], v[44:47]\n"
    "buffer_load_dwordx4 v0, s[40:43], s51 offen offset:3072 lds\n"
    "v_mfma_f32_16x16x32_bf16 v[52:55], a[144:147], v[92:95], v[52:55]\n"
    "v_mfma_f32_16x16x32_bf16 v[60:63], a[224:227], v[92:95], v[60:63]\n"
    "ds_read_b128 v[92:95], v1 offset:19456\n"
    "s_waitcnt lgkmcnt(10)\n"
    "v_mfma_f32_16x16x32_bf16 v[40:43], a[68:71], v[96:99], v[40:43]\n"
    "s_add_u32 m0, s61, 4096\n"
    "s_add_u32 s51, s50, 4096\n"
    "buffer_load_dwordx4 v0, s[40:43], s51 offen offset:0 lds\n"
    "v_mfma_f32_16x16x32_bf16 v[48:51], a[148:151], v[96:99], v[48:51]\n"
    "v_mfma_f32_16x16x32_bf16 v[56:59], a[228:231], v[96:99], v[56:59]\n"
    "ds_read_b128 v[96:99], v1 offset:39936\n"
    "s_waitcnt lgkmcnt(8)\n"
    "v_mfma_f32_16x16x32_bf16 v[44:47], a[68:71], v[80:83], v[44:47]\n"
    "buffer_load_dwordx4 v0, s[40:43], s51 offen offset:1024 lds\n"
    "v_mfma_f32_16x16x32_bf16 v[52:55], a[148:151], v[80:83], v[52:55]\n"
    "v_mfma_f32_16x16x32_bf16 v[60:63], a[228:231], v[80:83], v[60:63]\n"
    "s_add_u32 s64, s64, 40960\n"
    "s_cmp_ge_u32 s64, 122880\n"
    "s_cselect_b32 s64, 0, s64\n"
    "v_add_u32 v1, s64, v0\n"
    "ds_read_b128 v[80:83], v1 offset:0\n"
    "s_waitcnt lgkmcnt(6)\n"
    "v_mfma_f32_16x16x32_bf16 v[40:43], a[72:75], v[84:87], v[40:43]\n"
    "buffer_load_dwordx4 v0, s[40:43], s51 offen offset:2048 lds\n"
    "v_mfma_f32_16x16x32_bf16 v[48:51], a[152:155], v[84:87], v[48:51]\n"
    "v_mfma_f32_16x16x32_bf16 v[56:59], a[232:235], v[84:87], v[56:59]\n"
    "ds_read_b128 v[84:87], v1 offset:20480\n"
    "s_waitcnt lgkmcnt(4)\n"
    "v_mfma_f32_16x16x32_bf16 v[44:47], a[72:75], v[88:91], v[44:47]\n"
    "buffer_load_dwordx4 v0, s[40:43], s51 offen offset:3072 lds\n"
    "v_mfma_f32_16x16x32_bf16 v[52:55], a[152:155], v[88:91], v[52:55]\n"
    "v_mfma_f32_16x16x32_bf16 v[60:63], a[232:235], v[88:91], v[60:63]\n"
    "ds_read_b128 v[88:91], v1 offset:1024\n"
    "s_waitcnt lgkmcnt(4)\n"
    "v_mfma_f32_16x16x32_bf16 v[40:43], a[76:79], v[92:95], v[40:43]\n"
    "s_add_u32 m0, s61, 8192\n"
    "s_add_u32 s51, s50, 8192\n"
    "buffer_load_dwordx4 v0, s[40:43], s51 offen offset:0 lds\n"
    "v_mfma_f32_16x16x32_bf16 v[48:51], a[156:159], v[92:95], v[48:51]\n"
    "v_mfma_f32_16x16x32_bf16 v[56:59], a[236:239], v[92:95], v[56:59]\n"
    "ds_read_b128 v[92:95], v1 offset:21504\n"
    "s_waitcnt lgkmcnt(4)\n"
    "v_mfma_f32_16x16x32_bf16 v[44:47], a[76:79], v[96:99], v[44:47]\n"
    "buffer_load_dwordx4 v0, s[40:43], s51 offen offset:1024 lds\n"
    "s_add_u32 s50, s50, 40960\n"
    "s_add_u32 s61, s61, 40960\n"
    "s_cmp_ge_u32 s61, 122880\n"
    "s_cselect_b32 s51, 122880, 0\n"
    "s_sub_u32 s61, s61, s51\n"
    "v_mfma_f32_16x16x32_bf16 v[52:55], a[156:159], v[96:99], v[52:55]\n"
    "v_mfma_f32_16x16x32_bf16 v[60:63], a[236:239], v[96:99], v[60:63]\n"
    "ds_read_b128 v[96:99], v1 offset:2048\n"
    "s_cmp_gt_u32 s45, 1\n"
    "s_nop 5\n"
    "s_cbranch_scc1 pq_nb1_%=\n"
    "s_cmp_eq_u32 s47, 0\n"
    "s_cbranch_scc1 pq_nb1_%=\n"
    "v_swap_b32 v118, v120\n"
    "v_swap_b32 v119, v121\n"
    "v_swap_b32 v126, v128\n"
    "v_swap_b32 v127, v129\n"
    "v_swap_b32 v134, v136\n"
    "v_swap_b32 v135, v137\n"
    "v_swap_b32 v142, v144\n"
    "v_swap_b32 v143, v145\n"
    "v_swap_b32 v150, v152\n"
    "v_swap_b32 v151, v153\n"
    "v_swap_b32 v158, v160\n"
    "v_swap_b32 v159, v161\n"
    "v_mfma_f32_16x16x32_bf16 v[64:67], v[116:119], v[188:191], v[212:215]\n"
    "v_mfma_f32_16x16x32_bf16 v[68:71], v[120:123], v[188:191], v[216:219]\n"
    "v_mfma_f32_16x16x32_bf16 v[72:75], v[164:167], v[188:191], v[220:223]\n"
    "v_mfma_f32_16x16x32_bf16 v[64:67], v[124:127], v[192:195], v[64:67]\n"
    "v_mfma_f32_16x16x32_bf16 v[68:71], v[128:131], v[192:195], v[68:71]\n"
    "v_mfma_f32_16x16x32_bf16 v[72:75], v[168:171], v[192:195], v[72:75]\n"
    "v_mfma_f32_16x16x32_bf16 v[64:67], v[132:135], v[196:199], v[64:67]\n"
    "v_mfma_f32_16x16x32_bf16 v[68:71], v[136:139], v[196:199], v[68:71]\n"
    "v_mfma_f32_16x16x32_bf16 v[72:75], v[172:175], v[196:199], v[72:75]\n"
    "v_mfma_f32_16x16x32_bf16 v[64:67], v[140:143], v[200:203], v[64:67]\n"
    "v_mfma_f32_16x16x32_bf16 v[68:71], v[144:147], v[200:203], v[68:71]\n"
    "v_mfma_f32_16x16x32_bf16 v[72:75], v[176:179], v[200:203], v[72:75]\n"
    "v_mfma_f32_16x16x32_bf16 v[64:67], v[148:151], v[204:207], v[64:67]\n"
    "v_mfma_f32_16x16x32_bf16 v[68:71], v[152:155], v[204:207], v[68:71]\n"
    "v_mfma_f32_16x16x32_bf16 v[72:75], v[180:183], v[204:207], v[72:75]\n"
    "v_mfma_f32_16x16x32_bf16 v[64:67], v[156:159], v[208:211], v[64:67]\n"
    "v_mfma_f32_16x16x32_bf16 v[68:71], v[160:163], v[208:211], v[68:71]\n"
    "v_mfma_f32_16x16x32_bf16 v[72:75], v[184:187], v[208:211], v[72:75]\n"
    "s_nop 5\n"
    "v_max3_f32 v76, v64, v65, v66\n"
    "v_max3_f32 v76, v76, v67, v68\n"
    "v_max3_f32 v76, v76, v69, v70\n"
    "v_max3_f32 v76, v76, v71, v72\n"
    "v_max3_f32 v76, v76, v73, v74\n"
    "v_max_f32 v76, v76, v75\n"
    "v_mov_b32 v77, v76\n"
    "s_nop 1\n"
    "v_permlane16_swap_b32 v76, v77\n"
    "v_max_f32 v76, v76, v77\n"
    "v_mov_b32 v77, v76\n"
    "s_nop 1\n"
    "v_permlane32_swap_b32 v76, v77\n"
    "v_max_f32 v76, v76, v77\n"
    "v_mul_f32 v76, 0xbdd53b95, v76\n"
    "v_fmamk_f32 v64, v64, 0x3dd53b95, v76\n"
    "v_fmamk_f32 v65, v65, 0x3dd53b95, v76\n"
    "v_fmamk_f32 v66, v66, 0x3dd53b95, v76\n"
    "v_fmamk_f32 v67, v67, 0x3dd53b95, v76\n"
    "v_fmamk_f32 v68, v68, 0x3dd53b95, v76\n"
    "v_fmamk_f32 v69, v69, 0x3dd53b95, v76\n"
    "v_fmamk_f32 v70, v70, 0x3dd53b95, v76\n"
    "v_fmamk_f32 v71, v71, 0x3dd53b95, v76\n"
    "v_fmamk_f32 v72, v72, 0x3dd53b95, v76\n"
    "v_fmamk_f32 v73, v73, 0x3dd53b95, v76\n"
    "v_fmamk_f32 v74, v74, 0x3dd53b95, v76\n"
    "v_fmamk_f32 v75, v75, 0x3dd53b95, v76\n"
    "v_exp_f32 v64, v64\n"
    "v_exp_f32 v65, v65\n"
    "v_exp_f32 v66, v66\n"
    "v_exp_f32 v67, v67\n"
    "v_exp_f32 v68, v68\n"
    "v_exp_f32 v69, v69\n"
    "v_exp_f32 v70, v70\n"
    "v_exp_f32 v71, v71\n"
    "v_exp_f32 v72, v72\n"
    "v_exp_f32 v73, v73\n"
    "v_exp_f32 v74, v74\n"
    "v_exp_f32 v75, v75\n"
    "v_add_f32 v76, v64, v65\n"
    "v_add_f32 v76, v76, v66\n"
    "v_add_f32 v76, v76, v67\n"
    "v_add_f32 v76, v76, v68\n"
    "v_add_f32 v76, v76, v69\n"
    "v_add_f32 v76, v76, v70\n"
    "v_add_f32 v76, v76, v71\n"
    "v_add_f32 v76, v76, v72\n"
    "v_add_f32 v76, v76, v73\n"
    "v_add_f32 v76, v76, v74\n"
    "v_add_f32 v76, v76, v75\n"
    "v_mov_b32 v77, v76\n"
    "s_nop 1\n"
    "v_permlane16_swap_b32 v76, v77\n"
    "v_add_f32 v76, v76, v77\n"
    "v_mov_b32 v77, v76\n"
    "s_nop 1\n"
    "v_permlane32_swap_b32 v76, v77\n"
    "v_add_f32 v76, v76, v77\n"
    "v_rcp_f32 v76, v76\n"
    "s_nop 0\n"
    "v_mul_f32 v64, v64, v76\n"
    "v_mul_f32 v65, v65, v76\n"
    "v_mul_f32 v66, v66, v76\n"
    "v_mul_f32 v67, v67, v76\n"
    "v_mul_f32 v68, v68, v76\n"
    "v_mul_f32 v69, v69, v76\n"
    "v_mul_f32 v70, v70, v76\n"
    "v_mul_f32 v71, v71, v76\n"
    "v_mul_f32 v72, v72, v76\n"
    "v_mul_f32 v73, v73, v76\n"
    "v_mul_f32 v74, v74, v76\n"
    "v_mul_f32 v75, v75, v76\n"
    "v_cvt_pk_bf16_f32 v104, v64, v65\n"
    "v_cvt_pk_bf16_f32 v105, v66, v67\n"
    "v_cvt_pk_bf16_f32 v106, v68, v69\n"
    "v_cvt_pk_bf16_f32 v107, v70, v71\n"
    "v_cvt_pk_bf16_f32 v108, v72, v73\n"
    "v_cvt_pk_bf16_f32 v109, v74, v75\n"
    "v_mov_b32 v110, v108\n"
    "v_mov_b32 v111, v109\n"
    "s_nop 0\n"
    "v_permlane16_swap_b32 v108, v110\n"
    "v_permlane16_swap_b32 v109, v111\n"
    "s_lshl_b32 s54, s53, 1\n"
    "s_add_u32 s54, s54, s45\n"
    "s_lshl_b32 s54, s54, 10\n"
    "s_add_u32 s54, s54, 223936512\n"
    "buffer_store_dwordx4 v[104:107], v0, s[40:43], s54 offen sc1\n"
    "s_sub_u32 s54, s47, 1\n"
    "s_lshl_b32 s54, s54, 1\n"
    "s_add_u32 s54, s54, s45\n"
    "s_lshl_b32 s54, s54, 3\n"
    "s_lshr_b32 s55, s44, 3\n"
    "s_lshl_b32 s55, s55, 1\n"
    "s_add_u32 s54, s54, s55\n"
    "s_and_b32 s55, s44, 1\n"
    "s_add_u32 s54, s54, s55\n"
    "s_lshl_b32 s54, s54, 10\n"
    "s_add_u32 s54, s54, 228130816\n"
    "buffer_store_dwordx4 v[108:111], v12, s[40:43], s54 offen sc1\n"
    "s_nop 1\n"
    "pq_nb1_%=:\n"
    "s_add_u32 s47, s47, 1\n"
    "s_cmp_lt_u32 s47, 64\n"
    "s_cbranch_scc1 pq_loop_%=\n"
    "s_waitcnt lgkmcnt(0)\n"
    "v_cvt_pk_bf16_f32 v104, v40, v41\n"
    "v_cvt_pk_bf16_f32 v105, v42, v43\n"
    "v_cvt_pk_bf16_f32 v106, v44, v45\n"
    "v_cvt_pk_bf16_f32 v107, v46, v47\n"
    "ds_write2st64_b64 v6, v[104:105], v[106:107] offset0:24 offset1:26\n"
    "v_cvt_pk_bf16_f32 v108, v48, v49\n"
    "v_cvt_pk_bf16_f32 v109, v50, v51\n"
    "v_cvt_pk_bf16_f32 v110, v52, v53\n"
    "v_cvt_pk_bf16_f32 v111, v54, v55\n"
    "ds_write2st64_b64 v2, v[108:109], v[110:111] offset0:24 offset1:26\n"
    "v_cvt_pk_bf16_f32 v112, v56, v57\n"
    "v_cvt_pk_bf16_f32 v113, v58, v59\n"
    "v_cvt_pk_bf16_f32 v114, v60, v61\n"
    "v_cvt_pk_bf16_f32 v115, v62, v63\n"
    "ds_write2st64_b64 v7, v[112:113], v[114:115] offset0:24 offset1:26\n"
    "s_waitcnt lgkmcnt(0)\n"
    "s_barrier\n"
    "s_cmp_gt_u32 s45, 1\n"
    "s_cbranch_scc1 pq_nc_%=\n"
    "s_mov_b32 s60, 0\n"
    "v_cmp_gt_u32 vcc, s47, v9\n"
    "s_nop 4\n"
    "s_cbranch_vccz pq_kme_ok_%=\n"
    "pq_kme_poll_%=:\n"
    "buffer_load_dword v9, v10, s[40:43], s58 offen sc1\n"
    "s_waitcnt vmcnt(0)\n"
    "v_cmp_gt_u32 vcc, s47, v9\n"
    "s_nop 4\n"
    "s_cbranch_vccz pq_kme_ok_%=\n"
    "s_sleep 2\n"
    "s_add_u32 s60, s60, 1\n"
    "s_cmp_gt_u32 s60, 65536\n"
    "s_cbranch_scc1 pq_abort_%=\n"
    "s_and_b32 s59, s60, 63\n"
    "s_cbranch_scc1 pq_kme_poll_%=\n"
    "v_mov_b32 v4, 1024\n"
    "buffer_load_dword v4, v4, s[40:43], s58 offen sc1\n"
    "s_waitcnt vmcnt(0)\n"
    "v_readfirstlane_b32 s59, v4\n"
    "s_nop 0\n"
    "s_cmp_lg_u32 s59, 0\n"
    "s_nop 2\n"
    "s_cbranch_scc1 pq_end_%=\n"
    "s_branch pq_kme_poll_%=\n"
    "pq_kme_ok_%=:\n"
    "s_sub_u32 s53, s47, 1\n"
    "s_lshl_b32 s53, s53, 5\n"
    "s_add_u32 s53, s53, s44\n"
    "s_mul_i32 s54, s53, 12288\n"
    "s_add_u32 s54, s54, 62455808\n"
    "s_add_u32 s55, s54, 4096\n"
    "s_add_u32 s56, s54, 8192\n"
    "s_mul_i32 s57, s53, 3072\n"
    "s_add_u32 s57, s57, 217645056\n"
    "buffer_load_dwordx4 v[116:119], v0, s[40:43], s54 offen sc1\n"
    "buffer_load_dwordx4 v[120:123], v0, s[40:43], s54 offen offset:1024 sc1\n"
    "buffer_load_dwordx4 v[124:127], v0, s[40:43], s54 offen offset:3072 sc1\n"
    "buffer_load_dwordx4 v[128:131], v0, s[40:43], s55 offen sc1\n"
    "buffer_load_dwordx4 v[132:135], v0, s[40:43], s55 offen offset:2048 sc1\n"
    "buffer_load_dwordx4 v[136:139], v0, s[40:43], s55 offen offset:3072 sc1\n"
    "buffer_load_dwordx4 v[140:143], v0, s[40:43], s56 offen offset:1024 sc1\n"
    "buffer_load_dwordx4 v[144:147], v0, s[40:43], s56 offen offset:2048 sc1\n"
    "buffer_load_dwordx4 v[148:151], v0, s[40:43], s54 offen offset:2048 sc1\n"
    "buffer_load_dwordx4 v[152:155], v0, s[40:43], s55 offen offset:1024 sc1\n"
    "buffer_load_dwordx4 v[156:159], v0, s[40:43], s56 offen sc1\n"
    "buffer_load_dwordx4 v[160:163], v0, s[40:43], s56 offen offset:3072 sc1\n"
    "buffer_load_dwordx4 v[164:167], v11, s[40:43], s57 offen sc1\n"
    "buffer_load_dwordx4 v[168:171], v11, s[40:43], s57 offen offset:512 sc1\n"
    "buffer_load_dwordx4 v[172:175], v11, s[40:43], s57 offen offset:1024 sc1\n"
    "buffer_load_dwordx4 v[176:179], v11, s[40:43], s57 offen offset:1536 sc1\n"
    "buffer_load_dwordx4 v[180:183], v11, s[40:43], s57 offen offset:2048 sc1\n"
    "buffer_load_dwordx4 v[184:187], v11, s[40:43], s57 offen offset:2560 sc1\n"
    "ds_read_b128 v[188:191], v8 offset:12288\n"
    "ds_read_b128 v[192:195], v8 offset:14336\n"
    "ds_read_b128 v[196:199], v8 offset:16384\n"
    "ds_read_b128 v[200:203], v8 offset:18432\n"
    "ds_read_b128 v[204:207], v8 offset:20480\n"
    "ds_read_b128 v[208:211], v8 offset:22528\n"
    "s_waitcnt vmcnt(16)\n"
    "v_swap_b32 v118, v120\n"
    "v_swap_b32 v119, v121\n"
    "s_waitcnt vmcnt(14)\n"
    "v_swap_b32 v126, v128\n"
    "v_swap_b32 v127, v129\n"
    "s_waitcnt vmcnt(12)\n"
    "v_swap_b32 v134, v136\n"
    "v_swap_b32 v135, v137\n"
    "s_waitcnt vmcnt(10)\n"
    "v_swap_b32 v142, v144\n"
    "v_swap_b32 v143, v145\n"
    "s_waitcnt vmcnt(8)\n"
    "v_swap_b32 v150, v152\n"
    "v_swap_b32 v151, v153\n"
    "s_waitcnt vmcnt(6)\n"
    "v_swap_b32 v158, v160\n"
    "v_swap_b32 v159, v161\n"
    "s_waitcnt lgkmcnt(5)\n"
    "v_mfma_f32_16x16x32_bf16 v[64:67], v[116:119], v[188:191], v[212:215]\n"
    "v_mfma_f32_16x16x32_bf16 v[68:71], v[120:123], v[188:191], v[216:219]\n"
    "s_waitcnt vmcnt(5)\n"
    "v_mfma_f32_16x16x32_bf16 v[72:75], v[164:167], v[188:191], v[220:223]\n"
    "s_waitcnt lgkmcnt(4)\n"
    "v_mfma_f32_16x16x32_bf16 v[64:67], v[124:127], v[192:195], v[64:67]\n"
    "v_mfma_f32_16x16x32_bf16 v[68:71], v[128:131], v[192:195], v[68:71]\n"
    "s_waitcnt vmcnt(4)\n"
    "v_mfma_f32_16x16x32_bf16 v[72:75], v[168:171], v[192:195], v[72:75]\n"
    "s_waitcnt lgkmcnt(3)\n"
    "v_mfma_f32_16x16x32_bf16 v[64:67], v[132:135], v[196:199], v[64:67]\n"
    "v_mfma_f32_16x16x32_bf16 v[68:71], v[136:139], v[196:199], v[68:71]\n"
    "s_waitcnt vmcnt(3)\n"
    "v_mfma_f32_16x16x32_bf16 v[72:75], v[172:175], v[196:199], v[72:75]\n"
    "s_waitcnt lgkmcnt(2)\n"
    "v_mfma_f32_16x16x32_bf16 v[64:67], v[140:143], v[200:203], v[64:67]\n"
    "v_mfma_f32_16x16x32_bf16 v[68:71], v[144:147], v[200:203], v[68:71]\n"
    "s_waitcnt vmcnt(2)\n"
    "v_mfma_f32_16x16x32_bf16 v[72:75], v[176:179], v[200:203], v[72:75]\n"
    "s_waitcnt lgkmcnt(1)\n"
    "v_mfma_f32_16x16x32_bf16 v[64:67], v[148:151], v[204:207], v[64:67]\n"
    "v_mfma_f32_16x16x32_bf16 v[68:71], v[152:155], v[204:207], v[68:71]\n"
    "s_waitcnt vmcnt(1)\n"
    "v_mfma_f32_16x16x32_bf16 v[72:75], v[180:183], v[204:207], v[72:75]\n"
    "s_waitcnt lgkmcnt(0)\n"
    "v_mfma_f32_16x16x32_bf16 v[64:67], v[156:159], v[208:211], v[64:67]\n"
    "v_mfma_f32_16x16x32_bf16 v[68:71], v[160:163], v[208:211], v[68:71]\n"
    "s_waitcnt vmcnt(0)\n"
    "v_mfma_f32_16x16x32_bf16 v[72:75], v[184:187], v[208:211], v[72:75]\n"
    "s_nop 4\n"
    "v_max3_f32 v76, v64, v65, v66\n"
    "v_max3_f32 v76, v76, v67, v68\n"
    "v_max3_f32 v76, v76, v69, v70\n"
    "v_max3_f32 v76, v76, v71, v72\n"
    "v_max3_f32 v76, v76, v73, v74\n"
    "v_max_f32 v76, v76, v75\n"
    "v_mov_b32 v77, v76\n"
    "s_nop 1\n"
    "v_permlane16_swap_b32 v76, v77\n"
    "v_max_f32 v76, v76, v77\n"
    "v_mov_b32 v77, v76\n"
    "s_nop 1\n"
    "v_permlane32_swap_b32 v76, v77\n"
    "v_max_f32 v76, v76, v77\n"
    "v_mul_f32 v76, 0xbdd53b95, v76\n"
    "v_fmamk_f32 v64, v64, 0x3dd53b95, v76\n"
    "v_fmamk_f32 v65, v65, 0x3dd53b95, v76\n"
    "v_fmamk_f32 v66, v66, 0x3dd53b95, v76\n"
    "v_fmamk_f32 v67, v67, 0x3dd53b95, v76\n"
    "v_fmamk_f32 v68, v68, 0x3dd53b95, v76\n"
    "v_fmamk_f32 v69, v69, 0x3dd53b95, v76\n"
    "v_fmamk_f32 v70, v70, 0x3dd53b95, v76\n"
    "v_fmamk_f32 v71, v71, 0x3dd53b95, v76\n"
    "v_fmamk_f32 v72, v72, 0x3dd53b95, v76\n"
    "v_fmamk_f32 v73, v73, 0x3dd53b95, v76\n"
    "v_fmamk_f32 v74, v74, 0x3dd53b95, v76\n"
    "v_fmamk_f32 v75, v75, 0x3dd53b95, v76\n"
    "v_exp_f32 v64, v64\n"
    "v_exp_f32 v65, v65\n"
    "v_exp_f32 v66, v66\n"
    "v_exp_f32 v67, v67\n"
    "v_exp_f32 v68, v68\n"
    "v_exp_f32 v69, v69\n"
    "v_exp_f32 v70, v70\n"
    "v_exp_f32 v71, v71\n"
    "v_exp_f32 v72, v72\n"
    "v_exp_f32 v73, v73\n"
    "v_exp_f32 v74, v74\n"
    "v_exp_f32 v75, v75\n"
    "v_add_f32 v76, v64, v65\n"
    "v_add_f32 v76, v76, v66\n"
    "v_add_f32 v76, v76, v67\n"
    "v_add_f32 v76, v76, v68\n"
    "v_add_f32 v76, v76, v69\n"
    "v_add_f32 v76, v76, v70\n"
    "v_add_f32 v76, v76, v71\n"
    "v_add_f32 v76, v76, v72\n"
    "v_add_f32 v76, v76, v73\n"
    "v_add_f32 v76, v76, v74\n"
    "v_add_f32 v76, v76, v75\n"
    "v_mov_b32 v77, v76\n"
    "s_nop 1\n"
    "v_permlane16_swap_b32 v76, v77\n"
    "v_add_f32 v76, v76, v77\n"
    "v_mov_b32 v77, v76\n"
    "s_nop 1\n"
    "v_permlane32_swap_b32 v76, v77\n"
    "v_add_f32 v76, v76, v77\n"
    "v_rcp_f32 v76, v76\n"
    "s_nop 0\n"
    "v_mul_f32 v64, v64, v76\n"
    "v_mul_f32 v65, v65, v76\n"
    "v_mul_f32 v66, v66, v76\n"
    "v_mul_f32 v67, v67, v76\n"
    "v_mul_f32 v68, v68, v76\n"
    "v_mul_f32 v69, v69, v76\n"
    "v_mul_f32 v70, v70, v76\n"
    "v_mul_f32 v71, v71, v76\n"
    "v_mul_f32 v72, v72, v76\n"
    "v_mul_f32 v73, v73, v76\n"
    "v_mul_f32 v74, v74, v76\n"
    "v_mul_f32 v75, v75, v76\n"
    "v_cvt_pk_bf16_f32 v104, v64, v65\n"
    "v_cvt_pk_bf16_f32 v105, v66, v67\n"
    "v_cvt_pk_bf16_f32 v106, v68, v69\n"
    "v_cvt_pk_bf16_f32 v107, v70, v71\n"
    "v_cvt_pk_bf16_f32 v108, v72, v73\n"
    "v_cvt_pk_bf16_f32 v109, v74, v75\n"
    "v_mov_b32 v110, v108\n"
    "v_mov_b32 v111, v109\n"
    "s_nop 0\n"
    "v_permlane16_swap_b32 v108, v110\n"
    "v_permlane16_swap_b32 v109, v111\n"
    "s_lshl_b32 s54, s53, 1\n"
    "s_add_u32 s54, s54, s45\n"
    "s_lshl_b32 s54, s54, 10\n"
    "s_add_u32 s54, s54, 223936512\n"
    "buffer_store_dwordx4 v[104:107], v0, s[40:43], s54 offen sc1\n"
    "s_sub_u32 s54, s47, 1\n"
    "s_lshl_b32 s54, s54, 1\n"
    "s_add_u32 s54, s54, s45\n"
    "s_lshl_b32 s54, s54, 3\n"
    "s_lshr_b32 s55, s44, 3\n"
    "s_lshl_b32 s55, s55, 1\n"
    "s_add_u32 s54, s54, s55\n"
    "s_and_b32 s55, s44, 1\n"
    "s_add_u32 s54, s54, s55\n"
    "s_lshl_b32 s54, s54, 10\n"
    "s_add_u32 s54, s54, 228130816\n"
    "buffer_store_dwordx4 v[108:111], v12, s[40:43], s54 offen sc1\n"
    "s_nop 1\n"
    "pq_nc_%=:\n"
    "s_waitcnt vmcnt(0)\n"
    "s_waitcnt lgkmcnt(0)\n"
    "s_barrier\n"
    "v_mov_b32 v4, 64\n"
    "buffer_store_dword v4, v3, s[40:43], s58 offen sc1\n"
    "s_nop 1\n"
    "s_branch pq_end_%=\n"
    "pq_abort_%=:\n"
    "v_mov_b32 v4, 1\n"
    "v_mov_b32 v5, 1024\n"
    "buffer_store_dword v4, v5, s[40:43], s58 offen sc1\n"
    "pq_end_%=:\n"
    "s_endpgm\n"
    :: ROLE_INPUTS : ROLE_CLOBBERS);
}
DEV void role_projkv(unsigned lo, unsigned hi, int id, int w, int aux, const Tune& tn) {
    asm volatile(
    "s_mov_b32 s40, %0\n"
    "s_mov_b32 s41, %1\n"
    "s_mov_b32 s42, %2\n"
    "s_mov_b32 s43, %3\n"
    "s_mov_b32 s44, %4\n"
    "s_mov_b32 s45, %5\n"
    "s_mov_b32 s46, %6\n"
    "s_mov_b32 s66, %7\n"
    "s_mov_b32 s67, %8\n"
    "s_mov_b32 s68, %9\n"
    "s_mov_b32 s69, %10\n"
    "s_mov_b32 s70, %11\n"
    "s_mov_b32 s71, %12\n"
    "s_mov_b32 s72, %13\n"
    "s_mov_b32 s73, %14\n"
    "s_mov_b64 exec, -1\n"
    "v_mbcnt_lo_u32_b32 v15, -1, 0\n"
    "v_mbcnt_hi_u32_b32 v15, -1, v15\n"
    "v_lshlrev_b32 v0, 4, v15\n"
    "s_mov_b32 s58, 2097152\n"
    "s_mov_b32 s92, 0\n"
    "s_lshl_b32 s48, s46, 5\n"
    "s_add_u32 s48, s48, s44\n"
    "s_mul_i32 s48, s48, 12\n"
    "s_mul_i32 s49, s45, 3\n"
    "s_add_u32 s48, s48, s49\n"
    "s_mul_i32 s48, s48, 20480\n"
    "s_add_u32 s48, s48, 2162688\n"
    "s_mul_i32 s62, s45, 10240\n"
    "s_add_u32 s50, s62, 59834368\n"
    "s_mov_b32 s61, s62\n"
    "s_add_u32 m0, s61, 0\n"
    "s_add_u32 s51, s50, 0\n"
    "buffer_load_dwordx4 v0, s[40:43], s51 offen offset:0 lds\n"
    "buffer_load_dwordx4 v0, s[40:43], s51 offen offset:1024 lds\n"
    "buffer_load_dwordx4 v0, s[40:43], s51 offen offset:2048 lds\n"
    "buffer_load_dwordx4 v0, s[40:43], s51 offen offset:3072 lds\n"
    "s_add_u32 m0, s61, 4096\n"
    "s_add_u32 s51, s50, 4096\n"
    "buffer_load_dwordx4 v0, s[40:43], s51 offen offset:0 lds\n"
    "buffer_load_dwordx4 v0, s[40:43], s51 offen offset:1024 lds\n"
    "buffer_load_dwordx4 v0, s[40:43], s51 offen offset:2048 lds\n"
    "buffer_load_dwordx4 v0, s[40:43], s51 offen offset:3072 lds\n"
    "s_add_u32 m0, s61, 8192\n"
    "s_add_u32 s51, s50, 8192\n"
    "buffer_load_dwordx4 v0, s[40:43], s51 offen offset:0 lds\n"
    "buffer_load_dwordx4 v0, s[40:43], s51 offen offset:1024 lds\n"
    "s_add_u32 s50, s50, 40960\n"
    "s_add_u32 s61, s61, 40960\n"
    "s_cmp_ge_u32 s61, 122880\n"
    "s_cselect_b32 s51, 122880, 0\n"
    "s_sub_u32 s61, s61, s51\n"
    "s_add_u32 m0, s61, 0\n"
    "s_add_u32 s51, s50, 0\n"
    "buffer_load_dwordx4 v0, s[40:43], s51 offen offset:0 lds\n"
    "buffer_load_dwordx4 v0, s[40:43], s51 offen offset:1024 lds\n"
    "buffer_load_dwordx4 v0, s[40:43], s51 offen offset:2048 lds\n"
    "buffer_load_dwordx4 v0, s[40:43], s51 offen offset:3072 lds\n"
    "s_add_u32 m0, s61, 4096\n"
    "s_add_u32 s51, s50, 4096\n"
    "buffer_load_dwordx4 v0, s[40:43], s51 offen offset:0 lds\n"
    "buffer_load_dwordx4 v0, s[40:43], s51 offen offset:1024 lds\n"
    "buffer_load_dwordx4 v0, s[40:43], s51 offen offset:2048 lds\n"
    "buffer_load_dwordx4 v0, s[40:43], s51 offen offset:3072 lds\n"
    "s_add_u32 m0, s61, 8192\n"
    "s_add_u32 s51, s50, 8192\n"
    "buffer_load_dwordx4 v0, s[40:43], s51 offen offset:0 lds\n"
    "buffer_load_dwordx4 v0, s[40:43], s51 offen offset:1024 lds\n"
    "s_add_u32 s50, s50, 40960\n"
    "s_add_u32 s61, s61, 40960\n"
    "s_cmp_ge_u32 s61, 122880\n"
    "s_cselect_b32 s51, 122880, 0\n"
    "s_sub_u32 s61, s61, s51\n"
    "buffer_load_dwordx4 a[0:3], v0, s[40:43], s48 offen\n"
    "buffer_load_dwordx4 a[4:7], v0, s[40:43], s48 offen offset:1024\n"
    "buffer_load_dwordx4 a[8:11], v0, s[40:43], s48 offen offset:2048\n"
    "buffer_load_dwordx4 a[12:15], v0, s[40:43], s48 offen offset:3072\n"
    "s_add_u32 s48, s48, 4096\n"
    "buffer_load_dwordx4 a[16:19], v0, s[40:43], s48 offen\n"
    "buffer_load_dwordx4 a[20:23], v0, s[40:43], s48 offen offset:1024\n"
    "buffer_load_dwordx4 a[24:27], v0, s[40:43], s48 offen offset:2048\n"
    "buffer_load_dwordx4 a[28:31], v0, s[40:43], s48 offen offset:3072\n"
    "s_add_u32 s48, s48, 4096\n"
    "buffer_load_dwordx4 a[32:35], v0, s[40:43], s48 offen\n"
    "buffer_load_dwordx4 a[36:39], v0, s[40:43], s48 offen offset:1024\n"
    "buffer_load_dwordx4 a[40:43], v0, s[40:43], s48 offen offset:2048\n"
    "buffer_load_dwordx4 a[44:47], v0, s[40:43], s48 offen offset:3072\n"
    "s_add_u32 s48, s48, 4096\n"
    "buffer_load_dwordx4 a[48:51], v0, s[40:43], s48 offen\n"
    "buffer_load_dwordx4 a[52:55], v0, s[40:43], s48 offen offset:1024\n"
    "buffer_load_dwordx4 a[56:59], v0, s[40:43], s48 offen offset:2048\n"
    "buffer_load_dwordx4 a[60:63], v0, s[40:43], s48 offen offset:3072\n"
    "s_add_u32 s48, s48, 4096\n"
    "buffer_load_dwordx4 a[64:67], v0, s[40:43], s48 offen\n"
    "buffer_load_dwordx4 a[68:71], v0, s[40:43], s48 offen offset:1024\n"
    "buffer_load_dwordx4 a[72:75], v0, s[40:43], s48 offen offset:2048\n"
    "buffer_load_dwordx4 a[76:79], v0, s[40:43], s48 offen offset:3072\n"
    "s_add_u32 s48, s48, 4096\n"
    "buffer_load_dwordx4 a[80:83], v0, s[40:43], s48 offen\n"
    "buffer_load_dwordx4 a[84:87], v0, s[40:43], s48 offen offset:1024\n"
    "buffer_load_dwordx4 a[88:91], v0, s[40:43], s48 offen offset:2048\n"
    "buffer_load_dwordx4 a[92:95], v0, s[40:43], s48 offen offset:3072\n"
    "s_add_u32 s48, s48, 4096\n"
    "buffer_load_dwordx4 a[96:99], v0, s[40:43], s48 offen\n"
    "buffer_load_dwordx4 a[100:103], v0, s[40:43], s48 offen offset:1024\n"
    "buffer_load_dwordx4 a[104:107], v0, s[40:43], s48 offen offset:2048\n"
    "buffer_load_dwordx4 a[108:111], v0, s[40:43], s48 offen offset:3072\n"
    "s_add_u32 s48, s48, 4096\n"
    "buffer_load_dwordx4 a[112:115], v0, s[40:43], s48 offen\n"
    "buffer_load_dwordx4 a[116:119], v0, s[40:43], s48 offen offset:1024\n"
    "buffer_load_dwordx4 a[120:123], v0, s[40:43], s48 offen offset:2048\n"
    "buffer_load_dwordx4 a[124:127], v0, s[40:43], s48 offen offset:3072\n"
    "s_add_u32 s48, s48, 4096\n"
    "buffer_load_dwordx4 a[128:131], v0, s[40:43], s48 offen\n"
    "buffer_load_dwordx4 a[132:135], v0, s[40:43], s48 offen offset:1024\n"
    "buffer_load_dwordx4 a[136:139], v0, s[40:43], s48 offen offset:2048\n"
    "buffer_load_dwordx4 a[140:143], v0, s[40:43], s48 offen offset:3072\n"
    "s_add_u32 s48, s48, 4096\n"
    "buffer_load_dwordx4 a[144:147], v0, s[40:43], s48 offen\n"
    "buffer_load_dwordx4 a[148:151], v0, s[40:43], s48 offen offset:1024\n"
    "buffer_load_dwordx4 a[152:155], v0, s[40:43], s48 offen offset:2048\n"
    "buffer_load_dwordx4 a[156:159], v0, s[40:43], s48 offen offset:3072\n"
    "s_add_u32 s48, s48, 4096\n"
    "buffer_load_dwordx4 a[160:163], v0, s[40:43], s48 offen\n"
    "buffer_load_dwordx4 a[164:167], v0, s[40:43], s48 offen offset:1024\n"
    "buffer_load_dwordx4 a[168:171], v0, s[40:43], s48 offen offset:2048\n"
    "s_waitcnt vmcnt(61)\n"
    "buffer_load_dwordx4 a[172:175], v0, s[40:43], s48 offen offset:3072\n"
    "s_add_u32 s48, s48, 4096\n"
    "buffer_load_dwordx4 a[176:179], v0, s[40:43], s48 offen\n"
    "s_waitcnt vmcnt(61)\n"
    "buffer_load_dwordx4 a[180:183], v0, s[40:43], s48 offen offset:1024\n"
    "buffer_load_dwordx4 a[184:187], v0, s[40:43], s48 offen offset:2048\n"
    "s_waitcnt vmcnt(61)\n"
    "buffer_load_dwordx4 a[188:191], v0, s[40:43], s48 offen offset:3072\n"
    "s_add_u32 s48, s48, 4096\n"
    "buffer_load_dwordx4 a[192:195], v0, s[40:43], s48 offen\n"
    "s_waitcnt vmcnt(61)\n"
    "buffer_load_dwordx4 a[196:199], v0, s[40:43], s48 offen offset:1024\n"
    "buffer_load_dwordx4 a[200:203], v0, s[40:43], s48 offen offset:2048\n"
    "s_waitcnt vmcnt(61)\n"
    "buffer_load_dwordx4 a[204:207], v0, s[40:43], s48 offen offset:3072\n"
    "s_add_u32 s48, s48, 4096\n"
    "buffer_load_dwordx4 a[208:211], v0, s[40:43], s48 offen\n"
    "s_waitcnt vmcnt(61)\n"
    "buffer_load_dwordx4 a[212:215], v0, s[40:43], s48 offen offset:1024\n"
    "buffer_load_dwordx4 a[216:219], v0, s[40:43], s48 offen offset:2048\n"
    "s_waitcnt vmcnt(61)\n"
    "buffer_load_dwordx4 a[220:223], v0, s[40:43], s48 offen offset:3072\n"
    "s_add_u32 s48, s48, 4096\n"
    "buffer_load_dwordx4 a[224:227], v0, s[40:43], s48 offen\n"
    "s_waitcnt vmcnt(61)\n"
    "buffer_load_dwordx4 a[228:231], v0, s[40:43], s48 offen offset:1024\n"
    "buffer_load_dwordx4 a[232:235], v0, s[40:43], s48 offen offset:2048\n"
    "s_waitcnt vmcnt(61)\n"
    "buffer_load_dwordx4 a[236:239], v0, s[40:43], s48 offen offset:3072\n"
    "v_mov_b32 v1, v0\n"
    "s_mov_b32 s64, 0\n"
    "s_lshl_b32 s48, s44, 7\n"
    "s_mov_b32 s49, 6144\n"
    "s_mov_b32 s63, 87621632\n"
    "s_cmp_eq_u32 s46, 1\n"
    "s_cselect_b32 s49, 2048, s49\n"
    "s_cselect_b32 s63, 62455808, s63\n"
    "s_add_u32 s49, s49, s48\n"
    "s_mul_i32 s53, s44, 12288\n"
    "s_add_u32 s63, s63, s53\n"
    "s_mul_i32 s53, s45, 3072\n"
    "s_add_u32 s63, s63, s53\n"
    "v_mov_b32 v3, s49\n"
    "v_or_b32 v4, s45, v15\n"
    "v_cmp_eq_u32 vcc, 0, v4\n"
    "v_mov_b32 v4, 2147483632\n"
    "s_nop 0\n"
    "v_cndmask_b32 v3, v4, v3, vcc\n"
    "s_mov_b32 s47, 0\n"
    "s_waitcnt vmcnt(0)\n"
    "s_waitcnt lgkmcnt(0)\n"
    "s_barrier\n"
    "s_mov_b32 s65, 0x40000000\n"
    "ds_read_b128 v[80:83], v1 offset:0\n"
    "ds_read_b128 v[84:87], v1 offset:20480\n"
    "ds_read_b128 v[88:91], v1 offset:1024\n"
    "ds_read_b128 v[92:95], v1 offset:21504\n"
    "ds_read_b128 v[96:99], v1 offset:2048\n"
    "pk_loop_%=:\n"
    "s_waitcnt lgkmcnt(4)\n"
    "v_mfma_f32_16x16x32_bf16 v[16:19], a[0:3], v[80:83], 0\n"
    "v_mfma_f32_16x16x32_bf16 v[24:27], a[80:83], v[80:83], 0\n"
    "v_mfma_f32_16x16x32_bf16 v[32:35], a[160:163], v[80:83], 0\n"
    "ds_read_b128 v[80:83], v1 offset:22528\n"
    "s_waitcnt lgkmcnt(4)\n"
    "v_mfma_f32_16x16x32_bf16 v[20:23], a[0:3], v[84:87], 0\n"
    "v_mfma_f32_16x16x32_bf16 v[28:31], a[80:83], v[84:87], 0\n"
    "v_mfma_f32_16x16x32_bf16 v[36:39], a[160:163], v[84:87], 0\n"
    "ds_read_b128 v[84:87], v1 offset:3072\n"
    "s_waitcnt lgkmcnt(4)\n"
    "v_mfma_f32_16x16x32_bf16 v[16:19], a[4:7], v[88:91], v[16:19]\n"
    "v_mfma_f32_16x16x32_bf16 v[24:27], a[84:87], v[88:91], v[24:27]\n"
    "v_mfma_f32_16x16x32_bf16 v[32:35], a[164:167], v[88:91], v[32:35]\n"
    "ds_read_b128 v[88:91], v1 offset:23552\n"
    "s_waitcnt lgkmcnt(4)\n"
    "v_mfma_f32_16x16x32_bf16 v[20:23], a[4:7], v[92:95], v[20:23]\n"
    "v_cvt_pk_bf16_f32 v104, v40, v41\n"
    "v_mfma_f32_16x16x32_bf16 v[28:31], a[84:87], v[92:95], v[28:31]\n"
    "v_cvt_pk_bf16_f32 v105, v42, v43\n"
    "v_mfma_f32_16x16x32_bf16 v[36:39], a[164:167], v[92:95], v[36:39]\n"
    "ds_read_b128 v[92:95], v1 offset:4096\n"
    "s_waitcnt lgkmcnt(4)\n"
    "v_mfma_f32_16x16x32_bf16 v[16:19], a[8:11], v[96:99], v[16:19]\n"
    "v_cvt_pk_bf16_f32 v106, v44, v45\n"
    "v_mfma_f32_16x16x32_bf16 v[24:27], a[88:91], v[96:99], v[24:27]\n"
    "v_cvt_pk_bf16_f32 v107, v46, v47\n"
    "v_mfma_f32_16x16x32_bf16 v[32:35], a[168:171], v[96:99], v[32:35]\n"
    "ds_read_b128 v[96:99], v1 offset:24576\n"
    "s_waitcnt lgkmcnt(4)\n"
    "v_mfma_f32_16x16x32_bf16 v[20:23], a[8:11], v[80:83], v[20:23]\n"
    "buffer_store_dwordx4 v[104:107], v0, s[40:43], s65 offen offset:0 sc1\n"
    "v_mfma_f32_16x16x32_bf16 v[28:31], a[88:91], v[80:83], v[28:31]\n"
    "v_cvt_pk_bf16_f32 v108, v48, v49\n"
    "v_mfma_f32_16x16x32_bf16 v[36:39], a[168:171], v[80:83], v[36:39]\n"
    "ds_read_b128 v[80:83], v1 offset:5120\n"
    "s_waitcnt lgkmcnt(4)\n"
    "v_mfma_f32_16x16x32_bf16 v[16:19], a[12:15], v[84:87], v[16:19]\n"
    "v_cvt_pk_bf16_f32 v109, v50, v51\n"
    "v_mfma_f32_16x16x32_bf16 v[24:27], a[92:95], v[84:87], v[24:27]\n"
    "v_cvt_pk_bf16_f32 v110, v52, v53\n"
    "v_mfma_f32_16x16x32_bf16 v[32:35], a[172:175], v[84:87], v[32:35]\n"
    "ds_read_b128 v[84:87], v1 offset:25600\n"
    "s_waitcnt lgkmcnt(4)\n"
    "v_mfma_f32_16x16x32_bf16 v[20:23], a[12:15], v[88:91], v[20:23]\n"
    "v_cvt_pk_bf16_f32 v111, v54, v55\n"
    "v_mfma_f32_16x16x32_bf16 v[28:31], a[92:95], v[88:91], v[28:31]\n"
    "buffer_store_dwordx4 v[108:111], v0, s[40:43], s65 offen offset:1024 sc1\n"
    "v_mfma_f32_16x16x32_bf16 v[36:39], a[172:175], v[88:91], v[36:39]\n"
    "ds_read_b128 v[88:91], v1 offset:6144\n"
    "s_waitcnt lgkmcnt(4)\n"
    "v_mfma_f32_16x16x32_bf16 v[16:19], a[16:19], v[92:95], v[16:19]\n"
    "v_cvt_pk_bf16_f32 v112, v56, v57\n"
    "v_mfma_f32_16x16x32_bf16 v[24:27], a[96:99], v[92:95], v[24:27]\n"
    "v_cvt_pk_bf16_f32 v113, v58, v59\n"
    "v_mfma_f32_16x16x32_bf16 v[32:35], a[176:179], v[92:95], v[32:35]\n"
    "ds_read_b128 v[92:95], v1 offset:26624\n"
    "s_waitcnt lgkmcnt(4)\n"
    "v_mfma_f32_16x16x32_bf16 v[20:23], a[16:19], v[96:99], v[20:23]\n"
    "v_cvt_pk_bf16_f32 v114, v60, v61\n"
    "v_mfma_f32_16x16x32_bf16 v[28:31], a[96:99], v[96:99], v[28:31]\n"
    "v_cvt_pk_bf16_f32 v115, v62, v63\n"
    "v_mfma_f32_16x16x32_bf16 v[36:39], a[176:179], v[96:99], v[36:39]\n"
    "ds_read_b128 v[96:99], v1 offset:7168\n"
    "s_waitcnt lgkmcnt(4)\n"
    "v_mfma_f32_16x16x32_bf16 v[16:19], a[20:23], v[80:83], v[16:19]\n"
    "buffer_store_dwordx4 v[112:115], v0, s[40:43], s65 offen offset:2048 sc1\n"
    "v_mfma_f32_16x16x32_bf16 v[24:27], a[100:103], v[80:83], v[24:27]\n"
    "v_mfma_f32_16x16x32_bf16 v[32:35], a[180:183], v[80:83], v[32:35]\n"
    "ds_read_b128 v[80:83], v1 offset:27648\n"
    "s_waitcnt lgkmcnt(4)\n"
    "v_mfma_f32_16x16x32_bf16 v[20:23], a[20:23], v[84:87], v[20:23]\n"
    "v_mfma_f32_16x16x32_bf16 v[28:31], a[100:103], v[84:87], v[28:31]\n"
    "v_mfma_f32_16x16x32_bf16 v[36:39], a[180:183], v[84:87], v[36:39]\n"
    "ds_read_b128 v[84:87], v1 offset:8192\n"
    "s_waitcnt lgkmcnt(4)\n"
    "v_mfma_f32_16x16x32_bf16 v[16:19], a[24:27], v[88:91], v[16:19]\n"
    "v_mfma_f32_16x16x32_bf16 v[24:27], a[104:107], v[88:91], v[24:27]\n"
    "v_mfma_f32_16x16x32_bf16 v[32:35], a[184:187], v[88:91], v[32:35]\n"
    "ds_read_b128 v[88:91], v1 offset:28672\n"
    "s_waitcnt lgkmcnt(4)\n"
    "v_mfma_f32_16x16x32_bf16 v[20:23], a[24:27], v[92:95], v[20:23]\n"
    "v_mfma_f32_16x16x32_bf16 v[28:31], a[104:107], v[92:95], v[28:31]\n"
    "v_mfma_f32_16x16x32_bf16 v[36:39], a[184:187], v[92:95], v[36:39]\n"
    "ds_read_b128 v[92:95], v1 offset:9216\n"
    "s_waitcnt lgkmcnt(4)\n"
    "v_mfma_f32_16x16x32_bf16 v[16:19], a[28:31], v[96:99], v[16:19]\n"
    "v_mfma_f32_16x16x32_bf16 v[24:27], a[108:111], v[96:99], v[24:27]\n"
    "v_mfma_f32_16x16x32_bf16 v[32:35], a[188:191], v[96:99], v[32:35]\n"
    "ds_read_b128 v[96:99], v1 offset:29696\n"
    "s_waitcnt lgkmcnt(4)\n"
    "v_mfma_f32_16x16x32_bf16 v[20:23], a[28:31], v[80:83], v[20:23]\n"
    "v_mfma_f32_16x16x32_bf16 v[28:31], a[108:111], v[80:83], v[28:31]\n"
    "v_mfma_f32_16x16x32_bf16 v[36:39], a[188:191], v[80:83], v[36:39]\n"
    "ds_read_b128 v[80:83], v1 offset:10240\n"
    "s_waitcnt lgkmcnt(4)\n"
    "v_mfma_f32_16x16x32_bf16 v[16:19], a[32:35], v[84:87], v[16:19]\n"
    "v_mfma_f32_16x16x32_bf16 v[24:27], a[112:115], v[84:87], v[24:27]\n"
    "v_mfma_f32_16x16x32_bf16 v[32:35], a[192:195], v[84:87], v[32:35]\n"
    "ds_read_b128 v[84:87], v1 offset:30720\n"
    "s_waitcnt lgkmcnt(4)\n"
    "v_mfma_f32_16x16x32_bf16 v[20:23], a[32:35], v[88:91], v[20:23]\n"
    "v_mfma_f32_16x16x32_bf16 v[28:31], a[112:115], v[88:91], v[28:31]\n"
    "v_mfma_f32_16x16x32_bf16 v[36:39], a[192:195], v[88:91], v[36:39]\n"
    "ds_read_b128 v[88:91], v1 offset:11264\n"
    "s_waitcnt lgkmcnt(4)\n"
    "v_mfma_f32_16x16x32_bf16 v[16:19], a[36:39], v[92:95], v[16:19]\n"
    "v_mfma_f32_16x16x32_bf16 v[24:27], a[116:119], v[92:95], v[24:27]\n"
    "v_mfma_f32_16x16x32_bf16 v[32:35], a[196:199], v[92:95], v[32:35]\n"
    "ds_read_b128 v[92:95], v1 offset:31744\n"
    "s_waitcnt lgkmcnt(4)\n"
    "v_mfma_f32_16x16x32_bf16 v[20:23], a[36:39], v[96:99], v[20:23]\n"
    "v_mfma_f32_16x16x32_bf16 v[28:31], a[116:119], v[96:99], v[28:31]\n"
    "v_mfma_f32_16x16x32_bf16 v[36:39], a[196:199], v[96:99], v[36:39]\n"
    "ds_read_b128 v[96:99], v1 offset:12288\n"
    "s_waitcnt lgkmcnt(4)\n"
    "v_mfma_f32_16x16x32_bf16 v[16:19], a[40:43], v[80:83], v[16:19]\n"
    "v_mfma_f32_16x16x32_bf16 v[24:27], a[120:123], v[80:83], v[24:27]\n"
    "v_mfma_f32_16x16x32_bf16 v[32:35], a[200:203], v[80:83], v[32:35]\n"
    "ds_read_b128 v[80:83], v1 offset:32768\n"
    "s_waitcnt lgkmcnt(4)\n"
    "v_mfma_f32_16x16x32_bf16 v[20:23], a[40:43], v[84:87], v[20:23]\n"
    "v_mfma_f32_16x16x32_bf16 v[28:31], a[120:123], v[84:87], v[28:31]\n"
    "v_mfma_f32_16x16x32_bf16 v[36:39], a[200:203], v[84:87], v[36:39]\n"
    "ds_read_b128 v[84:87], v1 offset:13312\n"
    "s_waitcnt lgkmcnt(4)\n"
    "v_mfma_f32_16x16x32_bf16 v[16:19], a[44:47], v[88:91], v[16:19]\n"
    "v_mfma_f32_16x16x32_bf16 v[24:27], a[124:127], v[88:91], v[24:27]\n"
    "v_mfma_f32_16x16x32_bf16 v[32:35], a[204:207], v[88:91], v[32:35]\n"
    "ds_read_b128 v[88:91], v1 offset:33792\n"
    "s_waitcnt lgkmcnt(4)\n"
    "v_mfma_f32_16x16x32_bf16 v[20:23], a[44:47], v[92:95], v[20:23]\n"
    "v_mfma_f32_16x16x32_bf16 v[28:31], a[124:127], v[92:95], v[28:31]\n"
    "v_mfma_f32_16x16x32_bf16 v[36:39], a[204:207], v[92:95], v[36:39]\n"
    "ds_read_b128 v[92:95], v1 offset:14336\n"
    "s_waitcnt lgkmcnt(4)\n"
    "v_mfma_f32_16x16x32_bf16 v[16:19], a[48:51], v[96:99], v[16:19]\n"
    "v_mfma_f32_16x16x32_bf16 v[24:27], a[128:131], v[96:99], v[24:27]\n"
    "v_mfma_f32_16x16x32_bf16 v[32:35], a[208:211], v[96:99], v[32:35]\n"
    "ds_read_b128 v[96:99], v1 offset:34816\n"
    "s_waitcnt lgkmcnt(4)\n"
    "v_mfma_f32_16x16x32_bf16 v[20:23], a[48:51], v[80:83], v[20:23]\n"
    "v_mfma_f32_16x16x32_bf16 v[28:31], a[128:131], v[80:83], v[28:31]\n"
    "v_mfma_f32_16x16x32_bf16 v[36:39], a[208:211], v[80:83], v[36:39]\n"
    "ds_read_b128 v[80:83], v1 offset:15360\n"
    "s_waitcnt lgkmcnt(4)\n"
    "v_mfma_f32_16x16x32_bf16 v[16:19], a[52:55], v[84:87], v[16:19]\n"
    "v_mfma_f32_16x16x32_bf16 v[24:27], a[132:135], v[84:87], v[24:27]\n"
    "v_mfma_f32_16x16x32_bf16 v[32:35], a[212:215], v[84:87], v[32:35]\n"
    "ds_read_b128 v[84:87], v1 offset:35840\n"
    "s_waitcnt lgkmcnt(4)\n"
    "v_mfma_f32_16x16x32_bf16 v[20:23], a[52:55], v[88:91], v[20:23]\n"
    "v_mfma_f32_16x16x32_bf16 v[28:31], a[132:135], v[88:91], v[28:31]\n"
    "v_mfma_f32_16x16x32_bf16 v[36:39], a[212:215], v[88:91], v[36:39]\n"
    "ds_read_b128 v[88:91], v1 offset:16384\n"
    "s_waitcnt lgkmcnt(4)\n"
    "v_mfma_f32_16x16x32_bf16 v[16:19], a[56:59], v[92:95], v[16:19]\n"
    "v_mfma_f32_16x16x32_bf16 v[24:27], a[136:139], v[92:95], v[24:27]\n"
    "v_mfma_f32_16x16x32_bf16 v[32:35], a[216:219], v[92:95], v[32:35]\n"
    "ds_read_b128 v[92:95], v1 offset:36864\n"
    "s_waitcnt lgkmcnt(4)\n"
    "v_mfma_f32_16x16x32_bf16 v[20:23], a[56:59], v[96:99], v[20:23]\n"
    "v_mfma_f32_16x16x32_bf16 v[28:31], a[136:139], v[96:99], v[28:31]\n"
    "v_mfma_f32_16x16x32_bf16 v[36:39], a[216:219], v[96:99], v[36:39]\n"
    "s_waitcnt vmcnt(0)\n"
    "s_barrier\n"
    "v_mov_b32 v4, s47\n"
    "buffer_store_dword v4, v3, s[40:43], s58 offen sc1\n"
    "ds_read_b128 v[96:99], v1 offset:17408\n"
    "s_waitcnt lgkmcnt(4)\n"
    "v_mfma_f32_16x16x32_bf16 v[16:19], a[60:63], v[80:83], v[16:19]\n"
    "s_add_u32 m0, s61, 0\n"
    "s_add_u32 s51, s50, 0\n"
    "buffer_load_dwordx4 v0, s[40:43], s51 offen offset:0 lds\n"
    "v_mfma_f32_16x16x32_bf16 v[24:27], a[140:143], v[80:83], v[24:27]\n"
    "v_mfma_f32_16x16x32_bf16 v[32:35], a[220:223], v[80:83], v[32:35]\n"
    "ds_read_b128 v[80:83], v1 offset:37888\n"
    "s_waitcnt lgkmcnt(4)\n"
    "v_mfma_f32_16x16x32_bf16 v[20:23], a[60:63], v[84:87], v[20:23]\n"
    "buffer_load_dwordx4 v0, s[40:43], s51 offen offset:1024 lds\n"
    "v_mfma_f32_16x16x32_bf16 v[28:31], a[140:143], v[84:87], v[28:31]\n"
    "v_mfma_f32_16x16x32_bf16 v[36:39], a[220:223], v[84:87], v[36:39]\n"
    "ds_read_b128 v[84:87], v1 offset:18432\n"
    "s_waitcnt lgkmcnt(4)\n"
    "v_mfma_f32_16x16x32_bf16 v[16:19], a[64:67], v[88:91], v[16:19]\n"
    "buffer_load_dwordx4 v0, s[40:43], s51 offen offset:2048 lds\n"
    "v_mfma_f32_16x16x32_bf16 v[24:27], a[144:147], v[88:91], v[24:27]\n"
    "v_mfma_f32_16x16x32_bf16 v[32:35], a[224:227], v[88:91], v[32:35]\n"
    "ds_read_b128 v[88:91], v1 offset:38912\n"
    "s_waitcnt lgkmcnt(4)\n"
    "v_mfma_f32_16x16x32_bf16 v[20:23], a[64:67], v[92:95], v[20:23]\n"
    "buffer_load_dwordx4 v0, s[40:43], s51 offen offset:3072 lds\n"
    "v_mfma_f32_16x16x32_bf16 v[28:31], a[144:147], v[92:95], v[28:31]\n"
    "v_mfma_f32_16x16x32_bf16 v[36:39], a[224:227], v[92:95], v[36:39]\n"
    "ds_read_b128 v[92:95], v1 offset:19456\n"
    "s_waitcnt lgkmcnt(4)\n"
    "v_mfma_f32_16x16x32_bf16 v[16:19], a[68:71], v[96:99], v[16:19]\n"
    "s_add_u32 m0, s61, 4096\n"
    "s_add_u32 s51, s50, 4096\n"
    "buffer_load_dwordx4 v0, s[40:43], s51 offen offset:0 lds\n"
    "v_mfma_f32_16x16x32_bf16 v[24:27], a[148:151], v[96:99], v[24:27]\n"
    "v_mfma_f32_16x16x32_bf16 v[32:35], a[228:231], v[96:99], v[32:35]\n"
    "ds_read_b128 v[96:99], v1 offset:39936\n"
    "s_waitcnt lgkmcnt(4)\n"
    "v_mfma_f32_16x16x32_bf16 v[20:23], a[68:71], v[80:83], v[20:23]\n"
    "buffer_load_dwordx4 v0, s[40:43], s51 offen offset:1024 lds\n"
    "v_mfma_f32_16x16x32_bf16 v[28:31], a[148:151], v[80:83], v[28:31]\n"
    "v_mfma_f32_16x16x32_bf16 v[36:39], a[228:231], v[80:83], v[36:39]\n"
    "s_add_u32 s64, s64, 40960\n"
    "s_cmp_ge_u32 s64, 122880\n"
    "s_cselect_b32 s64, 0, s64\n"
    "v_add_u32 v1, s64, v0\n"
    "ds_read_b128 v[80:83], v1 offset:0\n"
    "s_waitcnt lgkmcnt(4)\n"
    "v_mfma_f32_16x16x32_bf16 v[16:19], a[72:75], v[84:87], v[16:19]\n"
    "buffer_load_dwordx4 v0, s[40:43], s51 offen offset:2048 lds\n"
    "v_mfma_f32_16x16x32_bf16 v[24:27], a[152:155], v[84:87], v[24:27]\n"
    "v_mfma_f32_16x16x32_bf16 v[32:35], a[232:235], v[84:87], v[32:35]\n"
    "ds_read_b128 v[84:87], v1 offset:20480\n"
    "s_waitcnt lgkmcnt(4)\n"
    "v_mfma_f32_16x16x32_bf16 v[20:23], a[72:75], v[88:91], v[20:23]\n"
    "buffer_load_dwordx4 v0, s[40:43], s51 offen offset:3072 lds\n"
    "v_mfma_f32_16x16x32_bf16 v[28:31], a[152:155], v[88:91], v[28:31]\n"
    "v_mfma_f32_16x16x32_bf16 v[36:39], a[232:235], v[88:91], v[36:39]\n"
    "ds_read_b128 v[88:91], v1 offset:1024\n"
    "s_waitcnt lgkmcnt(4)\n"
    "v_mfma_f32_16x16x32_bf16 v[16:19], a[76:79], v[92:95], v[16:19]\n"
    "s_add_u32 m0, s61, 8192\n"
    "s_add_u32 s51, s50, 8192\n"
    "buffer_load_dwordx4 v0, s[40:43], s51 offen offset:0 lds\n"
    "v_mfma_f32_16x16x32_bf16 v[24:27], a[156:159], v[92:95], v[24:27]\n"
    "v_mfma_f32_16x16x32_bf16 v[32:35], a[236:239], v[92:95], v[32:35]\n"
    "ds_read_b128 v[92:95], v1 offset:21504\n"
    "s_waitcnt lgkmcnt(4)\n"
    "v_mfma_f32_16x16x32_bf16 v[20:23], a[76:79], v[96:99], v[20:23]\n"
    "buffer_load_dwordx4 v0, s[40:43], s51 offen offset:1024 lds\n"
    "s_add_u32 s50, s50, 40960\n"
    "s_add_u32 s61, s61, 40960\n"
    "s_cmp_ge_u32 s61, 122880\n"
    "s_cselect_b32 s51, 122880, 0\n"
    "s_sub_u32 s61, s61, s51\n"
    "v_mfma_f32_16x16x32_bf16 v[28:31], a[156:159], v[96:99], v[28:31]\n"
    "v_mfma_f32_16x16x32_bf16 v[36:39], a[236:239], v[96:99], v[36:39]\n"
    "ds_read_b128 v[96:99], v1 offset:2048\n"
    "s_mov_b32 s65, s63\n"
    "s_add_u32 s63, s63, 393216\n"
    "s_add_u32 s47, s47, 1\n"
    "s_waitcnt lgkmcnt(4)\n"
    "v_mfma_f32_16x16x32_bf16 v[40:43], a[0:3], v[80:83], 0\n"
    "v_mfma_f32_16x16x32_bf16 v[48:51], a[80:83], v[80:83], 0\n"
    "v_mfma_f32_16x16x32_bf16 v[56:59], a[160:163], v[80:83], 0\n"
    "ds_read_b128 v[80:83], v1 offset:22528\n"
    "s_waitcnt lgkmcnt(4)\n"
    "v_mfma_f32_16x16x32_bf16 v[44:47], a[0:3], v[84:87], 0\n"
    "v_mfma_f32_16x16x32_bf16 v[52:55], a[80:83], v[84:87], 0\n"
    "v_mfma_f32_16x16x32_bf16 v[60:63], a[160:163], v[84:87], 0\n"
    "ds_read_b128 v[84:87], v1 offset:3072\n"
    "s_waitcnt lgkmcnt(4)\n"
    "v_mfma_f32_16x16x32_bf16 v[40:43], a[4:7], v[88:91], v[40:43]\n"
    "v_mfma_f32_16x16x32_bf16 v[48:51], a[84:87], v[88:91], v[48:51]\n"
    "v_mfma_f32_16x16x32_bf16 v[56:59], a[164:167], v[88:91], v[56:59]\n"
    "ds_read_b128 v[88:91], v1 offset:23552\n"
    "s_waitcnt lgkmcnt(4)\n"
    "v_mfma_f32_16x16x32_bf16 v[44:47], a[4:7], v[92:95], v[44:47]\n"
    "v_cvt_pk_bf16_f32 v104, v16, v17\n"
    "v_mfma_f32_16x16x32_bf16 v[52:55], a[84:87], v[92:95], v[52:55]\n"
    "v_cvt_pk_bf16_f32 v105, v18, v19\n"
    "v_mfma_f32_16x16x32_bf16 v[60:63], a[164:167], v[92:95], v[60:63]\n"
    "ds_read_b128 v[92:95], v1 offset:4096\n"
    "s_waitcnt lgkmcnt(4)\n"
    "v_mfma_f32_16x16x32_bf16 v[40:43], a[8:11], v[96:99], v[40:43]\n"
    "v_cvt_pk_bf16_f32 v106, v20, v21\n"
    "v_mfma_f32_16x16x32_bf16 v[48:51], a[88:91], v[96:99], v[48:51]\n"
    "v_cvt_pk_bf16_f32 v107, v22, v23\n"
    "v_mfma_f32_16x16x32_bf16 v[56:59], a[168:171], v[96:99], v[56:59]\n"
    "ds_read_b128 v[96:99], v1 offset:24576\n"
    "s_waitcnt lgkmcnt(4)\n"
    "v_mfma_f32_16x16x32_bf16 v[44:47], a[8:11], v[80:83], v[44:47]\n"
    "buffer_store_dwordx4 v[104:107], v0, s[40:43], s65 offen offset:0 sc1\n"
    "v_mfma_f32_16x16x32_bf16 v[52:55], a[88:91], v[80:83], v[52:55]\n"
    "v_cvt_pk_bf16_f32 v108, v24, v25\n"
    "v_mfma_f32_16x16x32_bf16 v[60:63], a[168:171], v[80:83], v[60:63]\n"
    "ds_read_b128 v[80:83], v1 offset:5120\n"
    "s_waitcnt lgkmcnt(4)\n"
    "v_mfma_f32_16x16x32_bf16 v[40:43], a[12:15], v[84:87], v[40:43]\n"
    "v_cvt_pk_bf16_f32 v109, v26, v27\n"
    "v_mfma_f32_16x16x32_bf16 v[48:51], a[92:95], v[84:87], v[48:51]\n"
    "v_cvt_pk_bf16_f32 v110, v28, v29\n"
    "v_mfma_f32_16x16x32_bf16 v[56:59], a[172:175], v[84:87], v[56:59]\n"
    "ds_read_b128 v[84:87], v1 offset:25600\n"
    "s_waitcnt lgkmcnt(4)\n"
    "v_mfma_f32_16x16x32_bf16 v[44:47], a[12:15], v[88:91], v[44:47]\n"
    "v_cvt_pk_bf16_f32 v111, v30, v31\n"
    "v_mfma_f32_16x16x32_bf16 v[52:55], a[92:95], v[88:91], v[52:55]\n"
    "buffer_store_dwordx4 v[108:111], v0, s[40:43], s65 offen offset:1024 sc1\n"
    "v_mfma_f32_16x16x32_bf16 v[60:63], a[172:175], v[88:91], v[60:63]\n"
    "ds_read_b128 v[88:91], v1 offset:6144\n"
    "s_waitcnt lgkmcnt(4)\n"
    "v_mfma_f32_16x16x32_bf16 v[40:43], a[16:19], v[92:95], v[40:43]\n"
    "v_cvt_pk_bf16_f32 v112, v32, v33\n"
    "v_mfma_f32_16x16x32_bf16 v[48:51], a[96:99], v[92:95], v[48:51]\n"
    "v_cvt_pk_bf16_f32 v113, v34, v35\n"
    "v_mfma_f32_16x16x32_bf16 v[56:59], a[176:179], v[92:95], v[56:59]\n"
    "ds_read_b128 v[92:95], v1 offset:26624\n"
    "s_waitcnt lgkmcnt(4)\n"
    "v_mfma_f32_16x16x32_bf16 v[44:47], a[16:19], v[96:99], v[44:47]\n"
    "v_cvt_pk_bf16_f32 v114, v36, v37\n"
    "v_mfma_f32_16x16x32_bf16 v[52:55], a[96:99], v[96:99], v[52:55]\n"
    "v_cvt_pk_bf16_f32 v115, v38, v39\n"
    "v_mfma_f32_16x16x32_bf16 v[60:63], a[176:179], v[96:99], v[60:63]\n"
    "ds_read_b128 v[96:99], v1 offset:7168\n"
    "s_waitcnt lgkmcnt(4)\n"
    "v_mfma_f32_16x16x32_bf16 v[40:43], a[20:23], v[80:83], v[40:43]\n"
    "buffer_store_dwordx4 v[112:115], v0, s[40:43], s65 offen offset:2048 sc1\n"
    "v_mfma_f32_16x16x32_bf16 v[48:51], a[100:103], v[80:83], v[48:51]\n"
    "v_mfma_f32_16x16x32_bf16 v[56:59], a[180:183], v[80:83], v[56:59]\n"
    "ds_read_b128 v[80:83], v1 offset:27648\n"
    "s_waitcnt lgkmcnt(4)\n"
    "v_mfma_f32_16x16x32_bf16 v[44:47], a[20:23], v[84:87], v[44:47]\n"
    "v_mfma_f32_16x16x32_bf16 v[52:55], a[100:103], v[84:87], v[52:55]\n"
    "v_mfma_f32_16x16x32_bf16 v[60:63], a[180:183], v[84:87], v[60:63]\n"
    "ds_read_b128 v[84:87], v1 offset:8192\n"
    "s_waitcnt lgkmcnt(4)\n"
    "v_mfma_f32_16x16x32_bf16 v[40:43], a[24:27], v[88:91], v[40:43]\n"
    "v_mfma_f32_16x16x32_bf16 v[48:51], a[104:107], v[88:91], v[48:51]\n"
    "v_mfma_f32_16x16x32_bf16 v[56:59], a[184:187], v[88:91], v[56:59]\n"
    "ds_read_b128 v[88:91], v1 offset:28672\n"
    "s_waitcnt lgkmcnt(4)\n"
    "v_mfma_f32_16x16x32_bf16 v[44:47], a[24:27], v[92:95], v[44:47]\n"
    "v_mfma_f32_16x16x32_bf16 v[52:55], a[104:107], v[92:95], v[52:55]\n"
    "v_mfma_f32_16x16x32_bf16 v[60:63], a[184:187], v[92:95], v[60:63]\n"
    "ds_read_b128 v[92:95], v1 offset:9216\n"
    "s_waitcnt lgkmcnt(4)\n"
    "v_mfma_f32_16x16x32_bf16 v[40:43], a[28:31], v[96:99], v[40:43]\n"
    "v_mfma_f32_16x16x32_bf16 v[48:51], a[108:111], v[96:99], v[48:51]\n"
    "v_mfma_f32_16x16x32_bf16 v[56:59], a[188:191], v[96:99], v[56:59]\n"
    "ds_read_b128 v[96:99], v1 offset:29696\n"
    "s_waitcnt lgkmcnt(4)\n"
    "v_mfma_f32_16x16x32_bf16 v[44:47], a[28:31], v[80:83], v[44:47]\n"
    "v_mfma_f32_16x16x32_bf16 v[52:55], a[108:111], v[80:83], v[52:55]\n"
    "v_mfma_f32_16x16x32_bf16 v[60:63], a[188:191], v[80:83], v[60:63]\n"
    "ds_read_b128 v[80:83], v1 offset:10240\n"
    "s_waitcnt lgkmcnt(4)\n"
    "v_mfma_f32_16x16x32_bf16 v[40:43], a[32:35], v[84:87], v[40:43]\n"
    "v_mfma_f32_16x16x32_bf16 v[48:51], a[112:115], v[84:87], v[48:51]\n"
    "v_mfma_f32_16x16x32_bf16 v[56:59], a[192:195], v[84:87], v[56:59]\n"
    "ds_read_b128 v[84:87], v1 offset:30720\n"
    "s_waitcnt lgkmcnt(4)\n"
    "v_mfma_f32_16x16x32_bf16 v[44:47], a[32:35], v[88:91], v[44:47]\n"
    "v_mfma_f32_16x16x32_bf16 v[52:55], a[112:115], v[88:91], v[52:55]\n"
    "v_mfma_f32_16x16x32_bf16 v[60:63], a[192:195], v[88:91], v[60:63]\n"
    "ds_read_b128 v[88:91], v1 offset:11264\n"
    "s_waitcnt lgkmcnt(4)\n"
    "v_mfma_f32_16x16x32_bf16 v[40:43], a[36:39], v[92:95], v[40:43]\n"
    "v_mfma_f32_16x16x32_bf16 v[48:51], a[116:119], v[92:95], v[48:51]\n"
    "v_mfma_f32_16x16x32_bf16 v[56:59], a[196:199], v[92:95], v[56:59]\n"
    "ds_read_b128 v[92:95], v1 offset:31744\n"
    "s_waitcnt lgkmcnt(4)\n"
    "v_mfma_f32_16x16x32_bf16 v[44:47], a[36:39], v[96:99], v[44:47]\n"
    "v_mfma_f32_16x16x32_bf16 v[52:55], a[116:119], v[96:99], v[52:55]\n"
    "v_mfma_f32_16x16x32_bf16 v[60:63], a[196:199], v[96:99], v[60:63]\n"
    "ds_read_b128 v[96:99], v1 offset:12288\n"
    "s_waitcnt lgkmcnt(4)\n"
    "v_mfma_f32_16x16x32_bf16 v[40:43], a[40:43], v[80:83], v[40:43]\n"
    "v_mfma_f32_16x16x32_bf16 v[48:51], a[120:123], v[80:83], v[48:51]\n"
    "v_mfma_f32_16x16x32_bf16 v[56:59], a[200:203], v[80:83], v[56:59]\n"
    "ds_read_b128 v[80:83], v1 offset:32768\n"
    "s_waitcnt lgkmcnt(4)\n"
    "v_mfma_f32_16x16x32_bf16 v[44:47], a[40:43], v[84:87], v[44:47]\n"
    "v_mfma_f32_16x16x32_bf16 v[52:55], a[120:123], v[84:87], v[52:55]\n"
    "v_mfma_f32_16x16x32_bf16 v[60:63], a[200:203], v[84:87], v[60:63]\n"
    "ds_read_b128 v[84:87], v1 offset:13312\n"
    "s_waitcnt lgkmcnt(4)\n"
    "v_mfma_f32_16x16x32_bf16 v[40:43], a[44:47], v[88:91], v[40:43]\n"
    "v_mfma_f32_16x16x32_bf16 v[48:51], a[124:127], v[88:91], v[48:51]\n"
    "v_mfma_f32_16x16x32_bf16 v[56:59], a[204:207], v[88:91], v[56:59]\n"
    "ds_read_b128 v[88:91], v1 offset:33792\n"
    "s_waitcnt lgkmcnt(4)\n"
    "v_mfma_f32_16x16x32_bf16 v[44:47], a[44:47], v[92:95], v[44:47]\n"
    "v_mfma_f32_16x16x32_bf16 v[52:55], a[124:127], v[92:95], v[52:55]\n"
    "v_mfma_f32_16x16x32_bf16 v[60:63], a[204:207], v[92:95], v[60:63]\n"
    "ds_read_b128 v[92:95], v1 offset:14336\n"
    "s_waitcnt lgkmcnt(4)\n"
    "v_mfma_f32_16x16x32_bf16 v[40:43], a[48:51], v[96:99], v[40:43]\n"
    "v_mfma_f32_16x16x32_bf16 v[48:51], a[128:131], v[96:99], v[48:51]\n"
    "v_mfma_f32_16x16x32_bf16 v[56:59], a[208:211], v[96:99], v[56:59]\n"
    "ds_read_b128 v[96:99], v1 offset:34816\n"
    "s_waitcnt lgkmcnt(4)\n"
    "v_mfma_f32_16x16x32_bf16 v[44:47], a[48:51], v[80:83], v[44:47]\n"
    "v_mfma_f32_16x16x32_bf16 v[52:55], a[128:131], v[80:83], v[52:55]\n"
    "v_mfma_f32_16x16x32_bf16 v[60:63], a[208:211], v[80:83], v[60:63]\n"
    "ds_read_b128 v[80:83], v1 offset:15360\n"
    "s_waitcnt lgkmcnt(4)\n"
    "v_mfma_f32_16x16x32_bf16 v[40:43], a[52:55], v[84:87], v[40:43]\n"
    "v_mfma_f32_16x16x32_bf16 v[48:51], a[132:135], v[84:87], v[48:51]\n"
    "v_mfma_f32_16x16x32_bf16 v[56:59], a[212:215], v[84:87], v[56:59]\n"
    "ds_read_b128 v[84:87], v1 offset:35840\n"
    "s_waitcnt lgkmcnt(4)\n"
    "v_mfma_f32_16x16x32_bf16 v[44:47], a[52:55], v[88:91], v[44:47]\n"
    "v_mfma_f32_16x16x32_bf16 v[52:55], a[132:135], v[88:91], v[52:55]\n"
    "v_mfma_f32_16x16x32_bf16 v[60:63], a[212:215], v[88:91], v[60:63]\n"
    "ds_read_b128 v[88:91], v1 offset:16384\n"
    "s_waitcnt lgkmcnt(4)\n"
    "v_mfma_f32_16x16x32_bf16 v[40:43], a[56:59], v[92:95], v[40:43]\n"
    "v_mfma_f32_16x16x32_bf16 v[48:51], a[136:139], v[92:95], v[48:51]\n"
    "v_mfma_f32_16x16x32_bf16 v[56:59], a[216:219], v[92:95], v[56:59]\n"
    "ds_read_b128 v[92:95], v1 offset:36864\n"
    "s_waitcnt lgkmcnt(4)\n"
    "v_mfma_f32_16x16x32_bf16 v[44:47], a[56:59], v[96:99], v[44:47]\n"
    "v_mfma_f32_16x16x32_bf16 v[52:55], a[136:139], v[96:99], v[52:55]\n"
    "v_mfma_f32_16x16x32_bf16 v[60:63], a[216:219], v[96:99], v[60:63]\n"
    "s_waitcnt vmcnt(0)\n"
    "s_barrier\n"
    "v_mov_b32 v4, s47\n"
    "buffer_store_dword v4, v3, s[40:43], s58 offen sc1\n"
    "ds_read_b128 v[96:99], v1 offset:17408\n"
    "s_waitcnt lgkmcnt(4)\n"
    "v_mfma_f32_16x16x32_bf16 v[40:43], a[60:63], v[80:83], v[40:43]\n"
    "s_add_u32 m0, s61, 0\n"
    "s_add_u32 s51, s50, 0\n"
    "buffer_load_dwordx4 v0, s[40:43], s51 offen offset:0 lds\n"
    "v_mfma_f32_16x16x32_bf16 v[48:51], a[140:143], v[80:83], v[48:51]\n"
    "v_mfma_f32_16x16x32_bf16 v[56:59], a[220:223], v[80:83], v[56:59]\n"
    "ds_read_b128 v[80:83], v1 offset:37888\n"
    "s_waitcnt lgkmcnt(4)\n"
    "v_mfma_f32_16x16x32_bf16 v[44:47], a[60:63], v[84:87], v[44:47]\n"
    "buffer_load_dwordx4 v0, s[40:43], s51 offen offset:1024 lds\n"
    "v_mfma_f32_16x16x32_bf16 v[52:55], a[140:143], v[84:87], v[52:55]\n"
    "v_mfma_f32_16x16x32_bf16 v[60:63], a[220:223], v[84:87], v[60:63]\n"
    "ds_read_b128 v[84:87], v1 offset:18432\n"
    "s_waitcnt lgkmcnt(4)\n"
    "v_mfma_f32_16x16x32_bf16 v[40:43], a[64:67], v[88:91], v[40:43]\n"
    "buffer_load_dwordx4 v0, s[40:43], s51 offen offset:2048 lds\n"
    "v_mfma_f32_16x16x32_bf16 v[48:51], a[144:147], v[88:91], v[48:51]\n"
    "v_mfma_f32_16x16x32_bf16 v[56:59], a[224:227], v[88:91], v[56:59]\n"
    "ds_read_b128 v[88:91], v1 offset:38912\n"
    "s_waitcnt lgkmcnt(4)\n"
    "v_mfma_f32_16x16x32_bf16 v[44:47], a[64:67], v[92:95], v[44:47]\n"
    "buffer_load_dwordx4 v0, s[40:43], s51 offen offset:3072 lds\n"
    "v_mfma_f32_16x16x32_bf16 v[52:55], a[144:147], v[92:95], v[52:55]\n"
    "v_mfma_f32_16x16x32_bf16 v[60:63], a[224:227], v[92:95], v[60:63]\n"
    "ds_read_b128 v[92:95], v1 offset:19456\n"
    "s_waitcnt lgkmcnt(4)\n"
    "v_mfma_f32_16x16x32_bf16 v[40:43], a[68:71], v[96:99], v[40:43]\n"
    "s_add_u32 m0, s61, 4096\n"
    "s_add_u32 s51, s50, 4096\n"
    "buffer_load_dwordx4 v0, s[40:43], s51 offen offset:0 lds\n"
    "v_mfma_f32_16x16x32_bf16 v[48:51], a[148:151], v[96:99], v[48:51]\n"
    "v_mfma_f32_16x16x32_bf16 v[56:59], a[228:231], v[96:99], v[56:59]\n"
    "ds_read_b128 v[96:99], v1 offset:39936\n"
    "s_waitcnt lgkmcnt(4)\n"
    "v_mfma_f32_16x16x32_bf16 v[44:47], a[68:71], v[80:83], v[44:47]\n"
    "buffer_load_dwordx4 v0, s[40:43], s51 offen offset:1024 lds\n"
    "v_mfma_f32_16x16x32_bf16 v[52:55], a[148:151], v[80:83], v[52:55]\n"
    "v_mfma_f32_16x16x32_bf16 v[60:63], a[228:231], v[80:83], v[60:63]\n"
    "s_add_u32 s64, s64, 40960\n"
    "s_cmp_ge_u32 s64, 122880\n"
    "s_cselect_b32 s64, 0, s64\n"
    "v_add_u32 v1, s64, v0\n"
    "ds_read_b128 v[80:83], v1 offset:0\n"
    "s_waitcnt lgkmcnt(4)\n"
    "v_mfma_f32_16x16x32_bf16 v[40:43], a[72:75], v[84:87], v[40:43]\n"
    "buffer_load_dwordx4 v0, s[40:43], s51 offen offset:2048 lds\n"
    "v_mfma_f32_16x16x32_bf16 v[48:51], a[152:155], v[84:87], v[48:51]\n"
    "v_mfma_f32_16x16x32_bf16 v[56:59], a[232:235], v[84:87], v[56:59]\n"
    "ds_read_b128 v[84:87], v1 offset:20480\n"
    "s_waitcnt lgkmcnt(4)\n"
    "v_mfma_f32_16x16x32_bf16 v[44:47], a[72:75], v[88:91], v[44:47]\n"
    "buffer_load_dwordx4 v0, s[40:43], s51 offen offset:3072 lds\n"
    "v_mfma_f32_16x16x32_bf16 v[52:55], a[152:155], v[88:91], v[52:55]\n"
    "v_mfma_f32_16x16x32_bf16 v[60:63], a[232:235], v[88:91], v[60:63]\n"
    "ds_read_b128 v[88:91], v1 offset:1024\n"
    "s_waitcnt lgkmcnt(4)\n"
    "v_mfma_f32_16x16x32_bf16 v[40:43], a[76:79], v[92:95], v[40:43]\n"
    "s_add_u32 m0, s61, 8192\n"
    "s_add_u32 s51, s50, 8192\n"
    "buffer_load_dwordx4 v0, s[40:43], s51 offen offset:0 lds\n"
    "v_mfma_f32_16x16x32_bf16 v[48:51], a[156:159], v[92:95], v[48:51]\n"
    "v_mfma_f32_16x16x32_bf16 v[56:59], a[236:239], v[92:95], v[56:59]\n"
    "ds_read_b128 v[92:95], v1 offset:21504\n"
    "s_waitcnt lgkmcnt(4)\n"
    "v_mfma_f32_16x16x32_bf16 v[44:47], a[76:79], v[96:99], v[44:47]\n"
    "buffer_load_dwordx4 v0, s[40:43], s51 offen offset:1024 lds\n"
    "s_add_u32 s50, s50, 40960\n"
    "s_add_u32 s61, s61, 40960\n"
    "s_cmp_ge_u32 s61, 122880\n"
    "s_cselect_b32 s51, 122880, 0\n"
    "s_sub_u32 s61, s61, s51\n"
    "v_mfma_f32_16x16x32_bf16 v[52:55], a[156:159], v[96:99], v[52:55]\n"
    "v_mfma_f32_16x16x32_bf16 v[60:63], a[236:239], v[96:99], v[60:63]\n"
    "ds_read_b128 v[96:99], v1 offset:2048\n"
    "s_mov_b32 s65, s63\n"
    "s_add_u32 s63, s63, 393216\n"
    "s_add_u32 s47, s47, 1\n"
    "s_cmp_lt_u32 s47, 64\n"
    "s_cbranch_scc1 pk_loop_%=\n"
    "s_waitcnt lgkmcnt(0)\n"
    "v_cvt_pk_bf16_f32 v104, v40, v41\n"
    "v_cvt_pk_bf16_f32 v105, v42, v43\n"
    "v_cvt_pk_bf16_f32 v106, v44, v45\n"
    "v_cvt_pk_bf16_f32 v107, v46, v47\n"
    "buffer_store_dwordx4 v[104:107], v0, s[40:43], s65 offen offset:0 sc1\n"
    "v_cvt_pk_bf16_f32 v108, v48, v49\n"
    "v_cvt_pk_bf16_f32 v109, v50, v51\n"
    "v_cvt_pk_bf16_f32 v110, v52, v53\n"
    "v_cvt_pk_bf16_f32 v111, v54, v55\n"
    "buffer_store_dwordx4 v[108:111], v0, s[40:43], s65 offen offset:1024 sc1\n"
    "v_cvt_pk_bf16_f32 v112, v56, v57\n"
    "v_cvt_pk_bf16_f32 v113, v58, v59\n"
    "v_cvt_pk_bf16_f32 v114, v60, v61\n"
    "v_cvt_pk_bf16_f32 v115, v62, v63\n"
    "buffer_store_dwordx4 v[112:115], v0, s[40:43], s65 offen offset:2048 sc1\n"
    "s_waitcnt vmcnt(0)\n"
    "s_waitcnt lgkmcnt(0)\n"
    "s_barrier\n"
    "v_mov_b32 v4, 64\n"
    "buffer_store_dword v4, v3, s[40:43], s58 offen sc1\n"
    "s_nop 1\n"
    "s_branch pk_end_%=\n"
    "pk_abort_%=:\n"
    "v_mov_b32 v4, 1\n"
    "v_mov_b32 v5, 1024\n"
    "buffer_store_dword v4, v5, s[40:43], s58 offen sc1\n"
    "pk_end_%=:\n"
    "s_endpgm\n"
    :: ROLE_INPUTS : ROLE_CLOBBERS);
}
DEV void role_out(unsigned lo, unsigned hi, int id, int w, int aux, const Tune& tn) {
    asm volatile(
    "s_mov_b32 s40, %0\n"
    "s_mov_b32 s41, %1\n"
    "s_mov_b32 s42, %2\n"
    "s_mov_b32 s43, %3\n"
    "s_mov_b32 s44, %4\n"
    "s_mov_b32 s45, %5\n"
    "s_mov_b32 s46, %6\n"
    "s_mov_b32 s66, %7\n"
    "s_mov_b32 s67, %8\n"
    "s_mov_b32 s68, %9\n"
    "s_mov_b32 s69, %10\n"
    "s_mov_b32 s70, %11\n"
    "s_mov_b32 s71, %12\n"
    "s_mov_b32 s72, %13\n"
    "s_mov_b32 s73, %14\n"
    "s_mov_b64 exec, -1\n"
    "v_mbcnt_lo_u32_b32 v15, -1, 0\n"
    "v_mbcnt_hi_u32_b32 v15, -1, v15\n"
    "v_lshlrev_b32 v0, 4, v15\n"
    "s_mov_b32 s58, 2097152\n"
    "s_mov_b32 s92, 0\n"
    "s_mul_i32 s48, s44, 40\n"
    "s_mul_i32 s49, s45, 10\n"
    "s_add_u32 s48, s48, s49\n"
    "s_mul_i32 s48, s48, 6144\n"
    "s_add_u32 s48, s48, 25755648\n"
    "buffer_load_dwordx4 a[0:3], v0, s[40:43], s48 offen\n"
    "buffer_load_dwordx4 a[4:7], v0, s[40:43], s48 offen offset:1024\n"
    "buffer_load_dwordx4 a[8:11], v0, s[40:43], s48 offen offset:2048\n"
    "buffer_load_dwordx4 a[12:15], v0, s[40:43], s48 offen offset:3072\n"
    "s_add_u32 s48, s48, 4096\n"
    "buffer_load_dwordx4 a[16:19], v0, s[40:43], s48 offen\n"
    "buffer_load_dwordx4 a[20:23], v0, s[40:43], s48 offen offset:1024\n"
    "buffer_load_dwordx4 a[24:27], v0, s[40:43], s48 offen offset:2048\n"
    "buffer_load_dwordx4 a[28:31], v0, s[40:43], s48 offen offset:3072\n"
    "s_add_u32 s48, s48, 4096\n"
    "buffer_load_dwordx4 a[32:35], v0, s[40:43], s48 offen\n"
    "buffer_load_dwordx4 a[36:39], v0, s[40:43], s48 offen offset:1024\n"
    "buffer_load_dwordx4 a[40:43], v0, s[40:43], s48 offen offset:2048\n"
    "buffer_load_dwordx4 a[44:47], v0, s[40:43], s48 offen offset:3072\n"
    "s_add_u32 s48, s48, 4096\n"
    "buffer_load_dwordx4 a[48:51], v0, s[40:43], s48 offen\n"
    "buffer_load_dwordx4 a[52:55], v0, s[40:43], s48 offen offset:1024\n"
    "buffer_load_dwordx4 a[56:59], v0, s[40:43], s48 offen offset:2048\n"
    "buffer_load_dwordx4 a[60:63], v0, s[40:43], s48 offen offset:3072\n"
    "s_add_u32 s48, s48, 4096\n"
    "buffer_load_dwordx4 a[64:67], v0, s[40:43], s48 offen\n"
    "buffer_load_dwordx4 a[68:71], v0, s[40:43], s48 offen offset:1024\n"
    "buffer_load_dwordx4 a[72:75], v0, s[40:43], s48 offen offset:2048\n"
    "buffer_load_dwordx4 a[76:79], v0, s[40:43], s48 offen offset:3072\n"
    "s_add_u32 s48, s48, 4096\n"
    "buffer_load_dwordx4 a[80:83], v0, s[40:43], s48 offen\n"
    "buffer_load_dwordx4 a[84:87], v0, s[40:43], s48 offen offset:1024\n"
    "buffer_load_dwordx4 a[88:91], v0, s[40:43], s48 offen offset:2048\n"
    "buffer_load_dwordx4 a[92:95], v0, s[40:43], s48 offen offset:3072\n"
    "s_add_u32 s48, s48, 4096\n"
    "buffer_load_dwordx4 a[96:99], v0, s[40:43], s48 offen\n"
    "buffer_load_dwordx4 a[100:103], v0, s[40:43], s48 offen offset:1024\n"
    "buffer_load_dwordx4 a[104:107], v0, s[40:43], s48 offen offset:2048\n"
    "buffer_load_dwordx4 a[108:111], v0, s[40:43], s48 offen offset:3072\n"
    "s_add_u32 s48, s48, 4096\n"
    "buffer_load_dwordx4 a[112:115], v0, s[40:43], s48 offen\n"
    "buffer_load_dwordx4 a[116:119], v0, s[40:43], s48 offen offset:1024\n"
    "buffer_load_dwordx4 a[120:123], v0, s[40:43], s48 offen offset:2048\n"
    "buffer_load_dwordx4 a[124:127], v0, s[40:43], s48 offen offset:3072\n"
    "s_add_u32 s48, s48, 4096\n"
    "buffer_load_dwordx4 a[128:131], v0, s[40:43], s48 offen\n"
    "buffer_load_dwordx4 a[132:135], v0, s[40:43], s48 offen offset:1024\n"
    "buffer_load_dwordx4 a[136:139], v0, s[40:43], s48 offen offset:2048\n"
    "buffer_load_dwordx4 a[140:143], v0, s[40:43], s48 offen offset:3072\n"
    "s_add_u32 s48, s48, 4096\n"
    "buffer_load_dwordx4 a[144:147], v0, s[40:43], s48 offen\n"
    "buffer_load_dwordx4 a[148:151], v0, s[40:43], s48 offen offset:1024\n"
    "buffer_load_dwordx4 a[152:155], v0, s[40:43], s48 offen offset:2048\n"
    "buffer_load_dwordx4 a[156:159], v0, s[40:43], s48 offen offset:3072\n"
    "s_add_u32 s48, s48, 4096\n"
    "buffer_load_dwordx4 a[160:163], v0, s[40:43], s48 offen\n"
    "buffer_load_dwordx4 a[164:167], v0, s[40:43], s48 offen offset:1024\n"
    "buffer_load_dwordx4 a[168:171], v0, s[40:43], s48 offen offset:2048\n"
    "buffer_load_dwordx4 a[172:175], v0, s[40:43], s48 offen offset:3072\n"
    "s_add_u32 s48, s48, 4096\n"
    "buffer_load_dwordx4 a[176:179], v0, s[40:43], s48 offen\n"
    "buffer_load_dwordx4 a[180:183], v0, s[40:43], s48 offen offset:1024\n"
    "buffer_load_dwordx4 a[184:187], v0, s[40:43], s48 offen offset:2048\n"
    "buffer_load_dwordx4 a[188:191], v0, s[40:43], s48 offen offset:3072\n"
    "s_add_u32 s48, s48, 4096\n"
    "buffer_load_dwordx4 a[192:195], v0, s[40:43], s48 offen\n"
    "buffer_load_dwordx4 a[196:199], v0, s[40:43], s48 offen offset:1024\n"
    "buffer_load_dwordx4 a[200:203], v0, s[40:43], s48 offen offset:2048\n"
    "buffer_load_dwordx4 a[204:207], v0, s[40:43], s48 offen offset:3072\n"
    "s_add_u32 s48, s48, 4096\n"
    "buffer_load_dwordx4 a[208:211], v0, s[40:43], s48 offen\n"
    "buffer_load_dwordx4 a[212:215], v0, s[40:43], s48 offen offset:1024\n"
    "buffer_load_dwordx4 a[216:219], v0, s[40:43], s48 offen offset:2048\n"
    "buffer_load_dwordx4 a[220:223], v0, s[40:43], s48 offen offset:3072\n"
    "s_add_u32 s48, s48, 4096\n"
    "buffer_load_dwordx4 a[224:227], v0, s[40:43], s48 offen\n"
    "buffer_load_dwordx4 a[228:231], v0, s[40:43], s48 offen offset:1024\n"
    "buffer_load_dwordx4 a[232:235], v0, s[40:43], s48 offen offset:2048\n"
    "buffer_load_dwordx4 a[236:239], v0, s[40:43], s48 offen offset:3072\n"
    "v_and_b32 v1, 0x370, v0\n"
    "v_bfe_u32 v4, v15, 3, 1\n"
    "v_lshl_add_u32 v1, v4, 14, v1\n"
    "s_lshr_b32 s48, s44, 1\n"
    "s_lshl_b32 s48, s48, 10\n"
    "s_and_b32 s49, s44, 1\n"
    "s_lshl_b32 s49, s49, 7\n"
    "s_add_u32 s48, s48, s49\n"
    "v_add_u32 v1, s48, v1\n"
    "s_mul_i32 s63, s45, 327680\n"
    "s_add_u32 s63, s63, 112787456\n"
    "s_mul_i32 s48, s45, 3\n"
    "s_add_u32 s48, s48, 0\n"
    "s_and_b32 s49, s48, 1\n"
    "s_lshr_b32 s48, s48, 1\n"
    "s_lshl_b32 s48, s48, 11\n"
    "s_lshl3_add_u32 s48, s49, s48\n"
    "v_add_u32 v8, s48, v0\n"
    "s_mul_i32 s48, s45, 3\n"
    "s_add_u32 s48, s48, 1\n"
    "s_and_b32 s49, s48, 1\n"
    "s_lshr_b32 s48, s48, 1\n"
    "s_lshl_b32 s48, s48, 11\n"
    "s_lshl3_add_u32 s48, s49, s48\n"
    "v_add_u32 v9, s48, v0\n"
    "s_mul_i32 s48, s45, 3\n"
    "s_add_u32 s48, s48, 2\n"
    "s_and_b32 s49, s48, 1\n"
    "s_lshr_b32 s48, s48, 1\n"
    "s_lshl_b32 s48, s48, 11\n"
    "s_lshl3_add_u32 s48, s49, s48\n"
    "v_add_u32 v10, s48, v0\n"
    "s_mul_i32 s49, s45, 3072\n"
    "s_mul_i32 s50, s44, 12288\n"
    "s_add_u32 s50, s50, 87621632\n"
    "s_add_u32 s50, s50, s49\n"
    "s_lshl_b32 s48, s44, 7\n"
    "s_add_u32 s49, s48, 6144\n"
    "v_mov_b32 v7, s49\n"
    "v_mov_b32 v6, 0\n"
    "s_add_u32 s49, s48, 10240\n"
    "v_mov_b32 v2, s49\n"
    "v_or_b32 v4, s45, v15\n"
    "v_cmp_eq_u32 vcc, 0, v4\n"
    "v_mov_b32 v4, 2147483632\n"
    "s_nop 0\n"
    "v_cndmask_b32 v2, v4, v2, vcc\n"
    "s_mov_b32 s47, 0\n"
    "s_waitcnt vmcnt(0)\n"
    "s_mov_b32 s55, 1\n"
    "s_mov_b32 s64, 0\n"
    "s_mov_b32 s60, 0\n"
    "v_cmp_gt_u32 vcc, s55, v6\n"
    "s_nop 4\n"
    "s_cbranch_vccz po_v0_ok_%=\n"
    "po_v0_poll_%=:\n"
    "buffer_load_dword v6, v7, s[40:43], s58 offen sc1\n"
    "s_waitcnt vmcnt(0)\n"
    "v_cmp_gt_u32 vcc, s55, v6\n"
    "s_nop 4\n"
    "s_cbranch_vccz po_v0_ok_%=\n"
    "s_sleep 2\n"
    "s_add_u32 s60, s60, 1\n"
    "s_cmp_gt_u32 s60, 65536\n"
    "s_cbranch_scc1 po_abort_%=\n"
    "s_and_b32 s59, s60, 63\n"
    "s_cbranch_scc1 po_v0_poll_%=\n"
    "v_mov_b32 v4, 1024\n"
    "buffer_load_dword v4, v4, s[40:43], s58 offen sc1\n"
    "s_waitcnt vmcnt(0)\n"
    "v_readfirstlane_b32 s59, v4\n"
    "s_nop 0\n"
    "s_cmp_lg_u32 s59, 0\n"
    "s_nop 2\n"
    "s_cbranch_scc1 po_end_%=\n"
    "s_branch po_v0_poll_%=\n"
    "po_v0_ok_%=:\n"
    "buffer_load_dwordx4 v[144:147], v0, s[40:43], s50 offen offset:0 sc1\n"
    "buffer_load_dwordx4 v[148:151], v0, s[40:43], s50 offen offset:1024 sc1\n"
    "buffer_load_dwordx4 v[152:155], v0, s[40:43], s50 offen offset:2048 sc1\n"
    "s_waitcnt vmcnt(2)\n"
    "ds_write2st64_b64 v8, v[144:145], v[146:147] offset0:0 offset1:2\n"
    "s_waitcnt vmcnt(1)\n"
    "ds_write2st64_b64 v9, v[148:149], v[150:151] offset0:0 offset1:2\n"
    "s_waitcnt vmcnt(0)\n"
    "ds_write2st64_b64 v10, v[152:153], v[154:155] offset0:0 offset1:2\n"
    "s_waitcnt lgkmcnt(0)\n"
    "s_barrier\n"
    "ds_read_b128 v[16:19], v0 offset:0\n"
    "ds_read_b128 v[20:23], v0 offset:1024\n"
    "ds_read_b128 v[24:27], v0 offset:2048\n"
    "ds_read_b128 v[28:31], v0 offset:3072\n"
    "ds_read_b128 v[32:35], v0 offset:4096\n"
    "ds_read_b128 v[36:39], v0 offset:5120\n"
    "ds_read_b128 v[40:43], v0 offset:6144\n"
    "ds_read_b128 v[44:47], v0 offset:7168\n"
    "ds_read_b128 v[48:51], v0 offset:8192\n"
    "ds_read_b128 v[52:55], v0 offset:9216\n"
    "ds_read_b128 v[56:59], v0 offset:10240\n"
    "ds_read_b128 v[60:63], v0 offset:11264\n"
    "s_waitcnt lgkmcnt(0)\n"
    "s_mov_b32 s53, 0x40000000\n"
    "po_loop_%=:\n"
    "s_add_u32 s55, s47, 2\n"
    "s_min_u32 s55, s55, 64\n"
    "s_add_u32 s50, s50, 393216\n"
    "v_cmp_gt_u32 vcc, s55, v6\n"
    "s_nop 0\n"
    "s_cmp_eq_u64 vcc, 0\n"
    "s_cselect_b32 s56, s50, 0x40000000\n"
    "v_mfma_f32_16x16x32_bf16 v[112:115], v[16:19], a[0:3], 0\n"
    "buffer_load_dwordx4 v[144:147], v0, s[40:43], s56 offen offset:0 sc1\n"
    "v_mfma_f32_16x16x32_bf16 v[116:119], v[20:23], a[0:3], 0\n"
    "buffer_load_dwordx4 v[148:151], v0, s[40:43], s56 offen offset:1024 sc1\n"
    "v_mfma_f32_16x16x32_bf16 v[112:115], v[24:27], a[4:7], v[112:115]\n"
    "buffer_load_dwordx4 v[152:155], v0, s[40:43], s56 offen offset:2048 sc1\n"
    "v_mfma_f32_16x16x32_bf16 v[116:119], v[28:31], a[4:7], v[116:119]\n"
    "buffer_load_dword v6, v7, s[40:43], s58 offen sc1\n"
    "v_mfma_f32_16x16x32_bf16 v[112:115], v[32:35], a[8:11], v[112:115]\n"
    "v_mfma_f32_16x16x32_bf16 v[116:119], v[36:39], a[8:11], v[116:119]\n"
    "v_mfma_f32_16x16x32_bf16 v[112:115], v[40:43], a[12:15], v[112:115]\n"
    "v_cvt_pk_bf16_f32 v140, v120, v121\n"
    "v_mfma_f32_16x16x32_bf16 v[116:119], v[44:47], a[12:15], v[116:119]\n"
    "v_cvt_pk_bf16_f32 v141, v122, v123\n"
    "v_mfma_f32_16x16x32_bf16 v[112:115], v[48:51], a[16:19], v[112:115]\n"
    "v_cvt_pk_bf16_f32 v142, v124, v125\n"
    "v_mfma_f32_16x16x32_bf16 v[116:119], v[52:55], a[16:19], v[116:119]\n"
    "v_cvt_pk_bf16_f32 v143, v126, v127\n"
    "v_mfma_f32_16x16x32_bf16 v[112:115], v[56:59], a[20:23], v[112:115]\n"
    "buffer_store_dwordx4 v[140:143], v1, s[40:43], s53 offen sc1\n"
    "v_mfma_f32_16x16x32_bf16 v[116:119], v[60:63], a[20:23], v[116:119]\n"
    "s_mov_b32 s53, s63\n"
    "v_mfma_f32_16x16x32_bf16 v[120:123], v[16:19], a[24:27], 0\n"
    "v_mfma_f32_16x16x32_bf16 v[124:127], v[20:23], a[24:27], 0\n"
    "v_mfma_f32_16x16x32_bf16 v[120:123], v[24:27], a[28:31], v[120:123]\n"
    "v_mfma_f32_16x16x32_bf16 v[124:127], v[28:31], a[28:31], v[124:127]\n"
    "v_mfma_f32_16x16x32_bf16 v[120:123], v[32:35], a[32:35], v[120:123]\n"
    "v_mfma_f32_16x16x32_bf16 v[124:127], v[36:39], a[32:35], v[124:127]\n"
    "v_mfma_f32_16x16x32_bf16 v[120:123], v[40:43], a[36:39], v[120:123]\n"
    "v_cvt_pk_bf16_f32 v136, v112, v113\n"
    "v_mfma_f32_16x16x32_bf16 v[124:127], v[44:47], a[36:39], v[124:127]\n"
    "v_cvt_pk_bf16_f32 v137, v114, v115\n"
    "v_mfma_f32_16x16x32_bf16 v[120:123], v[48:51], a[40:43], v[120:123]\n"
    "v_cvt_pk_bf16_f32 v138, v116, v117\n"
    "v_mfma_f32_16x16x32_bf16 v[124:127], v[52:55], a[40:43], v[124:127]\n"
    "v_cvt_pk_bf16_f32 v139, v118, v119\n"
    "v_mfma_f32_16x16x32_bf16 v[120:123], v[56:59], a[44:47], v[120:123]\n"
    "buffer_store_dwordx4 v[136:139], v1, s[40:43], s53 offen sc1\n"
    "v_mfma_f32_16x16x32_bf16 v[124:127], v[60:63], a[44:47], v[124:127]\n"
    "s_add_u32 s53, s53, 32768\n"
    "v_mfma_f32_16x16x32_bf16 v[112:115], v[16:19], a[48:51], 0\n"
    "v_mfma_f32_16x16x32_bf16 v[116:119], v[20:23], a[48:51], 0\n"
    "v_mfma_f32_16x16x32_bf16 v[112:115], v[24:27], a[52:55], v[112:115]\n"
    "v_mfma_f32_16x16x32_bf16 v[116:119], v[28:31], a[52:55], v[116:119]\n"
    "v_mfma_f32_16x16x32_bf16 v[112:115], v[32:35], a[56:59], v[112:115]\n"
    "v_mfma_f32_16x16x32_bf16 v[116:119], v[36:39], a[56:59], v[116:119]\n"
    "v_mfma_f32_16x16x32_bf16 v[112:115], v[40:43], a[60:63], v[112:115]\n"
    "v_cvt_pk_bf16_f32 v140, v120, v121\n"
    "v_mfma_f32_16x16x32_bf16 v[116:119], v[44:47], a[60:63], v[116:119]\n"
    "v_cvt_pk_bf16_f32 v141, v122, v123\n"
    "v_mfma_f32_16x16x32_bf16 v[112:115], v[48:51], a[64:67], v[112:115]\n"
    "v_cvt_pk_bf16_f32 v142, v124, v125\n"
    "v_mfma_f32_16x16x32_bf16 v[116:119], v[52:55], a[64:67], v[116:119]\n"
    "v_cvt_pk_bf16_f32 v143, v126, v127\n"
    "v_mfma_f32_16x16x32_bf16 v[112:115], v[56:59], a[68:71], v[112:115]\n"
    "buffer_store_dwordx4 v[140:143], v1, s[40:43], s53 offen sc1\n"
    "v_mfma_f32_16x16x32_bf16 v[116:119], v[60:63], a[68:71], v[116:119]\n"
    "s_add_u32 s53, s53, 32768\n"
    "v_mfma_f32_16x16x32_bf16 v[120:123], v[16:19], a[72:75], 0\n"
    "v_mfma_f32_16x16x32_bf16 v[124:127], v[20:23], a[72:75], 0\n"
    "v_mfma_f32_16x16x32_bf16 v[120:123], v[24:27], a[76:79], v[120:123]\n"
    "v_mfma_f32_16x16x32_bf16 v[124:127], v[28:31], a[76:79], v[124:127]\n"
    "v_mfma_f32_16x16x32_bf16 v[120:123], v[32:35], a[80:83], v[120:123]\n"
    "v_mfma_f32_16x16x32_bf16 v[124:127], v[36:39], a[80:83], v[124:127]\n"
    "v_mfma_f32_16x16x32_bf16 v[120:123], v[40:43], a[84:87], v[120:123]\n"
    "v_cvt_pk_bf16_f32 v136, v112, v113\n"
    "v_mfma_f32_16x16x32_bf16 v[124:127], v[44:47], a[84:87], v[124:127]\n"
    "v_cvt_pk_bf16_f32 v137, v114, v115\n"
    "v_mfma_f32_16x16x32_bf16 v[120:123], v[48:51], a[88:91], v[120:123]\n"
    "v_cvt_pk_bf16_f32 v138, v116, v117\n"
    "v_mfma_f32_16x16x32_bf16 v[124:127], v[52:55], a[88:91], v[124:127]\n"
    "v_cvt_pk_bf16_f32 v139, v118, v119\n"
    "v_mfma_f32_16x16x32_bf16 v[120:123], v[56:59], a[92:95], v[120:123]\n"
    "buffer_store_dwordx4 v[136:139], v1, s[40:43], s53 offen sc1\n"
    "v_mfma_f32_16x16x32_bf16 v[124:127], v[60:63], a[92:95], v[124:127]\n"
    "s_add_u32 s53, s53, 32768\n"
    "v_mfma_f32_16x16x32_bf16 v[112:115], v[16:19], a[96:99], 0\n"
    "v_mfma_f32_16x16x32_bf16 v[116:119], v[20:23], a[96:99], 0\n"
    "v_mfma_f32_16x16x32_bf16 v[112:115], v[24:27], a[100:103], v[112:115]\n"
    "v_mfma_f32_16x16x32_bf16 v[116:119], v[28:31], a[100:103], v[116:119]\n"
    "v_mfma_f32_16x16x32_bf16 v[112:115], v[32:35], a[104:107], v[112:115]\n"
    "v_mfma_f32_16x16x32_bf16 v[116:119], v[36:39], a[104:107], v[116:119]\n"
    "v_mfma_f32_16x16x32_bf16 v[112:115], v[40:43], a[108:111], v[112:115]\n"
    "v_cvt_pk_bf16_f32 v140, v120, v121\n"
    "v_mfma_f32_16x16x32_bf16 v[116:119], v[44:47], a[108:111], v[116:119]\n"
    "v_cvt_pk_bf16_f32 v141, v122, v123\n"
    "v_mfma_f32_16x16x32_bf16 v[112:115], v[48:51], a[112:115], v[112:115]\n"
    "v_cvt_pk_bf16_f32 v142, v124, v125\n"
    "v_mfma_f32_16x16x32_bf16 v[116:119], v[52:55], a[112:115], v[116:119]\n"
    "v_cvt_pk_bf16_f32 v143, v126, v127\n"
    "v_mfma_f32_16x16x32_bf16 v[112:115], v[56:59], a[116:119], v[112:115]\n"
    "buffer_store_dwordx4 v[140:143], v1, s[40:43], s53 offen sc1\n"
    "v_mfma_f32_16x16x32_bf16 v[116:119], v[60:63], a[116:119], v[116:119]\n"
    "s_add_u32 s53, s53, 32768\n"
    "v_mfma_f32_16x16x32_bf16 v[120:123], v[16:19], a[120:123], 0\n"
    "v_mfma_f32_16x16x32_bf16 v[124:127], v[20:23], a[120:123], 0\n"
    "v_mfma_f32_16x16x32_bf16 v[120:123], v[24:27], a[124:127], v[120:123]\n"
    "v_mfma_f32_16x16x32_bf16 v[124:127], v[28:31], a[124:127], v[124:127]\n"
    "v_mfma_f32_16x16x32_bf16 v[120:123], v[32:35], a[128:131], v[120:123]\n"
    "v_mfma_f32_16x16x32_bf16 v[124:127], v[36:39], a[128:131], v[124:127]\n"
    "v_mfma_f32_16x16x32_bf16 v[120:123], v[40:43], a[132:135], v[120:123]\n"
    "v_cvt_pk_bf16_f32 v136, v112, v113\n"
    "v_mfma_f32_16x16x32_bf16 v[124:127], v[44:47], a[132:135], v[124:127]\n"
    "v_cvt_pk_bf16_f32 v137, v114, v115\n"
    "v_mfma_f32_16x16x32_bf16 v[120:123], v[48:51], a[136:139], v[120:123]\n"
    "v_cvt_pk_bf16_f32 v138, v116, v117\n"
    "v_mfma_f32_16x16x32_bf16 v[124:127], v[52:55], a[136:139], v[124:127]\n"
    "v_cvt_pk_bf16_f32 v139, v118, v119\n"
    "v_mfma_f32_16x16x32_bf16 v[120:123], v[56:59], a[140:143], v[120:123]\n"
    "buffer_store_dwordx4 v[136:139], v1, s[40:43], s53 offen sc1\n"
    "v_mfma_f32_16x16x32_bf16 v[124:127], v[60:63], a[140:143], v[124:127]\n"
    "s_add_u32 s53, s53, 32768\n"
    "v_mfma_f32_16x16x32_bf16 v[112:115], v[16:19], a[144:147], 0\n"
    "v_mfma_f32_16x16x32_bf16 v[116:119], v[20:23], a[144:147], 0\n"
    "v_mfma_f32_16x16x32_bf16 v[112:115], v[24:27], a[148:151], v[112:115]\n"
    "v_mfma_f32_16x16x32_bf16 v[116:119], v[28:31], a[148:151], v[116:119]\n"
    "v_mfma_f32_16x16x32_bf16 v[112:115], v[32:35], a[152:155], v[112:115]\n"
    "v_mfma_f32_16x16x32_bf16 v[116:119], v[36:39], a[152:155], v[116:119]\n"
    "v_mfma_f32_16x16x32_bf16 v[112:115], v[40:43], a[156:159], v[112:115]\n"
    "v_cvt_pk_bf16_f32 v140, v120, v121\n"
    "v_mfma_f32_16x16x32_bf16 v[116:119], v[44:47], a[156:159], v[116:119]\n"
    "v_cvt_pk_bf16_f32 v141, v122, v123\n"
    "v_mfma_f32_16x16x32_bf16 v[112:115], v[48:51], a[160:163], v[112:115]\n"
    "v_cvt_pk_bf16_f32 v142, v124, v125\n"
    "v_mfma_f32_16x16x32_bf16 v[116:119], v[52:55], a[160:163], v[116:119]\n"
    "v_cvt_pk_bf16_f32 v143, v126, v127\n"
    "v_mfma_f32_16x16x32_bf16 v[112:115], v[56:59], a[164:167], v[112:115]\n"
    "buffer_store_dwordx4 v[140:143], v1, s[40:43], s53 offen sc1\n"
    "v_mfma_f32_16x16x32_bf16 v[116:119], v[60:63], a[164:167], v[116:119]\n"
    "s_add_u32 s53, s53, 32768\n"
    "s_waitcnt vmcnt(6)\n"
    "s_cmp_eq_u32 s56, s50\n"
    "s_cbranch_scc1 po_have0_%=\n"
    "s_mov_b32 s60, 0\n"
    "v_cmp_gt_u32 vcc, s55, v6\n"
    "s_nop 4\n"
    "s_cbranch_vccz po_v10_ok_%=\n"
    "po_v10_poll_%=:\n"
    "buffer_load_dword v6, v7, s[40:43], s58 offen sc1\n"
    "s_waitcnt vmcnt(0)\n"
    "v_cmp_gt_u32 vcc, s55, v6\n"
    "s_nop 4\n"
    "s_cbranch_vccz po_v10_ok_%=\n"
    "s_sleep 2\n"
    "s_add_u32 s60, s60, 1\n"
    "s_cmp_gt_u32 s60, 65536\n"
    "s_cbranch_scc1 po_abort_%=\n"
    "s_and_b32 s59, s60, 63\n"
    "s_cbranch_scc1 po_v10_poll_%=\n"
    "v_mov_b32 v4, 1024\n"
    "buffer_load_dword v4, v4, s[40:43], s58 offen sc1\n"
    "s_waitcnt vmcnt(0)\n"
    "v_readfirstlane_b32 s59, v4\n"
    "s_nop 0\n"
    "s_cmp_lg_u32 s59, 0\n"
    "s_nop 2\n"
    "s_cbranch_scc1 po_end_%=\n"
    "s_branch po_v10_poll_%=\n"
    "po_v10_ok_%=:\n"
    "buffer_load_dwordx4 v[144:147], v0, s[40:43], s50 offen offset:0 sc1\n"
    "buffer_load_dwordx4 v[148:151], v0, s[40:43], s50 offen offset:1024 sc1\n"
    "buffer_load_dwordx4 v[152:155], v0, s[40:43], s50 offen offset:2048 sc1\n"
    "s_waitcnt vmcnt(0)\n"
    "po_have0_%=:\n"
    "v_mfma_f32_16x16x32_bf16 v[120:123], v[16:19], a[168:171], 0\n"
    "ds_write2st64_b64 v8, v[144:145], v[146:147] offset0:24 offset1:26\n"
    "v_mfma_f32_16x16x32_bf16 v[124:127], v[20:23], a[168:171], 0\n"
    "ds_write2st64_b64 v9, v[148:149], v[150:151] offset0:24 offset1:26\n"
    "v_mfma_f32_16x16x32_bf16 v[120:123], v[24:27], a[172:175], v[120:123]\n"
    "ds_write2st64_b64 v10, v[152:153], v[154:155] offset0:24 offset1:26\n"
    "v_mfma_f32_16x16x32_bf16 v[124:127], v[28:31], a[172:175], v[124:127]\n"
    "v_mfma_f32_16x16x32_bf16 v[120:123], v[32:35], a[176:179], v[120:123]\n"
    "v_mfma_f32_16x16x32_bf16 v[124:127], v[36:39], a[176:179], v[124:127]\n"
    "v_mfma_f32_16x16x32_bf16 v[120:123], v[40:43], a[180:183], v[120:123]\n"
    "v_cvt_pk_bf16_f32 v136, v112, v113\n"
    "v_mfma_f32_16x16x32_bf16 v[124:127], v[44:47], a[180:183], v[124:127]\n"
    "v_cvt_pk_bf16_f32 v137, v114, v115\n"
    "v_mfma_f32_16x16x32_bf16 v[120:123], v[48:51], a[184:187], v[120:123]\n"
    "v_cvt_pk_bf16_f32 v138, v116, v117\n"
    "v_mfma_f32_16x16x32_bf16 v[124:127], v[52:55], a[184:187], v[124:127]\n"
    "v_cvt_pk_bf16_f32 v139, v118, v119\n"
    "v_mfma_f32_16x16x32_bf16 v[120:123], v[56:59], a[188:191], v[120:123]\n"
    "buffer_store_dwordx4 v[136:139], v1, s[40:43], s53 offen sc1\n"
    "v_mfma_f32_16x16x32_bf16 v[124:127], v[60:63], a[188:191], v[124:127]\n"
    "s_add_u32 s53, s53, 32768\n"
    "s_waitcnt lgkmcnt(0)\n"
    "s_barrier\n"
    "s_max_u32 s64, s64, s47\n"
    "v_mov_b32 v4, s64\n"
    "buffer_store_dword v4, v2, s[40:43], s58 offen sc1\n"
    "v_mfma_f32_16x16x32_bf16 v[112:115], v[16:19], a[192:195], 0\n"
    "ds_read_b128 v[64:67], v0 offset:12288\n"
    "v_mfma_f32_16x16x32_bf16 v[116:119], v[20:23], a[192:195], 0\n"
    "ds_read_b128 v[68:71], v0 offset:13312\n"
    "v_mfma_f32_16x16x32_bf16 v[112:115], v[24:27], a[196:199], v[112:115]\n"
    "ds_read_b128 v[72:75], v0 offset:14336\n"
    "v_mfma_f32_16x16x32_bf16 v[116:119], v[28:31], a[196:199], v[116:119]\n"
    "ds_read_b128 v[76:79], v0 offset:15360\n"
    "v_mfma_f32_16x16x32_bf16 v[112:115], v[32:35], a[200:203], v[112:115]\n"
    "ds_read_b128 v[80:83], v0 offset:16384\n"
    "v_mfma_f32_16x16x32_bf16 v[116:119], v[36:39], a[200:203], v[116:119]\n"
    "ds_read_b128 v[84:87], v0 offset:17408\n"
    "v_mfma_f32_16x16x32_bf16 v[112:115], v[40:43], a[204:207], v[112:115]\n"
    "v_cvt_pk_bf16_f32 v140, v120, v121\n"
    "v_mfma_f32_16x16x32_bf16 v[116:119], v[44:47], a[204:207], v[116:119]\n"
    "v_cvt_pk_bf16_f32 v141, v122, v123\n"
    "v_mfma_f32_16x16x32_bf16 v[112:115], v[48:51], a[208:211], v[112:115]\n"
    "v_cvt_pk_bf16_f32 v142, v124, v125\n"
    "v_mfma_f32_16x16x32_bf16 v[116:119], v[52:55], a[208:211], v[116:119]\n"
    "v_cvt_pk_bf16_f32 v143, v126, v127\n"
    "v_mfma_f32_16x16x32_bf16 v[112:115], v[56:59], a[212:215], v[112:115]\n"
    "buffer_store_dwordx4 v[140:143], v1, s[40:43], s53 offen sc1\n"
    "v_mfma_f32_16x16x32_bf16 v[116:119], v[60:63], a[212:215], v[116:119]\n"
    "s_add_u32 s53, s53, 32768\n"
    "v_mfma_f32_16x16x32_bf16 v[120:123], v[16:19], a[216:219], 0\n"
    "ds_read_b128 v[88:91], v0 offset:18432\n"
    "v_mfma_f32_16x16x32_bf16 v[124:127], v[20:23], a[216:219], 0\n"
    "ds_read_b128 v[92:95], v0 offset:19456\n"
    "v_mfma_f32_16x16x32_bf16 v[120:123], v[24:27], a[220:223], v[120:123]\n"
    "ds_read_b128 v[96:99], v0 offset:20480\n"
    "v_mfma_f32_16x16x32_bf16 v[124:127], v[28:31], a[220:223], v[124:127]\n"
    "ds_read_b128 v[100:103], v0 offset:21504\n"
    "v_mfma_f32_16x16x32_bf16 v[120:123], v[32:35], a[224:227], v[120:123]\n"
    "ds_read_b128 v[104:107], v0 offset:22528\n"
    "v_mfma_f32_16x16x32_bf16 v[124:127], v[36:39], a[224:227], v[124:127]\n"
    "ds_read_b128 v[108:111], v0 offset:23552\n"
    "v_mfma_f32_16x16x32_bf16 v[120:123], v[40:43], a[228:231], v[120:123]\n"
    "v_cvt_pk_bf16_f32 v136, v112, v113\n"
    "v_mfma_f32_16x16x32_bf16 v[124:127], v[44:47], a[228:231], v[124:127]\n"
    "v_cvt_pk_bf16_f32 v137, v114, v115\n"
    "v_mfma_f32_16x16x32_bf16 v[120:123], v[48:51], a[232:235], v[120:123]\n"
    "v_cvt_pk_bf16_f32 v138, v116, v117\n"
    "v_mfma_f32_16x16x32_bf16 v[124:127], v[52:55], a[232:235], v[124:127]\n"
    "v_cvt_pk_bf16_f32 v139, v118, v119\n"
    "v_mfma_f32_16x16x32_bf16 v[120:123], v[56:59], a[236:239], v[120:123]\n"
    "buffer_store_dwordx4 v[136:139], v1, s[40:43], s53 offen sc1\n"
    "v_mfma_f32_16x16x32_bf16 v[124:127], v[60:63], a[236:239], v[124:127]\n"
    "s_add_u32 s53, s53, 32768\n"
    "s_waitcnt lgkmcnt(0)\n"
    "s_add_u32 s63, s63, 1310720\n"
    "s_sub_u32 s59, s47, 2\n"
    "s_cmp_lt_u32 s59, 61\n"
    "s_cbranch_scc1 po_late0_%=\n"
    "v_cvt_pk_bf16_f32 v140, v120, v121\n"
    "v_cvt_pk_bf16_f32 v141, v122, v123\n"
    "v_cvt_pk_bf16_f32 v142, v124, v125\n"
    "v_cvt_pk_bf16_f32 v143, v126, v127\n"
    "buffer_store_dwordx4 v[140:143], v1, s[40:43], s53 offen sc1\n"
    "s_add_u32 s64, s47, 1\n"
    "v_mov_b32 v4, s64\n"
    "s_waitcnt vmcnt(0)\n"
    "s_barrier\n"
    "buffer_store_dword v4, v2, s[40:43], s58 offen sc1\n"
    "s_cmp_eq_u32 s47, 63\n"
    "s_nop 0\n"
    "s_cbranch_scc1 po_end_%=\n"
    "po_late0_%=:\n"
    "s_add_u32 s47, s47, 1\n"
    "s_add_u32 s55, s47, 2\n"
    "s_min_u32 s55, s55, 64\n"
    "s_add_u32 s50, s50, 393216\n"
    "v_cmp_gt_u32 vcc, s55, v6\n"
    "s_nop 0\n"
    "s_cmp_eq_u64 vcc, 0\n"
    "s_cselect_b32 s56, s50, 0x40000000\n"
    "v_mfma_f32_16x16x32_bf16 v[112:115], v[64:67], a[0:3], 0\n"
    "buffer_load_dwordx4 v[144:147], v0, s[40:43], s56 offen offset:0 sc1\n"
    "v_mfma_f32_16x16x32_bf16 v[116:119], v[68:71], a[0:3], 0\n"
    "buffer_load_dwordx4 v[148:151], v0, s[40:43], s56 offen offset:1024 sc1\n"
    "v_mfma_f32_16x16x32_bf16 v[112:115], v[72:75], a[4:7], v[112:115]\n"
    "buffer_load_dwordx4 v[152:155], v0, s[40:43], s56 offen offset:2048 sc1\n"
    "v_mfma_f32_16x16x32_bf16 v[116:119], v[76:79], a[4:7], v[116:119]\n"
    "buffer_load_dword v6, v7, s[40:43], s58 offen sc1\n"
    "v_mfma_f32_16x16x32_bf16 v[112:115], v[80:83], a[8:11], v[112:115]\n"
    "v_mfma_f32_16x16x32_bf16 v[116:119], v[84:87], a[8:11], v[116:119]\n"
    "v_mfma_f32_16x16x32_bf16 v[112:115], v[88:91], a[12:15], v[112:115]\n"
    "v_cvt_pk_bf16_f32 v140, v120, v121\n"
    "v_mfma_f32_16x16x32_bf16 v[116:119], v[92:95], a[12:15], v[116:119]\n"
    "v_cvt_pk_bf16_f32 v141, v122, v123\n"
    "v_mfma_f32_16x16x32_bf16 v[112:115], v[96:99], a[16:19], v[112:115]\n"
    "v_cvt_pk_bf16_f32 v142, v124, v125\n"
    "v_mfma_f32_16x16x32_bf16 v[116:119], v[100:103], a[16:19], v[116:119]\n"
    "v_cvt_pk_bf16_f32 v143, v126, v127\n"
    "v_mfma_f32_16x16x32_bf16 v[112:115], v[104:107], a[20:23], v[112:115]\n"
    "buffer_store_dwordx4 v[140:143], v1, s[40:43], s53 offen sc1\n"
    "v_mfma_f32_16x16x32_bf16 v[116:119], v[108:111], a[20:23], v[116:119]\n"
    "s_mov_b32 s53, s63\n"
    "v_mfma_f32_16x16x32_bf16 v[120:123], v[64:67], a[24:27], 0\n"
    "v_mfma_f32_16x16x32_bf16 v[124:127], v[68:71], a[24:27], 0\n"
    "v_mfma_f32_16x16x32_bf16 v[120:123], v[72:75], a[28:31], v[120:123]\n"
    "v_mfma_f32_16x16x32_bf16 v[124:127], v[76:79], a[28:31], v[124:127]\n"
    "v_mfma_f32_16x16x32_bf16 v[120:123], v[80:83], a[32:35], v[120:123]\n"
    "v_mfma_f32_16x16x32_bf16 v[124:127], v[84:87], a[32:35], v[124:127]\n"
    "v_mfma_f32_16x16x32_bf16 v[120:123], v[88:91], a[36:39], v[120:123]\n"
    "v_cvt_pk_bf16_f32 v136, v112, v113\n"
    "v_mfma_f32_16x16x32_bf16 v[124:127], v[92:95], a[36:39], v[124:127]\n"
    "v_cvt_pk_bf16_f32 v137, v114, v115\n"
    "v_mfma_f32_16x16x32_bf16 v[120:123], v[96:99], a[40:43], v[120:123]\n"
    "v_cvt_pk_bf16_f32 v138, v116, v117\n"
    "v_mfma_f32_16x16x32_bf16 v[124:127], v[100:103], a[40:43], v[124:127]\n"
    "v_cvt_pk_bf16_f32 v139, v118, v119\n"
    "v_mfma_f32_16x16x32_bf16 v[120:123], v[104:107], a[44:47], v[120:123]\n"
    "buffer_store_dwordx4 v[136:139], v1, s[40:43], s53 offen sc1\n"
    "v_mfma_f32_16x16x32_bf16 v[124:127], v[108:111], a[44:47], v[124:127]\n"
    "s_add_u32 s53, s53, 32768\n"
    "v_mfma_f32_16x16x32_bf16 v[112:115], v[64:67], a[48:51], 0\n"
    "v_mfma_f32_16x16x32_bf16 v[116:119], v[68:71], a[48:51], 0\n"
    "v_mfma_f32_16x16x32_bf16 v[112:115], v[72:75], a[52:55], v[112:115]\n"
    "v_mfma_f32_16x16x32_bf16 v[116:119], v[76:79], a[52:55], v[116:119]\n"
    "v_mfma_f32_16x16x32_bf16 v[112:115], v[80:83], a[56:59], v[112:115]\n"
    "v_mfma_f32_16x16x32_bf16 v[116:119], v[84:87], a[56:59], v[116:119]\n"
    "v_mfma_f32_16x16x32_bf16 v[112:115], v[88:91], a[60:63], v[112:115]\n"
    "v_cvt_pk_bf16_f32 v140, v120, v121\n"
    "v_mfma_f32_16x16x32_bf16 v[116:119], v[92:95], a[60:63], v[116:119]\n"
    "v_cvt_pk_bf16_f32 v141, v122, v123\n"
    "v_mfma_f32_16x16x32_bf16 v[112:115], v[96:99], a[64:67], v[112:115]\n"
    "v_cvt_pk_bf16_f32 v142, v124, v125\n"
    "v_mfma_f32_16x16x32_bf16 v[116:119], v[100:103], a[64:67], v[116:119]\n"
    "v_cvt_pk_bf16_f32 v143, v126, v127\n"
    "v_mfma_f32_16x16x32_bf16 v[112:115], v[104:107], a[68:71], v[112:115]\n"
    "buffer_store_dwordx4 v[140:143], v1, s[40:43], s53 offen sc1\n"
    "v_mfma_f32_16x16x32_bf16 v[116:119], v[108:111], a[68:71], v[116:119]\n"
    "s_add_u32 s53, s53, 32768\n"
    "v_mfma_f32_16x16x32_bf16 v[120:123], v[64:67], a[72:75], 0\n"
    "v_mfma_f32_16x16x32_bf16 v[124:127], v[68:71], a[72:75], 0\n"
    "v_mfma_f32_16x16x32_bf16 v[120:123], v[72:75], a[76:79], v[120:123]\n"
    "v_mfma_f32_16x16x32_bf16 v[124:127], v[76:79], a[76:79], v[124:127]\n"
    "v_mfma_f32_16x16x32_bf16 v[120:123], v[80:83], a[80:83], v[120:123]\n"
    "v_mfma_f32_16x16x32_bf16 v[124:127], v[84:87], a[80:83], v[124:127]\n"
    "v_mfma_f32_16x16x32_bf16 v[120:123], v[88:91], a[84:87], v[120:123]\n"
    "v_cvt_pk_bf16_f32 v136, v112, v113\n"
    "v_mfma_f32_16x16x32_bf16 v[124:127], v[92:95], a[84:87], v[124:127]\n"
    "v_cvt_pk_bf16_f32 v137, v114, v115\n"
    "v_mfma_f32_16x16x32_bf16 v[120:123], v[96:99], a[88:91], v[120:123]\n"
    "v_cvt_pk_bf16_f32 v138, v116, v117\n"
    "v_mfma_f32_16x16x32_bf16 v[124:127], v[100:103], a[88:91], v[124:127]\n"
    "v_cvt_pk_bf16_f32 v139, v118, v119\n"
    "v_mfma_f32_16x16x32_bf16 v[120:123], v[104:107], a[92:95], v[120:123]\n"
    "buffer_store_dwordx4 v[136:139], v1, s[40:43], s53 offen sc1\n"
    "v_mfma_f32_16x16x32_bf16 v[124:127], v[108:111], a[92:95], v[124:127]\n"
    "s_add_u32 s53, s53, 32768\n"
    "v_mfma_f32_16x16x32_bf16 v[112:115], v[64:67], a[96:99], 0\n"
    "v_mfma_f32_16x16x32_bf16 v[116:119], v[68:71], a[96:99], 0\n"
    "v_mfma_f32_16x16x32_bf16 v[112:115], v[72:75], a[100:103], v[112:115]\n"
    "v_mfma_f32_16x16x32_bf16 v[116:119], v[76:79], a[100:103], v[116:119]\n"
    "v_mfma_f32_16x16x32_bf16 v[112:115], v[80:83], a[104:107], v[112:115]\n"
    "v_mfma_f32_16x16x32_bf16 v[116:119], v[84:87], a[104:107], v[116:119]\n"
    "v_mfma_f32_16x16x32_bf16 v[112:115], v[88:91], a[108:111], v[112:115]\n"
    "v_cvt_pk_bf16_f32 v140, v120, v121\n"
    "v_mfma_f32_16x16x32_bf16 v[116:119], v[92:95], a[108:111], v[116:119]\n"
    "v_cvt_pk_bf16_f32 v141, v122, v123\n"
    "v_mfma_f32_16x16x32_bf16 v[112:115], v[96:99], a[112:115], v[112:115]\n"
    "v_cvt_pk_bf16_f32 v142, v124, v125\n"
    "v_mfma_f32_16x16x32_bf16 v[116:119], v[100:103], a[112:115], v[116:119]\n"
    "v_cvt_pk_bf16_f32 v143, v126, v127\n"
    "v_mfma_f32_16x16x32_bf16 v[112:115], v[104:107], a[116:119], v[112:115]\n"
    "buffer_store_dwordx4 v[140:143], v1, s[40:43], s53 offen sc1\n"
    "v_mfma_f32_16x16x32_bf16 v[116:119], v[108:111], a[116:119], v[116:119]\n"
    "s_add_u32 s53, s53, 32768\n"
    "v_mfma_f32_16x16x32_bf16 v[120:123], v[64:67], a[120:123], 0\n"
    "v_mfma_f32_16x16x32_bf16 v[124:127], v[68:71], a[120:123], 0\n"
    "v_mfma_f32_16x16x32_bf16 v[120:123], v[72:75], a[124:127], v[120:123]\n"
    "v_mfma_f32_16x16x32_bf16 v[124:127], v[76:79], a[124:127], v[124:127]\n"
    "v_mfma_f32_16x16x32_bf16 v[120:123], v[80:83], a[128:131], v[120:123]\n"
    "v_mfma_f32_16x16x32_bf16 v[124:127], v[84:87], a[128:131], v[124:127]\n"
    "v_mfma_f32_16x16x32_bf16 v[120:123], v[88:91], a[132:135], v[120:123]\n"
    "v_cvt_pk_bf16_f32 v136, v112, v113\n"
    "v_mfma_f32_16x16x32_bf16 v[124:127], v[92:95], a[132:135], v[124:127]\n"
    "v_cvt_pk_bf16_f32 v137, v114, v115\n"
    "v_mfma_f32_16x16x32_bf16 v[120:123], v[96:99], a[136:139], v[120:123]\n"
    "v_cvt_pk_bf16_f32 v138, v116, v117\n"
    "v_mfma_f32_16x16x32_bf16 v[124:127], v[100:103], a[136:139], v[124:127]\n"
    "v_cvt_pk_bf16_f32 v139, v118, v119\n"
    "v_mfma_f32_16x16x32_bf16 v[120:123], v[104:107], a[140:143], v[120:123]\n"
    "buffer_store_dwordx4 v[136:139], v1, s[40:43], s53 offen sc1\n"
    "v_mfma_f32_16x16x32_bf16 v[124:127], v[108:111], a[140:143], v[124:127]\n"
    "s_add_u32 s53, s53, 32768\n"
    "v_mfma_f32_16x16x32_bf16 v[112:115], v[64:67], a[144:147], 0\n"
    "v_mfma_f32_16x16x32_bf16 v[116:119], v[68:71], a[144:147], 0\n"
    "v_mfma_f32_16x16x32_bf16 v[112:115], v[72:75], a[148:151], v[112:115]\n"
    "v_mfma_f32_16x16x32_bf16 v[116:119], v[76:79], a[148:151], v[116:119]\n"
    "v_mfma_f32_16x16x32_bf16 v[112:115], v[80:83], a[152:155], v[112:115]\n"
    "v_mfma_f32_16x16x32_bf16 v[116:119], v[84:87], a[152:155], v[116:119]\n"
    "v_mfma_f32_16x16x32_bf16 v[112:115], v[88:91], a[156:159], v[112:115]\n"
    "v_cvt_pk_bf16_f32 v140, v120, v121\n"
    "v_mfma_f32_16x16x32_bf16 v[116:119], v[92:95], a[156:159], v[116:119]\n"
    "v_cvt_pk_bf16_f32 v141, v122, v123\n"
    "v_mfma_f32_16x16x32_bf16 v[112:115], v[96:99], a[160:163], v[112:115]\n"
    "v_cvt_pk_bf16_f32 v142, v124, v125\n"
    "v_mfma_f32_16x16x32_bf16 v[116:119], v[100:103], a[160:163], v[116:119]\n"
    "v_cvt_pk_bf16_f32 v143, v126, v127\n"
    "v_mfma_f32_16x16x32_bf16 v[112:115], v[104:107], a[164:167], v[112:115]\n"
    "buffer_store_dwordx4 v[140:143], v1, s[40:43], s53 offen sc1\n"
    "v_mfma_f32_16x16x32_bf16 v[116:119], v[108:111], a[164:167], v[116:119]\n"
    "s_add_u32 s53, s53, 32768\n"
    "s_waitcnt vmcnt(6)\n"
    "s_cmp_eq_u32 s56, s50\n"
    "s_cbranch_scc1 po_have1_%=\n"
    "s_mov_b32 s60, 0\n"
    "v_cmp_gt_u32 vcc, s55, v6\n"
    "s_nop 4\n"
    "s_cbranch_vccz po_v11_ok_%=\n"
    "po_v11_poll_%=:\n"
    "buffer_load_dword v6, v7, s[40:43], s58 offen sc1\n"
    "s_waitcnt vmcnt(0)\n"
    "v_cmp_gt_u32 vcc, s55, v6\n"
    "s_nop 4\n"
    "s_cbranch_vccz po_v11_ok_%=\n"
    "s_sleep 2\n"
    "s_add_u32 s60, s60, 1\n"
    "s_cmp_gt_u32 s60, 65536\n"
    "s_cbranch_scc1 po_abort_%=\n"
    "s_and_b32 s59, s60, 63\n"
    "s_cbranch_scc1 po_v11_poll_%=\n"
    "v_mov_b32 v4, 1024\n"
    "buffer_load_dword v4, v4, s[40:43], s58 offen sc1\n"
    "s_waitcnt vmcnt(0)\n"
    "v_readfirstlane_b32 s59, v4\n"
    "s_nop 0\n"
    "s_cmp_lg_u32 s59, 0\n"
    "s_nop 2\n"
    "s_cbranch_scc1 po_end_%=\n"
    "s_branch po_v11_poll_%=\n"
    "po_v11_ok_%=:\n"
    "buffer_load_dwordx4 v[144:147], v0, s[40:43], s50 offen offset:0 sc1\n"
    "buffer_load_dwordx4 v[148:151], v0, s[40:43], s50 offen offset:1024 sc1\n"
    "buffer_load_dwordx4 v[152:155], v0, s[40:43], s50 offen offset:2048 sc1\n"
    "s_waitcnt vmcnt(0)\n"
    "po_have1_%=:\n"
    "v_mfma_f32_16x16x32_bf16 v[120:123], v[64:67], a[168:171], 0\n"
    "ds_write2st64_b64 v8, v[144:145], v[146:147] offset0:0 offset1:2\n"
    "v_mfma_f32_16x16x32_bf16 v[124:127], v[68:71], a[168:171], 0\n"
    "ds_write2st64_b64 v9, v[148:149], v[150:151] offset0:0 offset1:2\n"
    "v_mfma_f32_16x16x32_bf16 v[120:123], v[72:75], a[172:175], v[120:123]\n"
    "ds_write2st64_b64 v10, v[152:153], v[154:155] offset0:0 offset1:2\n"
    "v_mfma_f32_16x16x32_bf16 v[124:127], v[76:79], a[172:175], v[124:127]\n"
    "v_mfma_f32_16x16x32_bf16 v[120:123], v[80:83], a[176:179], v[120:123]\n"
    "v_mfma_f32_16x16x32_bf16 v[124:127], v[84:87], a[176:179], v[124:127]\n"
    "v_mfma_f32_16x16x32_bf16 v[120:123], v[88:91], a[180:183], v[120:123]\n"
    "v_cvt_pk_bf16_f32 v136, v112, v113\n"
    "v_mfma_f32_16x16x32_bf16 v[124:127], v[92:95], a[180:183], v[124:127]\n"
    "v_cvt_pk_bf16_f32 v137, v114, v115\n"
    "v_mfma_f32_16x16x32_bf16 v[120:123], v[96:99], a[184:187], v[120:123]\n"
    "v_cvt_pk_bf16_f32 v138, v116, v117\n"
    "v_mfma_f32_16x16x32_bf16 v[124:127], v[100:103], a[184:187], v[124:127]\n"
    "v_cvt_pk_bf16_f32 v139, v118, v119\n"
    "v_mfma_f32_16x16x32_bf16 v[120:123], v[104:107], a[188:191], v[120:123]\n"
    "buffer_store_dwordx4 v[136:139], v1, s[40:43], s53 offen sc1\n"
    "v_mfma_f32_16x16x32_bf16 v[124:127], v[108:111], a[188:191], v[124:127]\n"
    "s_add_u32 s53, s53, 32768\n"
    "s_waitcnt lgkmcnt(0)\n"
    "s_barrier\n"
    "s_max_u32 s64, s64, s47\n"
    "v_mov_b32 v4, s64\n"
    "buffer_store_dword v4, v2, s[40:43], s58 offen sc1\n"
    "v_mfma_f32_16x16x32_bf16 v[112:115], v[64:67], a[192:195], 0\n"
    "ds_read_b128 v[16:19], v0 offset:0\n"
    "v_mfma_f32_16x16x32_bf16 v[116:119], v[68:71], a[192:195], 0\n"
    "ds_read_b128 v[20:23], v0 offset:1024\n"
    "v_mfma_f32_16x16x32_bf16 v[112:115], v[72:75], a[196:199], v[112:115]\n"
    "ds_read_b128 v[24:27], v0 offset:2048\n"
    "v_mfma_f32_16x16x32_bf16 v[116:119], v[76:79], a[196:199], v[116:119]\n"
    "ds_read_b128 v[28:31], v0 offset:3072\n"
    "v_mfma_f32_16x16x32_bf16 v[112:115], v[80:83], a[200:203], v[112:115]\n"
    "ds_read_b128 v[32:35], v0 offset:4096\n"
    "v_mfma_f32_16x16x32_bf16 v[116:119], v[84:87], a[200:203], v[116:119]\n"
    "ds_read_b128 v[36:39], v0 offset:5120\n"
    "v_mfma_f32_16x16x32_bf16 v[112:115], v[88:91], a[204:207], v[112:115]\n"
    "v_cvt_pk_bf16_f32 v140, v120, v121\n"
    "v_mfma_f32_16x16x32_bf16 v[116:119], v[92:95], a[204:207], v[116:119]\n"
    "v_cvt_pk_bf16_f32 v141, v122, v123\n"
    "v_mfma_f32_16x16x32_bf16 v[112:115], v[96:99], a[208:211], v[112:115]\n"
    "v_cvt_pk_bf16_f32 v142, v124, v125\n"
    "v_mfma_f32_16x16x32_bf16 v[116:119], v[100:103], a[208:211], v[116:119]\n"
    "v_cvt_pk_bf16_f32 v143, v126, v127\n"
    "v_mfma_f32_16x16x32_bf16 v[112:115], v[104:107], a[212:215], v[112:115]\n"
    "buffer_store_dwordx4 v[140:143], v1, s[40:43], s53 offen sc1\n"
    "v_mfma_f32_16x16x32_bf16 v[116:119], v[108:111], a[212:215], v[116:119]\n"
    "s_add_u32 s53, s53, 32768\n"
    "v_mfma_f32_16x16x32_bf16 v[120:123], v[64:67], a[216:219], 0\n"
    "ds_read_b128 v[40:43], v0 offset:6144\n"
    "v_mfma_f32_16x16x32_bf16 v[124:127], v[68:71], a[216:219], 0\n"
    "ds_read_b128 v[44:47], v0 offset:7168\n"
    "v_mfma_f32_16x16x32_bf16 v[120:123], v[72:75], a[220:223], v[120:123]\n"
    "ds_read_b128 v[48:51], v0 offset:8192\n"
    "v_mfma_f32_16x16x32_bf16 v[124:127], v[76:79], a[220:223], v[124:127]\n"
    "ds_read_b128 v[52:55], v0 offset:9216\n"
    "v_mfma_f32_16x16x32_bf16 v[120:123], v[80:83], a[224:227], v[120:123]\n"
    "ds_read_b128 v[56:59], v0 offset:10240\n"
    "v_mfma_f32_16x16x32_bf16 v[124:127], v[84:87], a[224:227], v[124:127]\n"
    "ds_read_b128 v[60:63], v0 offset:11264\n"
    "v_mfma_f32_16x16x32_bf16 v[120:123], v[88:91], a[228:231], v[120:123]\n"
    "v_cvt_pk_bf16_f32 v136, v112, v113\n"
    "v_mfma_f32_16x16x32_bf16 v[124:127], v[92:95], a[228:231], v[124:127]\n"
    "v_cvt_pk_bf16_f32 v137, v114, v115\n"
    "v_mfma_f32_16x16x32_bf16 v[120:123], v[96:99], a[232:235], v[120:123]\n"
    "v_cvt_pk_bf16_f32 v138, v116, v117\n"
    "v_mfma_f32_16x16x32_bf16 v[124:127], v[100:103], a[232:235], v[124:127]\n"
    "v_cvt_pk_bf16_f32 v139, v118, v119\n"
    "v_mfma_f32_16x16x32_bf16 v[120:123], v[104:107], a[236:239], v[120:123]\n"
    "buffer_store_dwordx4 v[136:139], v1, s[40:43], s53 offen sc1\n"
    "v_mfma_f32_16x16x32_bf16 v[124:127], v[108:111], a[236:239], v[124:127]\n"
    "s_add_u32 s53, s53, 32768\n"
    "s_waitcnt lgkmcnt(0)\n"
    "s_add_u32 s63, s63, 1310720\n"
    "s_sub_u32 s59, s47, 2\n"
    "s_cmp_lt_u32 s59, 61\n"
    "s_cbranch_scc1 po_late1_%=\n"
    "v_cvt_pk_bf16_f32 v140, v120, v121\n"
    "v_cvt_pk_bf16_f32 v141, v122, v123\n"
    "v_cvt_pk_bf16_f32 v142, v124, v125\n"
    "v_cvt_pk_bf16_f32 v143, v126, v127\n"
    "buffer_store_dwordx4 v[140:143], v1, s[40:43], s53 offen sc1\n"
    "s_add_u32 s64, s47, 1\n"
    "v_mov_b32 v4, s64\n"
    "s_waitcnt vmcnt(0)\n"
    "s_barrier\n"
    "buffer_store_dword v4, v2, s[40:43], s58 offen sc1\n"
    "s_cmp_eq_u32 s47, 63\n"
    "s_nop 0\n"
    "s_cbranch_scc1 po_end_%=\n"
    "po_late1_%=:\n"
    "s_add_u32 s47, s47, 1\n"
    "s_branch po_loop_%=\n"
    "s_branch po_end_%=\n"
    "po_abort_%=:\n"
    "v_mov_b32 v4, 1\n"
    "v_mov_b32 v5, 1024\n"
    "buffer_store_dword v4, v5, s[40:43], s58 offen sc1\n"
    "po_end_%=:\n"
    "s_endpgm\n"
    :: ROLE_INPUTS : ROLE_CLOBBERS);
}

__global__ void __launch_bounds__(256, 1) layer_a(char* ws, Tune tn) {
    extern __shared__ __attribute__((aligned(16))) char smem[];
    LAS char* lds = (LAS char*)smem;
    rsrc_t r = __builtin_amdgcn_make_buffer_rsrc(ws, 0, WS_BYTES, 0x00020000);
    const int tid = threadIdx.x, lane = tid & 63, w = __builtin_amdgcn_readfirstlane(tid >> 6);
    const unsigned xcc = xcc_id();
    if (tid == 0) {
        const unsigned nearg = ld4(r, 0, OFF_FLAGS + FL_NEAR) & 1u;
        const unsigned pos = ((((xcc >> 2) ^ nearg) & 1u) << 2) | (xcc & 3u);
        unsigned role = pos * 32 + ((blockIdx.x >> 3) & 31);
        unsigned* claim = (unsigned*)(ws + OFF_FLAGS + FL_CLAIM);
        for (int n = 0; n < 256; ++n, role = (role + 1) & 255)
            if (__hip_atomic_exchange(claim + role, 1u, __ATOMIC_RELAXED, __HIP_MEMORY_SCOPE_AGENT) == 0u) break;
        *(LAS unsigned*)lds = role;
    }
    __syncthreads();
    const int role = __builtin_amdgcn_readfirstlane(*(LAS unsigned*)lds);
    __syncthreads();
    const int pos = role >> 5, idx = role & 31;
    const unsigned lo = (unsigned)(uintptr_t)ws, hi = (unsigned)((uintptr_t)ws >> 32) & 0xffffu;
    if (pos < 4) {
        if (idx < 8) role_head(lo, hi, pos * 8 + idx, w, xcc & 3, tn);
        else if (idx < 28) role_col(lo, hi, pos * 20 + idx - 8, w, xcc & 3, tn);
    } else {
        const int h = (pos - 4) * 8 + (idx & 7), kind = idx >> 3;
        if (kind == 0) role_projq(lo, hi, h, w, 0, tn);
        else if (kind < 3) role_projkv(lo, hi, h, w, kind, tn);
        else role_out(lo, hi, h, w, 0, tn);
    }
}

DEV f4 ldf4(rsrc_t r, unsigned v, unsigned s) { return __builtin_bit_cast(f4, ld16(r, v, s)); }
DEV void stf4(f4 d, rsrc_t r, unsigned v, unsigned s) { st16(__builtin_bit_cast(u4, d), r, v, s); }
DEV void task_tok(rsrc_t r, LAS char* lds, int s, int cg, int w, int lane) {
    const int g = lane >> 4, m = lane & 15;
    const unsigned uo = (m >> 3) * 16384 + (g * 16 + (m & 7)) * 16, vo = (m >> 3) * 4096 + (g * 16 + (m & 7)) * 16;
    const int tile = 2 * s + (w >> 1), kk = 2 * cg + (w & 1);
    const unsigned xo = (unsigned)(tile * 20 + kk) * 2048;
    u4 p[10][2], u[10][4];
#pragma unroll
    for (int hh = 0; hh < 10; ++hh) {
#pragma unroll
        for (int tt = 0; tt < 2; ++tt)
            p[hh][tt] = hh < 8 ? ld16(r, lane * 16, OFF_PTX + (unsigned)((s * 32 + 8 * w + hh) * 2 + tt) * 1024) : ld16(r, lane * 16, OFF_PTM + (unsigned)((s * 2 + tt) * 8 + w * 2 + hh - 8) * 1024);
#pragma unroll
        for (int c = 0; c < 4; ++c)
            u[hh][c] = hh < 8 ? ld16(r, uo + (hh & 1) * 128, OFF_UX + (unsigned)(s * 80 + 8 * cg + 2 * c) * 16384 + (4 * w + (hh >> 1)) * 1024)
                              : ld16(r, vo + (hh & 1) * 128, OFF_VO + (unsigned)(s * 80 + 8 * cg + 2 * c) * 4096 + w * 1024);
    }
    f4 x0 = ldf4(r, lane * 16, OFF_XA + xo), x1 = ldf4(r, lane * 16 + 1024, OFF_XA + xo);
    memfence();
    f4 acc[4][2];
#pragma unroll
    for (int c = 0; c < 4; ++c) { acc[c][0] = (f4){0, 0, 0, 0}; acc[c][1] = acc[c][0]; }
#pragma unroll
    for (int hh = 0; hh < 10; ++hh)
#pragma unroll
        for (int c = 0; c < 4; ++c)
#pragma unroll
            for (int tt = 0; tt < 2; ++tt) acc[c][tt] = mfma(u[hh][c], p[hh][tt], acc[c][tt]);
#pragma unroll
    for (int c = 0; c < 4; ++c)
#pragma unroll
        for (int tt = 0; tt < 2; ++tt) *(LAS f4*)(lds + (w * 8 + tt * 4 + c) * 1024 + lane * 16) = acc[c][tt];
    bar();
#pragma unroll
    for (int q = 0; q < 4; ++q) { x0 += *(LAS f4*)(lds + (q * 8 + 2 * w) * 1024 + lane * 16); x1 += *(LAS f4*)(lds + (q * 8 + 2 * w + 1) * 1024 + lane * 16); }
    stf4(x0, r, lane * 16, OFF_XB + xo); stf4(x1, r, lane * 16 + 1024, OFF_XB + xo);
    st16((u4){pk2(x0[0], x0[1]), pk2(x0[2], x0[3]), pk2(x1[0], x1[1]), pk2(x1[2], x1[3])}, r, lane * 16, OFF_XR + xo / 2);
}

constexpr float LOG2E = 1.4426950408889634f;
DEV void hedge(const float (&p)[20], unsigned (&o)[20]) {
    float mx = p[0], mn = p[0];
#pragma unroll
    for (int f = 1; f < 20; ++f) { mx = fmaxf(mx, p[f]); mn = fminf(mn, p[f]); }
    float a[20], b[20], sa = 0.f, sb = 0.f;
    const float ca = -mx * LOG2E, cb = mn * LOG2E;
#pragma unroll
    for (int f = 0; f < 20; ++f) { a[f] = __builtin_amdgcn_exp2f(fmaf(p[f], LOG2E, ca)); b[f] = __builtin_amdgcn_exp2f(fmaf(p[f], -LOG2E, cb)); sa += a[f]; sb += b[f]; }
    sa = __builtin_amdgcn_rcpf(sa); sb = __builtin_amdgcn_rcpf(sb);
#pragma unroll
    for (int f = 0; f < 10; ++f) { o[f] = pk2(a[2 * f] * sa, a[2 * f + 1] * sa); o[10 + f] = pk2(b[2 * f] * sb, b[2 * f + 1] * sb); }
}
DEV void task_b1(rsrc_t r, LAS char* lds, int tb, int hg, int w, int lane) {
    const int g = lane >> 4, m = lane & 15, tile = 4 * tb + w;
    u4 ws_[15], x[20];
    const unsigned wb = OFF_WB + (unsigned)(hg * 300 + w * 15) * 1024;
#pragma unroll
    for (int q = 0; q < 15; ++q) ws_[q] = ld16p(r, lane * 16, wb + q * 1024);
#pragma unroll
    for (int kk = 0; kk < 20; ++kk) x[kk] = ld16(r, lane * 16, OFF_XR + (unsigned)(tile * 20 + kk) * 1024);
    f4 acc[15], s1 = {0, 0, 0, 0}, s2 = s1;
#pragma unroll
    for (int t = 0; t < 15; ++t) acc[t] = s1;
    const u4 ones = {0x3f803f80u, 0x3f803f80u, 0x3f803f80u, 0x3f803f80u};
#pragma unroll
    for (int c = 0; c < 5; ++c) {
        LAS char* buf = lds + (c & 1) * 61440 + lane * 16;
#pragma unroll
        for (int q = 0; q < 15; ++q) *(LAS u4*)(buf + (w * 15 + q) * 1024) = ws_[q];
        if (c < 4) {
#pragma unroll
            for (int q = 0; q < 15; ++q) ws_[q] = ld16p(r, lane * 16, wb + ((c + 1) * 60 + q) * 1024);
        }
        bar();
#pragma unroll
        for (int k = 0; k < 4; ++k) {
#pragma unroll
            for (int t = 0; t < 15; ++t) acc[t] = mfma(*(LAS u4*)(buf + (k * 15 + t) * 1024), x[4 * c + k], acc[t]);
            s1 = mfma(ones, x[4 * c + k], s1); s2 = mfma(x[4 * c + k], x[4 * c + k], s2);
        }
    }
    const float mu = s1[0] * (1.f / 640.f);
    float sq = (m & 2) ? ((m & 1) ? s2[3] : s2[2]) : ((m & 1) ? s2[1] : s2[0]);
    sq = xadd32(xadd16((m >> 2) == g ? sq : 0.f));
    const float rs = rsqrtf(fmaxf(sq * (1.f / 640.f) - mu * mu, 0.f) + 1e-5f), mr = -mu * rs;
    float p[3][20];
#pragma unroll
    for (int t = 0; t < 15; ++t) {
        const f4 cs = __builtin_bit_cast(f4, ld16p(r, g * 16, OFF_CS + (hg * 15 + t) * 64));
#pragma unroll
        for (int q = 0; q < 4; ++q) p[t / 5][(t % 5) * 4 + q] = fmaf(acc[t][q], rs, mr * cs[q]);
    }
    unsigned o[20];
    hedge(p[1], o);
    LAS char* row = lds + 61440 + (g * 64 + w * 16 + m) * 176;
#pragma unroll
    for (int q = 0; q < 5; ++q) *(LAS u4*)(row + q * 16) = (u4){o[4 * q], o[4 * q + 1], o[4 * q + 2], o[4 * q + 3]};
    *(LAS u4*)(row + 80) = (u4){0, 0, 0, 0};
    *(LAS u4*)(row + 96) = (u4){pk2(p[2][0], p[2][1]), pk2(p[2][2], p[2][3]), pk2(p[2][4], p[2][5]), pk2(p[2][6], p[2][7])};
    *(LAS u4*)(row + 112) = (u4){pk2(p[2][8], p[2][9]), pk2(p[2][10], p[2][11]), pk2(p[2][12], p[2][13]), pk2(p[2][14], p[2][15])};
    *(LAS u4*)(row + 128) = (u4){pk2(p[2][16], p[2][17]), pk2(p[2][18], p[2][19]), 0x3f80u, 0};
    *(LAS u4*)(row + 144) = (u4){0, 0, 0, 0};
    bar();
    LAS char* img = lds + 61440 + (w * 64 + (lane >> 2)) * 176 + (lane & 3) * 8;
    f4 kv[3][2];
#pragma unroll
    for (int ks = 0; ks < 2; ++ks) {
        u4 f[5];
#pragma unroll
        for (int q = 0; q < 5; ++q) { const u2 lo = trread(img + ks * 32 * 176 + q * 32), hi = trread(img + (ks * 32 + 16) * 176 + q * 32); f[q] = (u4){lo.x, lo.y, hi.x, hi.y}; }
#pragma unroll
        for (int a = 0; a < 3; ++a)
#pragma unroll
            for (int b = 0; b < 2; ++b) kv[a][b] = mfma(f[a], f[3 + b], ks ? kv[a][b] : (f4){0, 0, 0, 0});
    }
#pragma unroll
    for (int a = 0; a < 3; ++a)
#pragma unroll
        for (int b = 0; b < 2; ++b) stf4(kv[a][b], r, lane * 16, OFF_SLAB + (unsigned)((tb * 32 + 4 * hg + w) * 6 + a * 2 + b) * 1024);
    hedge(p[0], o);
#pragma unroll
    for (int q = 0; q < 5; ++q) st16((u4){o[4 * q], o[4 * q + 1], o[4 * q + 2], o[4 * q + 3]}, r, lane * 16, OFF_PHI + (unsigned)((tile * 8 + hg) * 5 + q) * 1024);
}
DEV void task_red(rsrc_t r, LAS char* lds, int h, int w, int lane) {
    f4 v[8][6];
#pragma unroll
    for (int t = 0; t < 8; ++t)
#pragma unroll
        for (int q = 0; q < 6; ++q) v[t][q] = ldf4(r, lane * 16, OFF_SLAB + (unsigned)(((8 * w + t) * 32 + h) * 6 + q) * 1024);
    memfence();
#pragma unroll
    for (int q = 0; q < 6; ++q) *(LAS f4*)(lds + (w * 6 + q) * 1024 + lane * 16) = ((v[0][q] + v[1][q]) + (v[2][q] + v[3][q])) + ((v[4][q] + v[5][q]) + (v[6][q] + v[7][q]));
    bar();
    for (int q = w; q < 6; q += 4) {
        LAS char* p = lds + q * 1024 + lane * 16;
        const f4 s = (*(LAS f4*)p + *(LAS f4*)(p + 6144)) + (*(LAS f4*)(p + 12288) + *(LAS f4*)(p + 18432));
        const int f = 16 * (q >> 1) + 4 * (lane >> 4), d = 16 * (q & 1) + (lane & 15);
        st8((u2){pk2(s[0], s[1]), pk2(s[2], s[3])}, r, (f < 40 && d < 24) ? (((d >> 2) * 5 + (f >> 3)) * 16 + (h & 3) * 4 + (d & 3)) * 16 + (f & 7) * 2 : OOB, OFF_KVC + (h >> 2) * 7680);
    }
}
DEV void task_b2(rsrc_t r, LAS char* lds, int tile, int w, int lane, u16* out) {
    const int g = lane >> 4, m = lane & 15;
    LAS char* kvl = lds + w * 15616;
    LAS char* ol = lds + 62464;
    u4 ph[10], kc[15], wo[2][20];
#pragma unroll
    for (int q = 0; q < 15; ++q) kc[q] = ld16(r, lane * 16, OFF_KVC + w * 15360 + q * 1024);
#pragma unroll
    for (int q = 0; q < 10; ++q) ph[q] = ld16(r, lane * 16, OFF_PHI + (unsigned)(tile * 40 + w * 10 + q) * 1024);
    const unsigned wb = OFF_WO2 + (unsigned)w * 200 * 1024;
#pragma unroll
    for (int k = 0; k < 20; ++k) wo[0][k] = ld16p(r, lane * 16, wb + k * 1024);
    f4 x[5][2];
    const unsigned xv = (4 * (g >> 1) + (g & 1)) * 256 + m * 16, xo = (unsigned)(tile * 20 + 5 * w) * 2048;
#pragma unroll
    for (int k = 0; k < 5; ++k) { x[k][0] = ldf4(r, xv, OFF_XB + xo + k * 2048); x[k][1] = ldf4(r, xv + 512, OFF_XB + xo + k * 2048); }
    memfence();
#pragma unroll
    for (int q = 0; q < 15; ++q) *(LAS u4*)(kvl + q * 1024 + lane * 16) = kc[q];
    *(LAS unsigned*)(kvl + 15360 + lane * 4) = 0;
    const bool act = (m >> 2) == g;
    LAS char* ka = act ? kvl + m * 16 : kvl + 15360;
    const int ks = act ? 256 : 0;
    unsigned o[20];
#pragma unroll
    for (int y = 0; y < 2; ++y) {
        f4 n[6];
#pragma unroll
        for (int t = 0; t < 6; ++t) {
            n[t] = (f4){0, 0, 0, 0};
#pragma unroll
            for (int q = 0; q < 5; ++q) n[t] = mfma(*(LAS u4*)(ka + ks * (y * 30 + t * 5 + q)), ph[5 * y + q], n[t]);
        }
        const float inv = 1.f / (n[5][0] + 1e-6f);
#pragma unroll
        for (int t = 0; t < 5; ++t) { o[10 * y + 2 * t] = pk2(n[t][0] * inv, n[t][1] * inv); o[10 * y + 2 * t + 1] = pk2(n[t][2] * inv, n[t][3] * inv); }
    }
#pragma unroll
    for (int q = 0; q < 5; ++q) *(LAS u4*)(ol + (5 * w + q) * 1024 + lane * 16) = (u4){o[4 * q], o[4 * q + 1], o[4 * q + 2], o[4 * q + 3]};
    bar();
    u4 of[20];
#pragma unroll
    for (int k = 0; k < 20; ++k) of[k] = *(LAS u4*)(ol + k * 1024 + lane * 16);
    float sum = 0.f;
#pragma unroll
    for (int c = 0; c < 10; ++c) {
        if (c < 9) {
#pragma unroll
            for (int k = 0; k < 20; ++k) wo[(c + 1) & 1][k] = ld16p(r, lane * 16, wb + ((c + 1) * 20 + k) * 1024);
        }
        memfence();
        f4 a = {0, 0, 0, 0};
#pragma unroll
        for (int k = 0; k < 20; ++k) a = mfma(wo[c & 1][k], of[k], a);
        const int e = 2 * (c & 1);
        x[c >> 1][0][e] += a[0]; x[c >> 1][0][e + 1] += a[1]; x[c >> 1][1][e] += a[2]; x[c >> 1][1][e + 1] += a[3];
    }
    if (out) {
        u16* dst = out + (size_t)(tile * 16 + m) * 640 + 160 * w + 16 * (g >> 1) + 4 * (g & 1);
#pragma unroll
        for (int k = 0; k < 5; ++k) {
            *(u2*)(dst + 32 * k) = (u2){pk2(x[k][0][0], x[k][0][1]), pk2(x[k][0][2], x[k][0][3])};
            *(u2*)(dst + 32 * k + 8) = (u2){pk2(x[k][1][0], x[k][1][1]), pk2(x[k][1][2], x[k][1][3])};
        }
        return;
    }
    LAS float* st = (LAS float*)(lds + 83968);
#pragma unroll
    for (int k = 0; k < 5; ++k) {
        stf4(x[k][0], r, xv, OFF_XA + xo + k * 2048); stf4(x[k][1], r, xv + 512, OFF_XA + xo + k * 2048);
#pragma unroll
        for (int q = 0; q < 4; ++q) sum += x[k][0][q] + x[k][1][q];
    }
    st[w * 64 + lane] = xadd32(xadd16(sum));
    bar();
    const float mu = ((st[m] + st[64 + m]) + (st[128 + m] + st[192 + m])) * (1.f / 640.f);
    float sq = 0.f;
#pragma unroll
    for (int k = 0; k < 5; ++k)
#pragma unroll
        for (int q = 0; q < 4; ++q) { x[k][0][q] -= mu; x[k][1][q] -= mu; sq += x[k][0][q] * x[k][0][q] + x[k][1][q] * x[k][1][q]; }
    st[256 + w * 64 + lane] = xadd32(xadd16(sq));
    bar();
    const float rs = rsqrtf(((st[256 + m] + st[320 + m]) + (st[384 + m] + st[448 + m])) * (1.f / 640.f) + 1e-5f);
#pragma unroll
    for (int k = 0; k < 5; ++k)
        st16((u4){pk2(x[k][0][0] * rs, x[k][0][1] * rs), pk2(x[k][1][0] * rs, x[k][1][1] * rs), pk2(x[k][0][2] * rs, x[k][0][3] * rs), pk2(x[k][1][2] * rs, x[k][1][3] * rs)}, r, lane * 16,
             OFF_XF + (unsigned)(tile * 20 + 5 * w + k) * 1024);
}
template <int KIND>
__global__ void __launch_bounds__(256, 1) run_task(char* ws, u16* out) {
    extern __shared__ __attribute__((aligned(16))) char smem[];
    rsrc_t r = __builtin_amdgcn_make_buffer_rsrc(ws, 0, WS_BYTES, 0x00020000);
    const int lane = threadIdx.x & 63, w = __builtin_amdgcn_readfirstlane(threadIdx.x >> 6), b = blockIdx.x;
    if (KIND == 0) task_tok(r, (LAS char*)smem, b / 10, b % 10, w, lane);
    if (KIND == 1) task_b1(r, (LAS char*)smem, b >> 3, b & 7, w, lane);
    if (KIND == 2) task_red(r, (LAS char*)smem, b, w, lane);
    if (KIND == 3) task_b2(r, (LAS char*)smem, b, w, lane, out);
}

__global__ void __launch_bounds__(256) prep_b(const u16* __restrict__ wqkv, const u16* __restrict__ wphi, const u16* __restrict__ wo, char* ws) {
    __shared__ float red[4][16];
    const int tid = threadIdx.x, w = tid >> 6, lane = tid & 63, g = lane >> 4, m = lane & 15, b = blockIdx.x;
    if (b < 120) {
        const int hg = b / 15, t = b % 15, part = t / 5, h = 4 * hg + (m >> 2), f = 4 * (t % 5) + (m & 3);
        float ph[20], cs = 0.f;
        for (int d = 0; d < 20; ++d) ph[d] = part < 2 ? bf2f(wphi[h * 400 + d * 20 + f]) : (d == f ? 1.f : 0.f);
        for (int kk = w; kk < 20; kk += 4) {
            float v[8];
            for (int j = 0; j < 8; ++j) {
                const u16* src = wqkv + (size_t)(32 * kk + 16 * (j >> 2) + 4 * g + (j & 3)) * 1920 + part * 640 + h * 20;
                float a = 0.f;
                for (int d = 0; d < 5; ++d) { const u2 q = *(const u2*)(src + 4 * d); a += asf(q.x << 16) * ph[4 * d] + asf(q.x & 0xffff0000u) * ph[4 * d + 1] + asf(q.y << 16) * ph[4 * d + 2] + asf(q.y & 0xffff0000u) * ph[4 * d + 3]; }
                v[j] = a;
            }
            const u4 o = {pk2(v[0], v[1]), pk2(v[2], v[3]), pk2(v[4], v[5]), pk2(v[6], v[7])};
            for (int q = 0; q < 4; ++q) cs += asf(o[q] << 16) + asf(o[q] & 0xffff0000u);
            *(u4*)(ws + OFF_WB + (size_t)((hg * 20 + kk) * 15 + t) * 1024 + lane * 16) = o;
        }
        cs = xadd32(xadd16(cs));
        if (lane < 16) red[w][lane] = cs;
        __syncthreads();
        if (tid < 16) *(float*)(ws + OFF_CS + (b * 16 + tid) * 4) = (red[0][tid] + red[1][tid]) + (red[2][tid] + red[3][tid]);
    } else {
        const int ct = b - 120, c = 32 * (ct >> 1) + dperm(m >> 2, 4 * (ct & 1) + (m & 3));
        for (int kq = w; kq < 20; kq += 4) {
            unsigned v[8];
            for (int j = 0; j < 8; ++j) { const int idx = 8 * (kq % 5) + j; v[j] = wo[(size_t)((4 * (2 * (kq / 5) + idx / 20) + g) * 20 + idx % 20) * 640 + c]; }
            *(u4*)(ws + OFF_WO2 + (size_t)(ct * 20 + kq) * 1024 + lane * 16) = (u4){v[0] | v[1] << 16, v[2] | v[3] << 16, v[4] | v[5] << 16, v[6] | v[7] << 16};
        }
    }
}

extern "C" void kernel_launch(void* const* d_in, const int* in_sizes, int n_in, void* d_out, int out_size, void* d_ws, size_t ws_size, hipStream_t stream) {
    const u16* x = (const u16*)d_in[0];
    const u16* mem0 = (const u16*)d_in[1];
    const u16 *Wq = (const u16*)d_in[2], *Wk = (const u16*)d_in[3], *Wv = (const u16*)d_in[4], *Wo = (const u16*)d_in[5];
    const u16 *wqkv = (const u16*)d_in[8], *wphi = (const u16*)d_in[9], *wo = (const u16*)d_in[10];
    char* ws = (char*)(((uintptr_t)d_ws + 0x1FFFFF) & ~(uintptr_t)0x1FFFFF);
    (void)hipFuncSetAttribute((const void*)layer_a, hipFuncAttributeMaxDynamicSharedMemorySize, LDS_BYTES);
    Tune tn = {{52,124,15,66,18,2,1,0}};
    prep_w<<<960 + 192, 256, 0, stream>>>(Wq, Wk, Wv, Wo, ws);
    gemm_b<<<640, 256, 0, stream>>>(Wv, ws);
    prep_b<<<160, 256, 0, stream>>>(wqkv, wphi, wo, ws);
    (void)hipFuncSetAttribute((const void*)run_task<1>, hipFuncAttributeMaxDynamicSharedMemorySize, 122880);
    (void)hipFuncSetAttribute((const void*)run_task<3>, hipFuncAttributeMaxDynamicSharedMemorySize, 90112);
    for (int l = 0; l < 2; ++l) {
        ln_xf<<<l ? 66 : 128 + 66, 256, 0, stream>>>(x, mem0, ws, l ? 128 : 0);
        layer_a<<<256, 256, LDS_BYTES, stream>>>(ws, tn);
        run_task<0><<<640, 256, 32768, stream>>>(ws, nullptr);
        run_task<1><<<256, 256, 122880, stream>>>(ws, nullptr);
        run_task<2><<<32, 256, 24576, stream>>>(ws, nullptr);
        run_task<3><<<128, 256, 90112, stream>>>(ws, l ? (u16*)d_out : nullptr);
    }
}
```

```cpp
#include <hip/hip_runtime.h>
#include <hip/hip_bf16.h>
#include <stdint.h>

typedef __bf16 bf2 __attribute__((ext_vector_type(2)));
typedef __bf16 bf8 __attribute__((ext_vector_type(8)));
typedef __bf16 bf16v __attribute__((ext_vector_type(16)));
typedef float f4 __attribute__((ext_vector_type(4)));
typedef unsigned u2 __attribute__((ext_vector_type(2)));
typedef unsigned u4 __attribute__((ext_vector_type(4)));
typedef unsigned u8v __attribute__((ext_vector_type(8)));
typedef short s4 __attribute__((ext_vector_type(4)));
typedef unsigned short u16;
#define LAS __attribute__((address_space(3)))
#define DEV __device__ __forceinline__
typedef __amdgpu_buffer_rsrc_t rsrc_t;

constexpr unsigned SENT = 0xFFFFFFFFu;
constexpr int SC1 = 16;
constexpr unsigned SPIN_CAP = 1u << 16;

constexpr unsigned RING = 4u;
constexpr unsigned MCOPY = 4u;
constexpr unsigned PSLOT = 49152u;
constexpr unsigned MSLOT = 32768u;
constexpr unsigned OFF_PRING = 0u;
constexpr unsigned OFF_MRING = 786432u;
constexpr unsigned OFF_SCHED = 1310720u;
constexpr unsigned RING_BYTES = 1441792u;
constexpr unsigned OFF_PROBE = 2031616u;
constexpr unsigned OFF_FLAGS = 2097152u;
constexpr unsigned FL_CLAIM = 0u;
constexpr unsigned FL_ABORT = 1024u;
constexpr unsigned FL_NEAR = 425856u;
constexpr unsigned FL_VX = 2048u;
constexpr unsigned FL_KM = 6144u;
constexpr unsigned FL_CK = 10240u;
constexpr unsigned FL_CU = 26880u;
constexpr unsigned FL_CX = 43520u;
constexpr unsigned FL_CT = 60416u;
constexpr unsigned FL_CD = 257024u;
constexpr unsigned FL_CB = 265216u;
constexpr unsigned FL_CR = 267264u;
constexpr unsigned FL_TK = 267520u;
constexpr unsigned FL_RP = 267776u;
constexpr unsigned FL_END = 400896u;
constexpr unsigned OFF_WT = 2523136u;
constexpr unsigned OFF_WOT = 26116096u;
constexpr unsigned OFF_BT = 33980416u;
constexpr unsigned OFF_XF = 60194816u;
constexpr unsigned OFF_KX = 65437696u;
constexpr unsigned OFF_VX = 90603520u;
constexpr unsigned OFF_UX = 115802112u;
constexpr unsigned OFF_VO = 199688192u;
constexpr unsigned OFF_KM = 220659712u;
constexpr unsigned OFF_PTX = 226951168u;
constexpr unsigned OFF_PTM = 231145472u;
constexpr unsigned OFF_XA = 232194048u;
constexpr unsigned OFF_XB = 237436928u;
constexpr unsigned OFF_XR = 242679808u;
constexpr unsigned OFF_WB = 245301248u;
constexpr unsigned OFF_CS = 247758848u;
constexpr unsigned OFF_WO2 = 247767040u;
constexpr unsigned OFF_PHI = 248586240u;
constexpr unsigned OFF_SLAB = 253829120u;
constexpr unsigned OFF_KVC = 260120576u;
constexpr unsigned OFF_PROF = 260186112u;
constexpr unsigned WS_BYTES = 262283264u;
constexpr unsigned OOB = 2147483632u;
constexpr unsigned NSEG = 64u;

DEV unsigned ringphys(unsigned o) { return ((o >> 13) << 14) | (o & 8191); }
DEV unsigned pk2(float a, float b) { bf2 v = {(__bf16)a, (__bf16)b}; return __builtin_bit_cast(unsigned, v); }
DEV float asf(unsigned v) { return __builtin_bit_cast(float, v); }
DEV float bf2f(u16 v) { return __builtin_bit_cast(float, (unsigned)v << 16); }
DEV f4 mfma(u4 a, u4 b, f4 c) { return __builtin_amdgcn_mfma_f32_16x16x32_bf16(__builtin_bit_cast(bf8, a), __builtin_bit_cast(bf8, b), c, 0, 0, 0); }
DEV f4 smfmac(u4 a, u8v b, f4 c, int idx) { return __builtin_amdgcn_smfmac_f32_16x16x64_bf16(__builtin_bit_cast(bf8, a), __builtin_bit_cast(bf16v, b), c, idx, 0, 0); }
DEV u4 ld16(rsrc_t r, unsigned v, unsigned s) { return __builtin_amdgcn_raw_buffer_load_b128(r, v, s, SC1); }
DEV u4 ld16p(rsrc_t r, unsigned v, unsigned s) { return __builtin_amdgcn_raw_buffer_load_b128(r, v, s, 0); }
DEV u2 ld8(rsrc_t r, unsigned v, unsigned s) { return __builtin_amdgcn_raw_buffer_load_b64(r, v, s, SC1); }
DEV unsigned ld4(rsrc_t r, unsigned v, unsigned s) { return __builtin_amdgcn_raw_buffer_load_b32(r, v, s, SC1); }
DEV void st16(u4 d, rsrc_t r, unsigned v, unsigned s) { __builtin_amdgcn_raw_buffer_store_b128(d, r, v, s, SC1); asm volatile("s_nop 1" :: "v"(d) : "memory"); }
DEV void st8(u2 d, rsrc_t r, unsigned v, unsigned s) { __builtin_amdgcn_raw_buffer_store_b64(d, r, v, s, SC1); }
DEV void st4(unsigned d, rsrc_t r, unsigned v, unsigned s) { __builtin_amdgcn_raw_buffer_store_b32(d, r, v, s, SC1); }
DEV u8v cat(u4 a, u4 b) { return __builtin_shufflevector(a, b, 0, 1, 2, 3, 4, 5, 6, 7); }
DEV unsigned umax3(unsigned a, unsigned b, unsigned c) { return max(max(a, b), c); }
DEV unsigned umax4(u4 v, unsigned m) { return umax3(umax3(v.x, v.y, v.z), v.w, m); }
DEV void memfence() { asm volatile("" ::: "memory"); __builtin_amdgcn_sched_barrier(0); }
DEV void drain() { asm volatile("s_waitcnt vmcnt(0)" ::: "memory"); }
DEV void bar() { asm volatile("s_waitcnt lgkmcnt(0)\n\ts_barrier" ::: "memory"); }
DEV unsigned xcc_id() { return (unsigned)__builtin_amdgcn_s_getreg(20 | (3 << 11)) & 0xfu; }
DEV float fold32(float x, float y) {
    auto r = __builtin_amdgcn_permlane32_swap(__builtin_bit_cast(unsigned, x), __builtin_bit_cast(unsigned, y), false, false);
    return asf(r[0]) + asf(r[1]);
}
DEV u2 trread(LAS char* p) { return __builtin_bit_cast(u2, __builtin_amdgcn_ds_read_tr16_b64_v4i16((LAS s4*)p)); }
template <int CTRL, int RM, int BM> DEV float dppf(float old, float v) {
    return __builtin_bit_cast(float, __builtin_amdgcn_update_dpp(__builtin_bit_cast(int, old), __builtin_bit_cast(int, v), CTRL, RM, BM, false));
}
DEV float xmax16(float v) { auto r = __builtin_amdgcn_permlane16_swap(__builtin_bit_cast(unsigned, v), __builtin_bit_cast(unsigned, v), false, false); return fmaxf(asf(r[0]), asf(r[1])); }
DEV float xmax32(float v) { auto r = __builtin_amdgcn_permlane32_swap(__builtin_bit_cast(unsigned, v), __builtin_bit_cast(unsigned, v), false, false); return fmaxf(asf(r[0]), asf(r[1])); }
DEV float xadd16(float v) { auto r = __builtin_amdgcn_permlane16_swap(__builtin_bit_cast(unsigned, v), __builtin_bit_cast(unsigned, v), false, false); return asf(r[0]) + asf(r[1]); }
DEV float xadd32(float v) { auto r = __builtin_amdgcn_permlane32_swap(__builtin_bit_cast(unsigned, v), __builtin_bit_cast(unsigned, v), false, false); return asf(r[0]) + asf(r[1]); }

DEV int dperm(int g, int j) { return 16 * (g >> 1) + 8 * ((j >> 1) & 1) + 4 * (g & 1) + 2 * (j >> 2) + (j & 1); }

__global__ void __launch_bounds__(256) prep_w(const u16* __restrict__ Wq, const u16* __restrict__ Wk, const u16* __restrict__ Wv, const u16* __restrict__ Wo, char* ws) {
    __shared__ __attribute__((aligned(16))) u16 tile[32 * 648];
    const int tid = threadIdx.x, b = blockIdx.x;
    if (b < 960) {
        const int mat = b / 320, h = (b % 320) / 10, t = b % 10;
        const u16* W = mat == 0 ? Wq : (mat == 1 ? Wk : Wv);
        for (int c = tid; c < 64 * 24; c += 256) { const int row = c / 24, ch = c % 24; *(u4*)(tile + row * 200 + ch * 8) = *(const u4*)(W + (size_t)(64 * t + row) * 6144 + h * 192 + ch * 8); }
        __syncthreads();
        u4* out = (u4*)(ws + OFF_WT);
        for (int f = tid; f < 12 * 128; f += 256) {
            const int et = f >> 7, u = (f >> 6) & 1, lane = f & 63, g = lane >> 4, n = lane & 15;
            unsigned w[4];
            for (int q = 0; q < 4; ++q) w[q] = tile[(32 * u + dperm(g, 2 * q)) * 200 + 16 * et + n] | ((unsigned)tile[(32 * u + dperm(g, 2 * q + 1)) * 200 + 16 * et + n] << 16);
            out[((size_t)(((mat * 32 + h) * 12 + et) * 10 + t) * 2 + u) * 64 + lane] = (u4){w[0], w[1], w[2], w[3]};
        }
    } else {
        const int h = (b - 960) / 6, ks = (b - 960) % 6;
        for (int c = tid; c < 32 * 80; c += 256) { const int row = c / 80, ch = c % 80; *(u4*)(tile + row * 648 + ch * 8) = *(const u4*)(Wo + (size_t)(h * 192 + 32 * ks + row) * 640 + ch * 8); }
        __syncthreads();
        u4* out = (u4*)(ws + OFF_WOT);
        for (int f = tid; f < 40 * 64; f += 256) {
            const int ct = f >> 6, lane = f & 63, g = lane >> 4, n = lane & 15;
            unsigned w[4];
            for (int q = 0; q < 4; ++q) { const int e = 16 * (q >> 1) + 4 * g + 2 * (q & 1); w[q] = tile[e * 648 + 16 * ct + n] | ((unsigned)tile[(e + 1) * 648 + 16 * ct + n] << 16); }
            out[(size_t)((h * 40 + ct) * 6 + ks) * 64 + lane] = (u4){w[0], w[1], w[2], w[3]};
        }
    }
}

__global__ void __launch_bounds__(256) gemm_b(const u16* __restrict__ Wv, char* ws) {
    const int h = blockIdx.x / 20, U = blockIdx.x % 20, tid = threadIdx.x, w = tid >> 6, lane = tid & 63, g = lane >> 4, m = lane & 15;
    u4 a[2][6];
    for (int x = 0; x < 2; ++x) {
        const int d = 32 * U + dperm(m >> 2, 4 * x + (m & 3));
        const u16* src = Wv + (size_t)d * 6144 + h * 192 + 4 * g;
        for (int ks = 0; ks < 6; ++ks) { const u2 lo = *(const u2*)(src + 32 * ks), hi = *(const u2*)(src + 32 * ks + 16); a[x][ks] = (u4){lo.x, lo.y, hi.x, hi.y}; }
    }
    const u4* wot = (const u4*)(ws + OFF_WOT);
    u4* out = (u4*)(ws + OFF_BT);
    for (int c = 0; c < 10; ++c) {
        const int ct = 10 * w + c;
        f4 acc0 = {0, 0, 0, 0}, acc1 = {0, 0, 0, 0};
        for (int ks = 0; ks < 6; ++ks) { const u4 bb = wot[(size_t)((h * 40 + ct) * 6 + ks) * 64 + lane]; acc0 = mfma(a[0][ks], bb, acc0); acc1 = mfma(a[1][ks], bb, acc1); }
        const int slice = 2 * ct + (m >> 3);
        out[((size_t)(slice * 16 + (h >> 1)) * 20 + U) * 64 + g * 16 + (h & 1) * 8 + (m & 7)] = (u4){pk2(acc0[0], acc0[1]), pk2(acc0[2], acc0[3]), pk2(acc1[0], acc1[1]), pk2(acc1[2], acc1[3])};
    }
}

__global__ void __launch_bounds__(256) ln_xf(const u16* __restrict__ x, const u16* __restrict__ mem0, char* ws, int first) {
    __shared__ __attribute__((aligned(16))) u16 xn[16 * 648];
    const int tid = threadIdx.x, w = tid >> 6, lane = tid & 63, b = blockIdx.x + first;
    rsrc_t r = __builtin_amdgcn_make_buffer_rsrc(ws, 0, WS_BYTES, 0x00020000);
    if (b >= 128) {
        const int q = b - 128;
        if (q < 64) {
            for (unsigned o = (q * 256 + tid) * 16; o < RING_BYTES; o += 64 * 256 * 16) {
                u4 v = {SENT, SENT, SENT, SENT};
                if (o >= OFF_MRING && o < OFF_MRING + MCOPY * MSLOT) {
                    const unsigned p = (o - OFF_MRING) % MSLOT, lo = ((p >> 14) << 13) | (p & 8191);
                    if (!(p & 8192) && lo < 10240) v = *(const u4*)(mem0 + ((lo >> 4) & 7) * 640 + (lo >> 7) * 8);
                }
                st16(v, r, o, 0);
            }
            for (unsigned o = (q * 256 + tid) * 16; o < FL_END; o += 64 * 4096) st16((u4){(o >= FL_CX && o < FL_CX + 64 * 128) ? 2u : 0u, 0, 0, 0}, r, o, OFF_FLAGS);
        } else if (w == 0) {
            unsigned best[2] = {1u << 30, 1u << 30};
            for (int rep = 0; rep < 8; ++rep) for (int c = 0; c < 2; ++c) {
                drain();
                const unsigned long long t0 = __builtin_amdgcn_s_memtime();
                asm volatile("s_waitcnt lgkmcnt(0)" ::: "memory");
                st16((u4){0, 0, 0, 0}, r, lane < 8 ? lane * 16 : OOB, OFF_PROBE + c * 8192 + (rep & 1) * 4096);
                drain();
                const unsigned d = (unsigned)(__builtin_amdgcn_s_memtime() - t0);
                best[c] = min(best[c], d);
            }
            st4((xcc_id() >> 2) ^ (best[0] > best[1] ? 1u : 0u), r, lane == 0 ? 0 : OOB, OFF_FLAGS + FL_NEAR);
        }
        return;
    }
    for (int q = 0; q < 4; ++q) {
        const int m = w * 4 + q, tok = b * 16 + m;
        float v[10], sum = 0.f;
        for (int j = 0; j < 10; ++j) { v[j] = bf2f(x[(size_t)tok * 640 + j * 64 + lane]); sum += v[j]; }
        for (int o = 32; o; o >>= 1) sum += __shfl_xor(sum, o);
        const float mean = sum * (1.f / 640.f);
        float var = 0.f;
        for (int j = 0; j < 10; ++j) { const float d = v[j] - mean; var += d * d; }
        for (int o = 32; o; o >>= 1) var += __shfl_xor(var, o);
        const float rs = rsqrtf(var * (1.f / 640.f) + 1e-5f);
        for (int j = 0; j < 10; ++j) {
            const u16 y = (u16)pk2((v[j] - mean) * rs, 0.f);
            xn[m * 648 + j * 64 + lane] = y;
            *(float*)(ws + OFF_XA + ((size_t)(b * 160 + j * 16 + (lane >> 2)) * 16 + m) * 16 + (lane & 3) * 4) = v[j];
        }
    }
    __syncthreads();
    u4* out = (u4*)(ws + OFF_XF);
    for (int f = tid; f < 20 * 64; f += 256) {
        const int kk = f >> 6, l = f & 63, g = l >> 4, m = l & 15;
        const unsigned* row = (const unsigned*)(xn + m * 648 + 32 * kk + 16 * (g >> 1) + 4 * (g & 1));
        out[(size_t)(b * 20 + kk) * 64 + l] = (u4){row[0], row[4], row[1], row[5]};
    }
}

constexpr int LDS_BYTES = 152 * 1024 + 64;
struct Tune { int p[8]; };
#define CHAIN_CLOBBERS "v0", "v1", "v2", "v3", "v4", "v5", "v6", "v7", "v8", "v9", "v10", "v11", "v12", "v13", "v14", "v15", "v16", "v17", "v18", "v19", "v20", "v21", "v22", "v23", "v24", "v25", "v26", "v27", "v28", "v29", "v30", "v31", "v32", "v33", "v34", "v35", "v36", "v37", "v38", "v39", "v40", "v41", "v42", "v43", "v44", "v45", "v46", "v47", "v48", "v49", "v50", "v51", "v52", "v53", "v54", "v55", "v56", "v57", "v58", "v59", "v60", "v61", "v62", "v63", "v64", "v65", "v66", "v67", "v68", "v69", "v70", "v71", "v72", "v73", "v74", "v75", "v76", "v77", "v78", "v79", "v80", "v81", "v82", "v83", "v84", "v85", "v86", "v87", "v88", "v89", "v90", "v91", "v92", "v93", "v94", "v95", "v96", "v97", "v98", "v99", "v100", "v101", "v102", "v103", "v104", "v105", "v106", "v107", "v108", "v109", "v110", "v111", "v112", "v113", "v114", "v115", "v116", "v117", "v118", "v119", "v120", "v121", "v122", "v123", "v124", "v125", "v126", "v127", "v128", "v129", "v130", "v131", "v132", "v133", "v134", "v135", "v136", "v137", "v138", "v139", "v140", "v141", "v142", "v143", "v144", "v145", "v146", "v147", "v148", "v149", "v150", "v151", "v152", "v153", "v154", "v155", "v156", "v157", "v158", "v159", "v160", "v161", "v162", "v163", "v164", "v165", "v166", "v167", "v168", "v169", "v170", "v171", "v172", "v173", "v174", "v175", "v176", "v177", "v178", "v179", "v180", "v181", "v182", "v183", "v184", "v185", "v186", "v187", "v188", "v189", "v190", "v191", "v192", "v193", "v194", "v195", "v196", "v197", "v198", "v199", "v200", "v201", "v202", "v203", "v204", "v205", "v206", "v207", "v208", "v209", "v210", "v211", "v212", "v213", "v214", "v215", "v216", "v217", "v218", "v219", "v220", "v221", "v222", "v223", "v224", "v225", "v226", "v227", "v228", "v229", "v230", "v231", "v232", "v233", "v234", "v235", "v236", "v237", "v238", "v239", "v240", "v241", "v242", "v243", "v244", "v245", "v246", "v247", "v248", "v249", "v250", "v251", "v252", "v253", "v254", "v255", "a0", "a1", "a2", "a3", "a4", "a5", "a6", "a7", "a8", "a9", "a10", "a11", "a12", "a13", "a14", "a15", "a16", "a17", "a18", "a19", "a20", "a21", "a22", "a23", "a24", "a25", "a26", "a27", "a28", "a29", "a30", "a31", "a32", "a33", "a34", "a35", "a36", "a37", "a38", "a39", "a40", "a41", "a42", "a43", "a44", "a45", "a46", "a47", "a48", "a49", "a50", "a51", "a52", "a53", "a54", "a55", "a56", "a57", "a58", "a59", "a60", "a61", "a62", "a63", "a64", "a65", "a66", "a67", "a68", "a69", "a70", "a71", "a72", "a73", "a74", "a75", "a76", "a77", "a78", "a79", "a80", "a81", "a82", "a83", "a84", "a85", "a86", "a87", "a88", "a89", "a90", "a91", "a92", "a93", "a94", "a95", "a96", "a97", "a98", "a99", "a100", "a101", "a102", "a103", "a104", "a105", "a106", "a107", "a108", "a109", "a110", "a111", "a112", "a113", "a114", "a115", "a116", "a117", "a118", "a119", "a120", "a121", "a122", "a123", "a124", "a125", "a126", "a127", "a128", "a129", "a130", "a131", "a132", "a133", "a134", "a135", "a136", "a137", "a138", "a139", "a140", "a141", "a142", "a143", "a144", "a145", "a146", "a147", "a148", "a149", "a150", "a151", "a152", "a153", "a154", "a155", "a156", "a157", "a158", "a159", "a160", "a161", "a162", "a163", "a164", "a165", "a166", "a167", "a168", "a169", "a170", "a171", "a172", "a173", "a174", "a175", "a176", "a177", "a178", "a179", "a180", "a181", "a182", "a183", "a184", "a185", "a186", "a187", "a188", "a189", "a190", "a191", "a192", "a193", "a194", "a195", "a196", "a197", "a198", "a199", "a200", "a201", "a202", "a203", "a204", "a205", "a206", "a207", "a208", "a209", "a210", "a211", "a212", "a213", "a214", "a215", "a216", "a217", "a218", "a219", "a220", "a221", "a222", "a223", "a224", "a225", "a226", "a227", "a228", "a229", "a230", "a231", "a232", "a233", "a234", "a235", "a236", "a237", "a238", "a239", "a240", "a241", "a242", "a243", "a244", "a245", "a246", "a247", "a248", "a249", "a250", "a251", "a252", "a253", "a254", "a255", "s40", "s41", "s42", "s43", "s44", "s45", "s46", "s47", "s66", "s67", "s68", "s69", "s70", "s71", "s72", "s73", "s99", "vcc", "scc", "memory"
#define SRD_INPUTS "s"(lo), "s"(hi), "s"(WS_BYTES), "s"(0x00020000u), "s"(id), "s"(w), "s"(aux)
#define CHAIN_INPUTS SRD_INPUTS, "s"(tn.p[0]), "s"(tn.p[1]), "s"(tn.p[2]), "s"(tn.p[3]), "s"(tn.p[4]), "s"(tn.p[5]), "s"(tn.p[6]), "s"(tn.p[7])
DEV void role_head(unsigned lo, unsigned hi, int id, int w, int aux, const Tune& tn) {
    asm volatile(
    "s_mov_b32 s40, %0\n"
    "s_mov_b32 s41, %1\n"
    "s_mov_b32 s42, %2\n"
    "s_mov_b32 s43, %3\n"
    "s_mov_b32 s44, %4\n"
    "s_mov_b32 s45, %5\n"
    "s_mov_b32 s46, %6\n"
    "s_mov_b32 s66, %7\n"
    "s_mov_b32 s67, %8\n"
    "s_mov_b32 s68, %9\n"
    "s_mov_b32 s69, %10\n"
    "s_mov_b32 s70, %11\n"
    "s_mov_b32 s71, %12\n"
    "s_mov_b32 s72, %13\n"
    "s_mov_b32 s73, %14\n"
    "s_mov_b64 exec, -1\n"
    "v_mbcnt_lo_u32_b32 v15, -1, 0\n"
    "v_mbcnt_hi_u32_b32 v15, -1, v15\n"
    "v_lshlrev_b32 v0, 4, v15\n"
    "s_mov_b32 s58, 2097152\n"
    "s_mov_b32 s92, 0\n"
    "s_lshl_b32 s48, s45, 15\n"
    "v_add_u32 v2, s48, v0\n"
    "s_lshl_b32 s50, s44, 2\n"
    "s_add_u32 s50, s50, s45\n"
    "s_mul_i32 s50, s50, 3\n"
    "s_mul_i32 s51, s50, 6554\n"
    "s_lshr_b32 s51, s51, 16\n"
    "s_mul_i32 s51, s51, 10\n"
    "s_sub_u32 s50, s50, s51\n"
    "s_lshl_b32 s20, s50, 10\n"
    "s_add_u32 s50, s50, 1\n"
    "s_cmp_eq_u32 s50, 10\n"
    "s_cselect_b32 s50, 0, s50\n"
    "s_lshl_b32 s21, s50, 10\n"
    "s_add_u32 s50, s50, 1\n"
    "s_cmp_eq_u32 s50, 10\n"
    "s_cselect_b32 s50, 0, s50\n"
    "s_lshl_b32 s22, s50, 10\n"
    "s_add_u32 s50, s50, 1\n"
    "s_cmp_eq_u32 s50, 10\n"
    "s_cselect_b32 s50, 0, s50\n"
    "s_lshl_b32 s23, s50, 10\n"
    "s_add_u32 s50, s50, 1\n"
    "s_cmp_eq_u32 s50, 10\n"
    "s_cselect_b32 s50, 0, s50\n"
    "s_lshl_b32 s24, s50, 10\n"
    "s_add_u32 s50, s50, 1\n"
    "s_cmp_eq_u32 s50, 10\n"
    "s_cselect_b32 s50, 0, s50\n"
    "s_lshl_b32 s25, s50, 10\n"
    "s_add_u32 s50, s50, 1\n"
    "s_cmp_eq_u32 s50, 10\n"
    "s_cselect_b32 s50, 0, s50\n"
    "s_lshl_b32 s26, s50, 10\n"
    "s_add_u32 s50, s50, 1\n"
    "s_cmp_eq_u32 s50, 10\n"
    "s_cselect_b32 s50, 0, s50\n"
    "s_lshl_b32 s27, s50, 10\n"
    "s_add_u32 s50, s50, 1\n"
    "s_cmp_eq_u32 s50, 10\n"
    "s_cselect_b32 s50, 0, s50\n"
    "s_lshl_b32 s28, s50, 10\n"
    "s_add_u32 s50, s50, 1\n"
    "s_cmp_eq_u32 s50, 10\n"
    "s_cselect_b32 s50, 0, s50\n"
    "s_lshl_b32 s29, s50, 10\n"
    "s_add_u32 s50, s50, 1\n"
    "s_cmp_eq_u32 s50, 10\n"
    "s_cselect_b32 s50, 0, s50\n"
    "s_mul_i32 s48, s44, 12\n"
    "s_mul_i32 s49, s45, 3\n"
    "s_add_u32 s48, s48, s49\n"
    "s_add_u32 s48, s48, 0\n"
    "s_mul_i32 s48, s48, 20480\n"
    "s_add_u32 s48, s48, 2523136\n"
    "s_lshl1_add_u32 s49, s20, s48\n"
    "buffer_load_dwordx4 a[0:3], v0, s[40:43], s49 offen\n"
    "buffer_load_dwordx4 a[4:7], v0, s[40:43], s49 offen offset:1024\n"
    "s_lshl1_add_u32 s49, s21, s48\n"
    "buffer_load_dwordx4 a[24:27], v0, s[40:43], s49 offen\n"
    "buffer_load_dwordx4 a[28:31], v0, s[40:43], s49 offen offset:1024\n"
    "s_lshl1_add_u32 s49, s22, s48\n"
    "buffer_load_dwordx4 a[40:43], v0, s[40:43], s49 offen\n"
    "buffer_load_dwordx4 a[44:47], v0, s[40:43], s49 offen offset:1024\n"
    "s_lshl1_add_u32 s49, s23, s48\n"
    "buffer_load_dwordx4 a[64:67], v0, s[40:43], s49 offen\n"
    "buffer_load_dwordx4 a[68:71], v0, s[40:43], s49 offen offset:1024\n"
    "s_lshl1_add_u32 s49, s24, s48\n"
    "buffer_load_dwordx4 a[88:91], v0, s[40:43], s49 offen\n"
    "buffer_load_dwordx4 a[92:95], v0, s[40:43], s49 offen offset:1024\n"
    "s_lshl1_add_u32 s49, s25, s48\n"
    "buffer_load_dwordx4 a[104:107], v0, s[40:43], s49 offen\n"
    "buffer_load_dwordx4 a[108:111], v0, s[40:43], s49 offen offset:1024\n"
    "s_lshl1_add_u32 s49, s26, s48\n"
    "buffer_load_dwordx4 a[128:131], v0, s[40:43], s49 offen\n"
    "buffer_load_dwordx4 a[132:135], v0, s[40:43], s49 offen offset:1024\n"
    "s_lshl1_add_u32 s49, s27, s48\n"
    "buffer_load_dwordx4 a[152:155], v0, s[40:43], s49 offen\n"
    "buffer_load_dwordx4 a[156:159], v0, s[40:43], s49 offen offset:1024\n"
    "s_lshl1_add_u32 s49, s28, s48\n"
    "buffer_load_dwordx4 a[168:171], v0, s[40:43], s49 offen\n"
    "buffer_load_dwordx4 a[172:175], v0, s[40:43], s49 offen offset:1024\n"
    "s_lshl1_add_u32 s49, s29, s48\n"
    "buffer_load_dwordx4 a[192:195], v0, s[40:43], s49 offen\n"
    "buffer_load_dwordx4 a[196:199], v0, s[40:43], s49 offen offset:1024\n"
    "s_mul_i32 s48, s44, 12\n"
    "s_mul_i32 s49, s45, 3\n"
    "s_add_u32 s48, s48, s49\n"
    "s_add_u32 s48, s48, 384\n"
    "s_mul_i32 s48, s48, 20480\n"
    "s_add_u32 s48, s48, 2523136\n"
    "s_lshl1_add_u32 s49, s20, s48\n"
    "buffer_load_dwordx4 a[16:19], v0, s[40:43], s49 offen\n"
    "buffer_load_dwordx4 a[20:23], v0, s[40:43], s49 offen offset:1024\n"
    "s_lshl1_add_u32 s49, s21, s48\n"
    "buffer_load_dwordx4 a[32:35], v0, s[40:43], s49 offen\n"
    "buffer_load_dwordx4 a[36:39], v0, s[40:43], s49 offen offset:1024\n"
    "s_lshl1_add_u32 s49, s22, s48\n"
    "buffer_load_dwordx4 a[56:59], v0, s[40:43], s49 offen\n"
    "buffer_load_dwordx4 a[60:63], v0, s[40:43], s49 offen offset:1024\n"
    "s_lshl1_add_u32 s49, s23, s48\n"
    "buffer_load_dwordx4 a[80:83], v0, s[40:43], s49 offen\n"
    "buffer_load_dwordx4 a[84:87], v0, s[40:43], s49 offen offset:1024\n"
    "s_lshl1_add_u32 s49, s24, s48\n"
    "buffer_load_dwordx4 a[96:99], v0, s[40:43], s49 offen\n"
    "buffer_load_dwordx4 a[100:103], v0, s[40:43], s49 offen offset:1024\n"
    "s_lshl1_add_u32 s49, s25, s48\n"
    "buffer_load_dwordx4 a[120:123], v0, s[40:43], s49 offen\n"
    "buffer_load_dwordx4 a[124:127], v0, s[40:43], s49 offen offset:1024\n"
    "s_lshl1_add_u32 s49, s26, s48\n"
    "buffer_load_dwordx4 a[144:147], v0, s[40:43], s49 offen\n"
    "buffer_load_dwordx4 a[148:151], v0, s[40:43], s49 offen offset:1024\n"
    "s_lshl1_add_u32 s49, s27, s48\n"
    "buffer_load_dwordx4 a[160:163], v0, s[40:43], s49 offen\n"
    "buffer_load_dwordx4 a[164:167], v0, s[40:43], s49 offen offset:1024\n"
    "s_lshl1_add_u32 s49, s28, s48\n"
    "buffer_load_dwordx4 a[184:187], v0, s[40:43], s49 offen\n"
    "buffer_load_dwordx4 a[188:191], v0, s[40:43], s49 offen offset:1024\n"
    "s_lshl1_add_u32 s49, s29, s48\n"
    "buffer_load_dwordx4 a[208:211], v0, s[40:43], s49 offen\n"
    "buffer_load_dwordx4 a[212:215], v0, s[40:43], s49 offen offset:1024\n"
    "s_mul_i32 s48, s44, 12\n"
    "s_mul_i32 s49, s45, 3\n"
    "s_add_u32 s48, s48, s49\n"
    "s_add_u32 s48, s48, 1\n"
    "s_mul_i32 s48, s48, 20480\n"
    "s_add_u32 s48, s48, 2523136\n"
    "s_lshl1_add_u32 s49, s20, s48\n"
    "buffer_load_dwordx4 a[8:11], v0, s[40:43], s49 offen\n"
    "buffer_load_dwordx4 a[12:15], v0, s[40:43], s49 offen offset:1024\n"
    "s_lshl1_add_u32 s49, s21, s48\n"
    "buffer_load_dwordx4 v[32:35], v0, s[40:43], s49 offen\n"
    "buffer_load_dwordx4 v[36:39], v0, s[40:43], s49 offen offset:1024\n"
    "s_lshl1_add_u32 s49, s22, s48\n"
    "buffer_load_dwordx4 a[48:51], v0, s[40:43], s49 offen\n"
    "buffer_load_dwordx4 a[52:55], v0, s[40:43], s49 offen offset:1024\n"
    "s_lshl1_add_u32 s49, s23, s48\n"
    "buffer_load_dwordx4 a[72:75], v0, s[40:43], s49 offen\n"
    "buffer_load_dwordx4 a[76:79], v0, s[40:43], s49 offen offset:1024\n"
    "s_lshl1_add_u32 s49, s24, s48\n"
    "buffer_load_dwordx4 v[64:67], v0, s[40:43], s49 offen\n"
    "buffer_load_dwordx4 v[68:71], v0, s[40:43], s49 offen offset:1024\n"
    "s_lshl1_add_u32 s49, s25, s48\n"
    "buffer_load_dwordx4 a[112:115], v0, s[40:43], s49 offen\n"
    "buffer_load_dwordx4 a[116:119], v0, s[40:43], s49 offen offset:1024\n"
    "s_lshl1_add_u32 s49, s26, s48\n"
    "buffer_load_dwordx4 a[136:139], v0, s[40:43], s49 offen\n"
    "buffer_load_dwordx4 a[140:143], v0, s[40:43], s49 offen offset:1024\n"
    "s_lshl1_add_u32 s49, s27, s48\n"
    "buffer_load_dwordx4 v[96:99], v0, s[40:43], s49 offen\n"
    "buffer_load_dwordx4 v[100:103], v0, s[40:43], s49 offen offset:1024\n"
    "s_lshl1_add_u32 s49, s28, s48\n"
    "buffer_load_dwordx4 a[176:179], v0, s[40:43], s49 offen\n"
    "buffer_load_dwordx4 a[180:183], v0, s[40:43], s49 offen offset:1024\n"
    "s_lshl1_add_u32 s49, s29, s48\n"
    "buffer_load_dwordx4 a[200:203], v0, s[40:43], s49 offen\n"
    "buffer_load_dwordx4 a[204:207], v0, s[40:43], s49 offen offset:1024\n"
    "s_mul_i32 s48, s44, 12\n"
    "s_mul_i32 s49, s45, 3\n"
    "s_add_u32 s48, s48, s49\n"
    "s_add_u32 s48, s48, 385\n"
    "s_mul_i32 s48, s48, 20480\n"
    "s_add_u32 s48, s48, 2523136\n"
    "s_lshl1_add_u32 s49, s20, s48\n"
    "buffer_load_dwordx4 v[16:19], v0, s[40:43], s49 offen\n"
    "buffer_load_dwordx4 v[20:23], v0, s[40:43], s49 offen offset:1024\n"
    "s_lshl1_add_u32 s49, s21, s48\n"
    "buffer_load_dwordx4 v[24:27], v0, s[40:43], s49 offen\n"
    "s_waitcnt vmcnt(61)\n"
    "buffer_load_dwordx4 v[28:31], v0, s[40:43], s49 offen offset:1024\n"
    "s_lshl1_add_u32 s49, s22, s48\n"
    "buffer_load_dwordx4 v[40:43], v0, s[40:43], s49 offen\n"
    "s_waitcnt vmcnt(61)\n"
    "buffer_load_dwordx4 v[44:47], v0, s[40:43], s49 offen offset:1024\n"
    "s_lshl1_add_u32 s49, s23, s48\n"
    "buffer_load_dwordx4 v[48:51], v0, s[40:43], s49 offen\n"
    "s_waitcnt vmcnt(61)\n"
    "buffer_load_dwordx4 v[52:55], v0, s[40:43], s49 offen offset:1024\n"
    "s_lshl1_add_u32 s49, s24, s48\n"
    "buffer_load_dwordx4 v[56:59], v0, s[40:43], s49 offen\n"
    "s_waitcnt vmcnt(61)\n"
    "buffer_load_dwordx4 v[60:63], v0, s[40:43], s49 offen offset:1024\n"
    "s_lshl1_add_u32 s49, s25, s48\n"
    "buffer_load_dwordx4 v[72:75], v0, s[40:43], s49 offen\n"
    "s_waitcnt vmcnt(61)\n"
    "buffer_load_dwordx4 v[76:79], v0, s[40:43], s49 offen offset:1024\n"
    "s_lshl1_add_u32 s49, s26, s48\n"
    "buffer_load_dwordx4 v[80:83], v0, s[40:43], s49 offen\n"
    "s_waitcnt vmcnt(61)\n"
    "buffer_load_dwordx4 v[84:87], v0, s[40:43], s49 offen offset:1024\n"
    "s_lshl1_add_u32 s49, s27, s48\n"
    "buffer_load_dwordx4 v[88:91], v0, s[40:43], s49 offen\n"
    "s_waitcnt vmcnt(61)\n"
    "buffer_load_dwordx4 v[92:95], v0, s[40:43], s49 offen offset:1024\n"
    "s_lshl1_add_u32 s49, s28, s48\n"
    "buffer_load_dwordx4 v[104:107], v0, s[40:43], s49 offen\n"
    "s_waitcnt vmcnt(61)\n"
    "buffer_load_dwordx4 v[108:111], v0, s[40:43], s49 offen offset:1024\n"
    "s_lshl1_add_u32 s49, s29, s48\n"
    "buffer_load_dwordx4 v[112:115], v0, s[40:43], s49 offen\n"
    "s_waitcnt vmcnt(61)\n"
    "buffer_load_dwordx4 v[116:119], v0, s[40:43], s49 offen offset:1024\n"
    "s_mul_i32 s48, s44, 12\n"
    "s_mul_i32 s49, s45, 3\n"
    "s_add_u32 s48, s48, s49\n"
    "s_add_u32 s48, s48, 2\n"
    "s_mul_i32 s48, s48, 20480\n"
    "s_add_u32 s48, s48, 2523136\n"
    "s_lshl1_add_u32 s49, s20, s48\n"
    "buffer_load_dwordx4 a[216:219], v0, s[40:43], s49 offen\n"
    "s_waitcnt vmcnt(61)\n"
    "buffer_load_dwordx4 a[220:223], v0, s[40:43], s49 offen offset:1024\n"
    "s_lshl1_add_u32 s49, s21, s48\n"
    "buffer_load_dwordx4 a[224:227], v0, s[40:43], s49 offen\n"
    "s_waitcnt vmcnt(61)\n"
    "buffer_load_dwordx4 a[228:231], v0, s[40:43], s49 offen offset:1024\n"
    "s_lshl1_add_u32 s49, s22, s48\n"
    "buffer_load_dwordx4 a[232:235], v0, s[40:43], s49 offen\n"
    "s_waitcnt vmcnt(61)\n"
    "buffer_load_dwordx4 a[236:239], v0, s[40:43], s49 offen offset:1024\n"
    "s_lshl1_add_u32 s49, s23, s48\n"
    "buffer_load_dwordx4 a[240:243], v0, s[40:43], s49 offen\n"
    "s_waitcnt vmcnt(61)\n"
    "buffer_load_dwordx4 a[244:247], v0, s[40:43], s49 offen offset:1024\n"
    "s_lshl1_add_u32 s49, s24, s48\n"
    "buffer_load_dwordx4 v[160:163], v0, s[40:43], s49 offen\n"
    "s_waitcnt vmcnt(61)\n"
    "buffer_load_dwordx4 v[164:167], v0, s[40:43], s49 offen offset:1024\n"
    "s_lshl1_add_u32 s49, s25, s48\n"
    "buffer_load_dwordx4 v[176:179], v0, s[40:43], s49 offen\n"
    "s_waitcnt vmcnt(61)\n"
    "buffer_load_dwordx4 v[180:183], v0, s[40:43], s49 offen offset:1024\n"
    "s_lshl1_add_u32 s49, s26, s48\n"
    "buffer_load_dwordx4 v[192:195], v0, s[40:43], s49 offen\n"
    "s_waitcnt vmcnt(61)\n"
    "buffer_load_dwordx4 v[196:199], v0, s[40:43], s49 offen offset:1024\n"
    "s_lshl1_add_u32 s49, s27, s48\n"
    "buffer_load_dwordx4 v[208:211], v0, s[40:43], s49 offen\n"
    "s_waitcnt vmcnt(61)\n"
    "buffer_load_dwordx4 v[212:215], v0, s[40:43], s49 offen offset:1024\n"
    "s_lshl1_add_u32 s49, s28, s48\n"
    "buffer_load_dwordx4 v[224:227], v0, s[40:43], s49 offen\n"
    "s_waitcnt vmcnt(61)\n"
    "buffer_load_dwordx4 v[228:231], v0, s[40:43], s49 offen offset:1024\n"
    "s_lshl1_add_u32 s49, s29, s48\n"
    "buffer_load_dwordx4 v[240:243], v0, s[40:43], s49 offen\n"
    "s_waitcnt vmcnt(61)\n"
    "buffer_load_dwordx4 v[244:247], v0, s[40:43], s49 offen offset:1024\n"
    "s_mul_i32 s48, s44, 12\n"
    "s_mul_i32 s49, s45, 3\n"
    "s_add_u32 s48, s48, s49\n"
    "s_add_u32 s48, s48, 386\n"
    "s_mul_i32 s48, s48, 20480\n"
    "s_add_u32 s48, s48, 2523136\n"
    "s_lshl1_add_u32 s49, s20, s48\n"
    "buffer_load_dwordx4 v[120:123], v0, s[40:43], s49 offen\n"
    "s_waitcnt vmcnt(61)\n"
    "buffer_load_dwordx4 v[124:127], v0, s[40:43], s49 offen offset:1024\n"
    "s_lshl1_add_u32 s49, s21, s48\n"
    "buffer_load_dwordx4 v[128:131], v0, s[40:43], s49 offen\n"
    "s_waitcnt vmcnt(61)\n"
    "buffer_load_dwordx4 v[132:135], v0, s[40:43], s49 offen offset:1024\n"
    "s_lshl1_add_u32 s49, s22, s48\n"
    "buffer_load_dwordx4 v[136:139], v0, s[40:43], s49 offen\n"
    "s_waitcnt vmcnt(61)\n"
    "buffer_load_dwordx4 v[140:143], v0, s[40:43], s49 offen offset:1024\n"
    "s_lshl1_add_u32 s49, s23, s48\n"
    "buffer_load_dwordx4 a[248:251], v0, s[40:43], s49 offen\n"
    "s_waitcnt vmcnt(61)\n"
    "buffer_load_dwordx4 a[252:255], v0, s[40:43], s49 offen offset:1024\n"
    "s_lshl1_add_u32 s49, s24, s48\n"
    "buffer_load_dwordx4 v[168:171], v0, s[40:43], s49 offen\n"
    "s_waitcnt vmcnt(61)\n"
    "buffer_load_dwordx4 v[172:175], v0, s[40:43], s49 offen offset:1024\n"
    "s_lshl1_add_u32 s49, s25, s48\n"
    "buffer_load_dwordx4 v[184:187], v0, s[40:43], s49 offen\n"
    "s_waitcnt vmcnt(61)\n"
    "buffer_load_dwordx4 v[188:191], v0, s[40:43], s49 offen offset:1024\n"
    "s_lshl1_add_u32 s49, s26, s48\n"
    "buffer_load_dwordx4 v[200:203], v0, s[40:43], s49 offen\n"
    "s_waitcnt vmcnt(61)\n"
    "buffer_load_dwordx4 v[204:207], v0, s[40:43], s49 offen offset:1024\n"
    "s_lshl1_add_u32 s49, s27, s48\n"
    "buffer_load_dwordx4 v[216:219], v0, s[40:43], s49 offen\n"
    "s_waitcnt vmcnt(61)\n"
    "buffer_load_dwordx4 v[220:223], v0, s[40:43], s49 offen offset:1024\n"
    "s_lshl1_add_u32 s49, s28, s48\n"
    "buffer_load_dwordx4 v[232:235], v0, s[40:43], s49 offen\n"
    "s_waitcnt vmcnt(61)\n"
    "buffer_load_dwordx4 v[236:239], v0, s[40:43], s49 offen offset:1024\n"
    "s_lshl1_add_u32 s49, s29, s48\n"
    "buffer_load_dwordx4 v[248:251], v0, s[40:43], s49 offen\n"
    "s_waitcnt vmcnt(61)\n"
    "buffer_load_dwordx4 v[252:255], v0, s[40:43], s49 offen offset:1024\n"
    "s_and_b32 s49, s20, 0x2000\n"
    "s_add_u32 s20, s20, s49\n"
    "s_and_b32 s49, s21, 0x2000\n"
    "s_add_u32 s21, s21, s49\n"
    "s_and_b32 s49, s22, 0x2000\n"
    "s_add_u32 s22, s22, s49\n"
    "s_and_b32 s49, s23, 0x2000\n"
    "s_add_u32 s23, s23, s49\n"
    "s_and_b32 s49, s24, 0x2000\n"
    "s_add_u32 s24, s24, s49\n"
    "s_and_b32 s49, s25, 0x2000\n"
    "s_add_u32 s25, s25, s49\n"
    "s_and_b32 s49, s26, 0x2000\n"
    "s_add_u32 s26, s26, s49\n"
    "s_and_b32 s49, s27, 0x2000\n"
    "s_add_u32 s27, s27, s49\n"
    "s_and_b32 s49, s28, 0x2000\n"
    "s_add_u32 s28, s28, s49\n"
    "s_and_b32 s49, s29, 0x2000\n"
    "s_add_u32 s29, s29, s49\n"
    "s_waitcnt vmcnt(59)\n"
    "ds_write_b128 v2, v[16:19] offset:0\n"
    "s_waitcnt vmcnt(58)\n"
    "ds_write_b128 v2, v[20:23] offset:1024\n"
    "s_waitcnt vmcnt(57)\n"
    "ds_write_b128 v2, v[24:27] offset:2048\n"
    "s_waitcnt vmcnt(56)\n"
    "ds_write_b128 v2, v[28:31] offset:3072\n"
    "ds_write_b128 v2, v[32:35] offset:4096\n"
    "ds_write_b128 v2, v[36:39] offset:5120\n"
    "s_waitcnt vmcnt(55)\n"
    "ds_write_b128 v2, v[40:43] offset:6144\n"
    "s_waitcnt vmcnt(54)\n"
    "ds_write_b128 v2, v[44:47] offset:7168\n"
    "s_waitcnt vmcnt(53)\n"
    "ds_write_b128 v2, v[48:51] offset:8192\n"
    "s_waitcnt vmcnt(52)\n"
    "ds_write_b128 v2, v[52:55] offset:9216\n"
    "s_waitcnt vmcnt(51)\n"
    "ds_write_b128 v2, v[56:59] offset:10240\n"
    "s_waitcnt vmcnt(50)\n"
    "ds_write_b128 v2, v[60:63] offset:11264\n"
    "ds_write_b128 v2, v[64:67] offset:12288\n"
    "ds_write_b128 v2, v[68:71] offset:13312\n"
    "s_waitcnt vmcnt(49)\n"
    "ds_write_b128 v2, v[72:75] offset:14336\n"
    "s_waitcnt vmcnt(48)\n"
    "ds_write_b128 v2, v[76:79] offset:15360\n"
    "s_waitcnt lgkmcnt(14)\n"
    "s_waitcnt vmcnt(47)\n"
    "ds_write_b128 v2, v[80:83] offset:16384\n"
    "s_waitcnt vmcnt(46)\n"
    "ds_write_b128 v2, v[84:87] offset:17408\n"
    "s_waitcnt lgkmcnt(14)\n"
    "s_waitcnt vmcnt(45)\n"
    "ds_write_b128 v2, v[88:91] offset:18432\n"
    "s_waitcnt vmcnt(44)\n"
    "ds_write_b128 v2, v[92:95] offset:19456\n"
    "s_waitcnt lgkmcnt(14)\n"
    "ds_write_b128 v2, v[96:99] offset:20480\n"
    "ds_write_b128 v2, v[100:103] offset:21504\n"
    "s_waitcnt lgkmcnt(14)\n"
    "s_waitcnt vmcnt(43)\n"
    "ds_write_b128 v2, v[104:107] offset:22528\n"
    "s_waitcnt vmcnt(42)\n"
    "ds_write_b128 v2, v[108:111] offset:23552\n"
    "s_waitcnt lgkmcnt(14)\n"
    "s_waitcnt vmcnt(41)\n"
    "ds_write_b128 v2, v[112:115] offset:24576\n"
    "s_waitcnt vmcnt(40)\n"
    "ds_write_b128 v2, v[116:119] offset:25600\n"
    "s_waitcnt lgkmcnt(14)\n"
    "s_waitcnt vmcnt(19)\n"
    "ds_write_b128 v2, v[120:123] offset:26624\n"
    "s_waitcnt vmcnt(18)\n"
    "ds_write_b128 v2, v[124:127] offset:27648\n"
    "s_waitcnt lgkmcnt(14)\n"
    "s_waitcnt vmcnt(17)\n"
    "ds_write_b128 v2, v[128:131] offset:28672\n"
    "s_waitcnt vmcnt(16)\n"
    "ds_write_b128 v2, v[132:135] offset:29696\n"
    "s_waitcnt lgkmcnt(14)\n"
    "s_waitcnt vmcnt(15)\n"
    "ds_write_b128 v2, v[136:139] offset:30720\n"
    "s_waitcnt vmcnt(14)\n"
    "ds_write_b128 v2, v[140:143] offset:31744\n"
    "s_waitcnt lgkmcnt(14)\n"
    "s_waitcnt vmcnt(0) lgkmcnt(0)\n"
    "v_lshrrev_b32 v152, 4, v15\n"
    "v_and_b32 v153, 7, v15\n"
    "v_bfe_u32 v154, v15, 3, 1\n"
    "v_cmp_eq_u32 vcc, 1, v154\n"
    "v_mov_b32 v13, 0x4444\n"
    "v_mov_b32 v1, 0xeeee\n"
    "v_cndmask_b32 v1, v13, v1, vcc\n"
    "v_mov_b32 v13, 0xff800000\n"
    "v_cndmask_b32 v8, 0, v13, vcc\n"
    "v_cndmask_b32 v9, 0, v13, vcc\n"
    "v_cndmask_b32 v10, 0, v13, vcc\n"
    "v_cndmask_b32 v11, 0, v13, vcc\n"
    "v_bfe_u32 v13, v15, 2, 2\n"
    "v_xor_b32 v13, v13, v152\n"
    "v_lshlrev_b32 v13, 3, v13\n"
    "v_and_b32 v3, 15, v15\n"
    "v_lshl_add_u32 v3, v3, 5, v13\n"
    "s_mul_i32 s48, s45, 1536\n"
    "s_add_u32 s48, s48, 143360\n"
    "v_add_u32 v3, s48, v3\n"
    "v_xor_b32 v4, v15, v152\n"
    "v_lshlrev_b32 v4, 3, v4\n"
    "v_add_u32 v4, 143360, v4\n"
    "v_add_u32 v5, 131072, v0\n"
    "s_lshr_b32 s48, s44, 1\n"
    "s_lshl_b32 s48, s48, 10\n"
    "s_and_b32 s49, s44, 1\n"
    "s_lshl_b32 s49, s49, 7\n"
    "s_add_u32 s50, s48, s49\n"
    "s_lshl_b32 s51, s44, 7\n"
    "s_add_u32 s51, s51, 16384\n"
    "s_mov_b32 s47, 0\n"
    "s_mov_b32 s61, 6144\n"
    "s_cmp_lg_u32 s45, 0\n"
    "s_cbranch_scc1 hw_wx_%=\n"
    "v_lshlrev_b32 v6, 8, v152\n"
    "v_add_u32 v6, s50, v6\n"
    "v_mov_b32 v7, s51\n"
    "v_cmp_eq_u32 vcc, 0, v154\n"
    "s_nop 1\n"
    "v_cndmask_b32 v6, v7, v6, vcc\n"
    "v_lshl_add_u32 v6, v153, 4, v6\n"
    "v_and_b32 v13, 0xffffe000, v6\n"
    "v_add_u32 v6, v6, v13\n"
    "v_lshrrev_b32 v7, 3, v15\n"
    "v_cmp_eq_u32 s[56:57], 5, v7\n"
    "s_nop 0\n"
    "s_or_b64 vcc, vcc, s[56:57]\n"
    "v_mov_b32 v13, 2147483632\n"
    "v_cndmask_b32 v6, v13, v6, vcc\n"
    "v_mov_b32 v14, 0\n"
    "v_cmp_eq_u32 vcc, 0, v15\n"
    "v_mov_b32 v13, 2147483632\n"
    "s_nop 0\n"
    "v_cndmask_b32 v14, v13, v14, vcc\n"
    "s_cmp_eq_u32 s44, 0\n"
    "s_cselect_b32 s59, 4, 0\n"
    "v_lshlrev_b32 v7, 15, v15\n"
    "v_cmp_gt_u32 vcc, s59, v15\n"
    "v_mov_b32 v13, 2147483632\n"
    "s_nop 0\n"
    "v_cndmask_b32 v7, v13, v7, vcc\n"
    "s_lshl_b32 s59, s59, 1\n"
    "v_lshlrev_b32 v155, 7, v15\n"
    "v_cmp_gt_u32 vcc, s59, v15\n"
    "v_mov_b32 v13, 2147483632\n"
    "s_nop 0\n"
    "v_cndmask_b32 v155, v13, v155, vcc\n"
    "s_mov_b32 s64, 0x3dd53b95\n"
    "s_mov_b32 s65, s64\n"
    "s_lshr_b32 s48, s44, 3\n"
    "s_lshl_b32 s48, s48, 7\n"
    "v_add_u32 v159, s48, v14\n"
    "s_mul_i32 s10, s67, 23\n"
    "s_sub_u32 s10, s10, s71\n"
    "s_mul_i32 s11, s66, 23\n"
    "s_sub_u32 s11, s11, s72\n"
    "hw_loopa_%=:\n"
    "s_and_b32 s48, s47, 3\n"
    "s_mul_i32 s53, s48, 196608\n"
    "s_lshl_b32 s50, s48, 2\n"
    "s_add_u32 s50, s50, s46\n"
    "s_mul_i32 s50, s50, 32768\n"
    "s_add_u32 s50, s50, 786432\n"
    "v_add_u32 v158, s50, v0\n"
    "s_add_u32 s50, s53, 49152\n"
    "s_add_u32 s51, s53, 98304\n"
    "s_add_u32 s52, s53, 147456\n"
    "v_mov_b32 v56, 0\n"
    "v_mov_b32 v57, 0\n"
    "v_mov_b32 v58, 0\n"
    "v_mov_b32 v59, 0\n"
    "v_mov_b32 v60, 0\n"
    "v_mov_b32 v61, 0\n"
    "v_mov_b32 v62, 0\n"
    "v_mov_b32 v63, 0\n"
    "v_mov_b32 v64, 0\n"
    "v_mov_b32 v65, 0\n"
    "v_mov_b32 v66, 0\n"
    "v_mov_b32 v67, 0\n"
    "v_mov_b32 v68, 0\n"
    "v_mov_b32 v69, 0\n"
    "v_mov_b32 v70, 0\n"
    "v_mov_b32 v71, 0\n"
    "v_mov_b32 v72, 0\n"
    "v_mov_b32 v73, 0\n"
    "v_mov_b32 v74, 0\n"
    "v_mov_b32 v75, 0\n"
    "v_mov_b32 v76, 0\n"
    "v_mov_b32 v77, 0\n"
    "v_mov_b32 v78, 0\n"
    "v_mov_b32 v79, 0\n"
    "ds_read_b128 v[80:83], v2 offset:0\n"
    "ds_read_b128 v[84:87], v2 offset:1024\n"
    "ds_read_b128 v[88:91], v2 offset:2048\n"
    "ds_read_b128 v[92:95], v2 offset:3072\n"
    "ds_read_b128 v[96:99], v2 offset:4096\n"
    "ds_read_b128 v[100:103], v2 offset:5120\n"
    "s_and_b32 s59, s47, 64\n"
    "s_add_u32 s63, s47, s59\n"
    "s_lshl_b32 s63, s63, 7\n"
    "s_add_u32 s63, s63, 1310720\n"
    "s_lshl_b32 s59, s46, 15\n"
    "s_add_u32 s59, s59, s63\n"
    "s_add_u32 s14, s40, s59\n"
    "s_addc_u32 s15, s41, 0\n"
    "s_lshl_b32 s12, s47, 10\n"
    "s_add_u32 s12, s12, 2364928\n"
    "s_waitcnt lgkmcnt(0)\n"
    "s_and_b32 s59, s47, 63\n"
    "s_cbranch_scc1 hw_timed_%=\n"
    "s_add_u32 s75, s47, 6\n"
    "s_barrier\n"
    "s_memrealtime s[56:57]\n"
    "s_waitcnt lgkmcnt(0)\n"
    "s_add_u32 s62, s56, s68\n"
    "s_add_u32 s59, s62, s67\n"
    "v_mov_b32 v152, s59\n"
    "s_add_u32 s59, s59, s66\n"
    "v_mov_b32 v153, s59\n"
    "s_nop 1\n"
    "s_branch hw_go_%=\n"
    "hw_timed_%=:\n"
    "s_mov_b32 s62, s17\n"
    "s_cmp_lg_u32 s62, -1\n"
    "s_cbranch_scc1 hw_rs_ok_%=\n"
    "s_sub_u32 s55, s47, 1\n"
    "s_and_b32 s59, s55, 64\n"
    "s_add_u32 s54, s55, s59\n"
    "s_lshl_b32 s54, s54, 7\n"
    "s_add_u32 s54, s54, 1310720\n"
    "s_mov_b32 s60, 0\n"
    "hw_rs_%=:\n"
    "buffer_load_dwordx2 v[152:153], v14, s[40:43], s54 offen sc1\n"
    "s_waitcnt vmcnt(0)\n"
    "v_readfirstlane_b32 s55, v152\n"
    "v_readfirstlane_b32 s62, v153\n"
    "s_nop 0\n"
    "s_max_u32 s59, s55, s62\n"
    "s_cmp_lg_u32 s59, -1\n"
    "s_nop 1\n"
    "s_cbranch_scc1 hw_rs_ok_%=\n"
    "s_sleep 4\n"
    "s_add_u32 s60, s60, 1\n"
    "s_cmp_gt_u32 s60, 65536\n"
    "s_cbranch_scc1 hw_abort_%=\n"
    "s_and_b32 s59, s60, 63\n"
    "s_cbranch_scc1 hw_rs_%=\n"
    "v_mov_b32 v152, 1024\n"
    "buffer_load_dword v152, v152, s[40:43], s58 offen sc1\n"
    "s_waitcnt vmcnt(0)\n"
    "v_readfirstlane_b32 s59, v152\n"
    "s_nop 0\n"
    "s_cmp_lg_u32 s59, 0\n"
    "s_nop 2\n"
    "s_cbranch_scc1 hw_end_%=\n"
    "s_branch hw_rs_%=\n"
    "hw_rs_ok_%=:\n"
    "s_add_u32 s59, s62, s67\n"
    "v_mov_b32 v152, s59\n"
    "s_add_u32 s59, s59, s66\n"
    "v_mov_b32 v153, s59\n"
    "s_memrealtime s[56:57]\n"
    "s_waitcnt lgkmcnt(0)\n"
    "s_sub_u32 s59, s62, s56\n"
    "s_cmp_gt_u32 s59, 1000\n"
    "s_cbranch_scc1 hw_wu_go_%=\n"
    "hw_wu_%=:\n"
    "s_memrealtime s[56:57]\n"
    "s_waitcnt lgkmcnt(0)\n"
    "s_sub_u32 s59, s62, s56\n"
    "s_cmp_gt_i32 s59, 15\n"
    "s_cbranch_scc1 hw_wu_%=\n"
    "s_memrealtime s[30:31]\n"
    "s_nop 1\n"
    "s_memrealtime s[18:19]\n"
    "s_nop 1\n"
    "s_memrealtime s[34:35]\n"
    "s_nop 1\n"
    "s_memrealtime s[36:37]\n"
    "s_nop 1\n"
    "s_memrealtime s[38:39]\n"
    "s_nop 1\n"
    "s_memrealtime s[94:95]\n"
    "s_nop 1\n"
    "s_memrealtime s[96:97]\n"
    "s_nop 1\n"
    "s_memrealtime s[98:99]\n"
    "s_nop 1\n"
    "s_getpc_b64 s[56:57]\n"
    "hw_wu_pc_%=:\n"
    "s_waitcnt lgkmcnt(0)\n"
    "s_add_u32 s18, s18, s34\n"
    "s_add_u32 s18, s18, s36\n"
    "s_add_u32 s18, s18, s38\n"
    "s_add_u32 s18, s18, s94\n"
    "s_add_u32 s18, s18, s96\n"
    "s_add_u32 s18, s18, s98\n"
    "s_mul_i32 s34, s30, 7\n"
    "s_sub_u32 s18, s18, s34\n"
    "s_mul_i32 s18, s18, 3\n"
    "s_sub_u32 s59, s62, s30\n"
    "s_mul_i32 s59, s59, 23\n"
    "s_sub_u32 s59, s59, s18\n"
    "s_sub_u32 s59, s59, 170\n"
    "s_max_i32 s59, s59, 0\n"
    "s_min_i32 s59, s59, 252\n"
    "s_andn2_b32 s59, s59, 3\n"
    "s_sub_u32 s59, hw_wu_e_%=-hw_wu_pc_%=, s59\n"
    "s_add_u32 s56, s56, s59\n"
    "s_addc_u32 s57, s57, 0\n"
    "s_setpc_b64 s[56:57]\n"
    "s_nop 0\n"
    "s_nop 0\n"
    "s_nop 0\n"
    "s_nop 0\n"
    "s_nop 0\n"
    "s_nop 0\n"
    "s_nop 0\n"
    "s_nop 0\n"
    "s_nop 0\n"
    "s_nop 0\n"
    "s_nop 0\n"
    "s_nop 0\n"
    "s_nop 0\n"
    "s_nop 0\n"
    "s_nop 0\n"
    "s_nop 0\n"
    "s_nop 0\n"
    "s_nop 0\n"
    "s_nop 0\n"
    "s_nop 0\n"
    "s_nop 0\n"
    "s_nop 0\n"
    "s_nop 0\n"
    "s_nop 0\n"
    "s_nop 0\n"
    "s_nop 0\n"
    "s_nop 0\n"
    "s_nop 0\n"
    "s_nop 0\n"
    "s_nop 0\n"
    "s_nop 0\n"
    "s_nop 0\n"
    "s_nop 0\n"
    "s_nop 0\n"
    "s_nop 0\n"
    "s_nop 0\n"
    "s_nop 0\n"
    "s_nop 0\n"
    "s_nop 0\n"
    "s_nop 0\n"
    "s_nop 0\n"
    "s_nop 0\n"
    "s_nop 0\n"
    "s_nop 0\n"
    "s_nop 0\n"
    "s_nop 0\n"
    "s_nop 0\n"
    "s_nop 0\n"
    "s_nop 0\n"
    "s_nop 0\n"
    "s_nop 0\n"
    "s_nop 0\n"
    "s_nop 0\n"
    "s_nop 0\n"
    "s_nop 0\n"
    "s_nop 0\n"
    "s_nop 0\n"
    "s_nop 0\n"
    "s_nop 0\n"
    "s_nop 0\n"
    "s_nop 0\n"
    "s_nop 0\n"
    "s_nop 0\n"
    "hw_wu_e_%=:\n"
    "hw_wu_go_%=:\n"
    "s_barrier\n"
    "hw_go_%=:\n"
    "s_mov_b32 s60, 0\n"
    "hw_retrya_%=:\n"
    "buffer_load_dwordx4 v[16:19], v158, s[40:43], s20 offen sc1\n"
    "ds_read_b128 v[104:107], v5 offset:0\n"
    "s_nop 6\n"
    "buffer_load_dwordx4 v[20:23], v158, s[40:43], s21 offen sc1\n"
    "ds_read_b128 v[108:111], v5 offset:1024\n"
    "s_nop 6\n"
    "buffer_load_dwordx4 v[24:27], v158, s[40:43], s22 offen sc1\n"
    "ds_read_b128 v[112:115], v5 offset:2048\n"
    "s_nop 6\n"
    "buffer_load_dwordx4 v[28:31], v158, s[40:43], s23 offen sc1\n"
    "ds_read_b128 v[116:119], v5 offset:3072\n"
    "s_nop 6\n"
    "buffer_load_dwordx4 v[32:35], v158, s[40:43], s24 offen sc1\n"
    "ds_read_b128 v[120:123], v5 offset:4096\n"
    "s_nop 6\n"
    "buffer_load_dwordx4 v[36:39], v158, s[40:43], s25 offen sc1\n"
    "ds_read_b128 v[124:127], v5 offset:5120\n"
    "s_nop 6\n"
    "buffer_load_dwordx4 v[40:43], v158, s[40:43], s26 offen sc1\n"
    "ds_read_b128 v[128:131], v5 offset:6144\n"
    "s_nop 6\n"
    "buffer_load_dwordx4 v[44:47], v158, s[40:43], s27 offen sc1\n"
    "ds_read_b128 v[132:135], v5 offset:7168\n"
    "s_nop 6\n"
    "buffer_load_dwordx4 v[48:51], v158, s[40:43], s28 offen sc1\n"
    "s_nop 7\n"
    "buffer_load_dwordx4 v[52:55], v158, s[40:43], s29 offen sc1\n"
    "s_waitcnt lgkmcnt(0)\n"
    "s_waitcnt vmcnt(9)\n"
    "v_smfmac_f32_16x16x64_bf16 v[68:71], v[16:19], v[80:87], v1\n"
    "ds_read_b128 v[80:83], v2 offset:6144\n"
    "v_smfmac_f32_16x16x64_bf16 v[56:59], v[16:19], a[0:7], v1\n"
    "ds_read_b128 v[84:87], v2 offset:7168\n"
    "v_smfmac_f32_16x16x64_bf16 v[64:67], v[16:19], a[8:15], v1\n"
    "v_smfmac_f32_16x16x64_bf16 v[60:63], v[16:19], a[16:23], v1\n"
    "s_waitcnt vmcnt(8)\n"
    "v_smfmac_f32_16x16x64_bf16 v[68:71], v[20:23], v[88:95], v1\n"
    "ds_read_b128 v[88:91], v2 offset:8192\n"
    "v_smfmac_f32_16x16x64_bf16 v[56:59], v[20:23], a[24:31], v1\n"
    "ds_read_b128 v[92:95], v2 offset:9216\n"
    "v_smfmac_f32_16x16x64_bf16 v[64:67], v[20:23], v[96:103], v1\n"
    "ds_read_b128 v[96:99], v2 offset:10240\n"
    "v_smfmac_f32_16x16x64_bf16 v[60:63], v[20:23], a[32:39], v1\n"
    "ds_read_b128 v[100:103], v2 offset:11264\n"
    "s_waitcnt vmcnt(7) lgkmcnt(4)\n"
    "v_smfmac_f32_16x16x64_bf16 v[68:71], v[24:27], v[80:87], v1\n"
    "ds_read_b128 v[80:83], v2 offset:12288\n"
    "v_smfmac_f32_16x16x64_bf16 v[56:59], v[24:27], a[40:47], v1\n"
    "ds_read_b128 v[84:87], v2 offset:13312\n"
    "v_smfmac_f32_16x16x64_bf16 v[64:67], v[24:27], a[48:55], v1\n"
    "v_smfmac_f32_16x16x64_bf16 v[60:63], v[24:27], a[56:63], v1\n"
    "v_max3_u32 v12, v16, v20, v24\n"
    "s_waitcnt vmcnt(6) lgkmcnt(4)\n"
    "v_smfmac_f32_16x16x64_bf16 v[68:71], v[28:31], v[88:95], v1\n"
    "ds_read_b128 v[88:91], v2 offset:14336\n"
    "v_smfmac_f32_16x16x64_bf16 v[56:59], v[28:31], a[64:71], v1\n"
    "ds_read_b128 v[92:95], v2 offset:15360\n"
    "v_smfmac_f32_16x16x64_bf16 v[64:67], v[28:31], a[72:79], v1\n"
    "v_cmp_eq_u32 vcc, -1, v12\n"
    "v_smfmac_f32_16x16x64_bf16 v[60:63], v[28:31], a[80:87], v1\n"
    "s_cbranch_vccnz hw_polla_%=\n"
    "s_waitcnt vmcnt(5) lgkmcnt(4)\n"
    "v_smfmac_f32_16x16x64_bf16 v[68:71], v[32:35], v[96:103], v1\n"
    "ds_read_b128 v[96:99], v2 offset:16384\n"
    "v_smfmac_f32_16x16x64_bf16 v[56:59], v[32:35], a[88:95], v1\n"
    "ds_read_b128 v[100:103], v2 offset:17408\n"
    "s_waitcnt lgkmcnt(4)\n"
    "v_smfmac_f32_16x16x64_bf16 v[64:67], v[32:35], v[80:87], v1\n"
    "ds_read_b128 v[80:83], v2 offset:18432\n"
    "v_smfmac_f32_16x16x64_bf16 v[60:63], v[32:35], a[96:103], v1\n"
    "ds_read_b128 v[84:87], v2 offset:19456\n"
    "s_waitcnt vmcnt(4) lgkmcnt(4)\n"
    "v_smfmac_f32_16x16x64_bf16 v[68:71], v[36:39], v[88:95], v1\n"
    "ds_read_b128 v[88:91], v2 offset:20480\n"
    "v_smfmac_f32_16x16x64_bf16 v[56:59], v[36:39], a[104:111], v1\n"
    "ds_read_b128 v[92:95], v2 offset:21504\n"
    "v_smfmac_f32_16x16x64_bf16 v[64:67], v[36:39], a[112:119], v1\n"
    "v_smfmac_f32_16x16x64_bf16 v[60:63], v[36:39], a[120:127], v1\n"
    "s_waitcnt vmcnt(3) lgkmcnt(4)\n"
    "v_smfmac_f32_16x16x64_bf16 v[68:71], v[40:43], v[96:103], v1\n"
    "ds_read_b128 v[96:99], v2 offset:22528\n"
    "v_smfmac_f32_16x16x64_bf16 v[56:59], v[40:43], a[128:135], v1\n"
    "ds_read_b128 v[100:103], v2 offset:23552\n"
    "v_smfmac_f32_16x16x64_bf16 v[64:67], v[40:43], a[136:143], v1\n"
    "v_smfmac_f32_16x16x64_bf16 v[60:63], v[40:43], a[144:151], v1\n"
    "s_waitcnt vmcnt(2) lgkmcnt(4)\n"
    "v_smfmac_f32_16x16x64_bf16 v[68:71], v[44:47], v[80:87], v1\n"
    "ds_read_b128 v[80:83], v2 offset:24576\n"
    "v_smfmac_f32_16x16x64_bf16 v[56:59], v[44:47], a[152:159], v1\n"
    "ds_read_b128 v[84:87], v2 offset:25600\n"
    "s_waitcnt lgkmcnt(4)\n"
    "v_smfmac_f32_16x16x64_bf16 v[64:67], v[44:47], v[88:95], v1\n"
    "ds_read_b128 v[88:91], v2 offset:26624\n"
    "v_smfmac_f32_16x16x64_bf16 v[60:63], v[44:47], a[160:167], v1\n"
    "ds_read_b128 v[92:95], v2 offset:27648\n"
    "s_waitcnt vmcnt(1) lgkmcnt(4)\n"
    "v_smfmac_f32_16x16x64_bf16 v[68:71], v[48:51], v[96:103], v1\n"
    "ds_read_b128 v[96:99], v2 offset:28672\n"
    "v_smfmac_f32_16x16x64_bf16 v[56:59], v[48:51], a[168:175], v1\n"
    "ds_read_b128 v[100:103], v2 offset:29696\n"
    "v_smfmac_f32_16x16x64_bf16 v[64:67], v[48:51], a[176:183], v1\n"
    "v_smfmac_f32_16x16x64_bf16 v[60:63], v[48:51], a[184:191], v1\n"
    "s_waitcnt vmcnt(0) lgkmcnt(4)\n"
    "v_smfmac_f32_16x16x64_bf16 v[68:71], v[52:55], v[80:87], v1\n"
    "ds_read_b128 v[80:83], v2 offset:30720\n"
    "v_smfmac_f32_16x16x64_bf16 v[56:59], v[52:55], a[192:199], v1\n"
    "ds_read_b128 v[84:87], v2 offset:31744\n"
    "v_smfmac_f32_16x16x64_bf16 v[64:67], v[52:55], a[200:207], v1\n"
    "v_smfmac_f32_16x16x64_bf16 v[60:63], v[52:55], a[208:215], v1\n"
    "v_smfmac_f32_16x16x64_bf16 v[72:75], v[16:19], a[216:223], v1\n"
    "v_mov_b32 v13, v13\n"
    "v_mov_b32 v13, v13\n"
    "v_mov_b32 v13, v13\n"
    "v_mov_b32 v13, v13\n"
    "v_mov_b32 v13, v13\n"
    "v_mov_b32 v13, v13\n"
    "v_mov_b32 v13, v13\n"
    "v_permlane32_swap_b32 v56, v60\n"
    "v_permlane32_swap_b32 v57, v61\n"
    "s_waitcnt lgkmcnt(4)\n"
    "v_smfmac_f32_16x16x64_bf16 v[76:79], v[16:19], v[88:95], v1\n"
    "v_permlane32_swap_b32 v58, v62\n"
    "v_permlane32_swap_b32 v59, v63\n"
    "v_smfmac_f32_16x16x64_bf16 v[72:75], v[20:23], a[224:231], v1\n"
    "v_add_f32 v56, v56, v60\n"
    "v_add_f32 v57, v57, v61\n"
    "s_waitcnt lgkmcnt(2)\n"
    "v_smfmac_f32_16x16x64_bf16 v[76:79], v[20:23], v[96:103], v1\n"
    "v_add_f32 v58, v58, v62\n"
    "v_add_f32 v59, v59, v63\n"
    "v_smfmac_f32_16x16x64_bf16 v[72:75], v[24:27], a[232:239], v1\n"
    "v_cmp_u_f32 vcc, v56, v57\n"
    "v_cmp_u_f32 s[56:57], v58, v59\n"
    "s_waitcnt lgkmcnt(0)\n"
    "v_smfmac_f32_16x16x64_bf16 v[76:79], v[24:27], v[80:87], v1\n"
    "s_or_b64 vcc, vcc, s[56:57]\n"
    "s_cbranch_vccnz hw_faila_%=\n"
    "hw_resumea_%=:\n"
    "buffer_store_dwordx2 v[152:153], v7, s[40:43], s63 offen sc1\n"
    "v_cvt_pk_bf16_f32 v60, v56, v57\n"
    "v_smfmac_f32_16x16x64_bf16 v[72:75], v[28:31], a[240:247], v1\n"
    "v_cvt_pk_bf16_f32 v61, v58, v59\n"
    "ds_write_b64 v3, v[60:61] offset:0\n"
    "v_smfmac_f32_16x16x64_bf16 v[76:79], v[28:31], a[248:255], v1\n"
    "v_permlane32_swap_b32 v64, v68\n"
    "v_permlane32_swap_b32 v65, v69\n"
    "v_smfmac_f32_16x16x64_bf16 v[72:75], v[32:35], v[160:167], v1\n"
    "v_permlane32_swap_b32 v66, v70\n"
    "v_permlane32_swap_b32 v67, v71\n"
    "v_smfmac_f32_16x16x64_bf16 v[76:79], v[32:35], v[168:175], v1\n"
    "v_add_f32 v64, v64, v68\n"
    "v_smfmac_f32_16x16x64_bf16 v[72:75], v[36:39], v[176:183], v1\n"
    "v_add_f32 v65, v65, v69\n"
    "v_smfmac_f32_16x16x64_bf16 v[76:79], v[36:39], v[184:191], v1\n"
    "v_add_f32 v66, v66, v70\n"
    "v_smfmac_f32_16x16x64_bf16 v[72:75], v[40:43], v[192:199], v1\n"
    "v_add_f32 v67, v67, v71\n"
    "v_smfmac_f32_16x16x64_bf16 v[76:79], v[40:43], v[200:207], v1\n"
    "v_cvt_pk_bf16_f32 v68, v64, v65\n"
    "v_smfmac_f32_16x16x64_bf16 v[72:75], v[44:47], v[208:215], v1\n"
    "v_cvt_pk_bf16_f32 v69, v66, v67\n"
    "v_smfmac_f32_16x16x64_bf16 v[76:79], v[44:47], v[216:223], v1\n"
    "ds_write_b64 v3, v[68:69] offset:512\n"
    "buffer_load_dword v154, v155, s[40:43], s12 offen sc1\n"
    "v_smfmac_f32_16x16x64_bf16 v[72:75], v[48:51], v[224:231], v1\n"
    "ds_read_b128 v[16:19], v5 offset:8192\n"
    "v_smfmac_f32_16x16x64_bf16 v[76:79], v[48:51], v[232:239], v1\n"
    "ds_read_b128 v[20:23], v5 offset:9216\n"
    "v_smfmac_f32_16x16x64_bf16 v[72:75], v[52:55], v[240:247], v1\n"
    "ds_read_b128 v[24:27], v5 offset:10240\n"
    "v_smfmac_f32_16x16x64_bf16 v[76:79], v[52:55], v[248:255], v1\n"
    "ds_read_b128 v[28:31], v5 offset:11264\n"
    "s_waitcnt lgkmcnt(4)\n"
    "s_barrier\n"
    "ds_read_b64_tr_b16 v[136:137], v4 offset:0\n"
    "ds_read_b64_tr_b16 v[138:139], v4 offset:512\n"
    "v_mov_b32 v13, v13\n"
    "ds_read_b64_tr_b16 v[140:141], v4 offset:1536\n"
    "ds_read_b64_tr_b16 v[142:143], v4 offset:2048\n"
    "v_mov_b32 v13, v13\n"
    "ds_read_b64_tr_b16 v[144:145], v4 offset:3072\n"
    "ds_read_b64_tr_b16 v[146:147], v4 offset:3584\n"
    "v_mov_b32 v13, v13\n"
    "ds_read_b64_tr_b16 v[148:149], v4 offset:4608\n"
    "ds_read_b64_tr_b16 v[150:151], v4 offset:5120\n"
    "v_mov_b32 v13, v13\n"
    "v_permlane32_swap_b32 v72, v76\n"
    "v_permlane32_swap_b32 v73, v77\n"
    "v_permlane32_swap_b32 v74, v78\n"
    "v_permlane32_swap_b32 v75, v79\n"
    "v_pk_add_f32 v[72:73], v[72:73], v[76:77]\n"
    "v_pk_add_f32 v[74:75], v[74:75], v[78:79]\n"
    "v_cvt_pk_bf16_f32 v76, v72, v73\n"
    "v_cvt_pk_bf16_f32 v77, v74, v75\n"
    "ds_write_b64 v3, v[76:77] offset:1024\n"
    "s_waitcnt lgkmcnt(7)\n"
    "v_mfma_f32_16x16x32_bf16 v[56:59], v[104:107], v[136:139], v[8:11]\n"
    "v_mfma_f32_16x16x32_bf16 v[60:63], v[108:111], v[136:139], v[8:11]\n"
    "v_mfma_f32_16x16x32_bf16 v[64:67], v[136:139], v[136:139], 0\n"
    "s_waitcnt lgkmcnt(5)\n"
    "v_mfma_f32_16x16x32_bf16 v[56:59], v[112:115], v[140:143], v[56:59]\n"
    "s_waitcnt lgkmcnt(0)\n"
    "s_barrier\n"
    "ds_read_b64_tr_b16 v[32:33], v4 offset:1024\n"
    "ds_read_b64_tr_b16 v[34:35], v4 offset:2560\n"
    "ds_read_b64_tr_b16 v[36:37], v4 offset:4096\n"
    "ds_read_b64_tr_b16 v[38:39], v4 offset:5632\n"
    "v_mfma_f32_16x16x32_bf16 v[60:63], v[116:119], v[140:143], v[60:63]\n"
    "v_mfma_f32_16x16x32_bf16 v[64:67], v[140:143], v[140:143], v[64:67]\n"
    "v_mfma_f32_16x16x32_bf16 v[56:59], v[120:123], v[144:147], v[56:59]\n"
    "v_mfma_f32_16x16x32_bf16 v[60:63], v[124:127], v[144:147], v[60:63]\n"
    "v_mfma_f32_16x16x32_bf16 v[64:67], v[144:147], v[144:147], v[64:67]\n"
    "v_mfma_f32_16x16x32_bf16 v[56:59], v[128:131], v[148:151], v[56:59]\n"
    "v_mfma_f32_16x16x32_bf16 v[60:63], v[132:135], v[148:151], v[60:63]\n"
    "v_mfma_f32_16x16x32_bf16 v[64:67], v[148:151], v[148:151], v[64:67]\n"
    "s_waitcnt lgkmcnt(2)\n"
    "v_mfma_f32_16x16x32_bf16 v[64:67], v[32:35], v[32:35], v[64:67]\n"
    "s_waitcnt lgkmcnt(0)\n"
    "v_mfma_f32_16x16x32_bf16 v[64:67], v[36:39], v[36:39], v[64:67]\n"
    "v_mfma_f32_16x16x32_bf16 v[56:59], v[16:19], v[32:35], v[56:59]\n"
    "v_mfma_f32_16x16x32_bf16 v[56:59], v[24:27], v[36:39], v[56:59]\n"
    "v_mfma_f32_16x16x32_bf16 v[60:63], v[20:23], v[32:35], v[60:63]\n"
    "v_mfma_f32_16x16x32_bf16 v[60:63], v[28:31], v[36:39], v[60:63]\n"
    "s_load_dwordx2 s[16:17], s[14:15], 0x0\n"
    "v_mov_b32 v13, v13\n"
    "v_mov_b32 v13, v13\n"
    "v_mov_b32 v13, v13\n"
    "v_permlane16_swap_b32 v64, v72\n"
    "v_permlane16_swap_b32 v65, v73\n"
    "v_permlane16_swap_b32 v66, v74\n"
    "v_permlane16_swap_b32 v67, v75\n"
    "v_mov_b32_dpp v56, v64 row_shr:8 row_mask:0x4 bank_mask:0xc\n"
    "v_mov_b32_dpp v57, v65 row_shr:8 row_mask:0x4 bank_mask:0xc\n"
    "v_mov_b32_dpp v58, v66 row_shr:8 row_mask:0x4 bank_mask:0xc\n"
    "v_mov_b32_dpp v59, v67 row_shr:8 row_mask:0x4 bank_mask:0xc\n"
    "v_mov_b32_dpp v60, v72 row_shr:8 row_mask:0x4 bank_mask:0xc\n"
    "v_mov_b32_dpp v61, v73 row_shr:8 row_mask:0x4 bank_mask:0xc\n"
    "v_mov_b32_dpp v62, v74 row_shr:8 row_mask:0x4 bank_mask:0xc\n"
    "v_mov_b32_dpp v63, v75 row_shr:8 row_mask:0x4 bank_mask:0xc\n"
    "v_max3_f32 v68, v60, v61, v62\n"
    "v_max3_f32 v69, v56, v57, v58\n"
    "v_max3_f32 v68, v68, v63, v59\n"
    "v_max_f32 v68, v68, v69\n"
    "s_nop 1\n"
    "v_max_f32_dpp v68, v68, v68 row_ror:8 row_mask:0xf bank_mask:0xf\n"
    "v_mov_b32 v69, v68\n"
    "s_nop 1\n"
    "v_permlane16_swap_b32 v68, v69\n"
    "v_max_f32 v68, v68, v69\n"
    "v_mov_b32 v69, v68\n"
    "s_nop 1\n"
    "v_permlane32_swap_b32 v68, v69\n"
    "v_max_f32 v68, v68, v69\n"
    "v_mul_f32 v68, 0xbdd53b95, v68\n"
    "v_pk_fma_f32 v[56:57], v[56:57], s[64:65], v[68:69] op_sel_hi:[1,1,0]\n"
    "v_pk_fma_f32 v[58:59], v[58:59], s[64:65], v[68:69] op_sel_hi:[1,1,0]\n"
    "v_pk_fma_f32 v[60:61], v[60:61], s[64:65], v[68:69] op_sel_hi:[1,1,0]\n"
    "v_pk_fma_f32 v[62:63], v[62:63], s[64:65], v[68:69] op_sel_hi:[1,1,0]\n"
    "v_exp_f32 v56, v56\n"
    "v_exp_f32 v57, v57\n"
    "v_exp_f32 v58, v58\n"
    "v_exp_f32 v59, v59\n"
    "v_exp_f32 v60, v60\n"
    "v_exp_f32 v61, v61\n"
    "v_exp_f32 v62, v62\n"
    "v_exp_f32 v63, v63\n"
    "v_pk_add_f32 v[64:65], v[56:57], v[58:59]\n"
    "v_pk_add_f32 v[66:67], v[60:61], v[62:63]\n"
    "v_pk_add_f32 v[64:65], v[64:65], v[66:67]\n"
    "v_add_f32 v68, v64, v65\n"
    "s_nop 1\n"
    "v_add_f32_dpp v68, v68, v68 row_ror:8 row_mask:0xf bank_mask:0xf\n"
    "v_mov_b32 v69, v68\n"
    "s_nop 1\n"
    "v_permlane16_swap_b32 v68, v69\n"
    "v_add_f32 v68, v68, v69\n"
    "v_mov_b32 v69, v68\n"
    "s_nop 1\n"
    "v_permlane32_swap_b32 v68, v69\n"
    "v_add_f32 v68, v68, v69\n"
    "v_rcp_f32 v68, v68\n"
    "s_nop 0\n"
    "v_pk_mul_f32 v[56:57], v[56:57], v[68:69] op_sel_hi:[1,0]\n"
    "v_pk_mul_f32 v[58:59], v[58:59], v[68:69] op_sel_hi:[1,0]\n"
    "v_pk_mul_f32 v[60:61], v[60:61], v[68:69] op_sel_hi:[1,0]\n"
    "v_pk_mul_f32 v[62:63], v[62:63], v[68:69] op_sel_hi:[1,0]\n"
    "v_cvt_pk_bf16_f32 v64, v56, v57\n"
    "v_cvt_pk_bf16_f32 v65, v58, v59\n"
    "v_cvt_pk_bf16_f32 v66, v60, v61\n"
    "v_cvt_pk_bf16_f32 v67, v62, v63\n"
    "buffer_store_dwordx4 v[64:67], v6, s[40:43], s53 offen sc1\n"
    "buffer_store_dwordx4 v[64:67], v6, s[40:43], s50 offen sc1\n"
    "buffer_store_dwordx4 v[64:67], v6, s[40:43], s51 offen sc1\n"
    "buffer_store_dwordx4 v[64:67], v6, s[40:43], s52 offen sc1\n"
    "s_memrealtime s[30:31]\n"
    "s_nop 1\n"
    "s_memrealtime s[18:19]\n"
    "s_nop 1\n"
    "s_memrealtime s[34:35]\n"
    "s_nop 1\n"
    "s_memrealtime s[36:37]\n"
    "s_nop 1\n"
    "s_memrealtime s[38:39]\n"
    "s_nop 1\n"
    "s_memrealtime s[94:95]\n"
    "s_nop 1\n"
    "s_memrealtime s[96:97]\n"
    "s_nop 1\n"
    "s_memrealtime s[98:99]\n"
    "s_nop 1\n"
    "s_waitcnt lgkmcnt(0)\n"
    "s_add_u32 s18, s18, s34\n"
    "s_add_u32 s18, s18, s36\n"
    "s_add_u32 s18, s18, s38\n"
    "s_add_u32 s18, s18, s94\n"
    "s_add_u32 s18, s18, s96\n"
    "s_add_u32 s18, s18, s98\n"
    "s_mul_i32 s34, s30, 7\n"
    "s_sub_u32 s18, s18, s34\n"
    "s_mul_i32 s18, s18, 3\n"
    "s_sub_u32 s59, s30, s62\n"
    "s_mul_i32 s59, s59, 23\n"
    "s_add_u32 s59, s59, s18\n"
    "v_mov_b32 v13, s59\n"
    "s_lshl_b32 s34, s47, 10\n"
    "s_add_u32 s34, s34, 2366976\n"
    "s_cmp_eq_u32 s60, 0\n"
    "s_cselect_b32 s34, s34, 0x40000000\n"
    "buffer_atomic_umax v13, v159, s[40:43], s34 offen\n"
    "s_waitcnt vmcnt(5)\n"
    "v_readlane_b32 s0, v154, 0\n"
    "v_readlane_b32 s1, v154, 1\n"
    "v_readlane_b32 s2, v154, 2\n"
    "v_readlane_b32 s3, v154, 3\n"
    "v_readlane_b32 s4, v154, 4\n"
    "v_readlane_b32 s5, v154, 5\n"
    "v_readlane_b32 s6, v154, 6\n"
    "v_readlane_b32 s7, v154, 7\n"
    "s_max_u32 s0, s0, s1\n"
    "s_max_u32 s0, s0, s2\n"
    "s_max_u32 s0, s0, s3\n"
    "s_sub_u32 s59, s0, s10\n"
    "s_add_u32 s57, s59, 500\n"
    "s_cmp_lt_u32 s57, 900\n"
    "s_cselect_b32 s59, s59, 0\n"
    "s_ashr_i32 s57, s59, 4\n"
    "s_max_i32 s59, s59, s57\n"
    "s_min_i32 s59, s59, s73\n"
    "s_add_u32 s10, s10, s59\n"
    "s_add_u32 s59, s10, s71\n"
    "s_mul_i32 s59, s59, 2850\n"
    "s_lshr_b32 s67, s59, 16\n"
    "s_max_u32 s4, s4, s5\n"
    "s_max_u32 s4, s4, s6\n"
    "s_max_u32 s4, s4, s7\n"
    "s_sub_u32 s59, s4, s11\n"
    "s_add_u32 s57, s59, 500\n"
    "s_cmp_lt_u32 s57, 900\n"
    "s_cselect_b32 s59, s59, 0\n"
    "s_ashr_i32 s57, s59, 4\n"
    "s_max_i32 s59, s59, s57\n"
    "s_min_i32 s59, s59, s73\n"
    "s_add_u32 s11, s11, s59\n"
    "s_add_u32 s59, s11, s72\n"
    "s_mul_i32 s59, s59, 2850\n"
    "s_lshr_b32 s66, s59, 16\n"
    "v_add_u32 v3, s61, v3\n"
    "v_add_u32 v4, s61, v4\n"
    "s_sub_u32 s61, 0, s61\n"
    "s_add_u32 s47, s47, 1\n"
    "s_cmp_lt_u32 s47, 128\n"
    "s_cbranch_scc1 hw_loopa_%=\n"
    "s_branch hw_end_%=\n"
    "hw_faila_%=:\n"
    "v_max_u32 v12, v16, v17\n"
    "v_max3_u32 v12, v18, v19, v12\n"
    "v_max3_u32 v12, v20, v21, v12\n"
    "v_max3_u32 v12, v22, v23, v12\n"
    "v_max3_u32 v12, v24, v25, v12\n"
    "v_max3_u32 v12, v26, v27, v12\n"
    "v_max3_u32 v12, v28, v29, v12\n"
    "v_max3_u32 v12, v30, v31, v12\n"
    "v_max3_u32 v12, v32, v33, v12\n"
    "v_max3_u32 v12, v34, v35, v12\n"
    "v_max3_u32 v12, v36, v37, v12\n"
    "v_max3_u32 v12, v38, v39, v12\n"
    "v_max3_u32 v12, v40, v41, v12\n"
    "v_max3_u32 v12, v42, v43, v12\n"
    "v_max3_u32 v12, v44, v45, v12\n"
    "v_max3_u32 v12, v46, v47, v12\n"
    "v_max3_u32 v12, v48, v49, v12\n"
    "v_max3_u32 v12, v50, v51, v12\n"
    "v_max3_u32 v12, v52, v53, v12\n"
    "v_max3_u32 v12, v54, v55, v12\n"
    "v_cmp_eq_u32 vcc, -1, v12\n"
    "s_nop 4\n"
    "s_cbranch_vccz hw_resumea_%=\n"
    "hw_polla_%=:\n"
    "s_nop 7\n"
    "s_waitcnt vmcnt(0) lgkmcnt(0)\n"
    "v_mov_b32 v56, 0\n"
    "v_mov_b32 v57, 0\n"
    "v_mov_b32 v58, 0\n"
    "v_mov_b32 v59, 0\n"
    "v_mov_b32 v60, 0\n"
    "v_mov_b32 v61, 0\n"
    "v_mov_b32 v62, 0\n"
    "v_mov_b32 v63, 0\n"
    "v_mov_b32 v64, 0\n"
    "v_mov_b32 v65, 0\n"
    "v_mov_b32 v66, 0\n"
    "v_mov_b32 v67, 0\n"
    "v_mov_b32 v68, 0\n"
    "v_mov_b32 v69, 0\n"
    "v_mov_b32 v70, 0\n"
    "v_mov_b32 v71, 0\n"
    "v_mov_b32 v72, 0\n"
    "v_mov_b32 v73, 0\n"
    "v_mov_b32 v74, 0\n"
    "v_mov_b32 v75, 0\n"
    "v_mov_b32 v76, 0\n"
    "v_mov_b32 v77, 0\n"
    "v_mov_b32 v78, 0\n"
    "v_mov_b32 v79, 0\n"
    "ds_read_b128 v[80:83], v2 offset:0\n"
    "ds_read_b128 v[84:87], v2 offset:1024\n"
    "ds_read_b128 v[88:91], v2 offset:2048\n"
    "ds_read_b128 v[92:95], v2 offset:3072\n"
    "ds_read_b128 v[96:99], v2 offset:4096\n"
    "ds_read_b128 v[100:103], v2 offset:5120\n"
    "v_sub_u32 v12, v158, v0\n"
    "v_lshl_add_u32 v12, v15, 7, v12\n"
    "v_and_b32 v18, 15, v15\n"
    "v_sub_u32 v18, v18, v15\n"
    "v_lshl_add_u32 v18, v18, 7, v12\n"
    "s_movk_i32 s55, 0x4000\n"
    "s_waitcnt vmcnt(0) lgkmcnt(0)\n"
    "s_add_u32 s60, s60, 1\n"
    "s_cmp_gt_u32 s60, 65536\n"
    "s_cbranch_scc1 hw_abort_%=\n"
    "s_and_b32 s59, s60, 63\n"
    "s_cbranch_scc1 hw_againa_%=\n"
    "v_mov_b32 v13, 1024\n"
    "buffer_load_dword v13, v13, s[40:43], s58 offen sc1\n"
    "s_waitcnt vmcnt(0)\n"
    "v_readfirstlane_b32 s59, v13\n"
    "s_nop 0\n"
    "s_cmp_lg_u32 s59, 0\n"
    "s_nop 2\n"
    "s_cbranch_scc1 hw_end_%=\n"
    "s_branch hw_againa_%=\n"
    "hw_againa_%=:\n"
    "buffer_load_dword v16, v12, s[40:43], 0 offen sc1\n"
    "buffer_load_dword v17, v18, s[40:43], s55 offen sc1\n"
    "s_waitcnt vmcnt(0)\n"
    "v_max_u32 v16, v16, v17\n"
    "v_cmp_eq_u32 vcc, -1, v16\n"
    "s_nop 4\n"
    "s_cbranch_vccz hw_relocka_%=\n"
    "s_add_u32 s60, s60, 1\n"
    "s_cmp_gt_u32 s60, 65536\n"
    "s_cbranch_scc1 hw_abort_%=\n"
    "s_and_b32 s59, s60, 63\n"
    "s_cbranch_scc1 hw_againa_%=\n"
    "v_mov_b32 v13, 1024\n"
    "buffer_load_dword v13, v13, s[40:43], s58 offen sc1\n"
    "s_waitcnt vmcnt(0)\n"
    "v_readfirstlane_b32 s59, v13\n"
    "s_nop 0\n"
    "s_cmp_lg_u32 s59, 0\n"
    "s_nop 2\n"
    "s_cbranch_scc1 hw_end_%=\n"
    "s_branch hw_againa_%=\n"
    "hw_relocka_%=:\n"
    "s_memrealtime s[56:57]\n"
    "s_waitcnt lgkmcnt(0)\n"
    "s_cmp_lt_u32 s47, s75\n"
    "s_cbranch_scc1 hw_nobump_%=\n"
    "s_add_u32 s71, s71, 12\n"
    "s_add_u32 s72, s72, 12\n"
    "hw_nobump_%=:\n"
    "s_add_u32 s59, s47, 2\n"
    "s_max_u32 s75, s75, s59\n"
    "s_add_u32 s62, s56, s68\n"
    "s_add_u32 s59, s62, s67\n"
    "v_mov_b32 v152, s59\n"
    "s_add_u32 s59, s59, s66\n"
    "v_mov_b32 v153, s59\n"
    "s_nop 1\n"
    "s_branch hw_retrya_%=\n"
    "hw_wx_%=:\n"
    "v_lshrrev_b32 v6, 3, v15\n"
    "v_lshlrev_b32 v6, 8, v6\n"
    "v_add_u32 v6, s50, v6\n"
    "v_mov_b32 v13, s51\n"
    "v_cmp_gt_u32 vcc, 32, v15\n"
    "s_nop 1\n"
    "v_cndmask_b32 v6, v13, v6, vcc\n"
    "v_lshl_add_u32 v6, v153, 4, v6\n"
    "v_and_b32 v13, 0xffffe000, v6\n"
    "v_add_u32 v6, v6, v13\n"
    "v_cmp_gt_u32 vcc, 40, v15\n"
    "v_mov_b32 v13, 2147483632\n"
    "s_nop 0\n"
    "v_cndmask_b32 v6, v13, v6, vcc\n"
    "v_lshlrev_b32 v7, 7, v152\n"
    "v_lshl_add_u32 v7, v153, 4, v7\n"
    "v_cmp_eq_u32 vcc, 1, v154\n"
    "v_mov_b32 v13, 2147483632\n"
    "s_nop 0\n"
    "v_cndmask_b32 v7, v13, v7, vcc\n"
    "s_cmp_eq_u32 s45, 1\n"
    "s_cselect_b64 vcc, -1, 0\n"
    "v_cndmask_b32 v6, v6, v7, vcc\n"
    "s_cmp_eq_u32 s45, 1\n"
    "s_cselect_b32 s59, 1, 0\n"
    "v_lshlrev_b32 v133, 7, v15\n"
    "v_cmp_gt_u32 vcc, s59, v15\n"
    "v_mov_b32 v13, 2147483632\n"
    "s_nop 0\n"
    "v_cndmask_b32 v133, v13, v133, vcc\n"
    "s_lshl_b32 s64, s44, 7\n"
    "s_add_u32 s64, s64, 2103296\n"
    "s_mov_b32 s48, 0\n"
    "s_cmp_eq_u32 s45, 2\n"
    "s_cselect_b32 s48, 2056, s48\n"
    "s_cmp_eq_u32 s45, 3\n"
    "s_cselect_b32 s48, 10240, s48\n"
    "v_add_u32 v129, s48, v5\n"
    "s_mov_b32 s48, 8\n"
    "s_cmp_eq_u32 s45, 2\n"
    "s_cselect_b32 s48, 8200, s48\n"
    "s_cmp_eq_u32 s45, 3\n"
    "s_cselect_b32 s48, 6144, s48\n"
    "v_add_u32 v130, s48, v5\n"
    "s_mov_b32 s48, 8192\n"
    "s_cmp_eq_u32 s45, 2\n"
    "s_cselect_b32 s48, 4096, s48\n"
    "s_cmp_eq_u32 s45, 3\n"
    "s_cselect_b32 s48, 6152, s48\n"
    "v_add_u32 v131, s48, v5\n"
    "s_mov_b32 s48, 2048\n"
    "s_cmp_eq_u32 s45, 2\n"
    "s_cselect_b32 s48, 4104, s48\n"
    "s_cmp_eq_u32 s45, 3\n"
    "s_cselect_b32 s48, 10248, s48\n"
    "v_add_u32 v132, s48, v5\n"
    "v_mov_b32 v104, -1\n"
    "v_mov_b32 v105, -1\n"
    "v_mov_b32 v106, -1\n"
    "v_mov_b32 v107, -1\n"
    "v_mov_b32 v108, -1\n"
    "v_mov_b32 v109, -1\n"
    "v_mov_b32 v110, -1\n"
    "v_mov_b32 v111, -1\n"
    "v_mov_b32 v112, -1\n"
    "v_mov_b32 v113, -1\n"
    "v_mov_b32 v114, -1\n"
    "v_mov_b32 v115, -1\n"
    "v_mov_b32 v116, -1\n"
    "v_mov_b32 v117, -1\n"
    "v_mov_b32 v118, -1\n"
    "v_mov_b32 v119, -1\n"
    "v_mov_b32 v120, -1\n"
    "v_mov_b32 v121, -1\n"
    "v_mov_b32 v122, -1\n"
    "v_mov_b32 v123, -1\n"
    "v_mov_b32 v124, -1\n"
    "v_mov_b32 v125, -1\n"
    "v_mov_b32 v126, -1\n"
    "v_mov_b32 v127, -1\n"
    "v_mov_b32 v8, 0\n"
    "s_mov_b32 s65, 32\n"
    "s_mov_b32 s63, 0x40000000\n"
    "hw_first_%=:\n"
    "s_mul_i32 s62, s44, 12288\n"
    "s_lshl_b32 s49, s45, 12\n"
    "s_add_u32 s62, s62, s49\n"
    "s_add_u32 s62, s62, 65433600\n"
    "s_mov_b32 s31, s62\n"
    "s_lshl_b32 s49, s47, 7\n"
    "s_add_u32 s49, s49, 10240\n"
    "v_mov_b32 v128, s49\n"
    "v_mov_b32 v14, 0\n"
    "s_mov_b32 s60, 0\n"
    "v_cmp_gt_u32 vcc, s65, v14\n"
    "s_nop 4\n"
    "s_cbranch_vccz hw_kx0_ok_%=\n"
    "hw_kx0_poll_%=:\n"
    "buffer_load_dword v14, v128, s[40:43], s58 offen sc1\n"
    "s_waitcnt vmcnt(0)\n"
    "v_cmp_gt_u32 vcc, s65, v14\n"
    "s_nop 4\n"
    "s_cbranch_vccz hw_kx0_ok_%=\n"
    "s_sleep 8\n"
    "s_add_u32 s60, s60, 1\n"
    "s_cmp_gt_u32 s60, 65536\n"
    "s_cbranch_scc1 hw_abort_%=\n"
    "s_and_b32 s59, s60, 63\n"
    "s_cbranch_scc1 hw_kx0_poll_%=\n"
    "v_mov_b32 v13, 1024\n"
    "buffer_load_dword v13, v13, s[40:43], s58 offen sc1\n"
    "s_waitcnt vmcnt(0)\n"
    "v_readfirstlane_b32 s59, v13\n"
    "s_nop 0\n"
    "s_cmp_lg_u32 s59, 0\n"
    "s_nop 2\n"
    "s_cbranch_scc1 hw_end_%=\n"
    "s_branch hw_kx0_poll_%=\n"
    "hw_kx0_ok_%=:\n"
    "buffer_load_dwordx4 v[136:139], v0, s[40:43], s62 offen sc1\n"
    "buffer_load_dwordx4 v[140:143], v0, s[40:43], s62 offen offset:1024 sc1\n"
    "buffer_load_dwordx4 v[144:147], v0, s[40:43], s62 offen offset:2048 sc1\n"
    "buffer_load_dwordx4 v[148:151], v0, s[40:43], s62 offen offset:3072 sc1\n"
    "s_waitcnt vmcnt(3)\n"
    "ds_write2st64_b64 v129, v[136:137], v[138:139] offset1:2\n"
    "s_waitcnt vmcnt(2)\n"
    "ds_write2st64_b64 v130, v[140:141], v[142:143] offset1:2\n"
    "s_waitcnt vmcnt(1)\n"
    "ds_write2st64_b64 v131, v[144:145], v[146:147] offset1:2\n"
    "s_waitcnt vmcnt(0)\n"
    "ds_write2st64_b64 v132, v[148:149], v[150:151] offset1:2\n"
    "v_add_u32 v128, 128, v128\n"
    "v_mov_b32 v14, 0\n"
    "s_nop 1\n"
    "hw_loopb_%=:\n"
    "v_cmp_gt_u32 vcc, s65, v14\n"
    "s_add_u32 s31, s31, 393216\n"
    "s_cmp_eq_u64 vcc, 0\n"
    "s_cselect_b32 s62, s31, 0x40000000\n"
    "v_mov_b32 v134, v128\n"
    "v_add_u32 v128, 128, v128\n"
    "s_and_b32 s48, s47, 3\n"
    "s_mul_i32 s53, s48, 196608\n"
    "s_lshl_b32 s50, s48, 2\n"
    "s_add_u32 s50, s50, s46\n"
    "s_mul_i32 s50, s50, 32768\n"
    "s_add_u32 s50, s50, 786432\n"
    "v_add_u32 v158, s50, v0\n"
    "s_add_u32 s50, s53, 49152\n"
    "s_add_u32 s51, s53, 98304\n"
    "s_add_u32 s52, s53, 147456\n"
    "v_mov_b32 v56, 0\n"
    "v_mov_b32 v57, 0\n"
    "v_mov_b32 v58, 0\n"
    "v_mov_b32 v59, 0\n"
    "v_mov_b32 v60, 0\n"
    "v_mov_b32 v61, 0\n"
    "v_mov_b32 v62, 0\n"
    "v_mov_b32 v63, 0\n"
    "v_mov_b32 v64, 0\n"
    "v_mov_b32 v65, 0\n"
    "v_mov_b32 v66, 0\n"
    "v_mov_b32 v67, 0\n"
    "v_mov_b32 v68, 0\n"
    "v_mov_b32 v69, 0\n"
    "v_mov_b32 v70, 0\n"
    "v_mov_b32 v71, 0\n"
    "v_mov_b32 v72, 0\n"
    "v_mov_b32 v73, 0\n"
    "v_mov_b32 v74, 0\n"
    "v_mov_b32 v75, 0\n"
    "v_mov_b32 v76, 0\n"
    "v_mov_b32 v77, 0\n"
    "v_mov_b32 v78, 0\n"
    "v_mov_b32 v79, 0\n"
    "ds_read_b128 v[80:83], v2 offset:0\n"
    "ds_read_b128 v[84:87], v2 offset:1024\n"
    "ds_read_b128 v[88:91], v2 offset:2048\n"
    "ds_read_b128 v[92:95], v2 offset:3072\n"
    "ds_read_b128 v[96:99], v2 offset:4096\n"
    "ds_read_b128 v[100:103], v2 offset:5120\n"
    "s_xor_b32 s48, s48, 2\n"
    "s_mul_i32 s54, s48, 196608\n"
    "s_and_b32 s63, s47, 63\n"
    "s_lshl_b32 s63, s63, 5\n"
    "s_add_u32 s63, s63, s44\n"
    "s_mul_i32 s63, s63, 3072\n"
    "s_add_u32 s63, s63, 220659712\n"
    "s_cmp_eq_u32 s45, 1\n"
    "s_cselect_b32 s63, s63, 0x40000000\n"
    "s_movk_i32 s49, 512\n"
    "s_cmp_eq_u32 s45, 2\n"
    "s_cselect_b32 s48, 49152, s49\n"
    "s_cselect_b32 s34, s54, s63\n"
    "s_cselect_b32 s49, 0x40000000, 0\n"
    "s_add_u32 s35, s34, s48\n"
    "s_add_u32 s36, s35, s48\n"
    "s_add_u32 s37, s36, s48\n"
    "s_add_u32 s38, s37, s48\n"
    "s_add_u32 s39, s38, s48\n"
    "s_or_b32 s38, s38, s49\n"
    "s_or_b32 s39, s39, s49\n"
    "s_waitcnt lgkmcnt(0)\n"
    "s_barrier\n"
    "s_mov_b32 s60, 0\n"
    "hw_retryb_%=:\n"
    "buffer_load_dwordx4 v[16:19], v158, s[40:43], s20 offen sc1\n"
    "s_nop 7\n"
    "buffer_load_dwordx4 v[20:23], v158, s[40:43], s21 offen sc1\n"
    "s_nop 7\n"
    "buffer_load_dwordx4 v[24:27], v158, s[40:43], s22 offen sc1\n"
    "s_nop 7\n"
    "buffer_load_dwordx4 v[28:31], v158, s[40:43], s23 offen sc1\n"
    "s_nop 7\n"
    "buffer_load_dwordx4 v[32:35], v158, s[40:43], s24 offen sc1\n"
    "s_nop 7\n"
    "buffer_load_dwordx4 v[36:39], v158, s[40:43], s25 offen sc1\n"
    "s_nop 7\n"
    "buffer_load_dwordx4 v[40:43], v158, s[40:43], s26 offen sc1\n"
    "s_nop 7\n"
    "buffer_load_dwordx4 v[44:47], v158, s[40:43], s27 offen sc1\n"
    "s_nop 7\n"
    "buffer_load_dwordx4 v[48:51], v158, s[40:43], s28 offen sc1\n"
    "s_nop 7\n"
    "buffer_load_dwordx4 v[52:55], v158, s[40:43], s29 offen sc1\n"
    "s_waitcnt vmcnt(9)\n"
    "v_smfmac_f32_16x16x64_bf16 v[68:71], v[16:19], v[80:87], v1\n"
    "ds_read_b128 v[80:83], v2 offset:6144\n"
    "v_smfmac_f32_16x16x64_bf16 v[56:59], v[16:19], a[0:7], v1\n"
    "ds_read_b128 v[84:87], v2 offset:7168\n"
    "v_smfmac_f32_16x16x64_bf16 v[64:67], v[16:19], a[8:15], v1\n"
    "v_smfmac_f32_16x16x64_bf16 v[60:63], v[16:19], a[16:23], v1\n"
    "buffer_store_dword v8, v133, s[40:43], s64 offen sc1\n"
    "s_waitcnt vmcnt(9)\n"
    "v_smfmac_f32_16x16x64_bf16 v[68:71], v[20:23], v[88:95], v1\n"
    "ds_read_b128 v[88:91], v2 offset:8192\n"
    "v_smfmac_f32_16x16x64_bf16 v[56:59], v[20:23], a[24:31], v1\n"
    "ds_read_b128 v[92:95], v2 offset:9216\n"
    "v_smfmac_f32_16x16x64_bf16 v[64:67], v[20:23], v[96:103], v1\n"
    "ds_read_b128 v[96:99], v2 offset:10240\n"
    "v_smfmac_f32_16x16x64_bf16 v[60:63], v[20:23], a[32:39], v1\n"
    "ds_read_b128 v[100:103], v2 offset:11264\n"
    "s_waitcnt vmcnt(8) lgkmcnt(4)\n"
    "v_smfmac_f32_16x16x64_bf16 v[68:71], v[24:27], v[80:87], v1\n"
    "ds_read_b128 v[80:83], v2 offset:12288\n"
    "v_smfmac_f32_16x16x64_bf16 v[56:59], v[24:27], a[40:47], v1\n"
    "ds_read_b128 v[84:87], v2 offset:13312\n"
    "v_smfmac_f32_16x16x64_bf16 v[64:67], v[24:27], a[48:55], v1\n"
    "v_smfmac_f32_16x16x64_bf16 v[60:63], v[24:27], a[56:63], v1\n"
    "v_max3_u32 v12, v16, v20, v24\n"
    "s_waitcnt vmcnt(7) lgkmcnt(4)\n"
    "v_smfmac_f32_16x16x64_bf16 v[68:71], v[28:31], v[88:95], v1\n"
    "ds_read_b128 v[88:91], v2 offset:14336\n"
    "v_smfmac_f32_16x16x64_bf16 v[56:59], v[28:31], a[64:71], v1\n"
    "ds_read_b128 v[92:95], v2 offset:15360\n"
    "v_smfmac_f32_16x16x64_bf16 v[64:67], v[28:31], a[72:79], v1\n"
    "v_cmp_eq_u32 vcc, -1, v12\n"
    "v_smfmac_f32_16x16x64_bf16 v[60:63], v[28:31], a[80:87], v1\n"
    "s_cbranch_vccnz hw_pollb_%=\n"
    "s_waitcnt vmcnt(6) lgkmcnt(4)\n"
    "v_smfmac_f32_16x16x64_bf16 v[68:71], v[32:35], v[96:103], v1\n"
    "ds_read_b128 v[96:99], v2 offset:16384\n"
    "v_smfmac_f32_16x16x64_bf16 v[56:59], v[32:35], a[88:95], v1\n"
    "ds_read_b128 v[100:103], v2 offset:17408\n"
    "s_waitcnt lgkmcnt(4)\n"
    "v_smfmac_f32_16x16x64_bf16 v[64:67], v[32:35], v[80:87], v1\n"
    "ds_read_b128 v[80:83], v2 offset:18432\n"
    "v_smfmac_f32_16x16x64_bf16 v[60:63], v[32:35], a[96:103], v1\n"
    "ds_read_b128 v[84:87], v2 offset:19456\n"
    "s_waitcnt vmcnt(5) lgkmcnt(4)\n"
    "v_smfmac_f32_16x16x64_bf16 v[68:71], v[36:39], v[88:95], v1\n"
    "ds_read_b128 v[88:91], v2 offset:20480\n"
    "v_smfmac_f32_16x16x64_bf16 v[56:59], v[36:39], a[104:111], v1\n"
    "ds_read_b128 v[92:95], v2 offset:21504\n"
    "v_smfmac_f32_16x16x64_bf16 v[64:67], v[36:39], a[112:119], v1\n"
    "buffer_load_dwordx4 v[136:139], v0, s[40:43], s62 offen offset:0 sc1\n"
    "v_smfmac_f32_16x16x64_bf16 v[60:63], v[36:39], a[120:127], v1\n"
    "buffer_load_dwordx4 v[140:143], v0, s[40:43], s62 offen offset:1024 sc1\n"
    "s_waitcnt vmcnt(6) lgkmcnt(4)\n"
    "v_smfmac_f32_16x16x64_bf16 v[68:71], v[40:43], v[96:103], v1\n"
    "ds_read_b128 v[96:99], v2 offset:22528\n"
    "v_smfmac_f32_16x16x64_bf16 v[56:59], v[40:43], a[128:135], v1\n"
    "ds_read_b128 v[100:103], v2 offset:23552\n"
    "v_smfmac_f32_16x16x64_bf16 v[64:67], v[40:43], a[136:143], v1\n"
    "buffer_load_dwordx4 v[144:147], v0, s[40:43], s62 offen offset:2048 sc1\n"
    "v_smfmac_f32_16x16x64_bf16 v[60:63], v[40:43], a[144:151], v1\n"
    "buffer_load_dwordx4 v[148:151], v0, s[40:43], s62 offen offset:3072 sc1\n"
    "s_waitcnt vmcnt(7) lgkmcnt(4)\n"
    "v_smfmac_f32_16x16x64_bf16 v[68:71], v[44:47], v[80:87], v1\n"
    "ds_read_b128 v[80:83], v2 offset:24576\n"
    "v_smfmac_f32_16x16x64_bf16 v[56:59], v[44:47], a[152:159], v1\n"
    "ds_read_b128 v[84:87], v2 offset:25600\n"
    "s_waitcnt lgkmcnt(4)\n"
    "v_smfmac_f32_16x16x64_bf16 v[64:67], v[44:47], v[88:95], v1\n"
    "ds_read_b128 v[88:91], v2 offset:26624\n"
    "v_smfmac_f32_16x16x64_bf16 v[60:63], v[44:47], a[160:167], v1\n"
    "ds_read_b128 v[92:95], v2 offset:27648\n"
    "s_waitcnt vmcnt(6) lgkmcnt(4)\n"
    "v_smfmac_f32_16x16x64_bf16 v[68:71], v[48:51], v[96:103], v1\n"
    "ds_read_b128 v[96:99], v2 offset:28672\n"
    "v_smfmac_f32_16x16x64_bf16 v[56:59], v[48:51], a[168:175], v1\n"
    "ds_read_b128 v[100:103], v2 offset:29696\n"
    "v_smfmac_f32_16x16x64_bf16 v[64:67], v[48:51], a[176:183], v1\n"
    "buffer_load_dword v14, v128, s[40:43], s58 offen sc1\n"
    "v_smfmac_f32_16x16x64_bf16 v[60:63], v[48:51], a[184:191], v1\n"
    "s_waitcnt vmcnt(6) lgkmcnt(4)\n"
    "v_smfmac_f32_16x16x64_bf16 v[68:71], v[52:55], v[80:87], v1\n"
    "ds_read_b128 v[80:83], v2 offset:30720\n"
    "v_smfmac_f32_16x16x64_bf16 v[56:59], v[52:55], a[192:199], v1\n"
    "ds_read_b128 v[84:87], v2 offset:31744\n"
    "v_smfmac_f32_16x16x64_bf16 v[64:67], v[52:55], a[200:207], v1\n"
    "v_smfmac_f32_16x16x64_bf16 v[60:63], v[52:55], a[208:215], v1\n"
    "v_smfmac_f32_16x16x64_bf16 v[72:75], v[16:19], a[216:223], v1\n"
    "v_mov_b32 v13, v13\n"
    "v_mov_b32 v13, v13\n"
    "v_mov_b32 v13, v13\n"
    "v_mov_b32 v13, v13\n"
    "v_mov_b32 v13, v13\n"
    "v_mov_b32 v13, v13\n"
    "v_mov_b32 v13, v13\n"
    "v_permlane32_swap_b32 v56, v60\n"
    "v_permlane32_swap_b32 v57, v61\n"
    "s_waitcnt lgkmcnt(4)\n"
    "v_smfmac_f32_16x16x64_bf16 v[76:79], v[16:19], v[88:95], v1\n"
    "v_permlane32_swap_b32 v58, v62\n"
    "v_permlane32_swap_b32 v59, v63\n"
    "v_smfmac_f32_16x16x64_bf16 v[72:75], v[20:23], a[224:231], v1\n"
    "v_add_f32 v56, v56, v60\n"
    "v_add_f32 v57, v57, v61\n"
    "s_waitcnt lgkmcnt(2)\n"
    "v_smfmac_f32_16x16x64_bf16 v[76:79], v[20:23], v[96:103], v1\n"
    "v_add_f32 v58, v58, v62\n"
    "v_add_f32 v59, v59, v63\n"
    "v_smfmac_f32_16x16x64_bf16 v[72:75], v[24:27], a[232:239], v1\n"
    "v_cmp_u_f32 vcc, v56, v57\n"
    "v_cmp_u_f32 s[56:57], v58, v59\n"
    "s_waitcnt lgkmcnt(0)\n"
    "v_smfmac_f32_16x16x64_bf16 v[76:79], v[24:27], v[80:87], v1\n"
    "s_or_b64 vcc, vcc, s[56:57]\n"
    "s_cbranch_vccnz hw_failb_%=\n"
    "hw_resumeb_%=:\n"
    "v_cvt_pk_bf16_f32 v60, v56, v57\n"
    "v_smfmac_f32_16x16x64_bf16 v[72:75], v[28:31], a[240:247], v1\n"
    "v_cvt_pk_bf16_f32 v61, v58, v59\n"
    "ds_write_b64 v3, v[60:61] offset:0\n"
    "v_smfmac_f32_16x16x64_bf16 v[76:79], v[28:31], a[248:255], v1\n"
    "v_permlane32_swap_b32 v64, v68\n"
    "v_permlane32_swap_b32 v65, v69\n"
    "v_smfmac_f32_16x16x64_bf16 v[72:75], v[32:35], v[160:167], v1\n"
    "v_permlane32_swap_b32 v66, v70\n"
    "v_permlane32_swap_b32 v67, v71\n"
    "v_smfmac_f32_16x16x64_bf16 v[76:79], v[32:35], v[168:175], v1\n"
    "v_add_f32 v64, v64, v68\n"
    "v_smfmac_f32_16x16x64_bf16 v[72:75], v[36:39], v[176:183], v1\n"
    "v_add_f32 v65, v65, v69\n"
    "v_smfmac_f32_16x16x64_bf16 v[76:79], v[36:39], v[184:191], v1\n"
    "v_add_f32 v66, v66, v70\n"
    "v_smfmac_f32_16x16x64_bf16 v[72:75], v[40:43], v[192:199], v1\n"
    "v_add_f32 v67, v67, v71\n"
    "v_smfmac_f32_16x16x64_bf16 v[76:79], v[40:43], v[200:207], v1\n"
    "v_cvt_pk_bf16_f32 v68, v64, v65\n"
    "v_smfmac_f32_16x16x64_bf16 v[72:75], v[44:47], v[208:215], v1\n"
    "v_cvt_pk_bf16_f32 v69, v66, v67\n"
    "v_smfmac_f32_16x16x64_bf16 v[76:79], v[44:47], v[216:223], v1\n"
    "ds_write_b64 v3, v[68:69] offset:512\n"
    "v_smfmac_f32_16x16x64_bf16 v[72:75], v[48:51], v[224:231], v1\n"
    "v_smfmac_f32_16x16x64_bf16 v[76:79], v[48:51], v[232:239], v1\n"
    "v_smfmac_f32_16x16x64_bf16 v[72:75], v[52:55], v[240:247], v1\n"
    "v_smfmac_f32_16x16x64_bf16 v[76:79], v[52:55], v[248:255], v1\n"
    "s_waitcnt lgkmcnt(0)\n"
    "s_barrier\n"
    "v_mov_b32 v13, v13\n"
    "v_mov_b32 v13, v13\n"
    "v_mov_b32 v13, v13\n"
    "v_mov_b32 v13, v13\n"
    "v_mov_b32 v13, v13\n"
    "v_mov_b32 v13, v13\n"
    "v_permlane32_swap_b32 v72, v76\n"
    "v_permlane32_swap_b32 v73, v77\n"
    "v_permlane32_swap_b32 v74, v78\n"
    "v_permlane32_swap_b32 v75, v79\n"
    "v_pk_add_f32 v[72:73], v[72:73], v[76:77]\n"
    "v_pk_add_f32 v[74:75], v[74:75], v[78:79]\n"
    "v_cvt_pk_bf16_f32 v76, v72, v73\n"
    "v_cvt_pk_bf16_f32 v77, v74, v75\n"
    "ds_write_b64 v3, v[76:77] offset:1024\n"
    "s_waitcnt lgkmcnt(0)\n"
    "s_barrier\n"
    "s_cmp_lg_u32 s45, 1\n"
    "s_cbranch_scc1 hw_n1_%=\n"
    "ds_read_b64_tr_b16 v[104:105], v4 offset:0\n"
    "ds_read_b64_tr_b16 v[106:107], v4 offset:512\n"
    "ds_read_b64_tr_b16 v[108:109], v4 offset:1536\n"
    "ds_read_b64_tr_b16 v[110:111], v4 offset:2048\n"
    "ds_read_b64_tr_b16 v[112:113], v4 offset:3072\n"
    "ds_read_b64_tr_b16 v[114:115], v4 offset:3584\n"
    "ds_read_b64_tr_b16 v[116:117], v4 offset:4608\n"
    "ds_read_b64_tr_b16 v[118:119], v4 offset:5120\n"
    "ds_read_b64_tr_b16 v[120:121], v4 offset:1024\n"
    "ds_read_b64_tr_b16 v[122:123], v4 offset:2560\n"
    "ds_read_b64_tr_b16 v[124:125], v4 offset:4096\n"
    "ds_read_b64_tr_b16 v[126:127], v4 offset:5632\n"
    "s_waitcnt lgkmcnt(0)\n"
    "hw_n1_%=:\n"
    "buffer_store_dwordx4 v[104:107], v6, s[40:43], s34 offen sc1\n"
    "buffer_store_dwordx4 v[108:111], v6, s[40:43], s35 offen sc1\n"
    "buffer_store_dwordx4 v[112:115], v6, s[40:43], s36 offen sc1\n"
    "buffer_store_dwordx4 v[116:119], v6, s[40:43], s37 offen sc1\n"
    "buffer_store_dwordx4 v[120:123], v6, s[40:43], s38 offen sc1\n"
    "buffer_store_dwordx4 v[124:127], v6, s[40:43], s39 offen sc1\n"
    "s_waitcnt vmcnt(10)\n"
    "ds_write2st64_b64 v129, v[136:137], v[138:139] offset1:2\n"
    "s_waitcnt vmcnt(9)\n"
    "ds_write2st64_b64 v130, v[140:141], v[142:143] offset1:2\n"
    "s_waitcnt vmcnt(8)\n"
    "ds_write2st64_b64 v131, v[144:145], v[146:147] offset1:2\n"
    "s_waitcnt vmcnt(7)\n"
    "ds_write2st64_b64 v132, v[148:149], v[150:151] offset1:2\n"
    "s_add_u32 s49, s47, 1\n"
    "v_mov_b32 v8, s49\n"
    "s_waitcnt vmcnt(6)\n"
    "v_add_u32 v3, s61, v3\n"
    "v_add_u32 v4, s61, v4\n"
    "s_sub_u32 s61, 0, s61\n"
    "s_add_u32 s47, s47, 1\n"
    "s_and_b32 s59, s47, 63\n"
    "s_cbranch_scc0 hw_flush_%=\n"
    "s_cmp_eq_u32 s62, s31\n"
    "s_cbranch_scc1 hw_loopb_%=\n"
    "v_mov_b32 v135, 0\n"
    "s_mov_b32 s60, 0\n"
    "v_cmp_gt_u32 vcc, s65, v135\n"
    "s_nop 4\n"
    "s_cbranch_vccz hw_kx1_ok_%=\n"
    "hw_kx1_poll_%=:\n"
    "buffer_load_dword v135, v134, s[40:43], s58 offen sc1\n"
    "s_waitcnt vmcnt(0)\n"
    "v_cmp_gt_u32 vcc, s65, v135\n"
    "s_nop 4\n"
    "s_cbranch_vccz hw_kx1_ok_%=\n"
    "s_sleep 4\n"
    "s_add_u32 s60, s60, 1\n"
    "s_cmp_gt_u32 s60, 65536\n"
    "s_cbranch_scc1 hw_abort_%=\n"
    "s_and_b32 s59, s60, 63\n"
    "s_cbranch_scc1 hw_kx1_poll_%=\n"
    "v_mov_b32 v13, 1024\n"
    "buffer_load_dword v13, v13, s[40:43], s58 offen sc1\n"
    "s_waitcnt vmcnt(0)\n"
    "v_readfirstlane_b32 s59, v13\n"
    "s_nop 0\n"
    "s_cmp_lg_u32 s59, 0\n"
    "s_nop 2\n"
    "s_cbranch_scc1 hw_end_%=\n"
    "s_branch hw_kx1_poll_%=\n"
    "hw_kx1_ok_%=:\n"
    "buffer_load_dwordx4 v[136:139], v0, s[40:43], s31 offen sc1\n"
    "buffer_load_dwordx4 v[140:143], v0, s[40:43], s31 offen offset:1024 sc1\n"
    "buffer_load_dwordx4 v[144:147], v0, s[40:43], s31 offen offset:2048 sc1\n"
    "buffer_load_dwordx4 v[148:151], v0, s[40:43], s31 offen offset:3072 sc1\n"
    "s_waitcnt vmcnt(3)\n"
    "ds_write2st64_b64 v129, v[136:137], v[138:139] offset1:2\n"
    "s_waitcnt vmcnt(2)\n"
    "ds_write2st64_b64 v130, v[140:141], v[142:143] offset1:2\n"
    "s_waitcnt vmcnt(1)\n"
    "ds_write2st64_b64 v131, v[144:145], v[146:147] offset1:2\n"
    "s_waitcnt vmcnt(0)\n"
    "ds_write2st64_b64 v132, v[148:149], v[150:151] offset1:2\n"
    "s_branch hw_loopb_%=\n"
    "hw_flush_%=:\n"
    "s_waitcnt vmcnt(0)\n"
    "buffer_store_dword v8, v133, s[40:43], s64 offen sc1\n"
    "s_cmp_lt_u32 s47, 128\n"
    "s_nop 0\n"
    "s_cbranch_scc1 hw_first_%=\n"
    "s_branch hw_end_%=\n"
    "hw_failb_%=:\n"
    "v_max_u32 v12, v16, v17\n"
    "v_max3_u32 v12, v18, v19, v12\n"
    "v_max3_u32 v12, v20, v21, v12\n"
    "v_max3_u32 v12, v22, v23, v12\n"
    "v_max3_u32 v12, v24, v25, v12\n"
    "v_max3_u32 v12, v26, v27, v12\n"
    "v_max3_u32 v12, v28, v29, v12\n"
    "v_max3_u32 v12, v30, v31, v12\n"
    "v_max3_u32 v12, v32, v33, v12\n"
    "v_max3_u32 v12, v34, v35, v12\n"
    "v_max3_u32 v12, v36, v37, v12\n"
    "v_max3_u32 v12, v38, v39, v12\n"
    "v_max3_u32 v12, v40, v41, v12\n"
    "v_max3_u32 v12, v42, v43, v12\n"
    "v_max3_u32 v12, v44, v45, v12\n"
    "v_max3_u32 v12, v46, v47, v12\n"
    "v_max3_u32 v12, v48, v49, v12\n"
    "v_max3_u32 v12, v50, v51, v12\n"
    "v_max3_u32 v12, v52, v53, v12\n"
    "v_max3_u32 v12, v54, v55, v12\n"
    "v_cmp_eq_u32 vcc, -1, v12\n"
    "s_nop 4\n"
    "s_cbranch_vccz hw_resumeb_%=\n"
    "hw_pollb_%=:\n"
    "s_nop 7\n"
    "s_waitcnt vmcnt(0) lgkmcnt(0)\n"
    "v_mov_b32 v56, 0\n"
    "v_mov_b32 v57, 0\n"
    "v_mov_b32 v58, 0\n"
    "v_mov_b32 v59, 0\n"
    "v_mov_b32 v60, 0\n"
    "v_mov_b32 v61, 0\n"
    "v_mov_b32 v62, 0\n"
    "v_mov_b32 v63, 0\n"
    "v_mov_b32 v64, 0\n"
    "v_mov_b32 v65, 0\n"
    "v_mov_b32 v66, 0\n"
    "v_mov_b32 v67, 0\n"
    "v_mov_b32 v68, 0\n"
    "v_mov_b32 v69, 0\n"
    "v_mov_b32 v70, 0\n"
    "v_mov_b32 v71, 0\n"
    "v_mov_b32 v72, 0\n"
    "v_mov_b32 v73, 0\n"
    "v_mov_b32 v74, 0\n"
    "v_mov_b32 v75, 0\n"
    "v_mov_b32 v76, 0\n"
    "v_mov_b32 v77, 0\n"
    "v_mov_b32 v78, 0\n"
    "v_mov_b32 v79, 0\n"
    "ds_read_b128 v[80:83], v2 offset:0\n"
    "ds_read_b128 v[84:87], v2 offset:1024\n"
    "ds_read_b128 v[88:91], v2 offset:2048\n"
    "ds_read_b128 v[92:95], v2 offset:3072\n"
    "ds_read_b128 v[96:99], v2 offset:4096\n"
    "ds_read_b128 v[100:103], v2 offset:5120\n"
    "v_sub_u32 v12, v158, v0\n"
    "v_lshl_add_u32 v12, v15, 7, v12\n"
    "v_and_b32 v18, 15, v15\n"
    "v_sub_u32 v18, v18, v15\n"
    "v_lshl_add_u32 v18, v18, 7, v12\n"
    "s_movk_i32 s55, 0x4000\n"
    "s_waitcnt vmcnt(0) lgkmcnt(0)\n"
    "s_add_u32 s60, s60, 1\n"
    "s_cmp_gt_u32 s60, 65536\n"
    "s_cbranch_scc1 hw_abort_%=\n"
    "s_and_b32 s59, s60, 63\n"
    "s_cbranch_scc1 hw_againb_%=\n"
    "v_mov_b32 v13, 1024\n"
    "buffer_load_dword v13, v13, s[40:43], s58 offen sc1\n"
    "s_waitcnt vmcnt(0)\n"
    "v_readfirstlane_b32 s59, v13\n"
    "s_nop 0\n"
    "s_cmp_lg_u32 s59, 0\n"
    "s_nop 2\n"
    "s_cbranch_scc1 hw_end_%=\n"
    "s_branch hw_againb_%=\n"
    "hw_againb_%=:\n"
    "buffer_load_dword v16, v12, s[40:43], 0 offen sc1\n"
    "buffer_load_dword v17, v18, s[40:43], s55 offen sc1\n"
    "s_waitcnt vmcnt(0)\n"
    "v_max_u32 v16, v16, v17\n"
    "v_cmp_eq_u32 vcc, -1, v16\n"
    "s_nop 4\n"
    "s_cbranch_vccz hw_retryb_%=\n"
    "s_add_u32 s60, s60, 1\n"
    "s_cmp_gt_u32 s60, 65536\n"
    "s_cbranch_scc1 hw_abort_%=\n"
    "s_and_b32 s59, s60, 63\n"
    "s_cbranch_scc1 hw_againb_%=\n"
    "v_mov_b32 v13, 1024\n"
    "buffer_load_dword v13, v13, s[40:43], s58 offen sc1\n"
    "s_waitcnt vmcnt(0)\n"
    "v_readfirstlane_b32 s59, v13\n"
    "s_nop 0\n"
    "s_cmp_lg_u32 s59, 0\n"
    "s_nop 2\n"
    "s_cbranch_scc1 hw_end_%=\n"
    "s_branch hw_againb_%=\n"
    "s_branch hw_end_%=\n"
    "hw_abort_%=:\n"
    "v_mov_b32 v13, 1\n"
    "v_mov_b32 v14, 1024\n"
    "buffer_store_dword v13, v14, s[40:43], s58 offen sc1\n"
    "hw_end_%=:\n"
    "s_endpgm\n"
    :: CHAIN_INPUTS : CHAIN_CLOBBERS);
    __builtin_unreachable();
}
DEV void role_col(unsigned lo, unsigned hi, int id, int w, int aux, const Tune& tn) {
    asm volatile(
    "s_mov_b32 s40, %0\n"
    "s_mov_b32 s41, %1\n"
    "s_mov_b32 s42, %2\n"
    "s_mov_b32 s43, %3\n"
    "s_mov_b32 s44, %4\n"
    "s_mov_b32 s45, %5\n"
    "s_mov_b32 s46, %6\n"
    "s_mov_b32 s66, %7\n"
    "s_mov_b32 s67, %8\n"
    "s_mov_b32 s68, %9\n"
    "s_mov_b32 s69, %10\n"
    "s_mov_b32 s70, %11\n"
    "s_mov_b32 s71, %12\n"
    "s_mov_b32 s72, %13\n"
    "s_mov_b32 s73, %14\n"
    "s_mov_b64 exec, -1\n"
    "v_mbcnt_lo_u32_b32 v15, -1, 0\n"
    "v_mbcnt_hi_u32_b32 v15, -1, v15\n"
    "v_lshlrev_b32 v0, 4, v15\n"
    "s_mov_b32 s58, 2097152\n"
    "s_mov_b32 s92, 0\n"
    "v_lshrrev_b32 v12, 4, v15\n"
    "v_and_b32 v13, 7, v15\n"
    "v_and_b32 v10, 8, v15\n"
    "v_cmp_eq_u32 vcc, 0, v10\n"
    "v_mov_b32 v10, 0xeeee\n"
    "v_mov_b32 v1, 0x4444\n"
    "v_cndmask_b32 v1, v10, v1, vcc\n"
    "s_lshl_b32 s48, s44, 7\n"
    "v_lshlrev_b32 v3, 4, v13\n"
    "v_add_u32 v3, s48, v3\n"
    "v_and_b32 v10, 0xffffe000, v3\n"
    "v_add_u32 v3, v3, v10\n"
    "v_lshl_add_u32 v3, v12, 15, v3\n"
    "v_mov_b32 v10, 2147483632\n"
    "v_cndmask_b32 v3, v10, v3, vcc\n"
    "s_mov_b32 s47, 0\n"
    "s_cmp_lg_u32 s45, 0\n"
    "s_cbranch_scc1 cw_wx_%=\n"
    "v_mov_b32 v5, 0\n"
    "v_cmp_eq_u32 vcc, 0, v15\n"
    "v_mov_b32 v10, 2147483632\n"
    "s_nop 0\n"
    "v_cndmask_b32 v5, v10, v5, vcc\n"
    "s_mov_b32 s65, 0\n"
    "v_and_b32 v2, 31, v15\n"
    "v_lshlrev_b32 v2, 7, v2\n"
    "v_mov_b32 v7, 0\n"
    "v_mov_b32 v8, 26880\n"
    "s_mov_b32 s55, 32\n"
    "s_mul_i32 s48, s44, 3277\n"
    "s_lshr_b32 s48, s48, 16\n"
    "s_lshl_b32 s48, s48, 7\n"
    "s_add_u32 s48, s48, 512\n"
    "v_add_u32 v4, s48, v5\n"
    "s_mov_b32 s48, 786432\n"
    "buffer_load_dwordx4 v[192:195], v3, s[40:43], s48 offen sc1\n"
    "s_waitcnt vmcnt(0)\n"
    "s_nop 0\n"
    "cw_loopa_%=:\n"
    "s_and_b32 s48, s47, 3\n"
    "s_lshl_b32 s50, s48, 2\n"
    "s_add_u32 s50, s50, s46\n"
    "s_mul_i32 s50, s50, 32768\n"
    "s_add_u32 s50, s50, 786432\n"
    "s_add_u32 s51, s50, 4096\n"
    "s_add_u32 s52, s50, 16384\n"
    "s_add_u32 s64, s65, s70\n"
    "s_and_b32 s61, s47, 31\n"
    "s_mul_i32 s61, s61, 80\n"
    "s_add_u32 s61, s61, s44\n"
    "s_lshl_b32 s61, s61, 15\n"
    "s_add_u32 s61, s61, 115802112\n"
    "s_lshr_b32 s59, s61, 8\n"
    "s_and_b32 s59, s59, 0x2000\n"
    "s_add_u32 s61, s61, s59\n"
    "s_mov_b32 s60, 0\n"
    "v_cmp_gt_u32 vcc, s55, v7\n"
    "s_nop 4\n"
    "s_cbranch_vccz cw_ux_ok_%=\n"
    "cw_ux_poll_%=:\n"
    "buffer_load_dword v7, v8, s[40:43], s58 offen sc1\n"
    "s_waitcnt vmcnt(0)\n"
    "v_cmp_gt_u32 vcc, s55, v7\n"
    "s_nop 4\n"
    "s_cbranch_vccz cw_ux_ok_%=\n"
    "s_sleep 6\n"
    "s_add_u32 s60, s60, 1\n"
    "s_cmp_gt_u32 s60, 65536\n"
    "s_cbranch_scc1 cw_abort_%=\n"
    "s_and_b32 s59, s60, 63\n"
    "s_cbranch_scc1 cw_ux_poll_%=\n"
    "v_mov_b32 v10, 1024\n"
    "buffer_load_dword v10, v10, s[40:43], s58 offen sc1\n"
    "s_waitcnt vmcnt(0)\n"
    "v_readfirstlane_b32 s59, v10\n"
    "s_nop 0\n"
    "s_cmp_lg_u32 s59, 0\n"
    "s_nop 2\n"
    "s_cbranch_scc1 cw_end_%=\n"
    "s_branch cw_ux_poll_%=\n"
    "cw_ux_ok_%=:\n"
    "v_add_u32 v8, 128, v8\n"
    "s_memrealtime s[56:57]\n"
    "s_waitcnt lgkmcnt(0)\n"
    "s_sub_u32 s59, s64, s56\n"
    "s_cmp_gt_u32 s59, 1000\n"
    "s_cbranch_scc1 cw_wm_go_%=\n"
    "cw_wm_%=:\n"
    "s_memrealtime s[56:57]\n"
    "s_waitcnt lgkmcnt(0)\n"
    "s_sub_u32 s59, s64, s56\n"
    "s_cmp_gt_i32 s59, 3\n"
    "s_cbranch_scc1 cw_wm_%=\n"
    "cw_wm_f_%=:\n"
    "s_sub_u32 s59, s59, 1\n"
    "s_cmp_ge_i32 s59, 0\n"
    "s_cbranch_scc1 cw_wm_f_%=\n"
    "cw_wm_go_%=:\n"
    "s_mov_b32 s60, 0\n"
    "buffer_load_dwordx4 v[32:35], v0, s[40:43], s50 offen sc1\n"
    "buffer_load_dwordx4 v[36:39], v0, s[40:43], s50 offen offset:1024 sc1\n"
    "buffer_load_dwordx4 v[40:43], v0, s[40:43], s50 offen offset:2048 sc1\n"
    "buffer_load_dwordx4 v[44:47], v0, s[40:43], s50 offen offset:3072 sc1\n"
    "buffer_load_dwordx4 v[48:51], v0, s[40:43], s51 offen sc1\n"
    "buffer_load_dwordx4 v[52:55], v0, s[40:43], s51 offen offset:1024 sc1\n"
    "buffer_load_dwordx4 v[56:59], v0, s[40:43], s51 offen offset:2048 sc1\n"
    "buffer_load_dwordx4 v[60:63], v0, s[40:43], s51 offen offset:3072 sc1\n"
    "buffer_load_dwordx4 v[64:67], v0, s[40:43], s52 offen sc1\n"
    "buffer_load_dwordx4 v[68:71], v0, s[40:43], s52 offen offset:1024 sc1\n"
    "buffer_load_dwordx4 v[112:115], v0, s[40:43], s61 offen sc1\n"
    "buffer_load_dwordx4 v[116:119], v0, s[40:43], s61 offen offset:1024 sc1\n"
    "buffer_load_dwordx4 v[120:123], v0, s[40:43], s61 offen offset:2048 sc1\n"
    "buffer_load_dwordx4 v[124:127], v0, s[40:43], s61 offen offset:3072 sc1\n"
    "s_add_u32 s61, s61, 4096\n"
    "buffer_load_dwordx4 v[128:131], v0, s[40:43], s61 offen sc1\n"
    "buffer_load_dwordx4 v[132:135], v0, s[40:43], s61 offen offset:1024 sc1\n"
    "buffer_load_dwordx4 v[136:139], v0, s[40:43], s61 offen offset:2048 sc1\n"
    "buffer_load_dwordx4 v[140:143], v0, s[40:43], s61 offen offset:3072 sc1\n"
    "s_add_u32 s61, s61, 12288\n"
    "buffer_load_dwordx4 v[144:147], v0, s[40:43], s61 offen sc1\n"
    "buffer_load_dwordx4 v[148:151], v0, s[40:43], s61 offen offset:1024 sc1\n"
    "buffer_load_dwordx4 v[152:155], v0, s[40:43], s61 offen offset:2048 sc1\n"
    "buffer_load_dwordx4 v[156:159], v0, s[40:43], s61 offen offset:3072 sc1\n"
    "s_add_u32 s61, s61, 4096\n"
    "buffer_load_dwordx4 v[160:163], v0, s[40:43], s61 offen sc1\n"
    "buffer_load_dwordx4 v[164:167], v0, s[40:43], s61 offen offset:1024 sc1\n"
    "buffer_load_dwordx4 v[168:171], v0, s[40:43], s61 offen offset:2048 sc1\n"
    "buffer_load_dwordx4 v[172:175], v0, s[40:43], s61 offen offset:3072 sc1\n"
    "s_add_u32 s61, s61, 4096\n"
    "buffer_load_dword v7, v8, s[40:43], s58 offen sc1\n"
    "cw_mcomp_%=:\n"
    "s_waitcnt vmcnt(26)\n"
    "ds_write_b128 v0, v[32:35] offset:4096\n"
    "s_waitcnt vmcnt(25)\n"
    "ds_write_b128 v0, v[36:39] offset:5120\n"
    "s_waitcnt vmcnt(24)\n"
    "ds_write_b128 v0, v[40:43] offset:6144\n"
    "s_waitcnt vmcnt(23)\n"
    "ds_write_b128 v0, v[44:47] offset:7168\n"
    "s_waitcnt vmcnt(22)\n"
    "ds_write_b128 v0, v[48:51] offset:8192\n"
    "s_waitcnt vmcnt(21)\n"
    "ds_write_b128 v0, v[52:55] offset:9216\n"
    "s_waitcnt vmcnt(20)\n"
    "ds_write_b128 v0, v[56:59] offset:10240\n"
    "s_waitcnt vmcnt(19)\n"
    "ds_write_b128 v0, v[60:63] offset:11264\n"
    "s_waitcnt vmcnt(18)\n"
    "ds_write_b128 v0, v[64:67] offset:12288\n"
    "s_waitcnt vmcnt(17)\n"
    "ds_write_b128 v0, v[68:71] offset:13312\n"
    "v_max_u32 v6, v32, v33\n"
    "v_max3_u32 v6, v34, v35, v6\n"
    "v_max3_u32 v6, v36, v37, v6\n"
    "v_max3_u32 v6, v38, v39, v6\n"
    "v_max3_u32 v6, v40, v41, v6\n"
    "v_max3_u32 v6, v42, v43, v6\n"
    "v_max3_u32 v6, v44, v45, v6\n"
    "v_max3_u32 v6, v46, v47, v6\n"
    "v_max3_u32 v6, v48, v49, v6\n"
    "v_max3_u32 v6, v50, v51, v6\n"
    "v_max3_u32 v6, v52, v53, v6\n"
    "v_max3_u32 v6, v54, v55, v6\n"
    "v_max3_u32 v6, v56, v57, v6\n"
    "v_max3_u32 v6, v58, v59, v6\n"
    "v_max3_u32 v6, v60, v61, v6\n"
    "v_max3_u32 v6, v62, v63, v6\n"
    "v_max3_u32 v6, v64, v65, v6\n"
    "v_max3_u32 v6, v66, v67, v6\n"
    "v_max3_u32 v6, v68, v69, v6\n"
    "v_max3_u32 v6, v70, v71, v6\n"
    "v_cmp_eq_u32 vcc, -1, v6\n"
    "s_nop 4\n"
    "s_cbranch_vccnz cw_mfail_%=\n"
    "s_waitcnt lgkmcnt(0)\n"
    "s_barrier\n"
    "s_mul_i32 s50, s48, 4\n"
    "s_and_b32 s51, s46, 3\n"
    "s_add_u32 s50, s50, s51\n"
    "s_mul_i32 s50, s50, 49152\n"
    "s_add_u32 s51, s50, 4096\n"
    "s_add_u32 s52, s50, 16384\n"
    "s_add_u32 s53, s50, 20480\n"
    "s_add_u32 s62, s50, 32768\n"
    "s_add_u32 s49, s47, 1\n"
    "s_and_b32 s49, s49, 3\n"
    "s_mul_i32 s63, s49, 131072\n"
    "s_add_u32 s63, s63, 786432\n"
    "s_and_b32 s59, s47, 63\n"
    "s_cmp_eq_u32 s59, 63\n"
    "s_cbranch_scc1 cw_last_%=\n"
    "s_add_u32 s64, s65, s69\n"
    "s_memrealtime s[56:57]\n"
    "s_waitcnt lgkmcnt(0)\n"
    "s_sub_u32 s59, s64, s56\n"
    "s_cmp_gt_u32 s59, 1000\n"
    "s_cbranch_scc1 cw_ws_go_%=\n"
    "cw_ws_%=:\n"
    "s_memrealtime s[56:57]\n"
    "s_waitcnt lgkmcnt(0)\n"
    "s_sub_u32 s59, s64, s56\n"
    "s_cmp_gt_i32 s59, 3\n"
    "s_cbranch_scc1 cw_ws_%=\n"
    "cw_ws_f_%=:\n"
    "s_sub_u32 s59, s59, 1\n"
    "s_cmp_ge_i32 s59, 0\n"
    "s_cbranch_scc1 cw_ws_f_%=\n"
    "cw_ws_go_%=:\n"
    "s_and_b32 s59, s47, 64\n"
    "s_add_u32 s54, s47, s59\n"
    "s_lshl_b32 s54, s54, 7\n"
    "s_add_u32 s54, s54, 1310720\n"
    "s_lshl_b32 s59, s46, 15\n"
    "s_add_u32 s59, s59, s54\n"
    "s_add_u32 s56, s40, s59\n"
    "s_addc_u32 s57, s41, 0\n"
    "s_load_dwordx2 s[64:65], s[56:57], 0x0\n"
    "s_waitcnt lgkmcnt(0)\n"
    "s_max_u32 s59, s64, s65\n"
    "s_cmp_lg_u32 s59, -1\n"
    "s_cbranch_scc1 cw_rs_ok_%=\n"
    "s_and_b32 s59, s47, 64\n"
    "s_add_u32 s54, s47, s59\n"
    "s_lshl_b32 s54, s54, 7\n"
    "s_add_u32 s54, s54, 1310720\n"
    "s_mov_b32 s60, 0\n"
    "cw_rs_%=:\n"
    "buffer_load_dwordx2 v[10:11], v5, s[40:43], s54 offen sc1\n"
    "s_waitcnt vmcnt(0)\n"
    "v_readfirstlane_b32 s64, v10\n"
    "v_readfirstlane_b32 s65, v11\n"
    "s_nop 0\n"
    "s_max_u32 s59, s64, s65\n"
    "s_cmp_lg_u32 s59, -1\n"
    "s_nop 1\n"
    "s_cbranch_scc1 cw_rs_ok_%=\n"
    "s_sleep 4\n"
    "s_add_u32 s60, s60, 1\n"
    "s_cmp_gt_u32 s60, 65536\n"
    "s_cbranch_scc1 cw_abort_%=\n"
    "s_and_b32 s59, s60, 63\n"
    "s_cbranch_scc1 cw_rs_%=\n"
    "v_mov_b32 v10, 1024\n"
    "buffer_load_dword v10, v10, s[40:43], s58 offen sc1\n"
    "s_waitcnt vmcnt(0)\n"
    "v_readfirstlane_b32 s59, v10\n"
    "s_nop 0\n"
    "s_cmp_lg_u32 s59, 0\n"
    "s_nop 2\n"
    "s_cbranch_scc1 cw_end_%=\n"
    "s_branch cw_rs_%=\n"
    "cw_rs_ok_%=:\n"
    "s_mov_b32 s60, 0\n"
    "s_memrealtime s[56:57]\n"
    "s_waitcnt lgkmcnt(0)\n"
    "s_sub_u32 s59, s64, s56\n"
    "s_cmp_gt_u32 s59, 1000\n"
    "s_cbranch_scc1 cw_wu_go_%=\n"
    "cw_wu_%=:\n"
    "s_memrealtime s[56:57]\n"
    "s_waitcnt lgkmcnt(0)\n"
    "s_sub_u32 s59, s64, s56\n"
    "s_cmp_gt_i32 s59, 15\n"
    "s_cbranch_scc1 cw_wu_%=\n"
    "s_memrealtime s[30:31]\n"
    "s_nop 1\n"
    "s_memrealtime s[18:19]\n"
    "s_nop 1\n"
    "s_memrealtime s[34:35]\n"
    "s_nop 1\n"
    "s_memrealtime s[36:37]\n"
    "s_nop 1\n"
    "s_memrealtime s[38:39]\n"
    "s_nop 1\n"
    "s_memrealtime s[94:95]\n"
    "s_nop 1\n"
    "s_memrealtime s[96:97]\n"
    "s_nop 1\n"
    "s_memrealtime s[98:99]\n"
    "s_nop 1\n"
    "s_getpc_b64 s[56:57]\n"
    "cw_wu_pc_%=:\n"
    "s_waitcnt lgkmcnt(0)\n"
    "s_add_u32 s18, s18, s34\n"
    "s_add_u32 s18, s18, s36\n"
    "s_add_u32 s18, s18, s38\n"
    "s_add_u32 s18, s18, s94\n"
    "s_add_u32 s18, s18, s96\n"
    "s_add_u32 s18, s18, s98\n"
    "s_mul_i32 s34, s30, 7\n"
    "s_sub_u32 s18, s18, s34\n"
    "s_mul_i32 s18, s18, 3\n"
    "s_sub_u32 s59, s64, s30\n"
    "s_mul_i32 s59, s59, 23\n"
    "s_sub_u32 s59, s59, s18\n"
    "s_sub_u32 s59, s59, 170\n"
    "s_max_i32 s59, s59, 0\n"
    "s_min_i32 s59, s59, 252\n"
    "s_andn2_b32 s59, s59, 3\n"
    "s_sub_u32 s59, cw_wu_e_%=-cw_wu_pc_%=, s59\n"
    "s_add_u32 s56, s56, s59\n"
    "s_addc_u32 s57, s57, 0\n"
    "s_setpc_b64 s[56:57]\n"
    "s_nop 0\n"
    "s_nop 0\n"
    "s_nop 0\n"
    "s_nop 0\n"
    "s_nop 0\n"
    "s_nop 0\n"
    "s_nop 0\n"
    "s_nop 0\n"
    "s_nop 0\n"
    "s_nop 0\n"
    "s_nop 0\n"
    "s_nop 0\n"
    "s_nop 0\n"
    "s_nop 0\n"
    "s_nop 0\n"
    "s_nop 0\n"
    "s_nop 0\n"
    "s_nop 0\n"
    "s_nop 0\n"
    "s_nop 0\n"
    "s_nop 0\n"
    "s_nop 0\n"
    "s_nop 0\n"
    "s_nop 0\n"
    "s_nop 0\n"
    "s_nop 0\n"
    "s_nop 0\n"
    "s_nop 0\n"
    "s_nop 0\n"
    "s_nop 0\n"
    "s_nop 0\n"
    "s_nop 0\n"
    "s_nop 0\n"
    "s_nop 0\n"
    "s_nop 0\n"
    "s_nop 0\n"
    "s_nop 0\n"
    "s_nop 0\n"
    "s_nop 0\n"
    "s_nop 0\n"
    "s_nop 0\n"
    "s_nop 0\n"
    "s_nop 0\n"
    "s_nop 0\n"
    "s_nop 0\n"
    "s_nop 0\n"
    "s_nop 0\n"
    "s_nop 0\n"
    "s_nop 0\n"
    "s_nop 0\n"
    "s_nop 0\n"
    "s_nop 0\n"
    "s_nop 0\n"
    "s_nop 0\n"
    "s_nop 0\n"
    "s_nop 0\n"
    "s_nop 0\n"
    "s_nop 0\n"
    "s_nop 0\n"
    "s_nop 0\n"
    "s_nop 0\n"
    "s_nop 0\n"
    "s_nop 0\n"
    "cw_wu_e_%=:\n"
    "cw_wu_go_%=:\n"
    "buffer_load_dwordx4 v[32:35], v0, s[40:43], s50 offen sc1\n"
    "buffer_load_dwordx4 v[36:39], v0, s[40:43], s50 offen offset:1024 sc1\n"
    "buffer_load_dwordx4 v[40:43], v0, s[40:43], s50 offen offset:2048 sc1\n"
    "buffer_load_dwordx4 v[44:47], v0, s[40:43], s50 offen offset:3072 sc1\n"
    "buffer_load_dwordx4 v[48:51], v0, s[40:43], s51 offen sc1\n"
    "buffer_load_dwordx4 v[52:55], v0, s[40:43], s51 offen offset:1024 sc1\n"
    "buffer_load_dwordx4 v[56:59], v0, s[40:43], s51 offen offset:2048 sc1\n"
    "buffer_load_dwordx4 v[60:63], v0, s[40:43], s51 offen offset:3072 sc1\n"
    "buffer_load_dwordx4 v[64:67], v0, s[40:43], s52 offen sc1\n"
    "buffer_load_dwordx4 v[68:71], v0, s[40:43], s52 offen offset:1024 sc1\n"
    "buffer_load_dwordx4 v[72:75], v0, s[40:43], s52 offen offset:2048 sc1\n"
    "buffer_load_dwordx4 v[76:79], v0, s[40:43], s52 offen offset:3072 sc1\n"
    "buffer_load_dwordx4 v[80:83], v0, s[40:43], s53 offen sc1\n"
    "buffer_load_dwordx4 v[84:87], v0, s[40:43], s53 offen offset:1024 sc1\n"
    "buffer_load_dwordx4 v[88:91], v0, s[40:43], s53 offen offset:2048 sc1\n"
    "buffer_load_dwordx4 v[92:95], v0, s[40:43], s53 offen offset:3072 sc1\n"
    "buffer_load_dwordx4 v[96:99], v0, s[40:43], s62 offen sc1\n"
    "buffer_load_dwordx4 v[100:103], v0, s[40:43], s62 offen offset:1024 sc1\n"
    "buffer_load_dwordx4 v[104:107], v0, s[40:43], s62 offen offset:2048 sc1\n"
    "buffer_load_dwordx4 v[108:111], v0, s[40:43], s62 offen offset:3072 sc1\n"
    "s_barrier\n"
    "ds_read_b128 v[176:179], v0 offset:0\n"
    "ds_read_b128 v[180:183], v0 offset:1024\n"
    "ds_read_b128 v[184:187], v0 offset:2048\n"
    "ds_read_b128 v[188:191], v0 offset:3072\n"
    "cw_pcomp_%=:\n"
    "s_waitcnt vmcnt(19)\n"
    "v_mfma_f32_16x16x32_bf16 v[24:27], v[112:115], v[32:35], 0\n"
    "s_waitcnt vmcnt(18)\n"
    "v_mfma_f32_16x16x32_bf16 v[24:27], v[116:119], v[36:39], v[24:27]\n"
    "s_waitcnt vmcnt(17)\n"
    "v_mfma_f32_16x16x32_bf16 v[24:27], v[120:123], v[40:43], v[24:27]\n"
    "s_waitcnt vmcnt(16)\n"
    "v_mfma_f32_16x16x32_bf16 v[24:27], v[124:127], v[44:47], v[24:27]\n"
    "s_waitcnt vmcnt(15)\n"
    "v_mfma_f32_16x16x32_bf16 v[24:27], v[128:131], v[48:51], v[24:27]\n"
    "s_waitcnt vmcnt(14)\n"
    "v_mfma_f32_16x16x32_bf16 v[24:27], v[132:135], v[52:55], v[24:27]\n"
    "s_waitcnt vmcnt(13)\n"
    "v_mfma_f32_16x16x32_bf16 v[24:27], v[136:139], v[56:59], v[24:27]\n"
    "s_waitcnt vmcnt(12)\n"
    "v_mfma_f32_16x16x32_bf16 v[24:27], v[140:143], v[60:63], v[24:27]\n"
    "s_waitcnt vmcnt(11)\n"
    "v_mfma_f32_16x16x32_bf16 v[24:27], v[144:147], v[64:67], v[24:27]\n"
    "s_waitcnt vmcnt(10)\n"
    "v_mfma_f32_16x16x32_bf16 v[24:27], v[148:151], v[68:71], v[24:27]\n"
    "s_waitcnt vmcnt(9)\n"
    "v_mfma_f32_16x16x32_bf16 v[24:27], v[152:155], v[72:75], v[24:27]\n"
    "s_waitcnt vmcnt(8)\n"
    "v_mfma_f32_16x16x32_bf16 v[24:27], v[156:159], v[76:79], v[24:27]\n"
    "s_waitcnt vmcnt(7)\n"
    "v_mfma_f32_16x16x32_bf16 v[24:27], v[160:163], v[80:83], v[24:27]\n"
    "s_waitcnt vmcnt(6)\n"
    "v_mfma_f32_16x16x32_bf16 v[24:27], v[164:167], v[84:87], v[24:27]\n"
    "s_waitcnt vmcnt(5)\n"
    "v_mfma_f32_16x16x32_bf16 v[24:27], v[168:171], v[88:91], v[24:27]\n"
    "s_waitcnt vmcnt(4)\n"
    "v_mfma_f32_16x16x32_bf16 v[24:27], v[172:175], v[92:95], v[24:27]\n"
    "s_waitcnt vmcnt(3) lgkmcnt(3)\n"
    "v_mfma_f32_16x16x32_bf16 v[24:27], v[176:179], v[96:99], v[24:27]\n"
    "s_waitcnt vmcnt(2) lgkmcnt(2)\n"
    "v_mfma_f32_16x16x32_bf16 v[24:27], v[180:183], v[100:103], v[24:27]\n"
    "s_waitcnt vmcnt(1) lgkmcnt(1)\n"
    "v_mfma_f32_16x16x32_bf16 v[24:27], v[184:187], v[104:107], v[24:27]\n"
    "s_waitcnt vmcnt(0) lgkmcnt(0)\n"
    "v_mfma_f32_16x16x32_bf16 v[24:27], v[188:191], v[108:111], v[24:27]\n"
    "v_mov_b32 v11, v11\n"
    "v_mov_b32 v11, v11\n"
    "v_mov_b32 v11, v11\n"
    "v_mov_b32 v11, v11\n"
    "v_mov_b32 v11, v11\n"
    "v_mov_b32 v11, v11\n"
    "v_mov_b32 v11, v11\n"
    "v_mov_b32 v11, v11\n"
    "v_cmp_u_f32 vcc, v24, v24\n"
    "v_permlane32_swap_b32 v24, v26\n"
    "v_permlane32_swap_b32 v25, v27\n"
    "s_nop 0\n"
    "v_add_f32_dpp v24, v26, v24 row_ror:8 row_mask:0xf bank_mask:0xf\n"
    "v_add_f32_dpp v25, v27, v25 row_ror:8 row_mask:0xf bank_mask:0xf\n"
    "v_cvt_pk_bf16_f32 v28, v24, v25\n"
    "v_cvt_pk_bf16_f32 v30, v24, v25\n"
    "s_nop 1\n"
    "v_permlane16_swap_b32 v28, v30\n"
    "v_mov_b32 v29, v28\n"
    "v_mov_b32 v31, v30\n"
    "s_nop 0\n"
    "v_permlane32_swap_b32 v28, v29\n"
    "v_permlane32_swap_b32 v30, v31\n"
    "s_cbranch_vccnz cw_pfail_%=\n"
    "cw_presume_%=:\n"
    "buffer_store_dwordx4 v[28:31], v3, s[40:43], s63 offen sc1\n"
    "s_memrealtime s[30:31]\n"
    "s_nop 1\n"
    "s_memrealtime s[18:19]\n"
    "s_nop 1\n"
    "s_memrealtime s[34:35]\n"
    "s_nop 1\n"
    "s_memrealtime s[36:37]\n"
    "s_nop 1\n"
    "s_memrealtime s[38:39]\n"
    "s_nop 1\n"
    "s_memrealtime s[94:95]\n"
    "s_nop 1\n"
    "s_memrealtime s[96:97]\n"
    "s_nop 1\n"
    "s_memrealtime s[98:99]\n"
    "s_nop 1\n"
    "s_waitcnt lgkmcnt(0)\n"
    "s_add_u32 s18, s18, s34\n"
    "s_add_u32 s18, s18, s36\n"
    "s_add_u32 s18, s18, s38\n"
    "s_add_u32 s18, s18, s94\n"
    "s_add_u32 s18, s18, s96\n"
    "s_add_u32 s18, s18, s98\n"
    "s_mul_i32 s34, s30, 7\n"
    "s_sub_u32 s18, s18, s34\n"
    "s_mul_i32 s18, s18, 3\n"
    "s_sub_u32 s59, s30, s64\n"
    "s_mul_i32 s59, s59, 23\n"
    "s_add_u32 s59, s59, s18\n"
    "v_mov_b32 v10, s59\n"
    "s_lshl_b32 s34, s47, 10\n"
    "s_add_u32 s34, s34, 2366976\n"
    "s_cmp_eq_u32 s60, 0\n"
    "s_cselect_b32 s34, s34, 0x40000000\n"
    "buffer_atomic_umax v10, v4, s[40:43], s34 offen\n"
    "s_add_u32 s47, s47, 1\n"
    "s_nop 0\n"
    "s_branch cw_loopa_%=\n"
    "cw_last_%=:\n"
    "s_cmp_eq_u32 s47, 127\n"
    "s_cbranch_scc1 cw_end_%=\n"
    "buffer_store_dwordx4 v[192:195], v3, s[40:43], s63 offen sc1\n"
    "s_waitcnt vmcnt(0)\n"
    "s_barrier\n"
    "s_add_u32 s47, s47, 1\n"
    "s_branch cw_loopa_%=\n"
    "cw_pagain_%=:\n"
    "buffer_load_dwordx4 v[32:35], v0, s[40:43], s50 offen sc1\n"
    "buffer_load_dwordx4 v[36:39], v0, s[40:43], s50 offen offset:1024 sc1\n"
    "buffer_load_dwordx4 v[40:43], v0, s[40:43], s50 offen offset:2048 sc1\n"
    "buffer_load_dwordx4 v[44:47], v0, s[40:43], s50 offen offset:3072 sc1\n"
    "buffer_load_dwordx4 v[48:51], v0, s[40:43], s51 offen sc1\n"
    "buffer_load_dwordx4 v[52:55], v0, s[40:43], s51 offen offset:1024 sc1\n"
    "buffer_load_dwordx4 v[56:59], v0, s[40:43], s51 offen offset:2048 sc1\n"
    "buffer_load_dwordx4 v[60:63], v0, s[40:43], s51 offen offset:3072 sc1\n"
    "buffer_load_dwordx4 v[64:67], v0, s[40:43], s52 offen sc1\n"
    "buffer_load_dwordx4 v[68:71], v0, s[40:43], s52 offen offset:1024 sc1\n"
    "buffer_load_dwordx4 v[72:75], v0, s[40:43], s52 offen offset:2048 sc1\n"
    "buffer_load_dwordx4 v[76:79], v0, s[40:43], s52 offen offset:3072 sc1\n"
    "buffer_load_dwordx4 v[80:83], v0, s[40:43], s53 offen sc1\n"
    "buffer_load_dwordx4 v[84:87], v0, s[40:43], s53 offen offset:1024 sc1\n"
    "buffer_load_dwordx4 v[88:91], v0, s[40:43], s53 offen offset:2048 sc1\n"
    "buffer_load_dwordx4 v[92:95], v0, s[40:43], s53 offen offset:3072 sc1\n"
    "buffer_load_dwordx4 v[96:99], v0, s[40:43], s62 offen sc1\n"
    "buffer_load_dwordx4 v[100:103], v0, s[40:43], s62 offen offset:1024 sc1\n"
    "buffer_load_dwordx4 v[104:107], v0, s[40:43], s62 offen offset:2048 sc1\n"
    "buffer_load_dwordx4 v[108:111], v0, s[40:43], s62 offen offset:3072 sc1\n"
    "s_nop 1\n"
    "s_branch cw_pcomp_%=\n"
    "cw_mfail_%=:\n"
    "s_sleep 6\n"
    "s_add_u32 s60, s60, 1\n"
    "s_cmp_gt_u32 s60, 65536\n"
    "s_cbranch_scc1 cw_abort_%=\n"
    "s_and_b32 s59, s60, 63\n"
    "s_cbranch_scc1 cw_mretry_%=\n"
    "v_mov_b32 v10, 1024\n"
    "buffer_load_dword v10, v10, s[40:43], s58 offen sc1\n"
    "s_waitcnt vmcnt(0)\n"
    "v_readfirstlane_b32 s59, v10\n"
    "s_nop 0\n"
    "s_cmp_lg_u32 s59, 0\n"
    "s_nop 2\n"
    "s_cbranch_scc1 cw_end_%=\n"
    "s_branch cw_mretry_%=\n"
    "cw_mretry_%=:\n"
    "buffer_load_dwordx4 v[32:35], v0, s[40:43], s50 offen sc1\n"
    "buffer_load_dwordx4 v[36:39], v0, s[40:43], s50 offen offset:1024 sc1\n"
    "buffer_load_dwordx4 v[40:43], v0, s[40:43], s50 offen offset:2048 sc1\n"
    "buffer_load_dwordx4 v[44:47], v0, s[40:43], s50 offen offset:3072 sc1\n"
    "buffer_load_dwordx4 v[48:51], v0, s[40:43], s51 offen sc1\n"
    "buffer_load_dwordx4 v[52:55], v0, s[40:43], s51 offen offset:1024 sc1\n"
    "buffer_load_dwordx4 v[56:59], v0, s[40:43], s51 offen offset:2048 sc1\n"
    "buffer_load_dwordx4 v[60:63], v0, s[40:43], s51 offen offset:3072 sc1\n"
    "buffer_load_dwordx4 v[64:67], v0, s[40:43], s52 offen sc1\n"
    "buffer_load_dwordx4 v[68:71], v0, s[40:43], s52 offen offset:1024 sc1\n"
    "s_waitcnt vmcnt(0)\n"
    "s_nop 0\n"
    "s_branch cw_mcomp_%=\n"
    "cw_pfail_%=:\n"
    "v_max_u32 v6, v32, v33\n"
    "v_max3_u32 v6, v34, v35, v6\n"
    "v_max3_u32 v6, v36, v37, v6\n"
    "v_max3_u32 v6, v38, v39, v6\n"
    "v_max3_u32 v6, v40, v41, v6\n"
    "v_max3_u32 v6, v42, v43, v6\n"
    "v_max3_u32 v6, v44, v45, v6\n"
    "v_max3_u32 v6, v46, v47, v6\n"
    "v_max3_u32 v6, v48, v49, v6\n"
    "v_max3_u32 v6, v50, v51, v6\n"
    "v_max3_u32 v6, v52, v53, v6\n"
    "v_max3_u32 v6, v54, v55, v6\n"
    "v_max3_u32 v6, v56, v57, v6\n"
    "v_max3_u32 v6, v58, v59, v6\n"
    "v_max3_u32 v6, v60, v61, v6\n"
    "v_max3_u32 v6, v62, v63, v6\n"
    "v_max3_u32 v6, v64, v65, v6\n"
    "v_max3_u32 v6, v66, v67, v6\n"
    "v_max3_u32 v6, v68, v69, v6\n"
    "v_max3_u32 v6, v70, v71, v6\n"
    "v_max3_u32 v6, v72, v73, v6\n"
    "v_max3_u32 v6, v74, v75, v6\n"
    "v_max3_u32 v6, v76, v77, v6\n"
    "v_max3_u32 v6, v78, v79, v6\n"
    "v_max3_u32 v6, v80, v81, v6\n"
    "v_max3_u32 v6, v82, v83, v6\n"
    "v_max3_u32 v6, v84, v85, v6\n"
    "v_max3_u32 v6, v86, v87, v6\n"
    "v_max3_u32 v6, v88, v89, v6\n"
    "v_max3_u32 v6, v90, v91, v6\n"
    "v_max3_u32 v6, v92, v93, v6\n"
    "v_max3_u32 v6, v94, v95, v6\n"
    "v_max3_u32 v6, v96, v97, v6\n"
    "v_max3_u32 v6, v98, v99, v6\n"
    "v_max3_u32 v6, v100, v101, v6\n"
    "v_max3_u32 v6, v102, v103, v6\n"
    "v_max3_u32 v6, v104, v105, v6\n"
    "v_max3_u32 v6, v106, v107, v6\n"
    "v_max3_u32 v6, v108, v109, v6\n"
    "v_max3_u32 v6, v110, v111, v6\n"
    "v_cmp_eq_u32 vcc, -1, v6\n"
    "s_nop 4\n"
    "s_cbranch_vccz cw_presume_%=\n"
    "s_cmp_lg_u32 s60, 0\n"
    "s_cbranch_scc1 cw_pspin_%=\n"
    "s_mov_b32 s60, 1\n"
    "s_branch cw_pagain_%=\n"
    "cw_ppoll_%=:\n"
    "buffer_load_dword v10, v2, s[40:43], s62 offen sc1\n"
    "s_waitcnt vmcnt(0)\n"
    "v_cmp_eq_u32 vcc, -1, v10\n"
    "s_nop 4\n"
    "s_cbranch_vccz cw_pagain_%=\n"
    "cw_pspin_%=:\n"
    "s_add_u32 s60, s60, 1\n"
    "s_cmp_gt_u32 s60, 65536\n"
    "s_cbranch_scc1 cw_abort_%=\n"
    "s_and_b32 s59, s60, 63\n"
    "s_cbranch_scc1 cw_ppoll_%=\n"
    "v_mov_b32 v10, 1024\n"
    "buffer_load_dword v10, v10, s[40:43], s58 offen sc1\n"
    "s_waitcnt vmcnt(0)\n"
    "v_readfirstlane_b32 s59, v10\n"
    "s_nop 0\n"
    "s_cmp_lg_u32 s59, 0\n"
    "s_nop 2\n"
    "s_cbranch_scc1 cw_end_%=\n"
    "s_branch cw_ppoll_%=\n"
    "cw_wx_%=:\n"
    "s_mul_i32 s61, s45, 6\n"
    "s_sub_u32 s61, s61, 6\n"
    "s_min_u32 s61, s61, 10\n"
    "s_lshl_b32 s48, s44, 4\n"
    "s_add_u32 s48, s48, s61\n"
    "s_mul_i32 s48, s48, 20480\n"
    "s_add_u32 s48, s48, 33980416\n"
    "s_lshl_b32 s49, s45, 15\n"
    "s_add_u32 s49, s49, -18432\n"
    "v_add_u32 v14, s49, v0\n"
    "buffer_load_dwordx4 v[16:19], v0, s[40:43], s48 offen\n"
    "buffer_load_dwordx4 v[20:23], v0, s[40:43], s48 offen offset:1024\n"
    "buffer_load_dwordx4 v[32:35], v0, s[40:43], s48 offen offset:2048\n"
    "buffer_load_dwordx4 v[36:39], v0, s[40:43], s48 offen offset:3072\n"
    "s_add_u32 s48, s48, 4096\n"
    "buffer_load_dwordx4 v[48:51], v0, s[40:43], s48 offen\n"
    "buffer_load_dwordx4 v[52:55], v0, s[40:43], s48 offen offset:1024\n"
    "buffer_load_dwordx4 v[64:67], v0, s[40:43], s48 offen offset:2048\n"
    "buffer_load_dwordx4 v[68:71], v0, s[40:43], s48 offen offset:3072\n"
    "s_add_u32 s48, s48, 4096\n"
    "buffer_load_dwordx4 v[80:83], v0, s[40:43], s48 offen\n"
    "buffer_load_dwordx4 v[84:87], v0, s[40:43], s48 offen offset:1024\n"
    "buffer_load_dwordx4 v[96:99], v0, s[40:43], s48 offen offset:2048\n"
    "buffer_load_dwordx4 v[100:103], v0, s[40:43], s48 offen offset:3072\n"
    "s_add_u32 s48, s48, 4096\n"
    "buffer_load_dwordx4 v[112:115], v0, s[40:43], s48 offen\n"
    "buffer_load_dwordx4 v[116:119], v0, s[40:43], s48 offen offset:1024\n"
    "buffer_load_dwordx4 v[120:123], v0, s[40:43], s48 offen offset:2048\n"
    "buffer_load_dwordx4 v[124:127], v0, s[40:43], s48 offen offset:3072\n"
    "s_add_u32 s48, s48, 4096\n"
    "buffer_load_dwordx4 v[128:131], v0, s[40:43], s48 offen\n"
    "buffer_load_dwordx4 v[132:135], v0, s[40:43], s48 offen offset:1024\n"
    "buffer_load_dwordx4 v[136:139], v0, s[40:43], s48 offen offset:2048\n"
    "buffer_load_dwordx4 v[140:143], v0, s[40:43], s48 offen offset:3072\n"
    "s_add_u32 s48, s48, 4096\n"
    "buffer_load_dwordx4 a[0:3], v0, s[40:43], s48 offen\n"
    "buffer_load_dwordx4 a[4:7], v0, s[40:43], s48 offen offset:1024\n"
    "buffer_load_dwordx4 a[32:35], v0, s[40:43], s48 offen offset:2048\n"
    "buffer_load_dwordx4 a[36:39], v0, s[40:43], s48 offen offset:3072\n"
    "s_add_u32 s48, s48, 4096\n"
    "buffer_load_dwordx4 a[64:67], v0, s[40:43], s48 offen\n"
    "buffer_load_dwordx4 a[68:71], v0, s[40:43], s48 offen offset:1024\n"
    "buffer_load_dwordx4 a[96:99], v0, s[40:43], s48 offen offset:2048\n"
    "buffer_load_dwordx4 a[100:103], v0, s[40:43], s48 offen offset:3072\n"
    "s_add_u32 s48, s48, 4096\n"
    "buffer_load_dwordx4 a[128:131], v0, s[40:43], s48 offen\n"
    "buffer_load_dwordx4 a[132:135], v0, s[40:43], s48 offen offset:1024\n"
    "buffer_load_dwordx4 a[160:163], v0, s[40:43], s48 offen offset:2048\n"
    "buffer_load_dwordx4 a[164:167], v0, s[40:43], s48 offen offset:3072\n"
    "s_add_u32 s48, s48, 4096\n"
    "buffer_load_dwordx4 a[192:195], v0, s[40:43], s48 offen\n"
    "buffer_load_dwordx4 a[196:199], v0, s[40:43], s48 offen offset:1024\n"
    "buffer_load_dwordx4 a[232:235], v0, s[40:43], s48 offen offset:2048\n"
    "buffer_load_dwordx4 a[236:239], v0, s[40:43], s48 offen offset:3072\n"
    "s_add_u32 s48, s48, 4096\n"
    "buffer_load_dwordx4 v[176:179], v0, s[40:43], s48 offen\n"
    "buffer_load_dwordx4 v[180:183], v0, s[40:43], s48 offen offset:1024\n"
    "buffer_load_dwordx4 v[216:219], v0, s[40:43], s48 offen offset:2048\n"
    "buffer_load_dwordx4 v[220:223], v0, s[40:43], s48 offen offset:3072\n"
    "s_add_u32 s48, s48, 4096\n"
    "buffer_load_dwordx4 a[8:11], v0, s[40:43], s48 offen\n"
    "buffer_load_dwordx4 a[12:15], v0, s[40:43], s48 offen offset:1024\n"
    "buffer_load_dwordx4 a[40:43], v0, s[40:43], s48 offen offset:2048\n"
    "buffer_load_dwordx4 a[44:47], v0, s[40:43], s48 offen offset:3072\n"
    "s_add_u32 s48, s48, 4096\n"
    "buffer_load_dwordx4 a[72:75], v0, s[40:43], s48 offen\n"
    "buffer_load_dwordx4 a[76:79], v0, s[40:43], s48 offen offset:1024\n"
    "buffer_load_dwordx4 a[104:107], v0, s[40:43], s48 offen offset:2048\n"
    "buffer_load_dwordx4 a[108:111], v0, s[40:43], s48 offen offset:3072\n"
    "s_add_u32 s48, s48, 4096\n"
    "buffer_load_dwordx4 a[136:139], v0, s[40:43], s48 offen\n"
    "buffer_load_dwordx4 a[140:143], v0, s[40:43], s48 offen offset:1024\n"
    "buffer_load_dwordx4 a[168:171], v0, s[40:43], s48 offen offset:2048\n"
    "buffer_load_dwordx4 a[172:175], v0, s[40:43], s48 offen offset:3072\n"
    "s_add_u32 s48, s48, 4096\n"
    "buffer_load_dwordx4 a[200:203], v0, s[40:43], s48 offen\n"
    "buffer_load_dwordx4 a[204:207], v0, s[40:43], s48 offen offset:1024\n"
    "buffer_load_dwordx4 a[240:243], v0, s[40:43], s48 offen offset:2048\n"
    "buffer_load_dwordx4 a[244:247], v0, s[40:43], s48 offen offset:3072\n"
    "s_add_u32 s48, s48, 4096\n"
    "buffer_load_dwordx4 v[184:187], v0, s[40:43], s48 offen\n"
    "buffer_load_dwordx4 v[188:191], v0, s[40:43], s48 offen offset:1024\n"
    "buffer_load_dwordx4 v[224:227], v0, s[40:43], s48 offen offset:2048\n"
    "buffer_load_dwordx4 v[228:231], v0, s[40:43], s48 offen offset:3072\n"
    "s_add_u32 s48, s48, 4096\n"
    "buffer_load_dwordx4 v[24:27], v0, s[40:43], s48 offen\n"
    "buffer_load_dwordx4 v[28:31], v0, s[40:43], s48 offen offset:1024\n"
    "buffer_load_dwordx4 v[40:43], v0, s[40:43], s48 offen offset:2048\n"
    "s_waitcnt vmcnt(61)\n"
    "buffer_load_dwordx4 v[44:47], v0, s[40:43], s48 offen offset:3072\n"
    "s_add_u32 s48, s48, 4096\n"
    "buffer_load_dwordx4 v[56:59], v0, s[40:43], s48 offen\n"
    "s_waitcnt vmcnt(61)\n"
    "buffer_load_dwordx4 v[60:63], v0, s[40:43], s48 offen offset:1024\n"
    "buffer_load_dwordx4 v[72:75], v0, s[40:43], s48 offen offset:2048\n"
    "s_waitcnt vmcnt(61)\n"
    "buffer_load_dwordx4 v[76:79], v0, s[40:43], s48 offen offset:3072\n"
    "s_add_u32 s48, s48, 4096\n"
    "buffer_load_dwordx4 v[88:91], v0, s[40:43], s48 offen\n"
    "s_waitcnt vmcnt(61)\n"
    "buffer_load_dwordx4 v[92:95], v0, s[40:43], s48 offen offset:1024\n"
    "buffer_load_dwordx4 v[104:107], v0, s[40:43], s48 offen offset:2048\n"
    "s_waitcnt vmcnt(61)\n"
    "buffer_load_dwordx4 v[108:111], v0, s[40:43], s48 offen offset:3072\n"
    "s_add_u32 s48, s48, 4096\n"
    "buffer_load_dwordx4 a[208:211], v0, s[40:43], s48 offen\n"
    "s_waitcnt vmcnt(61)\n"
    "buffer_load_dwordx4 a[212:215], v0, s[40:43], s48 offen offset:1024\n"
    "buffer_load_dwordx4 a[248:251], v0, s[40:43], s48 offen offset:2048\n"
    "s_waitcnt vmcnt(61)\n"
    "buffer_load_dwordx4 a[252:255], v0, s[40:43], s48 offen offset:3072\n"
    "s_add_u32 s48, s48, 4096\n"
    "buffer_load_dwordx4 v[192:195], v0, s[40:43], s48 offen\n"
    "s_waitcnt vmcnt(61)\n"
    "buffer_load_dwordx4 v[196:199], v0, s[40:43], s48 offen offset:1024\n"
    "buffer_load_dwordx4 v[232:235], v0, s[40:43], s48 offen offset:2048\n"
    "s_waitcnt vmcnt(61)\n"
    "buffer_load_dwordx4 v[236:239], v0, s[40:43], s48 offen offset:3072\n"
    "s_add_u32 s48, s48, 4096\n"
    "buffer_load_dwordx4 a[16:19], v0, s[40:43], s48 offen\n"
    "s_waitcnt vmcnt(61)\n"
    "buffer_load_dwordx4 a[20:23], v0, s[40:43], s48 offen offset:1024\n"
    "buffer_load_dwordx4 a[48:51], v0, s[40:43], s48 offen offset:2048\n"
    "s_waitcnt vmcnt(61)\n"
    "buffer_load_dwordx4 a[52:55], v0, s[40:43], s48 offen offset:3072\n"
    "s_add_u32 s48, s48, 4096\n"
    "buffer_load_dwordx4 a[80:83], v0, s[40:43], s48 offen\n"
    "s_waitcnt vmcnt(61)\n"
    "buffer_load_dwordx4 a[84:87], v0, s[40:43], s48 offen offset:1024\n"
    "buffer_load_dwordx4 a[112:115], v0, s[40:43], s48 offen offset:2048\n"
    "s_waitcnt vmcnt(61)\n"
    "buffer_load_dwordx4 a[116:119], v0, s[40:43], s48 offen offset:3072\n"
    "s_add_u32 s48, s48, 4096\n"
    "buffer_load_dwordx4 a[144:147], v0, s[40:43], s48 offen\n"
    "s_waitcnt vmcnt(61)\n"
    "buffer_load_dwordx4 a[148:151], v0, s[40:43], s48 offen offset:1024\n"
    "buffer_load_dwordx4 a[176:179], v0, s[40:43], s48 offen offset:2048\n"
    "s_waitcnt vmcnt(61)\n"
    "buffer_load_dwordx4 a[180:183], v0, s[40:43], s48 offen offset:3072\n"
    "s_add_u32 s48, s48, 4096\n"
    "buffer_load_dwordx4 a[216:219], v0, s[40:43], s48 offen\n"
    "s_waitcnt vmcnt(61)\n"
    "buffer_load_dwordx4 a[220:223], v0, s[40:43], s48 offen offset:1024\n"
    "buffer_load_dwordx4 v[160:163], v0, s[40:43], s48 offen offset:2048\n"
    "s_waitcnt vmcnt(61)\n"
    "buffer_load_dwordx4 v[164:167], v0, s[40:43], s48 offen offset:3072\n"
    "s_add_u32 s48, s48, 4096\n"
    "buffer_load_dwordx4 v[200:203], v0, s[40:43], s48 offen\n"
    "s_waitcnt vmcnt(61)\n"
    "buffer_load_dwordx4 v[204:207], v0, s[40:43], s48 offen offset:1024\n"
    "buffer_load_dwordx4 v[240:243], v0, s[40:43], s48 offen offset:2048\n"
    "s_waitcnt vmcnt(61)\n"
    "buffer_load_dwordx4 v[244:247], v0, s[40:43], s48 offen offset:3072\n"
    "s_add_u32 s48, s48, 4096\n"
    "buffer_load_dwordx4 a[24:27], v0, s[40:43], s48 offen\n"
    "s_waitcnt vmcnt(61)\n"
    "buffer_load_dwordx4 a[28:31], v0, s[40:43], s48 offen offset:1024\n"
    "buffer_load_dwordx4 a[56:59], v0, s[40:43], s48 offen offset:2048\n"
    "s_waitcnt vmcnt(61)\n"
    "buffer_load_dwordx4 a[60:63], v0, s[40:43], s48 offen offset:3072\n"
    "s_add_u32 s48, s48, 4096\n"
    "buffer_load_dwordx4 a[88:91], v0, s[40:43], s48 offen\n"
    "s_waitcnt vmcnt(61)\n"
    "buffer_load_dwordx4 a[92:95], v0, s[40:43], s48 offen offset:1024\n"
    "buffer_load_dwordx4 a[120:123], v0, s[40:43], s48 offen offset:2048\n"
    "s_waitcnt vmcnt(61)\n"
    "buffer_load_dwordx4 a[124:127], v0, s[40:43], s48 offen offset:3072\n"
    "s_add_u32 s48, s48, 4096\n"
    "buffer_load_dwordx4 a[152:155], v0, s[40:43], s48 offen\n"
    "s_waitcnt vmcnt(61)\n"
    "buffer_load_dwordx4 a[156:159], v0, s[40:43], s48 offen offset:1024\n"
    "buffer_load_dwordx4 a[184:187], v0, s[40:43], s48 offen offset:2048\n"
    "s_waitcnt vmcnt(61)\n"
    "buffer_load_dwordx4 a[188:191], v0, s[40:43], s48 offen offset:3072\n"
    "s_add_u32 s48, s48, 4096\n"
    "buffer_load_dwordx4 a[224:227], v0, s[40:43], s48 offen\n"
    "s_waitcnt vmcnt(61)\n"
    "buffer_load_dwordx4 a[228:231], v0, s[40:43], s48 offen offset:1024\n"
    "buffer_load_dwordx4 v[168:171], v0, s[40:43], s48 offen offset:2048\n"
    "s_waitcnt vmcnt(61)\n"
    "buffer_load_dwordx4 v[172:175], v0, s[40:43], s48 offen offset:3072\n"
    "s_add_u32 s48, s48, 4096\n"
    "buffer_load_dwordx4 v[208:211], v0, s[40:43], s48 offen\n"
    "s_waitcnt vmcnt(61)\n"
    "buffer_load_dwordx4 v[212:215], v0, s[40:43], s48 offen offset:1024\n"
    "buffer_load_dwordx4 v[248:251], v0, s[40:43], s48 offen offset:2048\n"
    "s_waitcnt vmcnt(61)\n"
    "buffer_load_dwordx4 v[252:255], v0, s[40:43], s48 offen offset:3072\n"
    "ds_write_b128 v14, v[16:19] offset:0\n"
    "ds_write_b128 v14, v[20:23] offset:1024\n"
    "s_waitcnt vmcnt(59)\n"
    "ds_write_b128 v14, v[24:27] offset:2048\n"
    "s_waitcnt vmcnt(58)\n"
    "ds_write_b128 v14, v[28:31] offset:3072\n"
    "ds_write_b128 v14, v[32:35] offset:4096\n"
    "ds_write_b128 v14, v[36:39] offset:5120\n"
    "s_waitcnt vmcnt(57)\n"
    "ds_write_b128 v14, v[40:43] offset:6144\n"
    "s_waitcnt vmcnt(56)\n"
    "ds_write_b128 v14, v[44:47] offset:7168\n"
    "ds_write_b128 v14, v[48:51] offset:8192\n"
    "ds_write_b128 v14, v[52:55] offset:9216\n"
    "s_waitcnt vmcnt(55)\n"
    "ds_write_b128 v14, v[56:59] offset:10240\n"
    "s_waitcnt vmcnt(54)\n"
    "ds_write_b128 v14, v[60:63] offset:11264\n"
    "ds_write_b128 v14, v[64:67] offset:12288\n"
    "ds_write_b128 v14, v[68:71] offset:13312\n"
    "s_waitcnt vmcnt(53)\n"
    "ds_write_b128 v14, v[72:75] offset:14336\n"
    "s_waitcnt vmcnt(52)\n"
    "ds_write_b128 v14, v[76:79] offset:15360\n"
    "s_waitcnt lgkmcnt(14)\n"
    "ds_write_b128 v14, v[80:83] offset:16384\n"
    "ds_write_b128 v14, v[84:87] offset:17408\n"
    "s_waitcnt lgkmcnt(14)\n"
    "s_waitcnt vmcnt(51)\n"
    "ds_write_b128 v14, v[88:91] offset:18432\n"
    "s_waitcnt vmcnt(50)\n"
    "ds_write_b128 v14, v[92:95] offset:19456\n"
    "s_waitcnt lgkmcnt(14)\n"
    "ds_write_b128 v14, v[96:99] offset:20480\n"
    "ds_write_b128 v14, v[100:103] offset:21504\n"
    "s_waitcnt lgkmcnt(14)\n"
    "s_waitcnt vmcnt(49)\n"
    "ds_write_b128 v14, v[104:107] offset:22528\n"
    "s_waitcnt vmcnt(48)\n"
    "ds_write_b128 v14, v[108:111] offset:23552\n"
    "s_waitcnt lgkmcnt(14)\n"
    "ds_write_b128 v14, v[112:115] offset:24576\n"
    "ds_write_b128 v14, v[116:119] offset:25600\n"
    "s_waitcnt lgkmcnt(14)\n"
    "ds_write_b128 v14, v[120:123] offset:26624\n"
    "ds_write_b128 v14, v[124:127] offset:27648\n"
    "s_waitcnt lgkmcnt(14)\n"
    "ds_write_b128 v14, v[128:131] offset:28672\n"
    "ds_write_b128 v14, v[132:135] offset:29696\n"
    "s_waitcnt lgkmcnt(14)\n"
    "ds_write_b128 v14, v[136:139] offset:30720\n"
    "ds_write_b128 v14, v[140:143] offset:31744\n"
    "s_waitcnt lgkmcnt(14)\n"
    "s_waitcnt vmcnt(0) lgkmcnt(0)\n"
    "v_mov_b32 v134, v14\n"
    "v_mov_b32 v128, -1\n"
    "v_mov_b32 v129, -1\n"
    "v_mov_b32 v130, -1\n"
    "v_mov_b32 v131, -1\n"
    "s_cmp_eq_u32 s45, 2\n"
    "s_cselect_b32 s59, 1, 0\n"
    "v_lshlrev_b32 v132, 7, v15\n"
    "v_cmp_gt_u32 vcc, s59, v15\n"
    "v_mov_b32 v10, 2147483632\n"
    "s_nop 0\n"
    "v_cndmask_b32 v132, v10, v132, vcc\n"
    "v_mov_b32 v133, 1\n"
    "s_mul_i32 s48, s44, 3277\n"
    "s_lshr_b32 s48, s48, 16\n"
    "s_lshl_b32 s48, s48, 7\n"
    "s_add_u32 s65, s48, 2158592\n"
    "s_mov_b32 s62, 0x40000000\n"
    "s_mov_b32 s63, 0x40000000\n"
    "s_mov_b32 s64, 0x40000000\n"
    "v_and_b32 v9, 0xf0, v0\n"
    "v_lshrrev_b32 v10, 1, v12\n"
    "v_lshl_add_u32 v9, v10, 8, v9\n"
    "v_and_b32 v10, 1, v12\n"
    "v_lshl_add_u32 v9, v10, 3, v9\n"
    "s_lshl_b32 s48, s61, 8\n"
    "v_add_u32 v9, s48, v9\n"
    "cw_loopb_%=:\n"
    "v_mov_b32 v56, 0\n"
    "v_mov_b32 v57, 0\n"
    "v_mov_b32 v58, 0\n"
    "v_mov_b32 v59, 0\n"
    "v_mov_b32 v60, 0\n"
    "v_mov_b32 v61, 0\n"
    "v_mov_b32 v62, 0\n"
    "v_mov_b32 v63, 0\n"
    "v_mov_b32 v64, 0\n"
    "v_mov_b32 v65, 0\n"
    "v_mov_b32 v66, 0\n"
    "v_mov_b32 v67, 0\n"
    "v_mov_b32 v68, 0\n"
    "v_mov_b32 v69, 0\n"
    "v_mov_b32 v70, 0\n"
    "v_mov_b32 v71, 0\n"
    "v_mov_b32 v72, 0\n"
    "v_mov_b32 v73, 0\n"
    "v_mov_b32 v74, 0\n"
    "v_mov_b32 v75, 0\n"
    "v_mov_b32 v76, 0\n"
    "v_mov_b32 v77, 0\n"
    "v_mov_b32 v78, 0\n"
    "v_mov_b32 v79, 0\n"
    "ds_read_b128 v[80:83], v134 offset:0\n"
    "ds_read_b128 v[84:87], v134 offset:1024\n"
    "ds_read_b128 v[88:91], v134 offset:2048\n"
    "ds_read_b128 v[92:95], v134 offset:3072\n"
    "ds_read_b128 v[96:99], v134 offset:4096\n"
    "ds_read_b128 v[100:103], v134 offset:5120\n"
    "s_add_u32 s49, s47, 3\n"
    "s_and_b32 s49, s49, 3\n"
    "s_mul_i32 s54, s49, 131072\n"
    "s_add_u32 s54, s54, 786432\n"
    "s_cmp_eq_u32 s45, 1\n"
    "s_cselect_b32 s54, s54, 0x40000000\n"
    "s_waitcnt vmcnt(0)\n"
    "s_waitcnt lgkmcnt(0)\n"
    "s_barrier\n"
    "ds_read_b128 v[16:19], v0 offset:4096\n"
    "ds_read_b128 v[20:23], v0 offset:5120\n"
    "ds_read_b128 v[24:27], v0 offset:6144\n"
    "ds_read_b128 v[28:31], v0 offset:7168\n"
    "ds_read_b128 v[32:35], v0 offset:8192\n"
    "ds_read_b128 v[36:39], v0 offset:9216\n"
    "ds_read_b128 v[40:43], v0 offset:10240\n"
    "ds_read_b128 v[44:47], v0 offset:11264\n"
    "ds_read_b128 v[48:51], v0 offset:12288\n"
    "ds_read_b128 v[52:55], v0 offset:13312\n"
    "s_waitcnt lgkmcnt(9)\n"
    "v_smfmac_f32_16x16x64_bf16 v[56:59], v[16:19], v[80:87], v1\n"
    "ds_read_b128 v[80:83], v134 offset:6144\n"
    "v_smfmac_f32_16x16x64_bf16 v[60:63], v[16:19], a[0:7], v1\n"
    "ds_read_b128 v[84:87], v134 offset:7168\n"
    "v_smfmac_f32_16x16x64_bf16 v[64:67], v[16:19], a[8:15], v1\n"
    "buffer_store_dwordx4 v[128:131], v3, s[40:43], s54 offen sc1\n"
    "v_smfmac_f32_16x16x64_bf16 v[68:71], v[16:19], v[88:95], v1\n"
    "ds_read_b128 v[88:91], v134 offset:8192\n"
    "v_smfmac_f32_16x16x64_bf16 v[72:75], v[16:19], a[16:23], v1\n"
    "ds_read_b128 v[92:95], v134 offset:9216\n"
    "v_smfmac_f32_16x16x64_bf16 v[76:79], v[16:19], a[24:31], v1\n"
    "buffer_atomic_add v133, v132, s[40:43], s64 offen\n"
    "s_waitcnt lgkmcnt(12)\n"
    "v_smfmac_f32_16x16x64_bf16 v[56:59], v[20:23], v[96:103], v1\n"
    "ds_read_b128 v[96:99], v134 offset:10240\n"
    "v_smfmac_f32_16x16x64_bf16 v[60:63], v[20:23], a[32:39], v1\n"
    "ds_read_b128 v[100:103], v134 offset:11264\n"
    "v_smfmac_f32_16x16x64_bf16 v[64:67], v[20:23], a[40:47], v1\n"
    "buffer_store_dwordx4 v[104:107], v0, s[40:43], s62 offen offset:0 sc1\n"
    "s_waitcnt lgkmcnt(4)\n"
    "v_smfmac_f32_16x16x64_bf16 v[68:71], v[20:23], v[80:87], v1\n"
    "ds_read_b128 v[80:83], v134 offset:12288\n"
    "v_smfmac_f32_16x16x64_bf16 v[72:75], v[20:23], a[48:55], v1\n"
    "ds_read_b128 v[84:87], v134 offset:13312\n"
    "v_smfmac_f32_16x16x64_bf16 v[76:79], v[20:23], a[56:63], v1\n"
    "buffer_store_dwordx4 v[108:111], v0, s[40:43], s62 offen offset:1024 sc1\n"
    "s_waitcnt lgkmcnt(4)\n"
    "v_smfmac_f32_16x16x64_bf16 v[56:59], v[24:27], v[88:95], v1\n"
    "ds_read_b128 v[88:91], v134 offset:14336\n"
    "v_smfmac_f32_16x16x64_bf16 v[60:63], v[24:27], a[64:71], v1\n"
    "ds_read_b128 v[92:95], v134 offset:15360\n"
    "v_smfmac_f32_16x16x64_bf16 v[64:67], v[24:27], a[72:79], v1\n"
    "buffer_store_dwordx4 v[112:115], v0, s[40:43], s62 offen offset:2048 sc1\n"
    "s_waitcnt lgkmcnt(4)\n"
    "v_smfmac_f32_16x16x64_bf16 v[68:71], v[24:27], v[96:103], v1\n"
    "ds_read_b128 v[96:99], v134 offset:16384\n"
    "v_smfmac_f32_16x16x64_bf16 v[72:75], v[24:27], a[80:87], v1\n"
    "ds_read_b128 v[100:103], v134 offset:17408\n"
    "v_smfmac_f32_16x16x64_bf16 v[76:79], v[24:27], a[88:95], v1\n"
    "buffer_store_dwordx4 v[116:119], v0, s[40:43], s62 offen offset:3072 sc1\n"
    "s_waitcnt lgkmcnt(4)\n"
    "v_smfmac_f32_16x16x64_bf16 v[56:59], v[28:31], v[80:87], v1\n"
    "ds_read_b128 v[80:83], v134 offset:18432\n"
    "v_smfmac_f32_16x16x64_bf16 v[60:63], v[28:31], a[96:103], v1\n"
    "ds_read_b128 v[84:87], v134 offset:19456\n"
    "v_smfmac_f32_16x16x64_bf16 v[64:67], v[28:31], a[104:111], v1\n"
    "s_waitcnt lgkmcnt(4)\n"
    "v_smfmac_f32_16x16x64_bf16 v[68:71], v[28:31], v[88:95], v1\n"
    "ds_read_b128 v[88:91], v134 offset:20480\n"
    "v_smfmac_f32_16x16x64_bf16 v[72:75], v[28:31], a[112:119], v1\n"
    "ds_read_b128 v[92:95], v134 offset:21504\n"
    "v_smfmac_f32_16x16x64_bf16 v[76:79], v[28:31], a[120:127], v1\n"
    "s_waitcnt lgkmcnt(4)\n"
    "v_smfmac_f32_16x16x64_bf16 v[56:59], v[32:35], v[96:103], v1\n"
    "ds_read_b128 v[96:99], v134 offset:22528\n"
    "v_smfmac_f32_16x16x64_bf16 v[60:63], v[32:35], a[128:135], v1\n"
    "ds_read_b128 v[100:103], v134 offset:23552\n"
    "v_smfmac_f32_16x16x64_bf16 v[64:67], v[32:35], a[136:143], v1\n"
    "s_waitcnt lgkmcnt(4)\n"
    "v_smfmac_f32_16x16x64_bf16 v[68:71], v[32:35], v[80:87], v1\n"
    "ds_read_b128 v[80:83], v134 offset:24576\n"
    "v_smfmac_f32_16x16x64_bf16 v[72:75], v[32:35], a[144:151], v1\n"
    "ds_read_b128 v[84:87], v134 offset:25600\n"
    "v_smfmac_f32_16x16x64_bf16 v[76:79], v[32:35], a[152:159], v1\n"
    "s_waitcnt lgkmcnt(4)\n"
    "v_smfmac_f32_16x16x64_bf16 v[56:59], v[36:39], v[88:95], v1\n"
    "ds_read_b128 v[88:91], v134 offset:26624\n"
    "v_smfmac_f32_16x16x64_bf16 v[60:63], v[36:39], a[160:167], v1\n"
    "ds_read_b128 v[92:95], v134 offset:27648\n"
    "v_smfmac_f32_16x16x64_bf16 v[64:67], v[36:39], a[168:175], v1\n"
    "s_waitcnt lgkmcnt(4)\n"
    "v_smfmac_f32_16x16x64_bf16 v[68:71], v[36:39], v[96:103], v1\n"
    "ds_read_b128 v[96:99], v134 offset:28672\n"
    "v_smfmac_f32_16x16x64_bf16 v[72:75], v[36:39], a[176:183], v1\n"
    "ds_read_b128 v[100:103], v134 offset:29696\n"
    "v_smfmac_f32_16x16x64_bf16 v[76:79], v[36:39], a[184:191], v1\n"
    "s_waitcnt lgkmcnt(4)\n"
    "v_smfmac_f32_16x16x64_bf16 v[56:59], v[40:43], v[80:87], v1\n"
    "ds_read_b128 v[80:83], v134 offset:30720\n"
    "v_smfmac_f32_16x16x64_bf16 v[60:63], v[40:43], a[192:199], v1\n"
    "ds_read_b128 v[84:87], v134 offset:31744\n"
    "v_smfmac_f32_16x16x64_bf16 v[64:67], v[40:43], a[200:207], v1\n"
    "v_smfmac_f32_16x16x64_bf16 v[68:71], v[40:43], a[208:215], v1\n"
    "v_smfmac_f32_16x16x64_bf16 v[72:75], v[40:43], a[216:223], v1\n"
    "v_smfmac_f32_16x16x64_bf16 v[76:79], v[40:43], a[224:231], v1\n"
    "s_waitcnt lgkmcnt(4)\n"
    "v_smfmac_f32_16x16x64_bf16 v[56:59], v[44:47], v[88:95], v1\n"
    "v_smfmac_f32_16x16x64_bf16 v[60:63], v[44:47], a[232:239], v1\n"
    "v_smfmac_f32_16x16x64_bf16 v[64:67], v[44:47], a[240:247], v1\n"
    "v_smfmac_f32_16x16x64_bf16 v[68:71], v[44:47], a[248:255], v1\n"
    "v_smfmac_f32_16x16x64_bf16 v[72:75], v[44:47], v[160:167], v1\n"
    "v_smfmac_f32_16x16x64_bf16 v[76:79], v[44:47], v[168:175], v1\n"
    "s_waitcnt lgkmcnt(2)\n"
    "v_smfmac_f32_16x16x64_bf16 v[56:59], v[48:51], v[96:103], v1\n"
    "v_smfmac_f32_16x16x64_bf16 v[60:63], v[48:51], v[176:183], v1\n"
    "v_smfmac_f32_16x16x64_bf16 v[64:67], v[48:51], v[184:191], v1\n"
    "v_smfmac_f32_16x16x64_bf16 v[68:71], v[48:51], v[192:199], v1\n"
    "v_smfmac_f32_16x16x64_bf16 v[72:75], v[48:51], v[200:207], v1\n"
    "v_smfmac_f32_16x16x64_bf16 v[76:79], v[48:51], v[208:215], v1\n"
    "s_waitcnt lgkmcnt(0)\n"
    "v_smfmac_f32_16x16x64_bf16 v[56:59], v[52:55], v[80:87], v1\n"
    "v_smfmac_f32_16x16x64_bf16 v[60:63], v[52:55], v[216:223], v1\n"
    "v_smfmac_f32_16x16x64_bf16 v[64:67], v[52:55], v[224:231], v1\n"
    "v_smfmac_f32_16x16x64_bf16 v[68:71], v[52:55], v[232:239], v1\n"
    "v_smfmac_f32_16x16x64_bf16 v[72:75], v[52:55], v[240:247], v1\n"
    "v_smfmac_f32_16x16x64_bf16 v[76:79], v[52:55], v[248:255], v1\n"
    "v_mov_b32 v11, v11\n"
    "v_mov_b32 v11, v11\n"
    "v_mov_b32 v11, v11\n"
    "v_mov_b32 v11, v11\n"
    "v_permlane32_swap_b32 v56, v60\n"
    "v_permlane32_swap_b32 v57, v61\n"
    "v_permlane32_swap_b32 v58, v62\n"
    "v_permlane32_swap_b32 v59, v63\n"
    "v_pk_add_f32 v[56:57], v[56:57], v[60:61]\n"
    "v_pk_add_f32 v[58:59], v[58:59], v[62:63]\n"
    "v_cvt_pk_bf16_f32 v60, v56, v57\n"
    "v_cvt_pk_bf16_f32 v61, v58, v59\n"
    "ds_write_b64 v9, v[60:61] offset:0\n"
    "v_permlane32_swap_b32 v64, v68\n"
    "v_permlane32_swap_b32 v65, v69\n"
    "v_permlane32_swap_b32 v66, v70\n"
    "v_permlane32_swap_b32 v67, v71\n"
    "v_pk_add_f32 v[64:65], v[64:65], v[68:69]\n"
    "v_pk_add_f32 v[66:67], v[66:67], v[70:71]\n"
    "v_cvt_pk_bf16_f32 v68, v64, v65\n"
    "v_cvt_pk_bf16_f32 v69, v66, v67\n"
    "ds_write_b64 v9, v[68:69] offset:512\n"
    "v_permlane32_swap_b32 v72, v76\n"
    "v_permlane32_swap_b32 v73, v77\n"
    "v_permlane32_swap_b32 v74, v78\n"
    "v_permlane32_swap_b32 v75, v79\n"
    "v_pk_add_f32 v[72:73], v[72:73], v[76:77]\n"
    "v_pk_add_f32 v[74:75], v[74:75], v[78:79]\n"
    "v_cvt_pk_bf16_f32 v76, v72, v73\n"
    "v_cvt_pk_bf16_f32 v77, v74, v75\n"
    "ds_write_b64 v9, v[76:77] offset:1024\n"
    "s_waitcnt lgkmcnt(0)\n"
    "s_barrier\n"
    "ds_read_b128 v[104:107], v0 offset:0\n"
    "ds_read_b128 v[108:111], v0 offset:1024\n"
    "ds_read_b128 v[112:115], v0 offset:2048\n"
    "ds_read_b128 v[116:119], v0 offset:3072\n"
    "s_and_b32 s62, s47, 63\n"
    "s_mul_i32 s62, s62, 80\n"
    "s_add_u32 s62, s62, s44\n"
    "s_lshl_b32 s62, s62, 12\n"
    "s_add_u32 s62, s62, 199688192\n"
    "s_cmp_eq_u32 s45, 2\n"
    "s_cselect_b32 s62, s62, 0x40000000\n"
    "s_mov_b32 s64, s63\n"
    "s_mul_i32 s63, s47, 1536\n"
    "s_add_u32 s63, s63, s65\n"
    "s_waitcnt lgkmcnt(0)\n"
    "s_add_u32 s47, s47, 1\n"
    "s_and_b32 s59, s47, 63\n"
    "s_cbranch_scc1 cw_loopb_%=\n"
    "buffer_store_dwordx4 v[104:107], v0, s[40:43], s62 offen offset:0 sc1\n"
    "buffer_store_dwordx4 v[108:111], v0, s[40:43], s62 offen offset:1024 sc1\n"
    "buffer_store_dwordx4 v[112:115], v0, s[40:43], s62 offen offset:2048 sc1\n"
    "buffer_store_dwordx4 v[116:119], v0, s[40:43], s62 offen offset:3072 sc1\n"
    "s_waitcnt vmcnt(0)\n"
    "buffer_atomic_add v133, v132, s[40:43], s64 offen\n"
    "buffer_atomic_add v133, v132, s[40:43], s63 offen\n"
    "s_mov_b32 s62, 0x40000000\n"
    "s_mov_b32 s63, 0x40000000\n"
    "s_mov_b32 s64, 0x40000000\n"
    "s_cmp_lt_u32 s47, 128\n"
    "s_cbranch_scc1 cw_loopb_%=\n"
    "s_branch cw_end_%=\n"
    "cw_abort_%=:\n"
    "v_mov_b32 v10, 1\n"
    "v_mov_b32 v11, 1024\n"
    "buffer_store_dword v10, v11, s[40:43], s58 offen sc1\n"
    "cw_end_%=:\n"
    "s_endpgm\n"
    :: CHAIN_INPUTS : CHAIN_CLOBBERS);
    __builtin_unreachable();
}
DEV void role_tok(unsigned lo, unsigned hi, int id, int w, int aux, const Tune& tn) {
    asm volatile(
    "s_mov_b32 s40, %0\n"
    "s_mov_b32 s41, %1\n"
    "s_mov_b32 s42, %2\n"
    "s_mov_b32 s43, %3\n"
    "s_mov_b32 s44, %4\n"
    "s_mov_b32 s45, %5\n"
    "s_mov_b32 s46, %6\n"
    "s_mov_b32 s66, %7\n"
    "s_mov_b32 s67, %8\n"
    "s_mov_b32 s68, %9\n"
    "s_mov_b32 s69, %10\n"
    "s_mov_b32 s70, %11\n"
    "s_mov_b32 s71, %12\n"
    "s_mov_b32 s72, %13\n"
    "s_mov_b32 s73, %14\n"
    "s_mov_b64 exec, -1\n"
    "v_mbcnt_lo_u32_b32 v15, -1, 0\n"
    "v_mbcnt_hi_u32_b32 v15, -1, v15\n"
    "v_lshlrev_b32 v0, 4, v15\n"
    "s_mov_b32 s58, 2097152\n"
    "s_mov_b32 s92, 0\n"
    "s_lshr_b32 s61, s44, 2\n"
    "s_and_b32 s47, s44, 3\n"
    "v_and_b32 v4, 0x370, v0\n"
    "v_bfe_u32 v5, v15, 3, 1\n"
    "v_lshl_add_u32 v1, v5, 15, v4\n"
    "v_lshl_add_u32 v2, v5, 12, v4\n"
    "s_cmp_eq_u32 s45, 0\n"
    "s_cselect_b32 s59, 1, 0\n"
    "v_lshlrev_b32 v3, 7, v15\n"
    "v_cmp_gt_u32 vcc, s59, v15\n"
    "v_mov_b32 v4, 2147483632\n"
    "s_nop 0\n"
    "v_cndmask_b32 v3, v4, v3, vcc\n"
    "v_mov_b32 v6, 1\n"
    "s_mul_i32 s48, s45, 20480\n"
    "v_add_u32 v10, s48, v0\n"
    "s_add_u32 s20, s45, 0\n"
    "s_min_u32 s20, s20, 9\n"
    "s_lshl_b32 s48, s20, 11\n"
    "v_add_u32 v11, s48, v0\n"
    "s_cmp_ge_u32 s20, 5\n"
    "s_cselect_b32 s48, 15, 0\n"
    "s_add_u32 s20, s20, s48\n"
    "s_mul_i32 s48, s61, 5\n"
    "s_add_u32 s20, s20, s48\n"
    "s_add_u32 s21, s45, 4\n"
    "s_min_u32 s21, s21, 9\n"
    "s_lshl_b32 s48, s21, 11\n"
    "v_add_u32 v12, s48, v0\n"
    "s_cmp_ge_u32 s21, 5\n"
    "s_cselect_b32 s48, 15, 0\n"
    "s_add_u32 s21, s21, s48\n"
    "s_mul_i32 s48, s61, 5\n"
    "s_add_u32 s21, s21, s48\n"
    "s_add_u32 s22, s45, 8\n"
    "s_min_u32 s22, s22, 9\n"
    "s_lshl_b32 s48, s22, 11\n"
    "v_add_u32 v13, s48, v0\n"
    "s_cmp_ge_u32 s22, 5\n"
    "s_cselect_b32 s48, 15, 0\n"
    "s_add_u32 s22, s22, s48\n"
    "s_mul_i32 s48, s61, 5\n"
    "s_add_u32 s22, s22, s48\n"
    "v_lshlrev_b32 v7, 7, v15\n"
    "s_lshl_b32 s48, s61, 7\n"
    "v_add_u32 v8, s48, v7\n"
    "v_cmp_eq_u32 vcc, 8, v15\n"
    "s_nop 1\n"
    "v_cndmask_b32 v7, v7, v8, vcc\n"
    "v_mov_b32 v8, 8\n"
    "v_mov_b32 v9, 20\n"
    "v_cndmask_b32 v8, v8, v9, vcc\n"
    "v_cmp_gt_u32 vcc, 9, v15\n"
    "s_nop 1\n"
    "v_cndmask_b32 v8, 0, v8, vcc\n"
    "v_mov_b32 v4, 2147483632\n"
    "v_cndmask_b32 v7, v4, v7, vcc\n"
    "v_mov_b32 v9, 32\n"
    "v_cmp_eq_u32 vcc, 0, v15\n"
    "s_nop 1\n"
    "v_cndmask_b32 v9, 0, v9, vcc\n"
    "s_mov_b32 s63, s47\n"
    "s_nop 0\n"
    "tk_first_%=:\n"
    "s_and_b32 s47, s63, 63\n"
    "s_lshl_b32 s54, s63, 7\n"
    "s_add_u32 s54, s54, 2124032\n"
    "s_cmp_lg_u32 s45, 0\n"
    "s_cbranch_scc1 tk_u0_ok_%=\n"
    "s_mov_b32 s60, 0\n"
    "tk_u0_%=:\n"
    "v_mov_b32 v4, 0\n"
    "buffer_atomic_add v4, v3, s[40:43], s54 offen sc0\n"
    "s_waitcnt vmcnt(0)\n"
    "v_cmp_gt_u32 vcc, v9, v4\n"
    "s_nop 4\n"
    "s_cbranch_vccz tk_u0_ok_%=\n"
    "s_sleep 4\n"
    "s_add_u32 s60, s60, 1\n"
    "s_cmp_gt_u32 s60, 65536\n"
    "s_cbranch_scc1 tk_abort_%=\n"
    "s_and_b32 s59, s60, 63\n"
    "s_cbranch_scc1 tk_u0_%=\n"
    "v_mov_b32 v4, 1024\n"
    "buffer_load_dword v4, v4, s[40:43], s58 offen sc1\n"
    "s_waitcnt vmcnt(0)\n"
    "v_readfirstlane_b32 s59, v4\n"
    "s_nop 0\n"
    "s_cmp_lg_u32 s59, 0\n"
    "s_nop 2\n"
    "s_cbranch_scc1 tk_end_%=\n"
    "s_branch tk_u0_%=\n"
    "tk_u0_ok_%=:\n"
    "s_barrier\n"
    "s_mov_b32 s64, 0\n"
    "s_add_u32 s50, s47, 0\n"
    "s_and_b32 s50, s50, 31\n"
    "s_mul_i32 s50, s50, 80\n"
    "s_mul_i32 s48, s61, 20\n"
    "s_add_u32 s50, s50, s48\n"
    "s_lshl_b32 s50, s50, 15\n"
    "s_lshl_b32 s48, s45, 12\n"
    "s_and_b32 s49, s48, 0x2000\n"
    "s_add_u32 s48, s48, s49\n"
    "s_add_u32 s50, s50, s48\n"
    "s_add_u32 s50, s50, 115802112\n"
    "s_or_b32 s50, s50, s64\n"
    "s_lshr_b32 s59, s50, 8\n"
    "s_and_b32 s59, s59, 0x2000\n"
    "s_add_u32 s30, s50, s59\n"
    "s_add_u32 s50, s50, 65536\n"
    "s_lshr_b32 s59, s50, 8\n"
    "s_and_b32 s59, s59, 0x2000\n"
    "s_add_u32 s31, s50, s59\n"
    "s_add_u32 s50, s50, 65536\n"
    "s_lshr_b32 s59, s50, 8\n"
    "s_and_b32 s59, s59, 0x2000\n"
    "s_add_u32 s34, s50, s59\n"
    "s_add_u32 s50, s50, 65536\n"
    "s_lshr_b32 s59, s50, 8\n"
    "s_and_b32 s59, s59, 0x2000\n"
    "s_add_u32 s35, s50, s59\n"
    "s_add_u32 s50, s50, 65536\n"
    "s_lshr_b32 s59, s50, 8\n"
    "s_and_b32 s59, s59, 0x2000\n"
    "s_add_u32 s36, s50, s59\n"
    "s_add_u32 s50, s50, 65536\n"
    "s_lshr_b32 s59, s50, 8\n"
    "s_and_b32 s59, s59, 0x2000\n"
    "s_add_u32 s37, s50, s59\n"
    "s_add_u32 s50, s50, 65536\n"
    "s_lshr_b32 s59, s50, 8\n"
    "s_and_b32 s59, s59, 0x2000\n"
    "s_add_u32 s38, s50, s59\n"
    "s_add_u32 s50, s50, 65536\n"
    "s_lshr_b32 s59, s50, 8\n"
    "s_and_b32 s59, s59, 0x2000\n"
    "s_add_u32 s39, s50, s59\n"
    "s_add_u32 s50, s50, 65536\n"
    "s_lshr_b32 s59, s50, 8\n"
    "s_and_b32 s59, s59, 0x2000\n"
    "s_add_u32 s96, s50, s59\n"
    "s_add_u32 s50, s50, 65536\n"
    "s_lshr_b32 s59, s50, 8\n"
    "s_and_b32 s59, s59, 0x2000\n"
    "s_add_u32 s97, s50, s59\n"
    "s_add_u32 s50, s50, 65536\n"
    "buffer_load_dwordx4 a[0:3], v1, s[40:43], s30 offen sc1\n"
    "buffer_load_dwordx4 a[4:7], v1, s[40:43], s31 offen sc1\n"
    "buffer_load_dwordx4 a[8:11], v1, s[40:43], s34 offen sc1\n"
    "buffer_load_dwordx4 a[12:15], v1, s[40:43], s35 offen sc1\n"
    "buffer_load_dwordx4 a[16:19], v1, s[40:43], s36 offen sc1\n"
    "buffer_load_dwordx4 a[20:23], v1, s[40:43], s37 offen sc1\n"
    "buffer_load_dwordx4 a[24:27], v1, s[40:43], s38 offen sc1\n"
    "buffer_load_dwordx4 a[28:31], v1, s[40:43], s39 offen sc1\n"
    "buffer_load_dwordx4 a[32:35], v1, s[40:43], s96 offen sc1\n"
    "buffer_load_dwordx4 a[36:39], v1, s[40:43], s97 offen sc1\n"
    "buffer_load_dwordx4 a[40:43], v1, s[40:43], s30 offen offset:128 sc1\n"
    "buffer_load_dwordx4 a[44:47], v1, s[40:43], s31 offen offset:128 sc1\n"
    "buffer_load_dwordx4 a[48:51], v1, s[40:43], s34 offen offset:128 sc1\n"
    "buffer_load_dwordx4 a[52:55], v1, s[40:43], s35 offen offset:128 sc1\n"
    "buffer_load_dwordx4 a[56:59], v1, s[40:43], s36 offen offset:128 sc1\n"
    "buffer_load_dwordx4 a[60:63], v1, s[40:43], s37 offen offset:128 sc1\n"
    "buffer_load_dwordx4 a[64:67], v1, s[40:43], s38 offen offset:128 sc1\n"
    "buffer_load_dwordx4 a[68:71], v1, s[40:43], s39 offen offset:128 sc1\n"
    "buffer_load_dwordx4 a[72:75], v1, s[40:43], s96 offen offset:128 sc1\n"
    "buffer_load_dwordx4 a[76:79], v1, s[40:43], s97 offen offset:128 sc1\n"
    "buffer_load_dwordx4 a[80:83], v1, s[40:43], s30 offen offset:1024 sc1\n"
    "buffer_load_dwordx4 a[84:87], v1, s[40:43], s31 offen offset:1024 sc1\n"
    "buffer_load_dwordx4 a[88:91], v1, s[40:43], s34 offen offset:1024 sc1\n"
    "buffer_load_dwordx4 a[92:95], v1, s[40:43], s35 offen offset:1024 sc1\n"
    "buffer_load_dwordx4 a[96:99], v1, s[40:43], s36 offen offset:1024 sc1\n"
    "buffer_load_dwordx4 a[100:103], v1, s[40:43], s37 offen offset:1024 sc1\n"
    "buffer_load_dwordx4 a[104:107], v1, s[40:43], s38 offen offset:1024 sc1\n"
    "buffer_load_dwordx4 a[108:111], v1, s[40:43], s39 offen offset:1024 sc1\n"
    "buffer_load_dwordx4 a[112:115], v1, s[40:43], s96 offen offset:1024 sc1\n"
    "buffer_load_dwordx4 a[116:119], v1, s[40:43], s97 offen offset:1024 sc1\n"
    "buffer_load_dwordx4 a[120:123], v1, s[40:43], s30 offen offset:1152 sc1\n"
    "buffer_load_dwordx4 a[124:127], v1, s[40:43], s31 offen offset:1152 sc1\n"
    "buffer_load_dwordx4 a[128:131], v1, s[40:43], s34 offen offset:1152 sc1\n"
    "buffer_load_dwordx4 a[132:135], v1, s[40:43], s35 offen offset:1152 sc1\n"
    "buffer_load_dwordx4 a[136:139], v1, s[40:43], s36 offen offset:1152 sc1\n"
    "buffer_load_dwordx4 a[140:143], v1, s[40:43], s37 offen offset:1152 sc1\n"
    "buffer_load_dwordx4 a[144:147], v1, s[40:43], s38 offen offset:1152 sc1\n"
    "buffer_load_dwordx4 a[148:151], v1, s[40:43], s39 offen offset:1152 sc1\n"
    "buffer_load_dwordx4 a[152:155], v1, s[40:43], s96 offen offset:1152 sc1\n"
    "buffer_load_dwordx4 a[156:159], v1, s[40:43], s97 offen offset:1152 sc1\n"
    "buffer_load_dwordx4 a[160:163], v1, s[40:43], s30 offen offset:2048 sc1\n"
    "buffer_load_dwordx4 a[164:167], v1, s[40:43], s31 offen offset:2048 sc1\n"
    "buffer_load_dwordx4 a[168:171], v1, s[40:43], s34 offen offset:2048 sc1\n"
    "buffer_load_dwordx4 a[172:175], v1, s[40:43], s35 offen offset:2048 sc1\n"
    "buffer_load_dwordx4 a[176:179], v1, s[40:43], s36 offen offset:2048 sc1\n"
    "buffer_load_dwordx4 a[180:183], v1, s[40:43], s37 offen offset:2048 sc1\n"
    "buffer_load_dwordx4 a[184:187], v1, s[40:43], s38 offen offset:2048 sc1\n"
    "buffer_load_dwordx4 a[188:191], v1, s[40:43], s39 offen offset:2048 sc1\n"
    "buffer_load_dwordx4 a[192:195], v1, s[40:43], s96 offen offset:2048 sc1\n"
    "buffer_load_dwordx4 a[196:199], v1, s[40:43], s97 offen offset:2048 sc1\n"
    "buffer_load_dwordx4 a[200:203], v1, s[40:43], s30 offen offset:2176 sc1\n"
    "buffer_load_dwordx4 a[204:207], v1, s[40:43], s31 offen offset:2176 sc1\n"
    "buffer_load_dwordx4 a[208:211], v1, s[40:43], s34 offen offset:2176 sc1\n"
    "buffer_load_dwordx4 a[212:215], v1, s[40:43], s35 offen offset:2176 sc1\n"
    "buffer_load_dwordx4 a[216:219], v1, s[40:43], s36 offen offset:2176 sc1\n"
    "buffer_load_dwordx4 a[220:223], v1, s[40:43], s37 offen offset:2176 sc1\n"
    "buffer_load_dwordx4 a[224:227], v1, s[40:43], s38 offen offset:2176 sc1\n"
    "buffer_load_dwordx4 a[228:231], v1, s[40:43], s39 offen offset:2176 sc1\n"
    "buffer_load_dwordx4 a[232:235], v1, s[40:43], s96 offen offset:2176 sc1\n"
    "buffer_load_dwordx4 a[236:239], v1, s[40:43], s97 offen offset:2176 sc1\n"
    "buffer_load_dwordx4 a[240:243], v1, s[40:43], s30 offen offset:3072 sc1\n"
    "buffer_load_dwordx4 a[244:247], v1, s[40:43], s31 offen offset:3072 sc1\n"
    "buffer_load_dwordx4 a[248:251], v1, s[40:43], s34 offen offset:3072 sc1\n"
    "s_waitcnt vmcnt(61)\n"
    "buffer_load_dwordx4 a[252:255], v1, s[40:43], s35 offen offset:3072 sc1\n"
    "buffer_load_dwordx4 v[192:195], v1, s[40:43], s36 offen offset:3072 sc1\n"
    "s_waitcnt vmcnt(61)\n"
    "buffer_load_dwordx4 v[196:199], v1, s[40:43], s37 offen offset:3072 sc1\n"
    "buffer_load_dwordx4 v[200:203], v1, s[40:43], s38 offen offset:3072 sc1\n"
    "s_waitcnt vmcnt(61)\n"
    "buffer_load_dwordx4 v[204:207], v1, s[40:43], s39 offen offset:3072 sc1\n"
    "buffer_load_dwordx4 v[208:211], v1, s[40:43], s96 offen offset:3072 sc1\n"
    "s_waitcnt vmcnt(61)\n"
    "buffer_load_dwordx4 v[212:215], v1, s[40:43], s97 offen offset:3072 sc1\n"
    "buffer_load_dwordx4 v[216:219], v1, s[40:43], s30 offen offset:3200 sc1\n"
    "s_waitcnt vmcnt(61)\n"
    "buffer_load_dwordx4 v[220:223], v1, s[40:43], s31 offen offset:3200 sc1\n"
    "buffer_load_dwordx4 v[224:227], v1, s[40:43], s34 offen offset:3200 sc1\n"
    "s_waitcnt vmcnt(61)\n"
    "buffer_load_dwordx4 v[228:231], v1, s[40:43], s35 offen offset:3200 sc1\n"
    "buffer_load_dwordx4 v[232:235], v1, s[40:43], s36 offen offset:3200 sc1\n"
    "s_waitcnt vmcnt(61)\n"
    "buffer_load_dwordx4 v[236:239], v1, s[40:43], s37 offen offset:3200 sc1\n"
    "buffer_load_dwordx4 v[240:243], v1, s[40:43], s38 offen offset:3200 sc1\n"
    "s_waitcnt vmcnt(61)\n"
    "buffer_load_dwordx4 v[244:247], v1, s[40:43], s39 offen offset:3200 sc1\n"
    "buffer_load_dwordx4 v[248:251], v1, s[40:43], s96 offen offset:3200 sc1\n"
    "s_waitcnt vmcnt(61)\n"
    "buffer_load_dwordx4 v[252:255], v1, s[40:43], s97 offen offset:3200 sc1\n"
    "tk_loop_%=:\n"
    "s_and_b32 s47, s63, 63\n"
    "s_cmp_lt_u32 s47, 60\n"
    "s_cselect_b32 s48, 4, 0\n"
    "s_cselect_b32 s64, 0, 0x40000000\n"
    "s_add_u32 s54, s63, s48\n"
    "s_lshl_b32 s54, s54, 7\n"
    "s_add_u32 s54, s54, 2124032\n"
    "s_cmp_lg_u32 s45, 0\n"
    "s_cbranch_scc1 tk_ux_ok_%=\n"
    "s_mov_b32 s60, 0\n"
    "tk_ux_%=:\n"
    "v_mov_b32 v4, 0\n"
    "buffer_atomic_add v4, v3, s[40:43], s54 offen sc0\n"
    "s_waitcnt vmcnt(0)\n"
    "v_cmp_gt_u32 vcc, v9, v4\n"
    "s_nop 4\n"
    "s_cbranch_vccz tk_ux_ok_%=\n"
    "s_sleep 4\n"
    "s_add_u32 s60, s60, 1\n"
    "s_cmp_gt_u32 s60, 65536\n"
    "s_cbranch_scc1 tk_abort_%=\n"
    "s_and_b32 s59, s60, 63\n"
    "s_cbranch_scc1 tk_ux_%=\n"
    "v_mov_b32 v4, 1024\n"
    "buffer_load_dword v4, v4, s[40:43], s58 offen sc1\n"
    "s_waitcnt vmcnt(0)\n"
    "v_readfirstlane_b32 s59, v4\n"
    "s_nop 0\n"
    "s_cmp_lg_u32 s59, 0\n"
    "s_nop 2\n"
    "s_cbranch_scc1 tk_end_%=\n"
    "s_branch tk_ux_%=\n"
    "tk_ux_ok_%=:\n"
    "s_barrier\n"
    "s_add_u32 s50, s47, 4\n"
    "s_and_b32 s50, s50, 31\n"
    "s_mul_i32 s50, s50, 80\n"
    "s_mul_i32 s48, s61, 20\n"
    "s_add_u32 s50, s50, s48\n"
    "s_lshl_b32 s50, s50, 15\n"
    "s_lshl_b32 s48, s45, 12\n"
    "s_and_b32 s49, s48, 0x2000\n"
    "s_add_u32 s48, s48, s49\n"
    "s_add_u32 s50, s50, s48\n"
    "s_add_u32 s50, s50, 115802112\n"
    "s_or_b32 s50, s50, s64\n"
    "s_lshr_b32 s59, s50, 8\n"
    "s_and_b32 s59, s59, 0x2000\n"
    "s_add_u32 s30, s50, s59\n"
    "s_add_u32 s50, s50, 65536\n"
    "s_lshr_b32 s59, s50, 8\n"
    "s_and_b32 s59, s59, 0x2000\n"
    "s_add_u32 s31, s50, s59\n"
    "s_add_u32 s50, s50, 65536\n"
    "s_lshr_b32 s59, s50, 8\n"
    "s_and_b32 s59, s59, 0x2000\n"
    "s_add_u32 s34, s50, s59\n"
    "s_add_u32 s50, s50, 65536\n"
    "s_lshr_b32 s59, s50, 8\n"
    "s_and_b32 s59, s59, 0x2000\n"
    "s_add_u32 s35, s50, s59\n"
    "s_add_u32 s50, s50, 65536\n"
    "s_lshr_b32 s59, s50, 8\n"
    "s_and_b32 s59, s59, 0x2000\n"
    "s_add_u32 s36, s50, s59\n"
    "s_add_u32 s50, s50, 65536\n"
    "s_lshr_b32 s59, s50, 8\n"
    "s_and_b32 s59, s59, 0x2000\n"
    "s_add_u32 s37, s50, s59\n"
    "s_add_u32 s50, s50, 65536\n"
    "s_lshr_b32 s59, s50, 8\n"
    "s_and_b32 s59, s59, 0x2000\n"
    "s_add_u32 s38, s50, s59\n"
    "s_add_u32 s50, s50, 65536\n"
    "s_lshr_b32 s59, s50, 8\n"
    "s_and_b32 s59, s59, 0x2000\n"
    "s_add_u32 s39, s50, s59\n"
    "s_add_u32 s50, s50, 65536\n"
    "s_lshr_b32 s59, s50, 8\n"
    "s_and_b32 s59, s59, 0x2000\n"
    "s_add_u32 s96, s50, s59\n"
    "s_add_u32 s50, s50, 65536\n"
    "s_lshr_b32 s59, s50, 8\n"
    "s_and_b32 s59, s59, 0x2000\n"
    "s_add_u32 s97, s50, s59\n"
    "s_add_u32 s50, s50, 65536\n"
    "s_mul_i32 s62, s47, 80\n"
    "s_mul_i32 s48, s61, 20\n"
    "s_add_u32 s62, s62, s48\n"
    "s_mul_i32 s54, s63, 1536\n"
    "s_add_u32 s54, s54, 2157568\n"
    "s_cmp_lg_u32 s45, 0\n"
    "s_cbranch_scc1 tk_pt_ok_%=\n"
    "s_mov_b32 s60, 0\n"
    "tk_pt_%=:\n"
    "v_mov_b32 v4, 0\n"
    "buffer_atomic_add v4, v7, s[40:43], s54 offen sc0\n"
    "s_waitcnt vmcnt(0)\n"
    "v_cmp_gt_u32 vcc, v8, v4\n"
    "s_nop 4\n"
    "s_cbranch_vccz tk_pt_ok_%=\n"
    "s_sleep 4\n"
    "s_add_u32 s60, s60, 1\n"
    "s_cmp_gt_u32 s60, 65536\n"
    "s_cbranch_scc1 tk_abort_%=\n"
    "s_and_b32 s59, s60, 63\n"
    "s_cbranch_scc1 tk_pt_%=\n"
    "v_mov_b32 v4, 1024\n"
    "buffer_load_dword v4, v4, s[40:43], s58 offen sc1\n"
    "s_waitcnt vmcnt(0)\n"
    "v_readfirstlane_b32 s59, v4\n"
    "s_nop 0\n"
    "s_cmp_lg_u32 s59, 0\n"
    "s_nop 2\n"
    "s_cbranch_scc1 tk_end_%=\n"
    "s_branch tk_pt_%=\n"
    "tk_pt_ok_%=:\n"
    "s_barrier\n"
    "s_lshl_b32 s50, s47, 5\n"
    "s_lshl_b32 s48, s45, 3\n"
    "s_add_u32 s50, s50, s48\n"
    "s_lshl_b32 s50, s50, 11\n"
    "s_add_u32 s50, s50, 226951168\n"
    "buffer_load_dwordx4 v[32:35], v0, s[40:43], s50 offen sc1\n"
    "buffer_load_dwordx4 v[36:39], v0, s[40:43], s50 offen offset:1024 sc1\n"
    "buffer_load_dwordx4 v[40:43], v0, s[40:43], s50 offen offset:2048 sc1\n"
    "buffer_load_dwordx4 v[44:47], v0, s[40:43], s50 offen offset:3072 sc1\n"
    "s_add_u32 s50, s50, 4096\n"
    "buffer_load_dwordx4 v[48:51], v0, s[40:43], s50 offen sc1\n"
    "buffer_load_dwordx4 v[52:55], v0, s[40:43], s50 offen offset:1024 sc1\n"
    "buffer_load_dwordx4 v[56:59], v0, s[40:43], s50 offen offset:2048 sc1\n"
    "buffer_load_dwordx4 v[60:63], v0, s[40:43], s50 offen offset:3072 sc1\n"
    "s_add_u32 s50, s50, 4096\n"
    "buffer_load_dwordx4 v[64:67], v0, s[40:43], s50 offen sc1\n"
    "buffer_load_dwordx4 v[68:71], v0, s[40:43], s50 offen offset:1024 sc1\n"
    "buffer_load_dwordx4 v[72:75], v0, s[40:43], s50 offen offset:2048 sc1\n"
    "buffer_load_dwordx4 v[76:79], v0, s[40:43], s50 offen offset:3072 sc1\n"
    "s_add_u32 s50, s50, 4096\n"
    "buffer_load_dwordx4 v[80:83], v0, s[40:43], s50 offen sc1\n"
    "buffer_load_dwordx4 v[84:87], v0, s[40:43], s50 offen offset:1024 sc1\n"
    "buffer_load_dwordx4 v[88:91], v0, s[40:43], s50 offen offset:2048 sc1\n"
    "buffer_load_dwordx4 v[92:95], v0, s[40:43], s50 offen offset:3072 sc1\n"
    "s_lshl_b32 s50, s47, 14\n"
    "s_lshl_b32 s48, s45, 11\n"
    "s_add_u32 s50, s50, s48\n"
    "s_add_u32 s50, s50, 231145472\n"
    "s_add_u32 s51, s50, 8192\n"
    "buffer_load_dwordx4 v[96:99], v0, s[40:43], s50 offen sc1\n"
    "buffer_load_dwordx4 v[100:103], v0, s[40:43], s51 offen sc1\n"
    "buffer_load_dwordx4 v[104:107], v0, s[40:43], s50 offen offset:1024 sc1\n"
    "buffer_load_dwordx4 v[108:111], v0, s[40:43], s51 offen offset:1024 sc1\n"
    "s_lshl_b32 s50, s62, 12\n"
    "s_lshl_b32 s48, s45, 10\n"
    "s_add_u32 s50, s50, s48\n"
    "s_add_u32 s50, s50, 199688192\n"
    "s_add_u32 s76, s50, 0\n"
    "s_add_u32 s77, s50, 8192\n"
    "s_add_u32 s78, s50, 16384\n"
    "s_add_u32 s79, s50, 24576\n"
    "s_add_u32 s80, s50, 32768\n"
    "s_add_u32 s81, s50, 40960\n"
    "s_add_u32 s82, s50, 49152\n"
    "s_add_u32 s83, s50, 57344\n"
    "s_add_u32 s84, s50, 65536\n"
    "s_add_u32 s85, s50, 73728\n"
    "s_mul_i32 s48, s47, 40\n"
    "s_add_u32 s48, s48, s20\n"
    "s_lshl_b32 s24, s48, 11\n"
    "s_add_u32 s27, s24, 232194048\n"
    "s_mul_i32 s48, s47, 40\n"
    "s_add_u32 s48, s48, s21\n"
    "s_lshl_b32 s25, s48, 11\n"
    "s_add_u32 s28, s25, 232194048\n"
    "s_mul_i32 s48, s47, 40\n"
    "s_add_u32 s48, s48, s22\n"
    "s_lshl_b32 s26, s48, 11\n"
    "s_add_u32 s29, s26, 232194048\n"
    "s_waitcnt vmcnt(19)\n"
    "v_mfma_f32_16x16x32_bf16 v[112:115], a[0:3], v[32:35], 0\n"
    "s_waitcnt vmcnt(18)\n"
    "v_mfma_f32_16x16x32_bf16 v[152:155], a[0:3], v[36:39], 0\n"
    "buffer_load_dwordx4 a[0:3], v2, s[40:43], s76 offen sc1\n"
    "v_mfma_f32_16x16x32_bf16 v[116:119], a[4:7], v[32:35], 0\n"
    "v_mfma_f32_16x16x32_bf16 v[156:159], a[4:7], v[36:39], 0\n"
    "buffer_load_dwordx4 a[4:7], v2, s[40:43], s77 offen sc1\n"
    "v_mfma_f32_16x16x32_bf16 v[120:123], a[8:11], v[32:35], 0\n"
    "v_mfma_f32_16x16x32_bf16 v[160:163], a[8:11], v[36:39], 0\n"
    "buffer_load_dwordx4 a[8:11], v2, s[40:43], s78 offen sc1\n"
    "v_mfma_f32_16x16x32_bf16 v[124:127], a[12:15], v[32:35], 0\n"
    "v_mfma_f32_16x16x32_bf16 v[164:167], a[12:15], v[36:39], 0\n"
    "buffer_load_dwordx4 a[12:15], v2, s[40:43], s79 offen sc1\n"
    "v_mfma_f32_16x16x32_bf16 v[128:131], a[16:19], v[32:35], 0\n"
    "v_mfma_f32_16x16x32_bf16 v[168:171], a[16:19], v[36:39], 0\n"
    "buffer_load_dwordx4 a[16:19], v2, s[40:43], s80 offen sc1\n"
    "v_mfma_f32_16x16x32_bf16 v[132:135], a[20:23], v[32:35], 0\n"
    "v_mfma_f32_16x16x32_bf16 v[172:175], a[20:23], v[36:39], 0\n"
    "buffer_load_dwordx4 a[20:23], v2, s[40:43], s81 offen sc1\n"
    "v_mfma_f32_16x16x32_bf16 v[136:139], a[24:27], v[32:35], 0\n"
    "v_mfma_f32_16x16x32_bf16 v[176:179], a[24:27], v[36:39], 0\n"
    "buffer_load_dwordx4 a[24:27], v2, s[40:43], s82 offen sc1\n"
    "v_mfma_f32_16x16x32_bf16 v[140:143], a[28:31], v[32:35], 0\n"
    "v_mfma_f32_16x16x32_bf16 v[180:183], a[28:31], v[36:39], 0\n"
    "buffer_load_dwordx4 a[28:31], v2, s[40:43], s83 offen sc1\n"
    "v_mfma_f32_16x16x32_bf16 v[144:147], a[32:35], v[32:35], 0\n"
    "v_mfma_f32_16x16x32_bf16 v[184:187], a[32:35], v[36:39], 0\n"
    "buffer_load_dwordx4 a[32:35], v2, s[40:43], s84 offen sc1\n"
    "v_mfma_f32_16x16x32_bf16 v[148:151], a[36:39], v[32:35], 0\n"
    "v_mfma_f32_16x16x32_bf16 v[188:191], a[36:39], v[36:39], 0\n"
    "buffer_load_dwordx4 a[36:39], v2, s[40:43], s85 offen sc1\n"
    "s_waitcnt vmcnt(27)\n"
    "v_mfma_f32_16x16x32_bf16 v[112:115], a[40:43], v[40:43], v[112:115]\n"
    "s_waitcnt vmcnt(26)\n"
    "v_mfma_f32_16x16x32_bf16 v[152:155], a[40:43], v[44:47], v[152:155]\n"
    "buffer_load_dwordx4 a[40:43], v2, s[40:43], s76 offen offset:128 sc1\n"
    "v_mfma_f32_16x16x32_bf16 v[116:119], a[44:47], v[40:43], v[116:119]\n"
    "v_mfma_f32_16x16x32_bf16 v[156:159], a[44:47], v[44:47], v[156:159]\n"
    "buffer_load_dwordx4 a[44:47], v2, s[40:43], s77 offen offset:128 sc1\n"
    "v_mfma_f32_16x16x32_bf16 v[120:123], a[48:51], v[40:43], v[120:123]\n"
    "v_mfma_f32_16x16x32_bf16 v[160:163], a[48:51], v[44:47], v[160:163]\n"
    "buffer_load_dwordx4 a[48:51], v2, s[40:43], s78 offen offset:128 sc1\n"
    "v_mfma_f32_16x16x32_bf16 v[124:127], a[52:55], v[40:43], v[124:127]\n"
    "v_mfma_f32_16x16x32_bf16 v[164:167], a[52:55], v[44:47], v[164:167]\n"
    "buffer_load_dwordx4 a[52:55], v2, s[40:43], s79 offen offset:128 sc1\n"
    "v_mfma_f32_16x16x32_bf16 v[128:131], a[56:59], v[40:43], v[128:131]\n"
    "v_mfma_f32_16x16x32_bf16 v[168:171], a[56:59], v[44:47], v[168:171]\n"
    "buffer_load_dwordx4 a[56:59], v2, s[40:43], s80 offen offset:128 sc1\n"
    "v_mfma_f32_16x16x32_bf16 v[132:135], a[60:63], v[40:43], v[132:135]\n"
    "v_mfma_f32_16x16x32_bf16 v[172:175], a[60:63], v[44:47], v[172:175]\n"
    "buffer_load_dwordx4 a[60:63], v2, s[40:43], s81 offen offset:128 sc1\n"
    "v_mfma_f32_16x16x32_bf16 v[136:139], a[64:67], v[40:43], v[136:139]\n"
    "v_mfma_f32_16x16x32_bf16 v[176:179], a[64:67], v[44:47], v[176:179]\n"
    "buffer_load_dwordx4 a[64:67], v2, s[40:43], s82 offen offset:128 sc1\n"
    "v_mfma_f32_16x16x32_bf16 v[140:143], a[68:71], v[40:43], v[140:143]\n"
    "v_mfma_f32_16x16x32_bf16 v[180:183], a[68:71], v[44:47], v[180:183]\n"
    "buffer_load_dwordx4 a[68:71], v2, s[40:43], s83 offen offset:128 sc1\n"
    "v_mfma_f32_16x16x32_bf16 v[144:147], a[72:75], v[40:43], v[144:147]\n"
    "v_mfma_f32_16x16x32_bf16 v[184:187], a[72:75], v[44:47], v[184:187]\n"
    "buffer_load_dwordx4 a[72:75], v2, s[40:43], s84 offen offset:128 sc1\n"
    "v_mfma_f32_16x16x32_bf16 v[148:151], a[76:79], v[40:43], v[148:151]\n"
    "v_mfma_f32_16x16x32_bf16 v[188:191], a[76:79], v[44:47], v[188:191]\n"
    "buffer_load_dwordx4 a[76:79], v2, s[40:43], s85 offen offset:128 sc1\n"
    "s_waitcnt vmcnt(35)\n"
    "v_mfma_f32_16x16x32_bf16 v[112:115], a[80:83], v[48:51], v[112:115]\n"
    "s_waitcnt vmcnt(34)\n"
    "v_mfma_f32_16x16x32_bf16 v[152:155], a[80:83], v[52:55], v[152:155]\n"
    "v_mfma_f32_16x16x32_bf16 v[116:119], a[84:87], v[48:51], v[116:119]\n"
    "v_mfma_f32_16x16x32_bf16 v[156:159], a[84:87], v[52:55], v[156:159]\n"
    "v_mfma_f32_16x16x32_bf16 v[120:123], a[88:91], v[48:51], v[120:123]\n"
    "v_mfma_f32_16x16x32_bf16 v[160:163], a[88:91], v[52:55], v[160:163]\n"
    "v_mfma_f32_16x16x32_bf16 v[124:127], a[92:95], v[48:51], v[124:127]\n"
    "v_mfma_f32_16x16x32_bf16 v[164:167], a[92:95], v[52:55], v[164:167]\n"
    "v_mfma_f32_16x16x32_bf16 v[128:131], a[96:99], v[48:51], v[128:131]\n"
    "v_mfma_f32_16x16x32_bf16 v[168:171], a[96:99], v[52:55], v[168:171]\n"
    "v_mfma_f32_16x16x32_bf16 v[132:135], a[100:103], v[48:51], v[132:135]\n"
    "v_mfma_f32_16x16x32_bf16 v[172:175], a[100:103], v[52:55], v[172:175]\n"
    "v_mfma_f32_16x16x32_bf16 v[136:139], a[104:107], v[48:51], v[136:139]\n"
    "v_mfma_f32_16x16x32_bf16 v[176:179], a[104:107], v[52:55], v[176:179]\n"
    "v_mfma_f32_16x16x32_bf16 v[140:143], a[108:111], v[48:51], v[140:143]\n"
    "v_mfma_f32_16x16x32_bf16 v[180:183], a[108:111], v[52:55], v[180:183]\n"
    "v_mfma_f32_16x16x32_bf16 v[144:147], a[112:115], v[48:51], v[144:147]\n"
    "v_mfma_f32_16x16x32_bf16 v[184:187], a[112:115], v[52:55], v[184:187]\n"
    "v_mfma_f32_16x16x32_bf16 v[148:151], a[116:119], v[48:51], v[148:151]\n"
    "v_mfma_f32_16x16x32_bf16 v[188:191], a[116:119], v[52:55], v[188:191]\n"
    "s_waitcnt vmcnt(33)\n"
    "v_mfma_f32_16x16x32_bf16 v[112:115], a[120:123], v[56:59], v[112:115]\n"
    "s_waitcnt vmcnt(32)\n"
    "v_mfma_f32_16x16x32_bf16 v[152:155], a[120:123], v[60:63], v[152:155]\n"
    "buffer_load_dwordx4 v[32:35], v0, s[40:43], s27 offen sc1\n"
    "v_mfma_f32_16x16x32_bf16 v[116:119], a[124:127], v[56:59], v[116:119]\n"
    "v_mfma_f32_16x16x32_bf16 v[156:159], a[124:127], v[60:63], v[156:159]\n"
    "buffer_load_dwordx4 v[36:39], v0, s[40:43], s27 offen offset:1024 sc1\n"
    "v_mfma_f32_16x16x32_bf16 v[120:123], a[128:131], v[56:59], v[120:123]\n"
    "v_mfma_f32_16x16x32_bf16 v[160:163], a[128:131], v[60:63], v[160:163]\n"
    "buffer_load_dwordx4 v[40:43], v0, s[40:43], s28 offen sc1\n"
    "v_mfma_f32_16x16x32_bf16 v[124:127], a[132:135], v[56:59], v[124:127]\n"
    "v_mfma_f32_16x16x32_bf16 v[164:167], a[132:135], v[60:63], v[164:167]\n"
    "buffer_load_dwordx4 v[44:47], v0, s[40:43], s28 offen offset:1024 sc1\n"
    "v_mfma_f32_16x16x32_bf16 v[128:131], a[136:139], v[56:59], v[128:131]\n"
    "v_mfma_f32_16x16x32_bf16 v[168:171], a[136:139], v[60:63], v[168:171]\n"
    "buffer_load_dwordx4 v[48:51], v0, s[40:43], s29 offen sc1\n"
    "v_mfma_f32_16x16x32_bf16 v[132:135], a[140:143], v[56:59], v[132:135]\n"
    "v_mfma_f32_16x16x32_bf16 v[172:175], a[140:143], v[60:63], v[172:175]\n"
    "buffer_load_dwordx4 v[52:55], v0, s[40:43], s29 offen offset:1024 sc1\n"
    "v_mfma_f32_16x16x32_bf16 v[136:139], a[144:147], v[56:59], v[136:139]\n"
    "v_mfma_f32_16x16x32_bf16 v[176:179], a[144:147], v[60:63], v[176:179]\n"
    "v_mfma_f32_16x16x32_bf16 v[140:143], a[148:151], v[56:59], v[140:143]\n"
    "v_mfma_f32_16x16x32_bf16 v[180:183], a[148:151], v[60:63], v[180:183]\n"
    "v_mfma_f32_16x16x32_bf16 v[144:147], a[152:155], v[56:59], v[144:147]\n"
    "v_mfma_f32_16x16x32_bf16 v[184:187], a[152:155], v[60:63], v[184:187]\n"
    "v_mfma_f32_16x16x32_bf16 v[148:151], a[156:159], v[56:59], v[148:151]\n"
    "v_mfma_f32_16x16x32_bf16 v[188:191], a[156:159], v[60:63], v[188:191]\n"
    "s_waitcnt vmcnt(37)\n"
    "v_mfma_f32_16x16x32_bf16 v[112:115], a[160:163], v[64:67], v[112:115]\n"
    "s_waitcnt vmcnt(36)\n"
    "v_mfma_f32_16x16x32_bf16 v[152:155], a[160:163], v[68:71], v[152:155]\n"
    "buffer_load_dwordx4 a[80:83], v1, s[40:43], s30 offen offset:1024 sc1\n"
    "v_mfma_f32_16x16x32_bf16 v[116:119], a[164:167], v[64:67], v[116:119]\n"
    "v_mfma_f32_16x16x32_bf16 v[156:159], a[164:167], v[68:71], v[156:159]\n"
    "buffer_load_dwordx4 a[84:87], v1, s[40:43], s31 offen offset:1024 sc1\n"
    "v_mfma_f32_16x16x32_bf16 v[120:123], a[168:171], v[64:67], v[120:123]\n"
    "v_mfma_f32_16x16x32_bf16 v[160:163], a[168:171], v[68:71], v[160:163]\n"
    "buffer_load_dwordx4 a[88:91], v1, s[40:43], s34 offen offset:1024 sc1\n"
    "v_mfma_f32_16x16x32_bf16 v[124:127], a[172:175], v[64:67], v[124:127]\n"
    "v_mfma_f32_16x16x32_bf16 v[164:167], a[172:175], v[68:71], v[164:167]\n"
    "buffer_load_dwordx4 a[92:95], v1, s[40:43], s35 offen offset:1024 sc1\n"
    "v_mfma_f32_16x16x32_bf16 v[128:131], a[176:179], v[64:67], v[128:131]\n"
    "v_mfma_f32_16x16x32_bf16 v[168:171], a[176:179], v[68:71], v[168:171]\n"
    "buffer_load_dwordx4 a[96:99], v1, s[40:43], s36 offen offset:1024 sc1\n"
    "v_mfma_f32_16x16x32_bf16 v[132:135], a[180:183], v[64:67], v[132:135]\n"
    "v_mfma_f32_16x16x32_bf16 v[172:175], a[180:183], v[68:71], v[172:175]\n"
    "buffer_load_dwordx4 a[100:103], v1, s[40:43], s37 offen offset:1024 sc1\n"
    "v_mfma_f32_16x16x32_bf16 v[136:139], a[184:187], v[64:67], v[136:139]\n"
    "v_mfma_f32_16x16x32_bf16 v[176:179], a[184:187], v[68:71], v[176:179]\n"
    "buffer_load_dwordx4 a[104:107], v1, s[40:43], s38 offen offset:1024 sc1\n"
    "v_mfma_f32_16x16x32_bf16 v[140:143], a[188:191], v[64:67], v[140:143]\n"
    "v_mfma_f32_16x16x32_bf16 v[180:183], a[188:191], v[68:71], v[180:183]\n"
    "buffer_load_dwordx4 a[108:111], v1, s[40:43], s39 offen offset:1024 sc1\n"
    "v_mfma_f32_16x16x32_bf16 v[144:147], a[192:195], v[64:67], v[144:147]\n"
    "v_mfma_f32_16x16x32_bf16 v[184:187], a[192:195], v[68:71], v[184:187]\n"
    "buffer_load_dwordx4 a[112:115], v1, s[40:43], s96 offen offset:1024 sc1\n"
    "v_mfma_f32_16x16x32_bf16 v[148:151], a[196:199], v[64:67], v[148:151]\n"
    "v_mfma_f32_16x16x32_bf16 v[188:191], a[196:199], v[68:71], v[188:191]\n"
    "buffer_load_dwordx4 a[116:119], v1, s[40:43], s97 offen offset:1024 sc1\n"
    "s_waitcnt vmcnt(45)\n"
    "v_mfma_f32_16x16x32_bf16 v[112:115], a[200:203], v[72:75], v[112:115]\n"
    "s_waitcnt vmcnt(44)\n"
    "v_mfma_f32_16x16x32_bf16 v[152:155], a[200:203], v[76:79], v[152:155]\n"
    "buffer_load_dwordx4 a[120:123], v1, s[40:43], s30 offen offset:1152 sc1\n"
    "v_mfma_f32_16x16x32_bf16 v[116:119], a[204:207], v[72:75], v[116:119]\n"
    "v_mfma_f32_16x16x32_bf16 v[156:159], a[204:207], v[76:79], v[156:159]\n"
    "buffer_load_dwordx4 a[124:127], v1, s[40:43], s31 offen offset:1152 sc1\n"
    "v_mfma_f32_16x16x32_bf16 v[120:123], a[208:211], v[72:75], v[120:123]\n"
    "v_mfma_f32_16x16x32_bf16 v[160:163], a[208:211], v[76:79], v[160:163]\n"
    "buffer_load_dwordx4 a[128:131], v1, s[40:43], s34 offen offset:1152 sc1\n"
    "v_mfma_f32_16x16x32_bf16 v[124:127], a[212:215], v[72:75], v[124:127]\n"
    "v_mfma_f32_16x16x32_bf16 v[164:167], a[212:215], v[76:79], v[164:167]\n"
    "buffer_load_dwordx4 a[132:135], v1, s[40:43], s35 offen offset:1152 sc1\n"
    "v_mfma_f32_16x16x32_bf16 v[128:131], a[216:219], v[72:75], v[128:131]\n"
    "v_mfma_f32_16x16x32_bf16 v[168:171], a[216:219], v[76:79], v[168:171]\n"
    "buffer_load_dwordx4 a[136:139], v1, s[40:43], s36 offen offset:1152 sc1\n"
    "v_mfma_f32_16x16x32_bf16 v[132:135], a[220:223], v[72:75], v[132:135]\n"
    "v_mfma_f32_16x16x32_bf16 v[172:175], a[220:223], v[76:79], v[172:175]\n"
    "buffer_load_dwordx4 a[140:143], v1, s[40:43], s37 offen offset:1152 sc1\n"
    "v_mfma_f32_16x16x32_bf16 v[136:139], a[224:227], v[72:75], v[136:139]\n"
    "v_mfma_f32_16x16x32_bf16 v[176:179], a[224:227], v[76:79], v[176:179]\n"
    "buffer_load_dwordx4 a[144:147], v1, s[40:43], s38 offen offset:1152 sc1\n"
    "v_mfma_f32_16x16x32_bf16 v[140:143], a[228:231], v[72:75], v[140:143]\n"
    "v_mfma_f32_16x16x32_bf16 v[180:183], a[228:231], v[76:79], v[180:183]\n"
    "buffer_load_dwordx4 a[148:151], v1, s[40:43], s39 offen offset:1152 sc1\n"
    "v_mfma_f32_16x16x32_bf16 v[144:147], a[232:235], v[72:75], v[144:147]\n"
    "v_mfma_f32_16x16x32_bf16 v[184:187], a[232:235], v[76:79], v[184:187]\n"
    "buffer_load_dwordx4 a[152:155], v1, s[40:43], s96 offen offset:1152 sc1\n"
    "v_mfma_f32_16x16x32_bf16 v[148:151], a[236:239], v[72:75], v[148:151]\n"
    "v_mfma_f32_16x16x32_bf16 v[188:191], a[236:239], v[76:79], v[188:191]\n"
    "buffer_load_dwordx4 a[156:159], v1, s[40:43], s97 offen offset:1152 sc1\n"
    "s_waitcnt vmcnt(53)\n"
    "v_mfma_f32_16x16x32_bf16 v[112:115], a[240:243], v[80:83], v[112:115]\n"
    "s_waitcnt vmcnt(52)\n"
    "v_mfma_f32_16x16x32_bf16 v[152:155], a[240:243], v[84:87], v[152:155]\n"
    "buffer_load_dwordx4 a[160:163], v1, s[40:43], s30 offen offset:2048 sc1\n"
    "v_mfma_f32_16x16x32_bf16 v[116:119], a[244:247], v[80:83], v[116:119]\n"
    "v_mfma_f32_16x16x32_bf16 v[156:159], a[244:247], v[84:87], v[156:159]\n"
    "buffer_load_dwordx4 a[164:167], v1, s[40:43], s31 offen offset:2048 sc1\n"
    "v_mfma_f32_16x16x32_bf16 v[120:123], a[248:251], v[80:83], v[120:123]\n"
    "v_mfma_f32_16x16x32_bf16 v[160:163], a[248:251], v[84:87], v[160:163]\n"
    "buffer_load_dwordx4 a[168:171], v1, s[40:43], s34 offen offset:2048 sc1\n"
    "v_mfma_f32_16x16x32_bf16 v[124:127], a[252:255], v[80:83], v[124:127]\n"
    "v_mfma_f32_16x16x32_bf16 v[164:167], a[252:255], v[84:87], v[164:167]\n"
    "buffer_load_dwordx4 a[172:175], v1, s[40:43], s35 offen offset:2048 sc1\n"
    "v_mfma_f32_16x16x32_bf16 v[128:131], v[192:195], v[80:83], v[128:131]\n"
    "v_mfma_f32_16x16x32_bf16 v[168:171], v[192:195], v[84:87], v[168:171]\n"
    "buffer_load_dwordx4 a[176:179], v1, s[40:43], s36 offen offset:2048 sc1\n"
    "v_mfma_f32_16x16x32_bf16 v[132:135], v[196:199], v[80:83], v[132:135]\n"
    "v_mfma_f32_16x16x32_bf16 v[172:175], v[196:199], v[84:87], v[172:175]\n"
    "buffer_load_dwordx4 a[180:183], v1, s[40:43], s37 offen offset:2048 sc1\n"
    "v_mfma_f32_16x16x32_bf16 v[136:139], v[200:203], v[80:83], v[136:139]\n"
    "v_mfma_f32_16x16x32_bf16 v[176:179], v[200:203], v[84:87], v[176:179]\n"
    "buffer_load_dwordx4 a[184:187], v1, s[40:43], s38 offen offset:2048 sc1\n"
    "v_mfma_f32_16x16x32_bf16 v[140:143], v[204:207], v[80:83], v[140:143]\n"
    "v_mfma_f32_16x16x32_bf16 v[180:183], v[204:207], v[84:87], v[180:183]\n"
    "buffer_load_dwordx4 a[188:191], v1, s[40:43], s39 offen offset:2048 sc1\n"
    "v_mfma_f32_16x16x32_bf16 v[144:147], v[208:211], v[80:83], v[144:147]\n"
    "v_mfma_f32_16x16x32_bf16 v[184:187], v[208:211], v[84:87], v[184:187]\n"
    "buffer_load_dwordx4 a[192:195], v1, s[40:43], s96 offen offset:2048 sc1\n"
    "v_mfma_f32_16x16x32_bf16 v[148:151], v[212:215], v[80:83], v[148:151]\n"
    "v_mfma_f32_16x16x32_bf16 v[188:191], v[212:215], v[84:87], v[188:191]\n"
    "buffer_load_dwordx4 a[196:199], v1, s[40:43], s97 offen offset:2048 sc1\n"
    "s_waitcnt vmcnt(61)\n"
    "v_mfma_f32_16x16x32_bf16 v[112:115], v[216:219], v[88:91], v[112:115]\n"
    "s_waitcnt vmcnt(60)\n"
    "v_mfma_f32_16x16x32_bf16 v[152:155], v[216:219], v[92:95], v[152:155]\n"
    "buffer_load_dwordx4 a[200:203], v1, s[40:43], s30 offen offset:2176 sc1\n"
    "v_mfma_f32_16x16x32_bf16 v[116:119], v[220:223], v[88:91], v[116:119]\n"
    "v_mfma_f32_16x16x32_bf16 v[156:159], v[220:223], v[92:95], v[156:159]\n"
    "buffer_load_dwordx4 a[204:207], v1, s[40:43], s31 offen offset:2176 sc1\n"
    "v_mfma_f32_16x16x32_bf16 v[120:123], v[224:227], v[88:91], v[120:123]\n"
    "v_mfma_f32_16x16x32_bf16 v[160:163], v[224:227], v[92:95], v[160:163]\n"
    "buffer_load_dwordx4 a[208:211], v1, s[40:43], s34 offen offset:2176 sc1\n"
    "s_waitcnt vmcnt(61)\n"
    "v_mfma_f32_16x16x32_bf16 v[124:127], v[228:231], v[88:91], v[124:127]\n"
    "v_mfma_f32_16x16x32_bf16 v[164:167], v[228:231], v[92:95], v[164:167]\n"
    "buffer_load_dwordx4 a[212:215], v1, s[40:43], s35 offen offset:2176 sc1\n"
    "v_mfma_f32_16x16x32_bf16 v[128:131], v[232:235], v[88:91], v[128:131]\n"
    "v_mfma_f32_16x16x32_bf16 v[168:171], v[232:235], v[92:95], v[168:171]\n"
    "buffer_load_dwordx4 a[216:219], v1, s[40:43], s36 offen offset:2176 sc1\n"
    "s_waitcnt vmcnt(61)\n"
    "v_mfma_f32_16x16x32_bf16 v[132:135], v[236:239], v[88:91], v[132:135]\n"
    "v_mfma_f32_16x16x32_bf16 v[172:175], v[236:239], v[92:95], v[172:175]\n"
    "buffer_load_dwordx4 a[220:223], v1, s[40:43], s37 offen offset:2176 sc1\n"
    "v_mfma_f32_16x16x32_bf16 v[136:139], v[240:243], v[88:91], v[136:139]\n"
    "v_mfma_f32_16x16x32_bf16 v[176:179], v[240:243], v[92:95], v[176:179]\n"
    "buffer_load_dwordx4 a[224:227], v1, s[40:43], s38 offen offset:2176 sc1\n"
    "s_waitcnt vmcnt(61)\n"
    "v_mfma_f32_16x16x32_bf16 v[140:143], v[244:247], v[88:91], v[140:143]\n"
    "v_mfma_f32_16x16x32_bf16 v[180:183], v[244:247], v[92:95], v[180:183]\n"
    "buffer_load_dwordx4 a[228:231], v1, s[40:43], s39 offen offset:2176 sc1\n"
    "v_mfma_f32_16x16x32_bf16 v[144:147], v[248:251], v[88:91], v[144:147]\n"
    "v_mfma_f32_16x16x32_bf16 v[184:187], v[248:251], v[92:95], v[184:187]\n"
    "buffer_load_dwordx4 a[232:235], v1, s[40:43], s96 offen offset:2176 sc1\n"
    "s_waitcnt vmcnt(61)\n"
    "v_mfma_f32_16x16x32_bf16 v[148:151], v[252:255], v[88:91], v[148:151]\n"
    "v_mfma_f32_16x16x32_bf16 v[188:191], v[252:255], v[92:95], v[188:191]\n"
    "buffer_load_dwordx4 a[236:239], v1, s[40:43], s97 offen offset:2176 sc1\n"
    "v_mfma_f32_16x16x32_bf16 v[112:115], a[0:3], v[96:99], v[112:115]\n"
    "v_mfma_f32_16x16x32_bf16 v[152:155], a[0:3], v[100:103], v[152:155]\n"
    "v_mfma_f32_16x16x32_bf16 v[116:119], a[4:7], v[96:99], v[116:119]\n"
    "v_mfma_f32_16x16x32_bf16 v[156:159], a[4:7], v[100:103], v[156:159]\n"
    "v_mfma_f32_16x16x32_bf16 v[120:123], a[8:11], v[96:99], v[120:123]\n"
    "v_mfma_f32_16x16x32_bf16 v[160:163], a[8:11], v[100:103], v[160:163]\n"
    "v_mfma_f32_16x16x32_bf16 v[124:127], a[12:15], v[96:99], v[124:127]\n"
    "v_mfma_f32_16x16x32_bf16 v[164:167], a[12:15], v[100:103], v[164:167]\n"
    "s_waitcnt vmcnt(61)\n"
    "v_mfma_f32_16x16x32_bf16 v[128:131], a[16:19], v[96:99], v[128:131]\n"
    "v_mfma_f32_16x16x32_bf16 v[168:171], a[16:19], v[100:103], v[168:171]\n"
    "s_waitcnt vmcnt(60)\n"
    "v_mfma_f32_16x16x32_bf16 v[132:135], a[20:23], v[96:99], v[132:135]\n"
    "v_mfma_f32_16x16x32_bf16 v[172:175], a[20:23], v[100:103], v[172:175]\n"
    "s_waitcnt vmcnt(59)\n"
    "v_mfma_f32_16x16x32_bf16 v[136:139], a[24:27], v[96:99], v[136:139]\n"
    "v_mfma_f32_16x16x32_bf16 v[176:179], a[24:27], v[100:103], v[176:179]\n"
    "s_waitcnt vmcnt(58)\n"
    "v_mfma_f32_16x16x32_bf16 v[140:143], a[28:31], v[96:99], v[140:143]\n"
    "v_mfma_f32_16x16x32_bf16 v[180:183], a[28:31], v[100:103], v[180:183]\n"
    "s_waitcnt vmcnt(57)\n"
    "v_mfma_f32_16x16x32_bf16 v[144:147], a[32:35], v[96:99], v[144:147]\n"
    "v_mfma_f32_16x16x32_bf16 v[184:187], a[32:35], v[100:103], v[184:187]\n"
    "s_waitcnt vmcnt(56)\n"
    "v_mfma_f32_16x16x32_bf16 v[148:151], a[36:39], v[96:99], v[148:151]\n"
    "v_mfma_f32_16x16x32_bf16 v[188:191], a[36:39], v[100:103], v[188:191]\n"
    "s_waitcnt vmcnt(55)\n"
    "v_mfma_f32_16x16x32_bf16 v[112:115], a[40:43], v[104:107], v[112:115]\n"
    "v_mfma_f32_16x16x32_bf16 v[152:155], a[40:43], v[108:111], v[152:155]\n"
    "s_waitcnt vmcnt(54)\n"
    "v_mfma_f32_16x16x32_bf16 v[116:119], a[44:47], v[104:107], v[116:119]\n"
    "v_mfma_f32_16x16x32_bf16 v[156:159], a[44:47], v[108:111], v[156:159]\n"
    "s_waitcnt vmcnt(53)\n"
    "v_mfma_f32_16x16x32_bf16 v[120:123], a[48:51], v[104:107], v[120:123]\n"
    "v_mfma_f32_16x16x32_bf16 v[160:163], a[48:51], v[108:111], v[160:163]\n"
    "s_waitcnt vmcnt(52)\n"
    "v_mfma_f32_16x16x32_bf16 v[124:127], a[52:55], v[104:107], v[124:127]\n"
    "v_mfma_f32_16x16x32_bf16 v[164:167], a[52:55], v[108:111], v[164:167]\n"
    "s_waitcnt vmcnt(51)\n"
    "v_mfma_f32_16x16x32_bf16 v[128:131], a[56:59], v[104:107], v[128:131]\n"
    "v_mfma_f32_16x16x32_bf16 v[168:171], a[56:59], v[108:111], v[168:171]\n"
    "s_waitcnt vmcnt(50)\n"
    "v_mfma_f32_16x16x32_bf16 v[132:135], a[60:63], v[104:107], v[132:135]\n"
    "v_mfma_f32_16x16x32_bf16 v[172:175], a[60:63], v[108:111], v[172:175]\n"
    "s_waitcnt vmcnt(49)\n"
    "v_mfma_f32_16x16x32_bf16 v[136:139], a[64:67], v[104:107], v[136:139]\n"
    "v_mfma_f32_16x16x32_bf16 v[176:179], a[64:67], v[108:111], v[176:179]\n"
    "s_waitcnt vmcnt(48)\n"
    "v_mfma_f32_16x16x32_bf16 v[140:143], a[68:71], v[104:107], v[140:143]\n"
    "v_mfma_f32_16x16x32_bf16 v[180:183], a[68:71], v[108:111], v[180:183]\n"
    "s_waitcnt vmcnt(47)\n"
    "v_mfma_f32_16x16x32_bf16 v[144:147], a[72:75], v[104:107], v[144:147]\n"
    "v_mfma_f32_16x16x32_bf16 v[184:187], a[72:75], v[108:111], v[184:187]\n"
    "s_waitcnt vmcnt(46)\n"
    "v_mfma_f32_16x16x32_bf16 v[148:151], a[76:79], v[104:107], v[148:151]\n"
    "v_mfma_f32_16x16x32_bf16 v[188:191], a[76:79], v[108:111], v[188:191]\n"
    "ds_write_b128 v10, v[112:115] offset:0\n"
    "ds_write_b128 v10, v[116:119] offset:1024\n"
    "ds_write_b128 v10, v[120:123] offset:2048\n"
    "ds_write_b128 v10, v[124:127] offset:3072\n"
    "ds_write_b128 v10, v[128:131] offset:4096\n"
    "ds_write_b128 v10, v[132:135] offset:5120\n"
    "ds_write_b128 v10, v[136:139] offset:6144\n"
    "ds_write_b128 v10, v[140:143] offset:7168\n"
    "ds_write_b128 v10, v[144:147] offset:8192\n"
    "ds_write_b128 v10, v[148:151] offset:9216\n"
    "ds_write_b128 v10, v[152:155] offset:10240\n"
    "ds_write_b128 v10, v[156:159] offset:11264\n"
    "ds_write_b128 v10, v[160:163] offset:12288\n"
    "ds_write_b128 v10, v[164:167] offset:13312\n"
    "ds_write_b128 v10, v[168:171] offset:14336\n"
    "ds_write_b128 v10, v[172:175] offset:15360\n"
    "s_waitcnt lgkmcnt(14)\n"
    "ds_write_b128 v10, v[176:179] offset:16384\n"
    "ds_write_b128 v10, v[180:183] offset:17408\n"
    "s_waitcnt lgkmcnt(14)\n"
    "ds_write_b128 v10, v[184:187] offset:18432\n"
    "ds_write_b128 v10, v[188:191] offset:19456\n"
    "s_waitcnt lgkmcnt(14)\n"
    "s_waitcnt lgkmcnt(0)\n"
    "s_barrier\n"
    "ds_read_b128 v[112:115], v11 offset:0\n"
    "ds_read_b128 v[116:119], v11 offset:20480\n"
    "ds_read_b128 v[120:123], v11 offset:40960\n"
    "ds_read_b128 v[124:127], v11 offset:61440\n"
    "ds_read_b128 v[128:131], v11 offset:1024\n"
    "ds_read_b128 v[132:135], v11 offset:21504\n"
    "ds_read_b128 v[136:139], v11 offset:41984\n"
    "ds_read_b128 v[140:143], v11 offset:62464\n"
    "ds_read_b128 v[144:147], v12 offset:0\n"
    "ds_read_b128 v[148:151], v12 offset:20480\n"
    "ds_read_b128 v[152:155], v12 offset:40960\n"
    "ds_read_b128 v[156:159], v12 offset:61440\n"
    "ds_read_b128 v[160:163], v12 offset:1024\n"
    "ds_read_b128 v[164:167], v12 offset:21504\n"
    "ds_read_b128 v[168:171], v12 offset:41984\n"
    "ds_read_b128 v[172:175], v12 offset:62464\n"
    "s_waitcnt lgkmcnt(14)\n"
    "ds_read_b128 v[176:179], v13 offset:0\n"
    "ds_read_b128 v[180:183], v13 offset:20480\n"
    "s_waitcnt lgkmcnt(14)\n"
    "ds_read_b128 v[184:187], v13 offset:40960\n"
    "ds_read_b128 v[188:191], v13 offset:61440\n"
    "s_waitcnt lgkmcnt(14)\n"
    "ds_read_b128 v[192:195], v13 offset:1024\n"
    "ds_read_b128 v[196:199], v13 offset:21504\n"
    "s_waitcnt lgkmcnt(14)\n"
    "ds_read_b128 v[200:203], v13 offset:41984\n"
    "ds_read_b128 v[204:207], v13 offset:62464\n"
    "s_waitcnt lgkmcnt(14)\n"
    "v_pk_add_f32 v[112:113], v[112:113], v[116:117]\n"
    "v_pk_add_f32 v[120:121], v[120:121], v[124:125]\n"
    "s_waitcnt vmcnt(45)\n"
    "v_pk_add_f32 v[32:33], v[32:33], v[112:113]\n"
    "v_pk_add_f32 v[32:33], v[32:33], v[120:121]\n"
    "v_pk_add_f32 v[114:115], v[114:115], v[118:119]\n"
    "v_pk_add_f32 v[122:123], v[122:123], v[126:127]\n"
    "v_pk_add_f32 v[34:35], v[34:35], v[114:115]\n"
    "v_pk_add_f32 v[34:35], v[34:35], v[122:123]\n"
    "v_pk_add_f32 v[128:129], v[128:129], v[132:133]\n"
    "v_pk_add_f32 v[136:137], v[136:137], v[140:141]\n"
    "s_waitcnt vmcnt(44)\n"
    "v_pk_add_f32 v[36:37], v[36:37], v[128:129]\n"
    "v_pk_add_f32 v[36:37], v[36:37], v[136:137]\n"
    "v_pk_add_f32 v[130:131], v[130:131], v[134:135]\n"
    "v_pk_add_f32 v[138:139], v[138:139], v[142:143]\n"
    "v_pk_add_f32 v[38:39], v[38:39], v[130:131]\n"
    "v_pk_add_f32 v[38:39], v[38:39], v[138:139]\n"
    "s_add_u32 s48, s24, 237436928\n"
    "s_lshr_b32 s49, s24, 1\n"
    "s_add_u32 s49, s49, 242679808\n"
    "v_cvt_pk_bf16_f32 v64, v32, v33\n"
    "v_cvt_pk_bf16_f32 v65, v34, v35\n"
    "v_cvt_pk_bf16_f32 v66, v36, v37\n"
    "v_cvt_pk_bf16_f32 v67, v38, v39\n"
    "buffer_store_dwordx4 v[64:67], v0, s[40:43], s49 offen sc1\n"
    "buffer_store_dwordx4 v[32:35], v0, s[40:43], s48 offen sc1\n"
    "buffer_store_dwordx4 v[36:39], v0, s[40:43], s48 offen offset:1024 sc1\n"
    "v_pk_add_f32 v[144:145], v[144:145], v[148:149]\n"
    "s_waitcnt lgkmcnt(12)\n"
    "v_pk_add_f32 v[152:153], v[152:153], v[156:157]\n"
    "s_waitcnt vmcnt(46)\n"
    "v_pk_add_f32 v[40:41], v[40:41], v[144:145]\n"
    "v_pk_add_f32 v[40:41], v[40:41], v[152:153]\n"
    "v_pk_add_f32 v[146:147], v[146:147], v[150:151]\n"
    "v_pk_add_f32 v[154:155], v[154:155], v[158:159]\n"
    "v_pk_add_f32 v[42:43], v[42:43], v[146:147]\n"
    "v_pk_add_f32 v[42:43], v[42:43], v[154:155]\n"
    "s_waitcnt lgkmcnt(10)\n"
    "v_pk_add_f32 v[160:161], v[160:161], v[164:165]\n"
    "s_waitcnt lgkmcnt(8)\n"
    "v_pk_add_f32 v[168:169], v[168:169], v[172:173]\n"
    "s_waitcnt vmcnt(45)\n"
    "v_pk_add_f32 v[44:45], v[44:45], v[160:161]\n"
    "v_pk_add_f32 v[44:45], v[44:45], v[168:169]\n"
    "v_pk_add_f32 v[162:163], v[162:163], v[166:167]\n"
    "v_pk_add_f32 v[170:171], v[170:171], v[174:175]\n"
    "v_pk_add_f32 v[46:47], v[46:47], v[162:163]\n"
    "v_pk_add_f32 v[46:47], v[46:47], v[170:171]\n"
    "s_add_u32 s48, s25, 237436928\n"
    "s_lshr_b32 s49, s25, 1\n"
    "s_add_u32 s49, s49, 242679808\n"
    "v_cvt_pk_bf16_f32 v68, v40, v41\n"
    "v_cvt_pk_bf16_f32 v69, v42, v43\n"
    "v_cvt_pk_bf16_f32 v70, v44, v45\n"
    "v_cvt_pk_bf16_f32 v71, v46, v47\n"
    "buffer_store_dwordx4 v[68:71], v0, s[40:43], s49 offen sc1\n"
    "buffer_store_dwordx4 v[40:43], v0, s[40:43], s48 offen sc1\n"
    "buffer_store_dwordx4 v[44:47], v0, s[40:43], s48 offen offset:1024 sc1\n"
    "s_waitcnt lgkmcnt(6)\n"
    "v_pk_add_f32 v[176:177], v[176:177], v[180:181]\n"
    "s_waitcnt lgkmcnt(4)\n"
    "v_pk_add_f32 v[184:185], v[184:185], v[188:189]\n"
    "s_waitcnt vmcnt(47)\n"
    "v_pk_add_f32 v[48:49], v[48:49], v[176:177]\n"
    "v_pk_add_f32 v[48:49], v[48:49], v[184:185]\n"
    "v_pk_add_f32 v[178:179], v[178:179], v[182:183]\n"
    "v_pk_add_f32 v[186:187], v[186:187], v[190:191]\n"
    "v_pk_add_f32 v[50:51], v[50:51], v[178:179]\n"
    "v_pk_add_f32 v[50:51], v[50:51], v[186:187]\n"
    "s_waitcnt lgkmcnt(2)\n"
    "v_pk_add_f32 v[192:193], v[192:193], v[196:197]\n"
    "s_waitcnt lgkmcnt(0)\n"
    "v_pk_add_f32 v[200:201], v[200:201], v[204:205]\n"
    "s_waitcnt vmcnt(46)\n"
    "v_pk_add_f32 v[52:53], v[52:53], v[192:193]\n"
    "v_pk_add_f32 v[52:53], v[52:53], v[200:201]\n"
    "v_pk_add_f32 v[194:195], v[194:195], v[198:199]\n"
    "v_pk_add_f32 v[202:203], v[202:203], v[206:207]\n"
    "v_pk_add_f32 v[54:55], v[54:55], v[194:195]\n"
    "v_pk_add_f32 v[54:55], v[54:55], v[202:203]\n"
    "s_add_u32 s48, s26, 237436928\n"
    "s_lshr_b32 s49, s26, 1\n"
    "s_add_u32 s49, s49, 242679808\n"
    "v_cvt_pk_bf16_f32 v72, v48, v49\n"
    "v_cvt_pk_bf16_f32 v73, v50, v51\n"
    "v_cvt_pk_bf16_f32 v74, v52, v53\n"
    "v_cvt_pk_bf16_f32 v75, v54, v55\n"
    "buffer_store_dwordx4 v[72:75], v0, s[40:43], s49 offen sc1\n"
    "buffer_store_dwordx4 v[48:51], v0, s[40:43], s48 offen sc1\n"
    "buffer_store_dwordx4 v[52:55], v0, s[40:43], s48 offen offset:1024 sc1\n"
    "s_lshr_b32 s54, s63, 1\n"
    "s_lshl_b32 s54, s54, 7\n"
    "s_add_u32 s54, s54, 2354176\n"
    "buffer_load_dwordx4 a[0:3], v1, s[40:43], s30 offen sc1\n"
    "buffer_load_dwordx4 a[4:7], v1, s[40:43], s31 offen sc1\n"
    "buffer_load_dwordx4 a[8:11], v1, s[40:43], s34 offen sc1\n"
    "buffer_load_dwordx4 a[12:15], v1, s[40:43], s35 offen sc1\n"
    "buffer_load_dwordx4 a[16:19], v1, s[40:43], s36 offen sc1\n"
    "buffer_load_dwordx4 a[20:23], v1, s[40:43], s37 offen sc1\n"
    "buffer_load_dwordx4 a[24:27], v1, s[40:43], s38 offen sc1\n"
    "buffer_load_dwordx4 a[28:31], v1, s[40:43], s39 offen sc1\n"
    "buffer_load_dwordx4 a[32:35], v1, s[40:43], s96 offen sc1\n"
    "buffer_load_dwordx4 a[36:39], v1, s[40:43], s97 offen sc1\n"
    "buffer_load_dwordx4 a[40:43], v1, s[40:43], s30 offen offset:128 sc1\n"
    "buffer_load_dwordx4 a[44:47], v1, s[40:43], s31 offen offset:128 sc1\n"
    "buffer_load_dwordx4 a[48:51], v1, s[40:43], s34 offen offset:128 sc1\n"
    "buffer_load_dwordx4 a[52:55], v1, s[40:43], s35 offen offset:128 sc1\n"
    "buffer_load_dwordx4 a[56:59], v1, s[40:43], s36 offen offset:128 sc1\n"
    "buffer_load_dwordx4 a[60:63], v1, s[40:43], s37 offen offset:128 sc1\n"
    "buffer_load_dwordx4 a[64:67], v1, s[40:43], s38 offen offset:128 sc1\n"
    "buffer_load_dwordx4 a[68:71], v1, s[40:43], s39 offen offset:128 sc1\n"
    "buffer_load_dwordx4 a[72:75], v1, s[40:43], s96 offen offset:128 sc1\n"
    "buffer_load_dwordx4 a[76:79], v1, s[40:43], s97 offen offset:128 sc1\n"
    "buffer_load_dwordx4 a[240:243], v1, s[40:43], s30 offen offset:3072 sc1\n"
    "buffer_load_dwordx4 a[244:247], v1, s[40:43], s31 offen offset:3072 sc1\n"
    "buffer_load_dwordx4 a[248:251], v1, s[40:43], s34 offen offset:3072 sc1\n"
    "buffer_load_dwordx4 a[252:255], v1, s[40:43], s35 offen offset:3072 sc1\n"
    "buffer_load_dwordx4 v[192:195], v1, s[40:43], s36 offen offset:3072 sc1\n"
    "buffer_load_dwordx4 v[196:199], v1, s[40:43], s37 offen offset:3072 sc1\n"
    "buffer_load_dwordx4 v[200:203], v1, s[40:43], s38 offen offset:3072 sc1\n"
    "buffer_load_dwordx4 v[204:207], v1, s[40:43], s39 offen offset:3072 sc1\n"
    "buffer_load_dwordx4 v[208:211], v1, s[40:43], s96 offen offset:3072 sc1\n"
    "buffer_load_dwordx4 v[212:215], v1, s[40:43], s97 offen offset:3072 sc1\n"
    "buffer_load_dwordx4 v[216:219], v1, s[40:43], s30 offen offset:3200 sc1\n"
    "buffer_load_dwordx4 v[220:223], v1, s[40:43], s31 offen offset:3200 sc1\n"
    "buffer_load_dwordx4 v[224:227], v1, s[40:43], s34 offen offset:3200 sc1\n"
    "buffer_load_dwordx4 v[228:231], v1, s[40:43], s35 offen offset:3200 sc1\n"
    "buffer_load_dwordx4 v[232:235], v1, s[40:43], s36 offen offset:3200 sc1\n"
    "buffer_load_dwordx4 v[236:239], v1, s[40:43], s37 offen offset:3200 sc1\n"
    "buffer_load_dwordx4 v[240:243], v1, s[40:43], s38 offen offset:3200 sc1\n"
    "buffer_load_dwordx4 v[244:247], v1, s[40:43], s39 offen offset:3200 sc1\n"
    "buffer_load_dwordx4 v[248:251], v1, s[40:43], s96 offen offset:3200 sc1\n"
    "buffer_load_dwordx4 v[252:255], v1, s[40:43], s97 offen offset:3200 sc1\n"
    "s_waitcnt vmcnt(40)\n"
    "s_waitcnt lgkmcnt(0)\n"
    "s_barrier\n"
    "buffer_atomic_add v6, v3, s[40:43], s54 offen\n"
    "s_add_u32 s63, s63, 4\n"
    "s_cmp_lt_u32 s47, 60\n"
    "s_cbranch_scc1 tk_loop_%=\n"
    "s_cmp_lt_u32 s63, 128\n"
    "s_cbranch_scc1 tk_first_%=\n"
    "s_branch tk_end_%=\n"
    "tk_abort_%=:\n"
    "v_mov_b32 v4, 1\n"
    "v_mov_b32 v5, 1024\n"
    "buffer_store_dword v4, v5, s[40:43], s58 offen sc1\n"
    "tk_end_%=:\n"
    "s_endpgm\n"
    :: CHAIN_INPUTS : CHAIN_CLOBBERS);
    __builtin_unreachable();
}
DEV void role_helper(unsigned lo, unsigned hi, int id, int w, int aux, const Tune& tn) {
    asm volatile(
    "s_mov_b32 s40, %0\n"
    "s_mov_b32 s41, %1\n"
    "s_mov_b32 s42, %2\n"
    "s_mov_b32 s43, %3\n"
    "s_mov_b32 s44, %4\n"
    "s_mov_b32 s45, %5\n"
    "s_mov_b32 s46, %6\n"
    "s_mov_b32 s66, %7\n"
    "s_mov_b32 s67, %8\n"
    "s_mov_b32 s68, %9\n"
    "s_mov_b32 s69, %10\n"
    "s_mov_b32 s70, %11\n"
    "s_mov_b32 s71, %12\n"
    "s_mov_b32 s72, %13\n"
    "s_mov_b32 s73, %14\n"
    "s_mov_b64 exec, -1\n"
    "v_mbcnt_lo_u32_b32 v15, -1, 0\n"
    "v_mbcnt_hi_u32_b32 v15, -1, v15\n"
    "v_lshlrev_b32 v0, 4, v15\n"
    "s_mov_b32 s58, 2097152\n"
    "s_mov_b32 s92, 0\n"
    "s_mov_b32 s21, 0\n"
    "s_mov_b32 s22, 0x3fb8aa3b\n"
    "s_mov_b32 s23, s22\n"
    "s_mov_b32 s24, 0xbfb8aa3b\n"
    "s_mov_b32 s25, s24\n"
    "hp_layer_%=:\n"
    "s_lshl_b32 s47, s21, 6\n"
    "s_cmp_eq_u32 s46, 0\n"
    "s_cbranch_scc1 hp_roleq_%=\n"
    "s_cmp_eq_u32 s46, 3\n"
    "s_cbranch_scc1 hp_roleo_%=\n"
    "s_lshl_b32 s48, s46, 5\n"
    "s_add_u32 s48, s48, s44\n"
    "s_mul_i32 s48, s48, 12\n"
    "s_mul_i32 s49, s45, 3\n"
    "s_add_u32 s48, s48, s49\n"
    "s_mul_i32 s48, s48, 20480\n"
    "s_add_u32 s48, s48, 2523136\n"
    "s_mul_i32 s62, s45, 10240\n"
    "s_mul_i32 s50, s47, 40960\n"
    "s_add_u32 s50, s50, s62\n"
    "s_add_u32 s50, s50, 60194816\n"
    "s_cmp_lg_u32 s47, 0\n"
    "s_cbranch_scc1 hpk_havew_%=\n"
    "buffer_load_dwordx4 a[0:3], v0, s[40:43], s48 offen\n"
    "buffer_load_dwordx4 a[4:7], v0, s[40:43], s48 offen offset:1024\n"
    "buffer_load_dwordx4 a[8:11], v0, s[40:43], s48 offen offset:2048\n"
    "buffer_load_dwordx4 a[12:15], v0, s[40:43], s48 offen offset:3072\n"
    "s_add_u32 s48, s48, 4096\n"
    "buffer_load_dwordx4 a[16:19], v0, s[40:43], s48 offen\n"
    "buffer_load_dwordx4 a[20:23], v0, s[40:43], s48 offen offset:1024\n"
    "buffer_load_dwordx4 a[24:27], v0, s[40:43], s48 offen offset:2048\n"
    "buffer_load_dwordx4 a[28:31], v0, s[40:43], s48 offen offset:3072\n"
    "s_add_u32 s48, s48, 4096\n"
    "buffer_load_dwordx4 a[32:35], v0, s[40:43], s48 offen\n"
    "buffer_load_dwordx4 a[36:39], v0, s[40:43], s48 offen offset:1024\n"
    "buffer_load_dwordx4 a[40:43], v0, s[40:43], s48 offen offset:2048\n"
    "buffer_load_dwordx4 a[44:47], v0, s[40:43], s48 offen offset:3072\n"
    "s_add_u32 s48, s48, 4096\n"
    "buffer_load_dwordx4 a[48:51], v0, s[40:43], s48 offen\n"
    "buffer_load_dwordx4 a[52:55], v0, s[40:43], s48 offen offset:1024\n"
    "buffer_load_dwordx4 a[56:59], v0, s[40:43], s48 offen offset:2048\n"
    "buffer_load_dwordx4 a[60:63], v0, s[40:43], s48 offen offset:3072\n"
    "s_add_u32 s48, s48, 4096\n"
    "buffer_load_dwordx4 a[64:67], v0, s[40:43], s48 offen\n"
    "buffer_load_dwordx4 a[68:71], v0, s[40:43], s48 offen offset:1024\n"
    "buffer_load_dwordx4 a[72:75], v0, s[40:43], s48 offen offset:2048\n"
    "buffer_load_dwordx4 a[76:79], v0, s[40:43], s48 offen offset:3072\n"
    "s_add_u32 s48, s48, 4096\n"
    "buffer_load_dwordx4 a[80:83], v0, s[40:43], s48 offen\n"
    "buffer_load_dwordx4 a[84:87], v0, s[40:43], s48 offen offset:1024\n"
    "buffer_load_dwordx4 a[88:91], v0, s[40:43], s48 offen offset:2048\n"
    "buffer_load_dwordx4 a[92:95], v0, s[40:43], s48 offen offset:3072\n"
    "s_add_u32 s48, s48, 4096\n"
    "buffer_load_dwordx4 a[96:99], v0, s[40:43], s48 offen\n"
    "buffer_load_dwordx4 a[100:103], v0, s[40:43], s48 offen offset:1024\n"
    "buffer_load_dwordx4 a[104:107], v0, s[40:43], s48 offen offset:2048\n"
    "buffer_load_dwordx4 a[108:111], v0, s[40:43], s48 offen offset:3072\n"
    "s_add_u32 s48, s48, 4096\n"
    "buffer_load_dwordx4 a[112:115], v0, s[40:43], s48 offen\n"
    "buffer_load_dwordx4 a[116:119], v0, s[40:43], s48 offen offset:1024\n"
    "buffer_load_dwordx4 a[120:123], v0, s[40:43], s48 offen offset:2048\n"
    "buffer_load_dwordx4 a[124:127], v0, s[40:43], s48 offen offset:3072\n"
    "s_add_u32 s48, s48, 4096\n"
    "buffer_load_dwordx4 a[128:131], v0, s[40:43], s48 offen\n"
    "buffer_load_dwordx4 a[132:135], v0, s[40:43], s48 offen offset:1024\n"
    "buffer_load_dwordx4 a[136:139], v0, s[40:43], s48 offen offset:2048\n"
    "buffer_load_dwordx4 a[140:143], v0, s[40:43], s48 offen offset:3072\n"
    "s_add_u32 s48, s48, 4096\n"
    "buffer_load_dwordx4 a[144:147], v0, s[40:43], s48 offen\n"
    "buffer_load_dwordx4 a[148:151], v0, s[40:43], s48 offen offset:1024\n"
    "buffer_load_dwordx4 a[152:155], v0, s[40:43], s48 offen offset:2048\n"
    "buffer_load_dwordx4 a[156:159], v0, s[40:43], s48 offen offset:3072\n"
    "s_add_u32 s48, s48, 4096\n"
    "buffer_load_dwordx4 a[160:163], v0, s[40:43], s48 offen\n"
    "buffer_load_dwordx4 a[164:167], v0, s[40:43], s48 offen offset:1024\n"
    "buffer_load_dwordx4 a[168:171], v0, s[40:43], s48 offen offset:2048\n"
    "buffer_load_dwordx4 a[172:175], v0, s[40:43], s48 offen offset:3072\n"
    "s_add_u32 s48, s48, 4096\n"
    "buffer_load_dwordx4 a[176:179], v0, s[40:43], s48 offen\n"
    "buffer_load_dwordx4 a[180:183], v0, s[40:43], s48 offen offset:1024\n"
    "buffer_load_dwordx4 a[184:187], v0, s[40:43], s48 offen offset:2048\n"
    "buffer_load_dwordx4 a[188:191], v0, s[40:43], s48 offen offset:3072\n"
    "s_add_u32 s48, s48, 4096\n"
    "buffer_load_dwordx4 a[192:195], v0, s[40:43], s48 offen\n"
    "buffer_load_dwordx4 a[196:199], v0, s[40:43], s48 offen offset:1024\n"
    "buffer_load_dwordx4 a[200:203], v0, s[40:43], s48 offen offset:2048\n"
    "buffer_load_dwordx4 a[204:207], v0, s[40:43], s48 offen offset:3072\n"
    "s_add_u32 s48, s48, 4096\n"
    "buffer_load_dwordx4 a[208:211], v0, s[40:43], s48 offen\n"
    "buffer_load_dwordx4 a[212:215], v0, s[40:43], s48 offen offset:1024\n"
    "buffer_load_dwordx4 a[216:219], v0, s[40:43], s48 offen offset:2048\n"
    "buffer_load_dwordx4 a[220:223], v0, s[40:43], s48 offen offset:3072\n"
    "s_add_u32 s48, s48, 4096\n"
    "buffer_load_dwordx4 a[224:227], v0, s[40:43], s48 offen\n"
    "buffer_load_dwordx4 a[228:231], v0, s[40:43], s48 offen offset:1024\n"
    "buffer_load_dwordx4 a[232:235], v0, s[40:43], s48 offen offset:2048\n"
    "buffer_load_dwordx4 a[236:239], v0, s[40:43], s48 offen offset:3072\n"
    "hpk_havew_%=:\n"
    "s_mov_b32 s61, s62\n"
    "s_lshl_b32 s48, s47, 7\n"
    "s_add_u32 s48, s48, 43520\n"
    "v_mov_b32 v225, s48\n"
    "s_mov_b32 s48, 1\n"
    "v_mov_b32 v224, 0\n"
    "s_mov_b32 s60, 0\n"
    "v_cmp_gt_u32 vcc, s48, v224\n"
    "s_nop 4\n"
    "s_cbranch_vccz hpk_xfp0_ok_%=\n"
    "hpk_xfp0_poll_%=:\n"
    "buffer_load_dword v224, v225, s[40:43], s58 offen sc1\n"
    "s_waitcnt vmcnt(0)\n"
    "v_cmp_gt_u32 vcc, s48, v224\n"
    "s_nop 4\n"
    "s_cbranch_vccz hpk_xfp0_ok_%=\n"
    "s_sleep 2\n"
    "s_add_u32 s60, s60, 1\n"
    "s_cmp_gt_u32 s60, 65536\n"
    "s_cbranch_scc1 hp_abort_%=\n"
    "s_and_b32 s59, s60, 63\n"
    "s_cbranch_scc1 hpk_xfp0_poll_%=\n"
    "v_mov_b32 v4, 1024\n"
    "buffer_load_dword v4, v4, s[40:43], s58 offen sc1\n"
    "s_waitcnt vmcnt(0)\n"
    "v_readfirstlane_b32 s59, v4\n"
    "s_nop 0\n"
    "s_cmp_lg_u32 s59, 0\n"
    "s_nop 2\n"
    "s_cbranch_scc1 hp_end_%=\n"
    "s_branch hpk_xfp0_poll_%=\n"
    "hpk_xfp0_ok_%=:\n"
    "v_add_u32 v225, 128, v225\n"
    "s_add_u32 m0, s61, 0\n"
    "s_add_u32 s51, s50, 0\n"
    "buffer_load_dwordx4 v0, s[40:43], s51 offen offset:0 sc1 lds\n"
    "buffer_load_dwordx4 v0, s[40:43], s51 offen offset:1024 sc1 lds\n"
    "buffer_load_dwordx4 v0, s[40:43], s51 offen offset:2048 sc1 lds\n"
    "buffer_load_dwordx4 v0, s[40:43], s51 offen offset:3072 sc1 lds\n"
    "s_add_u32 m0, s61, 4096\n"
    "s_add_u32 s51, s50, 4096\n"
    "buffer_load_dwordx4 v0, s[40:43], s51 offen offset:0 sc1 lds\n"
    "buffer_load_dwordx4 v0, s[40:43], s51 offen offset:1024 sc1 lds\n"
    "buffer_load_dwordx4 v0, s[40:43], s51 offen offset:2048 sc1 lds\n"
    "buffer_load_dwordx4 v0, s[40:43], s51 offen offset:3072 sc1 lds\n"
    "s_add_u32 m0, s61, 8192\n"
    "s_add_u32 s51, s50, 8192\n"
    "buffer_load_dwordx4 v0, s[40:43], s51 offen offset:0 sc1 lds\n"
    "buffer_load_dwordx4 v0, s[40:43], s51 offen offset:1024 sc1 lds\n"
    "s_add_u32 s50, s50, 40960\n"
    "s_add_u32 s61, s61, 40960\n"
    "s_cmp_ge_u32 s61, 122880\n"
    "s_cselect_b32 s51, 122880, 0\n"
    "s_sub_u32 s61, s61, s51\n"
    "v_mov_b32 v224, 0\n"
    "s_mov_b32 s60, 0\n"
    "v_cmp_gt_u32 vcc, s48, v224\n"
    "s_nop 4\n"
    "s_cbranch_vccz hpk_xfp1_ok_%=\n"
    "hpk_xfp1_poll_%=:\n"
    "buffer_load_dword v224, v225, s[40:43], s58 offen sc1\n"
    "s_waitcnt vmcnt(0)\n"
    "v_cmp_gt_u32 vcc, s48, v224\n"
    "s_nop 4\n"
    "s_cbranch_vccz hpk_xfp1_ok_%=\n"
    "s_sleep 2\n"
    "s_add_u32 s60, s60, 1\n"
    "s_cmp_gt_u32 s60, 65536\n"
    "s_cbranch_scc1 hp_abort_%=\n"
    "s_and_b32 s59, s60, 63\n"
    "s_cbranch_scc1 hpk_xfp1_poll_%=\n"
    "v_mov_b32 v4, 1024\n"
    "buffer_load_dword v4, v4, s[40:43], s58 offen sc1\n"
    "s_waitcnt vmcnt(0)\n"
    "v_readfirstlane_b32 s59, v4\n"
    "s_nop 0\n"
    "s_cmp_lg_u32 s59, 0\n"
    "s_nop 2\n"
    "s_cbranch_scc1 hp_end_%=\n"
    "s_branch hpk_xfp1_poll_%=\n"
    "hpk_xfp1_ok_%=:\n"
    "v_add_u32 v225, 128, v225\n"
    "s_add_u32 m0, s61, 0\n"
    "s_add_u32 s51, s50, 0\n"
    "buffer_load_dwordx4 v0, s[40:43], s51 offen offset:0 sc1 lds\n"
    "buffer_load_dwordx4 v0, s[40:43], s51 offen offset:1024 sc1 lds\n"
    "buffer_load_dwordx4 v0, s[40:43], s51 offen offset:2048 sc1 lds\n"
    "buffer_load_dwordx4 v0, s[40:43], s51 offen offset:3072 sc1 lds\n"
    "s_add_u32 m0, s61, 4096\n"
    "s_add_u32 s51, s50, 4096\n"
    "buffer_load_dwordx4 v0, s[40:43], s51 offen offset:0 sc1 lds\n"
    "buffer_load_dwordx4 v0, s[40:43], s51 offen offset:1024 sc1 lds\n"
    "buffer_load_dwordx4 v0, s[40:43], s51 offen offset:2048 sc1 lds\n"
    "buffer_load_dwordx4 v0, s[40:43], s51 offen offset:3072 sc1 lds\n"
    "s_add_u32 m0, s61, 8192\n"
    "s_add_u32 s51, s50, 8192\n"
    "buffer_load_dwordx4 v0, s[40:43], s51 offen offset:0 sc1 lds\n"
    "buffer_load_dwordx4 v0, s[40:43], s51 offen offset:1024 sc1 lds\n"
    "s_add_u32 s50, s50, 40960\n"
    "s_add_u32 s61, s61, 40960\n"
    "s_cmp_ge_u32 s61, 122880\n"
    "s_cselect_b32 s51, 122880, 0\n"
    "s_sub_u32 s61, s61, s51\n"
    "v_mov_b32 v224, 0\n"
    "v_mov_b32 v226, 1\n"
    "v_mov_b32 v1, v0\n"
    "s_mov_b32 s64, 0\n"
    "s_lshl_b32 s48, s44, 7\n"
    "s_cmp_eq_u32 s45, 0\n"
    "s_cselect_b32 s59, 1, 0\n"
    "v_lshlrev_b32 v3, 7, v15\n"
    "v_cmp_gt_u32 vcc, s59, v15\n"
    "v_mov_b32 v4, 2147483632\n"
    "s_nop 0\n"
    "v_cndmask_b32 v3, v4, v3, vcc\n"
    "s_add_u32 s53, s48, 2099200\n"
    "s_lshl_b32 s54, s47, 7\n"
    "s_add_u32 s54, s54, 2107392\n"
    "s_mov_b32 s63, 90603520\n"
    "s_cmp_eq_u32 s46, 1\n"
    "s_cselect_b32 s53, s54, s53\n"
    "s_cselect_b32 s54, 128, 0\n"
    "s_cselect_b32 s63, 65437696, s63\n"
    "s_mul_i32 s49, s44, 12288\n"
    "s_add_u32 s63, s63, s49\n"
    "s_mul_i32 s49, s45, 3072\n"
    "s_add_u32 s63, s63, s49\n"
    "s_mov_b32 s52, 0x40000000\n"
    "s_waitcnt vmcnt(0)\n"
    "s_waitcnt lgkmcnt(0)\n"
    "s_barrier\n"
    "s_mov_b32 s65, 0x40000000\n"
    "ds_read_b128 v[80:83], v1 offset:0\n"
    "ds_read_b128 v[84:87], v1 offset:20480\n"
    "ds_read_b128 v[88:91], v1 offset:1024\n"
    "ds_read_b128 v[92:95], v1 offset:21504\n"
    "ds_read_b128 v[96:99], v1 offset:2048\n"
    "hpk_loop_%=:\n"
    "s_waitcnt lgkmcnt(4)\n"
    "v_mfma_f32_16x16x32_bf16 v[16:19], a[0:3], v[80:83], 0\n"
    "v_mfma_f32_16x16x32_bf16 v[24:27], a[80:83], v[80:83], 0\n"
    "v_mfma_f32_16x16x32_bf16 v[32:35], a[160:163], v[80:83], 0\n"
    "ds_read_b128 v[80:83], v1 offset:22528\n"
    "s_waitcnt lgkmcnt(4)\n"
    "v_mfma_f32_16x16x32_bf16 v[20:23], a[0:3], v[84:87], 0\n"
    "v_mfma_f32_16x16x32_bf16 v[28:31], a[80:83], v[84:87], 0\n"
    "v_mfma_f32_16x16x32_bf16 v[36:39], a[160:163], v[84:87], 0\n"
    "ds_read_b128 v[84:87], v1 offset:3072\n"
    "s_waitcnt lgkmcnt(4)\n"
    "v_mfma_f32_16x16x32_bf16 v[16:19], a[4:7], v[88:91], v[16:19]\n"
    "v_mfma_f32_16x16x32_bf16 v[24:27], a[84:87], v[88:91], v[24:27]\n"
    "v_mfma_f32_16x16x32_bf16 v[32:35], a[164:167], v[88:91], v[32:35]\n"
    "ds_read_b128 v[88:91], v1 offset:23552\n"
    "s_waitcnt lgkmcnt(4)\n"
    "v_mfma_f32_16x16x32_bf16 v[20:23], a[4:7], v[92:95], v[20:23]\n"
    "v_cvt_pk_bf16_f32 v104, v40, v41\n"
    "v_mfma_f32_16x16x32_bf16 v[28:31], a[84:87], v[92:95], v[28:31]\n"
    "v_cvt_pk_bf16_f32 v105, v42, v43\n"
    "v_mfma_f32_16x16x32_bf16 v[36:39], a[164:167], v[92:95], v[36:39]\n"
    "ds_read_b128 v[92:95], v1 offset:4096\n"
    "s_waitcnt lgkmcnt(4)\n"
    "v_mfma_f32_16x16x32_bf16 v[16:19], a[8:11], v[96:99], v[16:19]\n"
    "v_cvt_pk_bf16_f32 v106, v44, v45\n"
    "v_mfma_f32_16x16x32_bf16 v[24:27], a[88:91], v[96:99], v[24:27]\n"
    "v_cvt_pk_bf16_f32 v107, v46, v47\n"
    "v_mfma_f32_16x16x32_bf16 v[32:35], a[168:171], v[96:99], v[32:35]\n"
    "ds_read_b128 v[96:99], v1 offset:24576\n"
    "s_waitcnt lgkmcnt(4)\n"
    "v_mfma_f32_16x16x32_bf16 v[20:23], a[8:11], v[80:83], v[20:23]\n"
    "buffer_store_dwordx4 v[104:107], v0, s[40:43], s65 offen offset:0 sc1\n"
    "v_mfma_f32_16x16x32_bf16 v[28:31], a[88:91], v[80:83], v[28:31]\n"
    "v_cvt_pk_bf16_f32 v108, v48, v49\n"
    "v_mfma_f32_16x16x32_bf16 v[36:39], a[168:171], v[80:83], v[36:39]\n"
    "ds_read_b128 v[80:83], v1 offset:5120\n"
    "s_waitcnt lgkmcnt(4)\n"
    "v_mfma_f32_16x16x32_bf16 v[16:19], a[12:15], v[84:87], v[16:19]\n"
    "v_cvt_pk_bf16_f32 v109, v50, v51\n"
    "v_mfma_f32_16x16x32_bf16 v[24:27], a[92:95], v[84:87], v[24:27]\n"
    "v_cvt_pk_bf16_f32 v110, v52, v53\n"
    "v_mfma_f32_16x16x32_bf16 v[32:35], a[172:175], v[84:87], v[32:35]\n"
    "ds_read_b128 v[84:87], v1 offset:25600\n"
    "s_waitcnt lgkmcnt(4)\n"
    "v_mfma_f32_16x16x32_bf16 v[20:23], a[12:15], v[88:91], v[20:23]\n"
    "v_cvt_pk_bf16_f32 v111, v54, v55\n"
    "v_mfma_f32_16x16x32_bf16 v[28:31], a[92:95], v[88:91], v[28:31]\n"
    "buffer_store_dwordx4 v[108:111], v0, s[40:43], s65 offen offset:1024 sc1\n"
    "v_mfma_f32_16x16x32_bf16 v[36:39], a[172:175], v[88:91], v[36:39]\n"
    "ds_read_b128 v[88:91], v1 offset:6144\n"
    "s_waitcnt lgkmcnt(4)\n"
    "v_mfma_f32_16x16x32_bf16 v[16:19], a[16:19], v[92:95], v[16:19]\n"
    "v_cvt_pk_bf16_f32 v112, v56, v57\n"
    "v_mfma_f32_16x16x32_bf16 v[24:27], a[96:99], v[92:95], v[24:27]\n"
    "v_cvt_pk_bf16_f32 v113, v58, v59\n"
    "v_mfma_f32_16x16x32_bf16 v[32:35], a[176:179], v[92:95], v[32:35]\n"
    "ds_read_b128 v[92:95], v1 offset:26624\n"
    "s_waitcnt lgkmcnt(4)\n"
    "v_mfma_f32_16x16x32_bf16 v[20:23], a[16:19], v[96:99], v[20:23]\n"
    "v_cvt_pk_bf16_f32 v114, v60, v61\n"
    "v_mfma_f32_16x16x32_bf16 v[28:31], a[96:99], v[96:99], v[28:31]\n"
    "v_cvt_pk_bf16_f32 v115, v62, v63\n"
    "v_mfma_f32_16x16x32_bf16 v[36:39], a[176:179], v[96:99], v[36:39]\n"
    "ds_read_b128 v[96:99], v1 offset:7168\n"
    "s_waitcnt lgkmcnt(4)\n"
    "v_mfma_f32_16x16x32_bf16 v[16:19], a[20:23], v[80:83], v[16:19]\n"
    "buffer_store_dwordx4 v[112:115], v0, s[40:43], s65 offen offset:2048 sc1\n"
    "v_mfma_f32_16x16x32_bf16 v[24:27], a[100:103], v[80:83], v[24:27]\n"
    "buffer_load_dword v224, v225, s[40:43], s58 offen sc1\n"
    "v_mfma_f32_16x16x32_bf16 v[32:35], a[180:183], v[80:83], v[32:35]\n"
    "ds_read_b128 v[80:83], v1 offset:27648\n"
    "s_waitcnt lgkmcnt(4)\n"
    "v_mfma_f32_16x16x32_bf16 v[20:23], a[20:23], v[84:87], v[20:23]\n"
    "v_mfma_f32_16x16x32_bf16 v[28:31], a[100:103], v[84:87], v[28:31]\n"
    "v_mfma_f32_16x16x32_bf16 v[36:39], a[180:183], v[84:87], v[36:39]\n"
    "ds_read_b128 v[84:87], v1 offset:8192\n"
    "s_waitcnt lgkmcnt(4)\n"
    "v_mfma_f32_16x16x32_bf16 v[16:19], a[24:27], v[88:91], v[16:19]\n"
    "v_mfma_f32_16x16x32_bf16 v[24:27], a[104:107], v[88:91], v[24:27]\n"
    "v_mfma_f32_16x16x32_bf16 v[32:35], a[184:187], v[88:91], v[32:35]\n"
    "ds_read_b128 v[88:91], v1 offset:28672\n"
    "s_waitcnt lgkmcnt(4)\n"
    "v_mfma_f32_16x16x32_bf16 v[20:23], a[24:27], v[92:95], v[20:23]\n"
    "v_mfma_f32_16x16x32_bf16 v[28:31], a[104:107], v[92:95], v[28:31]\n"
    "v_mfma_f32_16x16x32_bf16 v[36:39], a[184:187], v[92:95], v[36:39]\n"
    "ds_read_b128 v[92:95], v1 offset:9216\n"
    "s_waitcnt lgkmcnt(4)\n"
    "v_mfma_f32_16x16x32_bf16 v[16:19], a[28:31], v[96:99], v[16:19]\n"
    "v_mfma_f32_16x16x32_bf16 v[24:27], a[108:111], v[96:99], v[24:27]\n"
    "v_mfma_f32_16x16x32_bf16 v[32:35], a[188:191], v[96:99], v[32:35]\n"
    "ds_read_b128 v[96:99], v1 offset:29696\n"
    "s_waitcnt lgkmcnt(4)\n"
    "v_mfma_f32_16x16x32_bf16 v[20:23], a[28:31], v[80:83], v[20:23]\n"
    "v_mfma_f32_16x16x32_bf16 v[28:31], a[108:111], v[80:83], v[28:31]\n"
    "v_mfma_f32_16x16x32_bf16 v[36:39], a[188:191], v[80:83], v[36:39]\n"
    "ds_read_b128 v[80:83], v1 offset:10240\n"
    "s_waitcnt lgkmcnt(4)\n"
    "v_mfma_f32_16x16x32_bf16 v[16:19], a[32:35], v[84:87], v[16:19]\n"
    "v_mfma_f32_16x16x32_bf16 v[24:27], a[112:115], v[84:87], v[24:27]\n"
    "v_mfma_f32_16x16x32_bf16 v[32:35], a[192:195], v[84:87], v[32:35]\n"
    "ds_read_b128 v[84:87], v1 offset:30720\n"
    "s_waitcnt lgkmcnt(4)\n"
    "v_mfma_f32_16x16x32_bf16 v[20:23], a[32:35], v[88:91], v[20:23]\n"
    "v_mfma_f32_16x16x32_bf16 v[28:31], a[112:115], v[88:91], v[28:31]\n"
    "v_mfma_f32_16x16x32_bf16 v[36:39], a[192:195], v[88:91], v[36:39]\n"
    "ds_read_b128 v[88:91], v1 offset:11264\n"
    "s_waitcnt lgkmcnt(4)\n"
    "v_mfma_f32_16x16x32_bf16 v[16:19], a[36:39], v[92:95], v[16:19]\n"
    "v_mfma_f32_16x16x32_bf16 v[24:27], a[116:119], v[92:95], v[24:27]\n"
    "v_mfma_f32_16x16x32_bf16 v[32:35], a[196:199], v[92:95], v[32:35]\n"
    "ds_read_b128 v[92:95], v1 offset:31744\n"
    "s_waitcnt lgkmcnt(4)\n"
    "v_mfma_f32_16x16x32_bf16 v[20:23], a[36:39], v[96:99], v[20:23]\n"
    "v_mfma_f32_16x16x32_bf16 v[28:31], a[116:119], v[96:99], v[28:31]\n"
    "v_mfma_f32_16x16x32_bf16 v[36:39], a[196:199], v[96:99], v[36:39]\n"
    "ds_read_b128 v[96:99], v1 offset:12288\n"
    "s_waitcnt lgkmcnt(4)\n"
    "v_mfma_f32_16x16x32_bf16 v[16:19], a[40:43], v[80:83], v[16:19]\n"
    "v_mfma_f32_16x16x32_bf16 v[24:27], a[120:123], v[80:83], v[24:27]\n"
    "v_mfma_f32_16x16x32_bf16 v[32:35], a[200:203], v[80:83], v[32:35]\n"
    "ds_read_b128 v[80:83], v1 offset:32768\n"
    "s_waitcnt lgkmcnt(4)\n"
    "v_mfma_f32_16x16x32_bf16 v[20:23], a[40:43], v[84:87], v[20:23]\n"
    "v_mfma_f32_16x16x32_bf16 v[28:31], a[120:123], v[84:87], v[28:31]\n"
    "v_mfma_f32_16x16x32_bf16 v[36:39], a[200:203], v[84:87], v[36:39]\n"
    "ds_read_b128 v[84:87], v1 offset:13312\n"
    "s_waitcnt lgkmcnt(4)\n"
    "v_mfma_f32_16x16x32_bf16 v[16:19], a[44:47], v[88:91], v[16:19]\n"
    "v_mfma_f32_16x16x32_bf16 v[24:27], a[124:127], v[88:91], v[24:27]\n"
    "v_mfma_f32_16x16x32_bf16 v[32:35], a[204:207], v[88:91], v[32:35]\n"
    "ds_read_b128 v[88:91], v1 offset:33792\n"
    "s_waitcnt lgkmcnt(4)\n"
    "v_mfma_f32_16x16x32_bf16 v[20:23], a[44:47], v[92:95], v[20:23]\n"
    "v_mfma_f32_16x16x32_bf16 v[28:31], a[124:127], v[92:95], v[28:31]\n"
    "v_mfma_f32_16x16x32_bf16 v[36:39], a[204:207], v[92:95], v[36:39]\n"
    "ds_read_b128 v[92:95], v1 offset:14336\n"
    "s_waitcnt lgkmcnt(4)\n"
    "v_mfma_f32_16x16x32_bf16 v[16:19], a[48:51], v[96:99], v[16:19]\n"
    "v_mfma_f32_16x16x32_bf16 v[24:27], a[128:131], v[96:99], v[24:27]\n"
    "v_mfma_f32_16x16x32_bf16 v[32:35], a[208:211], v[96:99], v[32:35]\n"
    "ds_read_b128 v[96:99], v1 offset:34816\n"
    "s_waitcnt lgkmcnt(4)\n"
    "v_mfma_f32_16x16x32_bf16 v[20:23], a[48:51], v[80:83], v[20:23]\n"
    "v_mfma_f32_16x16x32_bf16 v[28:31], a[128:131], v[80:83], v[28:31]\n"
    "v_mfma_f32_16x16x32_bf16 v[36:39], a[208:211], v[80:83], v[36:39]\n"
    "ds_read_b128 v[80:83], v1 offset:15360\n"
    "s_waitcnt lgkmcnt(4)\n"
    "v_mfma_f32_16x16x32_bf16 v[16:19], a[52:55], v[84:87], v[16:19]\n"
    "v_mfma_f32_16x16x32_bf16 v[24:27], a[132:135], v[84:87], v[24:27]\n"
    "v_mfma_f32_16x16x32_bf16 v[32:35], a[212:215], v[84:87], v[32:35]\n"
    "ds_read_b128 v[84:87], v1 offset:35840\n"
    "s_waitcnt lgkmcnt(4)\n"
    "v_mfma_f32_16x16x32_bf16 v[20:23], a[52:55], v[88:91], v[20:23]\n"
    "v_mfma_f32_16x16x32_bf16 v[28:31], a[132:135], v[88:91], v[28:31]\n"
    "v_mfma_f32_16x16x32_bf16 v[36:39], a[212:215], v[88:91], v[36:39]\n"
    "ds_read_b128 v[88:91], v1 offset:16384\n"
    "s_waitcnt lgkmcnt(4)\n"
    "v_mfma_f32_16x16x32_bf16 v[16:19], a[56:59], v[92:95], v[16:19]\n"
    "v_mfma_f32_16x16x32_bf16 v[24:27], a[136:139], v[92:95], v[24:27]\n"
    "v_mfma_f32_16x16x32_bf16 v[32:35], a[216:219], v[92:95], v[32:35]\n"
    "ds_read_b128 v[92:95], v1 offset:36864\n"
    "s_waitcnt lgkmcnt(4)\n"
    "v_mfma_f32_16x16x32_bf16 v[20:23], a[56:59], v[96:99], v[20:23]\n"
    "v_mfma_f32_16x16x32_bf16 v[28:31], a[136:139], v[96:99], v[28:31]\n"
    "v_mfma_f32_16x16x32_bf16 v[36:39], a[216:219], v[96:99], v[36:39]\n"
    "s_waitcnt vmcnt(0)\n"
    "s_barrier\n"
    "buffer_atomic_add v226, v3, s[40:43], s52 offen\n"
    "s_and_b32 s48, s47, 63\n"
    "s_cmp_lt_u32 s48, 62\n"
    "s_cselect_b32 s48, 1, 0\n"
    "s_mov_b32 s60, 0\n"
    "v_cmp_gt_u32 vcc, s48, v224\n"
    "s_nop 4\n"
    "s_cbranch_vccz hpk_xf0_ok_%=\n"
    "hpk_xf0_poll_%=:\n"
    "buffer_load_dword v224, v225, s[40:43], s58 offen sc1\n"
    "s_waitcnt vmcnt(0)\n"
    "v_cmp_gt_u32 vcc, s48, v224\n"
    "s_nop 4\n"
    "s_cbranch_vccz hpk_xf0_ok_%=\n"
    "s_sleep 2\n"
    "s_add_u32 s60, s60, 1\n"
    "s_cmp_gt_u32 s60, 65536\n"
    "s_cbranch_scc1 hp_abort_%=\n"
    "s_and_b32 s59, s60, 63\n"
    "s_cbranch_scc1 hpk_xf0_poll_%=\n"
    "v_mov_b32 v4, 1024\n"
    "buffer_load_dword v4, v4, s[40:43], s58 offen sc1\n"
    "s_waitcnt vmcnt(0)\n"
    "v_readfirstlane_b32 s59, v4\n"
    "s_nop 0\n"
    "s_cmp_lg_u32 s59, 0\n"
    "s_nop 2\n"
    "s_cbranch_scc1 hp_end_%=\n"
    "s_branch hpk_xf0_poll_%=\n"
    "hpk_xf0_ok_%=:\n"
    "v_add_u32 v225, 128, v225\n"
    "ds_read_b128 v[96:99], v1 offset:17408\n"
    "s_waitcnt lgkmcnt(4)\n"
    "v_mfma_f32_16x16x32_bf16 v[16:19], a[60:63], v[80:83], v[16:19]\n"
    "s_add_u32 m0, s61, 0\n"
    "s_add_u32 s51, s50, 0\n"
    "buffer_load_dwordx4 v0, s[40:43], s51 offen offset:0 sc1 lds\n"
    "v_mfma_f32_16x16x32_bf16 v[24:27], a[140:143], v[80:83], v[24:27]\n"
    "v_mfma_f32_16x16x32_bf16 v[32:35], a[220:223], v[80:83], v[32:35]\n"
    "ds_read_b128 v[80:83], v1 offset:37888\n"
    "s_waitcnt lgkmcnt(4)\n"
    "v_mfma_f32_16x16x32_bf16 v[20:23], a[60:63], v[84:87], v[20:23]\n"
    "buffer_load_dwordx4 v0, s[40:43], s51 offen offset:1024 sc1 lds\n"
    "v_mfma_f32_16x16x32_bf16 v[28:31], a[140:143], v[84:87], v[28:31]\n"
    "v_mfma_f32_16x16x32_bf16 v[36:39], a[220:223], v[84:87], v[36:39]\n"
    "ds_read_b128 v[84:87], v1 offset:18432\n"
    "s_waitcnt lgkmcnt(4)\n"
    "v_mfma_f32_16x16x32_bf16 v[16:19], a[64:67], v[88:91], v[16:19]\n"
    "buffer_load_dwordx4 v0, s[40:43], s51 offen offset:2048 sc1 lds\n"
    "v_mfma_f32_16x16x32_bf16 v[24:27], a[144:147], v[88:91], v[24:27]\n"
    "v_mfma_f32_16x16x32_bf16 v[32:35], a[224:227], v[88:91], v[32:35]\n"
    "ds_read_b128 v[88:91], v1 offset:38912\n"
    "s_waitcnt lgkmcnt(4)\n"
    "v_mfma_f32_16x16x32_bf16 v[20:23], a[64:67], v[92:95], v[20:23]\n"
    "buffer_load_dwordx4 v0, s[40:43], s51 offen offset:3072 sc1 lds\n"
    "v_mfma_f32_16x16x32_bf16 v[28:31], a[144:147], v[92:95], v[28:31]\n"
    "v_mfma_f32_16x16x32_bf16 v[36:39], a[224:227], v[92:95], v[36:39]\n"
    "ds_read_b128 v[92:95], v1 offset:19456\n"
    "s_waitcnt lgkmcnt(4)\n"
    "v_mfma_f32_16x16x32_bf16 v[16:19], a[68:71], v[96:99], v[16:19]\n"
    "s_add_u32 m0, s61, 4096\n"
    "s_add_u32 s51, s50, 4096\n"
    "buffer_load_dwordx4 v0, s[40:43], s51 offen offset:0 sc1 lds\n"
    "v_mfma_f32_16x16x32_bf16 v[24:27], a[148:151], v[96:99], v[24:27]\n"
    "v_mfma_f32_16x16x32_bf16 v[32:35], a[228:231], v[96:99], v[32:35]\n"
    "ds_read_b128 v[96:99], v1 offset:39936\n"
    "s_waitcnt lgkmcnt(4)\n"
    "v_mfma_f32_16x16x32_bf16 v[20:23], a[68:71], v[80:83], v[20:23]\n"
    "buffer_load_dwordx4 v0, s[40:43], s51 offen offset:1024 sc1 lds\n"
    "v_mfma_f32_16x16x32_bf16 v[28:31], a[148:151], v[80:83], v[28:31]\n"
    "v_mfma_f32_16x16x32_bf16 v[36:39], a[228:231], v[80:83], v[36:39]\n"
    "s_add_u32 s64, s64, 40960\n"
    "s_cmp_ge_u32 s64, 122880\n"
    "s_cselect_b32 s64, 0, s64\n"
    "v_add_u32 v1, s64, v0\n"
    "ds_read_b128 v[80:83], v1 offset:0\n"
    "s_waitcnt lgkmcnt(4)\n"
    "v_mfma_f32_16x16x32_bf16 v[16:19], a[72:75], v[84:87], v[16:19]\n"
    "buffer_load_dwordx4 v0, s[40:43], s51 offen offset:2048 sc1 lds\n"
    "v_mfma_f32_16x16x32_bf16 v[24:27], a[152:155], v[84:87], v[24:27]\n"
    "v_mfma_f32_16x16x32_bf16 v[32:35], a[232:235], v[84:87], v[32:35]\n"
    "ds_read_b128 v[84:87], v1 offset:20480\n"
    "s_waitcnt lgkmcnt(4)\n"
    "v_mfma_f32_16x16x32_bf16 v[20:23], a[72:75], v[88:91], v[20:23]\n"
    "buffer_load_dwordx4 v0, s[40:43], s51 offen offset:3072 sc1 lds\n"
    "v_mfma_f32_16x16x32_bf16 v[28:31], a[152:155], v[88:91], v[28:31]\n"
    "v_mfma_f32_16x16x32_bf16 v[36:39], a[232:235], v[88:91], v[36:39]\n"
    "ds_read_b128 v[88:91], v1 offset:1024\n"
    "s_waitcnt lgkmcnt(4)\n"
    "v_mfma_f32_16x16x32_bf16 v[16:19], a[76:79], v[92:95], v[16:19]\n"
    "s_add_u32 m0, s61, 8192\n"
    "s_add_u32 s51, s50, 8192\n"
    "buffer_load_dwordx4 v0, s[40:43], s51 offen offset:0 sc1 lds\n"
    "v_mfma_f32_16x16x32_bf16 v[24:27], a[156:159], v[92:95], v[24:27]\n"
    "v_mfma_f32_16x16x32_bf16 v[32:35], a[236:239], v[92:95], v[32:35]\n"
    "ds_read_b128 v[92:95], v1 offset:21504\n"
    "s_waitcnt lgkmcnt(4)\n"
    "v_mfma_f32_16x16x32_bf16 v[20:23], a[76:79], v[96:99], v[20:23]\n"
    "buffer_load_dwordx4 v0, s[40:43], s51 offen offset:1024 sc1 lds\n"
    "s_add_u32 s50, s50, 40960\n"
    "s_add_u32 s61, s61, 40960\n"
    "s_cmp_ge_u32 s61, 122880\n"
    "s_cselect_b32 s51, 122880, 0\n"
    "s_sub_u32 s61, s61, s51\n"
    "v_mfma_f32_16x16x32_bf16 v[28:31], a[156:159], v[96:99], v[28:31]\n"
    "v_mfma_f32_16x16x32_bf16 v[36:39], a[236:239], v[96:99], v[36:39]\n"
    "ds_read_b128 v[96:99], v1 offset:2048\n"
    "s_mov_b32 s65, s63\n"
    "s_add_u32 s63, s63, 393216\n"
    "s_mov_b32 s52, s53\n"
    "s_add_u32 s53, s53, s54\n"
    "s_and_b32 s59, s47, 63\n"
    "s_cbranch_scc1 hpk_late_%=\n"
    "v_cvt_pk_bf16_f32 v104, v16, v17\n"
    "v_cvt_pk_bf16_f32 v105, v18, v19\n"
    "v_cvt_pk_bf16_f32 v106, v20, v21\n"
    "v_cvt_pk_bf16_f32 v107, v22, v23\n"
    "buffer_store_dwordx4 v[104:107], v0, s[40:43], s65 offen offset:0 sc1\n"
    "v_cvt_pk_bf16_f32 v108, v24, v25\n"
    "v_cvt_pk_bf16_f32 v109, v26, v27\n"
    "v_cvt_pk_bf16_f32 v110, v28, v29\n"
    "v_cvt_pk_bf16_f32 v111, v30, v31\n"
    "buffer_store_dwordx4 v[108:111], v0, s[40:43], s65 offen offset:1024 sc1\n"
    "v_cvt_pk_bf16_f32 v112, v32, v33\n"
    "v_cvt_pk_bf16_f32 v113, v34, v35\n"
    "v_cvt_pk_bf16_f32 v114, v36, v37\n"
    "v_cvt_pk_bf16_f32 v115, v38, v39\n"
    "buffer_store_dwordx4 v[112:115], v0, s[40:43], s65 offen offset:2048 sc1\n"
    "s_waitcnt vmcnt(0)\n"
    "s_barrier\n"
    "buffer_atomic_add v226, v3, s[40:43], s52 offen\n"
    "s_mov_b32 s52, 0x40000000\n"
    "s_nop 0\n"
    "hpk_late_%=:\n"
    "s_add_u32 s47, s47, 1\n"
    "s_waitcnt lgkmcnt(4)\n"
    "v_mfma_f32_16x16x32_bf16 v[40:43], a[0:3], v[80:83], 0\n"
    "v_mfma_f32_16x16x32_bf16 v[48:51], a[80:83], v[80:83], 0\n"
    "v_mfma_f32_16x16x32_bf16 v[56:59], a[160:163], v[80:83], 0\n"
    "ds_read_b128 v[80:83], v1 offset:22528\n"
    "s_waitcnt lgkmcnt(4)\n"
    "v_mfma_f32_16x16x32_bf16 v[44:47], a[0:3], v[84:87], 0\n"
    "v_mfma_f32_16x16x32_bf16 v[52:55], a[80:83], v[84:87], 0\n"
    "v_mfma_f32_16x16x32_bf16 v[60:63], a[160:163], v[84:87], 0\n"
    "ds_read_b128 v[84:87], v1 offset:3072\n"
    "s_waitcnt lgkmcnt(4)\n"
    "v_mfma_f32_16x16x32_bf16 v[40:43], a[4:7], v[88:91], v[40:43]\n"
    "v_mfma_f32_16x16x32_bf16 v[48:51], a[84:87], v[88:91], v[48:51]\n"
    "v_mfma_f32_16x16x32_bf16 v[56:59], a[164:167], v[88:91], v[56:59]\n"
    "ds_read_b128 v[88:91], v1 offset:23552\n"
    "s_waitcnt lgkmcnt(4)\n"
    "v_mfma_f32_16x16x32_bf16 v[44:47], a[4:7], v[92:95], v[44:47]\n"
    "v_cvt_pk_bf16_f32 v104, v16, v17\n"
    "v_mfma_f32_16x16x32_bf16 v[52:55], a[84:87], v[92:95], v[52:55]\n"
    "v_cvt_pk_bf16_f32 v105, v18, v19\n"
    "v_mfma_f32_16x16x32_bf16 v[60:63], a[164:167], v[92:95], v[60:63]\n"
    "ds_read_b128 v[92:95], v1 offset:4096\n"
    "s_waitcnt lgkmcnt(4)\n"
    "v_mfma_f32_16x16x32_bf16 v[40:43], a[8:11], v[96:99], v[40:43]\n"
    "v_cvt_pk_bf16_f32 v106, v20, v21\n"
    "v_mfma_f32_16x16x32_bf16 v[48:51], a[88:91], v[96:99], v[48:51]\n"
    "v_cvt_pk_bf16_f32 v107, v22, v23\n"
    "v_mfma_f32_16x16x32_bf16 v[56:59], a[168:171], v[96:99], v[56:59]\n"
    "ds_read_b128 v[96:99], v1 offset:24576\n"
    "s_waitcnt lgkmcnt(4)\n"
    "v_mfma_f32_16x16x32_bf16 v[44:47], a[8:11], v[80:83], v[44:47]\n"
    "buffer_store_dwordx4 v[104:107], v0, s[40:43], s65 offen offset:0 sc1\n"
    "v_mfma_f32_16x16x32_bf16 v[52:55], a[88:91], v[80:83], v[52:55]\n"
    "v_cvt_pk_bf16_f32 v108, v24, v25\n"
    "v_mfma_f32_16x16x32_bf16 v[60:63], a[168:171], v[80:83], v[60:63]\n"
    "ds_read_b128 v[80:83], v1 offset:5120\n"
    "s_waitcnt lgkmcnt(4)\n"
    "v_mfma_f32_16x16x32_bf16 v[40:43], a[12:15], v[84:87], v[40:43]\n"
    "v_cvt_pk_bf16_f32 v109, v26, v27\n"
    "v_mfma_f32_16x16x32_bf16 v[48:51], a[92:95], v[84:87], v[48:51]\n"
    "v_cvt_pk_bf16_f32 v110, v28, v29\n"
    "v_mfma_f32_16x16x32_bf16 v[56:59], a[172:175], v[84:87], v[56:59]\n"
    "ds_read_b128 v[84:87], v1 offset:25600\n"
    "s_waitcnt lgkmcnt(4)\n"
    "v_mfma_f32_16x16x32_bf16 v[44:47], a[12:15], v[88:91], v[44:47]\n"
    "v_cvt_pk_bf16_f32 v111, v30, v31\n"
    "v_mfma_f32_16x16x32_bf16 v[52:55], a[92:95], v[88:91], v[52:55]\n"
    "buffer_store_dwordx4 v[108:111], v0, s[40:43], s65 offen offset:1024 sc1\n"
    "v_mfma_f32_16x16x32_bf16 v[60:63], a[172:175], v[88:91], v[60:63]\n"
    "ds_read_b128 v[88:91], v1 offset:6144\n"
    "s_waitcnt lgkmcnt(4)\n"
    "v_mfma_f32_16x16x32_bf16 v[40:43], a[16:19], v[92:95], v[40:43]\n"
    "v_cvt_pk_bf16_f32 v112, v32, v33\n"
    "v_mfma_f32_16x16x32_bf16 v[48:51], a[96:99], v[92:95], v[48:51]\n"
    "v_cvt_pk_bf16_f32 v113, v34, v35\n"
    "v_mfma_f32_16x16x32_bf16 v[56:59], a[176:179], v[92:95], v[56:59]\n"
    "ds_read_b128 v[92:95], v1 offset:26624\n"
    "s_waitcnt lgkmcnt(4)\n"
    "v_mfma_f32_16x16x32_bf16 v[44:47], a[16:19], v[96:99], v[44:47]\n"
    "v_cvt_pk_bf16_f32 v114, v36, v37\n"
    "v_mfma_f32_16x16x32_bf16 v[52:55], a[96:99], v[96:99], v[52:55]\n"
    "v_cvt_pk_bf16_f32 v115, v38, v39\n"
    "v_mfma_f32_16x16x32_bf16 v[60:63], a[176:179], v[96:99], v[60:63]\n"
    "ds_read_b128 v[96:99], v1 offset:7168\n"
    "s_waitcnt lgkmcnt(4)\n"
    "v_mfma_f32_16x16x32_bf16 v[40:43], a[20:23], v[80:83], v[40:43]\n"
    "buffer_store_dwordx4 v[112:115], v0, s[40:43], s65 offen offset:2048 sc1\n"
    "v_mfma_f32_16x16x32_bf16 v[48:51], a[100:103], v[80:83], v[48:51]\n"
    "buffer_load_dword v224, v225, s[40:43], s58 offen sc1\n"
    "v_mfma_f32_16x16x32_bf16 v[56:59], a[180:183], v[80:83], v[56:59]\n"
    "ds_read_b128 v[80:83], v1 offset:27648\n"
    "s_waitcnt lgkmcnt(4)\n"
    "v_mfma_f32_16x16x32_bf16 v[44:47], a[20:23], v[84:87], v[44:47]\n"
    "v_mfma_f32_16x16x32_bf16 v[52:55], a[100:103], v[84:87], v[52:55]\n"
    "v_mfma_f32_16x16x32_bf16 v[60:63], a[180:183], v[84:87], v[60:63]\n"
    "ds_read_b128 v[84:87], v1 offset:8192\n"
    "s_waitcnt lgkmcnt(4)\n"
    "v_mfma_f32_16x16x32_bf16 v[40:43], a[24:27], v[88:91], v[40:43]\n"
    "v_mfma_f32_16x16x32_bf16 v[48:51], a[104:107], v[88:91], v[48:51]\n"
    "v_mfma_f32_16x16x32_bf16 v[56:59], a[184:187], v[88:91], v[56:59]\n"
    "ds_read_b128 v[88:91], v1 offset:28672\n"
    "s_waitcnt lgkmcnt(4)\n"
    "v_mfma_f32_16x16x32_bf16 v[44:47], a[24:27], v[92:95], v[44:47]\n"
    "v_mfma_f32_16x16x32_bf16 v[52:55], a[104:107], v[92:95], v[52:55]\n"
    "v_mfma_f32_16x16x32_bf16 v[60:63], a[184:187], v[92:95], v[60:63]\n"
    "ds_read_b128 v[92:95], v1 offset:9216\n"
    "s_waitcnt lgkmcnt(4)\n"
    "v_mfma_f32_16x16x32_bf16 v[40:43], a[28:31], v[96:99], v[40:43]\n"
    "v_mfma_f32_16x16x32_bf16 v[48:51], a[108:111], v[96:99], v[48:51]\n"
    "v_mfma_f32_16x16x32_bf16 v[56:59], a[188:191], v[96:99], v[56:59]\n"
    "ds_read_b128 v[96:99], v1 offset:29696\n"
    "s_waitcnt lgkmcnt(4)\n"
    "v_mfma_f32_16x16x32_bf16 v[44:47], a[28:31], v[80:83], v[44:47]\n"
    "v_mfma_f32_16x16x32_bf16 v[52:55], a[108:111], v[80:83], v[52:55]\n"
    "v_mfma_f32_16x16x32_bf16 v[60:63], a[188:191], v[80:83], v[60:63]\n"
    "ds_read_b128 v[80:83], v1 offset:10240\n"
    "s_waitcnt lgkmcnt(4)\n"
    "v_mfma_f32_16x16x32_bf16 v[40:43], a[32:35], v[84:87], v[40:43]\n"
    "v_mfma_f32_16x16x32_bf16 v[48:51], a[112:115], v[84:87], v[48:51]\n"
    "v_mfma_f32_16x16x32_bf16 v[56:59], a[192:195], v[84:87], v[56:59]\n"
    "ds_read_b128 v[84:87], v1 offset:30720\n"
    "s_waitcnt lgkmcnt(4)\n"
    "v_mfma_f32_16x16x32_bf16 v[44:47], a[32:35], v[88:91], v[44:47]\n"
    "v_mfma_f32_16x16x32_bf16 v[52:55], a[112:115], v[88:91], v[52:55]\n"
    "v_mfma_f32_16x16x32_bf16 v[60:63], a[192:195], v[88:91], v[60:63]\n"
    "ds_read_b128 v[88:91], v1 offset:11264\n"
    "s_waitcnt lgkmcnt(4)\n"
    "v_mfma_f32_16x16x32_bf16 v[40:43], a[36:39], v[92:95], v[40:43]\n"
    "v_mfma_f32_16x16x32_bf16 v[48:51], a[116:119], v[92:95], v[48:51]\n"
    "v_mfma_f32_16x16x32_bf16 v[56:59], a[196:199], v[92:95], v[56:59]\n"
    "ds_read_b128 v[92:95], v1 offset:31744\n"
    "s_waitcnt lgkmcnt(4)\n"
    "v_mfma_f32_16x16x32_bf16 v[44:47], a[36:39], v[96:99], v[44:47]\n"
    "v_mfma_f32_16x16x32_bf16 v[52:55], a[116:119], v[96:99], v[52:55]\n"
    "v_mfma_f32_16x16x32_bf16 v[60:63], a[196:199], v[96:99], v[60:63]\n"
    "ds_read_b128 v[96:99], v1 offset:12288\n"
    "s_waitcnt lgkmcnt(4)\n"
    "v_mfma_f32_16x16x32_bf16 v[40:43], a[40:43], v[80:83], v[40:43]\n"
    "v_mfma_f32_16x16x32_bf16 v[48:51], a[120:123], v[80:83], v[48:51]\n"
    "v_mfma_f32_16x16x32_bf16 v[56:59], a[200:203], v[80:83], v[56:59]\n"
    "ds_read_b128 v[80:83], v1 offset:32768\n"
    "s_waitcnt lgkmcnt(4)\n"
    "v_mfma_f32_16x16x32_bf16 v[44:47], a[40:43], v[84:87], v[44:47]\n"
    "v_mfma_f32_16x16x32_bf16 v[52:55], a[120:123], v[84:87], v[52:55]\n"
    "v_mfma_f32_16x16x32_bf16 v[60:63], a[200:203], v[84:87], v[60:63]\n"
    "ds_read_b128 v[84:87], v1 offset:13312\n"
    "s_waitcnt lgkmcnt(4)\n"
    "v_mfma_f32_16x16x32_bf16 v[40:43], a[44:47], v[88:91], v[40:43]\n"
    "v_mfma_f32_16x16x32_bf16 v[48:51], a[124:127], v[88:91], v[48:51]\n"
    "v_mfma_f32_16x16x32_bf16 v[56:59], a[204:207], v[88:91], v[56:59]\n"
    "ds_read_b128 v[88:91], v1 offset:33792\n"
    "s_waitcnt lgkmcnt(4)\n"
    "v_mfma_f32_16x16x32_bf16 v[44:47], a[44:47], v[92:95], v[44:47]\n"
    "v_mfma_f32_16x16x32_bf16 v[52:55], a[124:127], v[92:95], v[52:55]\n"
    "v_mfma_f32_16x16x32_bf16 v[60:63], a[204:207], v[92:95], v[60:63]\n"
    "ds_read_b128 v[92:95], v1 offset:14336\n"
    "s_waitcnt lgkmcnt(4)\n"
    "v_mfma_f32_16x16x32_bf16 v[40:43], a[48:51], v[96:99], v[40:43]\n"
    "v_mfma_f32_16x16x32_bf16 v[48:51], a[128:131], v[96:99], v[48:51]\n"
    "v_mfma_f32_16x16x32_bf16 v[56:59], a[208:211], v[96:99], v[56:59]\n"
    "ds_read_b128 v[96:99], v1 offset:34816\n"
    "s_waitcnt lgkmcnt(4)\n"
    "v_mfma_f32_16x16x32_bf16 v[44:47], a[48:51], v[80:83], v[44:47]\n"
    "v_mfma_f32_16x16x32_bf16 v[52:55], a[128:131], v[80:83], v[52:55]\n"
    "v_mfma_f32_16x16x32_bf16 v[60:63], a[208:211], v[80:83], v[60:63]\n"
    "ds_read_b128 v[80:83], v1 offset:15360\n"
    "s_waitcnt lgkmcnt(4)\n"
    "v_mfma_f32_16x16x32_bf16 v[40:43], a[52:55], v[84:87], v[40:43]\n"
    "v_mfma_f32_16x16x32_bf16 v[48:51], a[132:135], v[84:87], v[48:51]\n"
    "v_mfma_f32_16x16x32_bf16 v[56:59], a[212:215], v[84:87], v[56:59]\n"
    "ds_read_b128 v[84:87], v1 offset:35840\n"
    "s_waitcnt lgkmcnt(4)\n"
    "v_mfma_f32_16x16x32_bf16 v[44:47], a[52:55], v[88:91], v[44:47]\n"
    "v_mfma_f32_16x16x32_bf16 v[52:55], a[132:135], v[88:91], v[52:55]\n"
    "v_mfma_f32_16x16x32_bf16 v[60:63], a[212:215], v[88:91], v[60:63]\n"
    "ds_read_b128 v[88:91], v1 offset:16384\n"
    "s_waitcnt lgkmcnt(4)\n"
    "v_mfma_f32_16x16x32_bf16 v[40:43], a[56:59], v[92:95], v[40:43]\n"
    "v_mfma_f32_16x16x32_bf16 v[48:51], a[136:139], v[92:95], v[48:51]\n"
    "v_mfma_f32_16x16x32_bf16 v[56:59], a[216:219], v[92:95], v[56:59]\n"
    "ds_read_b128 v[92:95], v1 offset:36864\n"
    "s_waitcnt lgkmcnt(4)\n"
    "v_mfma_f32_16x16x32_bf16 v[44:47], a[56:59], v[96:99], v[44:47]\n"
    "v_mfma_f32_16x16x32_bf16 v[52:55], a[136:139], v[96:99], v[52:55]\n"
    "v_mfma_f32_16x16x32_bf16 v[60:63], a[216:219], v[96:99], v[60:63]\n"
    "s_waitcnt vmcnt(0)\n"
    "s_barrier\n"
    "buffer_atomic_add v226, v3, s[40:43], s52 offen\n"
    "s_and_b32 s48, s47, 63\n"
    "s_cmp_lt_u32 s48, 62\n"
    "s_cselect_b32 s48, 1, 0\n"
    "s_mov_b32 s60, 0\n"
    "v_cmp_gt_u32 vcc, s48, v224\n"
    "s_nop 4\n"
    "s_cbranch_vccz hpk_xf1_ok_%=\n"
    "hpk_xf1_poll_%=:\n"
    "buffer_load_dword v224, v225, s[40:43], s58 offen sc1\n"
    "s_waitcnt vmcnt(0)\n"
    "v_cmp_gt_u32 vcc, s48, v224\n"
    "s_nop 4\n"
    "s_cbranch_vccz hpk_xf1_ok_%=\n"
    "s_sleep 2\n"
    "s_add_u32 s60, s60, 1\n"
    "s_cmp_gt_u32 s60, 65536\n"
    "s_cbranch_scc1 hp_abort_%=\n"
    "s_and_b32 s59, s60, 63\n"
    "s_cbranch_scc1 hpk_xf1_poll_%=\n"
    "v_mov_b32 v4, 1024\n"
    "buffer_load_dword v4, v4, s[40:43], s58 offen sc1\n"
    "s_waitcnt vmcnt(0)\n"
    "v_readfirstlane_b32 s59, v4\n"
    "s_nop 0\n"
    "s_cmp_lg_u32 s59, 0\n"
    "s_nop 2\n"
    "s_cbranch_scc1 hp_end_%=\n"
    "s_branch hpk_xf1_poll_%=\n"
    "hpk_xf1_ok_%=:\n"
    "v_add_u32 v225, 128, v225\n"
    "ds_read_b128 v[96:99], v1 offset:17408\n"
    "s_waitcnt lgkmcnt(4)\n"
    "v_mfma_f32_16x16x32_bf16 v[40:43], a[60:63], v[80:83], v[40:43]\n"
    "s_add_u32 m0, s61, 0\n"
    "s_add_u32 s51, s50, 0\n"
    "buffer_load_dwordx4 v0, s[40:43], s51 offen offset:0 sc1 lds\n"
    "v_mfma_f32_16x16x32_bf16 v[48:51], a[140:143], v[80:83], v[48:51]\n"
    "v_mfma_f32_16x16x32_bf16 v[56:59], a[220:223], v[80:83], v[56:59]\n"
    "ds_read_b128 v[80:83], v1 offset:37888\n"
    "s_waitcnt lgkmcnt(4)\n"
    "v_mfma_f32_16x16x32_bf16 v[44:47], a[60:63], v[84:87], v[44:47]\n"
    "buffer_load_dwordx4 v0, s[40:43], s51 offen offset:1024 sc1 lds\n"
    "v_mfma_f32_16x16x32_bf16 v[52:55], a[140:143], v[84:87], v[52:55]\n"
    "v_mfma_f32_16x16x32_bf16 v[60:63], a[220:223], v[84:87], v[60:63]\n"
    "ds_read_b128 v[84:87], v1 offset:18432\n"
    "s_waitcnt lgkmcnt(4)\n"
    "v_mfma_f32_16x16x32_bf16 v[40:43], a[64:67], v[88:91], v[40:43]\n"
    "buffer_load_dwordx4 v0, s[40:43], s51 offen offset:2048 sc1 lds\n"
    "v_mfma_f32_16x16x32_bf16 v[48:51], a[144:147], v[88:91], v[48:51]\n"
    "v_mfma_f32_16x16x32_bf16 v[56:59], a[224:227], v[88:91], v[56:59]\n"
    "ds_read_b128 v[88:91], v1 offset:38912\n"
    "s_waitcnt lgkmcnt(4)\n"
    "v_mfma_f32_16x16x32_bf16 v[44:47], a[64:67], v[92:95], v[44:47]\n"
    "buffer_load_dwordx4 v0, s[40:43], s51 offen offset:3072 sc1 lds\n"
    "v_mfma_f32_16x16x32_bf16 v[52:55], a[144:147], v[92:95], v[52:55]\n"
    "v_mfma_f32_16x16x32_bf16 v[60:63], a[224:227], v[92:95], v[60:63]\n"
    "ds_read_b128 v[92:95], v1 offset:19456\n"
    "s_waitcnt lgkmcnt(4)\n"
    "v_mfma_f32_16x16x32_bf16 v[40:43], a[68:71], v[96:99], v[40:43]\n"
    "s_add_u32 m0, s61, 4096\n"
    "s_add_u32 s51, s50, 4096\n"
    "buffer_load_dwordx4 v0, s[40:43], s51 offen offset:0 sc1 lds\n"
    "v_mfma_f32_16x16x32_bf16 v[48:51], a[148:151], v[96:99], v[48:51]\n"
    "v_mfma_f32_16x16x32_bf16 v[56:59], a[228:231], v[96:99], v[56:59]\n"
    "ds_read_b128 v[96:99], v1 offset:39936\n"
    "s_waitcnt lgkmcnt(4)\n"
    "v_mfma_f32_16x16x32_bf16 v[44:47], a[68:71], v[80:83], v[44:47]\n"
    "buffer_load_dwordx4 v0, s[40:43], s51 offen offset:1024 sc1 lds\n"
    "v_mfma_f32_16x16x32_bf16 v[52:55], a[148:151], v[80:83], v[52:55]\n"
    "v_mfma_f32_16x16x32_bf16 v[60:63], a[228:231], v[80:83], v[60:63]\n"
    "s_add_u32 s64, s64, 40960\n"
    "s_cmp_ge_u32 s64, 122880\n"
    "s_cselect_b32 s64, 0, s64\n"
    "v_add_u32 v1, s64, v0\n"
    "ds_read_b128 v[80:83], v1 offset:0\n"
    "s_waitcnt lgkmcnt(4)\n"
    "v_mfma_f32_16x16x32_bf16 v[40:43], a[72:75], v[84:87], v[40:43]\n"
    "buffer_load_dwordx4 v0, s[40:43], s51 offen offset:2048 sc1 lds\n"
    "v_mfma_f32_16x16x32_bf16 v[48:51], a[152:155], v[84:87], v[48:51]\n"
    "v_mfma_f32_16x16x32_bf16 v[56:59], a[232:235], v[84:87], v[56:59]\n"
    "ds_read_b128 v[84:87], v1 offset:20480\n"
    "s_waitcnt lgkmcnt(4)\n"
    "v_mfma_f32_16x16x32_bf16 v[44:47], a[72:75], v[88:91], v[44:47]\n"
    "buffer_load_dwordx4 v0, s[40:43], s51 offen offset:3072 sc1 lds\n"
    "v_mfma_f32_16x16x32_bf16 v[52:55], a[152:155], v[88:91], v[52:55]\n"
    "v_mfma_f32_16x16x32_bf16 v[60:63], a[232:235], v[88:91], v[60:63]\n"
    "ds_read_b128 v[88:91], v1 offset:1024\n"
    "s_waitcnt lgkmcnt(4)\n"
    "v_mfma_f32_16x16x32_bf16 v[40:43], a[76:79], v[92:95], v[40:43]\n"
    "s_add_u32 m0, s61, 8192\n"
    "s_add_u32 s51, s50, 8192\n"
    "buffer_load_dwordx4 v0, s[40:43], s51 offen offset:0 sc1 lds\n"
    "v_mfma_f32_16x16x32_bf16 v[48:51], a[156:159], v[92:95], v[48:51]\n"
    "v_mfma_f32_16x16x32_bf16 v[56:59], a[236:239], v[92:95], v[56:59]\n"
    "ds_read_b128 v[92:95], v1 offset:21504\n"
    "s_waitcnt lgkmcnt(4)\n"
    "v_mfma_f32_16x16x32_bf16 v[44:47], a[76:79], v[96:99], v[44:47]\n"
    "buffer_load_dwordx4 v0, s[40:43], s51 offen offset:1024 sc1 lds\n"
    "s_add_u32 s50, s50, 40960\n"
    "s_add_u32 s61, s61, 40960\n"
    "s_cmp_ge_u32 s61, 122880\n"
    "s_cselect_b32 s51, 122880, 0\n"
    "s_sub_u32 s61, s61, s51\n"
    "v_mfma_f32_16x16x32_bf16 v[52:55], a[156:159], v[96:99], v[52:55]\n"
    "v_mfma_f32_16x16x32_bf16 v[60:63], a[236:239], v[96:99], v[60:63]\n"
    "ds_read_b128 v[96:99], v1 offset:2048\n"
    "s_mov_b32 s65, s63\n"
    "s_add_u32 s63, s63, 393216\n"
    "s_mov_b32 s52, s53\n"
    "s_add_u32 s53, s53, s54\n"
    "s_add_u32 s47, s47, 1\n"
    "s_and_b32 s59, s47, 63\n"
    "s_cbranch_scc1 hpk_loop_%=\n"
    "s_waitcnt lgkmcnt(0)\n"
    "v_cvt_pk_bf16_f32 v104, v40, v41\n"
    "v_cvt_pk_bf16_f32 v105, v42, v43\n"
    "v_cvt_pk_bf16_f32 v106, v44, v45\n"
    "v_cvt_pk_bf16_f32 v107, v46, v47\n"
    "buffer_store_dwordx4 v[104:107], v0, s[40:43], s65 offen offset:0 sc1\n"
    "v_cvt_pk_bf16_f32 v108, v48, v49\n"
    "v_cvt_pk_bf16_f32 v109, v50, v51\n"
    "v_cvt_pk_bf16_f32 v110, v52, v53\n"
    "v_cvt_pk_bf16_f32 v111, v54, v55\n"
    "buffer_store_dwordx4 v[108:111], v0, s[40:43], s65 offen offset:1024 sc1\n"
    "v_cvt_pk_bf16_f32 v112, v56, v57\n"
    "v_cvt_pk_bf16_f32 v113, v58, v59\n"
    "v_cvt_pk_bf16_f32 v114, v60, v61\n"
    "v_cvt_pk_bf16_f32 v115, v62, v63\n"
    "buffer_store_dwordx4 v[112:115], v0, s[40:43], s65 offen offset:2048 sc1\n"
    "s_waitcnt vmcnt(0)\n"
    "s_waitcnt lgkmcnt(0)\n"
    "s_barrier\n"
    "buffer_atomic_add v226, v3, s[40:43], s52 offen\n"
    "s_nop 1\n"
    "s_branch hp_ret_%=\n"
    "hp_roleq_%=:\n"
    "s_lshl_b32 s48, s46, 5\n"
    "s_add_u32 s48, s48, s44\n"
    "s_mul_i32 s48, s48, 12\n"
    "s_mul_i32 s49, s45, 3\n"
    "s_add_u32 s48, s48, s49\n"
    "s_mul_i32 s48, s48, 20480\n"
    "s_add_u32 s48, s48, 2523136\n"
    "s_mul_i32 s62, s45, 10240\n"
    "s_mul_i32 s50, s47, 40960\n"
    "s_add_u32 s50, s50, s62\n"
    "s_add_u32 s50, s50, 60194816\n"
    "s_cmp_lg_u32 s47, 0\n"
    "s_cbranch_scc1 hpq_havew_%=\n"
    "buffer_load_dwordx4 a[0:3], v0, s[40:43], s48 offen\n"
    "buffer_load_dwordx4 a[4:7], v0, s[40:43], s48 offen offset:1024\n"
    "buffer_load_dwordx4 a[8:11], v0, s[40:43], s48 offen offset:2048\n"
    "buffer_load_dwordx4 a[12:15], v0, s[40:43], s48 offen offset:3072\n"
    "s_add_u32 s48, s48, 4096\n"
    "buffer_load_dwordx4 a[16:19], v0, s[40:43], s48 offen\n"
    "buffer_load_dwordx4 a[20:23], v0, s[40:43], s48 offen offset:1024\n"
    "buffer_load_dwordx4 a[24:27], v0, s[40:43], s48 offen offset:2048\n"
    "buffer_load_dwordx4 a[28:31], v0, s[40:43], s48 offen offset:3072\n"
    "s_add_u32 s48, s48, 4096\n"
    "buffer_load_dwordx4 a[32:35], v0, s[40:43], s48 offen\n"
    "buffer_load_dwordx4 a[36:39], v0, s[40:43], s48 offen offset:1024\n"
    "buffer_load_dwordx4 a[40:43], v0, s[40:43], s48 offen offset:2048\n"
    "buffer_load_dwordx4 a[44:47], v0, s[40:43], s48 offen offset:3072\n"
    "s_add_u32 s48, s48, 4096\n"
    "buffer_load_dwordx4 a[48:51], v0, s[40:43], s48 offen\n"
    "buffer_load_dwordx4 a[52:55], v0, s[40:43], s48 offen offset:1024\n"
    "buffer_load_dwordx4 a[56:59], v0, s[40:43], s48 offen offset:2048\n"
    "buffer_load_dwordx4 a[60:63], v0, s[40:43], s48 offen offset:3072\n"
    "s_add_u32 s48, s48, 4096\n"
    "buffer_load_dwordx4 a[64:67], v0, s[40:43], s48 offen\n"
    "buffer_load_dwordx4 a[68:71], v0, s[40:43], s48 offen offset:1024\n"
    "buffer_load_dwordx4 a[72:75], v0, s[40:43], s48 offen offset:2048\n"
    "buffer_load_dwordx4 a[76:79], v0, s[40:43], s48 offen offset:3072\n"
    "s_add_u32 s48, s48, 4096\n"
    "buffer_load_dwordx4 a[80:83], v0, s[40:43], s48 offen\n"
    "buffer_load_dwordx4 a[84:87], v0, s[40:43], s48 offen offset:1024\n"
    "buffer_load_dwordx4 a[88:91], v0, s[40:43], s48 offen offset:2048\n"
    "buffer_load_dwordx4 a[92:95], v0, s[40:43], s48 offen offset:3072\n"
    "s_add_u32 s48, s48, 4096\n"
    "buffer_load_dwordx4 a[96:99], v0, s[40:43], s48 offen\n"
    "buffer_load_dwordx4 a[100:103], v0, s[40:43], s48 offen offset:1024\n"
    "buffer_load_dwordx4 a[104:107], v0, s[40:43], s48 offen offset:2048\n"
    "buffer_load_dwordx4 a[108:111], v0, s[40:43], s48 offen offset:3072\n"
    "s_add_u32 s48, s48, 4096\n"
    "buffer_load_dwordx4 a[112:115], v0, s[40:43], s48 offen\n"
    "buffer_load_dwordx4 a[116:119], v0, s[40:43], s48 offen offset:1024\n"
    "buffer_load_dwordx4 a[120:123], v0, s[40:43], s48 offen offset:2048\n"
    "buffer_load_dwordx4 a[124:127], v0, s[40:43], s48 offen offset:3072\n"
    "s_add_u32 s48, s48, 4096\n"
    "buffer_load_dwordx4 a[128:131], v0, s[40:43], s48 offen\n"
    "buffer_load_dwordx4 a[132:135], v0, s[40:43], s48 offen offset:1024\n"
    "buffer_load_dwordx4 a[136:139], v0, s[40:43], s48 offen offset:2048\n"
    "buffer_load_dwordx4 a[140:143], v0, s[40:43], s48 offen offset:3072\n"
    "s_add_u32 s48, s48, 4096\n"
    "buffer_load_dwordx4 a[144:147], v0, s[40:43], s48 offen\n"
    "buffer_load_dwordx4 a[148:151], v0, s[40:43], s48 offen offset:1024\n"
    "buffer_load_dwordx4 a[152:155], v0, s[40:43], s48 offen offset:2048\n"
    "buffer_load_dwordx4 a[156:159], v0, s[40:43], s48 offen offset:3072\n"
    "s_add_u32 s48, s48, 4096\n"
    "buffer_load_dwordx4 a[160:163], v0, s[40:43], s48 offen\n"
    "buffer_load_dwordx4 a[164:167], v0, s[40:43], s48 offen offset:1024\n"
    "buffer_load_dwordx4 a[168:171], v0, s[40:43], s48 offen offset:2048\n"
    "buffer_load_dwordx4 a[172:175], v0, s[40:43], s48 offen offset:3072\n"
    "s_add_u32 s48, s48, 4096\n"
    "buffer_load_dwordx4 a[176:179], v0, s[40:43], s48 offen\n"
    "buffer_load_dwordx4 a[180:183], v0, s[40:43], s48 offen offset:1024\n"
    "buffer_load_dwordx4 a[184:187], v0, s[40:43], s48 offen offset:2048\n"
    "buffer_load_dwordx4 a[188:191], v0, s[40:43], s48 offen offset:3072\n"
    "s_add_u32 s48, s48, 4096\n"
    "buffer_load_dwordx4 a[192:195], v0, s[40:43], s48 offen\n"
    "buffer_load_dwordx4 a[196:199], v0, s[40:43], s48 offen offset:1024\n"
    "buffer_load_dwordx4 a[200:203], v0, s[40:43], s48 offen offset:2048\n"
    "buffer_load_dwordx4 a[204:207], v0, s[40:43], s48 offen offset:3072\n"
    "s_add_u32 s48, s48, 4096\n"
    "buffer_load_dwordx4 a[208:211], v0, s[40:43], s48 offen\n"
    "buffer_load_dwordx4 a[212:215], v0, s[40:43], s48 offen offset:1024\n"
    "buffer_load_dwordx4 a[216:219], v0, s[40:43], s48 offen offset:2048\n"
    "buffer_load_dwordx4 a[220:223], v0, s[40:43], s48 offen offset:3072\n"
    "s_add_u32 s48, s48, 4096\n"
    "buffer_load_dwordx4 a[224:227], v0, s[40:43], s48 offen\n"
    "buffer_load_dwordx4 a[228:231], v0, s[40:43], s48 offen offset:1024\n"
    "buffer_load_dwordx4 a[232:235], v0, s[40:43], s48 offen offset:2048\n"
    "buffer_load_dwordx4 a[236:239], v0, s[40:43], s48 offen offset:3072\n"
    "hpq_havew_%=:\n"
    "s_mov_b32 s61, s62\n"
    "s_lshl_b32 s48, s47, 7\n"
    "s_add_u32 s48, s48, 43520\n"
    "v_mov_b32 v225, s48\n"
    "s_mov_b32 s48, 1\n"
    "v_mov_b32 v224, 0\n"
    "s_mov_b32 s60, 0\n"
    "v_cmp_gt_u32 vcc, s48, v224\n"
    "s_nop 4\n"
    "s_cbranch_vccz hpq_xfp0_ok_%=\n"
    "hpq_xfp0_poll_%=:\n"
    "buffer_load_dword v224, v225, s[40:43], s58 offen sc1\n"
    "s_waitcnt vmcnt(0)\n"
    "v_cmp_gt_u32 vcc, s48, v224\n"
    "s_nop 4\n"
    "s_cbranch_vccz hpq_xfp0_ok_%=\n"
    "s_sleep 2\n"
    "s_add_u32 s60, s60, 1\n"
    "s_cmp_gt_u32 s60, 65536\n"
    "s_cbranch_scc1 hp_abort_%=\n"
    "s_and_b32 s59, s60, 63\n"
    "s_cbranch_scc1 hpq_xfp0_poll_%=\n"
    "v_mov_b32 v4, 1024\n"
    "buffer_load_dword v4, v4, s[40:43], s58 offen sc1\n"
    "s_waitcnt vmcnt(0)\n"
    "v_readfirstlane_b32 s59, v4\n"
    "s_nop 0\n"
    "s_cmp_lg_u32 s59, 0\n"
    "s_nop 2\n"
    "s_cbranch_scc1 hp_end_%=\n"
    "s_branch hpq_xfp0_poll_%=\n"
    "hpq_xfp0_ok_%=:\n"
    "v_add_u32 v225, 128, v225\n"
    "s_add_u32 m0, s61, 0\n"
    "s_add_u32 s51, s50, 0\n"
    "buffer_load_dwordx4 v0, s[40:43], s51 offen offset:0 sc1 lds\n"
    "buffer_load_dwordx4 v0, s[40:43], s51 offen offset:1024 sc1 lds\n"
    "buffer_load_dwordx4 v0, s[40:43], s51 offen offset:2048 sc1 lds\n"
    "buffer_load_dwordx4 v0, s[40:43], s51 offen offset:3072 sc1 lds\n"
    "s_add_u32 m0, s61, 4096\n"
    "s_add_u32 s51, s50, 4096\n"
    "buffer_load_dwordx4 v0, s[40:43], s51 offen offset:0 sc1 lds\n"
    "buffer_load_dwordx4 v0, s[40:43], s51 offen offset:1024 sc1 lds\n"
    "buffer_load_dwordx4 v0, s[40:43], s51 offen offset:2048 sc1 lds\n"
    "buffer_load_dwordx4 v0, s[40:43], s51 offen offset:3072 sc1 lds\n"
    "s_add_u32 m0, s61, 8192\n"
    "s_add_u32 s51, s50, 8192\n"
    "buffer_load_dwordx4 v0, s[40:43], s51 offen offset:0 sc1 lds\n"
    "buffer_load_dwordx4 v0, s[40:43], s51 offen offset:1024 sc1 lds\n"
    "s_add_u32 s50, s50, 40960\n"
    "s_add_u32 s61, s61, 40960\n"
    "s_cmp_ge_u32 s61, 122880\n"
    "s_cselect_b32 s51, 122880, 0\n"
    "s_sub_u32 s61, s61, s51\n"
    "v_mov_b32 v224, 0\n"
    "s_mov_b32 s60, 0\n"
    "v_cmp_gt_u32 vcc, s48, v224\n"
    "s_nop 4\n"
    "s_cbranch_vccz hpq_xfp1_ok_%=\n"
    "hpq_xfp1_poll_%=:\n"
    "buffer_load_dword v224, v225, s[40:43], s58 offen sc1\n"
    "s_waitcnt vmcnt(0)\n"
    "v_cmp_gt_u32 vcc, s48, v224\n"
    "s_nop 4\n"
    "s_cbranch_vccz hpq_xfp1_ok_%=\n"
    "s_sleep 2\n"
    "s_add_u32 s60, s60, 1\n"
    "s_cmp_gt_u32 s60, 65536\n"
    "s_cbranch_scc1 hp_abort_%=\n"
    "s_and_b32 s59, s60, 63\n"
    "s_cbranch_scc1 hpq_xfp1_poll_%=\n"
    "v_mov_b32 v4, 1024\n"
    "buffer_load_dword v4, v4, s[40:43], s58 offen sc1\n"
    "s_waitcnt vmcnt(0)\n"
    "v_readfirstlane_b32 s59, v4\n"
    "s_nop 0\n"
    "s_cmp_lg_u32 s59, 0\n"
    "s_nop 2\n"
    "s_cbranch_scc1 hp_end_%=\n"
    "s_branch hpq_xfp1_poll_%=\n"
    "hpq_xfp1_ok_%=:\n"
    "v_add_u32 v225, 128, v225\n"
    "s_add_u32 m0, s61, 0\n"
    "s_add_u32 s51, s50, 0\n"
    "buffer_load_dwordx4 v0, s[40:43], s51 offen offset:0 sc1 lds\n"
    "buffer_load_dwordx4 v0, s[40:43], s51 offen offset:1024 sc1 lds\n"
    "buffer_load_dwordx4 v0, s[40:43], s51 offen offset:2048 sc1 lds\n"
    "buffer_load_dwordx4 v0, s[40:43], s51 offen offset:3072 sc1 lds\n"
    "s_add_u32 m0, s61, 4096\n"
    "s_add_u32 s51, s50, 4096\n"
    "buffer_load_dwordx4 v0, s[40:43], s51 offen offset:0 sc1 lds\n"
    "buffer_load_dwordx4 v0, s[40:43], s51 offen offset:1024 sc1 lds\n"
    "buffer_load_dwordx4 v0, s[40:43], s51 offen offset:2048 sc1 lds\n"
    "buffer_load_dwordx4 v0, s[40:43], s51 offen offset:3072 sc1 lds\n"
    "s_add_u32 m0, s61, 8192\n"
    "s_add_u32 s51, s50, 8192\n"
    "buffer_load_dwordx4 v0, s[40:43], s51 offen offset:0 sc1 lds\n"
    "buffer_load_dwordx4 v0, s[40:43], s51 offen offset:1024 sc1 lds\n"
    "s_add_u32 s50, s50, 40960\n"
    "s_add_u32 s61, s61, 40960\n"
    "s_cmp_ge_u32 s61, 122880\n"
    "s_cselect_b32 s51, 122880, 0\n"
    "s_sub_u32 s61, s61, s51\n"
    "v_mov_b32 v224, 0\n"
    "v_mov_b32 v226, 1\n"
    "v_mov_b32 v1, v0\n"
    "s_mov_b32 s64, 0\n"
    "s_lshl_b32 s48, s44, 7\n"
    "s_lshr_b32 s49, s44, 2\n"
    "s_lshl_b32 s49, s49, 7\n"
    "v_mov_b32 v3, s49\n"
    "v_cmp_eq_u32 vcc, 0, v15\n"
    "v_mov_b32 v4, 2147483632\n"
    "s_nop 0\n"
    "v_cndmask_b32 v3, v4, v3, vcc\n"
    "s_mov_b32 s52, 0x40000000\n"
    "v_lshrrev_b32 v13, 4, v15\n"
    "v_and_b32 v14, 15, v15\n"
    "s_lshl_b32 s49, s45, 11\n"
    "s_add_u32 s49, s49, 122880\n"
    "v_add_u32 v6, s49, v0\n"
    "s_lshr_b32 s49, s45, 1\n"
    "s_lshl_b32 s49, s49, 11\n"
    "s_and_b32 s53, s45, 1\n"
    "s_lshl_b32 s53, s53, 3\n"
    "s_add_u32 s49, s49, s53\n"
    "s_add_u32 s49, s49, 131072\n"
    "v_add_u32 v7, s49, v0\n"
    "s_lshl_b32 s49, s45, 10\n"
    "s_add_u32 s49, s49, 122880\n"
    "v_add_u32 v8, s49, v0\n"
    "v_mov_b32 v9, 0\n"
    "s_add_u32 s49, s48, 6144\n"
    "v_mov_b32 v10, s49\n"
    "v_and_b32 v11, 7, v15\n"
    "v_lshlrev_b32 v11, 4, v11\n"
    "v_lshl_add_u32 v11, v13, 7, v11\n"
    "s_bfe_u32 s49, s44, 0x20001\n"
    "s_lshl_b32 s49, s49, 8\n"
    "v_add_u32 v12, s49, v0\n"
    "v_cmp_gt_u32 vcc, 16, v15\n"
    "v_mov_b32 v4, 2147483632\n"
    "s_nop 0\n"
    "v_cndmask_b32 v12, v4, v12, vcc\n"
    "s_lshl_b32 s49, s45, 4\n"
    "v_add_u32 v14, s49, v14\n"
    "v_lshlrev_b32 v13, 2, v13\n"
    "v_sub_u32 v14, v14, v13\n"
    "v_mov_b32 v4, 0xff800000\n"
    "v_cmp_gt_i32 vcc, 0, v14\n"
    "s_nop 1\n"
    "v_cndmask_b32 v212, 0, v4, vcc\n"
    "v_cmp_gt_i32 vcc, 1, v14\n"
    "s_nop 1\n"
    "v_cndmask_b32 v213, 0, v4, vcc\n"
    "v_cmp_gt_i32 vcc, 2, v14\n"
    "s_nop 1\n"
    "v_cndmask_b32 v214, 0, v4, vcc\n"
    "v_cmp_gt_i32 vcc, 3, v14\n"
    "s_nop 1\n"
    "v_cndmask_b32 v215, 0, v4, vcc\n"
    "v_cmp_gt_i32 vcc, 16, v14\n"
    "s_nop 1\n"
    "v_cndmask_b32 v216, 0, v4, vcc\n"
    "v_cmp_gt_i32 vcc, 17, v14\n"
    "s_nop 1\n"
    "v_cndmask_b32 v217, 0, v4, vcc\n"
    "v_cmp_gt_i32 vcc, 18, v14\n"
    "s_nop 1\n"
    "v_cndmask_b32 v218, 0, v4, vcc\n"
    "v_cmp_gt_i32 vcc, 19, v14\n"
    "s_nop 1\n"
    "v_cndmask_b32 v219, 0, v4, vcc\n"
    "v_cmp_lt_u32 vcc, 31, v15\n"
    "s_nop 1\n"
    "v_cndmask_b32 v220, 0, v4, vcc\n"
    "v_cndmask_b32 v221, 0, v4, vcc\n"
    "v_cndmask_b32 v222, 0, v4, vcc\n"
    "v_cndmask_b32 v223, 0, v4, vcc\n"
    "s_waitcnt vmcnt(0)\n"
    "s_waitcnt lgkmcnt(0)\n"
    "s_barrier\n"
    "s_mov_b32 s65, s47\n"
    "s_sub_u32 s63, s47, 2\n"
    "s_cmp_gt_u32 s45, 1\n"
    "s_cselect_b32 s30, 0x40000000, 0\n"
    "s_mul_i32 s34, s44, 3072\n"
    "s_add_u32 s34, s34, 220463104\n"
    "s_mul_i32 s35, s63, 1536\n"
    "s_add_u32 s35, s35, 2157568\n"
    "s_lshl1_add_u32 s36, s44, s45\n"
    "s_lshl_b32 s36, s36, 10\n"
    "s_add_u32 s36, s36, 226820096\n"
    "s_lshr_b32 s37, s44, 3\n"
    "s_lshl_b32 s37, s37, 1\n"
    "s_and_b32 s48, s44, 1\n"
    "s_add_u32 s37, s37, s48\n"
    "s_lshl3_add_u32 s37, s45, s37\n"
    "s_lshl_b32 s37, s37, 10\n"
    "s_add_u32 s37, s37, 231112704\n"
    "s_mul_i32 s54, s44, 12288\n"
    "s_add_u32 s54, s54, 65044480\n"
    "s_or_b32 s54, s54, s30\n"
    "s_add_u32 s55, s54, 4096\n"
    "s_add_u32 s56, s54, 8192\n"
    "s_lshl_b32 s48, s47, 7\n"
    "s_add_u32 s48, s48, 10240\n"
    "v_mov_b32 v227, s48\n"
    "v_mov_b32 v236, 0\n"
    "s_mov_b32 s48, 32\n"
    "v_add_u32 v227, -128, v227\n"
    "v_mov_b32 v236, 32\n"
    "v_add_u32 v2, 8, v6\n"
    "ds_read_b128 v[80:83], v1 offset:0\n"
    "ds_read_b128 v[84:87], v1 offset:20480\n"
    "ds_read_b128 v[88:91], v1 offset:1024\n"
    "ds_read_b128 v[92:95], v1 offset:21504\n"
    "ds_read_b128 v[96:99], v1 offset:2048\n"
    "hpq_loop_%=:\n"
    "s_mov_b32 s29, s52\n"
    "s_waitcnt vmcnt(25)\n"
    "s_add_u32 s48, s63, 1\n"
    "s_cmp_ge_i32 s63, s65\n"
    "s_cselect_b32 s48, s48, 0\n"
    "s_cselect_b32 s49, s30, 0x40000000\n"
    "v_cmp_gt_u32 vcc, s48, v9\n"
    "s_cbranch_vccz hpq_kma0_ok_%=\n"
    "s_waitcnt vmcnt(0)\n"
    "buffer_atomic_add v226, v3, s[40:43], s29 offen\n"
    "s_mov_b32 s29, 0x40000000\n"
    "s_mov_b32 s60, 0\n"
    "v_cmp_gt_u32 vcc, s48, v9\n"
    "s_nop 4\n"
    "s_cbranch_vccz hpq_kma0_ok_%=\n"
    "hpq_kma0_poll_%=:\n"
    "buffer_load_dword v9, v10, s[40:43], s58 offen sc1\n"
    "s_waitcnt vmcnt(0)\n"
    "v_cmp_gt_u32 vcc, s48, v9\n"
    "s_nop 4\n"
    "s_cbranch_vccz hpq_kma0_ok_%=\n"
    "s_sleep 2\n"
    "s_add_u32 s60, s60, 1\n"
    "s_cmp_gt_u32 s60, 65536\n"
    "s_cbranch_scc1 hp_abort_%=\n"
    "s_and_b32 s59, s60, 63\n"
    "s_cbranch_scc1 hpq_kma0_poll_%=\n"
    "v_mov_b32 v4, 1024\n"
    "buffer_load_dword v4, v4, s[40:43], s58 offen sc1\n"
    "s_waitcnt vmcnt(0)\n"
    "v_readfirstlane_b32 s59, v4\n"
    "s_nop 0\n"
    "s_cmp_lg_u32 s59, 0\n"
    "s_nop 2\n"
    "s_cbranch_scc1 hp_end_%=\n"
    "s_branch hpq_kma0_poll_%=\n"
    "hpq_kma0_ok_%=:\n"
    "s_movk_i32 s48, 32\n"
    "s_mov_b32 s60, 0\n"
    "v_cmp_gt_u32 vcc, s48, v236\n"
    "s_nop 4\n"
    "s_cbranch_vccz hpq_kxa0_ok_%=\n"
    "hpq_kxa0_poll_%=:\n"
    "buffer_load_dword v236, v227, s[40:43], s58 offen sc1\n"
    "s_waitcnt vmcnt(0)\n"
    "v_cmp_gt_u32 vcc, s48, v236\n"
    "s_nop 4\n"
    "s_cbranch_vccz hpq_kxa0_ok_%=\n"
    "s_sleep 2\n"
    "s_add_u32 s60, s60, 1\n"
    "s_cmp_gt_u32 s60, 65536\n"
    "s_cbranch_scc1 hp_abort_%=\n"
    "s_and_b32 s59, s60, 63\n"
    "s_cbranch_scc1 hpq_kxa0_poll_%=\n"
    "v_mov_b32 v4, 1024\n"
    "buffer_load_dword v4, v4, s[40:43], s58 offen sc1\n"
    "s_waitcnt vmcnt(0)\n"
    "v_readfirstlane_b32 s59, v4\n"
    "s_nop 0\n"
    "s_cmp_lg_u32 s59, 0\n"
    "s_nop 2\n"
    "s_cbranch_scc1 hp_end_%=\n"
    "s_branch hpq_kxa0_poll_%=\n"
    "hpq_kxa0_ok_%=:\n"
    "v_add_u32 v227, 128, v227\n"
    "s_or_b32 s57, s34, s49\n"
    "s_add_u32 s34, s34, 98304\n"
    "s_or_b32 s52, s35, s49\n"
    "s_or_b32 s26, s36, s49\n"
    "s_or_b32 s27, s37, s49\n"
    "s_add_u32 s35, s35, 1536\n"
    "s_add_u32 s36, s36, 65536\n"
    "s_add_u32 s37, s37, 16384\n"
    "s_add_u32 s63, s63, 1\n"
    "s_waitcnt lgkmcnt(4)\n"
    "v_mfma_f32_16x16x32_bf16 v[16:19], a[0:3], v[80:83], 0\n"
    "buffer_load_dwordx4 v[164:167], v11, s[40:43], s57 offen sc1\n"
    "v_mfma_f32_16x16x32_bf16 v[24:27], a[80:83], v[80:83], 0\n"
    "buffer_load_dwordx4 v[168:171], v11, s[40:43], s57 offen offset:512 sc1\n"
    "v_mfma_f32_16x16x32_bf16 v[32:35], a[160:163], v[80:83], 0\n"
    "buffer_load_dwordx4 v[172:175], v11, s[40:43], s57 offen offset:1024 sc1\n"
    "ds_read_b128 v[80:83], v1 offset:22528\n"
    "s_waitcnt lgkmcnt(4)\n"
    "v_mfma_f32_16x16x32_bf16 v[20:23], a[0:3], v[84:87], 0\n"
    "buffer_load_dwordx4 v[176:179], v11, s[40:43], s57 offen offset:1536 sc1\n"
    "v_mfma_f32_16x16x32_bf16 v[28:31], a[80:83], v[84:87], 0\n"
    "buffer_load_dwordx4 v[180:183], v11, s[40:43], s57 offen offset:2048 sc1\n"
    "v_mfma_f32_16x16x32_bf16 v[36:39], a[160:163], v[84:87], 0\n"
    "buffer_load_dwordx4 v[184:187], v11, s[40:43], s57 offen offset:2560 sc1\n"
    "ds_read_b128 v[84:87], v1 offset:3072\n"
    "s_waitcnt lgkmcnt(4)\n"
    "v_mfma_f32_16x16x32_bf16 v[16:19], a[4:7], v[88:91], v[16:19]\n"
    "buffer_load_dword v236, v227, s[40:43], s58 offen sc1\n"
    "v_mfma_f32_16x16x32_bf16 v[24:27], a[84:87], v[88:91], v[24:27]\n"
    "v_mfma_f32_16x16x32_bf16 v[32:35], a[164:167], v[88:91], v[32:35]\n"
    "ds_read_b128 v[88:91], v1 offset:23552\n"
    "s_waitcnt lgkmcnt(4)\n"
    "v_mfma_f32_16x16x32_bf16 v[20:23], a[4:7], v[92:95], v[20:23]\n"
    "v_cvt_pk_bf16_f32 v104, v40, v41\n"
    "v_mfma_f32_16x16x32_bf16 v[28:31], a[84:87], v[92:95], v[28:31]\n"
    "v_cvt_pk_bf16_f32 v105, v42, v43\n"
    "v_mfma_f32_16x16x32_bf16 v[36:39], a[164:167], v[92:95], v[36:39]\n"
    "ds_read_b128 v[92:95], v1 offset:4096\n"
    "s_waitcnt lgkmcnt(4)\n"
    "v_mfma_f32_16x16x32_bf16 v[16:19], a[8:11], v[96:99], v[16:19]\n"
    "v_cvt_pk_bf16_f32 v106, v44, v45\n"
    "v_mfma_f32_16x16x32_bf16 v[24:27], a[88:91], v[96:99], v[24:27]\n"
    "v_cvt_pk_bf16_f32 v107, v46, v47\n"
    "v_mfma_f32_16x16x32_bf16 v[32:35], a[168:171], v[96:99], v[32:35]\n"
    "ds_read_b128 v[96:99], v1 offset:24576\n"
    "s_waitcnt lgkmcnt(4)\n"
    "v_mfma_f32_16x16x32_bf16 v[20:23], a[8:11], v[80:83], v[20:23]\n"
    "ds_write2st64_b64 v6, v[104:105], v[106:107] offset0:24 offset1:26\n"
    "v_mfma_f32_16x16x32_bf16 v[28:31], a[88:91], v[80:83], v[28:31]\n"
    "v_cvt_pk_bf16_f32 v108, v48, v49\n"
    "v_mfma_f32_16x16x32_bf16 v[36:39], a[168:171], v[80:83], v[36:39]\n"
    "ds_read_b128 v[80:83], v1 offset:5120\n"
    "s_waitcnt lgkmcnt(5)\n"
    "v_mfma_f32_16x16x32_bf16 v[16:19], a[12:15], v[84:87], v[16:19]\n"
    "v_cvt_pk_bf16_f32 v109, v50, v51\n"
    "v_mfma_f32_16x16x32_bf16 v[24:27], a[92:95], v[84:87], v[24:27]\n"
    "v_cvt_pk_bf16_f32 v110, v52, v53\n"
    "v_mfma_f32_16x16x32_bf16 v[32:35], a[172:175], v[84:87], v[32:35]\n"
    "ds_read_b128 v[84:87], v1 offset:25600\n"
    "s_waitcnt lgkmcnt(5)\n"
    "v_mfma_f32_16x16x32_bf16 v[20:23], a[12:15], v[88:91], v[20:23]\n"
    "v_cvt_pk_bf16_f32 v111, v54, v55\n"
    "v_mfma_f32_16x16x32_bf16 v[28:31], a[92:95], v[88:91], v[28:31]\n"
    "ds_write2st64_b64 v2, v[108:109], v[110:111] offset0:24 offset1:26\n"
    "v_mfma_f32_16x16x32_bf16 v[36:39], a[172:175], v[88:91], v[36:39]\n"
    "ds_read_b128 v[88:91], v1 offset:6144\n"
    "s_waitcnt lgkmcnt(6)\n"
    "v_mfma_f32_16x16x32_bf16 v[16:19], a[16:19], v[92:95], v[16:19]\n"
    "v_cvt_pk_bf16_f32 v112, v56, v57\n"
    "v_mfma_f32_16x16x32_bf16 v[24:27], a[96:99], v[92:95], v[24:27]\n"
    "v_cvt_pk_bf16_f32 v113, v58, v59\n"
    "v_mfma_f32_16x16x32_bf16 v[32:35], a[176:179], v[92:95], v[32:35]\n"
    "s_waitcnt vmcnt(20)\n"
    "ds_read_b128 v[92:95], v1 offset:26624\n"
    "s_waitcnt lgkmcnt(6)\n"
    "v_mfma_f32_16x16x32_bf16 v[20:23], a[16:19], v[96:99], v[20:23]\n"
    "v_cvt_pk_bf16_f32 v114, v60, v61\n"
    "v_mfma_f32_16x16x32_bf16 v[28:31], a[96:99], v[96:99], v[28:31]\n"
    "v_cvt_pk_bf16_f32 v115, v62, v63\n"
    "v_mfma_f32_16x16x32_bf16 v[36:39], a[176:179], v[96:99], v[36:39]\n"
    "v_swap_b32 v118, v120\n"
    "ds_read_b128 v[96:99], v1 offset:7168\n"
    "s_waitcnt lgkmcnt(5)\n"
    "v_mfma_f32_16x16x32_bf16 v[16:19], a[20:23], v[80:83], v[16:19]\n"
    "ds_write2st64_b64 v7, v[112:113], v[114:115] offset0:24 offset1:26\n"
    "v_mfma_f32_16x16x32_bf16 v[24:27], a[100:103], v[80:83], v[24:27]\n"
    "buffer_load_dword v224, v225, s[40:43], s58 offen sc1\n"
    "v_mfma_f32_16x16x32_bf16 v[32:35], a[180:183], v[80:83], v[32:35]\n"
    "v_swap_b32 v119, v121\n"
    "ds_read_b128 v[80:83], v1 offset:27648\n"
    "s_waitcnt lgkmcnt(6)\n"
    "v_mfma_f32_16x16x32_bf16 v[20:23], a[20:23], v[84:87], v[20:23]\n"
    "v_swap_b32 v126, v128\n"
    "v_mfma_f32_16x16x32_bf16 v[28:31], a[100:103], v[84:87], v[28:31]\n"
    "v_swap_b32 v127, v129\n"
    "v_mfma_f32_16x16x32_bf16 v[36:39], a[180:183], v[84:87], v[36:39]\n"
    "v_swap_b32 v134, v136\n"
    "ds_read_b128 v[84:87], v1 offset:8192\n"
    "s_waitcnt lgkmcnt(5)\n"
    "v_mfma_f32_16x16x32_bf16 v[16:19], a[24:27], v[88:91], v[16:19]\n"
    "v_swap_b32 v135, v137\n"
    "v_mfma_f32_16x16x32_bf16 v[24:27], a[104:107], v[88:91], v[24:27]\n"
    "v_swap_b32 v142, v144\n"
    "v_mfma_f32_16x16x32_bf16 v[32:35], a[184:187], v[88:91], v[32:35]\n"
    "v_swap_b32 v143, v145\n"
    "ds_read_b128 v[88:91], v1 offset:28672\n"
    "s_waitcnt lgkmcnt(5)\n"
    "v_mfma_f32_16x16x32_bf16 v[20:23], a[24:27], v[92:95], v[20:23]\n"
    "v_swap_b32 v150, v152\n"
    "v_mfma_f32_16x16x32_bf16 v[28:31], a[104:107], v[92:95], v[28:31]\n"
    "v_swap_b32 v151, v153\n"
    "v_mfma_f32_16x16x32_bf16 v[36:39], a[184:187], v[92:95], v[36:39]\n"
    "v_swap_b32 v158, v160\n"
    "ds_read_b128 v[92:95], v1 offset:9216\n"
    "s_waitcnt lgkmcnt(5)\n"
    "v_mfma_f32_16x16x32_bf16 v[16:19], a[28:31], v[96:99], v[16:19]\n"
    "v_swap_b32 v159, v161\n"
    "v_mfma_f32_16x16x32_bf16 v[24:27], a[108:111], v[96:99], v[24:27]\n"
    "v_mfma_f32_16x16x32_bf16 v[32:35], a[188:191], v[96:99], v[32:35]\n"
    "ds_read_b128 v[96:99], v1 offset:29696\n"
    "s_waitcnt lgkmcnt(4)\n"
    "v_mfma_f32_16x16x32_bf16 v[20:23], a[28:31], v[80:83], v[20:23]\n"
    "v_mfma_f32_16x16x32_bf16 v[64:67], v[116:119], v[188:191], v[212:215]\n"
    "v_mfma_f32_16x16x32_bf16 v[68:71], v[120:123], v[188:191], v[216:219]\n"
    "s_waitcnt vmcnt(7)\n"
    "v_mfma_f32_16x16x32_bf16 v[72:75], v[164:167], v[188:191], v[220:223]\n"
    "v_mfma_f32_16x16x32_bf16 v[64:67], v[124:127], v[192:195], v[64:67]\n"
    "v_mfma_f32_16x16x32_bf16 v[68:71], v[128:131], v[192:195], v[68:71]\n"
    "s_waitcnt vmcnt(6)\n"
    "v_mfma_f32_16x16x32_bf16 v[72:75], v[168:171], v[192:195], v[72:75]\n"
    "v_mfma_f32_16x16x32_bf16 v[64:67], v[132:135], v[196:199], v[64:67]\n"
    "v_mfma_f32_16x16x32_bf16 v[68:71], v[136:139], v[196:199], v[68:71]\n"
    "s_waitcnt vmcnt(5)\n"
    "v_mfma_f32_16x16x32_bf16 v[72:75], v[172:175], v[196:199], v[72:75]\n"
    "v_mfma_f32_16x16x32_bf16 v[64:67], v[140:143], v[200:203], v[64:67]\n"
    "v_mfma_f32_16x16x32_bf16 v[68:71], v[144:147], v[200:203], v[68:71]\n"
    "s_waitcnt vmcnt(4)\n"
    "v_mfma_f32_16x16x32_bf16 v[72:75], v[176:179], v[200:203], v[72:75]\n"
    "v_mfma_f32_16x16x32_bf16 v[64:67], v[148:151], v[204:207], v[64:67]\n"
    "v_mfma_f32_16x16x32_bf16 v[68:71], v[152:155], v[204:207], v[68:71]\n"
    "s_waitcnt vmcnt(3)\n"
    "v_mfma_f32_16x16x32_bf16 v[72:75], v[180:183], v[204:207], v[72:75]\n"
    "v_mfma_f32_16x16x32_bf16 v[64:67], v[156:159], v[208:211], v[64:67]\n"
    "v_mfma_f32_16x16x32_bf16 v[68:71], v[160:163], v[208:211], v[68:71]\n"
    "s_waitcnt vmcnt(2)\n"
    "v_mfma_f32_16x16x32_bf16 v[72:75], v[184:187], v[208:211], v[72:75]\n"
    "v_mfma_f32_16x16x32_bf16 v[28:31], a[108:111], v[80:83], v[28:31]\n"
    "v_mfma_f32_16x16x32_bf16 v[36:39], a[188:191], v[80:83], v[36:39]\n"
    "ds_read_b128 v[80:83], v1 offset:10240\n"
    "s_waitcnt lgkmcnt(4)\n"
    "v_mfma_f32_16x16x32_bf16 v[16:19], a[32:35], v[84:87], v[16:19]\n"
    "v_mfma_f32_16x16x32_bf16 v[24:27], a[112:115], v[84:87], v[24:27]\n"
    "v_max3_f32 v76, v64, v65, v66\n"
    "v_mfma_f32_16x16x32_bf16 v[32:35], a[192:195], v[84:87], v[32:35]\n"
    "buffer_load_dword v9, v10, s[40:43], s58 offen sc1\n"
    "ds_read_b128 v[84:87], v1 offset:30720\n"
    "s_waitcnt lgkmcnt(4)\n"
    "v_mfma_f32_16x16x32_bf16 v[20:23], a[32:35], v[88:91], v[20:23]\n"
    "v_max3_f32 v76, v76, v67, v68\n"
    "v_max3_f32 v76, v76, v69, v70\n"
    "v_mfma_f32_16x16x32_bf16 v[28:31], a[112:115], v[88:91], v[28:31]\n"
    "v_max3_f32 v76, v76, v71, v72\n"
    "v_max3_f32 v76, v76, v73, v74\n"
    "v_mfma_f32_16x16x32_bf16 v[36:39], a[192:195], v[88:91], v[36:39]\n"
    "buffer_load_dwordx4 v[116:119], v0, s[40:43], s54 offen sc1\n"
    "ds_read_b128 v[88:91], v1 offset:11264\n"
    "s_waitcnt lgkmcnt(4)\n"
    "v_mfma_f32_16x16x32_bf16 v[16:19], a[36:39], v[92:95], v[16:19]\n"
    "v_max_f32 v76, v76, v75\n"
    "v_mov_b32 v77, v76\n"
    "v_mfma_f32_16x16x32_bf16 v[24:27], a[116:119], v[92:95], v[24:27]\n"
    "s_nop 0\n"
    "v_permlane16_swap_b32 v76, v77\n"
    "v_mfma_f32_16x16x32_bf16 v[32:35], a[196:199], v[92:95], v[32:35]\n"
    "buffer_load_dwordx4 v[120:123], v0, s[40:43], s54 offen offset:1024 sc1\n"
    "ds_read_b128 v[92:95], v1 offset:31744\n"
    "s_waitcnt lgkmcnt(4)\n"
    "v_mfma_f32_16x16x32_bf16 v[20:23], a[36:39], v[96:99], v[20:23]\n"
    "v_max_f32 v76, v76, v77\n"
    "v_mov_b32 v77, v76\n"
    "v_mfma_f32_16x16x32_bf16 v[28:31], a[116:119], v[96:99], v[28:31]\n"
    "s_nop 0\n"
    "v_permlane32_swap_b32 v76, v77\n"
    "v_mfma_f32_16x16x32_bf16 v[36:39], a[196:199], v[96:99], v[36:39]\n"
    "buffer_load_dwordx4 v[124:127], v0, s[40:43], s54 offen offset:3072 sc1\n"
    "ds_read_b128 v[96:99], v1 offset:12288\n"
    "s_waitcnt lgkmcnt(4)\n"
    "v_mfma_f32_16x16x32_bf16 v[16:19], a[40:43], v[80:83], v[16:19]\n"
    "v_max_f32 v76, v76, v77\n"
    "v_mul_f32 v76, 0xbdd53b95, v76\n"
    "v_mfma_f32_16x16x32_bf16 v[24:27], a[120:123], v[80:83], v[24:27]\n"
    "v_fmamk_f32 v64, v64, 0x3dd53b95, v76\n"
    "v_fmamk_f32 v65, v65, 0x3dd53b95, v76\n"
    "v_mfma_f32_16x16x32_bf16 v[32:35], a[200:203], v[80:83], v[32:35]\n"
    "buffer_load_dwordx4 v[128:131], v0, s[40:43], s55 offen sc1\n"
    "ds_read_b128 v[80:83], v1 offset:32768\n"
    "s_waitcnt lgkmcnt(4)\n"
    "v_mfma_f32_16x16x32_bf16 v[20:23], a[40:43], v[84:87], v[20:23]\n"
    "v_fmamk_f32 v66, v66, 0x3dd53b95, v76\n"
    "v_fmamk_f32 v67, v67, 0x3dd53b95, v76\n"
    "v_mfma_f32_16x16x32_bf16 v[28:31], a[120:123], v[84:87], v[28:31]\n"
    "v_fmamk_f32 v68, v68, 0x3dd53b95, v76\n"
    "v_fmamk_f32 v69, v69, 0x3dd53b95, v76\n"
    "v_mfma_f32_16x16x32_bf16 v[36:39], a[200:203], v[84:87], v[36:39]\n"
    "buffer_load_dwordx4 v[132:135], v0, s[40:43], s55 offen offset:2048 sc1\n"
    "ds_read_b128 v[84:87], v1 offset:13312\n"
    "s_waitcnt lgkmcnt(4)\n"
    "v_mfma_f32_16x16x32_bf16 v[16:19], a[44:47], v[88:91], v[16:19]\n"
    "v_fmamk_f32 v70, v70, 0x3dd53b95, v76\n"
    "v_fmamk_f32 v71, v71, 0x3dd53b95, v76\n"
    "v_mfma_f32_16x16x32_bf16 v[24:27], a[124:127], v[88:91], v[24:27]\n"
    "v_fmamk_f32 v72, v72, 0x3dd53b95, v76\n"
    "v_fmamk_f32 v73, v73, 0x3dd53b95, v76\n"
    "v_mfma_f32_16x16x32_bf16 v[32:35], a[204:207], v[88:91], v[32:35]\n"
    "buffer_load_dwordx4 v[136:139], v0, s[40:43], s55 offen offset:3072 sc1\n"
    "ds_read_b128 v[88:91], v1 offset:33792\n"
    "s_waitcnt lgkmcnt(4)\n"
    "v_mfma_f32_16x16x32_bf16 v[20:23], a[44:47], v[92:95], v[20:23]\n"
    "v_fmamk_f32 v74, v74, 0x3dd53b95, v76\n"
    "v_fmamk_f32 v75, v75, 0x3dd53b95, v76\n"
    "v_mfma_f32_16x16x32_bf16 v[28:31], a[124:127], v[92:95], v[28:31]\n"
    "v_exp_f32 v64, v64\n"
    "v_mfma_f32_16x16x32_bf16 v[36:39], a[204:207], v[92:95], v[36:39]\n"
    "buffer_load_dwordx4 v[140:143], v0, s[40:43], s56 offen offset:1024 sc1\n"
    "ds_read_b128 v[92:95], v1 offset:14336\n"
    "s_waitcnt lgkmcnt(4)\n"
    "v_mfma_f32_16x16x32_bf16 v[16:19], a[48:51], v[96:99], v[16:19]\n"
    "v_exp_f32 v65, v65\n"
    "v_mfma_f32_16x16x32_bf16 v[24:27], a[128:131], v[96:99], v[24:27]\n"
    "v_exp_f32 v66, v66\n"
    "v_mfma_f32_16x16x32_bf16 v[32:35], a[208:211], v[96:99], v[32:35]\n"
    "buffer_load_dwordx4 v[144:147], v0, s[40:43], s56 offen offset:2048 sc1\n"
    "ds_read_b128 v[96:99], v1 offset:34816\n"
    "s_waitcnt lgkmcnt(4)\n"
    "v_mfma_f32_16x16x32_bf16 v[20:23], a[48:51], v[80:83], v[20:23]\n"
    "v_exp_f32 v67, v67\n"
    "v_mfma_f32_16x16x32_bf16 v[28:31], a[128:131], v[80:83], v[28:31]\n"
    "v_exp_f32 v68, v68\n"
    "v_mfma_f32_16x16x32_bf16 v[36:39], a[208:211], v[80:83], v[36:39]\n"
    "buffer_load_dwordx4 v[148:151], v0, s[40:43], s54 offen offset:2048 sc1\n"
    "ds_read_b128 v[80:83], v1 offset:15360\n"
    "s_waitcnt lgkmcnt(4)\n"
    "v_mfma_f32_16x16x32_bf16 v[16:19], a[52:55], v[84:87], v[16:19]\n"
    "v_exp_f32 v69, v69\n"
    "v_mfma_f32_16x16x32_bf16 v[24:27], a[132:135], v[84:87], v[24:27]\n"
    "v_exp_f32 v70, v70\n"
    "v_mfma_f32_16x16x32_bf16 v[32:35], a[212:215], v[84:87], v[32:35]\n"
    "buffer_load_dwordx4 v[152:155], v0, s[40:43], s55 offen offset:1024 sc1\n"
    "ds_read_b128 v[84:87], v1 offset:35840\n"
    "s_waitcnt lgkmcnt(4)\n"
    "v_mfma_f32_16x16x32_bf16 v[20:23], a[52:55], v[88:91], v[20:23]\n"
    "v_exp_f32 v71, v71\n"
    "v_mfma_f32_16x16x32_bf16 v[28:31], a[132:135], v[88:91], v[28:31]\n"
    "v_exp_f32 v72, v72\n"
    "v_mfma_f32_16x16x32_bf16 v[36:39], a[212:215], v[88:91], v[36:39]\n"
    "buffer_load_dwordx4 v[156:159], v0, s[40:43], s56 offen sc1\n"
    "ds_read_b128 v[88:91], v1 offset:16384\n"
    "s_waitcnt lgkmcnt(4)\n"
    "v_mfma_f32_16x16x32_bf16 v[16:19], a[56:59], v[92:95], v[16:19]\n"
    "v_exp_f32 v73, v73\n"
    "v_mfma_f32_16x16x32_bf16 v[24:27], a[136:139], v[92:95], v[24:27]\n"
    "v_exp_f32 v74, v74\n"
    "v_mfma_f32_16x16x32_bf16 v[32:35], a[216:219], v[92:95], v[32:35]\n"
    "buffer_load_dwordx4 v[160:163], v0, s[40:43], s56 offen offset:3072 sc1\n"
    "s_add_u32 s54, s54, 393216\n"
    "s_add_u32 s55, s55, 393216\n"
    "s_add_u32 s56, s56, 393216\n"
    "ds_read_b128 v[92:95], v1 offset:36864\n"
    "s_waitcnt lgkmcnt(4)\n"
    "v_mfma_f32_16x16x32_bf16 v[20:23], a[56:59], v[96:99], v[20:23]\n"
    "v_exp_f32 v75, v75\n"
    "v_mfma_f32_16x16x32_bf16 v[28:31], a[136:139], v[96:99], v[28:31]\n"
    "v_add_f32 v76, v64, v65\n"
    "v_add_f32 v76, v76, v66\n"
    "v_mfma_f32_16x16x32_bf16 v[36:39], a[216:219], v[96:99], v[36:39]\n"
    "s_waitcnt vmcnt(13) lgkmcnt(4)\n"
    "s_barrier\n"
    "buffer_atomic_add v226, v3, s[40:43], s29 offen\n"
    "s_and_b32 s48, s47, 63\n"
    "s_cmp_lt_u32 s48, 62\n"
    "s_cselect_b32 s48, 1, 0\n"
    "s_mov_b32 s60, 0\n"
    "v_cmp_gt_u32 vcc, s48, v224\n"
    "s_nop 4\n"
    "s_cbranch_vccz hpq_xf0_ok_%=\n"
    "hpq_xf0_poll_%=:\n"
    "buffer_load_dword v224, v225, s[40:43], s58 offen sc1\n"
    "s_waitcnt vmcnt(0)\n"
    "v_cmp_gt_u32 vcc, s48, v224\n"
    "s_nop 4\n"
    "s_cbranch_vccz hpq_xf0_ok_%=\n"
    "s_sleep 2\n"
    "s_add_u32 s60, s60, 1\n"
    "s_cmp_gt_u32 s60, 65536\n"
    "s_cbranch_scc1 hp_abort_%=\n"
    "s_and_b32 s59, s60, 63\n"
    "s_cbranch_scc1 hpq_xf0_poll_%=\n"
    "v_mov_b32 v4, 1024\n"
    "buffer_load_dword v4, v4, s[40:43], s58 offen sc1\n"
    "s_waitcnt vmcnt(0)\n"
    "v_readfirstlane_b32 s59, v4\n"
    "s_nop 0\n"
    "s_cmp_lg_u32 s59, 0\n"
    "s_nop 2\n"
    "s_cbranch_scc1 hp_end_%=\n"
    "s_branch hpq_xf0_poll_%=\n"
    "hpq_xf0_ok_%=:\n"
    "v_add_u32 v225, 128, v225\n"
    "ds_read_b128 v[96:99], v1 offset:17408\n"
    "s_waitcnt lgkmcnt(4)\n"
    "v_mfma_f32_16x16x32_bf16 v[16:19], a[60:63], v[80:83], v[16:19]\n"
    "ds_read_b128 v[188:191], v8 offset:12288\n"
    "v_add_f32 v76, v76, v67\n"
    "s_add_u32 m0, s61, 0\n"
    "s_add_u32 s51, s50, 0\n"
    "buffer_load_dwordx4 v0, s[40:43], s51 offen offset:0 sc1 lds\n"
    "v_mfma_f32_16x16x32_bf16 v[24:27], a[140:143], v[80:83], v[24:27]\n"
    "ds_read_b128 v[192:195], v8 offset:14336\n"
    "v_add_f32 v76, v76, v68\n"
    "v_add_f32 v76, v76, v69\n"
    "v_mfma_f32_16x16x32_bf16 v[32:35], a[220:223], v[80:83], v[32:35]\n"
    "v_add_f32 v76, v76, v70\n"
    "ds_read_b128 v[80:83], v1 offset:37888\n"
    "s_waitcnt lgkmcnt(6)\n"
    "v_mfma_f32_16x16x32_bf16 v[20:23], a[60:63], v[84:87], v[20:23]\n"
    "ds_read_b128 v[196:199], v8 offset:16384\n"
    "v_add_f32 v76, v76, v71\n"
    "buffer_load_dwordx4 v0, s[40:43], s51 offen offset:1024 sc1 lds\n"
    "v_mfma_f32_16x16x32_bf16 v[28:31], a[140:143], v[84:87], v[28:31]\n"
    "ds_read_b128 v[200:203], v8 offset:18432\n"
    "v_add_f32 v76, v76, v72\n"
    "v_add_f32 v76, v76, v73\n"
    "v_mfma_f32_16x16x32_bf16 v[36:39], a[220:223], v[84:87], v[36:39]\n"
    "v_add_f32 v76, v76, v74\n"
    "ds_read_b128 v[84:87], v1 offset:18432\n"
    "s_waitcnt lgkmcnt(8)\n"
    "v_mfma_f32_16x16x32_bf16 v[16:19], a[64:67], v[88:91], v[16:19]\n"
    "ds_read_b128 v[204:207], v8 offset:20480\n"
    "v_add_f32 v76, v76, v75\n"
    "buffer_load_dwordx4 v0, s[40:43], s51 offen offset:2048 sc1 lds\n"
    "v_mfma_f32_16x16x32_bf16 v[24:27], a[144:147], v[88:91], v[24:27]\n"
    "ds_read_b128 v[208:211], v8 offset:22528\n"
    "v_mov_b32 v77, v76\n"
    "s_nop 1\n"
    "v_permlane16_swap_b32 v76, v77\n"
    "v_mfma_f32_16x16x32_bf16 v[32:35], a[224:227], v[88:91], v[32:35]\n"
    "v_add_f32 v76, v76, v77\n"
    "ds_read_b128 v[88:91], v1 offset:38912\n"
    "s_waitcnt lgkmcnt(10)\n"
    "v_mfma_f32_16x16x32_bf16 v[20:23], a[64:67], v[92:95], v[20:23]\n"
    "v_mov_b32 v77, v76\n"
    "buffer_load_dwordx4 v0, s[40:43], s51 offen offset:3072 sc1 lds\n"
    "v_mfma_f32_16x16x32_bf16 v[28:31], a[144:147], v[92:95], v[28:31]\n"
    "v_permlane32_swap_b32 v76, v77\n"
    "v_mfma_f32_16x16x32_bf16 v[36:39], a[224:227], v[92:95], v[36:39]\n"
    "v_add_f32 v76, v76, v77\n"
    "ds_read_b128 v[92:95], v1 offset:19456\n"
    "s_waitcnt lgkmcnt(10)\n"
    "v_mfma_f32_16x16x32_bf16 v[16:19], a[68:71], v[96:99], v[16:19]\n"
    "v_rcp_f32 v76, v76\n"
    "s_add_u32 m0, s61, 4096\n"
    "s_add_u32 s51, s50, 4096\n"
    "buffer_load_dwordx4 v0, s[40:43], s51 offen offset:0 sc1 lds\n"
    "v_mfma_f32_16x16x32_bf16 v[24:27], a[148:151], v[96:99], v[24:27]\n"
    "v_mul_f32 v64, v64, v76\n"
    "v_mul_f32 v65, v65, v76\n"
    "v_mfma_f32_16x16x32_bf16 v[32:35], a[228:231], v[96:99], v[32:35]\n"
    "v_mul_f32 v66, v66, v76\n"
    "ds_read_b128 v[96:99], v1 offset:39936\n"
    "s_waitcnt lgkmcnt(8)\n"
    "v_mfma_f32_16x16x32_bf16 v[20:23], a[68:71], v[80:83], v[20:23]\n"
    "v_mul_f32 v67, v67, v76\n"
    "buffer_load_dwordx4 v0, s[40:43], s51 offen offset:1024 sc1 lds\n"
    "v_mfma_f32_16x16x32_bf16 v[28:31], a[148:151], v[80:83], v[28:31]\n"
    "v_mul_f32 v68, v68, v76\n"
    "v_mul_f32 v69, v69, v76\n"
    "v_mfma_f32_16x16x32_bf16 v[36:39], a[228:231], v[80:83], v[36:39]\n"
    "v_mul_f32 v70, v70, v76\n"
    "s_add_u32 s64, s64, 40960\n"
    "s_cmp_ge_u32 s64, 122880\n"
    "s_cselect_b32 s64, 0, s64\n"
    "v_add_u32 v1, s64, v0\n"
    "ds_read_b128 v[80:83], v1 offset:0\n"
    "s_waitcnt lgkmcnt(6)\n"
    "v_mfma_f32_16x16x32_bf16 v[16:19], a[72:75], v[84:87], v[16:19]\n"
    "v_mul_f32 v71, v71, v76\n"
    "buffer_load_dwordx4 v0, s[40:43], s51 offen offset:2048 sc1 lds\n"
    "v_mfma_f32_16x16x32_bf16 v[24:27], a[152:155], v[84:87], v[24:27]\n"
    "v_mul_f32 v72, v72, v76\n"
    "v_mul_f32 v73, v73, v76\n"
    "v_mfma_f32_16x16x32_bf16 v[32:35], a[232:235], v[84:87], v[32:35]\n"
    "v_mul_f32 v74, v74, v76\n"
    "ds_read_b128 v[84:87], v1 offset:20480\n"
    "s_waitcnt lgkmcnt(4)\n"
    "v_mfma_f32_16x16x32_bf16 v[20:23], a[72:75], v[88:91], v[20:23]\n"
    "v_mul_f32 v75, v75, v76\n"
    "buffer_load_dwordx4 v0, s[40:43], s51 offen offset:3072 sc1 lds\n"
    "v_mfma_f32_16x16x32_bf16 v[28:31], a[152:155], v[88:91], v[28:31]\n"
    "v_cvt_pk_bf16_f32 v228, v64, v65\n"
    "v_cvt_pk_bf16_f32 v229, v66, v67\n"
    "v_mfma_f32_16x16x32_bf16 v[36:39], a[232:235], v[88:91], v[36:39]\n"
    "v_cvt_pk_bf16_f32 v230, v68, v69\n"
    "ds_read_b128 v[88:91], v1 offset:1024\n"
    "s_waitcnt lgkmcnt(4)\n"
    "v_mfma_f32_16x16x32_bf16 v[16:19], a[76:79], v[92:95], v[16:19]\n"
    "v_cvt_pk_bf16_f32 v231, v70, v71\n"
    "s_add_u32 m0, s61, 8192\n"
    "s_add_u32 s51, s50, 8192\n"
    "buffer_load_dwordx4 v0, s[40:43], s51 offen offset:0 sc1 lds\n"
    "v_mfma_f32_16x16x32_bf16 v[24:27], a[156:159], v[92:95], v[24:27]\n"
    "v_cvt_pk_bf16_f32 v232, v72, v73\n"
    "v_cvt_pk_bf16_f32 v233, v74, v75\n"
    "v_mfma_f32_16x16x32_bf16 v[32:35], a[236:239], v[92:95], v[32:35]\n"
    "v_mov_b32 v234, v232\n"
    "ds_read_b128 v[92:95], v1 offset:21504\n"
    "s_waitcnt lgkmcnt(4)\n"
    "v_mfma_f32_16x16x32_bf16 v[20:23], a[76:79], v[96:99], v[20:23]\n"
    "v_mov_b32 v235, v233\n"
    "buffer_load_dwordx4 v0, s[40:43], s51 offen offset:1024 sc1 lds\n"
    "s_add_u32 s50, s50, 40960\n"
    "s_add_u32 s61, s61, 40960\n"
    "s_cmp_ge_u32 s61, 122880\n"
    "s_cselect_b32 s51, 122880, 0\n"
    "s_sub_u32 s61, s61, s51\n"
    "v_mfma_f32_16x16x32_bf16 v[28:31], a[156:159], v[96:99], v[28:31]\n"
    "buffer_store_dwordx4 v[228:231], v0, s[40:43], s26 offen sc1\n"
    "v_permlane16_swap_b32 v232, v234\n"
    "v_mfma_f32_16x16x32_bf16 v[36:39], a[236:239], v[96:99], v[36:39]\n"
    "v_permlane16_swap_b32 v233, v235\n"
    "ds_read_b128 v[96:99], v1 offset:2048\n"
    "buffer_store_dwordx4 v[232:235], v12, s[40:43], s27 offen sc1\n"
    "s_add_u32 s47, s47, 1\n"
    "s_mov_b32 s29, s52\n"
    "s_waitcnt vmcnt(25)\n"
    "s_add_u32 s48, s63, 1\n"
    "s_cmp_ge_i32 s63, s65\n"
    "s_cselect_b32 s48, s48, 0\n"
    "s_cselect_b32 s49, s30, 0x40000000\n"
    "v_cmp_gt_u32 vcc, s48, v9\n"
    "s_cbranch_vccz hpq_kma1_ok_%=\n"
    "s_waitcnt vmcnt(0)\n"
    "buffer_atomic_add v226, v3, s[40:43], s29 offen\n"
    "s_mov_b32 s29, 0x40000000\n"
    "s_mov_b32 s60, 0\n"
    "v_cmp_gt_u32 vcc, s48, v9\n"
    "s_nop 4\n"
    "s_cbranch_vccz hpq_kma1_ok_%=\n"
    "hpq_kma1_poll_%=:\n"
    "buffer_load_dword v9, v10, s[40:43], s58 offen sc1\n"
    "s_waitcnt vmcnt(0)\n"
    "v_cmp_gt_u32 vcc, s48, v9\n"
    "s_nop 4\n"
    "s_cbranch_vccz hpq_kma1_ok_%=\n"
    "s_sleep 2\n"
    "s_add_u32 s60, s60, 1\n"
    "s_cmp_gt_u32 s60, 65536\n"
    "s_cbranch_scc1 hp_abort_%=\n"
    "s_and_b32 s59, s60, 63\n"
    "s_cbranch_scc1 hpq_kma1_poll_%=\n"
    "v_mov_b32 v4, 1024\n"
    "buffer_load_dword v4, v4, s[40:43], s58 offen sc1\n"
    "s_waitcnt vmcnt(0)\n"
    "v_readfirstlane_b32 s59, v4\n"
    "s_nop 0\n"
    "s_cmp_lg_u32 s59, 0\n"
    "s_nop 2\n"
    "s_cbranch_scc1 hp_end_%=\n"
    "s_branch hpq_kma1_poll_%=\n"
    "hpq_kma1_ok_%=:\n"
    "s_movk_i32 s48, 32\n"
    "s_mov_b32 s60, 0\n"
    "v_cmp_gt_u32 vcc, s48, v236\n"
    "s_nop 4\n"
    "s_cbranch_vccz hpq_kxa1_ok_%=\n"
    "hpq_kxa1_poll_%=:\n"
    "buffer_load_dword v236, v227, s[40:43], s58 offen sc1\n"
    "s_waitcnt vmcnt(0)\n"
    "v_cmp_gt_u32 vcc, s48, v236\n"
    "s_nop 4\n"
    "s_cbranch_vccz hpq_kxa1_ok_%=\n"
    "s_sleep 2\n"
    "s_add_u32 s60, s60, 1\n"
    "s_cmp_gt_u32 s60, 65536\n"
    "s_cbranch_scc1 hp_abort_%=\n"
    "s_and_b32 s59, s60, 63\n"
    "s_cbranch_scc1 hpq_kxa1_poll_%=\n"
    "v_mov_b32 v4, 1024\n"
    "buffer_load_dword v4, v4, s[40:43], s58 offen sc1\n"
    "s_waitcnt vmcnt(0)\n"
    "v_readfirstlane_b32 s59, v4\n"
    "s_nop 0\n"
    "s_cmp_lg_u32 s59, 0\n"
    "s_nop 2\n"
    "s_cbranch_scc1 hp_end_%=\n"
    "s_branch hpq_kxa1_poll_%=\n"
    "hpq_kxa1_ok_%=:\n"
    "v_add_u32 v227, 128, v227\n"
    "s_or_b32 s57, s34, s49\n"
    "s_add_u32 s34, s34, 98304\n"
    "s_or_b32 s52, s35, s49\n"
    "s_or_b32 s26, s36, s49\n"
    "s_or_b32 s27, s37, s49\n"
    "s_add_u32 s35, s35, 1536\n"
    "s_add_u32 s36, s36, 65536\n"
    "s_add_u32 s37, s37, 16384\n"
    "s_add_u32 s63, s63, 1\n"
    "s_waitcnt lgkmcnt(4)\n"
    "v_mfma_f32_16x16x32_bf16 v[40:43], a[0:3], v[80:83], 0\n"
    "buffer_load_dwordx4 v[164:167], v11, s[40:43], s57 offen sc1\n"
    "v_mfma_f32_16x16x32_bf16 v[48:51], a[80:83], v[80:83], 0\n"
    "buffer_load_dwordx4 v[168:171], v11, s[40:43], s57 offen offset:512 sc1\n"
    "v_mfma_f32_16x16x32_bf16 v[56:59], a[160:163], v[80:83], 0\n"
    "buffer_load_dwordx4 v[172:175], v11, s[40:43], s57 offen offset:1024 sc1\n"
    "ds_read_b128 v[80:83], v1 offset:22528\n"
    "s_waitcnt lgkmcnt(4)\n"
    "v_mfma_f32_16x16x32_bf16 v[44:47], a[0:3], v[84:87], 0\n"
    "buffer_load_dwordx4 v[176:179], v11, s[40:43], s57 offen offset:1536 sc1\n"
    "v_mfma_f32_16x16x32_bf16 v[52:55], a[80:83], v[84:87], 0\n"
    "buffer_load_dwordx4 v[180:183], v11, s[40:43], s57 offen offset:2048 sc1\n"
    "v_mfma_f32_16x16x32_bf16 v[60:63], a[160:163], v[84:87], 0\n"
    "buffer_load_dwordx4 v[184:187], v11, s[40:43], s57 offen offset:2560 sc1\n"
    "ds_read_b128 v[84:87], v1 offset:3072\n"
    "s_waitcnt lgkmcnt(4)\n"
    "v_mfma_f32_16x16x32_bf16 v[40:43], a[4:7], v[88:91], v[40:43]\n"
    "buffer_load_dword v236, v227, s[40:43], s58 offen sc1\n"
    "v_mfma_f32_16x16x32_bf16 v[48:51], a[84:87], v[88:91], v[48:51]\n"
    "v_mfma_f32_16x16x32_bf16 v[56:59], a[164:167], v[88:91], v[56:59]\n"
    "ds_read_b128 v[88:91], v1 offset:23552\n"
    "s_waitcnt lgkmcnt(4)\n"
    "v_mfma_f32_16x16x32_bf16 v[44:47], a[4:7], v[92:95], v[44:47]\n"
    "v_cvt_pk_bf16_f32 v104, v16, v17\n"
    "v_mfma_f32_16x16x32_bf16 v[52:55], a[84:87], v[92:95], v[52:55]\n"
    "v_cvt_pk_bf16_f32 v105, v18, v19\n"
    "v_mfma_f32_16x16x32_bf16 v[60:63], a[164:167], v[92:95], v[60:63]\n"
    "ds_read_b128 v[92:95], v1 offset:4096\n"
    "s_waitcnt lgkmcnt(4)\n"
    "v_mfma_f32_16x16x32_bf16 v[40:43], a[8:11], v[96:99], v[40:43]\n"
    "v_cvt_pk_bf16_f32 v106, v20, v21\n"
    "v_mfma_f32_16x16x32_bf16 v[48:51], a[88:91], v[96:99], v[48:51]\n"
    "v_cvt_pk_bf16_f32 v107, v22, v23\n"
    "v_mfma_f32_16x16x32_bf16 v[56:59], a[168:171], v[96:99], v[56:59]\n"
    "ds_read_b128 v[96:99], v1 offset:24576\n"
    "s_waitcnt lgkmcnt(4)\n"
    "v_mfma_f32_16x16x32_bf16 v[44:47], a[8:11], v[80:83], v[44:47]\n"
    "ds_write2st64_b64 v6, v[104:105], v[106:107] offset0:0 offset1:2\n"
    "v_mfma_f32_16x16x32_bf16 v[52:55], a[88:91], v[80:83], v[52:55]\n"
    "v_cvt_pk_bf16_f32 v108, v24, v25\n"
    "v_mfma_f32_16x16x32_bf16 v[60:63], a[168:171], v[80:83], v[60:63]\n"
    "ds_read_b128 v[80:83], v1 offset:5120\n"
    "s_waitcnt lgkmcnt(5)\n"
    "v_mfma_f32_16x16x32_bf16 v[40:43], a[12:15], v[84:87], v[40:43]\n"
    "v_cvt_pk_bf16_f32 v109, v26, v27\n"
    "v_mfma_f32_16x16x32_bf16 v[48:51], a[92:95], v[84:87], v[48:51]\n"
    "v_cvt_pk_bf16_f32 v110, v28, v29\n"
    "v_mfma_f32_16x16x32_bf16 v[56:59], a[172:175], v[84:87], v[56:59]\n"
    "ds_read_b128 v[84:87], v1 offset:25600\n"
    "s_waitcnt lgkmcnt(5)\n"
    "v_mfma_f32_16x16x32_bf16 v[44:47], a[12:15], v[88:91], v[44:47]\n"
    "v_cvt_pk_bf16_f32 v111, v30, v31\n"
    "v_mfma_f32_16x16x32_bf16 v[52:55], a[92:95], v[88:91], v[52:55]\n"
    "ds_write2st64_b64 v2, v[108:109], v[110:111] offset0:0 offset1:2\n"
    "v_mfma_f32_16x16x32_bf16 v[60:63], a[172:175], v[88:91], v[60:63]\n"
    "ds_read_b128 v[88:91], v1 offset:6144\n"
    "s_waitcnt lgkmcnt(6)\n"
    "v_mfma_f32_16x16x32_bf16 v[40:43], a[16:19], v[92:95], v[40:43]\n"
    "v_cvt_pk_bf16_f32 v112, v32, v33\n"
    "v_mfma_f32_16x16x32_bf16 v[48:51], a[96:99], v[92:95], v[48:51]\n"
    "v_cvt_pk_bf16_f32 v113, v34, v35\n"
    "v_mfma_f32_16x16x32_bf16 v[56:59], a[176:179], v[92:95], v[56:59]\n"
    "s_waitcnt vmcnt(20)\n"
    "ds_read_b128 v[92:95], v1 offset:26624\n"
    "s_waitcnt lgkmcnt(6)\n"
    "v_mfma_f32_16x16x32_bf16 v[44:47], a[16:19], v[96:99], v[44:47]\n"
    "v_cvt_pk_bf16_f32 v114, v36, v37\n"
    "v_mfma_f32_16x16x32_bf16 v[52:55], a[96:99], v[96:99], v[52:55]\n"
    "v_cvt_pk_bf16_f32 v115, v38, v39\n"
    "v_mfma_f32_16x16x32_bf16 v[60:63], a[176:179], v[96:99], v[60:63]\n"
    "v_swap_b32 v118, v120\n"
    "ds_read_b128 v[96:99], v1 offset:7168\n"
    "s_waitcnt lgkmcnt(5)\n"
    "v_mfma_f32_16x16x32_bf16 v[40:43], a[20:23], v[80:83], v[40:43]\n"
    "ds_write2st64_b64 v7, v[112:113], v[114:115] offset0:0 offset1:2\n"
    "v_mfma_f32_16x16x32_bf16 v[48:51], a[100:103], v[80:83], v[48:51]\n"
    "buffer_load_dword v224, v225, s[40:43], s58 offen sc1\n"
    "v_mfma_f32_16x16x32_bf16 v[56:59], a[180:183], v[80:83], v[56:59]\n"
    "v_swap_b32 v119, v121\n"
    "ds_read_b128 v[80:83], v1 offset:27648\n"
    "s_waitcnt lgkmcnt(6)\n"
    "v_mfma_f32_16x16x32_bf16 v[44:47], a[20:23], v[84:87], v[44:47]\n"
    "v_swap_b32 v126, v128\n"
    "v_mfma_f32_16x16x32_bf16 v[52:55], a[100:103], v[84:87], v[52:55]\n"
    "v_swap_b32 v127, v129\n"
    "v_mfma_f32_16x16x32_bf16 v[60:63], a[180:183], v[84:87], v[60:63]\n"
    "v_swap_b32 v134, v136\n"
    "ds_read_b128 v[84:87], v1 offset:8192\n"
    "s_waitcnt lgkmcnt(5)\n"
    "v_mfma_f32_16x16x32_bf16 v[40:43], a[24:27], v[88:91], v[40:43]\n"
    "v_swap_b32 v135, v137\n"
    "v_mfma_f32_16x16x32_bf16 v[48:51], a[104:107], v[88:91], v[48:51]\n"
    "v_swap_b32 v142, v144\n"
    "v_mfma_f32_16x16x32_bf16 v[56:59], a[184:187], v[88:91], v[56:59]\n"
    "v_swap_b32 v143, v145\n"
    "ds_read_b128 v[88:91], v1 offset:28672\n"
    "s_waitcnt lgkmcnt(5)\n"
    "v_mfma_f32_16x16x32_bf16 v[44:47], a[24:27], v[92:95], v[44:47]\n"
    "v_swap_b32 v150, v152\n"
    "v_mfma_f32_16x16x32_bf16 v[52:55], a[104:107], v[92:95], v[52:55]\n"
    "v_swap_b32 v151, v153\n"
    "v_mfma_f32_16x16x32_bf16 v[60:63], a[184:187], v[92:95], v[60:63]\n"
    "v_swap_b32 v158, v160\n"
    "ds_read_b128 v[92:95], v1 offset:9216\n"
    "s_waitcnt lgkmcnt(5)\n"
    "v_mfma_f32_16x16x32_bf16 v[40:43], a[28:31], v[96:99], v[40:43]\n"
    "v_swap_b32 v159, v161\n"
    "v_mfma_f32_16x16x32_bf16 v[48:51], a[108:111], v[96:99], v[48:51]\n"
    "v_mfma_f32_16x16x32_bf16 v[56:59], a[188:191], v[96:99], v[56:59]\n"
    "ds_read_b128 v[96:99], v1 offset:29696\n"
    "s_waitcnt lgkmcnt(4)\n"
    "v_mfma_f32_16x16x32_bf16 v[44:47], a[28:31], v[80:83], v[44:47]\n"
    "v_mfma_f32_16x16x32_bf16 v[64:67], v[116:119], v[188:191], v[212:215]\n"
    "v_mfma_f32_16x16x32_bf16 v[68:71], v[120:123], v[188:191], v[216:219]\n"
    "s_waitcnt vmcnt(7)\n"
    "v_mfma_f32_16x16x32_bf16 v[72:75], v[164:167], v[188:191], v[220:223]\n"
    "v_mfma_f32_16x16x32_bf16 v[64:67], v[124:127], v[192:195], v[64:67]\n"
    "v_mfma_f32_16x16x32_bf16 v[68:71], v[128:131], v[192:195], v[68:71]\n"
    "s_waitcnt vmcnt(6)\n"
    "v_mfma_f32_16x16x32_bf16 v[72:75], v[168:171], v[192:195], v[72:75]\n"
    "v_mfma_f32_16x16x32_bf16 v[64:67], v[132:135], v[196:199], v[64:67]\n"
    "v_mfma_f32_16x16x32_bf16 v[68:71], v[136:139], v[196:199], v[68:71]\n"
    "s_waitcnt vmcnt(5)\n"
    "v_mfma_f32_16x16x32_bf16 v[72:75], v[172:175], v[196:199], v[72:75]\n"
    "v_mfma_f32_16x16x32_bf16 v[64:67], v[140:143], v[200:203], v[64:67]\n"
    "v_mfma_f32_16x16x32_bf16 v[68:71], v[144:147], v[200:203], v[68:71]\n"
    "s_waitcnt vmcnt(4)\n"
    "v_mfma_f32_16x16x32_bf16 v[72:75], v[176:179], v[200:203], v[72:75]\n"
    "v_mfma_f32_16x16x32_bf16 v[64:67], v[148:151], v[204:207], v[64:67]\n"
    "v_mfma_f32_16x16x32_bf16 v[68:71], v[152:155], v[204:207], v[68:71]\n"
    "s_waitcnt vmcnt(3)\n"
    "v_mfma_f32_16x16x32_bf16 v[72:75], v[180:183], v[204:207], v[72:75]\n"
    "v_mfma_f32_16x16x32_bf16 v[64:67], v[156:159], v[208:211], v[64:67]\n"
    "v_mfma_f32_16x16x32_bf16 v[68:71], v[160:163], v[208:211], v[68:71]\n"
    "s_waitcnt vmcnt(2)\n"
    "v_mfma_f32_16x16x32_bf16 v[72:75], v[184:187], v[208:211], v[72:75]\n"
    "v_mfma_f32_16x16x32_bf16 v[52:55], a[108:111], v[80:83], v[52:55]\n"
    "v_mfma_f32_16x16x32_bf16 v[60:63], a[188:191], v[80:83], v[60:63]\n"
    "ds_read_b128 v[80:83], v1 offset:10240\n"
    "s_waitcnt lgkmcnt(4)\n"
    "v_mfma_f32_16x16x32_bf16 v[40:43], a[32:35], v[84:87], v[40:43]\n"
    "v_mfma_f32_16x16x32_bf16 v[48:51], a[112:115], v[84:87], v[48:51]\n"
    "v_max3_f32 v76, v64, v65, v66\n"
    "v_mfma_f32_16x16x32_bf16 v[56:59], a[192:195], v[84:87], v[56:59]\n"
    "buffer_load_dword v9, v10, s[40:43], s58 offen sc1\n"
    "ds_read_b128 v[84:87], v1 offset:30720\n"
    "s_waitcnt lgkmcnt(4)\n"
    "v_mfma_f32_16x16x32_bf16 v[44:47], a[32:35], v[88:91], v[44:47]\n"
    "v_max3_f32 v76, v76, v67, v68\n"
    "v_max3_f32 v76, v76, v69, v70\n"
    "v_mfma_f32_16x16x32_bf16 v[52:55], a[112:115], v[88:91], v[52:55]\n"
    "v_max3_f32 v76, v76, v71, v72\n"
    "v_max3_f32 v76, v76, v73, v74\n"
    "v_mfma_f32_16x16x32_bf16 v[60:63], a[192:195], v[88:91], v[60:63]\n"
    "buffer_load_dwordx4 v[116:119], v0, s[40:43], s54 offen sc1\n"
    "ds_read_b128 v[88:91], v1 offset:11264\n"
    "s_waitcnt lgkmcnt(4)\n"
    "v_mfma_f32_16x16x32_bf16 v[40:43], a[36:39], v[92:95], v[40:43]\n"
    "v_max_f32 v76, v76, v75\n"
    "v_mov_b32 v77, v76\n"
    "v_mfma_f32_16x16x32_bf16 v[48:51], a[116:119], v[92:95], v[48:51]\n"
    "s_nop 0\n"
    "v_permlane16_swap_b32 v76, v77\n"
    "v_mfma_f32_16x16x32_bf16 v[56:59], a[196:199], v[92:95], v[56:59]\n"
    "buffer_load_dwordx4 v[120:123], v0, s[40:43], s54 offen offset:1024 sc1\n"
    "ds_read_b128 v[92:95], v1 offset:31744\n"
    "s_waitcnt lgkmcnt(4)\n"
    "v_mfma_f32_16x16x32_bf16 v[44:47], a[36:39], v[96:99], v[44:47]\n"
    "v_max_f32 v76, v76, v77\n"
    "v_mov_b32 v77, v76\n"
    "v_mfma_f32_16x16x32_bf16 v[52:55], a[116:119], v[96:99], v[52:55]\n"
    "s_nop 0\n"
    "v_permlane32_swap_b32 v76, v77\n"
    "v_mfma_f32_16x16x32_bf16 v[60:63], a[196:199], v[96:99], v[60:63]\n"
    "buffer_load_dwordx4 v[124:127], v0, s[40:43], s54 offen offset:3072 sc1\n"
    "ds_read_b128 v[96:99], v1 offset:12288\n"
    "s_waitcnt lgkmcnt(4)\n"
    "v_mfma_f32_16x16x32_bf16 v[40:43], a[40:43], v[80:83], v[40:43]\n"
    "v_max_f32 v76, v76, v77\n"
    "v_mul_f32 v76, 0xbdd53b95, v76\n"
    "v_mfma_f32_16x16x32_bf16 v[48:51], a[120:123], v[80:83], v[48:51]\n"
    "v_fmamk_f32 v64, v64, 0x3dd53b95, v76\n"
    "v_fmamk_f32 v65, v65, 0x3dd53b95, v76\n"
    "v_mfma_f32_16x16x32_bf16 v[56:59], a[200:203], v[80:83], v[56:59]\n"
    "buffer_load_dwordx4 v[128:131], v0, s[40:43], s55 offen sc1\n"
    "ds_read_b128 v[80:83], v1 offset:32768\n"
    "s_waitcnt lgkmcnt(4)\n"
    "v_mfma_f32_16x16x32_bf16 v[44:47], a[40:43], v[84:87], v[44:47]\n"
    "v_fmamk_f32 v66, v66, 0x3dd53b95, v76\n"
    "v_fmamk_f32 v67, v67, 0x3dd53b95, v76\n"
    "v_mfma_f32_16x16x32_bf16 v[52:55], a[120:123], v[84:87], v[52:55]\n"
    "v_fmamk_f32 v68, v68, 0x3dd53b95, v76\n"
    "v_fmamk_f32 v69, v69, 0x3dd53b95, v76\n"
    "v_mfma_f32_16x16x32_bf16 v[60:63], a[200:203], v[84:87], v[60:63]\n"
    "buffer_load_dwordx4 v[132:135], v0, s[40:43], s55 offen offset:2048 sc1\n"
    "ds_read_b128 v[84:87], v1 offset:13312\n"
    "s_waitcnt lgkmcnt(4)\n"
    "v_mfma_f32_16x16x32_bf16 v[40:43], a[44:47], v[88:91], v[40:43]\n"
    "v_fmamk_f32 v70, v70, 0x3dd53b95, v76\n"
    "v_fmamk_f32 v71, v71, 0x3dd53b95, v76\n"
    "v_mfma_f32_16x16x32_bf16 v[48:51], a[124:127], v[88:91], v[48:51]\n"
    "v_fmamk_f32 v72, v72, 0x3dd53b95, v76\n"
    "v_fmamk_f32 v73, v73, 0x3dd53b95, v76\n"
    "v_mfma_f32_16x16x32_bf16 v[56:59], a[204:207], v[88:91], v[56:59]\n"
    "buffer_load_dwordx4 v[136:139], v0, s[40:43], s55 offen offset:3072 sc1\n"
    "ds_read_b128 v[88:91], v1 offset:33792\n"
    "s_waitcnt lgkmcnt(4)\n"
    "v_mfma_f32_16x16x32_bf16 v[44:47], a[44:47], v[92:95], v[44:47]\n"
    "v_fmamk_f32 v74, v74, 0x3dd53b95, v76\n"
    "v_fmamk_f32 v75, v75, 0x3dd53b95, v76\n"
    "v_mfma_f32_16x16x32_bf16 v[52:55], a[124:127], v[92:95], v[52:55]\n"
    "v_exp_f32 v64, v64\n"
    "v_mfma_f32_16x16x32_bf16 v[60:63], a[204:207], v[92:95], v[60:63]\n"
    "buffer_load_dwordx4 v[140:143], v0, s[40:43], s56 offen offset:1024 sc1\n"
    "ds_read_b128 v[92:95], v1 offset:14336\n"
    "s_waitcnt lgkmcnt(4)\n"
    "v_mfma_f32_16x16x32_bf16 v[40:43], a[48:51], v[96:99], v[40:43]\n"
    "v_exp_f32 v65, v65\n"
    "v_mfma_f32_16x16x32_bf16 v[48:51], a[128:131], v[96:99], v[48:51]\n"
    "v_exp_f32 v66, v66\n"
    "v_mfma_f32_16x16x32_bf16 v[56:59], a[208:211], v[96:99], v[56:59]\n"
    "buffer_load_dwordx4 v[144:147], v0, s[40:43], s56 offen offset:2048 sc1\n"
    "ds_read_b128 v[96:99], v1 offset:34816\n"
    "s_waitcnt lgkmcnt(4)\n"
    "v_mfma_f32_16x16x32_bf16 v[44:47], a[48:51], v[80:83], v[44:47]\n"
    "v_exp_f32 v67, v67\n"
    "v_mfma_f32_16x16x32_bf16 v[52:55], a[128:131], v[80:83], v[52:55]\n"
    "v_exp_f32 v68, v68\n"
    "v_mfma_f32_16x16x32_bf16 v[60:63], a[208:211], v[80:83], v[60:63]\n"
    "buffer_load_dwordx4 v[148:151], v0, s[40:43], s54 offen offset:2048 sc1\n"
    "ds_read_b128 v[80:83], v1 offset:15360\n"
    "s_waitcnt lgkmcnt(4)\n"
    "v_mfma_f32_16x16x32_bf16 v[40:43], a[52:55], v[84:87], v[40:43]\n"
    "v_exp_f32 v69, v69\n"
    "v_mfma_f32_16x16x32_bf16 v[48:51], a[132:135], v[84:87], v[48:51]\n"
    "v_exp_f32 v70, v70\n"
    "v_mfma_f32_16x16x32_bf16 v[56:59], a[212:215], v[84:87], v[56:59]\n"
    "buffer_load_dwordx4 v[152:155], v0, s[40:43], s55 offen offset:1024 sc1\n"
    "ds_read_b128 v[84:87], v1 offset:35840\n"
    "s_waitcnt lgkmcnt(4)\n"
    "v_mfma_f32_16x16x32_bf16 v[44:47], a[52:55], v[88:91], v[44:47]\n"
    "v_exp_f32 v71, v71\n"
    "v_mfma_f32_16x16x32_bf16 v[52:55], a[132:135], v[88:91], v[52:55]\n"
    "v_exp_f32 v72, v72\n"
    "v_mfma_f32_16x16x32_bf16 v[60:63], a[212:215], v[88:91], v[60:63]\n"
    "buffer_load_dwordx4 v[156:159], v0, s[40:43], s56 offen sc1\n"
    "ds_read_b128 v[88:91], v1 offset:16384\n"
    "s_waitcnt lgkmcnt(4)\n"
    "v_mfma_f32_16x16x32_bf16 v[40:43], a[56:59], v[92:95], v[40:43]\n"
    "v_exp_f32 v73, v73\n"
    "v_mfma_f32_16x16x32_bf16 v[48:51], a[136:139], v[92:95], v[48:51]\n"
    "v_exp_f32 v74, v74\n"
    "v_mfma_f32_16x16x32_bf16 v[56:59], a[216:219], v[92:95], v[56:59]\n"
    "buffer_load_dwordx4 v[160:163], v0, s[40:43], s56 offen offset:3072 sc1\n"
    "s_add_u32 s54, s54, 393216\n"
    "s_add_u32 s55, s55, 393216\n"
    "s_add_u32 s56, s56, 393216\n"
    "ds_read_b128 v[92:95], v1 offset:36864\n"
    "s_waitcnt lgkmcnt(4)\n"
    "v_mfma_f32_16x16x32_bf16 v[44:47], a[56:59], v[96:99], v[44:47]\n"
    "v_exp_f32 v75, v75\n"
    "v_mfma_f32_16x16x32_bf16 v[52:55], a[136:139], v[96:99], v[52:55]\n"
    "v_add_f32 v76, v64, v65\n"
    "v_add_f32 v76, v76, v66\n"
    "v_mfma_f32_16x16x32_bf16 v[60:63], a[216:219], v[96:99], v[60:63]\n"
    "s_waitcnt vmcnt(13) lgkmcnt(4)\n"
    "s_barrier\n"
    "buffer_atomic_add v226, v3, s[40:43], s29 offen\n"
    "s_and_b32 s48, s47, 63\n"
    "s_cmp_lt_u32 s48, 62\n"
    "s_cselect_b32 s48, 1, 0\n"
    "s_mov_b32 s60, 0\n"
    "v_cmp_gt_u32 vcc, s48, v224\n"
    "s_nop 4\n"
    "s_cbranch_vccz hpq_xf1_ok_%=\n"
    "hpq_xf1_poll_%=:\n"
    "buffer_load_dword v224, v225, s[40:43], s58 offen sc1\n"
    "s_waitcnt vmcnt(0)\n"
    "v_cmp_gt_u32 vcc, s48, v224\n"
    "s_nop 4\n"
    "s_cbranch_vccz hpq_xf1_ok_%=\n"
    "s_sleep 2\n"
    "s_add_u32 s60, s60, 1\n"
    "s_cmp_gt_u32 s60, 65536\n"
    "s_cbranch_scc1 hp_abort_%=\n"
    "s_and_b32 s59, s60, 63\n"
    "s_cbranch_scc1 hpq_xf1_poll_%=\n"
    "v_mov_b32 v4, 1024\n"
    "buffer_load_dword v4, v4, s[40:43], s58 offen sc1\n"
    "s_waitcnt vmcnt(0)\n"
    "v_readfirstlane_b32 s59, v4\n"
    "s_nop 0\n"
    "s_cmp_lg_u32 s59, 0\n"
    "s_nop 2\n"
    "s_cbranch_scc1 hp_end_%=\n"
    "s_branch hpq_xf1_poll_%=\n"
    "hpq_xf1_ok_%=:\n"
    "v_add_u32 v225, 128, v225\n"
    "ds_read_b128 v[96:99], v1 offset:17408\n"
    "s_waitcnt lgkmcnt(4)\n"
    "v_mfma_f32_16x16x32_bf16 v[40:43], a[60:63], v[80:83], v[40:43]\n"
    "ds_read_b128 v[188:191], v8 offset:0\n"
    "v_add_f32 v76, v76, v67\n"
    "s_add_u32 m0, s61, 0\n"
    "s_add_u32 s51, s50, 0\n"
    "buffer_load_dwordx4 v0, s[40:43], s51 offen offset:0 sc1 lds\n"
    "v_mfma_f32_16x16x32_bf16 v[48:51], a[140:143], v[80:83], v[48:51]\n"
    "ds_read_b128 v[192:195], v8 offset:2048\n"
    "v_add_f32 v76, v76, v68\n"
    "v_add_f32 v76, v76, v69\n"
    "v_mfma_f32_16x16x32_bf16 v[56:59], a[220:223], v[80:83], v[56:59]\n"
    "v_add_f32 v76, v76, v70\n"
    "ds_read_b128 v[80:83], v1 offset:37888\n"
    "s_waitcnt lgkmcnt(6)\n"
    "v_mfma_f32_16x16x32_bf16 v[44:47], a[60:63], v[84:87], v[44:47]\n"
    "ds_read_b128 v[196:199], v8 offset:4096\n"
    "v_add_f32 v76, v76, v71\n"
    "buffer_load_dwordx4 v0, s[40:43], s51 offen offset:1024 sc1 lds\n"
    "v_mfma_f32_16x16x32_bf16 v[52:55], a[140:143], v[84:87], v[52:55]\n"
    "ds_read_b128 v[200:203], v8 offset:6144\n"
    "v_add_f32 v76, v76, v72\n"
    "v_add_f32 v76, v76, v73\n"
    "v_mfma_f32_16x16x32_bf16 v[60:63], a[220:223], v[84:87], v[60:63]\n"
    "v_add_f32 v76, v76, v74\n"
    "ds_read_b128 v[84:87], v1 offset:18432\n"
    "s_waitcnt lgkmcnt(8)\n"
    "v_mfma_f32_16x16x32_bf16 v[40:43], a[64:67], v[88:91], v[40:43]\n"
    "ds_read_b128 v[204:207], v8 offset:8192\n"
    "v_add_f32 v76, v76, v75\n"
    "buffer_load_dwordx4 v0, s[40:43], s51 offen offset:2048 sc1 lds\n"
    "v_mfma_f32_16x16x32_bf16 v[48:51], a[144:147], v[88:91], v[48:51]\n"
    "ds_read_b128 v[208:211], v8 offset:10240\n"
    "v_mov_b32 v77, v76\n"
    "s_nop 1\n"
    "v_permlane16_swap_b32 v76, v77\n"
    "v_mfma_f32_16x16x32_bf16 v[56:59], a[224:227], v[88:91], v[56:59]\n"
    "v_add_f32 v76, v76, v77\n"
    "ds_read_b128 v[88:91], v1 offset:38912\n"
    "s_waitcnt lgkmcnt(10)\n"
    "v_mfma_f32_16x16x32_bf16 v[44:47], a[64:67], v[92:95], v[44:47]\n"
    "v_mov_b32 v77, v76\n"
    "buffer_load_dwordx4 v0, s[40:43], s51 offen offset:3072 sc1 lds\n"
    "v_mfma_f32_16x16x32_bf16 v[52:55], a[144:147], v[92:95], v[52:55]\n"
    "v_permlane32_swap_b32 v76, v77\n"
    "v_mfma_f32_16x16x32_bf16 v[60:63], a[224:227], v[92:95], v[60:63]\n"
    "v_add_f32 v76, v76, v77\n"
    "ds_read_b128 v[92:95], v1 offset:19456\n"
    "s_waitcnt lgkmcnt(10)\n"
    "v_mfma_f32_16x16x32_bf16 v[40:43], a[68:71], v[96:99], v[40:43]\n"
    "v_rcp_f32 v76, v76\n"
    "s_add_u32 m0, s61, 4096\n"
    "s_add_u32 s51, s50, 4096\n"
    "buffer_load_dwordx4 v0, s[40:43], s51 offen offset:0 sc1 lds\n"
    "v_mfma_f32_16x16x32_bf16 v[48:51], a[148:151], v[96:99], v[48:51]\n"
    "v_mul_f32 v64, v64, v76\n"
    "v_mul_f32 v65, v65, v76\n"
    "v_mfma_f32_16x16x32_bf16 v[56:59], a[228:231], v[96:99], v[56:59]\n"
    "v_mul_f32 v66, v66, v76\n"
    "ds_read_b128 v[96:99], v1 offset:39936\n"
    "s_waitcnt lgkmcnt(8)\n"
    "v_mfma_f32_16x16x32_bf16 v[44:47], a[68:71], v[80:83], v[44:47]\n"
    "v_mul_f32 v67, v67, v76\n"
    "buffer_load_dwordx4 v0, s[40:43], s51 offen offset:1024 sc1 lds\n"
    "v_mfma_f32_16x16x32_bf16 v[52:55], a[148:151], v[80:83], v[52:55]\n"
    "v_mul_f32 v68, v68, v76\n"
    "v_mul_f32 v69, v69, v76\n"
    "v_mfma_f32_16x16x32_bf16 v[60:63], a[228:231], v[80:83], v[60:63]\n"
    "v_mul_f32 v70, v70, v76\n"
    "s_add_u32 s64, s64, 40960\n"
    "s_cmp_ge_u32 s64, 122880\n"
    "s_cselect_b32 s64, 0, s64\n"
    "v_add_u32 v1, s64, v0\n"
    "ds_read_b128 v[80:83], v1 offset:0\n"
    "s_waitcnt lgkmcnt(6)\n"
    "v_mfma_f32_16x16x32_bf16 v[40:43], a[72:75], v[84:87], v[40:43]\n"
    "v_mul_f32 v71, v71, v76\n"
    "buffer_load_dwordx4 v0, s[40:43], s51 offen offset:2048 sc1 lds\n"
    "v_mfma_f32_16x16x32_bf16 v[48:51], a[152:155], v[84:87], v[48:51]\n"
    "v_mul_f32 v72, v72, v76\n"
    "v_mul_f32 v73, v73, v76\n"
    "v_mfma_f32_16x16x32_bf16 v[56:59], a[232:235], v[84:87], v[56:59]\n"
    "v_mul_f32 v74, v74, v76\n"
    "ds_read_b128 v[84:87], v1 offset:20480\n"
    "s_waitcnt lgkmcnt(4)\n"
    "v_mfma_f32_16x16x32_bf16 v[44:47], a[72:75], v[88:91], v[44:47]\n"
    "v_mul_f32 v75, v75, v76\n"
    "buffer_load_dwordx4 v0, s[40:43], s51 offen offset:3072 sc1 lds\n"
    "v_mfma_f32_16x16x32_bf16 v[52:55], a[152:155], v[88:91], v[52:55]\n"
    "v_cvt_pk_bf16_f32 v228, v64, v65\n"
    "v_cvt_pk_bf16_f32 v229, v66, v67\n"
    "v_mfma_f32_16x16x32_bf16 v[60:63], a[232:235], v[88:91], v[60:63]\n"
    "v_cvt_pk_bf16_f32 v230, v68, v69\n"
    "ds_read_b128 v[88:91], v1 offset:1024\n"
    "s_waitcnt lgkmcnt(4)\n"
    "v_mfma_f32_16x16x32_bf16 v[40:43], a[76:79], v[92:95], v[40:43]\n"
    "v_cvt_pk_bf16_f32 v231, v70, v71\n"
    "s_add_u32 m0, s61, 8192\n"
    "s_add_u32 s51, s50, 8192\n"
    "buffer_load_dwordx4 v0, s[40:43], s51 offen offset:0 sc1 lds\n"
    "v_mfma_f32_16x16x32_bf16 v[48:51], a[156:159], v[92:95], v[48:51]\n"
    "v_cvt_pk_bf16_f32 v232, v72, v73\n"
    "v_cvt_pk_bf16_f32 v233, v74, v75\n"
    "v_mfma_f32_16x16x32_bf16 v[56:59], a[236:239], v[92:95], v[56:59]\n"
    "v_mov_b32 v234, v232\n"
    "ds_read_b128 v[92:95], v1 offset:21504\n"
    "s_waitcnt lgkmcnt(4)\n"
    "v_mfma_f32_16x16x32_bf16 v[44:47], a[76:79], v[96:99], v[44:47]\n"
    "v_mov_b32 v235, v233\n"
    "buffer_load_dwordx4 v0, s[40:43], s51 offen offset:1024 sc1 lds\n"
    "s_add_u32 s50, s50, 40960\n"
    "s_add_u32 s61, s61, 40960\n"
    "s_cmp_ge_u32 s61, 122880\n"
    "s_cselect_b32 s51, 122880, 0\n"
    "s_sub_u32 s61, s61, s51\n"
    "v_mfma_f32_16x16x32_bf16 v[52:55], a[156:159], v[96:99], v[52:55]\n"
    "buffer_store_dwordx4 v[228:231], v0, s[40:43], s26 offen sc1\n"
    "v_permlane16_swap_b32 v232, v234\n"
    "v_mfma_f32_16x16x32_bf16 v[60:63], a[236:239], v[96:99], v[60:63]\n"
    "v_permlane16_swap_b32 v233, v235\n"
    "ds_read_b128 v[96:99], v1 offset:2048\n"
    "buffer_store_dwordx4 v[232:235], v12, s[40:43], s27 offen sc1\n"
    "s_add_u32 s47, s47, 1\n"
    "s_and_b32 s59, s47, 63\n"
    "s_cbranch_scc1 hpq_loop_%=\n"
    "s_waitcnt lgkmcnt(0)\n"
    "v_cvt_pk_bf16_f32 v104, v40, v41\n"
    "v_cvt_pk_bf16_f32 v105, v42, v43\n"
    "v_cvt_pk_bf16_f32 v106, v44, v45\n"
    "v_cvt_pk_bf16_f32 v107, v46, v47\n"
    "ds_write2st64_b64 v6, v[104:105], v[106:107] offset0:24 offset1:26\n"
    "v_cvt_pk_bf16_f32 v108, v48, v49\n"
    "v_cvt_pk_bf16_f32 v109, v50, v51\n"
    "v_cvt_pk_bf16_f32 v110, v52, v53\n"
    "v_cvt_pk_bf16_f32 v111, v54, v55\n"
    "ds_write2st64_b64 v2, v[108:109], v[110:111] offset0:24 offset1:26\n"
    "v_cvt_pk_bf16_f32 v112, v56, v57\n"
    "v_cvt_pk_bf16_f32 v113, v58, v59\n"
    "v_cvt_pk_bf16_f32 v114, v60, v61\n"
    "v_cvt_pk_bf16_f32 v115, v62, v63\n"
    "ds_write2st64_b64 v7, v[112:113], v[114:115] offset0:24 offset1:26\n"
    "s_waitcnt lgkmcnt(0)\n"
    "s_barrier\n"
    "s_cmp_gt_u32 s45, 1\n"
    "s_cbranch_scc1 hp_ret_%=\n"
    "s_waitcnt vmcnt(13)\n"
    "s_mov_b32 s29, s52\n"
    "s_waitcnt vmcnt(25)\n"
    "s_add_u32 s48, s63, 1\n"
    "s_cmp_ge_i32 s63, s65\n"
    "s_cselect_b32 s48, s48, 0\n"
    "s_cselect_b32 s49, s30, 0x40000000\n"
    "v_cmp_gt_u32 vcc, s48, v9\n"
    "s_cbranch_vccz hpq_kme0_ok_%=\n"
    "s_waitcnt vmcnt(0)\n"
    "buffer_atomic_add v226, v3, s[40:43], s29 offen\n"
    "s_mov_b32 s29, 0x40000000\n"
    "s_mov_b32 s60, 0\n"
    "v_cmp_gt_u32 vcc, s48, v9\n"
    "s_nop 4\n"
    "s_cbranch_vccz hpq_kme0_ok_%=\n"
    "hpq_kme0_poll_%=:\n"
    "buffer_load_dword v9, v10, s[40:43], s58 offen sc1\n"
    "s_waitcnt vmcnt(0)\n"
    "v_cmp_gt_u32 vcc, s48, v9\n"
    "s_nop 4\n"
    "s_cbranch_vccz hpq_kme0_ok_%=\n"
    "s_sleep 2\n"
    "s_add_u32 s60, s60, 1\n"
    "s_cmp_gt_u32 s60, 65536\n"
    "s_cbranch_scc1 hp_abort_%=\n"
    "s_and_b32 s59, s60, 63\n"
    "s_cbranch_scc1 hpq_kme0_poll_%=\n"
    "v_mov_b32 v4, 1024\n"
    "buffer_load_dword v4, v4, s[40:43], s58 offen sc1\n"
    "s_waitcnt vmcnt(0)\n"
    "v_readfirstlane_b32 s59, v4\n"
    "s_nop 0\n"
    "s_cmp_lg_u32 s59, 0\n"
    "s_nop 2\n"
    "s_cbranch_scc1 hp_end_%=\n"
    "s_branch hpq_kme0_poll_%=\n"
    "hpq_kme0_ok_%=:\n"
    "s_movk_i32 s48, 32\n"
    "s_mov_b32 s60, 0\n"
    "v_cmp_gt_u32 vcc, s48, v236\n"
    "s_nop 4\n"
    "s_cbranch_vccz hpq_kxe0_ok_%=\n"
    "hpq_kxe0_poll_%=:\n"
    "buffer_load_dword v236, v227, s[40:43], s58 offen sc1\n"
    "s_waitcnt vmcnt(0)\n"
    "v_cmp_gt_u32 vcc, s48, v236\n"
    "s_nop 4\n"
    "s_cbranch_vccz hpq_kxe0_ok_%=\n"
    "s_sleep 2\n"
    "s_add_u32 s60, s60, 1\n"
    "s_cmp_gt_u32 s60, 65536\n"
    "s_cbranch_scc1 hp_abort_%=\n"
    "s_and_b32 s59, s60, 63\n"
    "s_cbranch_scc1 hpq_kxe0_poll_%=\n"
    "v_mov_b32 v4, 1024\n"
    "buffer_load_dword v4, v4, s[40:43], s58 offen sc1\n"
    "s_waitcnt vmcnt(0)\n"
    "v_readfirstlane_b32 s59, v4\n"
    "s_nop 0\n"
    "s_cmp_lg_u32 s59, 0\n"
    "s_nop 2\n"
    "s_cbranch_scc1 hp_end_%=\n"
    "s_branch hpq_kxe0_poll_%=\n"
    "hpq_kxe0_ok_%=:\n"
    "v_add_u32 v227, 128, v227\n"
    "s_or_b32 s57, s34, s49\n"
    "s_add_u32 s34, s34, 98304\n"
    "s_or_b32 s52, s35, s49\n"
    "s_or_b32 s26, s36, s49\n"
    "s_or_b32 s27, s37, s49\n"
    "s_add_u32 s35, s35, 1536\n"
    "s_add_u32 s36, s36, 65536\n"
    "s_add_u32 s37, s37, 16384\n"
    "s_add_u32 s63, s63, 1\n"
    "buffer_load_dwordx4 v[164:167], v11, s[40:43], s57 offen sc1\n"
    "buffer_load_dwordx4 v[168:171], v11, s[40:43], s57 offen offset:512 sc1\n"
    "buffer_load_dwordx4 v[172:175], v11, s[40:43], s57 offen offset:1024 sc1\n"
    "buffer_load_dwordx4 v[176:179], v11, s[40:43], s57 offen offset:1536 sc1\n"
    "buffer_load_dwordx4 v[180:183], v11, s[40:43], s57 offen offset:2048 sc1\n"
    "buffer_load_dwordx4 v[184:187], v11, s[40:43], s57 offen offset:2560 sc1\n"
    "v_swap_b32 v118, v120\n"
    "v_swap_b32 v119, v121\n"
    "v_swap_b32 v126, v128\n"
    "v_swap_b32 v127, v129\n"
    "v_swap_b32 v134, v136\n"
    "v_swap_b32 v135, v137\n"
    "v_swap_b32 v142, v144\n"
    "v_swap_b32 v143, v145\n"
    "v_swap_b32 v150, v152\n"
    "v_swap_b32 v151, v153\n"
    "v_swap_b32 v158, v160\n"
    "v_swap_b32 v159, v161\n"
    "v_mfma_f32_16x16x32_bf16 v[64:67], v[116:119], v[188:191], v[212:215]\n"
    "v_mfma_f32_16x16x32_bf16 v[68:71], v[120:123], v[188:191], v[216:219]\n"
    "s_waitcnt vmcnt(5)\n"
    "v_mfma_f32_16x16x32_bf16 v[72:75], v[164:167], v[188:191], v[220:223]\n"
    "v_mfma_f32_16x16x32_bf16 v[64:67], v[124:127], v[192:195], v[64:67]\n"
    "v_mfma_f32_16x16x32_bf16 v[68:71], v[128:131], v[192:195], v[68:71]\n"
    "s_waitcnt vmcnt(4)\n"
    "v_mfma_f32_16x16x32_bf16 v[72:75], v[168:171], v[192:195], v[72:75]\n"
    "v_mfma_f32_16x16x32_bf16 v[64:67], v[132:135], v[196:199], v[64:67]\n"
    "v_mfma_f32_16x16x32_bf16 v[68:71], v[136:139], v[196:199], v[68:71]\n"
    "s_waitcnt vmcnt(3)\n"
    "v_mfma_f32_16x16x32_bf16 v[72:75], v[172:175], v[196:199], v[72:75]\n"
    "v_mfma_f32_16x16x32_bf16 v[64:67], v[140:143], v[200:203], v[64:67]\n"
    "v_mfma_f32_16x16x32_bf16 v[68:71], v[144:147], v[200:203], v[68:71]\n"
    "s_waitcnt vmcnt(2)\n"
    "v_mfma_f32_16x16x32_bf16 v[72:75], v[176:179], v[200:203], v[72:75]\n"
    "v_mfma_f32_16x16x32_bf16 v[64:67], v[148:151], v[204:207], v[64:67]\n"
    "v_mfma_f32_16x16x32_bf16 v[68:71], v[152:155], v[204:207], v[68:71]\n"
    "s_waitcnt vmcnt(1)\n"
    "v_mfma_f32_16x16x32_bf16 v[72:75], v[180:183], v[204:207], v[72:75]\n"
    "v_mfma_f32_16x16x32_bf16 v[64:67], v[156:159], v[208:211], v[64:67]\n"
    "v_mfma_f32_16x16x32_bf16 v[68:71], v[160:163], v[208:211], v[68:71]\n"
    "s_waitcnt vmcnt(0)\n"
    "v_mfma_f32_16x16x32_bf16 v[72:75], v[184:187], v[208:211], v[72:75]\n"
    "buffer_load_dwordx4 v[116:119], v0, s[40:43], s54 offen sc1\n"
    "buffer_load_dwordx4 v[120:123], v0, s[40:43], s54 offen offset:1024 sc1\n"
    "buffer_load_dwordx4 v[124:127], v0, s[40:43], s54 offen offset:3072 sc1\n"
    "buffer_load_dwordx4 v[128:131], v0, s[40:43], s55 offen sc1\n"
    "buffer_load_dwordx4 v[132:135], v0, s[40:43], s55 offen offset:2048 sc1\n"
    "buffer_load_dwordx4 v[136:139], v0, s[40:43], s55 offen offset:3072 sc1\n"
    "buffer_load_dwordx4 v[140:143], v0, s[40:43], s56 offen offset:1024 sc1\n"
    "buffer_load_dwordx4 v[144:147], v0, s[40:43], s56 offen offset:2048 sc1\n"
    "buffer_load_dwordx4 v[148:151], v0, s[40:43], s54 offen offset:2048 sc1\n"
    "buffer_load_dwordx4 v[152:155], v0, s[40:43], s55 offen offset:1024 sc1\n"
    "buffer_load_dwordx4 v[156:159], v0, s[40:43], s56 offen sc1\n"
    "buffer_load_dwordx4 v[160:163], v0, s[40:43], s56 offen offset:3072 sc1\n"
    "s_add_u32 s54, s54, 393216\n"
    "s_add_u32 s55, s55, 393216\n"
    "s_add_u32 s56, s56, 393216\n"
    "v_max3_f32 v76, v64, v65, v66\n"
    "v_max3_f32 v76, v76, v67, v68\n"
    "v_max3_f32 v76, v76, v69, v70\n"
    "v_max3_f32 v76, v76, v71, v72\n"
    "v_max3_f32 v76, v76, v73, v74\n"
    "v_max_f32 v76, v76, v75\n"
    "v_mov_b32 v77, v76\n"
    "s_nop 1\n"
    "v_permlane16_swap_b32 v76, v77\n"
    "v_max_f32 v76, v76, v77\n"
    "v_mov_b32 v77, v76\n"
    "s_nop 1\n"
    "v_permlane32_swap_b32 v76, v77\n"
    "v_max_f32 v76, v76, v77\n"
    "v_mul_f32 v76, 0xbdd53b95, v76\n"
    "v_fmamk_f32 v64, v64, 0x3dd53b95, v76\n"
    "v_fmamk_f32 v65, v65, 0x3dd53b95, v76\n"
    "v_fmamk_f32 v66, v66, 0x3dd53b95, v76\n"
    "v_fmamk_f32 v67, v67, 0x3dd53b95, v76\n"
    "v_fmamk_f32 v68, v68, 0x3dd53b95, v76\n"
    "v_fmamk_f32 v69, v69, 0x3dd53b95, v76\n"
    "v_fmamk_f32 v70, v70, 0x3dd53b95, v76\n"
    "v_fmamk_f32 v71, v71, 0x3dd53b95, v76\n"
    "v_fmamk_f32 v72, v72, 0x3dd53b95, v76\n"
    "v_fmamk_f32 v73, v73, 0x3dd53b95, v76\n"
    "v_fmamk_f32 v74, v74, 0x3dd53b95, v76\n"
    "v_fmamk_f32 v75, v75, 0x3dd53b95, v76\n"
    "v_exp_f32 v64, v64\n"
    "v_exp_f32 v65, v65\n"
    "v_exp_f32 v66, v66\n"
    "v_exp_f32 v67, v67\n"
    "v_exp_f32 v68, v68\n"
    "v_exp_f32 v69, v69\n"
    "v_exp_f32 v70, v70\n"
    "v_exp_f32 v71, v71\n"
    "v_exp_f32 v72, v72\n"
    "v_exp_f32 v73, v73\n"
    "v_exp_f32 v74, v74\n"
    "v_exp_f32 v75, v75\n"
    "v_add_f32 v76, v64, v65\n"
    "v_add_f32 v76, v76, v66\n"
    "v_add_f32 v76, v76, v67\n"
    "v_add_f32 v76, v76, v68\n"
    "v_add_f32 v76, v76, v69\n"
    "v_add_f32 v76, v76, v70\n"
    "v_add_f32 v76, v76, v71\n"
    "v_add_f32 v76, v76, v72\n"
    "v_add_f32 v76, v76, v73\n"
    "v_add_f32 v76, v76, v74\n"
    "v_add_f32 v76, v76, v75\n"
    "v_mov_b32 v77, v76\n"
    "s_nop 1\n"
    "v_permlane16_swap_b32 v76, v77\n"
    "v_add_f32 v76, v76, v77\n"
    "v_mov_b32 v77, v76\n"
    "s_nop 1\n"
    "v_permlane32_swap_b32 v76, v77\n"
    "v_add_f32 v76, v76, v77\n"
    "v_rcp_f32 v76, v76\n"
    "s_nop 0\n"
    "v_mul_f32 v64, v64, v76\n"
    "v_mul_f32 v65, v65, v76\n"
    "v_mul_f32 v66, v66, v76\n"
    "v_mul_f32 v67, v67, v76\n"
    "v_mul_f32 v68, v68, v76\n"
    "v_mul_f32 v69, v69, v76\n"
    "v_mul_f32 v70, v70, v76\n"
    "v_mul_f32 v71, v71, v76\n"
    "v_mul_f32 v72, v72, v76\n"
    "v_mul_f32 v73, v73, v76\n"
    "v_mul_f32 v74, v74, v76\n"
    "v_mul_f32 v75, v75, v76\n"
    "v_cvt_pk_bf16_f32 v228, v64, v65\n"
    "v_cvt_pk_bf16_f32 v229, v66, v67\n"
    "v_cvt_pk_bf16_f32 v230, v68, v69\n"
    "v_cvt_pk_bf16_f32 v231, v70, v71\n"
    "v_cvt_pk_bf16_f32 v232, v72, v73\n"
    "v_cvt_pk_bf16_f32 v233, v74, v75\n"
    "v_mov_b32 v234, v232\n"
    "v_mov_b32 v235, v233\n"
    "buffer_store_dwordx4 v[228:231], v0, s[40:43], s26 offen sc1\n"
    "v_permlane16_swap_b32 v232, v234\n"
    "v_permlane16_swap_b32 v233, v235\n"
    "buffer_store_dwordx4 v[232:235], v12, s[40:43], s27 offen sc1\n"
    "s_waitcnt vmcnt(2)\n"
    "buffer_atomic_add v226, v3, s[40:43], s29 offen\n"
    "s_mov_b32 s29, s52\n"
    "s_waitcnt vmcnt(25)\n"
    "s_add_u32 s48, s63, 1\n"
    "s_cmp_ge_i32 s63, s65\n"
    "s_cselect_b32 s48, s48, 0\n"
    "s_cselect_b32 s49, s30, 0x40000000\n"
    "v_cmp_gt_u32 vcc, s48, v9\n"
    "s_cbranch_vccz hpq_kme1_ok_%=\n"
    "s_waitcnt vmcnt(0)\n"
    "buffer_atomic_add v226, v3, s[40:43], s29 offen\n"
    "s_mov_b32 s29, 0x40000000\n"
    "s_mov_b32 s60, 0\n"
    "v_cmp_gt_u32 vcc, s48, v9\n"
    "s_nop 4\n"
    "s_cbranch_vccz hpq_kme1_ok_%=\n"
    "hpq_kme1_poll_%=:\n"
    "buffer_load_dword v9, v10, s[40:43], s58 offen sc1\n"
    "s_waitcnt vmcnt(0)\n"
    "v_cmp_gt_u32 vcc, s48, v9\n"
    "s_nop 4\n"
    "s_cbranch_vccz hpq_kme1_ok_%=\n"
    "s_sleep 2\n"
    "s_add_u32 s60, s60, 1\n"
    "s_cmp_gt_u32 s60, 65536\n"
    "s_cbranch_scc1 hp_abort_%=\n"
    "s_and_b32 s59, s60, 63\n"
    "s_cbranch_scc1 hpq_kme1_poll_%=\n"
    "v_mov_b32 v4, 1024\n"
    "buffer_load_dword v4, v4, s[40:43], s58 offen sc1\n"
    "s_waitcnt vmcnt(0)\n"
    "v_readfirstlane_b32 s59, v4\n"
    "s_nop 0\n"
    "s_cmp_lg_u32 s59, 0\n"
    "s_nop 2\n"
    "s_cbranch_scc1 hp_end_%=\n"
    "s_branch hpq_kme1_poll_%=\n"
    "hpq_kme1_ok_%=:\n"
    "s_or_b32 s57, s34, s49\n"
    "s_add_u32 s34, s34, 98304\n"
    "s_or_b32 s52, s35, s49\n"
    "s_or_b32 s26, s36, s49\n"
    "s_or_b32 s27, s37, s49\n"
    "s_add_u32 s35, s35, 1536\n"
    "s_add_u32 s36, s36, 65536\n"
    "s_add_u32 s37, s37, 16384\n"
    "s_add_u32 s63, s63, 1\n"
    "buffer_load_dwordx4 v[164:167], v11, s[40:43], s57 offen sc1\n"
    "buffer_load_dwordx4 v[168:171], v11, s[40:43], s57 offen offset:512 sc1\n"
    "buffer_load_dwordx4 v[172:175], v11, s[40:43], s57 offen offset:1024 sc1\n"
    "buffer_load_dwordx4 v[176:179], v11, s[40:43], s57 offen offset:1536 sc1\n"
    "buffer_load_dwordx4 v[180:183], v11, s[40:43], s57 offen offset:2048 sc1\n"
    "buffer_load_dwordx4 v[184:187], v11, s[40:43], s57 offen offset:2560 sc1\n"
    "ds_read_b128 v[188:191], v8 offset:12288\n"
    "ds_read_b128 v[192:195], v8 offset:14336\n"
    "ds_read_b128 v[196:199], v8 offset:16384\n"
    "ds_read_b128 v[200:203], v8 offset:18432\n"
    "ds_read_b128 v[204:207], v8 offset:20480\n"
    "ds_read_b128 v[208:211], v8 offset:22528\n"
    "v_swap_b32 v118, v120\n"
    "v_swap_b32 v119, v121\n"
    "v_swap_b32 v126, v128\n"
    "v_swap_b32 v127, v129\n"
    "v_swap_b32 v134, v136\n"
    "v_swap_b32 v135, v137\n"
    "v_swap_b32 v142, v144\n"
    "v_swap_b32 v143, v145\n"
    "v_swap_b32 v150, v152\n"
    "v_swap_b32 v151, v153\n"
    "v_swap_b32 v158, v160\n"
    "v_swap_b32 v159, v161\n"
    "s_waitcnt lgkmcnt(5)\n"
    "v_mfma_f32_16x16x32_bf16 v[64:67], v[116:119], v[188:191], v[212:215]\n"
    "v_mfma_f32_16x16x32_bf16 v[68:71], v[120:123], v[188:191], v[216:219]\n"
    "s_waitcnt vmcnt(5)\n"
    "v_mfma_f32_16x16x32_bf16 v[72:75], v[164:167], v[188:191], v[220:223]\n"
    "s_waitcnt lgkmcnt(4)\n"
    "v_mfma_f32_16x16x32_bf16 v[64:67], v[124:127], v[192:195], v[64:67]\n"
    "v_mfma_f32_16x16x32_bf16 v[68:71], v[128:131], v[192:195], v[68:71]\n"
    "s_waitcnt vmcnt(4)\n"
    "v_mfma_f32_16x16x32_bf16 v[72:75], v[168:171], v[192:195], v[72:75]\n"
    "s_waitcnt lgkmcnt(3)\n"
    "v_mfma_f32_16x16x32_bf16 v[64:67], v[132:135], v[196:199], v[64:67]\n"
    "v_mfma_f32_16x16x32_bf16 v[68:71], v[136:139], v[196:199], v[68:71]\n"
    "s_waitcnt vmcnt(3)\n"
    "v_mfma_f32_16x16x32_bf16 v[72:75], v[172:175], v[196:199], v[72:75]\n"
    "s_waitcnt lgkmcnt(2)\n"
    "v_mfma_f32_16x16x32_bf16 v[64:67], v[140:143], v[200:203], v[64:67]\n"
    "v_mfma_f32_16x16x32_bf16 v[68:71], v[144:147], v[200:203], v[68:71]\n"
    "s_waitcnt vmcnt(2)\n"
    "v_mfma_f32_16x16x32_bf16 v[72:75], v[176:179], v[200:203], v[72:75]\n"
    "s_waitcnt lgkmcnt(1)\n"
    "v_mfma_f32_16x16x32_bf16 v[64:67], v[148:151], v[204:207], v[64:67]\n"
    "v_mfma_f32_16x16x32_bf16 v[68:71], v[152:155], v[204:207], v[68:71]\n"
    "s_waitcnt vmcnt(1)\n"
    "v_mfma_f32_16x16x32_bf16 v[72:75], v[180:183], v[204:207], v[72:75]\n"
    "s_waitcnt lgkmcnt(0)\n"
    "v_mfma_f32_16x16x32_bf16 v[64:67], v[156:159], v[208:211], v[64:67]\n"
    "v_mfma_f32_16x16x32_bf16 v[68:71], v[160:163], v[208:211], v[68:71]\n"
    "s_waitcnt vmcnt(0)\n"
    "v_mfma_f32_16x16x32_bf16 v[72:75], v[184:187], v[208:211], v[72:75]\n"
    "s_nop 4\n"
    "v_max3_f32 v76, v64, v65, v66\n"
    "v_max3_f32 v76, v76, v67, v68\n"
    "v_max3_f32 v76, v76, v69, v70\n"
    "v_max3_f32 v76, v76, v71, v72\n"
    "v_max3_f32 v76, v76, v73, v74\n"
    "v_max_f32 v76, v76, v75\n"
    "v_mov_b32 v77, v76\n"
    "s_nop 1\n"
    "v_permlane16_swap_b32 v76, v77\n"
    "v_max_f32 v76, v76, v77\n"
    "v_mov_b32 v77, v76\n"
    "s_nop 1\n"
    "v_permlane32_swap_b32 v76, v77\n"
    "v_max_f32 v76, v76, v77\n"
    "v_mul_f32 v76, 0xbdd53b95, v76\n"
    "v_fmamk_f32 v64, v64, 0x3dd53b95, v76\n"
    "v_fmamk_f32 v65, v65, 0x3dd53b95, v76\n"
    "v_fmamk_f32 v66, v66, 0x3dd53b95, v76\n"
    "v_fmamk_f32 v67, v67, 0x3dd53b95, v76\n"
    "v_fmamk_f32 v68, v68, 0x3dd53b95, v76\n"
    "v_fmamk_f32 v69, v69, 0x3dd53b95, v76\n"
    "v_fmamk_f32 v70, v70, 0x3dd53b95, v76\n"
    "v_fmamk_f32 v71, v71, 0x3dd53b95, v76\n"
    "v_fmamk_f32 v72, v72, 0x3dd53b95, v76\n"
    "v_fmamk_f32 v73, v73, 0x3dd53b95, v76\n"
    "v_fmamk_f32 v74, v74, 0x3dd53b95, v76\n"
    "v_fmamk_f32 v75, v75, 0x3dd53b95, v76\n"
    "v_exp_f32 v64, v64\n"
    "v_exp_f32 v65, v65\n"
    "v_exp_f32 v66, v66\n"
    "v_exp_f32 v67, v67\n"
    "v_exp_f32 v68, v68\n"
    "v_exp_f32 v69, v69\n"
    "v_exp_f32 v70, v70\n"
    "v_exp_f32 v71, v71\n"
    "v_exp_f32 v72, v72\n"
    "v_exp_f32 v73, v73\n"
    "v_exp_f32 v74, v74\n"
    "v_exp_f32 v75, v75\n"
    "v_add_f32 v76, v64, v65\n"
    "v_add_f32 v76, v76, v66\n"
    "v_add_f32 v76, v76, v67\n"
    "v_add_f32 v76, v76, v68\n"
    "v_add_f32 v76, v76, v69\n"
    "v_add_f32 v76, v76, v70\n"
    "v_add_f32 v76, v76, v71\n"
    "v_add_f32 v76, v76, v72\n"
    "v_add_f32 v76, v76, v73\n"
    "v_add_f32 v76, v76, v74\n"
    "v_add_f32 v76, v76, v75\n"
    "v_mov_b32 v77, v76\n"
    "s_nop 1\n"
    "v_permlane16_swap_b32 v76, v77\n"
    "v_add_f32 v76, v76, v77\n"
    "v_mov_b32 v77, v76\n"
    "s_nop 1\n"
    "v_permlane32_swap_b32 v76, v77\n"
    "v_add_f32 v76, v76, v77\n"
    "v_rcp_f32 v76, v76\n"
    "s_nop 0\n"
    "v_mul_f32 v64, v64, v76\n"
    "v_mul_f32 v65, v65, v76\n"
    "v_mul_f32 v66, v66, v76\n"
    "v_mul_f32 v67, v67, v76\n"
    "v_mul_f32 v68, v68, v76\n"
    "v_mul_f32 v69, v69, v76\n"
    "v_mul_f32 v70, v70, v76\n"
    "v_mul_f32 v71, v71, v76\n"
    "v_mul_f32 v72, v72, v76\n"
    "v_mul_f32 v73, v73, v76\n"
    "v_mul_f32 v74, v74, v76\n"
    "v_mul_f32 v75, v75, v76\n"
    "v_cvt_pk_bf16_f32 v228, v64, v65\n"
    "v_cvt_pk_bf16_f32 v229, v66, v67\n"
    "v_cvt_pk_bf16_f32 v230, v68, v69\n"
    "v_cvt_pk_bf16_f32 v231, v70, v71\n"
    "v_cvt_pk_bf16_f32 v232, v72, v73\n"
    "v_cvt_pk_bf16_f32 v233, v74, v75\n"
    "v_mov_b32 v234, v232\n"
    "v_mov_b32 v235, v233\n"
    "buffer_store_dwordx4 v[228:231], v0, s[40:43], s26 offen sc1\n"
    "v_permlane16_swap_b32 v232, v234\n"
    "v_permlane16_swap_b32 v233, v235\n"
    "buffer_store_dwordx4 v[232:235], v12, s[40:43], s27 offen sc1\n"
    "s_waitcnt vmcnt(2)\n"
    "buffer_atomic_add v226, v3, s[40:43], s29 offen\n"
    "s_waitcnt vmcnt(0)\n"
    "buffer_atomic_add v226, v3, s[40:43], s52 offen\n"
    "s_nop 1\n"
    "s_branch hp_ret_%=\n"
    "hp_roleo_%=:\n"
    "s_mul_i32 s48, s44, 40\n"
    "s_mul_i32 s49, s45, 10\n"
    "s_add_u32 s48, s48, s49\n"
    "s_mul_i32 s48, s48, 6144\n"
    "s_add_u32 s48, s48, 26116096\n"
    "s_cmp_lg_u32 s47, 0\n"
    "s_cbranch_scc1 hpo_havew_%=\n"
    "buffer_load_dwordx4 a[0:3], v0, s[40:43], s48 offen\n"
    "buffer_load_dwordx4 a[4:7], v0, s[40:43], s48 offen offset:1024\n"
    "buffer_load_dwordx4 a[8:11], v0, s[40:43], s48 offen offset:2048\n"
    "buffer_load_dwordx4 a[12:15], v0, s[40:43], s48 offen offset:3072\n"
    "s_add_u32 s48, s48, 4096\n"
    "buffer_load_dwordx4 a[16:19], v0, s[40:43], s48 offen\n"
    "buffer_load_dwordx4 a[20:23], v0, s[40:43], s48 offen offset:1024\n"
    "buffer_load_dwordx4 a[24:27], v0, s[40:43], s48 offen offset:2048\n"
    "buffer_load_dwordx4 a[28:31], v0, s[40:43], s48 offen offset:3072\n"
    "s_add_u32 s48, s48, 4096\n"
    "buffer_load_dwordx4 a[32:35], v0, s[40:43], s48 offen\n"
    "buffer_load_dwordx4 a[36:39], v0, s[40:43], s48 offen offset:1024\n"
    "buffer_load_dwordx4 a[40:43], v0, s[40:43], s48 offen offset:2048\n"
    "buffer_load_dwordx4 a[44:47], v0, s[40:43], s48 offen offset:3072\n"
    "s_add_u32 s48, s48, 4096\n"
    "buffer_load_dwordx4 a[48:51], v0, s[40:43], s48 offen\n"
    "buffer_load_dwordx4 a[52:55], v0, s[40:43], s48 offen offset:1024\n"
    "buffer_load_dwordx4 a[56:59], v0, s[40:43], s48 offen offset:2048\n"
    "buffer_load_dwordx4 a[60:63], v0, s[40:43], s48 offen offset:3072\n"
    "s_add_u32 s48, s48, 4096\n"
    "buffer_load_dwordx4 a[64:67], v0, s[40:43], s48 offen\n"
    "buffer_load_dwordx4 a[68:71], v0, s[40:43], s48 offen offset:1024\n"
    "buffer_load_dwordx4 a[72:75], v0, s[40:43], s48 offen offset:2048\n"
    "buffer_load_dwordx4 a[76:79], v0, s[40:43], s48 offen offset:3072\n"
    "s_add_u32 s48, s48, 4096\n"
    "buffer_load_dwordx4 a[80:83], v0, s[40:43], s48 offen\n"
    "buffer_load_dwordx4 a[84:87], v0, s[40:43], s48 offen offset:1024\n"
    "buffer_load_dwordx4 a[88:91], v0, s[40:43], s48 offen offset:2048\n"
    "buffer_load_dwordx4 a[92:95], v0, s[40:43], s48 offen offset:3072\n"
    "s_add_u32 s48, s48, 4096\n"
    "buffer_load_dwordx4 a[96:99], v0, s[40:43], s48 offen\n"
    "buffer_load_dwordx4 a[100:103], v0, s[40:43], s48 offen offset:1024\n"
    "buffer_load_dwordx4 a[104:107], v0, s[40:43], s48 offen offset:2048\n"
    "buffer_load_dwordx4 a[108:111], v0, s[40:43], s48 offen offset:3072\n"
    "s_add_u32 s48, s48, 4096\n"
    "buffer_load_dwordx4 a[112:115], v0, s[40:43], s48 offen\n"
    "buffer_load_dwordx4 a[116:119], v0, s[40:43], s48 offen offset:1024\n"
    "buffer_load_dwordx4 a[120:123], v0, s[40:43], s48 offen offset:2048\n"
    "buffer_load_dwordx4 a[124:127], v0, s[40:43], s48 offen offset:3072\n"
    "s_add_u32 s48, s48, 4096\n"
    "buffer_load_dwordx4 a[128:131], v0, s[40:43], s48 offen\n"
    "buffer_load_dwordx4 a[132:135], v0, s[40:43], s48 offen offset:1024\n"
    "buffer_load_dwordx4 a[136:139], v0, s[40:43], s48 offen offset:2048\n"
    "buffer_load_dwordx4 a[140:143], v0, s[40:43], s48 offen offset:3072\n"
    "s_add_u32 s48, s48, 4096\n"
    "buffer_load_dwordx4 a[144:147], v0, s[40:43], s48 offen\n"
    "buffer_load_dwordx4 a[148:151], v0, s[40:43], s48 offen offset:1024\n"
    "buffer_load_dwordx4 a[152:155], v0, s[40:43], s48 offen offset:2048\n"
    "buffer_load_dwordx4 a[156:159], v0, s[40:43], s48 offen offset:3072\n"
    "s_add_u32 s48, s48, 4096\n"
    "buffer_load_dwordx4 a[160:163], v0, s[40:43], s48 offen\n"
    "buffer_load_dwordx4 a[164:167], v0, s[40:43], s48 offen offset:1024\n"
    "buffer_load_dwordx4 a[168:171], v0, s[40:43], s48 offen offset:2048\n"
    "buffer_load_dwordx4 a[172:175], v0, s[40:43], s48 offen offset:3072\n"
    "s_add_u32 s48, s48, 4096\n"
    "buffer_load_dwordx4 a[176:179], v0, s[40:43], s48 offen\n"
    "buffer_load_dwordx4 a[180:183], v0, s[40:43], s48 offen offset:1024\n"
    "buffer_load_dwordx4 a[184:187], v0, s[40:43], s48 offen offset:2048\n"
    "buffer_load_dwordx4 a[188:191], v0, s[40:43], s48 offen offset:3072\n"
    "s_add_u32 s48, s48, 4096\n"
    "buffer_load_dwordx4 a[192:195], v0, s[40:43], s48 offen\n"
    "buffer_load_dwordx4 a[196:199], v0, s[40:43], s48 offen offset:1024\n"
    "buffer_load_dwordx4 a[200:203], v0, s[40:43], s48 offen offset:2048\n"
    "buffer_load_dwordx4 a[204:207], v0, s[40:43], s48 offen offset:3072\n"
    "s_add_u32 s48, s48, 4096\n"
    "buffer_load_dwordx4 a[208:211], v0, s[40:43], s48 offen\n"
    "buffer_load_dwordx4 a[212:215], v0, s[40:43], s48 offen offset:1024\n"
    "buffer_load_dwordx4 a[216:219], v0, s[40:43], s48 offen offset:2048\n"
    "buffer_load_dwordx4 a[220:223], v0, s[40:43], s48 offen offset:3072\n"
    "s_add_u32 s48, s48, 4096\n"
    "buffer_load_dwordx4 a[224:227], v0, s[40:43], s48 offen\n"
    "buffer_load_dwordx4 a[228:231], v0, s[40:43], s48 offen offset:1024\n"
    "buffer_load_dwordx4 a[232:235], v0, s[40:43], s48 offen offset:2048\n"
    "buffer_load_dwordx4 a[236:239], v0, s[40:43], s48 offen offset:3072\n"
    "hpo_havew_%=:\n"
    "v_and_b32 v1, 0x370, v0\n"
    "v_bfe_u32 v4, v15, 3, 1\n"
    "v_lshl_add_u32 v1, v4, 15, v1\n"
    "s_lshr_b32 s48, s44, 1\n"
    "s_lshl_b32 s48, s48, 10\n"
    "s_and_b32 s49, s48, 0x2000\n"
    "s_add_u32 s48, s48, s49\n"
    "s_and_b32 s49, s44, 1\n"
    "s_lshl_b32 s49, s49, 7\n"
    "s_add_u32 s48, s48, s49\n"
    "v_add_u32 v1, s48, v1\n"
    "s_mul_i32 s62, s45, 655360\n"
    "s_add_u32 s62, s62, 115802112\n"
    "s_mov_b32 s63, s62\n"
    "s_lshr_b32 s48, s47, 1\n"
    "s_lshl_b32 s48, s48, 7\n"
    "s_add_u32 s48, s48, 254976\n"
    "v_mov_b32 v13, s48\n"
    "v_mov_b32 v14, 0\n"
    "s_mul_i32 s48, s45, 3\n"
    "s_add_u32 s48, s48, 0\n"
    "s_and_b32 s49, s48, 1\n"
    "s_lshr_b32 s48, s48, 1\n"
    "s_lshl_b32 s48, s48, 11\n"
    "s_lshl3_add_u32 s48, s49, s48\n"
    "v_add_u32 v8, s48, v0\n"
    "s_mul_i32 s48, s45, 3\n"
    "s_add_u32 s48, s48, 1\n"
    "s_and_b32 s49, s48, 1\n"
    "s_lshr_b32 s48, s48, 1\n"
    "s_lshl_b32 s48, s48, 11\n"
    "s_lshl3_add_u32 s48, s49, s48\n"
    "v_add_u32 v9, s48, v0\n"
    "s_mul_i32 s48, s45, 3\n"
    "s_add_u32 s48, s48, 2\n"
    "s_and_b32 s49, s48, 1\n"
    "s_lshr_b32 s48, s48, 1\n"
    "s_lshl_b32 s48, s48, 11\n"
    "s_lshl3_add_u32 s48, s49, s48\n"
    "v_add_u32 v10, s48, v0\n"
    "s_mul_i32 s49, s45, 3072\n"
    "s_mul_i32 s50, s44, 12288\n"
    "s_add_u32 s50, s50, 90603520\n"
    "s_add_u32 s50, s50, s49\n"
    "s_lshl_b32 s48, s44, 7\n"
    "s_add_u32 s49, s48, 2048\n"
    "v_mov_b32 v7, s49\n"
    "v_mov_b32 v6, 0\n"
    "s_cmp_eq_u32 s45, 0\n"
    "s_cselect_b32 s59, 1, 0\n"
    "v_lshlrev_b32 v2, 7, v15\n"
    "v_cmp_gt_u32 vcc, s59, v15\n"
    "v_mov_b32 v4, 2147483632\n"
    "s_nop 0\n"
    "v_cndmask_b32 v2, v4, v2, vcc\n"
    "v_mov_b32 v12, 1\n"
    "s_add_u32 s61, s47, 64\n"
    "s_waitcnt vmcnt(0)\n"
    "s_add_u32 s55, s47, 1\n"
    "s_mov_b32 s64, 0x40000000\n"
    "s_mov_b32 s60, 0\n"
    "v_cmp_gt_u32 vcc, s55, v6\n"
    "s_nop 4\n"
    "s_cbranch_vccz hpo_v0_ok_%=\n"
    "hpo_v0_poll_%=:\n"
    "buffer_load_dword v6, v7, s[40:43], s58 offen sc1\n"
    "s_waitcnt vmcnt(0)\n"
    "v_cmp_gt_u32 vcc, s55, v6\n"
    "s_nop 4\n"
    "s_cbranch_vccz hpo_v0_ok_%=\n"
    "s_sleep 2\n"
    "s_add_u32 s60, s60, 1\n"
    "s_cmp_gt_u32 s60, 65536\n"
    "s_cbranch_scc1 hp_abort_%=\n"
    "s_and_b32 s59, s60, 63\n"
    "s_cbranch_scc1 hpo_v0_poll_%=\n"
    "v_mov_b32 v4, 1024\n"
    "buffer_load_dword v4, v4, s[40:43], s58 offen sc1\n"
    "s_waitcnt vmcnt(0)\n"
    "v_readfirstlane_b32 s59, v4\n"
    "s_nop 0\n"
    "s_cmp_lg_u32 s59, 0\n"
    "s_nop 2\n"
    "s_cbranch_scc1 hp_end_%=\n"
    "s_branch hpo_v0_poll_%=\n"
    "hpo_v0_ok_%=:\n"
    "buffer_load_dwordx4 v[144:147], v0, s[40:43], s50 offen offset:0 sc1\n"
    "buffer_load_dwordx4 v[148:151], v0, s[40:43], s50 offen offset:1024 sc1\n"
    "buffer_load_dwordx4 v[152:155], v0, s[40:43], s50 offen offset:2048 sc1\n"
    "s_waitcnt vmcnt(2)\n"
    "ds_write2st64_b64 v8, v[144:145], v[146:147] offset0:0 offset1:2\n"
    "s_waitcnt vmcnt(1)\n"
    "ds_write2st64_b64 v9, v[148:149], v[150:151] offset0:0 offset1:2\n"
    "s_waitcnt vmcnt(0)\n"
    "ds_write2st64_b64 v10, v[152:153], v[154:155] offset0:0 offset1:2\n"
    "s_waitcnt lgkmcnt(0)\n"
    "s_barrier\n"
    "ds_read_b128 v[16:19], v0 offset:0\n"
    "ds_read_b128 v[20:23], v0 offset:1024\n"
    "ds_read_b128 v[24:27], v0 offset:2048\n"
    "ds_read_b128 v[28:31], v0 offset:3072\n"
    "ds_read_b128 v[32:35], v0 offset:4096\n"
    "ds_read_b128 v[36:39], v0 offset:5120\n"
    "ds_read_b128 v[40:43], v0 offset:6144\n"
    "ds_read_b128 v[44:47], v0 offset:7168\n"
    "ds_read_b128 v[48:51], v0 offset:8192\n"
    "ds_read_b128 v[52:55], v0 offset:9216\n"
    "ds_read_b128 v[56:59], v0 offset:10240\n"
    "ds_read_b128 v[60:63], v0 offset:11264\n"
    "s_waitcnt lgkmcnt(0)\n"
    "s_mov_b32 s53, 0x40000000\n"
    "hpo_loop_%=:\n"
    "s_bitcmp1_b32 s47, 5\n"
    "s_cselect_b32 s48, 8, 0\n"
    "s_mov_b32 s60, 0\n"
    "v_cmp_gt_u32 vcc, s48, v14\n"
    "s_nop 4\n"
    "s_cbranch_vccz hpo_free_ok_%=\n"
    "hpo_free_poll_%=:\n"
    "buffer_load_dword v14, v13, s[40:43], s58 offen sc1\n"
    "s_waitcnt vmcnt(0)\n"
    "v_cmp_gt_u32 vcc, s48, v14\n"
    "s_nop 4\n"
    "s_cbranch_vccz hpo_free_ok_%=\n"
    "s_sleep 8\n"
    "s_add_u32 s60, s60, 1\n"
    "s_cmp_gt_u32 s60, 65536\n"
    "s_cbranch_scc1 hp_abort_%=\n"
    "s_and_b32 s59, s60, 63\n"
    "s_cbranch_scc1 hpo_free_poll_%=\n"
    "v_mov_b32 v4, 1024\n"
    "buffer_load_dword v4, v4, s[40:43], s58 offen sc1\n"
    "s_waitcnt vmcnt(0)\n"
    "v_readfirstlane_b32 s59, v4\n"
    "s_nop 0\n"
    "s_cmp_lg_u32 s59, 0\n"
    "s_nop 2\n"
    "s_cbranch_scc1 hp_end_%=\n"
    "s_branch hpo_free_poll_%=\n"
    "hpo_free_ok_%=:\n"
    "v_add_u32 v13, 128, v13\n"
    "s_add_u32 s55, s47, 2\n"
    "s_min_u32 s55, s55, s61\n"
    "s_add_u32 s50, s50, 393216\n"
    "v_cmp_gt_u32 vcc, s55, v6\n"
    "s_nop 0\n"
    "s_cmp_eq_u64 vcc, 0\n"
    "s_cselect_b32 s56, s50, 0x40000000\n"
    "v_mfma_f32_16x16x32_bf16 v[112:115], v[16:19], a[0:3], 0\n"
    "buffer_load_dwordx4 v[144:147], v0, s[40:43], s56 offen offset:0 sc1\n"
    "v_mfma_f32_16x16x32_bf16 v[116:119], v[20:23], a[0:3], 0\n"
    "buffer_load_dwordx4 v[148:151], v0, s[40:43], s56 offen offset:1024 sc1\n"
    "v_mfma_f32_16x16x32_bf16 v[112:115], v[24:27], a[4:7], v[112:115]\n"
    "buffer_load_dwordx4 v[152:155], v0, s[40:43], s56 offen offset:2048 sc1\n"
    "v_mfma_f32_16x16x32_bf16 v[116:119], v[28:31], a[4:7], v[116:119]\n"
    "buffer_load_dword v6, v7, s[40:43], s58 offen sc1\n"
    "v_mfma_f32_16x16x32_bf16 v[112:115], v[32:35], a[8:11], v[112:115]\n"
    "v_mfma_f32_16x16x32_bf16 v[116:119], v[36:39], a[8:11], v[116:119]\n"
    "v_mfma_f32_16x16x32_bf16 v[112:115], v[40:43], a[12:15], v[112:115]\n"
    "v_cvt_pk_bf16_f32 v140, v120, v121\n"
    "v_mfma_f32_16x16x32_bf16 v[116:119], v[44:47], a[12:15], v[116:119]\n"
    "v_cvt_pk_bf16_f32 v141, v122, v123\n"
    "v_mfma_f32_16x16x32_bf16 v[112:115], v[48:51], a[16:19], v[112:115]\n"
    "v_cvt_pk_bf16_f32 v142, v124, v125\n"
    "v_mfma_f32_16x16x32_bf16 v[116:119], v[52:55], a[16:19], v[116:119]\n"
    "v_cvt_pk_bf16_f32 v143, v126, v127\n"
    "v_mfma_f32_16x16x32_bf16 v[112:115], v[56:59], a[20:23], v[112:115]\n"
    "buffer_store_dwordx4 v[140:143], v1, s[40:43], s53 offen sc1\n"
    "v_mfma_f32_16x16x32_bf16 v[116:119], v[60:63], a[20:23], v[116:119]\n"
    "s_mov_b32 s52, s63\n"
    "s_lshr_b32 s59, s52, 8\n"
    "s_and_b32 s59, s59, 0x2000\n"
    "s_add_u32 s53, s52, s59\n"
    "v_mfma_f32_16x16x32_bf16 v[120:123], v[16:19], a[24:27], 0\n"
    "v_mfma_f32_16x16x32_bf16 v[124:127], v[20:23], a[24:27], 0\n"
    "v_mfma_f32_16x16x32_bf16 v[120:123], v[24:27], a[28:31], v[120:123]\n"
    "v_mfma_f32_16x16x32_bf16 v[124:127], v[28:31], a[28:31], v[124:127]\n"
    "v_mfma_f32_16x16x32_bf16 v[120:123], v[32:35], a[32:35], v[120:123]\n"
    "v_mfma_f32_16x16x32_bf16 v[124:127], v[36:39], a[32:35], v[124:127]\n"
    "v_mfma_f32_16x16x32_bf16 v[120:123], v[40:43], a[36:39], v[120:123]\n"
    "v_cvt_pk_bf16_f32 v136, v112, v113\n"
    "v_mfma_f32_16x16x32_bf16 v[124:127], v[44:47], a[36:39], v[124:127]\n"
    "v_cvt_pk_bf16_f32 v137, v114, v115\n"
    "v_mfma_f32_16x16x32_bf16 v[120:123], v[48:51], a[40:43], v[120:123]\n"
    "v_cvt_pk_bf16_f32 v138, v116, v117\n"
    "v_mfma_f32_16x16x32_bf16 v[124:127], v[52:55], a[40:43], v[124:127]\n"
    "v_cvt_pk_bf16_f32 v139, v118, v119\n"
    "v_mfma_f32_16x16x32_bf16 v[120:123], v[56:59], a[44:47], v[120:123]\n"
    "buffer_store_dwordx4 v[136:139], v1, s[40:43], s53 offen sc1\n"
    "v_mfma_f32_16x16x32_bf16 v[124:127], v[60:63], a[44:47], v[124:127]\n"
    "s_add_u32 s52, s52, 65536\n"
    "s_lshr_b32 s59, s52, 8\n"
    "s_and_b32 s59, s59, 0x2000\n"
    "s_add_u32 s53, s52, s59\n"
    "v_mfma_f32_16x16x32_bf16 v[112:115], v[16:19], a[48:51], 0\n"
    "v_mfma_f32_16x16x32_bf16 v[116:119], v[20:23], a[48:51], 0\n"
    "v_mfma_f32_16x16x32_bf16 v[112:115], v[24:27], a[52:55], v[112:115]\n"
    "v_mfma_f32_16x16x32_bf16 v[116:119], v[28:31], a[52:55], v[116:119]\n"
    "v_mfma_f32_16x16x32_bf16 v[112:115], v[32:35], a[56:59], v[112:115]\n"
    "v_mfma_f32_16x16x32_bf16 v[116:119], v[36:39], a[56:59], v[116:119]\n"
    "v_mfma_f32_16x16x32_bf16 v[112:115], v[40:43], a[60:63], v[112:115]\n"
    "v_cvt_pk_bf16_f32 v140, v120, v121\n"
    "v_mfma_f32_16x16x32_bf16 v[116:119], v[44:47], a[60:63], v[116:119]\n"
    "v_cvt_pk_bf16_f32 v141, v122, v123\n"
    "v_mfma_f32_16x16x32_bf16 v[112:115], v[48:51], a[64:67], v[112:115]\n"
    "v_cvt_pk_bf16_f32 v142, v124, v125\n"
    "v_mfma_f32_16x16x32_bf16 v[116:119], v[52:55], a[64:67], v[116:119]\n"
    "v_cvt_pk_bf16_f32 v143, v126, v127\n"
    "v_mfma_f32_16x16x32_bf16 v[112:115], v[56:59], a[68:71], v[112:115]\n"
    "buffer_store_dwordx4 v[140:143], v1, s[40:43], s53 offen sc1\n"
    "v_mfma_f32_16x16x32_bf16 v[116:119], v[60:63], a[68:71], v[116:119]\n"
    "s_add_u32 s52, s52, 65536\n"
    "s_lshr_b32 s59, s52, 8\n"
    "s_and_b32 s59, s59, 0x2000\n"
    "s_add_u32 s53, s52, s59\n"
    "v_mfma_f32_16x16x32_bf16 v[120:123], v[16:19], a[72:75], 0\n"
    "v_mfma_f32_16x16x32_bf16 v[124:127], v[20:23], a[72:75], 0\n"
    "v_mfma_f32_16x16x32_bf16 v[120:123], v[24:27], a[76:79], v[120:123]\n"
    "v_mfma_f32_16x16x32_bf16 v[124:127], v[28:31], a[76:79], v[124:127]\n"
    "v_mfma_f32_16x16x32_bf16 v[120:123], v[32:35], a[80:83], v[120:123]\n"
    "v_mfma_f32_16x16x32_bf16 v[124:127], v[36:39], a[80:83], v[124:127]\n"
    "v_mfma_f32_16x16x32_bf16 v[120:123], v[40:43], a[84:87], v[120:123]\n"
    "v_cvt_pk_bf16_f32 v136, v112, v113\n"
    "v_mfma_f32_16x16x32_bf16 v[124:127], v[44:47], a[84:87], v[124:127]\n"
    "v_cvt_pk_bf16_f32 v137, v114, v115\n"
    "v_mfma_f32_16x16x32_bf16 v[120:123], v[48:51], a[88:91], v[120:123]\n"
    "v_cvt_pk_bf16_f32 v138, v116, v117\n"
    "v_mfma_f32_16x16x32_bf16 v[124:127], v[52:55], a[88:91], v[124:127]\n"
    "v_cvt_pk_bf16_f32 v139, v118, v119\n"
    "v_mfma_f32_16x16x32_bf16 v[120:123], v[56:59], a[92:95], v[120:123]\n"
    "buffer_store_dwordx4 v[136:139], v1, s[40:43], s53 offen sc1\n"
    "v_mfma_f32_16x16x32_bf16 v[124:127], v[60:63], a[92:95], v[124:127]\n"
    "s_add_u32 s52, s52, 65536\n"
    "s_lshr_b32 s59, s52, 8\n"
    "s_and_b32 s59, s59, 0x2000\n"
    "s_add_u32 s53, s52, s59\n"
    "v_mfma_f32_16x16x32_bf16 v[112:115], v[16:19], a[96:99], 0\n"
    "v_mfma_f32_16x16x32_bf16 v[116:119], v[20:23], a[96:99], 0\n"
    "v_mfma_f32_16x16x32_bf16 v[112:115], v[24:27], a[100:103], v[112:115]\n"
    "v_mfma_f32_16x16x32_bf16 v[116:119], v[28:31], a[100:103], v[116:119]\n"
    "v_mfma_f32_16x16x32_bf16 v[112:115], v[32:35], a[104:107], v[112:115]\n"
    "v_mfma_f32_16x16x32_bf16 v[116:119], v[36:39], a[104:107], v[116:119]\n"
    "v_mfma_f32_16x16x32_bf16 v[112:115], v[40:43], a[108:111], v[112:115]\n"
    "v_cvt_pk_bf16_f32 v140, v120, v121\n"
    "v_mfma_f32_16x16x32_bf16 v[116:119], v[44:47], a[108:111], v[116:119]\n"
    "v_cvt_pk_bf16_f32 v141, v122, v123\n"
    "v_mfma_f32_16x16x32_bf16 v[112:115], v[48:51], a[112:115], v[112:115]\n"
    "v_cvt_pk_bf16_f32 v142, v124, v125\n"
    "v_mfma_f32_16x16x32_bf16 v[116:119], v[52:55], a[112:115], v[116:119]\n"
    "v_cvt_pk_bf16_f32 v143, v126, v127\n"
    "v_mfma_f32_16x16x32_bf16 v[112:115], v[56:59], a[116:119], v[112:115]\n"
    "buffer_store_dwordx4 v[140:143], v1, s[40:43], s53 offen sc1\n"
    "v_mfma_f32_16x16x32_bf16 v[116:119], v[60:63], a[116:119], v[116:119]\n"
    "s_add_u32 s52, s52, 65536\n"
    "s_lshr_b32 s59, s52, 8\n"
    "s_and_b32 s59, s59, 0x2000\n"
    "s_add_u32 s53, s52, s59\n"
    "v_mfma_f32_16x16x32_bf16 v[120:123], v[16:19], a[120:123], 0\n"
    "v_mfma_f32_16x16x32_bf16 v[124:127], v[20:23], a[120:123], 0\n"
    "v_mfma_f32_16x16x32_bf16 v[120:123], v[24:27], a[124:127], v[120:123]\n"
    "v_mfma_f32_16x16x32_bf16 v[124:127], v[28:31], a[124:127], v[124:127]\n"
    "v_mfma_f32_16x16x32_bf16 v[120:123], v[32:35], a[128:131], v[120:123]\n"
    "v_mfma_f32_16x16x32_bf16 v[124:127], v[36:39], a[128:131], v[124:127]\n"
    "v_mfma_f32_16x16x32_bf16 v[120:123], v[40:43], a[132:135], v[120:123]\n"
    "v_cvt_pk_bf16_f32 v136, v112, v113\n"
    "v_mfma_f32_16x16x32_bf16 v[124:127], v[44:47], a[132:135], v[124:127]\n"
    "v_cvt_pk_bf16_f32 v137, v114, v115\n"
    "v_mfma_f32_16x16x32_bf16 v[120:123], v[48:51], a[136:139], v[120:123]\n"
    "v_cvt_pk_bf16_f32 v138, v116, v117\n"
    "v_mfma_f32_16x16x32_bf16 v[124:127], v[52:55], a[136:139], v[124:127]\n"
    "v_cvt_pk_bf16_f32 v139, v118, v119\n"
    "v_mfma_f32_16x16x32_bf16 v[120:123], v[56:59], a[140:143], v[120:123]\n"
    "buffer_store_dwordx4 v[136:139], v1, s[40:43], s53 offen sc1\n"
    "v_mfma_f32_16x16x32_bf16 v[124:127], v[60:63], a[140:143], v[124:127]\n"
    "s_add_u32 s52, s52, 65536\n"
    "s_lshr_b32 s59, s52, 8\n"
    "s_and_b32 s59, s59, 0x2000\n"
    "s_add_u32 s53, s52, s59\n"
    "v_mfma_f32_16x16x32_bf16 v[112:115], v[16:19], a[144:147], 0\n"
    "v_mfma_f32_16x16x32_bf16 v[116:119], v[20:23], a[144:147], 0\n"
    "v_mfma_f32_16x16x32_bf16 v[112:115], v[24:27], a[148:151], v[112:115]\n"
    "v_mfma_f32_16x16x32_bf16 v[116:119], v[28:31], a[148:151], v[116:119]\n"
    "v_mfma_f32_16x16x32_bf16 v[112:115], v[32:35], a[152:155], v[112:115]\n"
    "v_mfma_f32_16x16x32_bf16 v[116:119], v[36:39], a[152:155], v[116:119]\n"
    "v_mfma_f32_16x16x32_bf16 v[112:115], v[40:43], a[156:159], v[112:115]\n"
    "v_cvt_pk_bf16_f32 v140, v120, v121\n"
    "v_mfma_f32_16x16x32_bf16 v[116:119], v[44:47], a[156:159], v[116:119]\n"
    "v_cvt_pk_bf16_f32 v141, v122, v123\n"
    "v_mfma_f32_16x16x32_bf16 v[112:115], v[48:51], a[160:163], v[112:115]\n"
    "v_cvt_pk_bf16_f32 v142, v124, v125\n"
    "v_mfma_f32_16x16x32_bf16 v[116:119], v[52:55], a[160:163], v[116:119]\n"
    "v_cvt_pk_bf16_f32 v143, v126, v127\n"
    "v_mfma_f32_16x16x32_bf16 v[112:115], v[56:59], a[164:167], v[112:115]\n"
    "buffer_store_dwordx4 v[140:143], v1, s[40:43], s53 offen sc1\n"
    "v_mfma_f32_16x16x32_bf16 v[116:119], v[60:63], a[164:167], v[116:119]\n"
    "s_add_u32 s52, s52, 65536\n"
    "s_lshr_b32 s59, s52, 8\n"
    "s_and_b32 s59, s59, 0x2000\n"
    "s_add_u32 s53, s52, s59\n"
    "s_waitcnt vmcnt(6)\n"
    "s_cmp_eq_u32 s56, s50\n"
    "s_cbranch_scc1 hpo_have0_%=\n"
    "s_mov_b32 s60, 0\n"
    "v_cmp_gt_u32 vcc, s55, v6\n"
    "s_nop 4\n"
    "s_cbranch_vccz hpo_v10_ok_%=\n"
    "hpo_v10_poll_%=:\n"
    "buffer_load_dword v6, v7, s[40:43], s58 offen sc1\n"
    "s_waitcnt vmcnt(0)\n"
    "v_cmp_gt_u32 vcc, s55, v6\n"
    "s_nop 4\n"
    "s_cbranch_vccz hpo_v10_ok_%=\n"
    "s_sleep 2\n"
    "s_add_u32 s60, s60, 1\n"
    "s_cmp_gt_u32 s60, 65536\n"
    "s_cbranch_scc1 hp_abort_%=\n"
    "s_and_b32 s59, s60, 63\n"
    "s_cbranch_scc1 hpo_v10_poll_%=\n"
    "v_mov_b32 v4, 1024\n"
    "buffer_load_dword v4, v4, s[40:43], s58 offen sc1\n"
    "s_waitcnt vmcnt(0)\n"
    "v_readfirstlane_b32 s59, v4\n"
    "s_nop 0\n"
    "s_cmp_lg_u32 s59, 0\n"
    "s_nop 2\n"
    "s_cbranch_scc1 hp_end_%=\n"
    "s_branch hpo_v10_poll_%=\n"
    "hpo_v10_ok_%=:\n"
    "buffer_load_dwordx4 v[144:147], v0, s[40:43], s50 offen offset:0 sc1\n"
    "buffer_load_dwordx4 v[148:151], v0, s[40:43], s50 offen offset:1024 sc1\n"
    "buffer_load_dwordx4 v[152:155], v0, s[40:43], s50 offen offset:2048 sc1\n"
    "s_waitcnt vmcnt(0)\n"
    "hpo_have0_%=:\n"
    "v_mfma_f32_16x16x32_bf16 v[120:123], v[16:19], a[168:171], 0\n"
    "ds_write2st64_b64 v8, v[144:145], v[146:147] offset0:24 offset1:26\n"
    "v_mfma_f32_16x16x32_bf16 v[124:127], v[20:23], a[168:171], 0\n"
    "ds_write2st64_b64 v9, v[148:149], v[150:151] offset0:24 offset1:26\n"
    "v_mfma_f32_16x16x32_bf16 v[120:123], v[24:27], a[172:175], v[120:123]\n"
    "ds_write2st64_b64 v10, v[152:153], v[154:155] offset0:24 offset1:26\n"
    "v_mfma_f32_16x16x32_bf16 v[124:127], v[28:31], a[172:175], v[124:127]\n"
    "v_mfma_f32_16x16x32_bf16 v[120:123], v[32:35], a[176:179], v[120:123]\n"
    "v_mfma_f32_16x16x32_bf16 v[124:127], v[36:39], a[176:179], v[124:127]\n"
    "v_mfma_f32_16x16x32_bf16 v[120:123], v[40:43], a[180:183], v[120:123]\n"
    "v_cvt_pk_bf16_f32 v136, v112, v113\n"
    "v_mfma_f32_16x16x32_bf16 v[124:127], v[44:47], a[180:183], v[124:127]\n"
    "v_cvt_pk_bf16_f32 v137, v114, v115\n"
    "v_mfma_f32_16x16x32_bf16 v[120:123], v[48:51], a[184:187], v[120:123]\n"
    "v_cvt_pk_bf16_f32 v138, v116, v117\n"
    "v_mfma_f32_16x16x32_bf16 v[124:127], v[52:55], a[184:187], v[124:127]\n"
    "v_cvt_pk_bf16_f32 v139, v118, v119\n"
    "v_mfma_f32_16x16x32_bf16 v[120:123], v[56:59], a[188:191], v[120:123]\n"
    "buffer_store_dwordx4 v[136:139], v1, s[40:43], s53 offen sc1\n"
    "v_mfma_f32_16x16x32_bf16 v[124:127], v[60:63], a[188:191], v[124:127]\n"
    "s_add_u32 s52, s52, 65536\n"
    "s_lshr_b32 s59, s52, 8\n"
    "s_and_b32 s59, s59, 0x2000\n"
    "s_add_u32 s53, s52, s59\n"
    "s_waitcnt lgkmcnt(0)\n"
    "s_barrier\n"
    "buffer_atomic_add v12, v2, s[40:43], s64 offen\n"
    "v_mfma_f32_16x16x32_bf16 v[112:115], v[16:19], a[192:195], 0\n"
    "ds_read_b128 v[64:67], v0 offset:12288\n"
    "v_mfma_f32_16x16x32_bf16 v[116:119], v[20:23], a[192:195], 0\n"
    "ds_read_b128 v[68:71], v0 offset:13312\n"
    "v_mfma_f32_16x16x32_bf16 v[112:115], v[24:27], a[196:199], v[112:115]\n"
    "ds_read_b128 v[72:75], v0 offset:14336\n"
    "v_mfma_f32_16x16x32_bf16 v[116:119], v[28:31], a[196:199], v[116:119]\n"
    "ds_read_b128 v[76:79], v0 offset:15360\n"
    "v_mfma_f32_16x16x32_bf16 v[112:115], v[32:35], a[200:203], v[112:115]\n"
    "ds_read_b128 v[80:83], v0 offset:16384\n"
    "v_mfma_f32_16x16x32_bf16 v[116:119], v[36:39], a[200:203], v[116:119]\n"
    "ds_read_b128 v[84:87], v0 offset:17408\n"
    "v_mfma_f32_16x16x32_bf16 v[112:115], v[40:43], a[204:207], v[112:115]\n"
    "v_cvt_pk_bf16_f32 v140, v120, v121\n"
    "v_mfma_f32_16x16x32_bf16 v[116:119], v[44:47], a[204:207], v[116:119]\n"
    "v_cvt_pk_bf16_f32 v141, v122, v123\n"
    "v_mfma_f32_16x16x32_bf16 v[112:115], v[48:51], a[208:211], v[112:115]\n"
    "v_cvt_pk_bf16_f32 v142, v124, v125\n"
    "v_mfma_f32_16x16x32_bf16 v[116:119], v[52:55], a[208:211], v[116:119]\n"
    "v_cvt_pk_bf16_f32 v143, v126, v127\n"
    "v_mfma_f32_16x16x32_bf16 v[112:115], v[56:59], a[212:215], v[112:115]\n"
    "buffer_store_dwordx4 v[140:143], v1, s[40:43], s53 offen sc1\n"
    "v_mfma_f32_16x16x32_bf16 v[116:119], v[60:63], a[212:215], v[116:119]\n"
    "s_add_u32 s52, s52, 65536\n"
    "s_lshr_b32 s59, s52, 8\n"
    "s_and_b32 s59, s59, 0x2000\n"
    "s_add_u32 s53, s52, s59\n"
    "v_mfma_f32_16x16x32_bf16 v[120:123], v[16:19], a[216:219], 0\n"
    "ds_read_b128 v[88:91], v0 offset:18432\n"
    "v_mfma_f32_16x16x32_bf16 v[124:127], v[20:23], a[216:219], 0\n"
    "ds_read_b128 v[92:95], v0 offset:19456\n"
    "v_mfma_f32_16x16x32_bf16 v[120:123], v[24:27], a[220:223], v[120:123]\n"
    "ds_read_b128 v[96:99], v0 offset:20480\n"
    "v_mfma_f32_16x16x32_bf16 v[124:127], v[28:31], a[220:223], v[124:127]\n"
    "ds_read_b128 v[100:103], v0 offset:21504\n"
    "v_mfma_f32_16x16x32_bf16 v[120:123], v[32:35], a[224:227], v[120:123]\n"
    "ds_read_b128 v[104:107], v0 offset:22528\n"
    "v_mfma_f32_16x16x32_bf16 v[124:127], v[36:39], a[224:227], v[124:127]\n"
    "ds_read_b128 v[108:111], v0 offset:23552\n"
    "v_mfma_f32_16x16x32_bf16 v[120:123], v[40:43], a[228:231], v[120:123]\n"
    "v_cvt_pk_bf16_f32 v136, v112, v113\n"
    "v_mfma_f32_16x16x32_bf16 v[124:127], v[44:47], a[228:231], v[124:127]\n"
    "v_cvt_pk_bf16_f32 v137, v114, v115\n"
    "v_mfma_f32_16x16x32_bf16 v[120:123], v[48:51], a[232:235], v[120:123]\n"
    "v_cvt_pk_bf16_f32 v138, v116, v117\n"
    "v_mfma_f32_16x16x32_bf16 v[124:127], v[52:55], a[232:235], v[124:127]\n"
    "v_cvt_pk_bf16_f32 v139, v118, v119\n"
    "v_mfma_f32_16x16x32_bf16 v[120:123], v[56:59], a[236:239], v[120:123]\n"
    "buffer_store_dwordx4 v[136:139], v1, s[40:43], s53 offen sc1\n"
    "v_mfma_f32_16x16x32_bf16 v[124:127], v[60:63], a[236:239], v[124:127]\n"
    "s_add_u32 s52, s52, 65536\n"
    "s_lshr_b32 s59, s52, 8\n"
    "s_and_b32 s59, s59, 0x2000\n"
    "s_add_u32 s53, s52, s59\n"
    "s_waitcnt lgkmcnt(0)\n"
    "s_add_u32 s63, s63, 2621440\n"
    "s_and_b32 s59, s47, 31\n"
    "s_cmp_eq_u32 s59, 31\n"
    "s_cselect_b32 s63, s62, s63\n"
    "s_lshl_b32 s64, s47, 7\n"
    "s_add_u32 s64, s64, 2124032\n"
    "s_and_b32 s57, s47, 63\n"
    "s_sub_u32 s59, s57, 2\n"
    "s_cmp_lt_u32 s59, 61\n"
    "s_cbranch_scc1 hpo_late0_%=\n"
    "v_cvt_pk_bf16_f32 v140, v120, v121\n"
    "v_cvt_pk_bf16_f32 v141, v122, v123\n"
    "v_cvt_pk_bf16_f32 v142, v124, v125\n"
    "v_cvt_pk_bf16_f32 v143, v126, v127\n"
    "buffer_store_dwordx4 v[140:143], v1, s[40:43], s53 offen sc1\n"
    "s_waitcnt vmcnt(0)\n"
    "s_barrier\n"
    "buffer_atomic_add v12, v2, s[40:43], s64 offen\n"
    "s_mov_b32 s64, 0x40000000\n"
    "s_cmp_eq_u32 s57, 63\n"
    "s_cbranch_scc1 hp_ret_%=\n"
    "hpo_late0_%=:\n"
    "s_add_u32 s47, s47, 1\n"
    "s_add_u32 s55, s47, 2\n"
    "s_min_u32 s55, s55, s61\n"
    "s_add_u32 s50, s50, 393216\n"
    "v_cmp_gt_u32 vcc, s55, v6\n"
    "s_nop 0\n"
    "s_cmp_eq_u64 vcc, 0\n"
    "s_cselect_b32 s56, s50, 0x40000000\n"
    "v_mfma_f32_16x16x32_bf16 v[112:115], v[64:67], a[0:3], 0\n"
    "buffer_load_dwordx4 v[144:147], v0, s[40:43], s56 offen offset:0 sc1\n"
    "v_mfma_f32_16x16x32_bf16 v[116:119], v[68:71], a[0:3], 0\n"
    "buffer_load_dwordx4 v[148:151], v0, s[40:43], s56 offen offset:1024 sc1\n"
    "v_mfma_f32_16x16x32_bf16 v[112:115], v[72:75], a[4:7], v[112:115]\n"
    "buffer_load_dwordx4 v[152:155], v0, s[40:43], s56 offen offset:2048 sc1\n"
    "v_mfma_f32_16x16x32_bf16 v[116:119], v[76:79], a[4:7], v[116:119]\n"
    "buffer_load_dword v6, v7, s[40:43], s58 offen sc1\n"
    "v_mfma_f32_16x16x32_bf16 v[112:115], v[80:83], a[8:11], v[112:115]\n"
    "buffer_load_dword v14, v13, s[40:43], s58 offen sc1\n"
    "v_mfma_f32_16x16x32_bf16 v[116:119], v[84:87], a[8:11], v[116:119]\n"
    "v_mfma_f32_16x16x32_bf16 v[112:115], v[88:91], a[12:15], v[112:115]\n"
    "v_cvt_pk_bf16_f32 v140, v120, v121\n"
    "v_mfma_f32_16x16x32_bf16 v[116:119], v[92:95], a[12:15], v[116:119]\n"
    "v_cvt_pk_bf16_f32 v141, v122, v123\n"
    "v_mfma_f32_16x16x32_bf16 v[112:115], v[96:99], a[16:19], v[112:115]\n"
    "v_cvt_pk_bf16_f32 v142, v124, v125\n"
    "v_mfma_f32_16x16x32_bf16 v[116:119], v[100:103], a[16:19], v[116:119]\n"
    "v_cvt_pk_bf16_f32 v143, v126, v127\n"
    "v_mfma_f32_16x16x32_bf16 v[112:115], v[104:107], a[20:23], v[112:115]\n"
    "buffer_store_dwordx4 v[140:143], v1, s[40:43], s53 offen sc1\n"
    "v_mfma_f32_16x16x32_bf16 v[116:119], v[108:111], a[20:23], v[116:119]\n"
    "s_mov_b32 s52, s63\n"
    "s_lshr_b32 s59, s52, 8\n"
    "s_and_b32 s59, s59, 0x2000\n"
    "s_add_u32 s53, s52, s59\n"
    "v_mfma_f32_16x16x32_bf16 v[120:123], v[64:67], a[24:27], 0\n"
    "v_mfma_f32_16x16x32_bf16 v[124:127], v[68:71], a[24:27], 0\n"
    "v_mfma_f32_16x16x32_bf16 v[120:123], v[72:75], a[28:31], v[120:123]\n"
    "v_mfma_f32_16x16x32_bf16 v[124:127], v[76:79], a[28:31], v[124:127]\n"
    "v_mfma_f32_16x16x32_bf16 v[120:123], v[80:83], a[32:35], v[120:123]\n"
    "v_mfma_f32_16x16x32_bf16 v[124:127], v[84:87], a[32:35], v[124:127]\n"
    "v_mfma_f32_16x16x32_bf16 v[120:123], v[88:91], a[36:39], v[120:123]\n"
    "v_cvt_pk_bf16_f32 v136, v112, v113\n"
    "v_mfma_f32_16x16x32_bf16 v[124:127], v[92:95], a[36:39], v[124:127]\n"
    "v_cvt_pk_bf16_f32 v137, v114, v115\n"
    "v_mfma_f32_16x16x32_bf16 v[120:123], v[96:99], a[40:43], v[120:123]\n"
    "v_cvt_pk_bf16_f32 v138, v116, v117\n"
    "v_mfma_f32_16x16x32_bf16 v[124:127], v[100:103], a[40:43], v[124:127]\n"
    "v_cvt_pk_bf16_f32 v139, v118, v119\n"
    "v_mfma_f32_16x16x32_bf16 v[120:123], v[104:107], a[44:47], v[120:123]\n"
    "buffer_store_dwordx4 v[136:139], v1, s[40:43], s53 offen sc1\n"
    "v_mfma_f32_16x16x32_bf16 v[124:127], v[108:111], a[44:47], v[124:127]\n"
    "s_add_u32 s52, s52, 65536\n"
    "s_lshr_b32 s59, s52, 8\n"
    "s_and_b32 s59, s59, 0x2000\n"
    "s_add_u32 s53, s52, s59\n"
    "v_mfma_f32_16x16x32_bf16 v[112:115], v[64:67], a[48:51], 0\n"
    "v_mfma_f32_16x16x32_bf16 v[116:119], v[68:71], a[48:51], 0\n"
    "v_mfma_f32_16x16x32_bf16 v[112:115], v[72:75], a[52:55], v[112:115]\n"
    "v_mfma_f32_16x16x32_bf16 v[116:119], v[76:79], a[52:55], v[116:119]\n"
    "v_mfma_f32_16x16x32_bf16 v[112:115], v[80:83], a[56:59], v[112:115]\n"
    "v_mfma_f32_16x16x32_bf16 v[116:119], v[84:87], a[56:59], v[116:119]\n"
    "v_mfma_f32_16x16x32_bf16 v[112:115], v[88:91], a[60:63], v[112:115]\n"
    "v_cvt_pk_bf16_f32 v140, v120, v121\n"
    "v_mfma_f32_16x16x32_bf16 v[116:119], v[92:95], a[60:63], v[116:119]\n"
    "v_cvt_pk_bf16_f32 v141, v122, v123\n"
    "v_mfma_f32_16x16x32_bf16 v[112:115], v[96:99], a[64:67], v[112:115]\n"
    "v_cvt_pk_bf16_f32 v142, v124, v125\n"
    "v_mfma_f32_16x16x32_bf16 v[116:119], v[100:103], a[64:67], v[116:119]\n"
    "v_cvt_pk_bf16_f32 v143, v126, v127\n"
    "v_mfma_f32_16x16x32_bf16 v[112:115], v[104:107], a[68:71], v[112:115]\n"
    "buffer_store_dwordx4 v[140:143], v1, s[40:43], s53 offen sc1\n"
    "v_mfma_f32_16x16x32_bf16 v[116:119], v[108:111], a[68:71], v[116:119]\n"
    "s_add_u32 s52, s52, 65536\n"
    "s_lshr_b32 s59, s52, 8\n"
    "s_and_b32 s59, s59, 0x2000\n"
    "s_add_u32 s53, s52, s59\n"
    "v_mfma_f32_16x16x32_bf16 v[120:123], v[64:67], a[72:75], 0\n"
    "v_mfma_f32_16x16x32_bf16 v[124:127], v[68:71], a[72:75], 0\n"
    "v_mfma_f32_16x16x32_bf16 v[120:123], v[72:75], a[76:79], v[120:123]\n"
    "v_mfma_f32_16x16x32_bf16 v[124:127], v[76:79], a[76:79], v[124:127]\n"
    "v_mfma_f32_16x16x32_bf16 v[120:123], v[80:83], a[80:83], v[120:123]\n"
    "v_mfma_f32_16x16x32_bf16 v[124:127], v[84:87], a[80:83], v[124:127]\n"
    "v_mfma_f32_16x16x32_bf16 v[120:123], v[88:91], a[84:87], v[120:123]\n"
    "v_cvt_pk_bf16_f32 v136, v112, v113\n"
    "v_mfma_f32_16x16x32_bf16 v[124:127], v[92:95], a[84:87], v[124:127]\n"
    "v_cvt_pk_bf16_f32 v137, v114, v115\n"
    "v_mfma_f32_16x16x32_bf16 v[120:123], v[96:99], a[88:91], v[120:123]\n"
    "v_cvt_pk_bf16_f32 v138, v116, v117\n"
    "v_mfma_f32_16x16x32_bf16 v[124:127], v[100:103], a[88:91], v[124:127]\n"
    "v_cvt_pk_bf16_f32 v139, v118, v119\n"
    "v_mfma_f32_16x16x32_bf16 v[120:123], v[104:107], a[92:95], v[120:123]\n"
    "buffer_store_dwordx4 v[136:139], v1, s[40:43], s53 offen sc1\n"
    "v_mfma_f32_16x16x32_bf16 v[124:127], v[108:111], a[92:95], v[124:127]\n"
    "s_add_u32 s52, s52, 65536\n"
    "s_lshr_b32 s59, s52, 8\n"
    "s_and_b32 s59, s59, 0x2000\n"
    "s_add_u32 s53, s52, s59\n"
    "v_mfma_f32_16x16x32_bf16 v[112:115], v[64:67], a[96:99], 0\n"
    "v_mfma_f32_16x16x32_bf16 v[116:119], v[68:71], a[96:99], 0\n"
    "v_mfma_f32_16x16x32_bf16 v[112:115], v[72:75], a[100:103], v[112:115]\n"
    "v_mfma_f32_16x16x32_bf16 v[116:119], v[76:79], a[100:103], v[116:119]\n"
    "v_mfma_f32_16x16x32_bf16 v[112:115], v[80:83], a[104:107], v[112:115]\n"
    "v_mfma_f32_16x16x32_bf16 v[116:119], v[84:87], a[104:107], v[116:119]\n"
    "v_mfma_f32_16x16x32_bf16 v[112:115], v[88:91], a[108:111], v[112:115]\n"
    "v_cvt_pk_bf16_f32 v140, v120, v121\n"
    "v_mfma_f32_16x16x32_bf16 v[116:119], v[92:95], a[108:111], v[116:119]\n"
    "v_cvt_pk_bf16_f32 v141, v122, v123\n"
    "v_mfma_f32_16x16x32_bf16 v[112:115], v[96:99], a[112:115], v[112:115]\n"
    "v_cvt_pk_bf16_f32 v142, v124, v125\n"
    "v_mfma_f32_16x16x32_bf16 v[116:119], v[100:103], a[112:115], v[116:119]\n"
    "v_cvt_pk_bf16_f32 v143, v126, v127\n"
    "v_mfma_f32_16x16x32_bf16 v[112:115], v[104:107], a[116:119], v[112:115]\n"
    "buffer_store_dwordx4 v[140:143], v1, s[40:43], s53 offen sc1\n"
    "v_mfma_f32_16x16x32_bf16 v[116:119], v[108:111], a[116:119], v[116:119]\n"
    "s_add_u32 s52, s52, 65536\n"
    "s_lshr_b32 s59, s52, 8\n"
    "s_and_b32 s59, s59, 0x2000\n"
    "s_add_u32 s53, s52, s59\n"
    "v_mfma_f32_16x16x32_bf16 v[120:123], v[64:67], a[120:123], 0\n"
    "v_mfma_f32_16x16x32_bf16 v[124:127], v[68:71], a[120:123], 0\n"
    "v_mfma_f32_16x16x32_bf16 v[120:123], v[72:75], a[124:127], v[120:123]\n"
    "v_mfma_f32_16x16x32_bf16 v[124:127], v[76:79], a[124:127], v[124:127]\n"
    "v_mfma_f32_16x16x32_bf16 v[120:123], v[80:83], a[128:131], v[120:123]\n"
    "v_mfma_f32_16x16x32_bf16 v[124:127], v[84:87], a[128:131], v[124:127]\n"
    "v_mfma_f32_16x16x32_bf16 v[120:123], v[88:91], a[132:135], v[120:123]\n"
    "v_cvt_pk_bf16_f32 v136, v112, v113\n"
    "v_mfma_f32_16x16x32_bf16 v[124:127], v[92:95], a[132:135], v[124:127]\n"
    "v_cvt_pk_bf16_f32 v137, v114, v115\n"
    "v_mfma_f32_16x16x32_bf16 v[120:123], v[96:99], a[136:139], v[120:123]\n"
    "v_cvt_pk_bf16_f32 v138, v116, v117\n"
    "v_mfma_f32_16x16x32_bf16 v[124:127], v[100:103], a[136:139], v[124:127]\n"
    "v_cvt_pk_bf16_f32 v139, v118, v119\n"
    "v_mfma_f32_16x16x32_bf16 v[120:123], v[104:107], a[140:143], v[120:123]\n"
    "buffer_store_dwordx4 v[136:139], v1, s[40:43], s53 offen sc1\n"
    "v_mfma_f32_16x16x32_bf16 v[124:127], v[108:111], a[140:143], v[124:127]\n"
    "s_add_u32 s52, s52, 65536\n"
    "s_lshr_b32 s59, s52, 8\n"
    "s_and_b32 s59, s59, 0x2000\n"
    "s_add_u32 s53, s52, s59\n"
    "v_mfma_f32_16x16x32_bf16 v[112:115], v[64:67], a[144:147], 0\n"
    "v_mfma_f32_16x16x32_bf16 v[116:119], v[68:71], a[144:147], 0\n"
    "v_mfma_f32_16x16x32_bf16 v[112:115], v[72:75], a[148:151], v[112:115]\n"
    "v_mfma_f32_16x16x32_bf16 v[116:119], v[76:79], a[148:151], v[116:119]\n"
    "v_mfma_f32_16x16x32_bf16 v[112:115], v[80:83], a[152:155], v[112:115]\n"
    "v_mfma_f32_16x16x32_bf16 v[116:119], v[84:87], a[152:155], v[116:119]\n"
    "v_mfma_f32_16x16x32_bf16 v[112:115], v[88:91], a[156:159], v[112:115]\n"
    "v_cvt_pk_bf16_f32 v140, v120, v121\n"
    "v_mfma_f32_16x16x32_bf16 v[116:119], v[92:95], a[156:159], v[116:119]\n"
    "v_cvt_pk_bf16_f32 v141, v122, v123\n"
    "v_mfma_f32_16x16x32_bf16 v[112:115], v[96:99], a[160:163], v[112:115]\n"
    "v_cvt_pk_bf16_f32 v142, v124, v125\n"
    "v_mfma_f32_16x16x32_bf16 v[116:119], v[100:103], a[160:163], v[116:119]\n"
    "v_cvt_pk_bf16_f32 v143, v126, v127\n"
    "v_mfma_f32_16x16x32_bf16 v[112:115], v[104:107], a[164:167], v[112:115]\n"
    "buffer_store_dwordx4 v[140:143], v1, s[40:43], s53 offen sc1\n"
    "v_mfma_f32_16x16x32_bf16 v[116:119], v[108:111], a[164:167], v[116:119]\n"
    "s_add_u32 s52, s52, 65536\n"
    "s_lshr_b32 s59, s52, 8\n"
    "s_and_b32 s59, s59, 0x2000\n"
    "s_add_u32 s53, s52, s59\n"
    "s_waitcnt vmcnt(6)\n"
    "s_cmp_eq_u32 s56, s50\n"
    "s_cbranch_scc1 hpo_have1_%=\n"
    "s_mov_b32 s60, 0\n"
    "v_cmp_gt_u32 vcc, s55, v6\n"
    "s_nop 4\n"
    "s_cbranch_vccz hpo_v11_ok_%=\n"
    "hpo_v11_poll_%=:\n"
    "buffer_load_dword v6, v7, s[40:43], s58 offen sc1\n"
    "s_waitcnt vmcnt(0)\n"
    "v_cmp_gt_u32 vcc, s55, v6\n"
    "s_nop 4\n"
    "s_cbranch_vccz hpo_v11_ok_%=\n"
    "s_sleep 2\n"
    "s_add_u32 s60, s60, 1\n"
    "s_cmp_gt_u32 s60, 65536\n"
    "s_cbranch_scc1 hp_abort_%=\n"
    "s_and_b32 s59, s60, 63\n"
    "s_cbranch_scc1 hpo_v11_poll_%=\n"
    "v_mov_b32 v4, 1024\n"
    "buffer_load_dword v4, v4, s[40:43], s58 offen sc1\n"
    "s_waitcnt vmcnt(0)\n"
    "v_readfirstlane_b32 s59, v4\n"
    "s_nop 0\n"
    "s_cmp_lg_u32 s59, 0\n"
    "s_nop 2\n"
    "s_cbranch_scc1 hp_end_%=\n"
    "s_branch hpo_v11_poll_%=\n"
    "hpo_v11_ok_%=:\n"
    "buffer_load_dwordx4 v[144:147], v0, s[40:43], s50 offen offset:0 sc1\n"
    "buffer_load_dwordx4 v[148:151], v0, s[40:43], s50 offen offset:1024 sc1\n"
    "buffer_load_dwordx4 v[152:155], v0, s[40:43], s50 offen offset:2048 sc1\n"
    "s_waitcnt vmcnt(0)\n"
    "hpo_have1_%=:\n"
    "v_mfma_f32_16x16x32_bf16 v[120:123], v[64:67], a[168:171], 0\n"
    "ds_write2st64_b64 v8, v[144:145], v[146:147] offset0:0 offset1:2\n"
    "v_mfma_f32_16x16x32_bf16 v[124:127], v[68:71], a[168:171], 0\n"
    "ds_write2st64_b64 v9, v[148:149], v[150:151] offset0:0 offset1:2\n"
    "v_mfma_f32_16x16x32_bf16 v[120:123], v[72:75], a[172:175], v[120:123]\n"
    "ds_write2st64_b64 v10, v[152:153], v[154:155] offset0:0 offset1:2\n"
    "v_mfma_f32_16x16x32_bf16 v[124:127], v[76:79], a[172:175], v[124:127]\n"
    "v_mfma_f32_16x16x32_bf16 v[120:123], v[80:83], a[176:179], v[120:123]\n"
    "v_mfma_f32_16x16x32_bf16 v[124:127], v[84:87], a[176:179], v[124:127]\n"
    "v_mfma_f32_16x16x32_bf16 v[120:123], v[88:91], a[180:183], v[120:123]\n"
    "v_cvt_pk_bf16_f32 v136, v112, v113\n"
    "v_mfma_f32_16x16x32_bf16 v[124:127], v[92:95], a[180:183], v[124:127]\n"
    "v_cvt_pk_bf16_f32 v137, v114, v115\n"
    "v_mfma_f32_16x16x32_bf16 v[120:123], v[96:99], a[184:187], v[120:123]\n"
    "v_cvt_pk_bf16_f32 v138, v116, v117\n"
    "v_mfma_f32_16x16x32_bf16 v[124:127], v[100:103], a[184:187], v[124:127]\n"
    "v_cvt_pk_bf16_f32 v139, v118, v119\n"
    "v_mfma_f32_16x16x32_bf16 v[120:123], v[104:107], a[188:191], v[120:123]\n"
    "buffer_store_dwordx4 v[136:139], v1, s[40:43], s53 offen sc1\n"
    "v_mfma_f32_16x16x32_bf16 v[124:127], v[108:111], a[188:191], v[124:127]\n"
    "s_add_u32 s52, s52, 65536\n"
    "s_lshr_b32 s59, s52, 8\n"
    "s_and_b32 s59, s59, 0x2000\n"
    "s_add_u32 s53, s52, s59\n"
    "s_waitcnt lgkmcnt(0)\n"
    "s_barrier\n"
    "buffer_atomic_add v12, v2, s[40:43], s64 offen\n"
    "v_mfma_f32_16x16x32_bf16 v[112:115], v[64:67], a[192:195], 0\n"
    "ds_read_b128 v[16:19], v0 offset:0\n"
    "v_mfma_f32_16x16x32_bf16 v[116:119], v[68:71], a[192:195], 0\n"
    "ds_read_b128 v[20:23], v0 offset:1024\n"
    "v_mfma_f32_16x16x32_bf16 v[112:115], v[72:75], a[196:199], v[112:115]\n"
    "ds_read_b128 v[24:27], v0 offset:2048\n"
    "v_mfma_f32_16x16x32_bf16 v[116:119], v[76:79], a[196:199], v[116:119]\n"
    "ds_read_b128 v[28:31], v0 offset:3072\n"
    "v_mfma_f32_16x16x32_bf16 v[112:115], v[80:83], a[200:203], v[112:115]\n"
    "ds_read_b128 v[32:35], v0 offset:4096\n"
    "v_mfma_f32_16x16x32_bf16 v[116:119], v[84:87], a[200:203], v[116:119]\n"
    "ds_read_b128 v[36:39], v0 offset:5120\n"
    "v_mfma_f32_16x16x32_bf16 v[112:115], v[88:91], a[204:207], v[112:115]\n"
    "v_cvt_pk_bf16_f32 v140, v120, v121\n"
    "v_mfma_f32_16x16x32_bf16 v[116:119], v[92:95], a[204:207], v[116:119]\n"
    "v_cvt_pk_bf16_f32 v141, v122, v123\n"
    "v_mfma_f32_16x16x32_bf16 v[112:115], v[96:99], a[208:211], v[112:115]\n"
    "v_cvt_pk_bf16_f32 v142, v124, v125\n"
    "v_mfma_f32_16x16x32_bf16 v[116:119], v[100:103], a[208:211], v[116:119]\n"
    "v_cvt_pk_bf16_f32 v143, v126, v127\n"
    "v_mfma_f32_16x16x32_bf16 v[112:115], v[104:107], a[212:215], v[112:115]\n"
    "buffer_store_dwordx4 v[140:143], v1, s[40:43], s53 offen sc1\n"
    "v_mfma_f32_16x16x32_bf16 v[116:119], v[108:111], a[212:215], v[116:119]\n"
    "s_add_u32 s52, s52, 65536\n"
    "s_lshr_b32 s59, s52, 8\n"
    "s_and_b32 s59, s59, 0x2000\n"
    "s_add_u32 s53, s52, s59\n"
    "v_mfma_f32_16x16x32_bf16 v[120:123], v[64:67], a[216:219], 0\n"
    "ds_read_b128 v[40:43], v0 offset:6144\n"
    "v_mfma_f32_16x16x32_bf16 v[124:127], v[68:71], a[216:219], 0\n"
    "ds_read_b128 v[44:47], v0 offset:7168\n"
    "v_mfma_f32_16x16x32_bf16 v[120:123], v[72:75], a[220:223], v[120:123]\n"
    "ds_read_b128 v[48:51], v0 offset:8192\n"
    "v_mfma_f32_16x16x32_bf16 v[124:127], v[76:79], a[220:223], v[124:127]\n"
    "ds_read_b128 v[52:55], v0 offset:9216\n"
    "v_mfma_f32_16x16x32_bf16 v[120:123], v[80:83], a[224:227], v[120:123]\n"
    "ds_read_b128 v[56:59], v0 offset:10240\n"
    "v_mfma_f32_16x16x32_bf16 v[124:127], v[84:87], a[224:227], v[124:127]\n"
    "ds_read_b128 v[60:63], v0 offset:11264\n"
    "v_mfma_f32_16x16x32_bf16 v[120:123], v[88:91], a[228:231], v[120:123]\n"
    "v_cvt_pk_bf16_f32 v136, v112, v113\n"
    "v_mfma_f32_16x16x32_bf16 v[124:127], v[92:95], a[228:231], v[124:127]\n"
    "v_cvt_pk_bf16_f32 v137, v114, v115\n"
    "v_mfma_f32_16x16x32_bf16 v[120:123], v[96:99], a[232:235], v[120:123]\n"
    "v_cvt_pk_bf16_f32 v138, v116, v117\n"
    "v_mfma_f32_16x16x32_bf16 v[124:127], v[100:103], a[232:235], v[124:127]\n"
    "v_cvt_pk_bf16_f32 v139, v118, v119\n"
    "v_mfma_f32_16x16x32_bf16 v[120:123], v[104:107], a[236:239], v[120:123]\n"
    "buffer_store_dwordx4 v[136:139], v1, s[40:43], s53 offen sc1\n"
    "v_mfma_f32_16x16x32_bf16 v[124:127], v[108:111], a[236:239], v[124:127]\n"
    "s_add_u32 s52, s52, 65536\n"
    "s_lshr_b32 s59, s52, 8\n"
    "s_and_b32 s59, s59, 0x2000\n"
    "s_add_u32 s53, s52, s59\n"
    "s_waitcnt lgkmcnt(0)\n"
    "s_add_u32 s63, s63, 2621440\n"
    "s_and_b32 s59, s47, 31\n"
    "s_cmp_eq_u32 s59, 31\n"
    "s_cselect_b32 s63, s62, s63\n"
    "s_lshl_b32 s64, s47, 7\n"
    "s_add_u32 s64, s64, 2124032\n"
    "s_and_b32 s57, s47, 63\n"
    "s_sub_u32 s59, s57, 2\n"
    "s_cmp_lt_u32 s59, 61\n"
    "s_cbranch_scc1 hpo_late1_%=\n"
    "v_cvt_pk_bf16_f32 v140, v120, v121\n"
    "v_cvt_pk_bf16_f32 v141, v122, v123\n"
    "v_cvt_pk_bf16_f32 v142, v124, v125\n"
    "v_cvt_pk_bf16_f32 v143, v126, v127\n"
    "buffer_store_dwordx4 v[140:143], v1, s[40:43], s53 offen sc1\n"
    "s_waitcnt vmcnt(0)\n"
    "s_barrier\n"
    "buffer_atomic_add v12, v2, s[40:43], s64 offen\n"
    "s_mov_b32 s64, 0x40000000\n"
    "s_cmp_eq_u32 s57, 63\n"
    "s_cbranch_scc1 hp_ret_%=\n"
    "hpo_late1_%=:\n"
    "s_add_u32 s47, s47, 1\n"
    "s_branch hpo_loop_%=\n"
    "s_branch hp_ret_%=\n"
    "hp_b1_%=:\n"
    "s_and_b32 s26, s20, 7\n"
    "s_lshr_b32 s27, s20, 3\n"
    "s_lshl2_add_u32 s28, s27, s45\n"
    "s_lshl_b32 s48, s45, 3\n"
    "s_min_u32 s48, s48, 22\n"
    "s_lshl_b32 s61, s48, 10\n"
    "s_mul_i32 s50, s26, 307200\n"
    "s_add_u32 s50, s50, s61\n"
    "s_add_u32 s50, s50, 245301248\n"
    "s_add_u32 m0, s61, 0\n"
    "s_add_u32 s51, s50, 0\n"
    "buffer_load_dwordx4 v0, s[40:43], s51 offen offset:0 lds\n"
    "buffer_load_dwordx4 v0, s[40:43], s51 offen offset:1024 lds\n"
    "buffer_load_dwordx4 v0, s[40:43], s51 offen offset:2048 lds\n"
    "buffer_load_dwordx4 v0, s[40:43], s51 offen offset:3072 lds\n"
    "s_add_u32 m0, s61, 4096\n"
    "s_add_u32 s51, s50, 4096\n"
    "buffer_load_dwordx4 v0, s[40:43], s51 offen offset:0 lds\n"
    "buffer_load_dwordx4 v0, s[40:43], s51 offen offset:1024 lds\n"
    "buffer_load_dwordx4 v0, s[40:43], s51 offen offset:2048 lds\n"
    "buffer_load_dwordx4 v0, s[40:43], s51 offen offset:3072 lds\n"
    "s_add_u32 m0, s61, 30720\n"
    "s_add_u32 s51, s50, 30720\n"
    "buffer_load_dwordx4 v0, s[40:43], s51 offen offset:0 lds\n"
    "buffer_load_dwordx4 v0, s[40:43], s51 offen offset:1024 lds\n"
    "buffer_load_dwordx4 v0, s[40:43], s51 offen offset:2048 lds\n"
    "buffer_load_dwordx4 v0, s[40:43], s51 offen offset:3072 lds\n"
    "s_add_u32 m0, s61, 34816\n"
    "s_add_u32 s51, s50, 34816\n"
    "buffer_load_dwordx4 v0, s[40:43], s51 offen offset:0 lds\n"
    "buffer_load_dwordx4 v0, s[40:43], s51 offen offset:1024 lds\n"
    "buffer_load_dwordx4 v0, s[40:43], s51 offen offset:2048 lds\n"
    "buffer_load_dwordx4 v0, s[40:43], s51 offen offset:3072 lds\n"
    "s_add_u32 m0, s61, 61440\n"
    "s_add_u32 s51, s50, 61440\n"
    "buffer_load_dwordx4 v0, s[40:43], s51 offen offset:0 lds\n"
    "buffer_load_dwordx4 v0, s[40:43], s51 offen offset:1024 lds\n"
    "buffer_load_dwordx4 v0, s[40:43], s51 offen offset:2048 lds\n"
    "buffer_load_dwordx4 v0, s[40:43], s51 offen offset:3072 lds\n"
    "s_add_u32 m0, s61, 65536\n"
    "s_add_u32 s51, s50, 65536\n"
    "buffer_load_dwordx4 v0, s[40:43], s51 offen offset:0 lds\n"
    "buffer_load_dwordx4 v0, s[40:43], s51 offen offset:1024 lds\n"
    "buffer_load_dwordx4 v0, s[40:43], s51 offen offset:2048 lds\n"
    "buffer_load_dwordx4 v0, s[40:43], s51 offen offset:3072 lds\n"
    "s_add_u32 m0, s61, 92160\n"
    "s_add_u32 s51, s50, 92160\n"
    "buffer_load_dwordx4 v0, s[40:43], s51 offen offset:0 lds\n"
    "buffer_load_dwordx4 v0, s[40:43], s51 offen offset:1024 lds\n"
    "buffer_load_dwordx4 v0, s[40:43], s51 offen offset:2048 lds\n"
    "buffer_load_dwordx4 v0, s[40:43], s51 offen offset:3072 lds\n"
    "s_add_u32 m0, s61, 96256\n"
    "s_add_u32 s51, s50, 96256\n"
    "buffer_load_dwordx4 v0, s[40:43], s51 offen offset:0 lds\n"
    "buffer_load_dwordx4 v0, s[40:43], s51 offen offset:1024 lds\n"
    "buffer_load_dwordx4 v0, s[40:43], s51 offen offset:2048 lds\n"
    "buffer_load_dwordx4 v0, s[40:43], s51 offen offset:3072 lds\n"
    "s_add_u32 m0, s61, 122880\n"
    "s_add_u32 s51, s50, 122880\n"
    "buffer_load_dwordx4 v0, s[40:43], s51 offen offset:0 lds\n"
    "buffer_load_dwordx4 v0, s[40:43], s51 offen offset:1024 lds\n"
    "buffer_load_dwordx4 v0, s[40:43], s51 offen offset:2048 lds\n"
    "buffer_load_dwordx4 v0, s[40:43], s51 offen offset:3072 lds\n"
    "s_add_u32 m0, s61, 126976\n"
    "s_add_u32 s51, s50, 126976\n"
    "buffer_load_dwordx4 v0, s[40:43], s51 offen offset:0 lds\n"
    "buffer_load_dwordx4 v0, s[40:43], s51 offen offset:1024 lds\n"
    "buffer_load_dwordx4 v0, s[40:43], s51 offen offset:2048 lds\n"
    "buffer_load_dwordx4 v0, s[40:43], s51 offen offset:3072 lds\n"
    "v_mov_b32 v188, 0x3f803f80\n"
    "v_mov_b32 v189, 0x3f803f80\n"
    "v_mov_b32 v190, 0x3f803f80\n"
    "v_mov_b32 v191, 0x3f803f80\n"
    "v_add_u32 v6, 61440, v0\n"
    "v_add_u32 v7, 122880, v0\n"
    "s_lshl_b32 s54, s21, 5\n"
    "s_add_u32 s54, s54, s27\n"
    "s_lshl_b32 s54, s54, 7\n"
    "s_add_u32 s54, s54, 2354176\n"
    "s_waitcnt vmcnt(0)\n"
    "s_cmp_lg_u32 s45, 0\n"
    "s_cbranch_scc1 hpb1_dep_ok_%=\n"
    "s_mov_b32 s60, 0\n"
    "hpb1_dep_%=:\n"
    "buffer_load_dword v4, off, s[40:43], s54 sc1\n"
    "s_waitcnt vmcnt(0)\n"
    "v_readfirstlane_b32 s59, v4\n"
    "s_nop 0\n"
    "s_cmp_ge_u32 s59, 8\n"
    "s_nop 2\n"
    "s_cbranch_scc1 hpb1_dep_ok_%=\n"
    "s_sleep 3\n"
    "s_add_u32 s60, s60, 1\n"
    "s_cmp_gt_u32 s60, 65536\n"
    "s_cbranch_scc1 hp_abort_%=\n"
    "s_and_b32 s59, s60, 63\n"
    "s_cbranch_scc1 hpb1_dep_%=\n"
    "v_mov_b32 v4, 1024\n"
    "buffer_load_dword v4, v4, s[40:43], s58 offen sc1\n"
    "s_waitcnt vmcnt(0)\n"
    "v_readfirstlane_b32 s59, v4\n"
    "s_nop 0\n"
    "s_cmp_lg_u32 s59, 0\n"
    "s_nop 2\n"
    "s_cbranch_scc1 hp_end_%=\n"
    "s_branch hpb1_dep_%=\n"
    "hpb1_dep_ok_%=:\n"
    "s_barrier\n"
    "s_mul_i32 s52, s28, 20480\n"
    "s_add_u32 s52, s52, 242679808\n"
    "buffer_load_dwordx4 v[16:19], v0, s[40:43], s52 offen sc1\n"
    "buffer_load_dwordx4 v[20:23], v0, s[40:43], s52 offen offset:1024 sc1\n"
    "buffer_load_dwordx4 v[24:27], v0, s[40:43], s52 offen offset:2048 sc1\n"
    "buffer_load_dwordx4 v[28:31], v0, s[40:43], s52 offen offset:3072 sc1\n"
    "s_add_u32 s52, s52, 4096\n"
    "buffer_load_dwordx4 v[32:35], v0, s[40:43], s52 offen sc1\n"
    "buffer_load_dwordx4 v[36:39], v0, s[40:43], s52 offen offset:1024 sc1\n"
    "buffer_load_dwordx4 v[40:43], v0, s[40:43], s52 offen offset:2048 sc1\n"
    "buffer_load_dwordx4 v[44:47], v0, s[40:43], s52 offen offset:3072 sc1\n"
    "s_add_u32 s52, s52, 4096\n"
    "buffer_load_dwordx4 v[48:51], v0, s[40:43], s52 offen sc1\n"
    "buffer_load_dwordx4 v[52:55], v0, s[40:43], s52 offen offset:1024 sc1\n"
    "buffer_load_dwordx4 v[56:59], v0, s[40:43], s52 offen offset:2048 sc1\n"
    "buffer_load_dwordx4 v[60:63], v0, s[40:43], s52 offen offset:3072 sc1\n"
    "s_add_u32 s52, s52, 4096\n"
    "buffer_load_dwordx4 v[64:67], v0, s[40:43], s52 offen sc1\n"
    "buffer_load_dwordx4 v[68:71], v0, s[40:43], s52 offen offset:1024 sc1\n"
    "buffer_load_dwordx4 v[72:75], v0, s[40:43], s52 offen offset:2048 sc1\n"
    "buffer_load_dwordx4 v[76:79], v0, s[40:43], s52 offen offset:3072 sc1\n"
    "s_add_u32 s52, s52, 4096\n"
    "buffer_load_dwordx4 v[80:83], v0, s[40:43], s52 offen sc1\n"
    "buffer_load_dwordx4 v[84:87], v0, s[40:43], s52 offen offset:1024 sc1\n"
    "buffer_load_dwordx4 v[88:91], v0, s[40:43], s52 offen offset:2048 sc1\n"
    "buffer_load_dwordx4 v[92:95], v0, s[40:43], s52 offen offset:3072 sc1\n"
    "ds_read_b128 v[164:167], v0 offset:0\n"
    "ds_read_b128 v[168:171], v0 offset:1024\n"
    "ds_read_b128 v[172:175], v0 offset:2048\n"
    "ds_read_b128 v[176:179], v0 offset:3072\n"
    "ds_read_b128 v[180:183], v0 offset:4096\n"
    "ds_read_b128 v[184:187], v0 offset:5120\n"
    "s_waitcnt vmcnt(19) lgkmcnt(5)\n"
    "v_mfma_f32_16x16x32_bf16 v[96:99], v[164:167], v[16:19], 0\n"
    "ds_read_b128 v[164:167], v0 offset:6144\n"
    "s_waitcnt lgkmcnt(5)\n"
    "v_mfma_f32_16x16x32_bf16 v[100:103], v[168:171], v[16:19], 0\n"
    "ds_read_b128 v[168:171], v0 offset:7168\n"
    "s_waitcnt lgkmcnt(5)\n"
    "v_mfma_f32_16x16x32_bf16 v[104:107], v[172:175], v[16:19], 0\n"
    "ds_read_b128 v[172:175], v0 offset:8192\n"
    "s_waitcnt lgkmcnt(5)\n"
    "v_mfma_f32_16x16x32_bf16 v[108:111], v[176:179], v[16:19], 0\n"
    "ds_read_b128 v[176:179], v0 offset:9216\n"
    "s_waitcnt lgkmcnt(5)\n"
    "v_mfma_f32_16x16x32_bf16 v[112:115], v[180:183], v[16:19], 0\n"
    "ds_read_b128 v[180:183], v0 offset:10240\n"
    "s_waitcnt lgkmcnt(5)\n"
    "v_mfma_f32_16x16x32_bf16 v[116:119], v[184:187], v[16:19], 0\n"
    "ds_read_b128 v[184:187], v0 offset:11264\n"
    "s_waitcnt lgkmcnt(5)\n"
    "v_mfma_f32_16x16x32_bf16 v[120:123], v[164:167], v[16:19], 0\n"
    "ds_read_b128 v[164:167], v0 offset:12288\n"
    "s_waitcnt lgkmcnt(5)\n"
    "v_mfma_f32_16x16x32_bf16 v[124:127], v[168:171], v[16:19], 0\n"
    "ds_read_b128 v[168:171], v0 offset:13312\n"
    "v_mfma_f32_16x16x32_bf16 v[156:159], v[188:191], v[16:19], 0\n"
    "s_waitcnt lgkmcnt(5)\n"
    "v_mfma_f32_16x16x32_bf16 v[128:131], v[172:175], v[16:19], 0\n"
    "ds_read_b128 v[172:175], v0 offset:14336\n"
    "s_waitcnt lgkmcnt(5)\n"
    "v_mfma_f32_16x16x32_bf16 v[132:135], v[176:179], v[16:19], 0\n"
    "ds_read_b128 v[176:179], v0 offset:15360\n"
    "s_waitcnt lgkmcnt(5)\n"
    "v_mfma_f32_16x16x32_bf16 v[136:139], v[180:183], v[16:19], 0\n"
    "ds_read_b128 v[180:183], v0 offset:16384\n"
    "s_waitcnt lgkmcnt(5)\n"
    "v_mfma_f32_16x16x32_bf16 v[140:143], v[184:187], v[16:19], 0\n"
    "ds_read_b128 v[184:187], v0 offset:17408\n"
    "s_waitcnt lgkmcnt(5)\n"
    "v_mfma_f32_16x16x32_bf16 v[144:147], v[164:167], v[16:19], 0\n"
    "ds_read_b128 v[164:167], v0 offset:18432\n"
    "s_waitcnt lgkmcnt(5)\n"
    "v_mfma_f32_16x16x32_bf16 v[148:151], v[168:171], v[16:19], 0\n"
    "ds_read_b128 v[168:171], v0 offset:19456\n"
    "s_waitcnt lgkmcnt(5)\n"
    "v_mfma_f32_16x16x32_bf16 v[152:155], v[172:175], v[16:19], 0\n"
    "ds_read_b128 v[172:175], v0 offset:20480\n"
    "v_mfma_f32_16x16x32_bf16 v[160:163], v[16:19], v[16:19], 0\n"
    "s_waitcnt vmcnt(18) lgkmcnt(5)\n"
    "v_mfma_f32_16x16x32_bf16 v[96:99], v[176:179], v[20:23], v[96:99]\n"
    "ds_read_b128 v[176:179], v0 offset:21504\n"
    "s_waitcnt lgkmcnt(5)\n"
    "v_mfma_f32_16x16x32_bf16 v[100:103], v[180:183], v[20:23], v[100:103]\n"
    "ds_read_b128 v[180:183], v0 offset:22528\n"
    "s_waitcnt lgkmcnt(5)\n"
    "v_mfma_f32_16x16x32_bf16 v[104:107], v[184:187], v[20:23], v[104:107]\n"
    "ds_read_b128 v[184:187], v0 offset:23552\n"
    "s_waitcnt lgkmcnt(5)\n"
    "v_mfma_f32_16x16x32_bf16 v[108:111], v[164:167], v[20:23], v[108:111]\n"
    "ds_read_b128 v[164:167], v0 offset:24576\n"
    "s_waitcnt lgkmcnt(5)\n"
    "v_mfma_f32_16x16x32_bf16 v[112:115], v[168:171], v[20:23], v[112:115]\n"
    "ds_read_b128 v[168:171], v0 offset:25600\n"
    "s_waitcnt lgkmcnt(5)\n"
    "v_mfma_f32_16x16x32_bf16 v[116:119], v[172:175], v[20:23], v[116:119]\n"
    "ds_read_b128 v[172:175], v0 offset:26624\n"
    "s_waitcnt lgkmcnt(5)\n"
    "v_mfma_f32_16x16x32_bf16 v[120:123], v[176:179], v[20:23], v[120:123]\n"
    "ds_read_b128 v[176:179], v0 offset:27648\n"
    "s_waitcnt lgkmcnt(5)\n"
    "v_mfma_f32_16x16x32_bf16 v[124:127], v[180:183], v[20:23], v[124:127]\n"
    "ds_read_b128 v[180:183], v0 offset:28672\n"
    "v_mfma_f32_16x16x32_bf16 v[156:159], v[188:191], v[20:23], v[156:159]\n"
    "s_waitcnt lgkmcnt(5)\n"
    "v_mfma_f32_16x16x32_bf16 v[128:131], v[184:187], v[20:23], v[128:131]\n"
    "ds_read_b128 v[184:187], v0 offset:29696\n"
    "s_waitcnt lgkmcnt(5)\n"
    "v_mfma_f32_16x16x32_bf16 v[132:135], v[164:167], v[20:23], v[132:135]\n"
    "ds_read_b128 v[164:167], v0 offset:30720\n"
    "s_waitcnt lgkmcnt(5)\n"
    "v_mfma_f32_16x16x32_bf16 v[136:139], v[168:171], v[20:23], v[136:139]\n"
    "ds_read_b128 v[168:171], v0 offset:31744\n"
    "s_waitcnt lgkmcnt(5)\n"
    "v_mfma_f32_16x16x32_bf16 v[140:143], v[172:175], v[20:23], v[140:143]\n"
    "ds_read_b128 v[172:175], v0 offset:32768\n"
    "s_waitcnt lgkmcnt(5)\n"
    "v_mfma_f32_16x16x32_bf16 v[144:147], v[176:179], v[20:23], v[144:147]\n"
    "ds_read_b128 v[176:179], v0 offset:33792\n"
    "s_waitcnt lgkmcnt(5)\n"
    "v_mfma_f32_16x16x32_bf16 v[148:151], v[180:183], v[20:23], v[148:151]\n"
    "ds_read_b128 v[180:183], v0 offset:34816\n"
    "s_waitcnt lgkmcnt(5)\n"
    "v_mfma_f32_16x16x32_bf16 v[152:155], v[184:187], v[20:23], v[152:155]\n"
    "ds_read_b128 v[184:187], v0 offset:35840\n"
    "v_mfma_f32_16x16x32_bf16 v[160:163], v[20:23], v[20:23], v[160:163]\n"
    "s_waitcnt vmcnt(17) lgkmcnt(5)\n"
    "v_mfma_f32_16x16x32_bf16 v[96:99], v[164:167], v[24:27], v[96:99]\n"
    "s_barrier\n"
    "ds_read_b128 v[164:167], v0 offset:36864\n"
    "s_waitcnt lgkmcnt(5)\n"
    "v_mfma_f32_16x16x32_bf16 v[100:103], v[168:171], v[24:27], v[100:103]\n"
    "ds_read_b128 v[168:171], v0 offset:37888\n"
    "s_add_u32 m0, s61, 0\n"
    "s_add_u32 s51, s50, 153600\n"
    "buffer_load_dwordx4 v0, s[40:43], s51 offen offset:0 lds\n"
    "s_waitcnt lgkmcnt(5)\n"
    "v_mfma_f32_16x16x32_bf16 v[104:107], v[172:175], v[24:27], v[104:107]\n"
    "ds_read_b128 v[172:175], v0 offset:38912\n"
    "s_waitcnt lgkmcnt(5)\n"
    "v_mfma_f32_16x16x32_bf16 v[108:111], v[176:179], v[24:27], v[108:111]\n"
    "ds_read_b128 v[176:179], v0 offset:39936\n"
    "s_waitcnt lgkmcnt(5)\n"
    "v_mfma_f32_16x16x32_bf16 v[112:115], v[180:183], v[24:27], v[112:115]\n"
    "ds_read_b128 v[180:183], v0 offset:40960\n"
    "buffer_load_dwordx4 v0, s[40:43], s51 offen offset:1024 lds\n"
    "s_waitcnt lgkmcnt(5)\n"
    "v_mfma_f32_16x16x32_bf16 v[116:119], v[184:187], v[24:27], v[116:119]\n"
    "ds_read_b128 v[184:187], v0 offset:41984\n"
    "s_waitcnt lgkmcnt(5)\n"
    "v_mfma_f32_16x16x32_bf16 v[120:123], v[164:167], v[24:27], v[120:123]\n"
    "ds_read_b128 v[164:167], v0 offset:43008\n"
    "s_waitcnt lgkmcnt(5)\n"
    "v_mfma_f32_16x16x32_bf16 v[124:127], v[168:171], v[24:27], v[124:127]\n"
    "ds_read_b128 v[168:171], v0 offset:44032\n"
    "buffer_load_dwordx4 v0, s[40:43], s51 offen offset:2048 lds\n"
    "v_mfma_f32_16x16x32_bf16 v[156:159], v[188:191], v[24:27], v[156:159]\n"
    "s_waitcnt lgkmcnt(5)\n"
    "v_mfma_f32_16x16x32_bf16 v[128:131], v[172:175], v[24:27], v[128:131]\n"
    "ds_read_b128 v[172:175], v0 offset:45056\n"
    "s_waitcnt lgkmcnt(5)\n"
    "v_mfma_f32_16x16x32_bf16 v[132:135], v[176:179], v[24:27], v[132:135]\n"
    "ds_read_b128 v[176:179], v0 offset:46080\n"
    "s_waitcnt lgkmcnt(5)\n"
    "v_mfma_f32_16x16x32_bf16 v[136:139], v[180:183], v[24:27], v[136:139]\n"
    "ds_read_b128 v[180:183], v0 offset:47104\n"
    "buffer_load_dwordx4 v0, s[40:43], s51 offen offset:3072 lds\n"
    "s_waitcnt lgkmcnt(5)\n"
    "v_mfma_f32_16x16x32_bf16 v[140:143], v[184:187], v[24:27], v[140:143]\n"
    "ds_read_b128 v[184:187], v0 offset:48128\n"
    "s_waitcnt lgkmcnt(5)\n"
    "v_mfma_f32_16x16x32_bf16 v[144:147], v[164:167], v[24:27], v[144:147]\n"
    "ds_read_b128 v[164:167], v0 offset:49152\n"
    "s_waitcnt lgkmcnt(5)\n"
    "v_mfma_f32_16x16x32_bf16 v[148:151], v[168:171], v[24:27], v[148:151]\n"
    "ds_read_b128 v[168:171], v0 offset:50176\n"
    "s_add_u32 m0, s61, 4096\n"
    "s_add_u32 s51, s50, 157696\n"
    "buffer_load_dwordx4 v0, s[40:43], s51 offen offset:0 lds\n"
    "s_waitcnt lgkmcnt(5)\n"
    "v_mfma_f32_16x16x32_bf16 v[152:155], v[172:175], v[24:27], v[152:155]\n"
    "ds_read_b128 v[172:175], v0 offset:51200\n"
    "v_mfma_f32_16x16x32_bf16 v[160:163], v[24:27], v[24:27], v[160:163]\n"
    "s_waitcnt vmcnt(21) lgkmcnt(5)\n"
    "v_mfma_f32_16x16x32_bf16 v[96:99], v[176:179], v[28:31], v[96:99]\n"
    "ds_read_b128 v[176:179], v0 offset:52224\n"
    "s_waitcnt lgkmcnt(5)\n"
    "v_mfma_f32_16x16x32_bf16 v[100:103], v[180:183], v[28:31], v[100:103]\n"
    "ds_read_b128 v[180:183], v0 offset:53248\n"
    "buffer_load_dwordx4 v0, s[40:43], s51 offen offset:1024 lds\n"
    "s_waitcnt lgkmcnt(5)\n"
    "v_mfma_f32_16x16x32_bf16 v[104:107], v[184:187], v[28:31], v[104:107]\n"
    "ds_read_b128 v[184:187], v0 offset:54272\n"
    "s_waitcnt lgkmcnt(5)\n"
    "v_mfma_f32_16x16x32_bf16 v[108:111], v[164:167], v[28:31], v[108:111]\n"
    "ds_read_b128 v[164:167], v0 offset:55296\n"
    "s_waitcnt lgkmcnt(5)\n"
    "v_mfma_f32_16x16x32_bf16 v[112:115], v[168:171], v[28:31], v[112:115]\n"
    "ds_read_b128 v[168:171], v0 offset:56320\n"
    "buffer_load_dwordx4 v0, s[40:43], s51 offen offset:2048 lds\n"
    "s_waitcnt lgkmcnt(5)\n"
    "v_mfma_f32_16x16x32_bf16 v[116:119], v[172:175], v[28:31], v[116:119]\n"
    "ds_read_b128 v[172:175], v0 offset:57344\n"
    "s_waitcnt lgkmcnt(5)\n"
    "v_mfma_f32_16x16x32_bf16 v[120:123], v[176:179], v[28:31], v[120:123]\n"
    "ds_read_b128 v[176:179], v0 offset:58368\n"
    "s_waitcnt lgkmcnt(5)\n"
    "v_mfma_f32_16x16x32_bf16 v[124:127], v[180:183], v[28:31], v[124:127]\n"
    "ds_read_b128 v[180:183], v0 offset:59392\n"
    "buffer_load_dwordx4 v0, s[40:43], s51 offen offset:3072 lds\n"
    "v_mfma_f32_16x16x32_bf16 v[156:159], v[188:191], v[28:31], v[156:159]\n"
    "s_waitcnt lgkmcnt(5)\n"
    "v_mfma_f32_16x16x32_bf16 v[128:131], v[184:187], v[28:31], v[128:131]\n"
    "ds_read_b128 v[184:187], v0 offset:60416\n"
    "s_waitcnt lgkmcnt(5)\n"
    "v_mfma_f32_16x16x32_bf16 v[132:135], v[164:167], v[28:31], v[132:135]\n"
    "ds_read_b128 v[164:167], v6 offset:0\n"
    "s_waitcnt lgkmcnt(5)\n"
    "v_mfma_f32_16x16x32_bf16 v[136:139], v[168:171], v[28:31], v[136:139]\n"
    "ds_read_b128 v[168:171], v6 offset:1024\n"
    "s_waitcnt lgkmcnt(5)\n"
    "v_mfma_f32_16x16x32_bf16 v[140:143], v[172:175], v[28:31], v[140:143]\n"
    "ds_read_b128 v[172:175], v6 offset:2048\n"
    "s_waitcnt lgkmcnt(5)\n"
    "v_mfma_f32_16x16x32_bf16 v[144:147], v[176:179], v[28:31], v[144:147]\n"
    "ds_read_b128 v[176:179], v6 offset:3072\n"
    "s_waitcnt lgkmcnt(5)\n"
    "v_mfma_f32_16x16x32_bf16 v[148:151], v[180:183], v[28:31], v[148:151]\n"
    "ds_read_b128 v[180:183], v6 offset:4096\n"
    "s_waitcnt lgkmcnt(5)\n"
    "v_mfma_f32_16x16x32_bf16 v[152:155], v[184:187], v[28:31], v[152:155]\n"
    "ds_read_b128 v[184:187], v6 offset:5120\n"
    "v_mfma_f32_16x16x32_bf16 v[160:163], v[28:31], v[28:31], v[160:163]\n"
    "s_waitcnt vmcnt(23) lgkmcnt(5)\n"
    "v_mfma_f32_16x16x32_bf16 v[96:99], v[164:167], v[32:35], v[96:99]\n"
    "s_barrier\n"
    "ds_read_b128 v[164:167], v6 offset:6144\n"
    "s_waitcnt lgkmcnt(5)\n"
    "v_mfma_f32_16x16x32_bf16 v[100:103], v[168:171], v[32:35], v[100:103]\n"
    "ds_read_b128 v[168:171], v6 offset:7168\n"
    "s_add_u32 m0, s61, 30720\n"
    "s_add_u32 s51, s50, 184320\n"
    "buffer_load_dwordx4 v0, s[40:43], s51 offen offset:0 lds\n"
    "s_waitcnt lgkmcnt(5)\n"
    "v_mfma_f32_16x16x32_bf16 v[104:107], v[172:175], v[32:35], v[104:107]\n"
    "ds_read_b128 v[172:175], v6 offset:8192\n"
    "s_waitcnt lgkmcnt(5)\n"
    "v_mfma_f32_16x16x32_bf16 v[108:111], v[176:179], v[32:35], v[108:111]\n"
    "ds_read_b128 v[176:179], v6 offset:9216\n"
    "s_waitcnt lgkmcnt(5)\n"
    "v_mfma_f32_16x16x32_bf16 v[112:115], v[180:183], v[32:35], v[112:115]\n"
    "ds_read_b128 v[180:183], v6 offset:10240\n"
    "buffer_load_dwordx4 v0, s[40:43], s51 offen offset:1024 lds\n"
    "s_waitcnt lgkmcnt(5)\n"
    "v_mfma_f32_16x16x32_bf16 v[116:119], v[184:187], v[32:35], v[116:119]\n"
    "ds_read_b128 v[184:187], v6 offset:11264\n"
    "s_waitcnt lgkmcnt(5)\n"
    "v_mfma_f32_16x16x32_bf16 v[120:123], v[164:167], v[32:35], v[120:123]\n"
    "ds_read_b128 v[164:167], v6 offset:12288\n"
    "s_waitcnt lgkmcnt(5)\n"
    "v_mfma_f32_16x16x32_bf16 v[124:127], v[168:171], v[32:35], v[124:127]\n"
    "ds_read_b128 v[168:171], v6 offset:13312\n"
    "buffer_load_dwordx4 v0, s[40:43], s51 offen offset:2048 lds\n"
    "v_mfma_f32_16x16x32_bf16 v[156:159], v[188:191], v[32:35], v[156:159]\n"
    "s_waitcnt lgkmcnt(5)\n"
    "v_mfma_f32_16x16x32_bf16 v[128:131], v[172:175], v[32:35], v[128:131]\n"
    "ds_read_b128 v[172:175], v6 offset:14336\n"
    "s_waitcnt lgkmcnt(5)\n"
    "v_mfma_f32_16x16x32_bf16 v[132:135], v[176:179], v[32:35], v[132:135]\n"
    "ds_read_b128 v[176:179], v6 offset:15360\n"
    "s_waitcnt lgkmcnt(5)\n"
    "v_mfma_f32_16x16x32_bf16 v[136:139], v[180:183], v[32:35], v[136:139]\n"
    "ds_read_b128 v[180:183], v6 offset:16384\n"
    "buffer_load_dwordx4 v0, s[40:43], s51 offen offset:3072 lds\n"
    "s_waitcnt lgkmcnt(5)\n"
    "v_mfma_f32_16x16x32_bf16 v[140:143], v[184:187], v[32:35], v[140:143]\n"
    "ds_read_b128 v[184:187], v6 offset:17408\n"
    "s_waitcnt lgkmcnt(5)\n"
    "v_mfma_f32_16x16x32_bf16 v[144:147], v[164:167], v[32:35], v[144:147]\n"
    "ds_read_b128 v[164:167], v6 offset:18432\n"
    "s_waitcnt lgkmcnt(5)\n"
    "v_mfma_f32_16x16x32_bf16 v[148:151], v[168:171], v[32:35], v[148:151]\n"
    "ds_read_b128 v[168:171], v6 offset:19456\n"
    "s_add_u32 m0, s61, 34816\n"
    "s_add_u32 s51, s50, 188416\n"
    "buffer_load_dwordx4 v0, s[40:43], s51 offen offset:0 lds\n"
    "s_waitcnt lgkmcnt(5)\n"
    "v_mfma_f32_16x16x32_bf16 v[152:155], v[172:175], v[32:35], v[152:155]\n"
    "ds_read_b128 v[172:175], v6 offset:20480\n"
    "v_mfma_f32_16x16x32_bf16 v[160:163], v[32:35], v[32:35], v[160:163]\n"
    "s_waitcnt vmcnt(27) lgkmcnt(5)\n"
    "v_mfma_f32_16x16x32_bf16 v[96:99], v[176:179], v[36:39], v[96:99]\n"
    "ds_read_b128 v[176:179], v6 offset:21504\n"
    "s_waitcnt lgkmcnt(5)\n"
    "v_mfma_f32_16x16x32_bf16 v[100:103], v[180:183], v[36:39], v[100:103]\n"
    "ds_read_b128 v[180:183], v6 offset:22528\n"
    "buffer_load_dwordx4 v0, s[40:43], s51 offen offset:1024 lds\n"
    "s_waitcnt lgkmcnt(5)\n"
    "v_mfma_f32_16x16x32_bf16 v[104:107], v[184:187], v[36:39], v[104:107]\n"
    "ds_read_b128 v[184:187], v6 offset:23552\n"
    "s_waitcnt lgkmcnt(5)\n"
    "v_mfma_f32_16x16x32_bf16 v[108:111], v[164:167], v[36:39], v[108:111]\n"
    "ds_read_b128 v[164:167], v6 offset:24576\n"
    "s_waitcnt lgkmcnt(5)\n"
    "v_mfma_f32_16x16x32_bf16 v[112:115], v[168:171], v[36:39], v[112:115]\n"
    "ds_read_b128 v[168:171], v6 offset:25600\n"
    "buffer_load_dwordx4 v0, s[40:43], s51 offen offset:2048 lds\n"
    "s_waitcnt lgkmcnt(5)\n"
    "v_mfma_f32_16x16x32_bf16 v[116:119], v[172:175], v[36:39], v[116:119]\n"
    "ds_read_b128 v[172:175], v6 offset:26624\n"
    "s_waitcnt lgkmcnt(5)\n"
    "v_mfma_f32_16x16x32_bf16 v[120:123], v[176:179], v[36:39], v[120:123]\n"
    "ds_read_b128 v[176:179], v6 offset:27648\n"
    "s_waitcnt lgkmcnt(5)\n"
    "v_mfma_f32_16x16x32_bf16 v[124:127], v[180:183], v[36:39], v[124:127]\n"
    "ds_read_b128 v[180:183], v6 offset:28672\n"
    "buffer_load_dwordx4 v0, s[40:43], s51 offen offset:3072 lds\n"
    "v_mfma_f32_16x16x32_bf16 v[156:159], v[188:191], v[36:39], v[156:159]\n"
    "s_waitcnt lgkmcnt(5)\n"
    "v_mfma_f32_16x16x32_bf16 v[128:131], v[184:187], v[36:39], v[128:131]\n"
    "ds_read_b128 v[184:187], v6 offset:29696\n"
    "s_waitcnt lgkmcnt(5)\n"
    "v_mfma_f32_16x16x32_bf16 v[132:135], v[164:167], v[36:39], v[132:135]\n"
    "ds_read_b128 v[164:167], v6 offset:30720\n"
    "s_waitcnt lgkmcnt(5)\n"
    "v_mfma_f32_16x16x32_bf16 v[136:139], v[168:171], v[36:39], v[136:139]\n"
    "ds_read_b128 v[168:171], v6 offset:31744\n"
    "s_waitcnt lgkmcnt(5)\n"
    "v_mfma_f32_16x16x32_bf16 v[140:143], v[172:175], v[36:39], v[140:143]\n"
    "ds_read_b128 v[172:175], v6 offset:32768\n"
    "s_waitcnt lgkmcnt(5)\n"
    "v_mfma_f32_16x16x32_bf16 v[144:147], v[176:179], v[36:39], v[144:147]\n"
    "ds_read_b128 v[176:179], v6 offset:33792\n"
    "s_waitcnt lgkmcnt(5)\n"
    "v_mfma_f32_16x16x32_bf16 v[148:151], v[180:183], v[36:39], v[148:151]\n"
    "ds_read_b128 v[180:183], v6 offset:34816\n"
    "s_waitcnt lgkmcnt(5)\n"
    "v_mfma_f32_16x16x32_bf16 v[152:155], v[184:187], v[36:39], v[152:155]\n"
    "ds_read_b128 v[184:187], v6 offset:35840\n"
    "v_mfma_f32_16x16x32_bf16 v[160:163], v[36:39], v[36:39], v[160:163]\n"
    "s_waitcnt vmcnt(29) lgkmcnt(5)\n"
    "v_mfma_f32_16x16x32_bf16 v[96:99], v[164:167], v[40:43], v[96:99]\n"
    "s_barrier\n"
    "ds_read_b128 v[164:167], v6 offset:36864\n"
    "s_waitcnt lgkmcnt(5)\n"
    "v_mfma_f32_16x16x32_bf16 v[100:103], v[168:171], v[40:43], v[100:103]\n"
    "ds_read_b128 v[168:171], v6 offset:37888\n"
    "s_add_u32 m0, s61, 61440\n"
    "s_add_u32 s51, s50, 215040\n"
    "buffer_load_dwordx4 v0, s[40:43], s51 offen offset:0 lds\n"
    "s_waitcnt lgkmcnt(5)\n"
    "v_mfma_f32_16x16x32_bf16 v[104:107], v[172:175], v[40:43], v[104:107]\n"
    "ds_read_b128 v[172:175], v6 offset:38912\n"
    "s_waitcnt lgkmcnt(5)\n"
    "v_mfma_f32_16x16x32_bf16 v[108:111], v[176:179], v[40:43], v[108:111]\n"
    "ds_read_b128 v[176:179], v6 offset:39936\n"
    "s_waitcnt lgkmcnt(5)\n"
    "v_mfma_f32_16x16x32_bf16 v[112:115], v[180:183], v[40:43], v[112:115]\n"
    "ds_read_b128 v[180:183], v6 offset:40960\n"
    "buffer_load_dwordx4 v0, s[40:43], s51 offen offset:1024 lds\n"
    "s_waitcnt lgkmcnt(5)\n"
    "v_mfma_f32_16x16x32_bf16 v[116:119], v[184:187], v[40:43], v[116:119]\n"
    "ds_read_b128 v[184:187], v6 offset:41984\n"
    "s_waitcnt lgkmcnt(5)\n"
    "v_mfma_f32_16x16x32_bf16 v[120:123], v[164:167], v[40:43], v[120:123]\n"
    "ds_read_b128 v[164:167], v6 offset:43008\n"
    "s_waitcnt lgkmcnt(5)\n"
    "v_mfma_f32_16x16x32_bf16 v[124:127], v[168:171], v[40:43], v[124:127]\n"
    "ds_read_b128 v[168:171], v6 offset:44032\n"
    "buffer_load_dwordx4 v0, s[40:43], s51 offen offset:2048 lds\n"
    "v_mfma_f32_16x16x32_bf16 v[156:159], v[188:191], v[40:43], v[156:159]\n"
    "s_waitcnt lgkmcnt(5)\n"
    "v_mfma_f32_16x16x32_bf16 v[128:131], v[172:175], v[40:43], v[128:131]\n"
    "ds_read_b128 v[172:175], v6 offset:45056\n"
    "s_waitcnt lgkmcnt(5)\n"
    "v_mfma_f32_16x16x32_bf16 v[132:135], v[176:179], v[40:43], v[132:135]\n"
    "ds_read_b128 v[176:179], v6 offset:46080\n"
    "s_waitcnt lgkmcnt(5)\n"
    "v_mfma_f32_16x16x32_bf16 v[136:139], v[180:183], v[40:43], v[136:139]\n"
    "ds_read_b128 v[180:183], v6 offset:47104\n"
    "buffer_load_dwordx4 v0, s[40:43], s51 offen offset:3072 lds\n"
    "s_waitcnt lgkmcnt(5)\n"
    "v_mfma_f32_16x16x32_bf16 v[140:143], v[184:187], v[40:43], v[140:143]\n"
    "ds_read_b128 v[184:187], v6 offset:48128\n"
    "s_waitcnt lgkmcnt(5)\n"
    "v_mfma_f32_16x16x32_bf16 v[144:147], v[164:167], v[40:43], v[144:147]\n"
    "ds_read_b128 v[164:167], v6 offset:49152\n"
    "s_waitcnt lgkmcnt(5)\n"
    "v_mfma_f32_16x16x32_bf16 v[148:151], v[168:171], v[40:43], v[148:151]\n"
    "ds_read_b128 v[168:171], v6 offset:50176\n"
    "s_add_u32 m0, s61, 65536\n"
    "s_add_u32 s51, s50, 219136\n"
    "buffer_load_dwordx4 v0, s[40:43], s51 offen offset:0 lds\n"
    "s_waitcnt lgkmcnt(5)\n"
    "v_mfma_f32_16x16x32_bf16 v[152:155], v[172:175], v[40:43], v[152:155]\n"
    "ds_read_b128 v[172:175], v6 offset:51200\n"
    "v_mfma_f32_16x16x32_bf16 v[160:163], v[40:43], v[40:43], v[160:163]\n"
    "s_waitcnt vmcnt(33) lgkmcnt(5)\n"
    "v_mfma_f32_16x16x32_bf16 v[96:99], v[176:179], v[44:47], v[96:99]\n"
    "ds_read_b128 v[176:179], v6 offset:52224\n"
    "s_waitcnt lgkmcnt(5)\n"
    "v_mfma_f32_16x16x32_bf16 v[100:103], v[180:183], v[44:47], v[100:103]\n"
    "ds_read_b128 v[180:183], v6 offset:53248\n"
    "buffer_load_dwordx4 v0, s[40:43], s51 offen offset:1024 lds\n"
    "s_waitcnt lgkmcnt(5)\n"
    "v_mfma_f32_16x16x32_bf16 v[104:107], v[184:187], v[44:47], v[104:107]\n"
    "ds_read_b128 v[184:187], v6 offset:54272\n"
    "s_waitcnt lgkmcnt(5)\n"
    "v_mfma_f32_16x16x32_bf16 v[108:111], v[164:167], v[44:47], v[108:111]\n"
    "ds_read_b128 v[164:167], v6 offset:55296\n"
    "s_waitcnt lgkmcnt(5)\n"
    "v_mfma_f32_16x16x32_bf16 v[112:115], v[168:171], v[44:47], v[112:115]\n"
    "ds_read_b128 v[168:171], v6 offset:56320\n"
    "buffer_load_dwordx4 v0, s[40:43], s51 offen offset:2048 lds\n"
    "s_waitcnt lgkmcnt(5)\n"
    "v_mfma_f32_16x16x32_bf16 v[116:119], v[172:175], v[44:47], v[116:119]\n"
    "ds_read_b128 v[172:175], v6 offset:57344\n"
    "s_waitcnt lgkmcnt(5)\n"
    "v_mfma_f32_16x16x32_bf16 v[120:123], v[176:179], v[44:47], v[120:123]\n"
    "ds_read_b128 v[176:179], v6 offset:58368\n"
    "s_waitcnt lgkmcnt(5)\n"
    "v_mfma_f32_16x16x32_bf16 v[124:127], v[180:183], v[44:47], v[124:127]\n"
    "ds_read_b128 v[180:183], v6 offset:59392\n"
    "buffer_load_dwordx4 v0, s[40:43], s51 offen offset:3072 lds\n"
    "v_mfma_f32_16x16x32_bf16 v[156:159], v[188:191], v[44:47], v[156:159]\n"
    "s_waitcnt lgkmcnt(5)\n"
    "v_mfma_f32_16x16x32_bf16 v[128:131], v[184:187], v[44:47], v[128:131]\n"
    "ds_read_b128 v[184:187], v6 offset:60416\n"
    "s_waitcnt lgkmcnt(5)\n"
    "v_mfma_f32_16x16x32_bf16 v[132:135], v[164:167], v[44:47], v[132:135]\n"
    "ds_read_b128 v[164:167], v7 offset:0\n"
    "s_waitcnt lgkmcnt(5)\n"
    "v_mfma_f32_16x16x32_bf16 v[136:139], v[168:171], v[44:47], v[136:139]\n"
    "ds_read_b128 v[168:171], v7 offset:1024\n"
    "s_waitcnt lgkmcnt(5)\n"
    "v_mfma_f32_16x16x32_bf16 v[140:143], v[172:175], v[44:47], v[140:143]\n"
    "ds_read_b128 v[172:175], v7 offset:2048\n"
    "s_waitcnt lgkmcnt(5)\n"
    "v_mfma_f32_16x16x32_bf16 v[144:147], v[176:179], v[44:47], v[144:147]\n"
    "ds_read_b128 v[176:179], v7 offset:3072\n"
    "s_waitcnt lgkmcnt(5)\n"
    "v_mfma_f32_16x16x32_bf16 v[148:151], v[180:183], v[44:47], v[148:151]\n"
    "ds_read_b128 v[180:183], v7 offset:4096\n"
    "s_waitcnt lgkmcnt(5)\n"
    "v_mfma_f32_16x16x32_bf16 v[152:155], v[184:187], v[44:47], v[152:155]\n"
    "ds_read_b128 v[184:187], v7 offset:5120\n"
    "v_mfma_f32_16x16x32_bf16 v[160:163], v[44:47], v[44:47], v[160:163]\n"
    "s_waitcnt vmcnt(35) lgkmcnt(5)\n"
    "v_mfma_f32_16x16x32_bf16 v[96:99], v[164:167], v[48:51], v[96:99]\n"
    "s_barrier\n"
    "ds_read_b128 v[164:167], v7 offset:6144\n"
    "s_waitcnt lgkmcnt(5)\n"
    "v_mfma_f32_16x16x32_bf16 v[100:103], v[168:171], v[48:51], v[100:103]\n"
    "ds_read_b128 v[168:171], v7 offset:7168\n"
    "s_add_u32 m0, s61, 92160\n"
    "s_add_u32 s51, s50, 245760\n"
    "buffer_load_dwordx4 v0, s[40:43], s51 offen offset:0 lds\n"
    "s_waitcnt lgkmcnt(5)\n"
    "v_mfma_f32_16x16x32_bf16 v[104:107], v[172:175], v[48:51], v[104:107]\n"
    "ds_read_b128 v[172:175], v7 offset:8192\n"
    "s_waitcnt lgkmcnt(5)\n"
    "v_mfma_f32_16x16x32_bf16 v[108:111], v[176:179], v[48:51], v[108:111]\n"
    "ds_read_b128 v[176:179], v7 offset:9216\n"
    "s_waitcnt lgkmcnt(5)\n"
    "v_mfma_f32_16x16x32_bf16 v[112:115], v[180:183], v[48:51], v[112:115]\n"
    "ds_read_b128 v[180:183], v7 offset:10240\n"
    "buffer_load_dwordx4 v0, s[40:43], s51 offen offset:1024 lds\n"
    "s_waitcnt lgkmcnt(5)\n"
    "v_mfma_f32_16x16x32_bf16 v[116:119], v[184:187], v[48:51], v[116:119]\n"
    "ds_read_b128 v[184:187], v7 offset:11264\n"
    "s_waitcnt lgkmcnt(5)\n"
    "v_mfma_f32_16x16x32_bf16 v[120:123], v[164:167], v[48:51], v[120:123]\n"
    "ds_read_b128 v[164:167], v7 offset:12288\n"
    "s_waitcnt lgkmcnt(5)\n"
    "v_mfma_f32_16x16x32_bf16 v[124:127], v[168:171], v[48:51], v[124:127]\n"
    "ds_read_b128 v[168:171], v7 offset:13312\n"
    "buffer_load_dwordx4 v0, s[40:43], s51 offen offset:2048 lds\n"
    "v_mfma_f32_16x16x32_bf16 v[156:159], v[188:191], v[48:51], v[156:159]\n"
    "s_waitcnt lgkmcnt(5)\n"
    "v_mfma_f32_16x16x32_bf16 v[128:131], v[172:175], v[48:51], v[128:131]\n"
    "ds_read_b128 v[172:175], v7 offset:14336\n"
    "s_waitcnt lgkmcnt(5)\n"
    "v_mfma_f32_16x16x32_bf16 v[132:135], v[176:179], v[48:51], v[132:135]\n"
    "ds_read_b128 v[176:179], v7 offset:15360\n"
    "s_waitcnt lgkmcnt(5)\n"
    "v_mfma_f32_16x16x32_bf16 v[136:139], v[180:183], v[48:51], v[136:139]\n"
    "ds_read_b128 v[180:183], v7 offset:16384\n"
    "buffer_load_dwordx4 v0, s[40:43], s51 offen offset:3072 lds\n"
    "s_waitcnt lgkmcnt(5)\n"
    "v_mfma_f32_16x16x32_bf16 v[140:143], v[184:187], v[48:51], v[140:143]\n"
    "ds_read_b128 v[184:187], v7 offset:17408\n"
    "s_waitcnt lgkmcnt(5)\n"
    "v_mfma_f32_16x16x32_bf16 v[144:147], v[164:167], v[48:51], v[144:147]\n"
    "ds_read_b128 v[164:167], v7 offset:18432\n"
    "s_waitcnt lgkmcnt(5)\n"
    "v_mfma_f32_16x16x32_bf16 v[148:151], v[168:171], v[48:51], v[148:151]\n"
    "ds_read_b128 v[168:171], v7 offset:19456\n"
    "s_add_u32 m0, s61, 96256\n"
    "s_add_u32 s51, s50, 249856\n"
    "buffer_load_dwordx4 v0, s[40:43], s51 offen offset:0 lds\n"
    "s_waitcnt lgkmcnt(5)\n"
    "v_mfma_f32_16x16x32_bf16 v[152:155], v[172:175], v[48:51], v[152:155]\n"
    "ds_read_b128 v[172:175], v7 offset:20480\n"
    "v_mfma_f32_16x16x32_bf16 v[160:163], v[48:51], v[48:51], v[160:163]\n"
    "s_waitcnt vmcnt(39) lgkmcnt(5)\n"
    "v_mfma_f32_16x16x32_bf16 v[96:99], v[176:179], v[52:55], v[96:99]\n"
    "ds_read_b128 v[176:179], v7 offset:21504\n"
    "s_waitcnt lgkmcnt(5)\n"
    "v_mfma_f32_16x16x32_bf16 v[100:103], v[180:183], v[52:55], v[100:103]\n"
    "ds_read_b128 v[180:183], v7 offset:22528\n"
    "buffer_load_dwordx4 v0, s[40:43], s51 offen offset:1024 lds\n"
    "s_waitcnt lgkmcnt(5)\n"
    "v_mfma_f32_16x16x32_bf16 v[104:107], v[184:187], v[52:55], v[104:107]\n"
    "ds_read_b128 v[184:187], v7 offset:23552\n"
    "s_waitcnt lgkmcnt(5)\n"
    "v_mfma_f32_16x16x32_bf16 v[108:111], v[164:167], v[52:55], v[108:111]\n"
    "ds_read_b128 v[164:167], v7 offset:24576\n"
    "s_waitcnt lgkmcnt(5)\n"
    "v_mfma_f32_16x16x32_bf16 v[112:115], v[168:171], v[52:55], v[112:115]\n"
    "ds_read_b128 v[168:171], v7 offset:25600\n"
    "buffer_load_dwordx4 v0, s[40:43], s51 offen offset:2048 lds\n"
    "s_waitcnt lgkmcnt(5)\n"
    "v_mfma_f32_16x16x32_bf16 v[116:119], v[172:175], v[52:55], v[116:119]\n"
    "ds_read_b128 v[172:175], v7 offset:26624\n"
    "s_waitcnt lgkmcnt(5)\n"
    "v_mfma_f32_16x16x32_bf16 v[120:123], v[176:179], v[52:55], v[120:123]\n"
    "ds_read_b128 v[176:179], v7 offset:27648\n"
    "s_waitcnt lgkmcnt(5)\n"
    "v_mfma_f32_16x16x32_bf16 v[124:127], v[180:183], v[52:55], v[124:127]\n"
    "ds_read_b128 v[180:183], v7 offset:28672\n"
    "buffer_load_dwordx4 v0, s[40:43], s51 offen offset:3072 lds\n"
    "v_mfma_f32_16x16x32_bf16 v[156:159], v[188:191], v[52:55], v[156:159]\n"
    "s_waitcnt lgkmcnt(5)\n"
    "v_mfma_f32_16x16x32_bf16 v[128:131], v[184:187], v[52:55], v[128:131]\n"
    "ds_read_b128 v[184:187], v7 offset:29696\n"
    "s_waitcnt lgkmcnt(5)\n"
    "v_mfma_f32_16x16x32_bf16 v[132:135], v[164:167], v[52:55], v[132:135]\n"
    "s_waitcnt vmcnt(16)\n"
    "s_barrier\n"
    "ds_read_b128 v[164:167], v0 offset:0\n"
    "s_waitcnt lgkmcnt(5)\n"
    "v_mfma_f32_16x16x32_bf16 v[136:139], v[168:171], v[52:55], v[136:139]\n"
    "ds_read_b128 v[168:171], v0 offset:1024\n"
    "s_waitcnt lgkmcnt(5)\n"
    "v_mfma_f32_16x16x32_bf16 v[140:143], v[172:175], v[52:55], v[140:143]\n"
    "ds_read_b128 v[172:175], v0 offset:2048\n"
    "s_waitcnt lgkmcnt(5)\n"
    "v_mfma_f32_16x16x32_bf16 v[144:147], v[176:179], v[52:55], v[144:147]\n"
    "ds_read_b128 v[176:179], v0 offset:3072\n"
    "s_waitcnt lgkmcnt(5)\n"
    "v_mfma_f32_16x16x32_bf16 v[148:151], v[180:183], v[52:55], v[148:151]\n"
    "ds_read_b128 v[180:183], v0 offset:4096\n"
    "s_waitcnt lgkmcnt(5)\n"
    "v_mfma_f32_16x16x32_bf16 v[152:155], v[184:187], v[52:55], v[152:155]\n"
    "ds_read_b128 v[184:187], v0 offset:5120\n"
    "v_mfma_f32_16x16x32_bf16 v[160:163], v[52:55], v[52:55], v[160:163]\n"
    "s_waitcnt lgkmcnt(5)\n"
    "v_mfma_f32_16x16x32_bf16 v[96:99], v[164:167], v[56:59], v[96:99]\n"
    "s_barrier\n"
    "ds_read_b128 v[164:167], v0 offset:6144\n"
    "s_waitcnt lgkmcnt(5)\n"
    "v_mfma_f32_16x16x32_bf16 v[100:103], v[168:171], v[56:59], v[100:103]\n"
    "ds_read_b128 v[168:171], v0 offset:7168\n"
    "s_add_u32 m0, s61, 122880\n"
    "s_add_u32 s51, s50, 276480\n"
    "buffer_load_dwordx4 v0, s[40:43], s51 offen offset:0 lds\n"
    "s_waitcnt lgkmcnt(5)\n"
    "v_mfma_f32_16x16x32_bf16 v[104:107], v[172:175], v[56:59], v[104:107]\n"
    "ds_read_b128 v[172:175], v0 offset:8192\n"
    "s_waitcnt lgkmcnt(5)\n"
    "v_mfma_f32_16x16x32_bf16 v[108:111], v[176:179], v[56:59], v[108:111]\n"
    "ds_read_b128 v[176:179], v0 offset:9216\n"
    "s_waitcnt lgkmcnt(5)\n"
    "v_mfma_f32_16x16x32_bf16 v[112:115], v[180:183], v[56:59], v[112:115]\n"
    "ds_read_b128 v[180:183], v0 offset:10240\n"
    "buffer_load_dwordx4 v0, s[40:43], s51 offen offset:1024 lds\n"
    "s_waitcnt lgkmcnt(5)\n"
    "v_mfma_f32_16x16x32_bf16 v[116:119], v[184:187], v[56:59], v[116:119]\n"
    "ds_read_b128 v[184:187], v0 offset:11264\n"
    "s_waitcnt lgkmcnt(5)\n"
    "v_mfma_f32_16x16x32_bf16 v[120:123], v[164:167], v[56:59], v[120:123]\n"
    "ds_read_b128 v[164:167], v0 offset:12288\n"
    "s_waitcnt lgkmcnt(5)\n"
    "v_mfma_f32_16x16x32_bf16 v[124:127], v[168:171], v[56:59], v[124:127]\n"
    "ds_read_b128 v[168:171], v0 offset:13312\n"
    "buffer_load_dwordx4 v0, s[40:43], s51 offen offset:2048 lds\n"
    "v_mfma_f32_16x16x32_bf16 v[156:159], v[188:191], v[56:59], v[156:159]\n"
    "s_waitcnt lgkmcnt(5)\n"
    "v_mfma_f32_16x16x32_bf16 v[128:131], v[172:175], v[56:59], v[128:131]\n"
    "ds_read_b128 v[172:175], v0 offset:14336\n"
    "s_waitcnt lgkmcnt(5)\n"
    "v_mfma_f32_16x16x32_bf16 v[132:135], v[176:179], v[56:59], v[132:135]\n"
    "ds_read_b128 v[176:179], v0 offset:15360\n"
    "s_waitcnt lgkmcnt(5)\n"
    "v_mfma_f32_16x16x32_bf16 v[136:139], v[180:183], v[56:59], v[136:139]\n"
    "ds_read_b128 v[180:183], v0 offset:16384\n"
    "buffer_load_dwordx4 v0, s[40:43], s51 offen offset:3072 lds\n"
    "s_waitcnt lgkmcnt(5)\n"
    "v_mfma_f32_16x16x32_bf16 v[140:143], v[184:187], v[56:59], v[140:143]\n"
    "ds_read_b128 v[184:187], v0 offset:17408\n"
    "s_waitcnt lgkmcnt(5)\n"
    "v_mfma_f32_16x16x32_bf16 v[144:147], v[164:167], v[56:59], v[144:147]\n"
    "ds_read_b128 v[164:167], v0 offset:18432\n"
    "s_waitcnt lgkmcnt(5)\n"
    "v_mfma_f32_16x16x32_bf16 v[148:151], v[168:171], v[56:59], v[148:151]\n"
    "ds_read_b128 v[168:171], v0 offset:19456\n"
    "s_add_u32 m0, s61, 126976\n"
    "s_add_u32 s51, s50, 280576\n"
    "buffer_load_dwordx4 v0, s[40:43], s51 offen offset:0 lds\n"
    "s_waitcnt lgkmcnt(5)\n"
    "v_mfma_f32_16x16x32_bf16 v[152:155], v[172:175], v[56:59], v[152:155]\n"
    "ds_read_b128 v[172:175], v0 offset:20480\n"
    "v_mfma_f32_16x16x32_bf16 v[160:163], v[56:59], v[56:59], v[160:163]\n"
    "s_waitcnt lgkmcnt(5)\n"
    "v_mfma_f32_16x16x32_bf16 v[96:99], v[176:179], v[60:63], v[96:99]\n"
    "ds_read_b128 v[176:179], v0 offset:21504\n"
    "s_waitcnt lgkmcnt(5)\n"
    "v_mfma_f32_16x16x32_bf16 v[100:103], v[180:183], v[60:63], v[100:103]\n"
    "ds_read_b128 v[180:183], v0 offset:22528\n"
    "buffer_load_dwordx4 v0, s[40:43], s51 offen offset:1024 lds\n"
    "s_waitcnt lgkmcnt(5)\n"
    "v_mfma_f32_16x16x32_bf16 v[104:107], v[184:187], v[60:63], v[104:107]\n"
    "ds_read_b128 v[184:187], v0 offset:23552\n"
    "s_waitcnt lgkmcnt(5)\n"
    "v_mfma_f32_16x16x32_bf16 v[108:111], v[164:167], v[60:63], v[108:111]\n"
    "ds_read_b128 v[164:167], v0 offset:24576\n"
    "s_waitcnt lgkmcnt(5)\n"
    "v_mfma_f32_16x16x32_bf16 v[112:115], v[168:171], v[60:63], v[112:115]\n"
    "ds_read_b128 v[168:171], v0 offset:25600\n"
    "buffer_load_dwordx4 v0, s[40:43], s51 offen offset:2048 lds\n"
    "s_waitcnt lgkmcnt(5)\n"
    "v_mfma_f32_16x16x32_bf16 v[116:119], v[172:175], v[60:63], v[116:119]\n"
    "ds_read_b128 v[172:175], v0 offset:26624\n"
    "s_waitcnt lgkmcnt(5)\n"
    "v_mfma_f32_16x16x32_bf16 v[120:123], v[176:179], v[60:63], v[120:123]\n"
    "ds_read_b128 v[176:179], v0 offset:27648\n"
    "s_waitcnt lgkmcnt(5)\n"
    "v_mfma_f32_16x16x32_bf16 v[124:127], v[180:183], v[60:63], v[124:127]\n"
    "ds_read_b128 v[180:183], v0 offset:28672\n"
    "buffer_load_dwordx4 v0, s[40:43], s51 offen offset:3072 lds\n"
    "v_mfma_f32_16x16x32_bf16 v[156:159], v[188:191], v[60:63], v[156:159]\n"
    "s_waitcnt lgkmcnt(5)\n"
    "v_mfma_f32_16x16x32_bf16 v[128:131], v[184:187], v[60:63], v[128:131]\n"
    "ds_read_b128 v[184:187], v0 offset:29696\n"
    "s_waitcnt lgkmcnt(5)\n"
    "v_mfma_f32_16x16x32_bf16 v[132:135], v[164:167], v[60:63], v[132:135]\n"
    "s_waitcnt vmcnt(16)\n"
    "s_barrier\n"
    "ds_read_b128 v[164:167], v0 offset:30720\n"
    "s_waitcnt lgkmcnt(5)\n"
    "v_mfma_f32_16x16x32_bf16 v[136:139], v[168:171], v[60:63], v[136:139]\n"
    "ds_read_b128 v[168:171], v0 offset:31744\n"
    "s_waitcnt lgkmcnt(5)\n"
    "v_mfma_f32_16x16x32_bf16 v[140:143], v[172:175], v[60:63], v[140:143]\n"
    "ds_read_b128 v[172:175], v0 offset:32768\n"
    "s_waitcnt lgkmcnt(5)\n"
    "v_mfma_f32_16x16x32_bf16 v[144:147], v[176:179], v[60:63], v[144:147]\n"
    "ds_read_b128 v[176:179], v0 offset:33792\n"
    "s_waitcnt lgkmcnt(5)\n"
    "v_mfma_f32_16x16x32_bf16 v[148:151], v[180:183], v[60:63], v[148:151]\n"
    "ds_read_b128 v[180:183], v0 offset:34816\n"
    "s_waitcnt lgkmcnt(5)\n"
    "v_mfma_f32_16x16x32_bf16 v[152:155], v[184:187], v[60:63], v[152:155]\n"
    "ds_read_b128 v[184:187], v0 offset:35840\n"
    "v_mfma_f32_16x16x32_bf16 v[160:163], v[60:63], v[60:63], v[160:163]\n"
    "s_waitcnt lgkmcnt(5)\n"
    "v_mfma_f32_16x16x32_bf16 v[96:99], v[164:167], v[64:67], v[96:99]\n"
    "ds_read_b128 v[164:167], v0 offset:36864\n"
    "s_waitcnt lgkmcnt(5)\n"
    "v_mfma_f32_16x16x32_bf16 v[100:103], v[168:171], v[64:67], v[100:103]\n"
    "ds_read_b128 v[168:171], v0 offset:37888\n"
    "s_waitcnt lgkmcnt(5)\n"
    "v_mfma_f32_16x16x32_bf16 v[104:107], v[172:175], v[64:67], v[104:107]\n"
    "ds_read_b128 v[172:175], v0 offset:38912\n"
    "s_waitcnt lgkmcnt(5)\n"
    "v_mfma_f32_16x16x32_bf16 v[108:111], v[176:179], v[64:67], v[108:111]\n"
    "ds_read_b128 v[176:179], v0 offset:39936\n"
    "s_waitcnt lgkmcnt(5)\n"
    "v_mfma_f32_16x16x32_bf16 v[112:115], v[180:183], v[64:67], v[112:115]\n"
    "ds_read_b128 v[180:183], v0 offset:40960\n"
    "s_waitcnt lgkmcnt(5)\n"
    "v_mfma_f32_16x16x32_bf16 v[116:119], v[184:187], v[64:67], v[116:119]\n"
    "ds_read_b128 v[184:187], v0 offset:41984\n"
    "s_waitcnt lgkmcnt(5)\n"
    "v_mfma_f32_16x16x32_bf16 v[120:123], v[164:167], v[64:67], v[120:123]\n"
    "ds_read_b128 v[164:167], v0 offset:43008\n"
    "s_waitcnt lgkmcnt(5)\n"
    "v_mfma_f32_16x16x32_bf16 v[124:127], v[168:171], v[64:67], v[124:127]\n"
    "ds_read_b128 v[168:171], v0 offset:44032\n"
    "v_mfma_f32_16x16x32_bf16 v[156:159], v[188:191], v[64:67], v[156:159]\n"
    "s_waitcnt lgkmcnt(5)\n"
    "v_mfma_f32_16x16x32_bf16 v[128:131], v[172:175], v[64:67], v[128:131]\n"
    "ds_read_b128 v[172:175], v0 offset:45056\n"
    "s_waitcnt lgkmcnt(5)\n"
    "v_mfma_f32_16x16x32_bf16 v[132:135], v[176:179], v[64:67], v[132:135]\n"
    "ds_read_b128 v[176:179], v0 offset:46080\n"
    "s_waitcnt lgkmcnt(5)\n"
    "v_mfma_f32_16x16x32_bf16 v[136:139], v[180:183], v[64:67], v[136:139]\n"
    "ds_read_b128 v[180:183], v0 offset:47104\n"
    "s_waitcnt lgkmcnt(5)\n"
    "v_mfma_f32_16x16x32_bf16 v[140:143], v[184:187], v[64:67], v[140:143]\n"
    "ds_read_b128 v[184:187], v0 offset:48128\n"
    "s_waitcnt lgkmcnt(5)\n"
    "v_mfma_f32_16x16x32_bf16 v[144:147], v[164:167], v[64:67], v[144:147]\n"
    "ds_read_b128 v[164:167], v0 offset:49152\n"
    "s_waitcnt lgkmcnt(5)\n"
    "v_mfma_f32_16x16x32_bf16 v[148:151], v[168:171], v[64:67], v[148:151]\n"
    "ds_read_b128 v[168:171], v0 offset:50176\n"
    "s_waitcnt lgkmcnt(5)\n"
    "v_mfma_f32_16x16x32_bf16 v[152:155], v[172:175], v[64:67], v[152:155]\n"
    "ds_read_b128 v[172:175], v0 offset:51200\n"
    "v_mfma_f32_16x16x32_bf16 v[160:163], v[64:67], v[64:67], v[160:163]\n"
    "s_waitcnt lgkmcnt(5)\n"
    "v_mfma_f32_16x16x32_bf16 v[96:99], v[176:179], v[68:71], v[96:99]\n"
    "ds_read_b128 v[176:179], v0 offset:52224\n"
    "s_waitcnt lgkmcnt(5)\n"
    "v_mfma_f32_16x16x32_bf16 v[100:103], v[180:183], v[68:71], v[100:103]\n"
    "ds_read_b128 v[180:183], v0 offset:53248\n"
    "s_waitcnt lgkmcnt(5)\n"
    "v_mfma_f32_16x16x32_bf16 v[104:107], v[184:187], v[68:71], v[104:107]\n"
    "ds_read_b128 v[184:187], v0 offset:54272\n"
    "s_waitcnt lgkmcnt(5)\n"
    "v_mfma_f32_16x16x32_bf16 v[108:111], v[164:167], v[68:71], v[108:111]\n"
    "ds_read_b128 v[164:167], v0 offset:55296\n"
    "s_waitcnt lgkmcnt(5)\n"
    "v_mfma_f32_16x16x32_bf16 v[112:115], v[168:171], v[68:71], v[112:115]\n"
    "ds_read_b128 v[168:171], v0 offset:56320\n"
    "s_waitcnt lgkmcnt(5)\n"
    "v_mfma_f32_16x16x32_bf16 v[116:119], v[172:175], v[68:71], v[116:119]\n"
    "ds_read_b128 v[172:175], v0 offset:57344\n"
    "s_waitcnt lgkmcnt(5)\n"
    "v_mfma_f32_16x16x32_bf16 v[120:123], v[176:179], v[68:71], v[120:123]\n"
    "ds_read_b128 v[176:179], v0 offset:58368\n"
    "s_waitcnt lgkmcnt(5)\n"
    "v_mfma_f32_16x16x32_bf16 v[124:127], v[180:183], v[68:71], v[124:127]\n"
    "ds_read_b128 v[180:183], v0 offset:59392\n"
    "v_mfma_f32_16x16x32_bf16 v[156:159], v[188:191], v[68:71], v[156:159]\n"
    "s_waitcnt lgkmcnt(5)\n"
    "v_mfma_f32_16x16x32_bf16 v[128:131], v[184:187], v[68:71], v[128:131]\n"
    "ds_read_b128 v[184:187], v0 offset:60416\n"
    "s_waitcnt lgkmcnt(5)\n"
    "v_mfma_f32_16x16x32_bf16 v[132:135], v[164:167], v[68:71], v[132:135]\n"
    "s_waitcnt vmcnt(16)\n"
    "s_barrier\n"
    "ds_read_b128 v[164:167], v6 offset:0\n"
    "s_waitcnt lgkmcnt(5)\n"
    "v_mfma_f32_16x16x32_bf16 v[136:139], v[168:171], v[68:71], v[136:139]\n"
    "ds_read_b128 v[168:171], v6 offset:1024\n"
    "s_waitcnt lgkmcnt(5)\n"
    "v_mfma_f32_16x16x32_bf16 v[140:143], v[172:175], v[68:71], v[140:143]\n"
    "ds_read_b128 v[172:175], v6 offset:2048\n"
    "s_waitcnt lgkmcnt(5)\n"
    "v_mfma_f32_16x16x32_bf16 v[144:147], v[176:179], v[68:71], v[144:147]\n"
    "ds_read_b128 v[176:179], v6 offset:3072\n"
    "s_waitcnt lgkmcnt(5)\n"
    "v_mfma_f32_16x16x32_bf16 v[148:151], v[180:183], v[68:71], v[148:151]\n"
    "ds_read_b128 v[180:183], v6 offset:4096\n"
    "s_waitcnt lgkmcnt(5)\n"
    "v_mfma_f32_16x16x32_bf16 v[152:155], v[184:187], v[68:71], v[152:155]\n"
    "ds_read_b128 v[184:187], v6 offset:5120\n"
    "v_mfma_f32_16x16x32_bf16 v[160:163], v[68:71], v[68:71], v[160:163]\n"
    "s_waitcnt lgkmcnt(5)\n"
    "v_mfma_f32_16x16x32_bf16 v[96:99], v[164:167], v[72:75], v[96:99]\n"
    "ds_read_b128 v[164:167], v6 offset:6144\n"
    "s_waitcnt lgkmcnt(5)\n"
    "v_mfma_f32_16x16x32_bf16 v[100:103], v[168:171], v[72:75], v[100:103]\n"
    "ds_read_b128 v[168:171], v6 offset:7168\n"
    "s_waitcnt lgkmcnt(5)\n"
    "v_mfma_f32_16x16x32_bf16 v[104:107], v[172:175], v[72:75], v[104:107]\n"
    "ds_read_b128 v[172:175], v6 offset:8192\n"
    "s_waitcnt lgkmcnt(5)\n"
    "v_mfma_f32_16x16x32_bf16 v[108:111], v[176:179], v[72:75], v[108:111]\n"
    "ds_read_b128 v[176:179], v6 offset:9216\n"
    "s_waitcnt lgkmcnt(5)\n"
    "v_mfma_f32_16x16x32_bf16 v[112:115], v[180:183], v[72:75], v[112:115]\n"
    "ds_read_b128 v[180:183], v6 offset:10240\n"
    "s_waitcnt lgkmcnt(5)\n"
    "v_mfma_f32_16x16x32_bf16 v[116:119], v[184:187], v[72:75], v[116:119]\n"
    "ds_read_b128 v[184:187], v6 offset:11264\n"
    "s_waitcnt lgkmcnt(5)\n"
    "v_mfma_f32_16x16x32_bf16 v[120:123], v[164:167], v[72:75], v[120:123]\n"
    "ds_read_b128 v[164:167], v6 offset:12288\n"
    "s_waitcnt lgkmcnt(5)\n"
    "v_mfma_f32_16x16x32_bf16 v[124:127], v[168:171], v[72:75], v[124:127]\n"
    "ds_read_b128 v[168:171], v6 offset:13312\n"
    "v_mfma_f32_16x16x32_bf16 v[156:159], v[188:191], v[72:75], v[156:159]\n"
    "s_waitcnt lgkmcnt(5)\n"
    "v_mfma_f32_16x16x32_bf16 v[128:131], v[172:175], v[72:75], v[128:131]\n"
    "ds_read_b128 v[172:175], v6 offset:14336\n"
    "s_waitcnt lgkmcnt(5)\n"
    "v_mfma_f32_16x16x32_bf16 v[132:135], v[176:179], v[72:75], v[132:135]\n"
    "ds_read_b128 v[176:179], v6 offset:15360\n"
    "s_waitcnt lgkmcnt(5)\n"
    "v_mfma_f32_16x16x32_bf16 v[136:139], v[180:183], v[72:75], v[136:139]\n"
    "ds_read_b128 v[180:183], v6 offset:16384\n"
    "s_waitcnt lgkmcnt(5)\n"
    "v_mfma_f32_16x16x32_bf16 v[140:143], v[184:187], v[72:75], v[140:143]\n"
    "ds_read_b128 v[184:187], v6 offset:17408\n"
    "s_waitcnt lgkmcnt(5)\n"
    "v_mfma_f32_16x16x32_bf16 v[144:147], v[164:167], v[72:75], v[144:147]\n"
    "ds_read_b128 v[164:167], v6 offset:18432\n"
    "s_waitcnt lgkmcnt(5)\n"
    "v_mfma_f32_16x16x32_bf16 v[148:151], v[168:171], v[72:75], v[148:151]\n"
    "ds_read_b128 v[168:171], v6 offset:19456\n"
    "s_waitcnt lgkmcnt(5)\n"
    "v_mfma_f32_16x16x32_bf16 v[152:155], v[172:175], v[72:75], v[152:155]\n"
    "ds_read_b128 v[172:175], v6 offset:20480\n"
    "v_mfma_f32_16x16x32_bf16 v[160:163], v[72:75], v[72:75], v[160:163]\n"
    "s_waitcnt lgkmcnt(5)\n"
    "v_mfma_f32_16x16x32_bf16 v[96:99], v[176:179], v[76:79], v[96:99]\n"
    "ds_read_b128 v[176:179], v6 offset:21504\n"
    "s_waitcnt lgkmcnt(5)\n"
    "v_mfma_f32_16x16x32_bf16 v[100:103], v[180:183], v[76:79], v[100:103]\n"
    "ds_read_b128 v[180:183], v6 offset:22528\n"
    "s_waitcnt lgkmcnt(5)\n"
    "v_mfma_f32_16x16x32_bf16 v[104:107], v[184:187], v[76:79], v[104:107]\n"
    "ds_read_b128 v[184:187], v6 offset:23552\n"
    "s_waitcnt lgkmcnt(5)\n"
    "v_mfma_f32_16x16x32_bf16 v[108:111], v[164:167], v[76:79], v[108:111]\n"
    "ds_read_b128 v[164:167], v6 offset:24576\n"
    "s_waitcnt lgkmcnt(5)\n"
    "v_mfma_f32_16x16x32_bf16 v[112:115], v[168:171], v[76:79], v[112:115]\n"
    "ds_read_b128 v[168:171], v6 offset:25600\n"
    "s_waitcnt lgkmcnt(5)\n"
    "v_mfma_f32_16x16x32_bf16 v[116:119], v[172:175], v[76:79], v[116:119]\n"
    "ds_read_b128 v[172:175], v6 offset:26624\n"
    "s_waitcnt lgkmcnt(5)\n"
    "v_mfma_f32_16x16x32_bf16 v[120:123], v[176:179], v[76:79], v[120:123]\n"
    "ds_read_b128 v[176:179], v6 offset:27648\n"
    "s_waitcnt lgkmcnt(5)\n"
    "v_mfma_f32_16x16x32_bf16 v[124:127], v[180:183], v[76:79], v[124:127]\n"
    "ds_read_b128 v[180:183], v6 offset:28672\n"
    "v_mfma_f32_16x16x32_bf16 v[156:159], v[188:191], v[76:79], v[156:159]\n"
    "s_waitcnt lgkmcnt(5)\n"
    "v_mfma_f32_16x16x32_bf16 v[128:131], v[184:187], v[76:79], v[128:131]\n"
    "ds_read_b128 v[184:187], v6 offset:29696\n"
    "s_waitcnt lgkmcnt(5)\n"
    "v_mfma_f32_16x16x32_bf16 v[132:135], v[164:167], v[76:79], v[132:135]\n"
    "s_waitcnt vmcnt(8)\n"
    "s_barrier\n"
    "ds_read_b128 v[164:167], v6 offset:30720\n"
    "s_waitcnt lgkmcnt(5)\n"
    "v_mfma_f32_16x16x32_bf16 v[136:139], v[168:171], v[76:79], v[136:139]\n"
    "ds_read_b128 v[168:171], v6 offset:31744\n"
    "s_waitcnt lgkmcnt(5)\n"
    "v_mfma_f32_16x16x32_bf16 v[140:143], v[172:175], v[76:79], v[140:143]\n"
    "ds_read_b128 v[172:175], v6 offset:32768\n"
    "s_waitcnt lgkmcnt(5)\n"
    "v_mfma_f32_16x16x32_bf16 v[144:147], v[176:179], v[76:79], v[144:147]\n"
    "ds_read_b128 v[176:179], v6 offset:33792\n"
    "s_waitcnt lgkmcnt(5)\n"
    "v_mfma_f32_16x16x32_bf16 v[148:151], v[180:183], v[76:79], v[148:151]\n"
    "ds_read_b128 v[180:183], v6 offset:34816\n"
    "s_waitcnt lgkmcnt(5)\n"
    "v_mfma_f32_16x16x32_bf16 v[152:155], v[184:187], v[76:79], v[152:155]\n"
    "ds_read_b128 v[184:187], v6 offset:35840\n"
    "v_mfma_f32_16x16x32_bf16 v[160:163], v[76:79], v[76:79], v[160:163]\n"
    "s_waitcnt lgkmcnt(5)\n"
    "v_mfma_f32_16x16x32_bf16 v[96:99], v[164:167], v[80:83], v[96:99]\n"
    "ds_read_b128 v[164:167], v6 offset:36864\n"
    "v_and_b32 v4, 48, v15\n"
    "s_mul_i32 s52, s26, 960\n"
    "s_add_u32 s52, s52, 247758848\n"
    "s_waitcnt lgkmcnt(5)\n"
    "v_mfma_f32_16x16x32_bf16 v[100:103], v[168:171], v[80:83], v[100:103]\n"
    "ds_read_b128 v[168:171], v6 offset:37888\n"
    "buffer_load_dwordx4 v[16:19], v4, s[40:43], s52 offen\n"
    "s_waitcnt lgkmcnt(5)\n"
    "v_mfma_f32_16x16x32_bf16 v[104:107], v[172:175], v[80:83], v[104:107]\n"
    "ds_read_b128 v[172:175], v6 offset:38912\n"
    "buffer_load_dwordx4 v[20:23], v4, s[40:43], s52 offen offset:64\n"
    "s_waitcnt lgkmcnt(5)\n"
    "v_mfma_f32_16x16x32_bf16 v[108:111], v[176:179], v[80:83], v[108:111]\n"
    "ds_read_b128 v[176:179], v6 offset:39936\n"
    "buffer_load_dwordx4 v[24:27], v4, s[40:43], s52 offen offset:128\n"
    "s_waitcnt lgkmcnt(5)\n"
    "v_mfma_f32_16x16x32_bf16 v[112:115], v[180:183], v[80:83], v[112:115]\n"
    "ds_read_b128 v[180:183], v6 offset:40960\n"
    "buffer_load_dwordx4 v[28:31], v4, s[40:43], s52 offen offset:192\n"
    "s_waitcnt lgkmcnt(5)\n"
    "v_mfma_f32_16x16x32_bf16 v[116:119], v[184:187], v[80:83], v[116:119]\n"
    "ds_read_b128 v[184:187], v6 offset:41984\n"
    "buffer_load_dwordx4 v[32:35], v4, s[40:43], s52 offen offset:256\n"
    "s_waitcnt lgkmcnt(5)\n"
    "v_mfma_f32_16x16x32_bf16 v[120:123], v[164:167], v[80:83], v[120:123]\n"
    "ds_read_b128 v[164:167], v6 offset:43008\n"
    "buffer_load_dwordx4 v[36:39], v4, s[40:43], s52 offen offset:320\n"
    "s_waitcnt lgkmcnt(5)\n"
    "v_mfma_f32_16x16x32_bf16 v[124:127], v[168:171], v[80:83], v[124:127]\n"
    "ds_read_b128 v[168:171], v6 offset:44032\n"
    "v_mfma_f32_16x16x32_bf16 v[156:159], v[188:191], v[80:83], v[156:159]\n"
    "buffer_load_dwordx4 v[40:43], v4, s[40:43], s52 offen offset:384\n"
    "s_waitcnt lgkmcnt(5)\n"
    "v_mfma_f32_16x16x32_bf16 v[128:131], v[172:175], v[80:83], v[128:131]\n"
    "ds_read_b128 v[172:175], v6 offset:45056\n"
    "buffer_load_dwordx4 v[44:47], v4, s[40:43], s52 offen offset:448\n"
    "s_waitcnt lgkmcnt(5)\n"
    "v_mfma_f32_16x16x32_bf16 v[132:135], v[176:179], v[80:83], v[132:135]\n"
    "ds_read_b128 v[176:179], v6 offset:46080\n"
    "buffer_load_dwordx4 v[48:51], v4, s[40:43], s52 offen offset:512\n"
    "s_waitcnt lgkmcnt(5)\n"
    "v_mfma_f32_16x16x32_bf16 v[136:139], v[180:183], v[80:83], v[136:139]\n"
    "ds_read_b128 v[180:183], v6 offset:47104\n"
    "buffer_load_dwordx4 v[52:55], v4, s[40:43], s52 offen offset:576\n"
    "s_waitcnt lgkmcnt(5)\n"
    "v_mfma_f32_16x16x32_bf16 v[140:143], v[184:187], v[80:83], v[140:143]\n"
    "ds_read_b128 v[184:187], v6 offset:48128\n"
    "buffer_load_dwordx4 v[56:59], v4, s[40:43], s52 offen offset:640\n"
    "s_waitcnt lgkmcnt(5)\n"
    "v_mfma_f32_16x16x32_bf16 v[144:147], v[164:167], v[80:83], v[144:147]\n"
    "ds_read_b128 v[164:167], v6 offset:49152\n"
    "buffer_load_dwordx4 v[60:63], v4, s[40:43], s52 offen offset:704\n"
    "s_waitcnt lgkmcnt(5)\n"
    "v_mfma_f32_16x16x32_bf16 v[148:151], v[168:171], v[80:83], v[148:151]\n"
    "ds_read_b128 v[168:171], v6 offset:50176\n"
    "buffer_load_dwordx4 v[64:67], v4, s[40:43], s52 offen offset:768\n"
    "s_waitcnt lgkmcnt(5)\n"
    "v_mfma_f32_16x16x32_bf16 v[152:155], v[172:175], v[80:83], v[152:155]\n"
    "ds_read_b128 v[172:175], v6 offset:51200\n"
    "v_mfma_f32_16x16x32_bf16 v[160:163], v[80:83], v[80:83], v[160:163]\n"
    "buffer_load_dwordx4 v[68:71], v4, s[40:43], s52 offen offset:832\n"
    "s_waitcnt lgkmcnt(5)\n"
    "v_mfma_f32_16x16x32_bf16 v[96:99], v[176:179], v[84:87], v[96:99]\n"
    "ds_read_b128 v[176:179], v6 offset:52224\n"
    "buffer_load_dwordx4 v[72:75], v4, s[40:43], s52 offen offset:896\n"
    "s_waitcnt lgkmcnt(5)\n"
    "v_mfma_f32_16x16x32_bf16 v[100:103], v[180:183], v[84:87], v[100:103]\n"
    "ds_read_b128 v[180:183], v6 offset:53248\n"
    "s_waitcnt lgkmcnt(5)\n"
    "v_mfma_f32_16x16x32_bf16 v[104:107], v[184:187], v[84:87], v[104:107]\n"
    "ds_read_b128 v[184:187], v6 offset:54272\n"
    "s_waitcnt lgkmcnt(5)\n"
    "v_mfma_f32_16x16x32_bf16 v[108:111], v[164:167], v[84:87], v[108:111]\n"
    "ds_read_b128 v[164:167], v6 offset:55296\n"
    "s_waitcnt lgkmcnt(5)\n"
    "v_mfma_f32_16x16x32_bf16 v[112:115], v[168:171], v[84:87], v[112:115]\n"
    "ds_read_b128 v[168:171], v6 offset:56320\n"
    "s_waitcnt lgkmcnt(5)\n"
    "v_mfma_f32_16x16x32_bf16 v[116:119], v[172:175], v[84:87], v[116:119]\n"
    "ds_read_b128 v[172:175], v6 offset:57344\n"
    "s_waitcnt lgkmcnt(5)\n"
    "v_mfma_f32_16x16x32_bf16 v[120:123], v[176:179], v[84:87], v[120:123]\n"
    "ds_read_b128 v[176:179], v6 offset:58368\n"
    "s_waitcnt lgkmcnt(5)\n"
    "v_mfma_f32_16x16x32_bf16 v[124:127], v[180:183], v[84:87], v[124:127]\n"
    "ds_read_b128 v[180:183], v6 offset:59392\n"
    "v_mfma_f32_16x16x32_bf16 v[156:159], v[188:191], v[84:87], v[156:159]\n"
    "s_waitcnt lgkmcnt(5)\n"
    "v_mfma_f32_16x16x32_bf16 v[128:131], v[184:187], v[84:87], v[128:131]\n"
    "ds_read_b128 v[184:187], v6 offset:60416\n"
    "s_waitcnt lgkmcnt(5)\n"
    "v_mfma_f32_16x16x32_bf16 v[132:135], v[164:167], v[84:87], v[132:135]\n"
    "s_waitcnt vmcnt(0)\n"
    "s_barrier\n"
    "ds_read_b128 v[164:167], v7 offset:0\n"
    "s_waitcnt lgkmcnt(5)\n"
    "v_mfma_f32_16x16x32_bf16 v[136:139], v[168:171], v[84:87], v[136:139]\n"
    "ds_read_b128 v[168:171], v7 offset:1024\n"
    "s_waitcnt lgkmcnt(5)\n"
    "v_mfma_f32_16x16x32_bf16 v[140:143], v[172:175], v[84:87], v[140:143]\n"
    "ds_read_b128 v[172:175], v7 offset:2048\n"
    "s_waitcnt lgkmcnt(5)\n"
    "v_mfma_f32_16x16x32_bf16 v[144:147], v[176:179], v[84:87], v[144:147]\n"
    "ds_read_b128 v[176:179], v7 offset:3072\n"
    "s_waitcnt lgkmcnt(5)\n"
    "v_mfma_f32_16x16x32_bf16 v[148:151], v[180:183], v[84:87], v[148:151]\n"
    "ds_read_b128 v[180:183], v7 offset:4096\n"
    "s_waitcnt lgkmcnt(5)\n"
    "v_mfma_f32_16x16x32_bf16 v[152:155], v[184:187], v[84:87], v[152:155]\n"
    "ds_read_b128 v[184:187], v7 offset:5120\n"
    "v_mfma_f32_16x16x32_bf16 v[160:163], v[84:87], v[84:87], v[160:163]\n"
    "s_waitcnt lgkmcnt(5)\n"
    "v_mfma_f32_16x16x32_bf16 v[96:99], v[164:167], v[88:91], v[96:99]\n"
    "ds_read_b128 v[164:167], v7 offset:6144\n"
    "s_waitcnt lgkmcnt(5)\n"
    "v_mfma_f32_16x16x32_bf16 v[100:103], v[168:171], v[88:91], v[100:103]\n"
    "ds_read_b128 v[168:171], v7 offset:7168\n"
    "s_waitcnt lgkmcnt(5)\n"
    "v_mfma_f32_16x16x32_bf16 v[104:107], v[172:175], v[88:91], v[104:107]\n"
    "ds_read_b128 v[172:175], v7 offset:8192\n"
    "s_waitcnt lgkmcnt(5)\n"
    "v_mfma_f32_16x16x32_bf16 v[108:111], v[176:179], v[88:91], v[108:111]\n"
    "ds_read_b128 v[176:179], v7 offset:9216\n"
    "s_waitcnt lgkmcnt(5)\n"
    "v_mfma_f32_16x16x32_bf16 v[112:115], v[180:183], v[88:91], v[112:115]\n"
    "ds_read_b128 v[180:183], v7 offset:10240\n"
    "s_waitcnt lgkmcnt(5)\n"
    "v_mfma_f32_16x16x32_bf16 v[116:119], v[184:187], v[88:91], v[116:119]\n"
    "ds_read_b128 v[184:187], v7 offset:11264\n"
    "s_waitcnt lgkmcnt(5)\n"
    "v_mfma_f32_16x16x32_bf16 v[120:123], v[164:167], v[88:91], v[120:123]\n"
    "ds_read_b128 v[164:167], v7 offset:12288\n"
    "s_waitcnt lgkmcnt(5)\n"
    "v_mfma_f32_16x16x32_bf16 v[124:127], v[168:171], v[88:91], v[124:127]\n"
    "ds_read_b128 v[168:171], v7 offset:13312\n"
    "v_mfma_f32_16x16x32_bf16 v[156:159], v[188:191], v[88:91], v[156:159]\n"
    "s_waitcnt lgkmcnt(5)\n"
    "v_mfma_f32_16x16x32_bf16 v[128:131], v[172:175], v[88:91], v[128:131]\n"
    "ds_read_b128 v[172:175], v7 offset:14336\n"
    "s_waitcnt lgkmcnt(5)\n"
    "v_mfma_f32_16x16x32_bf16 v[132:135], v[176:179], v[88:91], v[132:135]\n"
    "ds_read_b128 v[176:179], v7 offset:15360\n"
    "s_waitcnt lgkmcnt(5)\n"
    "v_mfma_f32_16x16x32_bf16 v[136:139], v[180:183], v[88:91], v[136:139]\n"
    "ds_read_b128 v[180:183], v7 offset:16384\n"
    "s_waitcnt lgkmcnt(5)\n"
    "v_mfma_f32_16x16x32_bf16 v[140:143], v[184:187], v[88:91], v[140:143]\n"
    "ds_read_b128 v[184:187], v7 offset:17408\n"
    "s_waitcnt lgkmcnt(5)\n"
    "v_mfma_f32_16x16x32_bf16 v[144:147], v[164:167], v[88:91], v[144:147]\n"
    "ds_read_b128 v[164:167], v7 offset:18432\n"
    "s_waitcnt lgkmcnt(5)\n"
    "v_mfma_f32_16x16x32_bf16 v[148:151], v[168:171], v[88:91], v[148:151]\n"
    "ds_read_b128 v[168:171], v7 offset:19456\n"
    "s_waitcnt lgkmcnt(5)\n"
    "v_mfma_f32_16x16x32_bf16 v[152:155], v[172:175], v[88:91], v[152:155]\n"
    "ds_read_b128 v[172:175], v7 offset:20480\n"
    "v_mfma_f32_16x16x32_bf16 v[160:163], v[88:91], v[88:91], v[160:163]\n"
    "s_waitcnt lgkmcnt(5)\n"
    "v_mfma_f32_16x16x32_bf16 v[96:99], v[176:179], v[92:95], v[96:99]\n"
    "ds_read_b128 v[176:179], v7 offset:21504\n"
    "s_waitcnt lgkmcnt(5)\n"
    "v_mfma_f32_16x16x32_bf16 v[100:103], v[180:183], v[92:95], v[100:103]\n"
    "ds_read_b128 v[180:183], v7 offset:22528\n"
    "s_waitcnt lgkmcnt(5)\n"
    "v_mfma_f32_16x16x32_bf16 v[104:107], v[184:187], v[92:95], v[104:107]\n"
    "ds_read_b128 v[184:187], v7 offset:23552\n"
    "s_waitcnt lgkmcnt(5)\n"
    "v_mfma_f32_16x16x32_bf16 v[108:111], v[164:167], v[92:95], v[108:111]\n"
    "ds_read_b128 v[164:167], v7 offset:24576\n"
    "s_waitcnt lgkmcnt(5)\n"
    "v_mfma_f32_16x16x32_bf16 v[112:115], v[168:171], v[92:95], v[112:115]\n"
    "ds_read_b128 v[168:171], v7 offset:25600\n"
    "s_waitcnt lgkmcnt(5)\n"
    "v_mfma_f32_16x16x32_bf16 v[116:119], v[172:175], v[92:95], v[116:119]\n"
    "ds_read_b128 v[172:175], v7 offset:26624\n"
    "s_waitcnt lgkmcnt(5)\n"
    "v_mfma_f32_16x16x32_bf16 v[120:123], v[176:179], v[92:95], v[120:123]\n"
    "ds_read_b128 v[176:179], v7 offset:27648\n"
    "s_waitcnt lgkmcnt(5)\n"
    "v_mfma_f32_16x16x32_bf16 v[124:127], v[180:183], v[92:95], v[124:127]\n"
    "ds_read_b128 v[180:183], v7 offset:28672\n"
    "v_mfma_f32_16x16x32_bf16 v[156:159], v[188:191], v[92:95], v[156:159]\n"
    "s_waitcnt lgkmcnt(5)\n"
    "v_mfma_f32_16x16x32_bf16 v[128:131], v[184:187], v[92:95], v[128:131]\n"
    "ds_read_b128 v[184:187], v7 offset:29696\n"
    "s_waitcnt lgkmcnt(5)\n"
    "v_mfma_f32_16x16x32_bf16 v[132:135], v[164:167], v[92:95], v[132:135]\n"
    "s_waitcnt lgkmcnt(4)\n"
    "v_mfma_f32_16x16x32_bf16 v[136:139], v[168:171], v[92:95], v[136:139]\n"
    "s_waitcnt lgkmcnt(3)\n"
    "v_mfma_f32_16x16x32_bf16 v[140:143], v[172:175], v[92:95], v[140:143]\n"
    "s_waitcnt lgkmcnt(2)\n"
    "v_mfma_f32_16x16x32_bf16 v[144:147], v[176:179], v[92:95], v[144:147]\n"
    "s_waitcnt lgkmcnt(1)\n"
    "v_mfma_f32_16x16x32_bf16 v[148:151], v[180:183], v[92:95], v[148:151]\n"
    "s_waitcnt lgkmcnt(0)\n"
    "v_mfma_f32_16x16x32_bf16 v[152:155], v[184:187], v[92:95], v[152:155]\n"
    "v_mfma_f32_16x16x32_bf16 v[160:163], v[92:95], v[92:95], v[160:163]\n"
    "v_and_b32 v4, 1, v15\n"
    "v_cmp_eq_u32 vcc, 1, v4\n"
    "s_nop 5\n"
    "v_cndmask_b32 v160, v160, v161, vcc\n"
    "v_cndmask_b32 v162, v162, v163, vcc\n"
    "v_and_b32 v4, 2, v15\n"
    "v_cmp_eq_u32 vcc, 2, v4\n"
    "s_nop 1\n"
    "v_cndmask_b32 v160, v160, v162, vcc\n"
    "v_bfe_u32 v4, v15, 2, 2\n"
    "v_lshrrev_b32 v5, 4, v15\n"
    "v_cmp_eq_u32 vcc, v4, v5\n"
    "s_nop 1\n"
    "v_cndmask_b32 v160, 0, v160, vcc\n"
    "v_mov_b32 v161, v160\n"
    "s_nop 1\n"
    "v_permlane16_swap_b32 v160, v161\n"
    "v_add_f32 v160, v160, v161\n"
    "v_mov_b32 v161, v160\n"
    "s_nop 1\n"
    "v_permlane32_swap_b32 v160, v161\n"
    "v_add_f32 v160, v160, v161\n"
    "v_mul_f32 v8, 0x3acccccd, v156\n"
    "v_mul_f32 v160, 0x3acccccd, v160\n"
    "v_fma_f32 v160, -v8, v8, v160\n"
    "v_max_f32 v160, 0, v160\n"
    "v_add_f32 v160, 0x3727c5ac, v160\n"
    "v_rsq_f32 v10, v160\n"
    "v_sub_f32 v8, 0, v8\n"
    "v_pk_fma_f32 v[96:97], v[16:17], v[8:9], v[96:97] op_sel_hi:[1,0,1]\n"
    "v_pk_mul_f32 v[96:97], v[96:97], v[10:11] op_sel_hi:[1,0]\n"
    "v_pk_fma_f32 v[98:99], v[18:19], v[8:9], v[98:99] op_sel_hi:[1,0,1]\n"
    "v_pk_mul_f32 v[98:99], v[98:99], v[10:11] op_sel_hi:[1,0]\n"
    "v_pk_fma_f32 v[100:101], v[20:21], v[8:9], v[100:101] op_sel_hi:[1,0,1]\n"
    "v_pk_mul_f32 v[100:101], v[100:101], v[10:11] op_sel_hi:[1,0]\n"
    "v_pk_fma_f32 v[102:103], v[22:23], v[8:9], v[102:103] op_sel_hi:[1,0,1]\n"
    "v_pk_mul_f32 v[102:103], v[102:103], v[10:11] op_sel_hi:[1,0]\n"
    "v_pk_fma_f32 v[104:105], v[24:25], v[8:9], v[104:105] op_sel_hi:[1,0,1]\n"
    "v_pk_mul_f32 v[104:105], v[104:105], v[10:11] op_sel_hi:[1,0]\n"
    "v_pk_fma_f32 v[106:107], v[26:27], v[8:9], v[106:107] op_sel_hi:[1,0,1]\n"
    "v_pk_mul_f32 v[106:107], v[106:107], v[10:11] op_sel_hi:[1,0]\n"
    "v_pk_fma_f32 v[108:109], v[28:29], v[8:9], v[108:109] op_sel_hi:[1,0,1]\n"
    "v_pk_mul_f32 v[108:109], v[108:109], v[10:11] op_sel_hi:[1,0]\n"
    "v_pk_fma_f32 v[110:111], v[30:31], v[8:9], v[110:111] op_sel_hi:[1,0,1]\n"
    "v_pk_mul_f32 v[110:111], v[110:111], v[10:11] op_sel_hi:[1,0]\n"
    "v_pk_fma_f32 v[112:113], v[32:33], v[8:9], v[112:113] op_sel_hi:[1,0,1]\n"
    "v_pk_mul_f32 v[112:113], v[112:113], v[10:11] op_sel_hi:[1,0]\n"
    "v_pk_fma_f32 v[114:115], v[34:35], v[8:9], v[114:115] op_sel_hi:[1,0,1]\n"
    "v_pk_mul_f32 v[114:115], v[114:115], v[10:11] op_sel_hi:[1,0]\n"
    "v_pk_fma_f32 v[116:117], v[36:37], v[8:9], v[116:117] op_sel_hi:[1,0,1]\n"
    "v_pk_mul_f32 v[116:117], v[116:117], v[10:11] op_sel_hi:[1,0]\n"
    "v_pk_fma_f32 v[118:119], v[38:39], v[8:9], v[118:119] op_sel_hi:[1,0,1]\n"
    "v_pk_mul_f32 v[118:119], v[118:119], v[10:11] op_sel_hi:[1,0]\n"
    "v_pk_fma_f32 v[120:121], v[40:41], v[8:9], v[120:121] op_sel_hi:[1,0,1]\n"
    "v_pk_mul_f32 v[120:121], v[120:121], v[10:11] op_sel_hi:[1,0]\n"
    "v_pk_fma_f32 v[122:123], v[42:43], v[8:9], v[122:123] op_sel_hi:[1,0,1]\n"
    "v_pk_mul_f32 v[122:123], v[122:123], v[10:11] op_sel_hi:[1,0]\n"
    "v_pk_fma_f32 v[124:125], v[44:45], v[8:9], v[124:125] op_sel_hi:[1,0,1]\n"
    "v_pk_mul_f32 v[124:125], v[124:125], v[10:11] op_sel_hi:[1,0]\n"
    "v_pk_fma_f32 v[126:127], v[46:47], v[8:9], v[126:127] op_sel_hi:[1,0,1]\n"
    "v_pk_mul_f32 v[126:127], v[126:127], v[10:11] op_sel_hi:[1,0]\n"
    "v_pk_fma_f32 v[128:129], v[48:49], v[8:9], v[128:129] op_sel_hi:[1,0,1]\n"
    "v_pk_mul_f32 v[128:129], v[128:129], v[10:11] op_sel_hi:[1,0]\n"
    "v_pk_fma_f32 v[130:131], v[50:51], v[8:9], v[130:131] op_sel_hi:[1,0,1]\n"
    "v_pk_mul_f32 v[130:131], v[130:131], v[10:11] op_sel_hi:[1,0]\n"
    "v_pk_fma_f32 v[132:133], v[52:53], v[8:9], v[132:133] op_sel_hi:[1,0,1]\n"
    "v_pk_mul_f32 v[132:133], v[132:133], v[10:11] op_sel_hi:[1,0]\n"
    "v_pk_fma_f32 v[134:135], v[54:55], v[8:9], v[134:135] op_sel_hi:[1,0,1]\n"
    "v_pk_mul_f32 v[134:135], v[134:135], v[10:11] op_sel_hi:[1,0]\n"
    "v_pk_fma_f32 v[136:137], v[56:57], v[8:9], v[136:137] op_sel_hi:[1,0,1]\n"
    "v_pk_mul_f32 v[136:137], v[136:137], v[10:11] op_sel_hi:[1,0]\n"
    "v_pk_fma_f32 v[138:139], v[58:59], v[8:9], v[138:139] op_sel_hi:[1,0,1]\n"
    "v_pk_mul_f32 v[138:139], v[138:139], v[10:11] op_sel_hi:[1,0]\n"
    "v_pk_fma_f32 v[140:141], v[60:61], v[8:9], v[140:141] op_sel_hi:[1,0,1]\n"
    "v_pk_mul_f32 v[140:141], v[140:141], v[10:11] op_sel_hi:[1,0]\n"
    "v_pk_fma_f32 v[142:143], v[62:63], v[8:9], v[142:143] op_sel_hi:[1,0,1]\n"
    "v_pk_mul_f32 v[142:143], v[142:143], v[10:11] op_sel_hi:[1,0]\n"
    "v_pk_fma_f32 v[144:145], v[64:65], v[8:9], v[144:145] op_sel_hi:[1,0,1]\n"
    "v_pk_mul_f32 v[144:145], v[144:145], v[10:11] op_sel_hi:[1,0]\n"
    "v_pk_fma_f32 v[146:147], v[66:67], v[8:9], v[146:147] op_sel_hi:[1,0,1]\n"
    "v_pk_mul_f32 v[146:147], v[146:147], v[10:11] op_sel_hi:[1,0]\n"
    "v_pk_fma_f32 v[148:149], v[68:69], v[8:9], v[148:149] op_sel_hi:[1,0,1]\n"
    "v_pk_mul_f32 v[148:149], v[148:149], v[10:11] op_sel_hi:[1,0]\n"
    "v_pk_fma_f32 v[150:151], v[70:71], v[8:9], v[150:151] op_sel_hi:[1,0,1]\n"
    "v_pk_mul_f32 v[150:151], v[150:151], v[10:11] op_sel_hi:[1,0]\n"
    "v_pk_fma_f32 v[152:153], v[72:73], v[8:9], v[152:153] op_sel_hi:[1,0,1]\n"
    "v_pk_mul_f32 v[152:153], v[152:153], v[10:11] op_sel_hi:[1,0]\n"
    "v_pk_fma_f32 v[154:155], v[74:75], v[8:9], v[154:155] op_sel_hi:[1,0,1]\n"
    "v_pk_mul_f32 v[154:155], v[154:155], v[10:11] op_sel_hi:[1,0]\n"
    "v_max3_f32 v8, v116, v117, v118\n"
    "v_max3_f32 v8, v8, v119, v120\n"
    "v_max3_f32 v8, v8, v121, v122\n"
    "v_max3_f32 v8, v8, v123, v124\n"
    "v_max3_f32 v8, v8, v125, v126\n"
    "v_max3_f32 v8, v8, v127, v128\n"
    "v_max3_f32 v8, v8, v129, v130\n"
    "v_max3_f32 v8, v8, v131, v132\n"
    "v_max3_f32 v8, v8, v133, v134\n"
    "v_max_f32 v8, v8, v135\n"
    "v_min3_f32 v9, v116, v117, v118\n"
    "v_min3_f32 v9, v9, v119, v120\n"
    "v_min3_f32 v9, v9, v121, v122\n"
    "v_min3_f32 v9, v9, v123, v124\n"
    "v_min3_f32 v9, v9, v125, v126\n"
    "v_min3_f32 v9, v9, v127, v128\n"
    "v_min3_f32 v9, v9, v129, v130\n"
    "v_min3_f32 v9, v9, v131, v132\n"
    "v_min3_f32 v9, v9, v133, v134\n"
    "v_min_f32 v9, v9, v135\n"
    "v_mul_f32 v8, 0xbfb8aa3b, v8\n"
    "v_mul_f32 v10, 0x3fb8aa3b, v9\n"
    "v_pk_fma_f32 v[16:17], v[116:117], s[22:23], v[8:9] op_sel_hi:[1,1,0]\n"
    "v_pk_fma_f32 v[36:37], v[116:117], s[24:25], v[10:11] op_sel_hi:[1,1,0]\n"
    "v_pk_fma_f32 v[18:19], v[118:119], s[22:23], v[8:9] op_sel_hi:[1,1,0]\n"
    "v_pk_fma_f32 v[38:39], v[118:119], s[24:25], v[10:11] op_sel_hi:[1,1,0]\n"
    "v_pk_fma_f32 v[20:21], v[120:121], s[22:23], v[8:9] op_sel_hi:[1,1,0]\n"
    "v_pk_fma_f32 v[40:41], v[120:121], s[24:25], v[10:11] op_sel_hi:[1,1,0]\n"
    "v_pk_fma_f32 v[22:23], v[122:123], s[22:23], v[8:9] op_sel_hi:[1,1,0]\n"
    "v_pk_fma_f32 v[42:43], v[122:123], s[24:25], v[10:11] op_sel_hi:[1,1,0]\n"
    "v_pk_fma_f32 v[24:25], v[124:125], s[22:23], v[8:9] op_sel_hi:[1,1,0]\n"
    "v_pk_fma_f32 v[44:45], v[124:125], s[24:25], v[10:11] op_sel_hi:[1,1,0]\n"
    "v_pk_fma_f32 v[26:27], v[126:127], s[22:23], v[8:9] op_sel_hi:[1,1,0]\n"
    "v_pk_fma_f32 v[46:47], v[126:127], s[24:25], v[10:11] op_sel_hi:[1,1,0]\n"
    "v_pk_fma_f32 v[28:29], v[128:129], s[22:23], v[8:9] op_sel_hi:[1,1,0]\n"
    "v_pk_fma_f32 v[48:49], v[128:129], s[24:25], v[10:11] op_sel_hi:[1,1,0]\n"
    "v_pk_fma_f32 v[30:31], v[130:131], s[22:23], v[8:9] op_sel_hi:[1,1,0]\n"
    "v_pk_fma_f32 v[50:51], v[130:131], s[24:25], v[10:11] op_sel_hi:[1,1,0]\n"
    "v_pk_fma_f32 v[32:33], v[132:133], s[22:23], v[8:9] op_sel_hi:[1,1,0]\n"
    "v_pk_fma_f32 v[52:53], v[132:133], s[24:25], v[10:11] op_sel_hi:[1,1,0]\n"
    "v_pk_fma_f32 v[34:35], v[134:135], s[22:23], v[8:9] op_sel_hi:[1,1,0]\n"
    "v_pk_fma_f32 v[54:55], v[134:135], s[24:25], v[10:11] op_sel_hi:[1,1,0]\n"
    "v_exp_f32 v16, v16\n"
    "v_exp_f32 v17, v17\n"
    "v_exp_f32 v18, v18\n"
    "v_exp_f32 v19, v19\n"
    "v_exp_f32 v20, v20\n"
    "v_exp_f32 v21, v21\n"
    "v_exp_f32 v22, v22\n"
    "v_exp_f32 v23, v23\n"
    "v_exp_f32 v24, v24\n"
    "v_exp_f32 v25, v25\n"
    "v_exp_f32 v26, v26\n"
    "v_exp_f32 v27, v27\n"
    "v_exp_f32 v28, v28\n"
    "v_exp_f32 v29, v29\n"
    "v_exp_f32 v30, v30\n"
    "v_exp_f32 v31, v31\n"
    "v_exp_f32 v32, v32\n"
    "v_exp_f32 v33, v33\n"
    "v_exp_f32 v34, v34\n"
    "v_exp_f32 v35, v35\n"
    "v_exp_f32 v36, v36\n"
    "v_exp_f32 v37, v37\n"
    "v_exp_f32 v38, v38\n"
    "v_exp_f32 v39, v39\n"
    "v_exp_f32 v40, v40\n"
    "v_exp_f32 v41, v41\n"
    "v_exp_f32 v42, v42\n"
    "v_exp_f32 v43, v43\n"
    "v_exp_f32 v44, v44\n"
    "v_exp_f32 v45, v45\n"
    "v_exp_f32 v46, v46\n"
    "v_exp_f32 v47, v47\n"
    "v_exp_f32 v48, v48\n"
    "v_exp_f32 v49, v49\n"
    "v_exp_f32 v50, v50\n"
    "v_exp_f32 v51, v51\n"
    "v_exp_f32 v52, v52\n"
    "v_exp_f32 v53, v53\n"
    "v_exp_f32 v54, v54\n"
    "v_exp_f32 v55, v55\n"
    "v_pk_add_f32 v[8:9], v[16:17], v[18:19]\n"
    "v_pk_add_f32 v[8:9], v[8:9], v[20:21]\n"
    "v_pk_add_f32 v[8:9], v[8:9], v[22:23]\n"
    "v_pk_add_f32 v[8:9], v[8:9], v[24:25]\n"
    "v_pk_add_f32 v[8:9], v[8:9], v[26:27]\n"
    "v_pk_add_f32 v[8:9], v[8:9], v[28:29]\n"
    "v_pk_add_f32 v[8:9], v[8:9], v[30:31]\n"
    "v_pk_add_f32 v[8:9], v[8:9], v[32:33]\n"
    "v_pk_add_f32 v[8:9], v[8:9], v[34:35]\n"
    "v_add_f32 v8, v8, v9\n"
    "v_pk_add_f32 v[10:11], v[36:37], v[38:39]\n"
    "v_pk_add_f32 v[10:11], v[10:11], v[40:41]\n"
    "v_pk_add_f32 v[10:11], v[10:11], v[42:43]\n"
    "v_pk_add_f32 v[10:11], v[10:11], v[44:45]\n"
    "v_pk_add_f32 v[10:11], v[10:11], v[46:47]\n"
    "v_pk_add_f32 v[10:11], v[10:11], v[48:49]\n"
    "v_pk_add_f32 v[10:11], v[10:11], v[50:51]\n"
    "v_pk_add_f32 v[10:11], v[10:11], v[52:53]\n"
    "v_pk_add_f32 v[10:11], v[10:11], v[54:55]\n"
    "v_add_f32 v10, v10, v11\n"
    "v_rcp_f32 v8, v8\n"
    "v_rcp_f32 v10, v10\n"
    "v_pk_mul_f32 v[16:17], v[16:17], v[8:9] op_sel_hi:[1,0]\n"
    "v_pk_mul_f32 v[18:19], v[18:19], v[8:9] op_sel_hi:[1,0]\n"
    "v_pk_mul_f32 v[20:21], v[20:21], v[8:9] op_sel_hi:[1,0]\n"
    "v_pk_mul_f32 v[22:23], v[22:23], v[8:9] op_sel_hi:[1,0]\n"
    "v_pk_mul_f32 v[24:25], v[24:25], v[8:9] op_sel_hi:[1,0]\n"
    "v_pk_mul_f32 v[26:27], v[26:27], v[8:9] op_sel_hi:[1,0]\n"
    "v_pk_mul_f32 v[28:29], v[28:29], v[8:9] op_sel_hi:[1,0]\n"
    "v_pk_mul_f32 v[30:31], v[30:31], v[8:9] op_sel_hi:[1,0]\n"
    "v_pk_mul_f32 v[32:33], v[32:33], v[8:9] op_sel_hi:[1,0]\n"
    "v_pk_mul_f32 v[34:35], v[34:35], v[8:9] op_sel_hi:[1,0]\n"
    "v_pk_mul_f32 v[36:37], v[36:37], v[10:11] op_sel_hi:[1,0]\n"
    "v_pk_mul_f32 v[38:39], v[38:39], v[10:11] op_sel_hi:[1,0]\n"
    "v_pk_mul_f32 v[40:41], v[40:41], v[10:11] op_sel_hi:[1,0]\n"
    "v_pk_mul_f32 v[42:43], v[42:43], v[10:11] op_sel_hi:[1,0]\n"
    "v_pk_mul_f32 v[44:45], v[44:45], v[10:11] op_sel_hi:[1,0]\n"
    "v_pk_mul_f32 v[46:47], v[46:47], v[10:11] op_sel_hi:[1,0]\n"
    "v_pk_mul_f32 v[48:49], v[48:49], v[10:11] op_sel_hi:[1,0]\n"
    "v_pk_mul_f32 v[50:51], v[50:51], v[10:11] op_sel_hi:[1,0]\n"
    "v_pk_mul_f32 v[52:53], v[52:53], v[10:11] op_sel_hi:[1,0]\n"
    "v_pk_mul_f32 v[54:55], v[54:55], v[10:11] op_sel_hi:[1,0]\n"
    "v_cvt_pk_bf16_f32 v56, v16, v17\n"
    "v_cvt_pk_bf16_f32 v57, v18, v19\n"
    "v_cvt_pk_bf16_f32 v58, v20, v21\n"
    "v_cvt_pk_bf16_f32 v59, v22, v23\n"
    "v_cvt_pk_bf16_f32 v60, v24, v25\n"
    "v_cvt_pk_bf16_f32 v61, v26, v27\n"
    "v_cvt_pk_bf16_f32 v62, v28, v29\n"
    "v_cvt_pk_bf16_f32 v63, v30, v31\n"
    "v_cvt_pk_bf16_f32 v64, v32, v33\n"
    "v_cvt_pk_bf16_f32 v65, v34, v35\n"
    "v_cvt_pk_bf16_f32 v66, v36, v37\n"
    "v_cvt_pk_bf16_f32 v67, v38, v39\n"
    "v_cvt_pk_bf16_f32 v68, v40, v41\n"
    "v_cvt_pk_bf16_f32 v69, v42, v43\n"
    "v_cvt_pk_bf16_f32 v70, v44, v45\n"
    "v_cvt_pk_bf16_f32 v71, v46, v47\n"
    "v_cvt_pk_bf16_f32 v72, v48, v49\n"
    "v_cvt_pk_bf16_f32 v73, v50, v51\n"
    "v_cvt_pk_bf16_f32 v74, v52, v53\n"
    "v_cvt_pk_bf16_f32 v75, v54, v55\n"
    "v_mov_b32 v76, 0\n"
    "v_mov_b32 v77, 0\n"
    "v_mov_b32 v78, 0\n"
    "v_mov_b32 v79, 0\n"
    "v_cvt_pk_bf16_f32 v80, v136, v137\n"
    "v_cvt_pk_bf16_f32 v81, v138, v139\n"
    "v_cvt_pk_bf16_f32 v82, v140, v141\n"
    "v_cvt_pk_bf16_f32 v83, v142, v143\n"
    "v_cvt_pk_bf16_f32 v84, v144, v145\n"
    "v_cvt_pk_bf16_f32 v85, v146, v147\n"
    "v_cvt_pk_bf16_f32 v86, v148, v149\n"
    "v_cvt_pk_bf16_f32 v87, v150, v151\n"
    "v_cvt_pk_bf16_f32 v88, v152, v153\n"
    "v_cvt_pk_bf16_f32 v89, v154, v155\n"
    "v_mov_b32 v90, 0x3f80\n"
    "v_mov_b32 v91, 0\n"
    "v_and_b32 v4, 15, v15\n"
    "v_lshl_add_u32 v4, v5, 6, v4\n"
    "s_lshl_b32 s48, s45, 4\n"
    "v_add_u32 v4, s48, v4\n"
    "v_mul_u32_u24 v4, 176, v4\n"
    "ds_write_b128 v4, v[56:59] offset:0\n"
    "ds_write_b128 v4, v[60:63] offset:16\n"
    "ds_write_b128 v4, v[64:67] offset:32\n"
    "ds_write_b128 v4, v[68:71] offset:48\n"
    "ds_write_b128 v4, v[72:75] offset:64\n"
    "ds_write_b128 v4, v[76:79] offset:80\n"
    "ds_write_b128 v4, v[80:83] offset:96\n"
    "ds_write_b128 v4, v[84:87] offset:112\n"
    "ds_write_b128 v4, v[88:91] offset:128\n"
    "ds_write_b128 v4, v[76:79] offset:144\n"
    "v_lshrrev_b32 v5, 2, v15\n"
    "s_lshl_b32 s48, s45, 6\n"
    "v_add_u32 v5, s48, v5\n"
    "v_mul_u32_u24 v5, 176, v5\n"
    "v_and_b32 v6, 3, v15\n"
    "v_lshl_add_u32 v6, v6, 3, v5\n"
    "s_waitcnt lgkmcnt(0)\n"
    "s_barrier\n"
    "ds_read_b64_tr_b16 v[116:117], v6 offset:0\n"
    "ds_read_b64_tr_b16 v[118:119], v6 offset:2816\n"
    "ds_read_b64_tr_b16 v[120:121], v6 offset:32\n"
    "ds_read_b64_tr_b16 v[122:123], v6 offset:2848\n"
    "ds_read_b64_tr_b16 v[124:125], v6 offset:64\n"
    "ds_read_b64_tr_b16 v[126:127], v6 offset:2880\n"
    "ds_read_b64_tr_b16 v[128:129], v6 offset:96\n"
    "ds_read_b64_tr_b16 v[130:131], v6 offset:2912\n"
    "ds_read_b64_tr_b16 v[132:133], v6 offset:128\n"
    "ds_read_b64_tr_b16 v[134:135], v6 offset:2944\n"
    "ds_read_b64_tr_b16 v[136:137], v6 offset:5632\n"
    "ds_read_b64_tr_b16 v[138:139], v6 offset:8448\n"
    "ds_read_b64_tr_b16 v[140:141], v6 offset:5664\n"
    "ds_read_b64_tr_b16 v[142:143], v6 offset:8480\n"
    "ds_read_b64_tr_b16 v[144:145], v6 offset:5696\n"
    "ds_read_b64_tr_b16 v[146:147], v6 offset:8512\n"
    "s_waitcnt lgkmcnt(14)\n"
    "ds_read_b64_tr_b16 v[148:149], v6 offset:5728\n"
    "ds_read_b64_tr_b16 v[150:151], v6 offset:8544\n"
    "s_waitcnt lgkmcnt(14)\n"
    "ds_read_b64_tr_b16 v[152:153], v6 offset:5760\n"
    "ds_read_b64_tr_b16 v[154:155], v6 offset:8576\n"
    "s_waitcnt lgkmcnt(14)\n"
    "s_waitcnt lgkmcnt(12)\n"
    "v_mfma_f32_16x16x32_bf16 v[192:195], v[116:119], v[128:131], 0\n"
    "s_waitcnt lgkmcnt(10)\n"
    "v_mfma_f32_16x16x32_bf16 v[196:199], v[116:119], v[132:135], 0\n"
    "v_mfma_f32_16x16x32_bf16 v[200:203], v[120:123], v[128:131], 0\n"
    "v_mfma_f32_16x16x32_bf16 v[204:207], v[120:123], v[132:135], 0\n"
    "v_mfma_f32_16x16x32_bf16 v[208:211], v[124:127], v[128:131], 0\n"
    "v_mfma_f32_16x16x32_bf16 v[212:215], v[124:127], v[132:135], 0\n"
    "s_waitcnt lgkmcnt(2)\n"
    "v_mfma_f32_16x16x32_bf16 v[192:195], v[136:139], v[148:151], v[192:195]\n"
    "s_waitcnt lgkmcnt(0)\n"
    "v_mfma_f32_16x16x32_bf16 v[196:199], v[136:139], v[152:155], v[196:199]\n"
    "v_mfma_f32_16x16x32_bf16 v[200:203], v[140:143], v[148:151], v[200:203]\n"
    "v_mfma_f32_16x16x32_bf16 v[204:207], v[140:143], v[152:155], v[204:207]\n"
    "v_mfma_f32_16x16x32_bf16 v[208:211], v[144:147], v[148:151], v[208:211]\n"
    "v_mfma_f32_16x16x32_bf16 v[212:215], v[144:147], v[152:155], v[212:215]\n"
    "s_lshl_b32 s48, s27, 5\n"
    "s_lshl2_add_u32 s48, s26, s48\n"
    "s_add_u32 s48, s48, s45\n"
    "s_mul_i32 s48, s48, 6144\n"
    "s_add_u32 s48, s48, 253829120\n"
    "s_add_u32 s49, s48, 4096\n"
    "buffer_store_dwordx4 v[192:195], v0, s[40:43], s48 offen sc1\n"
    "buffer_store_dwordx4 v[196:199], v0, s[40:43], s48 offen offset:1024 sc1\n"
    "buffer_store_dwordx4 v[200:203], v0, s[40:43], s48 offen offset:2048 sc1\n"
    "buffer_store_dwordx4 v[204:207], v0, s[40:43], s48 offen offset:3072 sc1\n"
    "buffer_store_dwordx4 v[208:211], v0, s[40:43], s49 offen sc1\n"
    "buffer_store_dwordx4 v[212:215], v0, s[40:43], s49 offen offset:1024 sc1\n"
    "v_max3_f32 v8, v96, v97, v98\n"
    "v_max3_f32 v8, v8, v99, v100\n"
    "v_max3_f32 v8, v8, v101, v102\n"
    "v_max3_f32 v8, v8, v103, v104\n"
    "v_max3_f32 v8, v8, v105, v106\n"
    "v_max3_f32 v8, v8, v107, v108\n"
    "v_max3_f32 v8, v8, v109, v110\n"
    "v_max3_f32 v8, v8, v111, v112\n"
    "v_max3_f32 v8, v8, v113, v114\n"
    "v_max_f32 v8, v8, v115\n"
    "v_min3_f32 v9, v96, v97, v98\n"
    "v_min3_f32 v9, v9, v99, v100\n"
    "v_min3_f32 v9, v9, v101, v102\n"
    "v_min3_f32 v9, v9, v103, v104\n"
    "v_min3_f32 v9, v9, v105, v106\n"
    "v_min3_f32 v9, v9, v107, v108\n"
    "v_min3_f32 v9, v9, v109, v110\n"
    "v_min3_f32 v9, v9, v111, v112\n"
    "v_min3_f32 v9, v9, v113, v114\n"
    "v_min_f32 v9, v9, v115\n"
    "v_mul_f32 v8, 0xbfb8aa3b, v8\n"
    "v_mul_f32 v10, 0x3fb8aa3b, v9\n"
    "v_pk_fma_f32 v[16:17], v[96:97], s[22:23], v[8:9] op_sel_hi:[1,1,0]\n"
    "v_pk_fma_f32 v[36:37], v[96:97], s[24:25], v[10:11] op_sel_hi:[1,1,0]\n"
    "v_pk_fma_f32 v[18:19], v[98:99], s[22:23], v[8:9] op_sel_hi:[1,1,0]\n"
    "v_pk_fma_f32 v[38:39], v[98:99], s[24:25], v[10:11] op_sel_hi:[1,1,0]\n"
    "v_pk_fma_f32 v[20:21], v[100:101], s[22:23], v[8:9] op_sel_hi:[1,1,0]\n"
    "v_pk_fma_f32 v[40:41], v[100:101], s[24:25], v[10:11] op_sel_hi:[1,1,0]\n"
    "v_pk_fma_f32 v[22:23], v[102:103], s[22:23], v[8:9] op_sel_hi:[1,1,0]\n"
    "v_pk_fma_f32 v[42:43], v[102:103], s[24:25], v[10:11] op_sel_hi:[1,1,0]\n"
    "v_pk_fma_f32 v[24:25], v[104:105], s[22:23], v[8:9] op_sel_hi:[1,1,0]\n"
    "v_pk_fma_f32 v[44:45], v[104:105], s[24:25], v[10:11] op_sel_hi:[1,1,0]\n"
    "v_pk_fma_f32 v[26:27], v[106:107], s[22:23], v[8:9] op_sel_hi:[1,1,0]\n"
    "v_pk_fma_f32 v[46:47], v[106:107], s[24:25], v[10:11] op_sel_hi:[1,1,0]\n"
    "v_pk_fma_f32 v[28:29], v[108:109], s[22:23], v[8:9] op_sel_hi:[1,1,0]\n"
    "v_pk_fma_f32 v[48:49], v[108:109], s[24:25], v[10:11] op_sel_hi:[1,1,0]\n"
    "v_pk_fma_f32 v[30:31], v[110:111], s[22:23], v[8:9] op_sel_hi:[1,1,0]\n"
    "v_pk_fma_f32 v[50:51], v[110:111], s[24:25], v[10:11] op_sel_hi:[1,1,0]\n"
    "v_pk_fma_f32 v[32:33], v[112:113], s[22:23], v[8:9] op_sel_hi:[1,1,0]\n"
    "v_pk_fma_f32 v[52:53], v[112:113], s[24:25], v[10:11] op_sel_hi:[1,1,0]\n"
    "v_pk_fma_f32 v[34:35], v[114:115], s[22:23], v[8:9] op_sel_hi:[1,1,0]\n"
    "v_pk_fma_f32 v[54:55], v[114:115], s[24:25], v[10:11] op_sel_hi:[1,1,0]\n"
    "v_exp_f32 v16, v16\n"
    "v_exp_f32 v17, v17\n"
    "v_exp_f32 v18, v18\n"
    "v_exp_f32 v19, v19\n"
    "v_exp_f32 v20, v20\n"
    "v_exp_f32 v21, v21\n"
    "v_exp_f32 v22, v22\n"
    "v_exp_f32 v23, v23\n"
    "v_exp_f32 v24, v24\n"
    "v_exp_f32 v25, v25\n"
    "v_exp_f32 v26, v26\n"
    "v_exp_f32 v27, v27\n"
    "v_exp_f32 v28, v28\n"
    "v_exp_f32 v29, v29\n"
    "v_exp_f32 v30, v30\n"
    "v_exp_f32 v31, v31\n"
    "v_exp_f32 v32, v32\n"
    "v_exp_f32 v33, v33\n"
    "v_exp_f32 v34, v34\n"
    "v_exp_f32 v35, v35\n"
    "v_exp_f32 v36, v36\n"
    "v_exp_f32 v37, v37\n"
    "v_exp_f32 v38, v38\n"
    "v_exp_f32 v39, v39\n"
    "v_exp_f32 v40, v40\n"
    "v_exp_f32 v41, v41\n"
    "v_exp_f32 v42, v42\n"
    "v_exp_f32 v43, v43\n"
    "v_exp_f32 v44, v44\n"
    "v_exp_f32 v45, v45\n"
    "v_exp_f32 v46, v46\n"
    "v_exp_f32 v47, v47\n"
    "v_exp_f32 v48, v48\n"
    "v_exp_f32 v49, v49\n"
    "v_exp_f32 v50, v50\n"
    "v_exp_f32 v51, v51\n"
    "v_exp_f32 v52, v52\n"
    "v_exp_f32 v53, v53\n"
    "v_exp_f32 v54, v54\n"
    "v_exp_f32 v55, v55\n"
    "v_pk_add_f32 v[8:9], v[16:17], v[18:19]\n"
    "v_pk_add_f32 v[8:9], v[8:9], v[20:21]\n"
    "v_pk_add_f32 v[8:9], v[8:9], v[22:23]\n"
    "v_pk_add_f32 v[8:9], v[8:9], v[24:25]\n"
    "v_pk_add_f32 v[8:9], v[8:9], v[26:27]\n"
    "v_pk_add_f32 v[8:9], v[8:9], v[28:29]\n"
    "v_pk_add_f32 v[8:9], v[8:9], v[30:31]\n"
    "v_pk_add_f32 v[8:9], v[8:9], v[32:33]\n"
    "v_pk_add_f32 v[8:9], v[8:9], v[34:35]\n"
    "v_add_f32 v8, v8, v9\n"
    "v_pk_add_f32 v[10:11], v[36:37], v[38:39]\n"
    "v_pk_add_f32 v[10:11], v[10:11], v[40:41]\n"
    "v_pk_add_f32 v[10:11], v[10:11], v[42:43]\n"
    "v_pk_add_f32 v[10:11], v[10:11], v[44:45]\n"
    "v_pk_add_f32 v[10:11], v[10:11], v[46:47]\n"
    "v_pk_add_f32 v[10:11], v[10:11], v[48:49]\n"
    "v_pk_add_f32 v[10:11], v[10:11], v[50:51]\n"
    "v_pk_add_f32 v[10:11], v[10:11], v[52:53]\n"
    "v_pk_add_f32 v[10:11], v[10:11], v[54:55]\n"
    "v_add_f32 v10, v10, v11\n"
    "v_rcp_f32 v8, v8\n"
    "v_rcp_f32 v10, v10\n"
    "v_pk_mul_f32 v[16:17], v[16:17], v[8:9] op_sel_hi:[1,0]\n"
    "v_pk_mul_f32 v[18:19], v[18:19], v[8:9] op_sel_hi:[1,0]\n"
    "v_pk_mul_f32 v[20:21], v[20:21], v[8:9] op_sel_hi:[1,0]\n"
    "v_pk_mul_f32 v[22:23], v[22:23], v[8:9] op_sel_hi:[1,0]\n"
    "v_pk_mul_f32 v[24:25], v[24:25], v[8:9] op_sel_hi:[1,0]\n"
    "v_pk_mul_f32 v[26:27], v[26:27], v[8:9] op_sel_hi:[1,0]\n"
    "v_pk_mul_f32 v[28:29], v[28:29], v[8:9] op_sel_hi:[1,0]\n"
    "v_pk_mul_f32 v[30:31], v[30:31], v[8:9] op_sel_hi:[1,0]\n"
    "v_pk_mul_f32 v[32:33], v[32:33], v[8:9] op_sel_hi:[1,0]\n"
    "v_pk_mul_f32 v[34:35], v[34:35], v[8:9] op_sel_hi:[1,0]\n"
    "v_pk_mul_f32 v[36:37], v[36:37], v[10:11] op_sel_hi:[1,0]\n"
    "v_pk_mul_f32 v[38:39], v[38:39], v[10:11] op_sel_hi:[1,0]\n"
    "v_pk_mul_f32 v[40:41], v[40:41], v[10:11] op_sel_hi:[1,0]\n"
    "v_pk_mul_f32 v[42:43], v[42:43], v[10:11] op_sel_hi:[1,0]\n"
    "v_pk_mul_f32 v[44:45], v[44:45], v[10:11] op_sel_hi:[1,0]\n"
    "v_pk_mul_f32 v[46:47], v[46:47], v[10:11] op_sel_hi:[1,0]\n"
    "v_pk_mul_f32 v[48:49], v[48:49], v[10:11] op_sel_hi:[1,0]\n"
    "v_pk_mul_f32 v[50:51], v[50:51], v[10:11] op_sel_hi:[1,0]\n"
    "v_pk_mul_f32 v[52:53], v[52:53], v[10:11] op_sel_hi:[1,0]\n"
    "v_pk_mul_f32 v[54:55], v[54:55], v[10:11] op_sel_hi:[1,0]\n"
    "v_cvt_pk_bf16_f32 v56, v16, v17\n"
    "v_cvt_pk_bf16_f32 v57, v18, v19\n"
    "v_cvt_pk_bf16_f32 v58, v20, v21\n"
    "v_cvt_pk_bf16_f32 v59, v22, v23\n"
    "v_cvt_pk_bf16_f32 v60, v24, v25\n"
    "v_cvt_pk_bf16_f32 v61, v26, v27\n"
    "v_cvt_pk_bf16_f32 v62, v28, v29\n"
    "v_cvt_pk_bf16_f32 v63, v30, v31\n"
    "v_cvt_pk_bf16_f32 v64, v32, v33\n"
    "v_cvt_pk_bf16_f32 v65, v34, v35\n"
    "v_cvt_pk_bf16_f32 v66, v36, v37\n"
    "v_cvt_pk_bf16_f32 v67, v38, v39\n"
    "v_cvt_pk_bf16_f32 v68, v40, v41\n"
    "v_cvt_pk_bf16_f32 v69, v42, v43\n"
    "v_cvt_pk_bf16_f32 v70, v44, v45\n"
    "v_cvt_pk_bf16_f32 v71, v46, v47\n"
    "v_cvt_pk_bf16_f32 v72, v48, v49\n"
    "v_cvt_pk_bf16_f32 v73, v50, v51\n"
    "v_cvt_pk_bf16_f32 v74, v52, v53\n"
    "v_cvt_pk_bf16_f32 v75, v54, v55\n"
    "s_lshl3_add_u32 s48, s28, s26\n"
    "s_mul_i32 s48, s48, 5120\n"
    "s_add_u32 s48, s48, 248586240\n"
    "s_add_u32 s49, s48, 4096\n"
    "buffer_store_dwordx4 v[56:59], v0, s[40:43], s48 offen sc1\n"
    "buffer_store_dwordx4 v[60:63], v0, s[40:43], s48 offen offset:1024 sc1\n"
    "buffer_store_dwordx4 v[64:67], v0, s[40:43], s48 offen offset:2048 sc1\n"
    "buffer_store_dwordx4 v[68:71], v0, s[40:43], s48 offen offset:3072 sc1\n"
    "buffer_store_dwordx4 v[72:75], v0, s[40:43], s49 offen sc1\n"
    "s_lshl3_add_u32 s54, s21, s26\n"
    "s_lshl_b32 s54, s54, 7\n"
    "s_add_u32 s54, s54, 2362368\n"
    "s_branch hp_done_%=\n"
    "hp_rd_%=:\n"
    "s_sub_u32 s26, s20, 256\n"
    "s_lshr_b32 s27, s26, 2\n"
    "s_lshl3_add_u32 s54, s21, s27\n"
    "s_lshl_b32 s54, s54, 7\n"
    "s_add_u32 s54, s54, 2362368\n"
    "s_cmp_lg_u32 s45, 0\n"
    "s_cbranch_scc1 hprd_dep_ok_%=\n"
    "s_mov_b32 s60, 0\n"
    "hprd_dep_%=:\n"
    "buffer_load_dword v4, off, s[40:43], s54 sc1\n"
    "s_waitcnt vmcnt(0)\n"
    "v_readfirstlane_b32 s59, v4\n"
    "s_nop 0\n"
    "s_cmp_ge_u32 s59, 32\n"
    "s_nop 2\n"
    "s_cbranch_scc1 hprd_dep_ok_%=\n"
    "s_sleep 3\n"
    "s_add_u32 s60, s60, 1\n"
    "s_cmp_gt_u32 s60, 65536\n"
    "s_cbranch_scc1 hp_abort_%=\n"
    "s_and_b32 s59, s60, 63\n"
    "s_cbranch_scc1 hprd_dep_%=\n"
    "v_mov_b32 v4, 1024\n"
    "buffer_load_dword v4, v4, s[40:43], s58 offen sc1\n"
    "s_waitcnt vmcnt(0)\n"
    "v_readfirstlane_b32 s59, v4\n"
    "s_nop 0\n"
    "s_cmp_lg_u32 s59, 0\n"
    "s_nop 2\n"
    "s_cbranch_scc1 hp_end_%=\n"
    "s_branch hprd_dep_%=\n"
    "hprd_dep_ok_%=:\n"
    "s_barrier\n"
    "s_lshl_b32 s48, s45, 8\n"
    "s_add_u32 s48, s48, s26\n"
    "s_mul_i32 s48, s48, 6144\n"
    "s_add_u32 s48, s48, 253829120\n"
    "s_add_u32 s49, s48, 4096\n"
    "buffer_load_dwordx4 v[16:19], v0, s[40:43], s48 offen sc1\n"
    "buffer_load_dwordx4 v[20:23], v0, s[40:43], s48 offen offset:1024 sc1\n"
    "buffer_load_dwordx4 v[24:27], v0, s[40:43], s48 offen offset:2048 sc1\n"
    "buffer_load_dwordx4 v[28:31], v0, s[40:43], s48 offen offset:3072 sc1\n"
    "buffer_load_dwordx4 v[32:35], v0, s[40:43], s49 offen sc1\n"
    "buffer_load_dwordx4 v[36:39], v0, s[40:43], s49 offen offset:1024 sc1\n"
    "s_add_u32 s48, s48, 196608\n"
    "s_add_u32 s49, s48, 4096\n"
    "buffer_load_dwordx4 v[40:43], v0, s[40:43], s48 offen sc1\n"
    "buffer_load_dwordx4 v[44:47], v0, s[40:43], s48 offen offset:1024 sc1\n"
    "buffer_load_dwordx4 v[48:51], v0, s[40:43], s48 offen offset:2048 sc1\n"
    "buffer_load_dwordx4 v[52:55], v0, s[40:43], s48 offen offset:3072 sc1\n"
    "buffer_load_dwordx4 v[56:59], v0, s[40:43], s49 offen sc1\n"
    "buffer_load_dwordx4 v[60:63], v0, s[40:43], s49 offen offset:1024 sc1\n"
    "s_add_u32 s48, s48, 196608\n"
    "s_add_u32 s49, s48, 4096\n"
    "buffer_load_dwordx4 v[64:67], v0, s[40:43], s48 offen sc1\n"
    "buffer_load_dwordx4 v[68:71], v0, s[40:43], s48 offen offset:1024 sc1\n"
    "buffer_load_dwordx4 v[72:75], v0, s[40:43], s48 offen offset:2048 sc1\n"
    "buffer_load_dwordx4 v[76:79], v0, s[40:43], s48 offen offset:3072 sc1\n"
    "buffer_load_dwordx4 v[80:83], v0, s[40:43], s49 offen sc1\n"
    "buffer_load_dwordx4 v[84:87], v0, s[40:43], s49 offen offset:1024 sc1\n"
    "s_add_u32 s48, s48, 196608\n"
    "s_add_u32 s49, s48, 4096\n"
    "buffer_load_dwordx4 v[88:91], v0, s[40:43], s48 offen sc1\n"
    "buffer_load_dwordx4 v[92:95], v0, s[40:43], s48 offen offset:1024 sc1\n"
    "buffer_load_dwordx4 v[96:99], v0, s[40:43], s48 offen offset:2048 sc1\n"
    "buffer_load_dwordx4 v[100:103], v0, s[40:43], s48 offen offset:3072 sc1\n"
    "buffer_load_dwordx4 v[104:107], v0, s[40:43], s49 offen sc1\n"
    "buffer_load_dwordx4 v[108:111], v0, s[40:43], s49 offen offset:1024 sc1\n"
    "s_add_u32 s48, s48, 196608\n"
    "s_add_u32 s49, s48, 4096\n"
    "buffer_load_dwordx4 v[112:115], v0, s[40:43], s48 offen sc1\n"
    "buffer_load_dwordx4 v[116:119], v0, s[40:43], s48 offen offset:1024 sc1\n"
    "buffer_load_dwordx4 v[120:123], v0, s[40:43], s48 offen offset:2048 sc1\n"
    "buffer_load_dwordx4 v[124:127], v0, s[40:43], s48 offen offset:3072 sc1\n"
    "buffer_load_dwordx4 v[128:131], v0, s[40:43], s49 offen sc1\n"
    "buffer_load_dwordx4 v[132:135], v0, s[40:43], s49 offen offset:1024 sc1\n"
    "s_add_u32 s48, s48, 196608\n"
    "s_add_u32 s49, s48, 4096\n"
    "buffer_load_dwordx4 v[136:139], v0, s[40:43], s48 offen sc1\n"
    "buffer_load_dwordx4 v[140:143], v0, s[40:43], s48 offen offset:1024 sc1\n"
    "buffer_load_dwordx4 v[144:147], v0, s[40:43], s48 offen offset:2048 sc1\n"
    "buffer_load_dwordx4 v[148:151], v0, s[40:43], s48 offen offset:3072 sc1\n"
    "buffer_load_dwordx4 v[152:155], v0, s[40:43], s49 offen sc1\n"
    "buffer_load_dwordx4 v[156:159], v0, s[40:43], s49 offen offset:1024 sc1\n"
    "s_add_u32 s48, s48, 196608\n"
    "s_add_u32 s49, s48, 4096\n"
    "buffer_load_dwordx4 v[160:163], v0, s[40:43], s48 offen sc1\n"
    "buffer_load_dwordx4 v[164:167], v0, s[40:43], s48 offen offset:1024 sc1\n"
    "buffer_load_dwordx4 v[168:171], v0, s[40:43], s48 offen offset:2048 sc1\n"
    "buffer_load_dwordx4 v[172:175], v0, s[40:43], s48 offen offset:3072 sc1\n"
    "buffer_load_dwordx4 v[176:179], v0, s[40:43], s49 offen sc1\n"
    "buffer_load_dwordx4 v[180:183], v0, s[40:43], s49 offen offset:1024 sc1\n"
    "s_add_u32 s48, s48, 196608\n"
    "s_add_u32 s49, s48, 4096\n"
    "buffer_load_dwordx4 v[184:187], v0, s[40:43], s48 offen sc1\n"
    "buffer_load_dwordx4 v[188:191], v0, s[40:43], s48 offen offset:1024 sc1\n"
    "buffer_load_dwordx4 v[192:195], v0, s[40:43], s48 offen offset:2048 sc1\n"
    "buffer_load_dwordx4 v[196:199], v0, s[40:43], s48 offen offset:3072 sc1\n"
    "buffer_load_dwordx4 v[200:203], v0, s[40:43], s49 offen sc1\n"
    "buffer_load_dwordx4 v[204:207], v0, s[40:43], s49 offen offset:1024 sc1\n"
    "s_add_u32 s48, s48, 196608\n"
    "s_mul_i32 s48, s45, 6144\n"
    "v_add_u32 v6, s48, v0\n"
    "s_waitcnt vmcnt(41)\n"
    "v_pk_add_f32 v[16:17], v[16:17], v[40:41]\n"
    "v_pk_add_f32 v[18:19], v[18:19], v[42:43]\n"
    "s_waitcnt vmcnt(40)\n"
    "v_pk_add_f32 v[20:21], v[20:21], v[44:45]\n"
    "v_pk_add_f32 v[22:23], v[22:23], v[46:47]\n"
    "s_waitcnt vmcnt(39)\n"
    "v_pk_add_f32 v[24:25], v[24:25], v[48:49]\n"
    "v_pk_add_f32 v[26:27], v[26:27], v[50:51]\n"
    "s_waitcnt vmcnt(38)\n"
    "v_pk_add_f32 v[28:29], v[28:29], v[52:53]\n"
    "v_pk_add_f32 v[30:31], v[30:31], v[54:55]\n"
    "s_waitcnt vmcnt(37)\n"
    "v_pk_add_f32 v[32:33], v[32:33], v[56:57]\n"
    "v_pk_add_f32 v[34:35], v[34:35], v[58:59]\n"
    "s_waitcnt vmcnt(36)\n"
    "v_pk_add_f32 v[36:37], v[36:37], v[60:61]\n"
    "v_pk_add_f32 v[38:39], v[38:39], v[62:63]\n"
    "s_waitcnt vmcnt(35)\n"
    "v_pk_add_f32 v[16:17], v[16:17], v[64:65]\n"
    "v_pk_add_f32 v[18:19], v[18:19], v[66:67]\n"
    "s_waitcnt vmcnt(34)\n"
    "v_pk_add_f32 v[20:21], v[20:21], v[68:69]\n"
    "v_pk_add_f32 v[22:23], v[22:23], v[70:71]\n"
    "s_waitcnt vmcnt(33)\n"
    "v_pk_add_f32 v[24:25], v[24:25], v[72:73]\n"
    "v_pk_add_f32 v[26:27], v[26:27], v[74:75]\n"
    "s_waitcnt vmcnt(32)\n"
    "v_pk_add_f32 v[28:29], v[28:29], v[76:77]\n"
    "v_pk_add_f32 v[30:31], v[30:31], v[78:79]\n"
    "s_waitcnt vmcnt(31)\n"
    "v_pk_add_f32 v[32:33], v[32:33], v[80:81]\n"
    "v_pk_add_f32 v[34:35], v[34:35], v[82:83]\n"
    "s_waitcnt vmcnt(30)\n"
    "v_pk_add_f32 v[36:37], v[36:37], v[84:85]\n"
    "v_pk_add_f32 v[38:39], v[38:39], v[86:87]\n"
    "s_waitcnt vmcnt(29)\n"
    "v_pk_add_f32 v[16:17], v[16:17], v[88:89]\n"
    "v_pk_add_f32 v[18:19], v[18:19], v[90:91]\n"
    "s_waitcnt vmcnt(28)\n"
    "v_pk_add_f32 v[20:21], v[20:21], v[92:93]\n"
    "v_pk_add_f32 v[22:23], v[22:23], v[94:95]\n"
    "s_waitcnt vmcnt(27)\n"
    "v_pk_add_f32 v[24:25], v[24:25], v[96:97]\n"
    "v_pk_add_f32 v[26:27], v[26:27], v[98:99]\n"
    "s_waitcnt vmcnt(26)\n"
    "v_pk_add_f32 v[28:29], v[28:29], v[100:101]\n"
    "v_pk_add_f32 v[30:31], v[30:31], v[102:103]\n"
    "s_waitcnt vmcnt(25)\n"
    "v_pk_add_f32 v[32:33], v[32:33], v[104:105]\n"
    "v_pk_add_f32 v[34:35], v[34:35], v[106:107]\n"
    "s_waitcnt vmcnt(24)\n"
    "v_pk_add_f32 v[36:37], v[36:37], v[108:109]\n"
    "v_pk_add_f32 v[38:39], v[38:39], v[110:111]\n"
    "s_waitcnt vmcnt(23)\n"
    "v_pk_add_f32 v[16:17], v[16:17], v[112:113]\n"
    "v_pk_add_f32 v[18:19], v[18:19], v[114:115]\n"
    "s_waitcnt vmcnt(22)\n"
    "v_pk_add_f32 v[20:21], v[20:21], v[116:117]\n"
    "v_pk_add_f32 v[22:23], v[22:23], v[118:119]\n"
    "s_waitcnt vmcnt(21)\n"
    "v_pk_add_f32 v[24:25], v[24:25], v[120:121]\n"
    "v_pk_add_f32 v[26:27], v[26:27], v[122:123]\n"
    "s_waitcnt vmcnt(20)\n"
    "v_pk_add_f32 v[28:29], v[28:29], v[124:125]\n"
    "v_pk_add_f32 v[30:31], v[30:31], v[126:127]\n"
    "s_waitcnt vmcnt(19)\n"
    "v_pk_add_f32 v[32:33], v[32:33], v[128:129]\n"
    "v_pk_add_f32 v[34:35], v[34:35], v[130:131]\n"
    "s_waitcnt vmcnt(18)\n"
    "v_pk_add_f32 v[36:37], v[36:37], v[132:133]\n"
    "v_pk_add_f32 v[38:39], v[38:39], v[134:135]\n"
    "s_waitcnt vmcnt(17)\n"
    "v_pk_add_f32 v[16:17], v[16:17], v[136:137]\n"
    "v_pk_add_f32 v[18:19], v[18:19], v[138:139]\n"
    "s_waitcnt vmcnt(16)\n"
    "v_pk_add_f32 v[20:21], v[20:21], v[140:141]\n"
    "v_pk_add_f32 v[22:23], v[22:23], v[142:143]\n"
    "s_waitcnt vmcnt(15)\n"
    "v_pk_add_f32 v[24:25], v[24:25], v[144:145]\n"
    "v_pk_add_f32 v[26:27], v[26:27], v[146:147]\n"
    "s_waitcnt vmcnt(14)\n"
    "v_pk_add_f32 v[28:29], v[28:29], v[148:149]\n"
    "v_pk_add_f32 v[30:31], v[30:31], v[150:151]\n"
    "s_waitcnt vmcnt(13)\n"
    "v_pk_add_f32 v[32:33], v[32:33], v[152:153]\n"
    "v_pk_add_f32 v[34:35], v[34:35], v[154:155]\n"
    "s_waitcnt vmcnt(12)\n"
    "v_pk_add_f32 v[36:37], v[36:37], v[156:157]\n"
    "v_pk_add_f32 v[38:39], v[38:39], v[158:159]\n"
    "s_waitcnt vmcnt(11)\n"
    "v_pk_add_f32 v[16:17], v[16:17], v[160:161]\n"
    "v_pk_add_f32 v[18:19], v[18:19], v[162:163]\n"
    "s_waitcnt vmcnt(10)\n"
    "v_pk_add_f32 v[20:21], v[20:21], v[164:165]\n"
    "v_pk_add_f32 v[22:23], v[22:23], v[166:167]\n"
    "s_waitcnt vmcnt(9)\n"
    "v_pk_add_f32 v[24:25], v[24:25], v[168:169]\n"
    "v_pk_add_f32 v[26:27], v[26:27], v[170:171]\n"
    "s_waitcnt vmcnt(8)\n"
    "v_pk_add_f32 v[28:29], v[28:29], v[172:173]\n"
    "v_pk_add_f32 v[30:31], v[30:31], v[174:175]\n"
    "s_waitcnt vmcnt(7)\n"
    "v_pk_add_f32 v[32:33], v[32:33], v[176:177]\n"
    "v_pk_add_f32 v[34:35], v[34:35], v[178:179]\n"
    "s_waitcnt vmcnt(6)\n"
    "v_pk_add_f32 v[36:37], v[36:37], v[180:181]\n"
    "v_pk_add_f32 v[38:39], v[38:39], v[182:183]\n"
    "s_waitcnt vmcnt(5)\n"
    "v_pk_add_f32 v[16:17], v[16:17], v[184:185]\n"
    "v_pk_add_f32 v[18:19], v[18:19], v[186:187]\n"
    "s_waitcnt vmcnt(4)\n"
    "v_pk_add_f32 v[20:21], v[20:21], v[188:189]\n"
    "v_pk_add_f32 v[22:23], v[22:23], v[190:191]\n"
    "s_waitcnt vmcnt(3)\n"
    "v_pk_add_f32 v[24:25], v[24:25], v[192:193]\n"
    "v_pk_add_f32 v[26:27], v[26:27], v[194:195]\n"
    "s_waitcnt vmcnt(2)\n"
    "v_pk_add_f32 v[28:29], v[28:29], v[196:197]\n"
    "v_pk_add_f32 v[30:31], v[30:31], v[198:199]\n"
    "s_waitcnt vmcnt(1)\n"
    "v_pk_add_f32 v[32:33], v[32:33], v[200:201]\n"
    "v_pk_add_f32 v[34:35], v[34:35], v[202:203]\n"
    "s_waitcnt vmcnt(0)\n"
    "v_pk_add_f32 v[36:37], v[36:37], v[204:205]\n"
    "v_pk_add_f32 v[38:39], v[38:39], v[206:207]\n"
    "ds_write_b128 v6, v[16:19] offset:0\n"
    "ds_write_b128 v6, v[20:23] offset:1024\n"
    "ds_write_b128 v6, v[24:27] offset:2048\n"
    "ds_write_b128 v6, v[28:31] offset:3072\n"
    "ds_write_b128 v6, v[32:35] offset:4096\n"
    "ds_write_b128 v6, v[36:39] offset:5120\n"
    "s_waitcnt lgkmcnt(0)\n"
    "s_barrier\n"
    "s_mul_i32 s50, s27, 7680\n"
    "s_add_u32 s50, s50, 260120576\n"
    "s_and_b32 s48, s26, 3\n"
    "s_lshl_b32 s48, s48, 6\n"
    "s_add_u32 s50, s50, s48\n"
    "s_add_u32 s48, s45, 0\n"
    "s_min_u32 s48, s48, 5\n"
    "s_lshl_b32 s49, s48, 10\n"
    "v_add_u32 v6, s49, v0\n"
    "ds_read_b128 v[16:19], v6 offset:0\n"
    "ds_read_b128 v[20:23], v6 offset:6144\n"
    "ds_read_b128 v[24:27], v6 offset:12288\n"
    "ds_read_b128 v[28:31], v6 offset:18432\n"
    "s_lshr_b32 s49, s48, 1\n"
    "s_lshl_b32 s49, s49, 2\n"
    "v_lshrrev_b32 v8, 4, v15\n"
    "v_add_u32 v8, s49, v8\n"
    "s_and_b32 s49, s48, 1\n"
    "s_lshl_b32 s49, s49, 4\n"
    "v_and_b32 v9, 15, v15\n"
    "v_add_u32 v9, s49, v9\n"
    "v_lshrrev_b32 v4, 2, v9\n"
    "v_mul_u32_u24 v4, 5, v4\n"
    "v_lshrrev_b32 v5, 1, v8\n"
    "v_add_u32 v4, v4, v5\n"
    "v_lshlrev_b32 v4, 8, v4\n"
    "v_and_b32 v5, 3, v9\n"
    "v_lshl_add_u32 v4, v5, 4, v4\n"
    "v_and_b32 v5, 1, v8\n"
    "v_lshl_add_u32 v10, v5, 3, v4\n"
    "v_cmp_gt_u32 vcc, 10, v8\n"
    "v_cmp_gt_u32 s[56:57], 24, v9\n"
    "s_nop 0\n"
    "s_and_b64 vcc, vcc, s[56:57]\n"
    "v_mov_b32 v4, 2147483632\n"
    "v_cndmask_b32 v10, v4, v10, vcc\n"
    "s_waitcnt lgkmcnt(2)\n"
    "v_pk_add_f32 v[16:17], v[16:17], v[20:21]\n"
    "s_waitcnt lgkmcnt(0)\n"
    "v_pk_add_f32 v[24:25], v[24:25], v[28:29]\n"
    "v_pk_add_f32 v[16:17], v[16:17], v[24:25]\n"
    "v_pk_add_f32 v[18:19], v[18:19], v[22:23]\n"
    "v_pk_add_f32 v[26:27], v[26:27], v[30:31]\n"
    "v_pk_add_f32 v[18:19], v[18:19], v[26:27]\n"
    "v_cvt_pk_bf16_f32 v32, v16, v17\n"
    "v_cvt_pk_bf16_f32 v33, v18, v19\n"
    "buffer_store_dwordx2 v[32:33], v10, s[40:43], s50 offen sc1\n"
    "s_add_u32 s48, s45, 4\n"
    "s_min_u32 s48, s48, 5\n"
    "s_lshl_b32 s49, s48, 10\n"
    "v_add_u32 v6, s49, v0\n"
    "ds_read_b128 v[48:51], v6 offset:0\n"
    "ds_read_b128 v[52:55], v6 offset:6144\n"
    "ds_read_b128 v[56:59], v6 offset:12288\n"
    "ds_read_b128 v[60:63], v6 offset:18432\n"
    "s_lshr_b32 s49, s48, 1\n"
    "s_lshl_b32 s49, s49, 2\n"
    "v_lshrrev_b32 v8, 4, v15\n"
    "v_add_u32 v8, s49, v8\n"
    "s_and_b32 s49, s48, 1\n"
    "s_lshl_b32 s49, s49, 4\n"
    "v_and_b32 v9, 15, v15\n"
    "v_add_u32 v9, s49, v9\n"
    "v_lshrrev_b32 v4, 2, v9\n"
    "v_mul_u32_u24 v4, 5, v4\n"
    "v_lshrrev_b32 v5, 1, v8\n"
    "v_add_u32 v4, v4, v5\n"
    "v_lshlrev_b32 v4, 8, v4\n"
    "v_and_b32 v5, 3, v9\n"
    "v_lshl_add_u32 v4, v5, 4, v4\n"
    "v_and_b32 v5, 1, v8\n"
    "v_lshl_add_u32 v11, v5, 3, v4\n"
    "v_cmp_gt_u32 vcc, 10, v8\n"
    "v_cmp_gt_u32 s[56:57], 24, v9\n"
    "s_nop 0\n"
    "s_and_b64 vcc, vcc, s[56:57]\n"
    "v_mov_b32 v4, 2147483632\n"
    "v_cndmask_b32 v11, v4, v11, vcc\n"
    "s_waitcnt lgkmcnt(2)\n"
    "v_pk_add_f32 v[48:49], v[48:49], v[52:53]\n"
    "s_waitcnt lgkmcnt(0)\n"
    "v_pk_add_f32 v[56:57], v[56:57], v[60:61]\n"
    "v_pk_add_f32 v[48:49], v[48:49], v[56:57]\n"
    "v_pk_add_f32 v[50:51], v[50:51], v[54:55]\n"
    "v_pk_add_f32 v[58:59], v[58:59], v[62:63]\n"
    "v_pk_add_f32 v[50:51], v[50:51], v[58:59]\n"
    "v_cvt_pk_bf16_f32 v64, v48, v49\n"
    "v_cvt_pk_bf16_f32 v65, v50, v51\n"
    "buffer_store_dwordx2 v[64:65], v11, s[40:43], s50 offen sc1\n"
    "s_lshl_b32 s54, s21, 7\n"
    "s_add_u32 s54, s54, 2364416\n"
    "s_branch hp_done_%=\n"
    "hp_b2_%=:\n"
    "s_sub_u32 s26, s20, 288\n"
    "s_mul_i32 s50, s45, 204800\n"
    "s_add_u32 s50, s50, 247767040\n"
    "buffer_load_dwordx4 v[128:131], v0, s[40:43], s50 offen\n"
    "buffer_load_dwordx4 v[132:135], v0, s[40:43], s50 offen offset:1024\n"
    "buffer_load_dwordx4 v[136:139], v0, s[40:43], s50 offen offset:2048\n"
    "buffer_load_dwordx4 v[140:143], v0, s[40:43], s50 offen offset:3072\n"
    "s_add_u32 s50, s50, 4096\n"
    "buffer_load_dwordx4 v[144:147], v0, s[40:43], s50 offen\n"
    "buffer_load_dwordx4 v[148:151], v0, s[40:43], s50 offen offset:1024\n"
    "buffer_load_dwordx4 v[152:155], v0, s[40:43], s50 offen offset:2048\n"
    "buffer_load_dwordx4 v[156:159], v0, s[40:43], s50 offen offset:3072\n"
    "s_add_u32 s50, s50, 4096\n"
    "buffer_load_dwordx4 v[160:163], v0, s[40:43], s50 offen\n"
    "buffer_load_dwordx4 v[164:167], v0, s[40:43], s50 offen offset:1024\n"
    "buffer_load_dwordx4 v[168:171], v0, s[40:43], s50 offen offset:2048\n"
    "buffer_load_dwordx4 v[172:175], v0, s[40:43], s50 offen offset:3072\n"
    "s_add_u32 s50, s50, 4096\n"
    "buffer_load_dwordx4 v[176:179], v0, s[40:43], s50 offen\n"
    "buffer_load_dwordx4 v[180:183], v0, s[40:43], s50 offen offset:1024\n"
    "buffer_load_dwordx4 v[184:187], v0, s[40:43], s50 offen offset:2048\n"
    "buffer_load_dwordx4 v[188:191], v0, s[40:43], s50 offen offset:3072\n"
    "s_add_u32 s50, s50, 4096\n"
    "buffer_load_dwordx4 v[192:195], v0, s[40:43], s50 offen\n"
    "buffer_load_dwordx4 v[196:199], v0, s[40:43], s50 offen offset:1024\n"
    "buffer_load_dwordx4 v[200:203], v0, s[40:43], s50 offen offset:2048\n"
    "buffer_load_dwordx4 v[204:207], v0, s[40:43], s50 offen offset:3072\n"
    "s_add_u32 s50, s50, 4096\n"
    "buffer_load_dwordx4 v[208:211], v0, s[40:43], s50 offen\n"
    "buffer_load_dwordx4 v[212:215], v0, s[40:43], s50 offen offset:1024\n"
    "buffer_load_dwordx4 v[216:219], v0, s[40:43], s50 offen offset:2048\n"
    "buffer_load_dwordx4 v[220:223], v0, s[40:43], s50 offen offset:3072\n"
    "s_add_u32 s50, s50, 4096\n"
    "buffer_load_dwordx4 v[224:227], v0, s[40:43], s50 offen\n"
    "buffer_load_dwordx4 v[228:231], v0, s[40:43], s50 offen offset:1024\n"
    "buffer_load_dwordx4 v[232:235], v0, s[40:43], s50 offen offset:2048\n"
    "buffer_load_dwordx4 v[236:239], v0, s[40:43], s50 offen offset:3072\n"
    "s_add_u32 s50, s50, 4096\n"
    "buffer_load_dwordx4 v[240:243], v0, s[40:43], s50 offen\n"
    "buffer_load_dwordx4 v[244:247], v0, s[40:43], s50 offen offset:1024\n"
    "buffer_load_dwordx4 v[248:251], v0, s[40:43], s50 offen offset:2048\n"
    "buffer_load_dwordx4 v[252:255], v0, s[40:43], s50 offen offset:3072\n"
    "v_lshlrev_b32 v4, 7, v15\n"
    "s_add_u32 s48, s50, 4096\n"
    "buffer_load_dword v24, v4, s[40:43], s48 offen\n"
    "s_add_u32 s48, s48, 8192\n"
    "buffer_load_dword v25, v4, s[40:43], s48 offen\n"
    "s_add_u32 s48, s48, 8192\n"
    "buffer_load_dword v26, v4, s[40:43], s48 offen\n"
    "s_add_u32 s48, s48, 8192\n"
    "buffer_load_dword v27, v4, s[40:43], s48 offen\n"
    "s_add_u32 s48, s48, 8192\n"
    "buffer_load_dword v28, v4, s[40:43], s48 offen\n"
    "s_add_u32 s48, s48, 8192\n"
    "buffer_load_dword v29, v4, s[40:43], s48 offen\n"
    "s_add_u32 s48, s48, 8192\n"
    "buffer_load_dword v30, v4, s[40:43], s48 offen\n"
    "s_add_u32 s48, s48, 8192\n"
    "buffer_load_dword v31, v4, s[40:43], s48 offen\n"
    "s_add_u32 s48, s48, 8192\n"
    "buffer_load_dword v32, v4, s[40:43], s48 offen\n"
    "s_add_u32 s48, s48, 8192\n"
    "buffer_load_dword v33, v4, s[40:43], s48 offen\n"
    "s_add_u32 s48, s48, 8192\n"
    "buffer_load_dword v34, v4, s[40:43], s48 offen\n"
    "s_add_u32 s48, s48, 8192\n"
    "buffer_load_dword v35, v4, s[40:43], s48 offen\n"
    "s_add_u32 s48, s48, 8192\n"
    "buffer_load_dword v36, v4, s[40:43], s48 offen\n"
    "s_add_u32 s48, s48, 8192\n"
    "buffer_load_dword v37, v4, s[40:43], s48 offen\n"
    "s_add_u32 s48, s48, 8192\n"
    "buffer_load_dword v38, v4, s[40:43], s48 offen\n"
    "s_add_u32 s48, s48, 8192\n"
    "buffer_load_dword v39, v4, s[40:43], s48 offen\n"
    "s_add_u32 s48, s48, 8192\n"
    "buffer_load_dword v40, v4, s[40:43], s48 offen\n"
    "s_add_u32 s48, s48, 8192\n"
    "buffer_load_dword v41, v4, s[40:43], s48 offen\n"
    "s_add_u32 s48, s48, 8192\n"
    "buffer_load_dword v42, v4, s[40:43], s48 offen\n"
    "s_add_u32 s48, s48, 8192\n"
    "buffer_load_dword v43, v4, s[40:43], s48 offen\n"
    "s_add_u32 s48, s48, 8192\n"
    "buffer_load_dword v44, v4, s[40:43], s48 offen\n"
    "s_add_u32 s48, s48, 8192\n"
    "v_mov_b32 v16, 0\n"
    "v_mov_b32 v17, 0\n"
    "v_mov_b32 v18, 0\n"
    "v_mov_b32 v19, 0\n"
    "s_mul_i32 s48, s45, 3840\n"
    "s_add_u32 s48, s48, 61440\n"
    "v_add_u32 v4, s48, v0\n"
    "ds_write_b128 v4, v[16:19] offset:0\n"
    "ds_write_b128 v4, v[16:19] offset:1024\n"
    "ds_write_b128 v4, v[16:19] offset:2048\n"
    "ds_write_b128 v4, v[16:19] offset:2816\n"
    "s_mul_i32 s27, s45, 15360\n"
    "v_add_u32 v6, s27, v0\n"
    "v_and_b32 v7, 0xf0, v0\n"
    "v_add_u32 v7, s27, v7\n"
    "v_mov_b32 v8, 61440\n"
    "v_bfe_u32 v4, v15, 2, 2\n"
    "v_lshrrev_b32 v5, 4, v15\n"
    "v_cmp_eq_u32 vcc, v4, v5\n"
    "s_nop 1\n"
    "v_cndmask_b32 v7, v8, v7, vcc\n"
    "v_and_b32 v8, 0x1f0, v0\n"
    "v_lshrrev_b32 v4, 5, v15\n"
    "v_lshl_add_u32 v8, v4, 10, v8\n"
    "s_mul_i32 s48, s45, 5120\n"
    "s_add_u32 s48, s48, 76800\n"
    "v_add_u32 v9, s48, v0\n"
    "s_lshl_b32 s54, s21, 7\n"
    "s_add_u32 s54, s54, 2364416\n"
    "s_waitcnt lgkmcnt(0)\n"
    "s_cmp_lg_u32 s45, 0\n"
    "s_cbranch_scc1 hpb2_dep_ok_%=\n"
    "s_mov_b32 s60, 0\n"
    "hpb2_dep_%=:\n"
    "buffer_load_dword v4, off, s[40:43], s54 sc1\n"
    "s_waitcnt vmcnt(0)\n"
    "v_readfirstlane_b32 s59, v4\n"
    "s_nop 0\n"
    "s_cmp_ge_u32 s59, 32\n"
    "s_nop 2\n"
    "s_cbranch_scc1 hpb2_dep_ok_%=\n"
    "s_sleep 3\n"
    "s_add_u32 s60, s60, 1\n"
    "s_cmp_gt_u32 s60, 65536\n"
    "s_cbranch_scc1 hp_abort_%=\n"
    "s_and_b32 s59, s60, 63\n"
    "s_cbranch_scc1 hpb2_dep_%=\n"
    "v_mov_b32 v4, 1024\n"
    "buffer_load_dword v4, v4, s[40:43], s58 offen sc1\n"
    "s_waitcnt vmcnt(0)\n"
    "v_readfirstlane_b32 s59, v4\n"
    "s_nop 0\n"
    "s_cmp_lg_u32 s59, 0\n"
    "s_nop 2\n"
    "s_cbranch_scc1 hp_end_%=\n"
    "s_branch hpb2_dep_%=\n"
    "hpb2_dep_ok_%=:\n"
    "s_barrier\n"
    "s_add_u32 s48, s27, 260120576\n"
    "buffer_load_dwordx4 v[56:59], v0, s[40:43], s48 offen sc1\n"
    "buffer_load_dwordx4 v[60:63], v0, s[40:43], s48 offen offset:1024 sc1\n"
    "buffer_load_dwordx4 v[64:67], v0, s[40:43], s48 offen offset:2048 sc1\n"
    "buffer_load_dwordx4 v[68:71], v0, s[40:43], s48 offen offset:3072 sc1\n"
    "s_add_u32 s48, s48, 4096\n"
    "buffer_load_dwordx4 v[72:75], v0, s[40:43], s48 offen sc1\n"
    "buffer_load_dwordx4 v[76:79], v0, s[40:43], s48 offen offset:1024 sc1\n"
    "buffer_load_dwordx4 v[80:83], v0, s[40:43], s48 offen offset:2048 sc1\n"
    "buffer_load_dwordx4 v[84:87], v0, s[40:43], s48 offen offset:3072 sc1\n"
    "s_add_u32 s48, s48, 4096\n"
    "buffer_load_dwordx4 v[88:91], v0, s[40:43], s48 offen sc1\n"
    "buffer_load_dwordx4 v[92:95], v0, s[40:43], s48 offen offset:1024 sc1\n"
    "s_waitcnt vmcnt(61)\n"
    "buffer_load_dwordx4 v[96:99], v0, s[40:43], s48 offen offset:2048 sc1\n"
    "buffer_load_dwordx4 v[100:103], v0, s[40:43], s48 offen offset:3072 sc1\n"
    "s_waitcnt vmcnt(61)\n"
    "s_add_u32 s48, s48, 4096\n"
    "buffer_load_dwordx4 v[104:107], v0, s[40:43], s48 offen sc1\n"
    "buffer_load_dwordx4 v[108:111], v0, s[40:43], s48 offen offset:1024 sc1\n"
    "s_waitcnt vmcnt(61)\n"
    "buffer_load_dwordx4 v[112:115], v0, s[40:43], s48 offen offset:2048 sc1\n"
    "s_lshl3_add_u32 s28, s26, s45\n"
    "s_mul_i32 s28, s28, 10240\n"
    "s_add_u32 s48, s28, 248586240\n"
    "s_add_u32 s49, s48, 4096\n"
    "buffer_load_dwordx4 v[16:19], v0, s[40:43], s48 offen sc1\n"
    "s_waitcnt vmcnt(61)\n"
    "buffer_load_dwordx4 v[20:23], v0, s[40:43], s48 offen offset:1024 sc1\n"
    "s_waitcnt vmcnt(34)\n"
    "buffer_load_dwordx4 v[24:27], v0, s[40:43], s48 offen offset:2048 sc1\n"
    "s_waitcnt vmcnt(31)\n"
    "buffer_load_dwordx4 v[28:31], v0, s[40:43], s48 offen offset:3072 sc1\n"
    "s_waitcnt vmcnt(28)\n"
    "buffer_load_dwordx4 v[32:35], v0, s[40:43], s49 offen sc1\n"
    "s_add_u32 s48, s28, 248591360\n"
    "s_add_u32 s49, s48, 4096\n"
    "s_waitcnt vmcnt(25)\n"
    "buffer_load_dwordx4 v[36:39], v0, s[40:43], s48 offen sc1\n"
    "s_waitcnt vmcnt(22)\n"
    "buffer_load_dwordx4 v[40:43], v0, s[40:43], s48 offen offset:1024 sc1\n"
    "s_waitcnt vmcnt(22)\n"
    "buffer_load_dwordx4 v[44:47], v0, s[40:43], s48 offen offset:2048 sc1\n"
    "buffer_load_dwordx4 v[48:51], v0, s[40:43], s48 offen offset:3072 sc1\n"
    "buffer_load_dwordx4 v[52:55], v0, s[40:43], s49 offen sc1\n"
    "s_waitcnt vmcnt(24)\n"
    "ds_write_b128 v6, v[56:59] offset:0\n"
    "s_waitcnt vmcnt(23)\n"
    "ds_write_b128 v6, v[60:63] offset:1024\n"
    "s_waitcnt vmcnt(22)\n"
    "ds_write_b128 v6, v[64:67] offset:2048\n"
    "s_waitcnt vmcnt(21)\n"
    "ds_write_b128 v6, v[68:71] offset:3072\n"
    "s_waitcnt vmcnt(20)\n"
    "ds_write_b128 v6, v[72:75] offset:4096\n"
    "s_waitcnt vmcnt(19)\n"
    "ds_write_b128 v6, v[76:79] offset:5120\n"
    "s_waitcnt vmcnt(18)\n"
    "ds_write_b128 v6, v[80:83] offset:6144\n"
    "s_waitcnt vmcnt(17)\n"
    "ds_write_b128 v6, v[84:87] offset:7168\n"
    "s_waitcnt vmcnt(16)\n"
    "ds_write_b128 v6, v[88:91] offset:8192\n"
    "s_waitcnt vmcnt(15)\n"
    "ds_write_b128 v6, v[92:95] offset:9216\n"
    "s_waitcnt vmcnt(14)\n"
    "ds_write_b128 v6, v[96:99] offset:10240\n"
    "s_waitcnt vmcnt(13)\n"
    "ds_write_b128 v6, v[100:103] offset:11264\n"
    "s_waitcnt vmcnt(12)\n"
    "ds_write_b128 v6, v[104:107] offset:12288\n"
    "s_waitcnt vmcnt(11)\n"
    "ds_write_b128 v6, v[108:111] offset:13312\n"
    "s_waitcnt vmcnt(10)\n"
    "ds_write_b128 v6, v[112:115] offset:14336\n"
    "s_waitcnt lgkmcnt(0)\n"
    "ds_read_b128 v[80:83], v7 offset:0\n"
    "ds_read_b128 v[84:87], v7 offset:256\n"
    "ds_read_b128 v[88:91], v7 offset:512\n"
    "ds_read_b128 v[92:95], v7 offset:768\n"
    "ds_read_b128 v[96:99], v7 offset:1024\n"
    "s_waitcnt vmcnt(9) lgkmcnt(4)\n"
    "v_mfma_f32_16x16x32_bf16 v[56:59], v[80:83], v[16:19], 0\n"
    "ds_read_b128 v[80:83], v7 offset:1280\n"
    "s_waitcnt vmcnt(8) lgkmcnt(4)\n"
    "v_mfma_f32_16x16x32_bf16 v[56:59], v[84:87], v[20:23], v[56:59]\n"
    "ds_read_b128 v[84:87], v7 offset:1536\n"
    "s_waitcnt vmcnt(7) lgkmcnt(4)\n"
    "v_mfma_f32_16x16x32_bf16 v[56:59], v[88:91], v[24:27], v[56:59]\n"
    "ds_read_b128 v[88:91], v7 offset:1792\n"
    "s_waitcnt vmcnt(6) lgkmcnt(4)\n"
    "v_mfma_f32_16x16x32_bf16 v[56:59], v[92:95], v[28:31], v[56:59]\n"
    "ds_read_b128 v[92:95], v7 offset:2048\n"
    "s_waitcnt vmcnt(5) lgkmcnt(4)\n"
    "v_mfma_f32_16x16x32_bf16 v[56:59], v[96:99], v[32:35], v[56:59]\n"
    "ds_read_b128 v[96:99], v7 offset:2304\n"
    "s_waitcnt lgkmcnt(4)\n"
    "v_mfma_f32_16x16x32_bf16 v[60:63], v[80:83], v[16:19], 0\n"
    "ds_read_b128 v[80:83], v7 offset:2560\n"
    "s_waitcnt lgkmcnt(4)\n"
    "v_mfma_f32_16x16x32_bf16 v[60:63], v[84:87], v[20:23], v[60:63]\n"
    "ds_read_b128 v[84:87], v7 offset:2816\n"
    "s_waitcnt lgkmcnt(4)\n"
    "v_mfma_f32_16x16x32_bf16 v[60:63], v[88:91], v[24:27], v[60:63]\n"
    "ds_read_b128 v[88:91], v7 offset:3072\n"
    "s_waitcnt lgkmcnt(4)\n"
    "v_mfma_f32_16x16x32_bf16 v[60:63], v[92:95], v[28:31], v[60:63]\n"
    "ds_read_b128 v[92:95], v7 offset:3328\n"
    "s_waitcnt lgkmcnt(4)\n"
    "v_mfma_f32_16x16x32_bf16 v[60:63], v[96:99], v[32:35], v[60:63]\n"
    "ds_read_b128 v[96:99], v7 offset:3584\n"
    "s_waitcnt lgkmcnt(4)\n"
    "v_mfma_f32_16x16x32_bf16 v[64:67], v[80:83], v[16:19], 0\n"
    "ds_read_b128 v[80:83], v7 offset:3840\n"
    "s_waitcnt lgkmcnt(4)\n"
    "v_mfma_f32_16x16x32_bf16 v[64:67], v[84:87], v[20:23], v[64:67]\n"
    "ds_read_b128 v[84:87], v7 offset:4096\n"
    "s_waitcnt lgkmcnt(4)\n"
    "v_mfma_f32_16x16x32_bf16 v[64:67], v[88:91], v[24:27], v[64:67]\n"
    "ds_read_b128 v[88:91], v7 offset:4352\n"
    "s_waitcnt lgkmcnt(4)\n"
    "v_mfma_f32_16x16x32_bf16 v[64:67], v[92:95], v[28:31], v[64:67]\n"
    "ds_read_b128 v[92:95], v7 offset:4608\n"
    "s_waitcnt lgkmcnt(4)\n"
    "v_mfma_f32_16x16x32_bf16 v[64:67], v[96:99], v[32:35], v[64:67]\n"
    "ds_read_b128 v[96:99], v7 offset:4864\n"
    "s_waitcnt lgkmcnt(4)\n"
    "v_mfma_f32_16x16x32_bf16 v[68:71], v[80:83], v[16:19], 0\n"
    "ds_read_b128 v[80:83], v7 offset:5120\n"
    "s_waitcnt lgkmcnt(4)\n"
    "v_mfma_f32_16x16x32_bf16 v[68:71], v[84:87], v[20:23], v[68:71]\n"
    "ds_read_b128 v[84:87], v7 offset:5376\n"
    "s_waitcnt lgkmcnt(4)\n"
    "v_mfma_f32_16x16x32_bf16 v[68:71], v[88:91], v[24:27], v[68:71]\n"
    "ds_read_b128 v[88:91], v7 offset:5632\n"
    "s_waitcnt lgkmcnt(4)\n"
    "v_mfma_f32_16x16x32_bf16 v[68:71], v[92:95], v[28:31], v[68:71]\n"
    "ds_read_b128 v[92:95], v7 offset:5888\n"
    "s_waitcnt lgkmcnt(4)\n"
    "v_mfma_f32_16x16x32_bf16 v[68:71], v[96:99], v[32:35], v[68:71]\n"
    "ds_read_b128 v[96:99], v7 offset:6144\n"
    "s_waitcnt lgkmcnt(4)\n"
    "v_mfma_f32_16x16x32_bf16 v[72:75], v[80:83], v[16:19], 0\n"
    "ds_read_b128 v[80:83], v7 offset:6400\n"
    "s_waitcnt lgkmcnt(4)\n"
    "v_mfma_f32_16x16x32_bf16 v[72:75], v[84:87], v[20:23], v[72:75]\n"
    "ds_read_b128 v[84:87], v7 offset:6656\n"
    "s_waitcnt lgkmcnt(4)\n"
    "v_mfma_f32_16x16x32_bf16 v[72:75], v[88:91], v[24:27], v[72:75]\n"
    "ds_read_b128 v[88:91], v7 offset:6912\n"
    "s_waitcnt lgkmcnt(4)\n"
    "v_mfma_f32_16x16x32_bf16 v[72:75], v[92:95], v[28:31], v[72:75]\n"
    "ds_read_b128 v[92:95], v7 offset:7168\n"
    "s_waitcnt lgkmcnt(4)\n"
    "v_mfma_f32_16x16x32_bf16 v[72:75], v[96:99], v[32:35], v[72:75]\n"
    "ds_read_b128 v[96:99], v7 offset:7424\n"
    "s_waitcnt lgkmcnt(4)\n"
    "v_mfma_f32_16x16x32_bf16 v[76:79], v[80:83], v[16:19], 0\n"
    "ds_read_b128 v[80:83], v7 offset:7680\n"
    "s_waitcnt lgkmcnt(4)\n"
    "v_mfma_f32_16x16x32_bf16 v[76:79], v[84:87], v[20:23], v[76:79]\n"
    "ds_read_b128 v[84:87], v7 offset:7936\n"
    "s_waitcnt lgkmcnt(4)\n"
    "v_mfma_f32_16x16x32_bf16 v[76:79], v[88:91], v[24:27], v[76:79]\n"
    "ds_read_b128 v[88:91], v7 offset:8192\n"
    "s_waitcnt lgkmcnt(4)\n"
    "v_mfma_f32_16x16x32_bf16 v[76:79], v[92:95], v[28:31], v[76:79]\n"
    "ds_read_b128 v[92:95], v7 offset:8448\n"
    "s_waitcnt lgkmcnt(4)\n"
    "v_mfma_f32_16x16x32_bf16 v[76:79], v[96:99], v[32:35], v[76:79]\n"
    "ds_read_b128 v[96:99], v7 offset:8704\n"
    "s_add_u32 s48, s28, 248627200\n"
    "s_add_u32 s49, s48, 4096\n"
    "buffer_load_dwordx4 v[16:19], v0, s[40:43], s48 offen sc1\n"
    "buffer_load_dwordx4 v[20:23], v0, s[40:43], s48 offen offset:1024 sc1\n"
    "buffer_load_dwordx4 v[24:27], v0, s[40:43], s48 offen offset:2048 sc1\n"
    "buffer_load_dwordx4 v[28:31], v0, s[40:43], s48 offen offset:3072 sc1\n"
    "buffer_load_dwordx4 v[32:35], v0, s[40:43], s49 offen sc1\n"
    "v_add_f32 v76, 0x358637bd, v76\n"
    "v_rcp_f32 v76, v76\n"
    "s_nop 0\n"
    "v_pk_mul_f32 v[56:57], v[56:57], v[76:77] op_sel_hi:[1,0]\n"
    "v_pk_mul_f32 v[58:59], v[58:59], v[76:77] op_sel_hi:[1,0]\n"
    "v_pk_mul_f32 v[60:61], v[60:61], v[76:77] op_sel_hi:[1,0]\n"
    "v_pk_mul_f32 v[62:63], v[62:63], v[76:77] op_sel_hi:[1,0]\n"
    "v_pk_mul_f32 v[64:65], v[64:65], v[76:77] op_sel_hi:[1,0]\n"
    "v_pk_mul_f32 v[66:67], v[66:67], v[76:77] op_sel_hi:[1,0]\n"
    "v_pk_mul_f32 v[68:69], v[68:69], v[76:77] op_sel_hi:[1,0]\n"
    "v_pk_mul_f32 v[70:71], v[70:71], v[76:77] op_sel_hi:[1,0]\n"
    "v_pk_mul_f32 v[72:73], v[72:73], v[76:77] op_sel_hi:[1,0]\n"
    "v_pk_mul_f32 v[74:75], v[74:75], v[76:77] op_sel_hi:[1,0]\n"
    "v_cvt_pk_bf16_f32 v100, v56, v57\n"
    "v_cvt_pk_bf16_f32 v101, v58, v59\n"
    "v_cvt_pk_bf16_f32 v102, v60, v61\n"
    "v_cvt_pk_bf16_f32 v103, v62, v63\n"
    "v_cvt_pk_bf16_f32 v104, v64, v65\n"
    "v_cvt_pk_bf16_f32 v105, v66, v67\n"
    "v_cvt_pk_bf16_f32 v106, v68, v69\n"
    "v_cvt_pk_bf16_f32 v107, v70, v71\n"
    "v_cvt_pk_bf16_f32 v108, v72, v73\n"
    "v_cvt_pk_bf16_f32 v109, v74, v75\n"
    "s_waitcnt vmcnt(9) lgkmcnt(4)\n"
    "v_mfma_f32_16x16x32_bf16 v[56:59], v[80:83], v[36:39], 0\n"
    "ds_read_b128 v[80:83], v7 offset:8960\n"
    "s_waitcnt vmcnt(8) lgkmcnt(4)\n"
    "v_mfma_f32_16x16x32_bf16 v[56:59], v[84:87], v[40:43], v[56:59]\n"
    "ds_read_b128 v[84:87], v7 offset:9216\n"
    "s_waitcnt vmcnt(7) lgkmcnt(4)\n"
    "v_mfma_f32_16x16x32_bf16 v[56:59], v[88:91], v[44:47], v[56:59]\n"
    "ds_read_b128 v[88:91], v7 offset:9472\n"
    "s_waitcnt vmcnt(6) lgkmcnt(4)\n"
    "v_mfma_f32_16x16x32_bf16 v[56:59], v[92:95], v[48:51], v[56:59]\n"
    "ds_read_b128 v[92:95], v7 offset:9728\n"
    "s_waitcnt vmcnt(5) lgkmcnt(4)\n"
    "v_mfma_f32_16x16x32_bf16 v[56:59], v[96:99], v[52:55], v[56:59]\n"
    "ds_read_b128 v[96:99], v7 offset:9984\n"
    "s_waitcnt lgkmcnt(4)\n"
    "v_mfma_f32_16x16x32_bf16 v[60:63], v[80:83], v[36:39], 0\n"
    "ds_read_b128 v[80:83], v7 offset:10240\n"
    "s_waitcnt lgkmcnt(4)\n"
    "v_mfma_f32_16x16x32_bf16 v[60:63], v[84:87], v[40:43], v[60:63]\n"
    "ds_read_b128 v[84:87], v7 offset:10496\n"
    "s_waitcnt lgkmcnt(4)\n"
    "v_mfma_f32_16x16x32_bf16 v[60:63], v[88:91], v[44:47], v[60:63]\n"
    "ds_read_b128 v[88:91], v7 offset:10752\n"
    "s_waitcnt lgkmcnt(4)\n"
    "v_mfma_f32_16x16x32_bf16 v[60:63], v[92:95], v[48:51], v[60:63]\n"
    "ds_read_b128 v[92:95], v7 offset:11008\n"
    "s_waitcnt lgkmcnt(4)\n"
    "v_mfma_f32_16x16x32_bf16 v[60:63], v[96:99], v[52:55], v[60:63]\n"
    "ds_read_b128 v[96:99], v7 offset:11264\n"
    "s_waitcnt lgkmcnt(4)\n"
    "v_mfma_f32_16x16x32_bf16 v[64:67], v[80:83], v[36:39], 0\n"
    "ds_read_b128 v[80:83], v7 offset:11520\n"
    "s_waitcnt lgkmcnt(4)\n"
    "v_mfma_f32_16x16x32_bf16 v[64:67], v[84:87], v[40:43], v[64:67]\n"
    "ds_read_b128 v[84:87], v7 offset:11776\n"
    "s_waitcnt lgkmcnt(4)\n"
    "v_mfma_f32_16x16x32_bf16 v[64:67], v[88:91], v[44:47], v[64:67]\n"
    "ds_read_b128 v[88:91], v7 offset:12032\n"
    "s_waitcnt lgkmcnt(4)\n"
    "v_mfma_f32_16x16x32_bf16 v[64:67], v[92:95], v[48:51], v[64:67]\n"
    "ds_read_b128 v[92:95], v7 offset:12288\n"
    "s_waitcnt lgkmcnt(4)\n"
    "v_mfma_f32_16x16x32_bf16 v[64:67], v[96:99], v[52:55], v[64:67]\n"
    "ds_read_b128 v[96:99], v7 offset:12544\n"
    "s_waitcnt lgkmcnt(4)\n"
    "v_mfma_f32_16x16x32_bf16 v[68:71], v[80:83], v[36:39], 0\n"
    "ds_read_b128 v[80:83], v7 offset:12800\n"
    "s_waitcnt lgkmcnt(4)\n"
    "v_mfma_f32_16x16x32_bf16 v[68:71], v[84:87], v[40:43], v[68:71]\n"
    "ds_read_b128 v[84:87], v7 offset:13056\n"
    "s_waitcnt lgkmcnt(4)\n"
    "v_mfma_f32_16x16x32_bf16 v[68:71], v[88:91], v[44:47], v[68:71]\n"
    "ds_read_b128 v[88:91], v7 offset:13312\n"
    "s_waitcnt lgkmcnt(4)\n"
    "v_mfma_f32_16x16x32_bf16 v[68:71], v[92:95], v[48:51], v[68:71]\n"
    "ds_read_b128 v[92:95], v7 offset:13568\n"
    "s_waitcnt lgkmcnt(4)\n"
    "v_mfma_f32_16x16x32_bf16 v[68:71], v[96:99], v[52:55], v[68:71]\n"
    "ds_read_b128 v[96:99], v7 offset:13824\n"
    "s_waitcnt lgkmcnt(4)\n"
    "v_mfma_f32_16x16x32_bf16 v[72:75], v[80:83], v[36:39], 0\n"
    "ds_read_b128 v[80:83], v7 offset:14080\n"
    "s_waitcnt lgkmcnt(4)\n"
    "v_mfma_f32_16x16x32_bf16 v[72:75], v[84:87], v[40:43], v[72:75]\n"
    "ds_read_b128 v[84:87], v7 offset:14336\n"
    "s_waitcnt lgkmcnt(4)\n"
    "v_mfma_f32_16x16x32_bf16 v[72:75], v[88:91], v[44:47], v[72:75]\n"
    "ds_read_b128 v[88:91], v7 offset:14592\n"
    "s_waitcnt lgkmcnt(4)\n"
    "v_mfma_f32_16x16x32_bf16 v[72:75], v[92:95], v[48:51], v[72:75]\n"
    "ds_read_b128 v[92:95], v7 offset:14848\n"
    "s_waitcnt lgkmcnt(4)\n"
    "v_mfma_f32_16x16x32_bf16 v[72:75], v[96:99], v[52:55], v[72:75]\n"
    "ds_read_b128 v[96:99], v7 offset:15104\n"
    "s_waitcnt lgkmcnt(4)\n"
    "v_mfma_f32_16x16x32_bf16 v[76:79], v[80:83], v[36:39], 0\n"
    "ds_read_b128 v[80:83], v7 offset:0\n"
    "s_waitcnt lgkmcnt(4)\n"
    "v_mfma_f32_16x16x32_bf16 v[76:79], v[84:87], v[40:43], v[76:79]\n"
    "ds_read_b128 v[84:87], v7 offset:256\n"
    "s_waitcnt lgkmcnt(4)\n"
    "v_mfma_f32_16x16x32_bf16 v[76:79], v[88:91], v[44:47], v[76:79]\n"
    "ds_read_b128 v[88:91], v7 offset:512\n"
    "s_waitcnt lgkmcnt(4)\n"
    "v_mfma_f32_16x16x32_bf16 v[76:79], v[92:95], v[48:51], v[76:79]\n"
    "ds_read_b128 v[92:95], v7 offset:768\n"
    "s_waitcnt lgkmcnt(4)\n"
    "v_mfma_f32_16x16x32_bf16 v[76:79], v[96:99], v[52:55], v[76:79]\n"
    "ds_read_b128 v[96:99], v7 offset:1024\n"
    "s_add_u32 s48, s28, 248632320\n"
    "s_add_u32 s49, s48, 4096\n"
    "buffer_load_dwordx4 v[36:39], v0, s[40:43], s48 offen sc1\n"
    "buffer_load_dwordx4 v[40:43], v0, s[40:43], s48 offen offset:1024 sc1\n"
    "buffer_load_dwordx4 v[44:47], v0, s[40:43], s48 offen offset:2048 sc1\n"
    "buffer_load_dwordx4 v[48:51], v0, s[40:43], s48 offen offset:3072 sc1\n"
    "buffer_load_dwordx4 v[52:55], v0, s[40:43], s49 offen sc1\n"
    "v_add_f32 v76, 0x358637bd, v76\n"
    "v_rcp_f32 v76, v76\n"
    "s_nop 0\n"
    "v_pk_mul_f32 v[56:57], v[56:57], v[76:77] op_sel_hi:[1,0]\n"
    "v_pk_mul_f32 v[58:59], v[58:59], v[76:77] op_sel_hi:[1,0]\n"
    "v_pk_mul_f32 v[60:61], v[60:61], v[76:77] op_sel_hi:[1,0]\n"
    "v_pk_mul_f32 v[62:63], v[62:63], v[76:77] op_sel_hi:[1,0]\n"
    "v_pk_mul_f32 v[64:65], v[64:65], v[76:77] op_sel_hi:[1,0]\n"
    "v_pk_mul_f32 v[66:67], v[66:67], v[76:77] op_sel_hi:[1,0]\n"
    "v_pk_mul_f32 v[68:69], v[68:69], v[76:77] op_sel_hi:[1,0]\n"
    "v_pk_mul_f32 v[70:71], v[70:71], v[76:77] op_sel_hi:[1,0]\n"
    "v_pk_mul_f32 v[72:73], v[72:73], v[76:77] op_sel_hi:[1,0]\n"
    "v_pk_mul_f32 v[74:75], v[74:75], v[76:77] op_sel_hi:[1,0]\n"
    "v_cvt_pk_bf16_f32 v110, v56, v57\n"
    "v_cvt_pk_bf16_f32 v111, v58, v59\n"
    "v_cvt_pk_bf16_f32 v112, v60, v61\n"
    "v_cvt_pk_bf16_f32 v113, v62, v63\n"
    "v_cvt_pk_bf16_f32 v114, v64, v65\n"
    "v_cvt_pk_bf16_f32 v115, v66, v67\n"
    "v_cvt_pk_bf16_f32 v116, v68, v69\n"
    "v_cvt_pk_bf16_f32 v117, v70, v71\n"
    "v_cvt_pk_bf16_f32 v118, v72, v73\n"
    "v_cvt_pk_bf16_f32 v119, v74, v75\n"
    "ds_write_b128 v9, v[100:103] offset:0\n"
    "ds_write_b128 v9, v[104:107] offset:1024\n"
    "ds_write_b128 v9, v[108:111] offset:2048\n"
    "ds_write_b128 v9, v[112:115] offset:3072\n"
    "ds_write_b128 v9, v[116:119] offset:4096\n"
    "s_waitcnt vmcnt(9) lgkmcnt(9)\n"
    "v_mfma_f32_16x16x32_bf16 v[56:59], v[80:83], v[16:19], 0\n"
    "ds_read_b128 v[80:83], v7 offset:1280\n"
    "s_waitcnt vmcnt(8) lgkmcnt(9)\n"
    "v_mfma_f32_16x16x32_bf16 v[56:59], v[84:87], v[20:23], v[56:59]\n"
    "ds_read_b128 v[84:87], v7 offset:1536\n"
    "s_waitcnt vmcnt(7) lgkmcnt(9)\n"
    "v_mfma_f32_16x16x32_bf16 v[56:59], v[88:91], v[24:27], v[56:59]\n"
    "ds_read_b128 v[88:91], v7 offset:1792\n"
    "s_waitcnt vmcnt(6) lgkmcnt(9)\n"
    "v_mfma_f32_16x16x32_bf16 v[56:59], v[92:95], v[28:31], v[56:59]\n"
    "ds_read_b128 v[92:95], v7 offset:2048\n"
    "s_waitcnt vmcnt(5) lgkmcnt(9)\n"
    "v_mfma_f32_16x16x32_bf16 v[56:59], v[96:99], v[32:35], v[56:59]\n"
    "ds_read_b128 v[96:99], v7 offset:2304\n"
    "s_waitcnt lgkmcnt(4)\n"
    "v_mfma_f32_16x16x32_bf16 v[60:63], v[80:83], v[16:19], 0\n"
    "ds_read_b128 v[80:83], v7 offset:2560\n"
    "s_waitcnt lgkmcnt(4)\n"
    "v_mfma_f32_16x16x32_bf16 v[60:63], v[84:87], v[20:23], v[60:63]\n"
    "ds_read_b128 v[84:87], v7 offset:2816\n"
    "s_waitcnt lgkmcnt(4)\n"
    "v_mfma_f32_16x16x32_bf16 v[60:63], v[88:91], v[24:27], v[60:63]\n"
    "ds_read_b128 v[88:91], v7 offset:3072\n"
    "s_waitcnt lgkmcnt(4)\n"
    "v_mfma_f32_16x16x32_bf16 v[60:63], v[92:95], v[28:31], v[60:63]\n"
    "ds_read_b128 v[92:95], v7 offset:3328\n"
    "s_waitcnt lgkmcnt(4)\n"
    "v_mfma_f32_16x16x32_bf16 v[60:63], v[96:99], v[32:35], v[60:63]\n"
    "ds_read_b128 v[96:99], v7 offset:3584\n"
    "s_waitcnt lgkmcnt(4)\n"
    "v_mfma_f32_16x16x32_bf16 v[64:67], v[80:83], v[16:19], 0\n"
    "ds_read_b128 v[80:83], v7 offset:3840\n"
    "s_waitcnt lgkmcnt(4)\n"
    "v_mfma_f32_16x16x32_bf16 v[64:67], v[84:87], v[20:23], v[64:67]\n"
    "ds_read_b128 v[84:87], v7 offset:4096\n"
    "s_waitcnt lgkmcnt(4)\n"
    "v_mfma_f32_16x16x32_bf16 v[64:67], v[88:91], v[24:27], v[64:67]\n"
    "ds_read_b128 v[88:91], v7 offset:4352\n"
    "s_waitcnt lgkmcnt(4)\n"
    "v_mfma_f32_16x16x32_bf16 v[64:67], v[92:95], v[28:31], v[64:67]\n"
    "ds_read_b128 v[92:95], v7 offset:4608\n"
    "s_waitcnt lgkmcnt(4)\n"
    "v_mfma_f32_16x16x32_bf16 v[64:67], v[96:99], v[32:35], v[64:67]\n"
    "ds_read_b128 v[96:99], v7 offset:4864\n"
    "s_waitcnt lgkmcnt(4)\n"
    "v_mfma_f32_16x16x32_bf16 v[68:71], v[80:83], v[16:19], 0\n"
    "ds_read_b128 v[80:83], v7 offset:5120\n"
    "s_waitcnt lgkmcnt(4)\n"
    "v_mfma_f32_16x16x32_bf16 v[68:71], v[84:87], v[20:23], v[68:71]\n"
    "ds_read_b128 v[84:87], v7 offset:5376\n"
    "s_waitcnt lgkmcnt(4)\n"
    "v_mfma_f32_16x16x32_bf16 v[68:71], v[88:91], v[24:27], v[68:71]\n"
    "ds_read_b128 v[88:91], v7 offset:5632\n"
    "s_waitcnt lgkmcnt(4)\n"
    "v_mfma_f32_16x16x32_bf16 v[68:71], v[92:95], v[28:31], v[68:71]\n"
    "ds_read_b128 v[92:95], v7 offset:5888\n"
    "s_waitcnt lgkmcnt(4)\n"
    "v_mfma_f32_16x16x32_bf16 v[68:71], v[96:99], v[32:35], v[68:71]\n"
    "ds_read_b128 v[96:99], v7 offset:6144\n"
    "s_waitcnt lgkmcnt(4)\n"
    "v_mfma_f32_16x16x32_bf16 v[72:75], v[80:83], v[16:19], 0\n"
    "ds_read_b128 v[80:83], v7 offset:6400\n"
    "s_waitcnt lgkmcnt(4)\n"
    "v_mfma_f32_16x16x32_bf16 v[72:75], v[84:87], v[20:23], v[72:75]\n"
    "ds_read_b128 v[84:87], v7 offset:6656\n"
    "s_waitcnt lgkmcnt(4)\n"
    "v_mfma_f32_16x16x32_bf16 v[72:75], v[88:91], v[24:27], v[72:75]\n"
    "ds_read_b128 v[88:91], v7 offset:6912\n"
    "s_waitcnt lgkmcnt(4)\n"
    "v_mfma_f32_16x16x32_bf16 v[72:75], v[92:95], v[28:31], v[72:75]\n"
    "ds_read_b128 v[92:95], v7 offset:7168\n"
    "s_waitcnt lgkmcnt(4)\n"
    "v_mfma_f32_16x16x32_bf16 v[72:75], v[96:99], v[32:35], v[72:75]\n"
    "ds_read_b128 v[96:99], v7 offset:7424\n"
    "s_waitcnt lgkmcnt(4)\n"
    "v_mfma_f32_16x16x32_bf16 v[76:79], v[80:83], v[16:19], 0\n"
    "ds_read_b128 v[80:83], v7 offset:7680\n"
    "s_waitcnt lgkmcnt(4)\n"
    "v_mfma_f32_16x16x32_bf16 v[76:79], v[84:87], v[20:23], v[76:79]\n"
    "ds_read_b128 v[84:87], v7 offset:7936\n"
    "s_waitcnt lgkmcnt(4)\n"
    "v_mfma_f32_16x16x32_bf16 v[76:79], v[88:91], v[24:27], v[76:79]\n"
    "ds_read_b128 v[88:91], v7 offset:8192\n"
    "s_waitcnt lgkmcnt(4)\n"
    "v_mfma_f32_16x16x32_bf16 v[76:79], v[92:95], v[28:31], v[76:79]\n"
    "ds_read_b128 v[92:95], v7 offset:8448\n"
    "s_waitcnt lgkmcnt(4)\n"
    "v_mfma_f32_16x16x32_bf16 v[76:79], v[96:99], v[32:35], v[76:79]\n"
    "ds_read_b128 v[96:99], v7 offset:8704\n"
    "s_nop 6\n"
    "v_add_f32 v76, 0x358637bd, v76\n"
    "v_rcp_f32 v76, v76\n"
    "s_nop 0\n"
    "v_pk_mul_f32 v[56:57], v[56:57], v[76:77] op_sel_hi:[1,0]\n"
    "v_pk_mul_f32 v[58:59], v[58:59], v[76:77] op_sel_hi:[1,0]\n"
    "v_pk_mul_f32 v[60:61], v[60:61], v[76:77] op_sel_hi:[1,0]\n"
    "v_pk_mul_f32 v[62:63], v[62:63], v[76:77] op_sel_hi:[1,0]\n"
    "v_pk_mul_f32 v[64:65], v[64:65], v[76:77] op_sel_hi:[1,0]\n"
    "v_pk_mul_f32 v[66:67], v[66:67], v[76:77] op_sel_hi:[1,0]\n"
    "v_pk_mul_f32 v[68:69], v[68:69], v[76:77] op_sel_hi:[1,0]\n"
    "v_pk_mul_f32 v[70:71], v[70:71], v[76:77] op_sel_hi:[1,0]\n"
    "v_pk_mul_f32 v[72:73], v[72:73], v[76:77] op_sel_hi:[1,0]\n"
    "v_pk_mul_f32 v[74:75], v[74:75], v[76:77] op_sel_hi:[1,0]\n"
    "v_cvt_pk_bf16_f32 v100, v56, v57\n"
    "v_cvt_pk_bf16_f32 v101, v58, v59\n"
    "v_cvt_pk_bf16_f32 v102, v60, v61\n"
    "v_cvt_pk_bf16_f32 v103, v62, v63\n"
    "v_cvt_pk_bf16_f32 v104, v64, v65\n"
    "v_cvt_pk_bf16_f32 v105, v66, v67\n"
    "v_cvt_pk_bf16_f32 v106, v68, v69\n"
    "v_cvt_pk_bf16_f32 v107, v70, v71\n"
    "v_cvt_pk_bf16_f32 v108, v72, v73\n"
    "v_cvt_pk_bf16_f32 v109, v74, v75\n"
    "s_waitcnt vmcnt(4) lgkmcnt(4)\n"
    "v_mfma_f32_16x16x32_bf16 v[56:59], v[80:83], v[36:39], 0\n"
    "ds_read_b128 v[80:83], v7 offset:8960\n"
    "s_waitcnt vmcnt(3) lgkmcnt(4)\n"
    "v_mfma_f32_16x16x32_bf16 v[56:59], v[84:87], v[40:43], v[56:59]\n"
    "ds_read_b128 v[84:87], v7 offset:9216\n"
    "s_waitcnt vmcnt(2) lgkmcnt(4)\n"
    "v_mfma_f32_16x16x32_bf16 v[56:59], v[88:91], v[44:47], v[56:59]\n"
    "ds_read_b128 v[88:91], v7 offset:9472\n"
    "s_waitcnt vmcnt(1) lgkmcnt(4)\n"
    "v_mfma_f32_16x16x32_bf16 v[56:59], v[92:95], v[48:51], v[56:59]\n"
    "ds_read_b128 v[92:95], v7 offset:9728\n"
    "s_waitcnt vmcnt(0) lgkmcnt(4)\n"
    "v_mfma_f32_16x16x32_bf16 v[56:59], v[96:99], v[52:55], v[56:59]\n"
    "ds_read_b128 v[96:99], v7 offset:9984\n"
    "s_waitcnt lgkmcnt(4)\n"
    "v_mfma_f32_16x16x32_bf16 v[60:63], v[80:83], v[36:39], 0\n"
    "ds_read_b128 v[80:83], v7 offset:10240\n"
    "s_waitcnt lgkmcnt(4)\n"
    "v_mfma_f32_16x16x32_bf16 v[60:63], v[84:87], v[40:43], v[60:63]\n"
    "ds_read_b128 v[84:87], v7 offset:10496\n"
    "s_waitcnt lgkmcnt(4)\n"
    "v_mfma_f32_16x16x32_bf16 v[60:63], v[88:91], v[44:47], v[60:63]\n"
    "ds_read_b128 v[88:91], v7 offset:10752\n"
    "s_waitcnt lgkmcnt(4)\n"
    "v_mfma_f32_16x16x32_bf16 v[60:63], v[92:95], v[48:51], v[60:63]\n"
    "ds_read_b128 v[92:95], v7 offset:11008\n"
    "s_waitcnt lgkmcnt(4)\n"
    "v_mfma_f32_16x16x32_bf16 v[60:63], v[96:99], v[52:55], v[60:63]\n"
    "ds_read_b128 v[96:99], v7 offset:11264\n"
    "s_waitcnt lgkmcnt(4)\n"
    "v_mfma_f32_16x16x32_bf16 v[64:67], v[80:83], v[36:39], 0\n"
    "ds_read_b128 v[80:83], v7 offset:11520\n"
    "s_waitcnt lgkmcnt(4)\n"
    "v_mfma_f32_16x16x32_bf16 v[64:67], v[84:87], v[40:43], v[64:67]\n"
    "ds_read_b128 v[84:87], v7 offset:11776\n"
    "s_waitcnt lgkmcnt(4)\n"
    "v_mfma_f32_16x16x32_bf16 v[64:67], v[88:91], v[44:47], v[64:67]\n"
    "ds_read_b128 v[88:91], v7 offset:12032\n"
    "s_waitcnt lgkmcnt(4)\n"
    "v_mfma_f32_16x16x32_bf16 v[64:67], v[92:95], v[48:51], v[64:67]\n"
    "ds_read_b128 v[92:95], v7 offset:12288\n"
    "s_waitcnt lgkmcnt(4)\n"
    "v_mfma_f32_16x16x32_bf16 v[64:67], v[96:99], v[52:55], v[64:67]\n"
    "ds_read_b128 v[96:99], v7 offset:12544\n"
    "s_waitcnt lgkmcnt(4)\n"
    "v_mfma_f32_16x16x32_bf16 v[68:71], v[80:83], v[36:39], 0\n"
    "ds_read_b128 v[80:83], v7 offset:12800\n"
    "s_waitcnt lgkmcnt(4)\n"
    "v_mfma_f32_16x16x32_bf16 v[68:71], v[84:87], v[40:43], v[68:71]\n"
    "ds_read_b128 v[84:87], v7 offset:13056\n"
    "s_waitcnt lgkmcnt(4)\n"
    "v_mfma_f32_16x16x32_bf16 v[68:71], v[88:91], v[44:47], v[68:71]\n"
    "ds_read_b128 v[88:91], v7 offset:13312\n"
    "s_waitcnt lgkmcnt(4)\n"
    "v_mfma_f32_16x16x32_bf16 v[68:71], v[92:95], v[48:51], v[68:71]\n"
    "ds_read_b128 v[92:95], v7 offset:13568\n"
    "s_waitcnt lgkmcnt(4)\n"
    "v_mfma_f32_16x16x32_bf16 v[68:71], v[96:99], v[52:55], v[68:71]\n"
    "ds_read_b128 v[96:99], v7 offset:13824\n"
    "s_waitcnt lgkmcnt(4)\n"
    "v_mfma_f32_16x16x32_bf16 v[72:75], v[80:83], v[36:39], 0\n"
    "ds_read_b128 v[80:83], v7 offset:14080\n"
    "s_waitcnt lgkmcnt(4)\n"
    "v_mfma_f32_16x16x32_bf16 v[72:75], v[84:87], v[40:43], v[72:75]\n"
    "ds_read_b128 v[84:87], v7 offset:14336\n"
    "s_waitcnt lgkmcnt(4)\n"
    "v_mfma_f32_16x16x32_bf16 v[72:75], v[88:91], v[44:47], v[72:75]\n"
    "ds_read_b128 v[88:91], v7 offset:14592\n"
    "s_waitcnt lgkmcnt(4)\n"
    "v_mfma_f32_16x16x32_bf16 v[72:75], v[92:95], v[48:51], v[72:75]\n"
    "ds_read_b128 v[92:95], v7 offset:14848\n"
    "s_waitcnt lgkmcnt(4)\n"
    "v_mfma_f32_16x16x32_bf16 v[72:75], v[96:99], v[52:55], v[72:75]\n"
    "ds_read_b128 v[96:99], v7 offset:15104\n"
    "s_waitcnt lgkmcnt(4)\n"
    "v_mfma_f32_16x16x32_bf16 v[76:79], v[80:83], v[36:39], 0\n"
    "s_waitcnt lgkmcnt(3)\n"
    "v_mfma_f32_16x16x32_bf16 v[76:79], v[84:87], v[40:43], v[76:79]\n"
    "s_waitcnt lgkmcnt(2)\n"
    "v_mfma_f32_16x16x32_bf16 v[76:79], v[88:91], v[44:47], v[76:79]\n"
    "s_waitcnt lgkmcnt(1)\n"
    "v_mfma_f32_16x16x32_bf16 v[76:79], v[92:95], v[48:51], v[76:79]\n"
    "s_waitcnt lgkmcnt(0)\n"
    "v_mfma_f32_16x16x32_bf16 v[76:79], v[96:99], v[52:55], v[76:79]\n"
    "s_nop 7\n"
    "v_add_f32 v76, 0x358637bd, v76\n"
    "v_rcp_f32 v76, v76\n"
    "s_nop 0\n"
    "v_pk_mul_f32 v[56:57], v[56:57], v[76:77] op_sel_hi:[1,0]\n"
    "v_pk_mul_f32 v[58:59], v[58:59], v[76:77] op_sel_hi:[1,0]\n"
    "v_pk_mul_f32 v[60:61], v[60:61], v[76:77] op_sel_hi:[1,0]\n"
    "v_pk_mul_f32 v[62:63], v[62:63], v[76:77] op_sel_hi:[1,0]\n"
    "v_pk_mul_f32 v[64:65], v[64:65], v[76:77] op_sel_hi:[1,0]\n"
    "v_pk_mul_f32 v[66:67], v[66:67], v[76:77] op_sel_hi:[1,0]\n"
    "v_pk_mul_f32 v[68:69], v[68:69], v[76:77] op_sel_hi:[1,0]\n"
    "v_pk_mul_f32 v[70:71], v[70:71], v[76:77] op_sel_hi:[1,0]\n"
    "v_pk_mul_f32 v[72:73], v[72:73], v[76:77] op_sel_hi:[1,0]\n"
    "v_pk_mul_f32 v[74:75], v[74:75], v[76:77] op_sel_hi:[1,0]\n"
    "v_cvt_pk_bf16_f32 v110, v56, v57\n"
    "v_cvt_pk_bf16_f32 v111, v58, v59\n"
    "v_cvt_pk_bf16_f32 v112, v60, v61\n"
    "v_cvt_pk_bf16_f32 v113, v62, v63\n"
    "v_cvt_pk_bf16_f32 v114, v64, v65\n"
    "v_cvt_pk_bf16_f32 v115, v66, v67\n"
    "v_cvt_pk_bf16_f32 v116, v68, v69\n"
    "v_cvt_pk_bf16_f32 v117, v70, v71\n"
    "v_cvt_pk_bf16_f32 v118, v72, v73\n"
    "v_cvt_pk_bf16_f32 v119, v74, v75\n"
    "ds_write_b128 v9, v[100:103] offset:20480\n"
    "ds_write_b128 v9, v[104:107] offset:21504\n"
    "ds_write_b128 v9, v[108:111] offset:22528\n"
    "ds_write_b128 v9, v[112:115] offset:23552\n"
    "ds_write_b128 v9, v[116:119] offset:24576\n"
    "v_add_u32 v9, 76800, v0\n"
    "s_waitcnt lgkmcnt(0)\n"
    "s_barrier\n"
    "ds_read_b128 v[96:99], v9 offset:0\n"
    "ds_read_b128 v[100:103], v9 offset:20480\n"
    "ds_read_b128 v[104:107], v9 offset:1024\n"
    "ds_read_b128 v[108:111], v9 offset:21504\n"
    "ds_read_b128 v[112:115], v9 offset:2048\n"
    "ds_read_b128 v[116:119], v9 offset:22528\n"
    "s_waitcnt lgkmcnt(5)\n"
    "v_mfma_f32_16x16x32_bf16 v[16:19], v[128:131], v[96:99], 0\n"
    "s_waitcnt lgkmcnt(4)\n"
    "v_mfma_f32_16x16x32_bf16 v[56:59], v[128:131], v[100:103], 0\n"
    "s_add_u32 s50, s50, 4096\n"
    "buffer_load_dwordx4 v[128:131], v0, s[40:43], s50 offen\n"
    "ds_read_b128 v[120:123], v9 offset:3072\n"
    "v_mfma_f32_16x16x32_bf16 v[20:23], v[132:135], v[96:99], 0\n"
    "v_mfma_f32_16x16x32_bf16 v[60:63], v[132:135], v[100:103], 0\n"
    "buffer_load_dwordx4 v[132:135], v0, s[40:43], s50 offen offset:1024\n"
    "v_mfma_f32_16x16x32_bf16 v[24:27], v[136:139], v[96:99], 0\n"
    "v_mfma_f32_16x16x32_bf16 v[64:67], v[136:139], v[100:103], 0\n"
    "buffer_load_dwordx4 v[136:139], v0, s[40:43], s50 offen offset:2048\n"
    "v_mfma_f32_16x16x32_bf16 v[28:31], v[140:143], v[96:99], 0\n"
    "v_mfma_f32_16x16x32_bf16 v[68:71], v[140:143], v[100:103], 0\n"
    "buffer_load_dwordx4 v[140:143], v0, s[40:43], s50 offen offset:3072\n"
    "v_mfma_f32_16x16x32_bf16 v[32:35], v[144:147], v[96:99], 0\n"
    "v_mfma_f32_16x16x32_bf16 v[72:75], v[144:147], v[100:103], 0\n"
    "s_add_u32 s50, s50, 4096\n"
    "buffer_load_dwordx4 v[144:147], v0, s[40:43], s50 offen\n"
    "v_mfma_f32_16x16x32_bf16 v[36:39], v[148:151], v[96:99], 0\n"
    "v_mfma_f32_16x16x32_bf16 v[76:79], v[148:151], v[100:103], 0\n"
    "buffer_load_dwordx4 v[148:151], v0, s[40:43], s50 offen offset:1024\n"
    "ds_read_b128 v[124:127], v9 offset:23552\n"
    "v_mfma_f32_16x16x32_bf16 v[40:43], v[152:155], v[96:99], 0\n"
    "v_mfma_f32_16x16x32_bf16 v[80:83], v[152:155], v[100:103], 0\n"
    "buffer_load_dwordx4 v[152:155], v0, s[40:43], s50 offen offset:2048\n"
    "v_mfma_f32_16x16x32_bf16 v[44:47], v[156:159], v[96:99], 0\n"
    "v_mfma_f32_16x16x32_bf16 v[84:87], v[156:159], v[100:103], 0\n"
    "buffer_load_dwordx4 v[156:159], v0, s[40:43], s50 offen offset:3072\n"
    "v_mfma_f32_16x16x32_bf16 v[48:51], v[160:163], v[96:99], 0\n"
    "v_mfma_f32_16x16x32_bf16 v[88:91], v[160:163], v[100:103], 0\n"
    "s_add_u32 s50, s50, 4096\n"
    "buffer_load_dwordx4 v[160:163], v0, s[40:43], s50 offen\n"
    "v_mfma_f32_16x16x32_bf16 v[52:55], v[164:167], v[96:99], 0\n"
    "v_mfma_f32_16x16x32_bf16 v[92:95], v[164:167], v[100:103], 0\n"
    "buffer_load_dwordx4 v[164:167], v0, s[40:43], s50 offen offset:1024\n"
    "s_waitcnt lgkmcnt(5)\n"
    "v_mfma_f32_16x16x32_bf16 v[16:19], v[168:171], v[104:107], v[16:19]\n"
    "s_waitcnt lgkmcnt(4)\n"
    "v_mfma_f32_16x16x32_bf16 v[56:59], v[168:171], v[108:111], v[56:59]\n"
    "buffer_load_dwordx4 v[168:171], v0, s[40:43], s50 offen offset:2048\n"
    "ds_read_b128 v[96:99], v9 offset:4096\n"
    "v_mfma_f32_16x16x32_bf16 v[20:23], v[172:175], v[104:107], v[20:23]\n"
    "v_mfma_f32_16x16x32_bf16 v[60:63], v[172:175], v[108:111], v[60:63]\n"
    "buffer_load_dwordx4 v[172:175], v0, s[40:43], s50 offen offset:3072\n"
    "v_mfma_f32_16x16x32_bf16 v[24:27], v[176:179], v[104:107], v[24:27]\n"
    "v_mfma_f32_16x16x32_bf16 v[64:67], v[176:179], v[108:111], v[64:67]\n"
    "s_add_u32 s50, s50, 4096\n"
    "buffer_load_dwordx4 v[176:179], v0, s[40:43], s50 offen\n"
    "v_mfma_f32_16x16x32_bf16 v[28:31], v[180:183], v[104:107], v[28:31]\n"
    "v_mfma_f32_16x16x32_bf16 v[68:71], v[180:183], v[108:111], v[68:71]\n"
    "buffer_load_dwordx4 v[180:183], v0, s[40:43], s50 offen offset:1024\n"
    "v_mfma_f32_16x16x32_bf16 v[32:35], v[184:187], v[104:107], v[32:35]\n"
    "v_mfma_f32_16x16x32_bf16 v[72:75], v[184:187], v[108:111], v[72:75]\n"
    "buffer_load_dwordx4 v[184:187], v0, s[40:43], s50 offen offset:2048\n"
    "v_mfma_f32_16x16x32_bf16 v[36:39], v[188:191], v[104:107], v[36:39]\n"
    "v_mfma_f32_16x16x32_bf16 v[76:79], v[188:191], v[108:111], v[76:79]\n"
    "buffer_load_dwordx4 v[188:191], v0, s[40:43], s50 offen offset:3072\n"
    "ds_read_b128 v[100:103], v9 offset:24576\n"
    "v_mfma_f32_16x16x32_bf16 v[40:43], v[192:195], v[104:107], v[40:43]\n"
    "v_mfma_f32_16x16x32_bf16 v[80:83], v[192:195], v[108:111], v[80:83]\n"
    "s_add_u32 s50, s50, 4096\n"
    "buffer_load_dwordx4 v[192:195], v0, s[40:43], s50 offen\n"
    "v_mfma_f32_16x16x32_bf16 v[44:47], v[196:199], v[104:107], v[44:47]\n"
    "v_mfma_f32_16x16x32_bf16 v[84:87], v[196:199], v[108:111], v[84:87]\n"
    "buffer_load_dwordx4 v[196:199], v0, s[40:43], s50 offen offset:1024\n"
    "v_mfma_f32_16x16x32_bf16 v[48:51], v[200:203], v[104:107], v[48:51]\n"
    "v_mfma_f32_16x16x32_bf16 v[88:91], v[200:203], v[108:111], v[88:91]\n"
    "buffer_load_dwordx4 v[200:203], v0, s[40:43], s50 offen offset:2048\n"
    "v_mfma_f32_16x16x32_bf16 v[52:55], v[204:207], v[104:107], v[52:55]\n"
    "v_mfma_f32_16x16x32_bf16 v[92:95], v[204:207], v[108:111], v[92:95]\n"
    "buffer_load_dwordx4 v[204:207], v0, s[40:43], s50 offen offset:3072\n"
    "s_waitcnt lgkmcnt(5)\n"
    "v_mfma_f32_16x16x32_bf16 v[16:19], v[208:211], v[112:115], v[16:19]\n"
    "s_waitcnt lgkmcnt(4)\n"
    "v_mfma_f32_16x16x32_bf16 v[56:59], v[208:211], v[116:119], v[56:59]\n"
    "s_add_u32 s50, s50, 4096\n"
    "buffer_load_dwordx4 v[208:211], v0, s[40:43], s50 offen\n"
    "ds_read_b128 v[104:107], v9 offset:5120\n"
    "v_mfma_f32_16x16x32_bf16 v[20:23], v[212:215], v[112:115], v[20:23]\n"
    "v_mfma_f32_16x16x32_bf16 v[60:63], v[212:215], v[116:119], v[60:63]\n"
    "buffer_load_dwordx4 v[212:215], v0, s[40:43], s50 offen offset:1024\n"
    "v_mfma_f32_16x16x32_bf16 v[24:27], v[216:219], v[112:115], v[24:27]\n"
    "v_mfma_f32_16x16x32_bf16 v[64:67], v[216:219], v[116:119], v[64:67]\n"
    "buffer_load_dwordx4 v[216:219], v0, s[40:43], s50 offen offset:2048\n"
    "v_mfma_f32_16x16x32_bf16 v[28:31], v[220:223], v[112:115], v[28:31]\n"
    "v_mfma_f32_16x16x32_bf16 v[68:71], v[220:223], v[116:119], v[68:71]\n"
    "buffer_load_dwordx4 v[220:223], v0, s[40:43], s50 offen offset:3072\n"
    "v_mfma_f32_16x16x32_bf16 v[32:35], v[224:227], v[112:115], v[32:35]\n"
    "v_mfma_f32_16x16x32_bf16 v[72:75], v[224:227], v[116:119], v[72:75]\n"
    "s_add_u32 s50, s50, 4096\n"
    "buffer_load_dwordx4 v[224:227], v0, s[40:43], s50 offen\n"
    "v_mfma_f32_16x16x32_bf16 v[36:39], v[228:231], v[112:115], v[36:39]\n"
    "v_mfma_f32_16x16x32_bf16 v[76:79], v[228:231], v[116:119], v[76:79]\n"
    "buffer_load_dwordx4 v[228:231], v0, s[40:43], s50 offen offset:1024\n"
    "ds_read_b128 v[108:111], v9 offset:25600\n"
    "v_mfma_f32_16x16x32_bf16 v[40:43], v[232:235], v[112:115], v[40:43]\n"
    "v_mfma_f32_16x16x32_bf16 v[80:83], v[232:235], v[116:119], v[80:83]\n"
    "buffer_load_dwordx4 v[232:235], v0, s[40:43], s50 offen offset:2048\n"
    "v_mfma_f32_16x16x32_bf16 v[44:47], v[236:239], v[112:115], v[44:47]\n"
    "v_mfma_f32_16x16x32_bf16 v[84:87], v[236:239], v[116:119], v[84:87]\n"
    "buffer_load_dwordx4 v[236:239], v0, s[40:43], s50 offen offset:3072\n"
    "v_mfma_f32_16x16x32_bf16 v[48:51], v[240:243], v[112:115], v[48:51]\n"
    "v_mfma_f32_16x16x32_bf16 v[88:91], v[240:243], v[116:119], v[88:91]\n"
    "s_add_u32 s50, s50, 4096\n"
    "buffer_load_dwordx4 v[240:243], v0, s[40:43], s50 offen\n"
    "v_mfma_f32_16x16x32_bf16 v[52:55], v[244:247], v[112:115], v[52:55]\n"
    "v_mfma_f32_16x16x32_bf16 v[92:95], v[244:247], v[116:119], v[92:95]\n"
    "buffer_load_dwordx4 v[244:247], v0, s[40:43], s50 offen offset:1024\n"
    "s_waitcnt lgkmcnt(5)\n"
    "v_mfma_f32_16x16x32_bf16 v[16:19], v[248:251], v[120:123], v[16:19]\n"
    "s_waitcnt lgkmcnt(4)\n"
    "v_mfma_f32_16x16x32_bf16 v[56:59], v[248:251], v[124:127], v[56:59]\n"
    "buffer_load_dwordx4 v[248:251], v0, s[40:43], s50 offen offset:2048\n"
    "ds_read_b128 v[112:115], v9 offset:6144\n"
    "v_mfma_f32_16x16x32_bf16 v[20:23], v[252:255], v[120:123], v[20:23]\n"
    "v_mfma_f32_16x16x32_bf16 v[60:63], v[252:255], v[124:127], v[60:63]\n"
    "buffer_load_dwordx4 v[252:255], v0, s[40:43], s50 offen offset:3072\n"
    "s_waitcnt vmcnt(31)\n"
    "v_mfma_f32_16x16x32_bf16 v[24:27], v[128:131], v[120:123], v[24:27]\n"
    "v_mfma_f32_16x16x32_bf16 v[64:67], v[128:131], v[124:127], v[64:67]\n"
    "s_add_u32 s50, s50, 4096\n"
    "buffer_load_dwordx4 v[128:131], v0, s[40:43], s50 offen\n"
    "s_waitcnt vmcnt(31)\n"
    "v_mfma_f32_16x16x32_bf16 v[28:31], v[132:135], v[120:123], v[28:31]\n"
    "v_mfma_f32_16x16x32_bf16 v[68:71], v[132:135], v[124:127], v[68:71]\n"
    "buffer_load_dwordx4 v[132:135], v0, s[40:43], s50 offen offset:1024\n"
    "s_waitcnt vmcnt(31)\n"
    "v_mfma_f32_16x16x32_bf16 v[32:35], v[136:139], v[120:123], v[32:35]\n"
    "v_mfma_f32_16x16x32_bf16 v[72:75], v[136:139], v[124:127], v[72:75]\n"
    "buffer_load_dwordx4 v[136:139], v0, s[40:43], s50 offen offset:2048\n"
    "s_waitcnt vmcnt(31)\n"
    "v_mfma_f32_16x16x32_bf16 v[36:39], v[140:143], v[120:123], v[36:39]\n"
    "v_mfma_f32_16x16x32_bf16 v[76:79], v[140:143], v[124:127], v[76:79]\n"
    "buffer_load_dwordx4 v[140:143], v0, s[40:43], s50 offen offset:3072\n"
    "ds_read_b128 v[116:119], v9 offset:26624\n"
    "s_waitcnt vmcnt(31)\n"
    "v_mfma_f32_16x16x32_bf16 v[40:43], v[144:147], v[120:123], v[40:43]\n"
    "v_mfma_f32_16x16x32_bf16 v[80:83], v[144:147], v[124:127], v[80:83]\n"
    "s_add_u32 s50, s50, 4096\n"
    "buffer_load_dwordx4 v[144:147], v0, s[40:43], s50 offen\n"
    "s_waitcnt vmcnt(31)\n"
    "v_mfma_f32_16x16x32_bf16 v[44:47], v[148:151], v[120:123], v[44:47]\n"
    "v_mfma_f32_16x16x32_bf16 v[84:87], v[148:151], v[124:127], v[84:87]\n"
    "buffer_load_dwordx4 v[148:151], v0, s[40:43], s50 offen offset:1024\n"
    "s_waitcnt vmcnt(31)\n"
    "v_mfma_f32_16x16x32_bf16 v[48:51], v[152:155], v[120:123], v[48:51]\n"
    "v_mfma_f32_16x16x32_bf16 v[88:91], v[152:155], v[124:127], v[88:91]\n"
    "buffer_load_dwordx4 v[152:155], v0, s[40:43], s50 offen offset:2048\n"
    "s_waitcnt vmcnt(31)\n"
    "v_mfma_f32_16x16x32_bf16 v[52:55], v[156:159], v[120:123], v[52:55]\n"
    "v_mfma_f32_16x16x32_bf16 v[92:95], v[156:159], v[124:127], v[92:95]\n"
    "buffer_load_dwordx4 v[156:159], v0, s[40:43], s50 offen offset:3072\n"
    "s_waitcnt vmcnt(31) lgkmcnt(5)\n"
    "v_mfma_f32_16x16x32_bf16 v[16:19], v[160:163], v[96:99], v[16:19]\n"
    "s_waitcnt lgkmcnt(4)\n"
    "v_mfma_f32_16x16x32_bf16 v[56:59], v[160:163], v[100:103], v[56:59]\n"
    "s_add_u32 s50, s50, 4096\n"
    "buffer_load_dwordx4 v[160:163], v0, s[40:43], s50 offen\n"
    "ds_read_b128 v[120:123], v9 offset:7168\n"
    "s_waitcnt vmcnt(31)\n"
    "v_mfma_f32_16x16x32_bf16 v[20:23], v[164:167], v[96:99], v[20:23]\n"
    "v_mfma_f32_16x16x32_bf16 v[60:63], v[164:167], v[100:103], v[60:63]\n"
    "buffer_load_dwordx4 v[164:167], v0, s[40:43], s50 offen offset:1024\n"
    "s_waitcnt vmcnt(31)\n"
    "v_mfma_f32_16x16x32_bf16 v[24:27], v[168:171], v[96:99], v[24:27]\n"
    "v_mfma_f32_16x16x32_bf16 v[64:67], v[168:171], v[100:103], v[64:67]\n"
    "buffer_load_dwordx4 v[168:171], v0, s[40:43], s50 offen offset:2048\n"
    "s_waitcnt vmcnt(31)\n"
    "v_mfma_f32_16x16x32_bf16 v[28:31], v[172:175], v[96:99], v[28:31]\n"
    "v_mfma_f32_16x16x32_bf16 v[68:71], v[172:175], v[100:103], v[68:71]\n"
    "buffer_load_dwordx4 v[172:175], v0, s[40:43], s50 offen offset:3072\n"
    "s_waitcnt vmcnt(31)\n"
    "v_mfma_f32_16x16x32_bf16 v[32:35], v[176:179], v[96:99], v[32:35]\n"
    "v_mfma_f32_16x16x32_bf16 v[72:75], v[176:179], v[100:103], v[72:75]\n"
    "s_add_u32 s50, s50, 4096\n"
    "buffer_load_dwordx4 v[176:179], v0, s[40:43], s50 offen\n"
    "s_waitcnt vmcnt(31)\n"
    "v_mfma_f32_16x16x32_bf16 v[36:39], v[180:183], v[96:99], v[36:39]\n"
    "v_mfma_f32_16x16x32_bf16 v[76:79], v[180:183], v[100:103], v[76:79]\n"
    "buffer_load_dwordx4 v[180:183], v0, s[40:43], s50 offen offset:1024\n"
    "ds_read_b128 v[124:127], v9 offset:27648\n"
    "s_waitcnt vmcnt(31)\n"
    "v_mfma_f32_16x16x32_bf16 v[40:43], v[184:187], v[96:99], v[40:43]\n"
    "v_mfma_f32_16x16x32_bf16 v[80:83], v[184:187], v[100:103], v[80:83]\n"
    "buffer_load_dwordx4 v[184:187], v0, s[40:43], s50 offen offset:2048\n"
    "s_waitcnt vmcnt(31)\n"
    "v_mfma_f32_16x16x32_bf16 v[44:47], v[188:191], v[96:99], v[44:47]\n"
    "v_mfma_f32_16x16x32_bf16 v[84:87], v[188:191], v[100:103], v[84:87]\n"
    "buffer_load_dwordx4 v[188:191], v0, s[40:43], s50 offen offset:3072\n"
    "s_waitcnt vmcnt(31)\n"
    "v_mfma_f32_16x16x32_bf16 v[48:51], v[192:195], v[96:99], v[48:51]\n"
    "v_mfma_f32_16x16x32_bf16 v[88:91], v[192:195], v[100:103], v[88:91]\n"
    "s_add_u32 s50, s50, 4096\n"
    "buffer_load_dwordx4 v[192:195], v0, s[40:43], s50 offen\n"
    "s_waitcnt vmcnt(31)\n"
    "v_mfma_f32_16x16x32_bf16 v[52:55], v[196:199], v[96:99], v[52:55]\n"
    "v_mfma_f32_16x16x32_bf16 v[92:95], v[196:199], v[100:103], v[92:95]\n"
    "buffer_load_dwordx4 v[196:199], v0, s[40:43], s50 offen offset:1024\n"
    "s_waitcnt vmcnt(31) lgkmcnt(5)\n"
    "v_mfma_f32_16x16x32_bf16 v[16:19], v[200:203], v[104:107], v[16:19]\n"
    "s_waitcnt lgkmcnt(4)\n"
    "v_mfma_f32_16x16x32_bf16 v[56:59], v[200:203], v[108:111], v[56:59]\n"
    "buffer_load_dwordx4 v[200:203], v0, s[40:43], s50 offen offset:2048\n"
    "ds_read_b128 v[96:99], v9 offset:8192\n"
    "s_waitcnt vmcnt(31)\n"
    "v_mfma_f32_16x16x32_bf16 v[20:23], v[204:207], v[104:107], v[20:23]\n"
    "v_mfma_f32_16x16x32_bf16 v[60:63], v[204:207], v[108:111], v[60:63]\n"
    "buffer_load_dwordx4 v[204:207], v0, s[40:43], s50 offen offset:3072\n"
    "s_waitcnt vmcnt(31)\n"
    "v_mfma_f32_16x16x32_bf16 v[24:27], v[208:211], v[104:107], v[24:27]\n"
    "v_mfma_f32_16x16x32_bf16 v[64:67], v[208:211], v[108:111], v[64:67]\n"
    "s_add_u32 s50, s50, 4096\n"
    "buffer_load_dwordx4 v[208:211], v0, s[40:43], s50 offen\n"
    "s_waitcnt vmcnt(31)\n"
    "v_mfma_f32_16x16x32_bf16 v[28:31], v[212:215], v[104:107], v[28:31]\n"
    "v_mfma_f32_16x16x32_bf16 v[68:71], v[212:215], v[108:111], v[68:71]\n"
    "buffer_load_dwordx4 v[212:215], v0, s[40:43], s50 offen offset:1024\n"
    "s_waitcnt vmcnt(31)\n"
    "v_mfma_f32_16x16x32_bf16 v[32:35], v[216:219], v[104:107], v[32:35]\n"
    "v_mfma_f32_16x16x32_bf16 v[72:75], v[216:219], v[108:111], v[72:75]\n"
    "buffer_load_dwordx4 v[216:219], v0, s[40:43], s50 offen offset:2048\n"
    "s_waitcnt vmcnt(31)\n"
    "v_mfma_f32_16x16x32_bf16 v[36:39], v[220:223], v[104:107], v[36:39]\n"
    "v_mfma_f32_16x16x32_bf16 v[76:79], v[220:223], v[108:111], v[76:79]\n"
    "buffer_load_dwordx4 v[220:223], v0, s[40:43], s50 offen offset:3072\n"
    "ds_read_b128 v[100:103], v9 offset:28672\n"
    "s_waitcnt vmcnt(31)\n"
    "v_mfma_f32_16x16x32_bf16 v[40:43], v[224:227], v[104:107], v[40:43]\n"
    "v_mfma_f32_16x16x32_bf16 v[80:83], v[224:227], v[108:111], v[80:83]\n"
    "s_add_u32 s50, s50, 4096\n"
    "buffer_load_dwordx4 v[224:227], v0, s[40:43], s50 offen\n"
    "s_waitcnt vmcnt(31)\n"
    "v_mfma_f32_16x16x32_bf16 v[44:47], v[228:231], v[104:107], v[44:47]\n"
    "v_mfma_f32_16x16x32_bf16 v[84:87], v[228:231], v[108:111], v[84:87]\n"
    "buffer_load_dwordx4 v[228:231], v0, s[40:43], s50 offen offset:1024\n"
    "s_waitcnt vmcnt(31)\n"
    "v_mfma_f32_16x16x32_bf16 v[48:51], v[232:235], v[104:107], v[48:51]\n"
    "v_mfma_f32_16x16x32_bf16 v[88:91], v[232:235], v[108:111], v[88:91]\n"
    "buffer_load_dwordx4 v[232:235], v0, s[40:43], s50 offen offset:2048\n"
    "s_waitcnt vmcnt(31)\n"
    "v_mfma_f32_16x16x32_bf16 v[52:55], v[236:239], v[104:107], v[52:55]\n"
    "v_mfma_f32_16x16x32_bf16 v[92:95], v[236:239], v[108:111], v[92:95]\n"
    "buffer_load_dwordx4 v[236:239], v0, s[40:43], s50 offen offset:3072\n"
    "s_waitcnt vmcnt(31) lgkmcnt(5)\n"
    "v_mfma_f32_16x16x32_bf16 v[16:19], v[240:243], v[112:115], v[16:19]\n"
    "s_waitcnt lgkmcnt(4)\n"
    "v_mfma_f32_16x16x32_bf16 v[56:59], v[240:243], v[116:119], v[56:59]\n"
    "s_add_u32 s50, s50, 4096\n"
    "buffer_load_dwordx4 v[240:243], v0, s[40:43], s50 offen\n"
    "ds_read_b128 v[104:107], v9 offset:9216\n"
    "s_waitcnt vmcnt(31)\n"
    "v_mfma_f32_16x16x32_bf16 v[20:23], v[244:247], v[112:115], v[20:23]\n"
    "v_mfma_f32_16x16x32_bf16 v[60:63], v[244:247], v[116:119], v[60:63]\n"
    "buffer_load_dwordx4 v[244:247], v0, s[40:43], s50 offen offset:1024\n"
    "s_waitcnt vmcnt(31)\n"
    "v_mfma_f32_16x16x32_bf16 v[24:27], v[248:251], v[112:115], v[24:27]\n"
    "v_mfma_f32_16x16x32_bf16 v[64:67], v[248:251], v[116:119], v[64:67]\n"
    "buffer_load_dwordx4 v[248:251], v0, s[40:43], s50 offen offset:2048\n"
    "s_waitcnt vmcnt(31)\n"
    "v_mfma_f32_16x16x32_bf16 v[28:31], v[252:255], v[112:115], v[28:31]\n"
    "v_mfma_f32_16x16x32_bf16 v[68:71], v[252:255], v[116:119], v[68:71]\n"
    "buffer_load_dwordx4 v[252:255], v0, s[40:43], s50 offen offset:3072\n"
    "s_waitcnt vmcnt(31)\n"
    "v_mfma_f32_16x16x32_bf16 v[32:35], v[128:131], v[112:115], v[32:35]\n"
    "v_mfma_f32_16x16x32_bf16 v[72:75], v[128:131], v[116:119], v[72:75]\n"
    "s_add_u32 s50, s50, 4096\n"
    "buffer_load_dwordx4 v[128:131], v0, s[40:43], s50 offen\n"
    "s_waitcnt vmcnt(31)\n"
    "v_mfma_f32_16x16x32_bf16 v[36:39], v[132:135], v[112:115], v[36:39]\n"
    "v_mfma_f32_16x16x32_bf16 v[76:79], v[132:135], v[116:119], v[76:79]\n"
    "buffer_load_dwordx4 v[132:135], v0, s[40:43], s50 offen offset:1024\n"
    "ds_read_b128 v[108:111], v9 offset:29696\n"
    "s_waitcnt vmcnt(31)\n"
    "v_mfma_f32_16x16x32_bf16 v[40:43], v[136:139], v[112:115], v[40:43]\n"
    "v_mfma_f32_16x16x32_bf16 v[80:83], v[136:139], v[116:119], v[80:83]\n"
    "buffer_load_dwordx4 v[136:139], v0, s[40:43], s50 offen offset:2048\n"
    "s_waitcnt vmcnt(31)\n"
    "v_mfma_f32_16x16x32_bf16 v[44:47], v[140:143], v[112:115], v[44:47]\n"
    "v_mfma_f32_16x16x32_bf16 v[84:87], v[140:143], v[116:119], v[84:87]\n"
    "buffer_load_dwordx4 v[140:143], v0, s[40:43], s50 offen offset:3072\n"
    "s_waitcnt vmcnt(31)\n"
    "v_mfma_f32_16x16x32_bf16 v[48:51], v[144:147], v[112:115], v[48:51]\n"
    "v_mfma_f32_16x16x32_bf16 v[88:91], v[144:147], v[116:119], v[88:91]\n"
    "s_add_u32 s50, s50, 4096\n"
    "buffer_load_dwordx4 v[144:147], v0, s[40:43], s50 offen\n"
    "s_waitcnt vmcnt(31)\n"
    "v_mfma_f32_16x16x32_bf16 v[52:55], v[148:151], v[112:115], v[52:55]\n"
    "v_mfma_f32_16x16x32_bf16 v[92:95], v[148:151], v[116:119], v[92:95]\n"
    "buffer_load_dwordx4 v[148:151], v0, s[40:43], s50 offen offset:1024\n"
    "s_waitcnt vmcnt(31) lgkmcnt(5)\n"
    "v_mfma_f32_16x16x32_bf16 v[16:19], v[152:155], v[120:123], v[16:19]\n"
    "s_waitcnt lgkmcnt(4)\n"
    "v_mfma_f32_16x16x32_bf16 v[56:59], v[152:155], v[124:127], v[56:59]\n"
    "buffer_load_dwordx4 v[152:155], v0, s[40:43], s50 offen offset:2048\n"
    "ds_read_b128 v[112:115], v9 offset:10240\n"
    "s_waitcnt vmcnt(31)\n"
    "v_mfma_f32_16x16x32_bf16 v[20:23], v[156:159], v[120:123], v[20:23]\n"
    "v_mfma_f32_16x16x32_bf16 v[60:63], v[156:159], v[124:127], v[60:63]\n"
    "buffer_load_dwordx4 v[156:159], v0, s[40:43], s50 offen offset:3072\n"
    "s_waitcnt vmcnt(31)\n"
    "v_mfma_f32_16x16x32_bf16 v[24:27], v[160:163], v[120:123], v[24:27]\n"
    "v_mfma_f32_16x16x32_bf16 v[64:67], v[160:163], v[124:127], v[64:67]\n"
    "s_add_u32 s50, s50, 4096\n"
    "buffer_load_dwordx4 v[160:163], v0, s[40:43], s50 offen\n"
    "s_waitcnt vmcnt(31)\n"
    "v_mfma_f32_16x16x32_bf16 v[28:31], v[164:167], v[120:123], v[28:31]\n"
    "v_mfma_f32_16x16x32_bf16 v[68:71], v[164:167], v[124:127], v[68:71]\n"
    "buffer_load_dwordx4 v[164:167], v0, s[40:43], s50 offen offset:1024\n"
    "s_waitcnt vmcnt(31)\n"
    "v_mfma_f32_16x16x32_bf16 v[32:35], v[168:171], v[120:123], v[32:35]\n"
    "v_mfma_f32_16x16x32_bf16 v[72:75], v[168:171], v[124:127], v[72:75]\n"
    "buffer_load_dwordx4 v[168:171], v0, s[40:43], s50 offen offset:2048\n"
    "s_waitcnt vmcnt(31)\n"
    "v_mfma_f32_16x16x32_bf16 v[36:39], v[172:175], v[120:123], v[36:39]\n"
    "v_mfma_f32_16x16x32_bf16 v[76:79], v[172:175], v[124:127], v[76:79]\n"
    "buffer_load_dwordx4 v[172:175], v0, s[40:43], s50 offen offset:3072\n"
    "ds_read_b128 v[116:119], v9 offset:30720\n"
    "s_waitcnt vmcnt(31)\n"
    "v_mfma_f32_16x16x32_bf16 v[40:43], v[176:179], v[120:123], v[40:43]\n"
    "v_mfma_f32_16x16x32_bf16 v[80:83], v[176:179], v[124:127], v[80:83]\n"
    "s_add_u32 s50, s50, 4096\n"
    "buffer_load_dwordx4 v[176:179], v0, s[40:43], s50 offen\n"
    "s_waitcnt vmcnt(31)\n"
    "v_mfma_f32_16x16x32_bf16 v[44:47], v[180:183], v[120:123], v[44:47]\n"
    "v_mfma_f32_16x16x32_bf16 v[84:87], v[180:183], v[124:127], v[84:87]\n"
    "buffer_load_dwordx4 v[180:183], v0, s[40:43], s50 offen offset:1024\n"
    "s_waitcnt vmcnt(31)\n"
    "v_mfma_f32_16x16x32_bf16 v[48:51], v[184:187], v[120:123], v[48:51]\n"
    "v_mfma_f32_16x16x32_bf16 v[88:91], v[184:187], v[124:127], v[88:91]\n"
    "buffer_load_dwordx4 v[184:187], v0, s[40:43], s50 offen offset:2048\n"
    "s_waitcnt vmcnt(31)\n"
    "v_mfma_f32_16x16x32_bf16 v[52:55], v[188:191], v[120:123], v[52:55]\n"
    "v_mfma_f32_16x16x32_bf16 v[92:95], v[188:191], v[124:127], v[92:95]\n"
    "buffer_load_dwordx4 v[188:191], v0, s[40:43], s50 offen offset:3072\n"
    "s_waitcnt vmcnt(31) lgkmcnt(5)\n"
    "v_mfma_f32_16x16x32_bf16 v[16:19], v[192:195], v[96:99], v[16:19]\n"
    "s_waitcnt lgkmcnt(4)\n"
    "v_mfma_f32_16x16x32_bf16 v[56:59], v[192:195], v[100:103], v[56:59]\n"
    "s_add_u32 s50, s50, 4096\n"
    "buffer_load_dwordx4 v[192:195], v0, s[40:43], s50 offen\n"
    "ds_read_b128 v[120:123], v9 offset:11264\n"
    "s_waitcnt vmcnt(31)\n"
    "v_mfma_f32_16x16x32_bf16 v[20:23], v[196:199], v[96:99], v[20:23]\n"
    "v_mfma_f32_16x16x32_bf16 v[60:63], v[196:199], v[100:103], v[60:63]\n"
    "buffer_load_dwordx4 v[196:199], v0, s[40:43], s50 offen offset:1024\n"
    "s_waitcnt vmcnt(31)\n"
    "v_mfma_f32_16x16x32_bf16 v[24:27], v[200:203], v[96:99], v[24:27]\n"
    "v_mfma_f32_16x16x32_bf16 v[64:67], v[200:203], v[100:103], v[64:67]\n"
    "buffer_load_dwordx4 v[200:203], v0, s[40:43], s50 offen offset:2048\n"
    "s_waitcnt vmcnt(31)\n"
    "v_mfma_f32_16x16x32_bf16 v[28:31], v[204:207], v[96:99], v[28:31]\n"
    "v_mfma_f32_16x16x32_bf16 v[68:71], v[204:207], v[100:103], v[68:71]\n"
    "buffer_load_dwordx4 v[204:207], v0, s[40:43], s50 offen offset:3072\n"
    "s_waitcnt vmcnt(31)\n"
    "v_mfma_f32_16x16x32_bf16 v[32:35], v[208:211], v[96:99], v[32:35]\n"
    "v_mfma_f32_16x16x32_bf16 v[72:75], v[208:211], v[100:103], v[72:75]\n"
    "s_add_u32 s50, s50, 4096\n"
    "buffer_load_dwordx4 v[208:211], v0, s[40:43], s50 offen\n"
    "s_waitcnt vmcnt(31)\n"
    "v_mfma_f32_16x16x32_bf16 v[36:39], v[212:215], v[96:99], v[36:39]\n"
    "v_mfma_f32_16x16x32_bf16 v[76:79], v[212:215], v[100:103], v[76:79]\n"
    "buffer_load_dwordx4 v[212:215], v0, s[40:43], s50 offen offset:1024\n"
    "ds_read_b128 v[124:127], v9 offset:31744\n"
    "s_waitcnt vmcnt(31)\n"
    "v_mfma_f32_16x16x32_bf16 v[40:43], v[216:219], v[96:99], v[40:43]\n"
    "v_mfma_f32_16x16x32_bf16 v[80:83], v[216:219], v[100:103], v[80:83]\n"
    "buffer_load_dwordx4 v[216:219], v0, s[40:43], s50 offen offset:2048\n"
    "s_waitcnt vmcnt(31)\n"
    "v_mfma_f32_16x16x32_bf16 v[44:47], v[220:223], v[96:99], v[44:47]\n"
    "v_mfma_f32_16x16x32_bf16 v[84:87], v[220:223], v[100:103], v[84:87]\n"
    "buffer_load_dwordx4 v[220:223], v0, s[40:43], s50 offen offset:3072\n"
    "s_waitcnt vmcnt(31)\n"
    "v_mfma_f32_16x16x32_bf16 v[48:51], v[224:227], v[96:99], v[48:51]\n"
    "v_mfma_f32_16x16x32_bf16 v[88:91], v[224:227], v[100:103], v[88:91]\n"
    "s_add_u32 s50, s50, 4096\n"
    "buffer_load_dwordx4 v[224:227], v0, s[40:43], s50 offen\n"
    "s_waitcnt vmcnt(31)\n"
    "v_mfma_f32_16x16x32_bf16 v[52:55], v[228:231], v[96:99], v[52:55]\n"
    "v_mfma_f32_16x16x32_bf16 v[92:95], v[228:231], v[100:103], v[92:95]\n"
    "buffer_load_dwordx4 v[228:231], v0, s[40:43], s50 offen offset:1024\n"
    "s_waitcnt vmcnt(31) lgkmcnt(5)\n"
    "v_mfma_f32_16x16x32_bf16 v[16:19], v[232:235], v[104:107], v[16:19]\n"
    "s_waitcnt lgkmcnt(4)\n"
    "v_mfma_f32_16x16x32_bf16 v[56:59], v[232:235], v[108:111], v[56:59]\n"
    "buffer_load_dwordx4 v[232:235], v0, s[40:43], s50 offen offset:2048\n"
    "ds_read_b128 v[96:99], v9 offset:12288\n"
    "s_waitcnt vmcnt(31)\n"
    "v_mfma_f32_16x16x32_bf16 v[20:23], v[236:239], v[104:107], v[20:23]\n"
    "v_mfma_f32_16x16x32_bf16 v[60:63], v[236:239], v[108:111], v[60:63]\n"
    "buffer_load_dwordx4 v[236:239], v0, s[40:43], s50 offen offset:3072\n"
    "s_waitcnt vmcnt(31)\n"
    "v_mfma_f32_16x16x32_bf16 v[24:27], v[240:243], v[104:107], v[24:27]\n"
    "v_mfma_f32_16x16x32_bf16 v[64:67], v[240:243], v[108:111], v[64:67]\n"
    "s_add_u32 s50, s50, 4096\n"
    "buffer_load_dwordx4 v[240:243], v0, s[40:43], s50 offen\n"
    "s_waitcnt vmcnt(31)\n"
    "v_mfma_f32_16x16x32_bf16 v[28:31], v[244:247], v[104:107], v[28:31]\n"
    "v_mfma_f32_16x16x32_bf16 v[68:71], v[244:247], v[108:111], v[68:71]\n"
    "buffer_load_dwordx4 v[244:247], v0, s[40:43], s50 offen offset:1024\n"
    "s_waitcnt vmcnt(31)\n"
    "v_mfma_f32_16x16x32_bf16 v[32:35], v[248:251], v[104:107], v[32:35]\n"
    "v_mfma_f32_16x16x32_bf16 v[72:75], v[248:251], v[108:111], v[72:75]\n"
    "buffer_load_dwordx4 v[248:251], v0, s[40:43], s50 offen offset:2048\n"
    "s_waitcnt vmcnt(31)\n"
    "v_mfma_f32_16x16x32_bf16 v[36:39], v[252:255], v[104:107], v[36:39]\n"
    "v_mfma_f32_16x16x32_bf16 v[76:79], v[252:255], v[108:111], v[76:79]\n"
    "buffer_load_dwordx4 v[252:255], v0, s[40:43], s50 offen offset:3072\n"
    "ds_read_b128 v[100:103], v9 offset:32768\n"
    "s_waitcnt vmcnt(31)\n"
    "v_mfma_f32_16x16x32_bf16 v[40:43], v[128:131], v[104:107], v[40:43]\n"
    "v_mfma_f32_16x16x32_bf16 v[80:83], v[128:131], v[108:111], v[80:83]\n"
    "s_add_u32 s50, s50, 4096\n"
    "buffer_load_dwordx4 v[128:131], v0, s[40:43], s50 offen\n"
    "s_waitcnt vmcnt(31)\n"
    "v_mfma_f32_16x16x32_bf16 v[44:47], v[132:135], v[104:107], v[44:47]\n"
    "v_mfma_f32_16x16x32_bf16 v[84:87], v[132:135], v[108:111], v[84:87]\n"
    "buffer_load_dwordx4 v[132:135], v0, s[40:43], s50 offen offset:1024\n"
    "s_waitcnt vmcnt(31)\n"
    "v_mfma_f32_16x16x32_bf16 v[48:51], v[136:139], v[104:107], v[48:51]\n"
    "v_mfma_f32_16x16x32_bf16 v[88:91], v[136:139], v[108:111], v[88:91]\n"
    "buffer_load_dwordx4 v[136:139], v0, s[40:43], s50 offen offset:2048\n"
    "s_waitcnt vmcnt(31)\n"
    "v_mfma_f32_16x16x32_bf16 v[52:55], v[140:143], v[104:107], v[52:55]\n"
    "v_mfma_f32_16x16x32_bf16 v[92:95], v[140:143], v[108:111], v[92:95]\n"
    "buffer_load_dwordx4 v[140:143], v0, s[40:43], s50 offen offset:3072\n"
    "s_waitcnt vmcnt(31) lgkmcnt(5)\n"
    "v_mfma_f32_16x16x32_bf16 v[16:19], v[144:147], v[112:115], v[16:19]\n"
    "s_waitcnt lgkmcnt(4)\n"
    "v_mfma_f32_16x16x32_bf16 v[56:59], v[144:147], v[116:119], v[56:59]\n"
    "s_add_u32 s50, s50, 4096\n"
    "buffer_load_dwordx4 v[144:147], v0, s[40:43], s50 offen\n"
    "ds_read_b128 v[104:107], v9 offset:13312\n"
    "s_waitcnt vmcnt(31)\n"
    "v_mfma_f32_16x16x32_bf16 v[20:23], v[148:151], v[112:115], v[20:23]\n"
    "v_mfma_f32_16x16x32_bf16 v[60:63], v[148:151], v[116:119], v[60:63]\n"
    "buffer_load_dwordx4 v[148:151], v0, s[40:43], s50 offen offset:1024\n"
    "s_waitcnt vmcnt(31)\n"
    "v_mfma_f32_16x16x32_bf16 v[24:27], v[152:155], v[112:115], v[24:27]\n"
    "v_mfma_f32_16x16x32_bf16 v[64:67], v[152:155], v[116:119], v[64:67]\n"
    "buffer_load_dwordx4 v[152:155], v0, s[40:43], s50 offen offset:2048\n"
    "s_waitcnt vmcnt(31)\n"
    "v_mfma_f32_16x16x32_bf16 v[28:31], v[156:159], v[112:115], v[28:31]\n"
    "v_mfma_f32_16x16x32_bf16 v[68:71], v[156:159], v[116:119], v[68:71]\n"
    "buffer_load_dwordx4 v[156:159], v0, s[40:43], s50 offen offset:3072\n"
    "s_waitcnt vmcnt(31)\n"
    "v_mfma_f32_16x16x32_bf16 v[32:35], v[160:163], v[112:115], v[32:35]\n"
    "v_mfma_f32_16x16x32_bf16 v[72:75], v[160:163], v[116:119], v[72:75]\n"
    "s_add_u32 s50, s50, 4096\n"
    "buffer_load_dwordx4 v[160:163], v0, s[40:43], s50 offen\n"
    "s_waitcnt vmcnt(31)\n"
    "v_mfma_f32_16x16x32_bf16 v[36:39], v[164:167], v[112:115], v[36:39]\n"
    "v_mfma_f32_16x16x32_bf16 v[76:79], v[164:167], v[116:119], v[76:79]\n"
    "buffer_load_dwordx4 v[164:167], v0, s[40:43], s50 offen offset:1024\n"
    "ds_read_b128 v[108:111], v9 offset:33792\n"
    "s_waitcnt vmcnt(31)\n"
    "v_mfma_f32_16x16x32_bf16 v[40:43], v[168:171], v[112:115], v[40:43]\n"
    "v_mfma_f32_16x16x32_bf16 v[80:83], v[168:171], v[116:119], v[80:83]\n"
    "buffer_load_dwordx4 v[168:171], v0, s[40:43], s50 offen offset:2048\n"
    "s_waitcnt vmcnt(31)\n"
    "v_mfma_f32_16x16x32_bf16 v[44:47], v[172:175], v[112:115], v[44:47]\n"
    "v_mfma_f32_16x16x32_bf16 v[84:87], v[172:175], v[116:119], v[84:87]\n"
    "buffer_load_dwordx4 v[172:175], v0, s[40:43], s50 offen offset:3072\n"
    "s_waitcnt vmcnt(31)\n"
    "v_mfma_f32_16x16x32_bf16 v[48:51], v[176:179], v[112:115], v[48:51]\n"
    "v_mfma_f32_16x16x32_bf16 v[88:91], v[176:179], v[116:119], v[88:91]\n"
    "s_add_u32 s50, s50, 4096\n"
    "buffer_load_dwordx4 v[176:179], v0, s[40:43], s50 offen\n"
    "s_waitcnt vmcnt(31)\n"
    "v_mfma_f32_16x16x32_bf16 v[52:55], v[180:183], v[112:115], v[52:55]\n"
    "v_mfma_f32_16x16x32_bf16 v[92:95], v[180:183], v[116:119], v[92:95]\n"
    "buffer_load_dwordx4 v[180:183], v0, s[40:43], s50 offen offset:1024\n"
    "s_waitcnt vmcnt(31) lgkmcnt(5)\n"
    "v_mfma_f32_16x16x32_bf16 v[16:19], v[184:187], v[120:123], v[16:19]\n"
    "s_waitcnt lgkmcnt(4)\n"
    "v_mfma_f32_16x16x32_bf16 v[56:59], v[184:187], v[124:127], v[56:59]\n"
    "buffer_load_dwordx4 v[184:187], v0, s[40:43], s50 offen offset:2048\n"
    "ds_read_b128 v[112:115], v9 offset:14336\n"
    "s_waitcnt vmcnt(31)\n"
    "v_mfma_f32_16x16x32_bf16 v[20:23], v[188:191], v[120:123], v[20:23]\n"
    "v_mfma_f32_16x16x32_bf16 v[60:63], v[188:191], v[124:127], v[60:63]\n"
    "buffer_load_dwordx4 v[188:191], v0, s[40:43], s50 offen offset:3072\n"
    "s_waitcnt vmcnt(31)\n"
    "v_mfma_f32_16x16x32_bf16 v[24:27], v[192:195], v[120:123], v[24:27]\n"
    "v_mfma_f32_16x16x32_bf16 v[64:67], v[192:195], v[124:127], v[64:67]\n"
    "s_add_u32 s50, s50, 4096\n"
    "buffer_load_dwordx4 v[192:195], v0, s[40:43], s50 offen\n"
    "s_waitcnt vmcnt(31)\n"
    "v_mfma_f32_16x16x32_bf16 v[28:31], v[196:199], v[120:123], v[28:31]\n"
    "v_mfma_f32_16x16x32_bf16 v[68:71], v[196:199], v[124:127], v[68:71]\n"
    "buffer_load_dwordx4 v[196:199], v0, s[40:43], s50 offen offset:1024\n"
    "s_waitcnt vmcnt(31)\n"
    "v_mfma_f32_16x16x32_bf16 v[32:35], v[200:203], v[120:123], v[32:35]\n"
    "v_mfma_f32_16x16x32_bf16 v[72:75], v[200:203], v[124:127], v[72:75]\n"
    "buffer_load_dwordx4 v[200:203], v0, s[40:43], s50 offen offset:2048\n"
    "s_waitcnt vmcnt(31)\n"
    "v_mfma_f32_16x16x32_bf16 v[36:39], v[204:207], v[120:123], v[36:39]\n"
    "v_mfma_f32_16x16x32_bf16 v[76:79], v[204:207], v[124:127], v[76:79]\n"
    "buffer_load_dwordx4 v[204:207], v0, s[40:43], s50 offen offset:3072\n"
    "ds_read_b128 v[116:119], v9 offset:34816\n"
    "s_waitcnt vmcnt(31)\n"
    "v_mfma_f32_16x16x32_bf16 v[40:43], v[208:211], v[120:123], v[40:43]\n"
    "v_mfma_f32_16x16x32_bf16 v[80:83], v[208:211], v[124:127], v[80:83]\n"
    "s_add_u32 s50, s50, 4096\n"
    "buffer_load_dwordx4 v[208:211], v0, s[40:43], s50 offen\n"
    "s_waitcnt vmcnt(31)\n"
    "v_mfma_f32_16x16x32_bf16 v[44:47], v[212:215], v[120:123], v[44:47]\n"
    "v_mfma_f32_16x16x32_bf16 v[84:87], v[212:215], v[124:127], v[84:87]\n"
    "buffer_load_dwordx4 v[212:215], v0, s[40:43], s50 offen offset:1024\n"
    "s_waitcnt vmcnt(31)\n"
    "v_mfma_f32_16x16x32_bf16 v[48:51], v[216:219], v[120:123], v[48:51]\n"
    "v_mfma_f32_16x16x32_bf16 v[88:91], v[216:219], v[124:127], v[88:91]\n"
    "buffer_load_dwordx4 v[216:219], v0, s[40:43], s50 offen offset:2048\n"
    "s_waitcnt vmcnt(31)\n"
    "v_mfma_f32_16x16x32_bf16 v[52:55], v[220:223], v[120:123], v[52:55]\n"
    "v_mfma_f32_16x16x32_bf16 v[92:95], v[220:223], v[124:127], v[92:95]\n"
    "buffer_load_dwordx4 v[220:223], v0, s[40:43], s50 offen offset:3072\n"
    "s_waitcnt vmcnt(31) lgkmcnt(5)\n"
    "v_mfma_f32_16x16x32_bf16 v[16:19], v[224:227], v[96:99], v[16:19]\n"
    "s_waitcnt lgkmcnt(4)\n"
    "v_mfma_f32_16x16x32_bf16 v[56:59], v[224:227], v[100:103], v[56:59]\n"
    "s_add_u32 s50, s50, 4096\n"
    "buffer_load_dwordx4 v[224:227], v0, s[40:43], s50 offen\n"
    "ds_read_b128 v[120:123], v9 offset:15360\n"
    "s_waitcnt vmcnt(31)\n"
    "v_mfma_f32_16x16x32_bf16 v[20:23], v[228:231], v[96:99], v[20:23]\n"
    "v_mfma_f32_16x16x32_bf16 v[60:63], v[228:231], v[100:103], v[60:63]\n"
    "buffer_load_dwordx4 v[228:231], v0, s[40:43], s50 offen offset:1024\n"
    "s_waitcnt vmcnt(31)\n"
    "v_mfma_f32_16x16x32_bf16 v[24:27], v[232:235], v[96:99], v[24:27]\n"
    "v_mfma_f32_16x16x32_bf16 v[64:67], v[232:235], v[100:103], v[64:67]\n"
    "buffer_load_dwordx4 v[232:235], v0, s[40:43], s50 offen offset:2048\n"
    "s_waitcnt vmcnt(31)\n"
    "v_mfma_f32_16x16x32_bf16 v[28:31], v[236:239], v[96:99], v[28:31]\n"
    "v_mfma_f32_16x16x32_bf16 v[68:71], v[236:239], v[100:103], v[68:71]\n"
    "buffer_load_dwordx4 v[236:239], v0, s[40:43], s50 offen offset:3072\n"
    "s_waitcnt vmcnt(31)\n"
    "v_mfma_f32_16x16x32_bf16 v[32:35], v[240:243], v[96:99], v[32:35]\n"
    "v_mfma_f32_16x16x32_bf16 v[72:75], v[240:243], v[100:103], v[72:75]\n"
    "s_add_u32 s50, s50, 4096\n"
    "buffer_load_dwordx4 v[240:243], v0, s[40:43], s50 offen\n"
    "s_waitcnt vmcnt(31)\n"
    "v_mfma_f32_16x16x32_bf16 v[36:39], v[244:247], v[96:99], v[36:39]\n"
    "v_mfma_f32_16x16x32_bf16 v[76:79], v[244:247], v[100:103], v[76:79]\n"
    "buffer_load_dwordx4 v[244:247], v0, s[40:43], s50 offen offset:1024\n"
    "ds_read_b128 v[124:127], v9 offset:35840\n"
    "s_waitcnt vmcnt(31)\n"
    "v_mfma_f32_16x16x32_bf16 v[40:43], v[248:251], v[96:99], v[40:43]\n"
    "v_mfma_f32_16x16x32_bf16 v[80:83], v[248:251], v[100:103], v[80:83]\n"
    "buffer_load_dwordx4 v[248:251], v0, s[40:43], s50 offen offset:2048\n"
    "s_waitcnt vmcnt(31)\n"
    "v_mfma_f32_16x16x32_bf16 v[44:47], v[252:255], v[96:99], v[44:47]\n"
    "v_mfma_f32_16x16x32_bf16 v[84:87], v[252:255], v[100:103], v[84:87]\n"
    "buffer_load_dwordx4 v[252:255], v0, s[40:43], s50 offen offset:3072\n"
    "s_waitcnt vmcnt(31)\n"
    "v_mfma_f32_16x16x32_bf16 v[48:51], v[128:131], v[96:99], v[48:51]\n"
    "v_mfma_f32_16x16x32_bf16 v[88:91], v[128:131], v[100:103], v[88:91]\n"
    "s_add_u32 s50, s50, 4096\n"
    "buffer_load_dwordx4 v[128:131], v0, s[40:43], s50 offen\n"
    "s_waitcnt vmcnt(31)\n"
    "v_mfma_f32_16x16x32_bf16 v[52:55], v[132:135], v[96:99], v[52:55]\n"
    "v_mfma_f32_16x16x32_bf16 v[92:95], v[132:135], v[100:103], v[92:95]\n"
    "buffer_load_dwordx4 v[132:135], v0, s[40:43], s50 offen offset:1024\n"
    "s_waitcnt vmcnt(31) lgkmcnt(5)\n"
    "v_mfma_f32_16x16x32_bf16 v[16:19], v[136:139], v[104:107], v[16:19]\n"
    "s_waitcnt lgkmcnt(4)\n"
    "v_mfma_f32_16x16x32_bf16 v[56:59], v[136:139], v[108:111], v[56:59]\n"
    "buffer_load_dwordx4 v[136:139], v0, s[40:43], s50 offen offset:2048\n"
    "ds_read_b128 v[96:99], v9 offset:16384\n"
    "s_waitcnt vmcnt(31)\n"
    "v_mfma_f32_16x16x32_bf16 v[20:23], v[140:143], v[104:107], v[20:23]\n"
    "v_mfma_f32_16x16x32_bf16 v[60:63], v[140:143], v[108:111], v[60:63]\n"
    "buffer_load_dwordx4 v[140:143], v0, s[40:43], s50 offen offset:3072\n"
    "s_waitcnt vmcnt(31)\n"
    "v_mfma_f32_16x16x32_bf16 v[24:27], v[144:147], v[104:107], v[24:27]\n"
    "v_mfma_f32_16x16x32_bf16 v[64:67], v[144:147], v[108:111], v[64:67]\n"
    "s_add_u32 s50, s50, 4096\n"
    "buffer_load_dwordx4 v[144:147], v0, s[40:43], s50 offen\n"
    "s_waitcnt vmcnt(31)\n"
    "v_mfma_f32_16x16x32_bf16 v[28:31], v[148:151], v[104:107], v[28:31]\n"
    "v_mfma_f32_16x16x32_bf16 v[68:71], v[148:151], v[108:111], v[68:71]\n"
    "buffer_load_dwordx4 v[148:151], v0, s[40:43], s50 offen offset:1024\n"
    "s_waitcnt vmcnt(31)\n"
    "v_mfma_f32_16x16x32_bf16 v[32:35], v[152:155], v[104:107], v[32:35]\n"
    "v_mfma_f32_16x16x32_bf16 v[72:75], v[152:155], v[108:111], v[72:75]\n"
    "buffer_load_dwordx4 v[152:155], v0, s[40:43], s50 offen offset:2048\n"
    "s_waitcnt vmcnt(31)\n"
    "v_mfma_f32_16x16x32_bf16 v[36:39], v[156:159], v[104:107], v[36:39]\n"
    "v_mfma_f32_16x16x32_bf16 v[76:79], v[156:159], v[108:111], v[76:79]\n"
    "buffer_load_dwordx4 v[156:159], v0, s[40:43], s50 offen offset:3072\n"
    "ds_read_b128 v[100:103], v9 offset:36864\n"
    "s_waitcnt vmcnt(31)\n"
    "v_mfma_f32_16x16x32_bf16 v[40:43], v[160:163], v[104:107], v[40:43]\n"
    "v_mfma_f32_16x16x32_bf16 v[80:83], v[160:163], v[108:111], v[80:83]\n"
    "s_add_u32 s50, s50, 4096\n"
    "buffer_load_dwordx4 v[160:163], v0, s[40:43], s50 offen\n"
    "s_waitcnt vmcnt(31)\n"
    "v_mfma_f32_16x16x32_bf16 v[44:47], v[164:167], v[104:107], v[44:47]\n"
    "v_mfma_f32_16x16x32_bf16 v[84:87], v[164:167], v[108:111], v[84:87]\n"
    "buffer_load_dwordx4 v[164:167], v0, s[40:43], s50 offen offset:1024\n"
    "s_waitcnt vmcnt(31)\n"
    "v_mfma_f32_16x16x32_bf16 v[48:51], v[168:171], v[104:107], v[48:51]\n"
    "v_mfma_f32_16x16x32_bf16 v[88:91], v[168:171], v[108:111], v[88:91]\n"
    "buffer_load_dwordx4 v[168:171], v0, s[40:43], s50 offen offset:2048\n"
    "s_waitcnt vmcnt(31)\n"
    "v_mfma_f32_16x16x32_bf16 v[52:55], v[172:175], v[104:107], v[52:55]\n"
    "v_mfma_f32_16x16x32_bf16 v[92:95], v[172:175], v[108:111], v[92:95]\n"
    "buffer_load_dwordx4 v[172:175], v0, s[40:43], s50 offen offset:3072\n"
    "s_waitcnt vmcnt(31) lgkmcnt(5)\n"
    "v_mfma_f32_16x16x32_bf16 v[16:19], v[176:179], v[112:115], v[16:19]\n"
    "s_waitcnt lgkmcnt(4)\n"
    "v_mfma_f32_16x16x32_bf16 v[56:59], v[176:179], v[116:119], v[56:59]\n"
    "s_add_u32 s50, s50, 4096\n"
    "buffer_load_dwordx4 v[176:179], v0, s[40:43], s50 offen\n"
    "ds_read_b128 v[104:107], v9 offset:17408\n"
    "s_waitcnt vmcnt(31)\n"
    "v_mfma_f32_16x16x32_bf16 v[20:23], v[180:183], v[112:115], v[20:23]\n"
    "v_mfma_f32_16x16x32_bf16 v[60:63], v[180:183], v[116:119], v[60:63]\n"
    "buffer_load_dwordx4 v[180:183], v0, s[40:43], s50 offen offset:1024\n"
    "s_waitcnt vmcnt(31)\n"
    "v_mfma_f32_16x16x32_bf16 v[24:27], v[184:187], v[112:115], v[24:27]\n"
    "v_mfma_f32_16x16x32_bf16 v[64:67], v[184:187], v[116:119], v[64:67]\n"
    "buffer_load_dwordx4 v[184:187], v0, s[40:43], s50 offen offset:2048\n"
    "s_waitcnt vmcnt(31)\n"
    "v_mfma_f32_16x16x32_bf16 v[28:31], v[188:191], v[112:115], v[28:31]\n"
    "v_mfma_f32_16x16x32_bf16 v[68:71], v[188:191], v[116:119], v[68:71]\n"
    "buffer_load_dwordx4 v[188:191], v0, s[40:43], s50 offen offset:3072\n"
    "s_waitcnt vmcnt(31)\n"
    "v_mfma_f32_16x16x32_bf16 v[32:35], v[192:195], v[112:115], v[32:35]\n"
    "v_mfma_f32_16x16x32_bf16 v[72:75], v[192:195], v[116:119], v[72:75]\n"
    "s_add_u32 s50, s50, 4096\n"
    "buffer_load_dwordx4 v[192:195], v0, s[40:43], s50 offen\n"
    "s_waitcnt vmcnt(31)\n"
    "v_mfma_f32_16x16x32_bf16 v[36:39], v[196:199], v[112:115], v[36:39]\n"
    "v_mfma_f32_16x16x32_bf16 v[76:79], v[196:199], v[116:119], v[76:79]\n"
    "buffer_load_dwordx4 v[196:199], v0, s[40:43], s50 offen offset:1024\n"
    "ds_read_b128 v[108:111], v9 offset:37888\n"
    "s_waitcnt vmcnt(31)\n"
    "v_mfma_f32_16x16x32_bf16 v[40:43], v[200:203], v[112:115], v[40:43]\n"
    "v_mfma_f32_16x16x32_bf16 v[80:83], v[200:203], v[116:119], v[80:83]\n"
    "buffer_load_dwordx4 v[200:203], v0, s[40:43], s50 offen offset:2048\n"
    "s_waitcnt vmcnt(31)\n"
    "v_mfma_f32_16x16x32_bf16 v[44:47], v[204:207], v[112:115], v[44:47]\n"
    "v_mfma_f32_16x16x32_bf16 v[84:87], v[204:207], v[116:119], v[84:87]\n"
    "buffer_load_dwordx4 v[204:207], v0, s[40:43], s50 offen offset:3072\n"
    "s_waitcnt vmcnt(31)\n"
    "v_mfma_f32_16x16x32_bf16 v[48:51], v[208:211], v[112:115], v[48:51]\n"
    "v_mfma_f32_16x16x32_bf16 v[88:91], v[208:211], v[116:119], v[88:91]\n"
    "s_add_u32 s50, s50, 4096\n"
    "buffer_load_dwordx4 v[208:211], v0, s[40:43], s50 offen\n"
    "s_waitcnt vmcnt(31)\n"
    "v_mfma_f32_16x16x32_bf16 v[52:55], v[212:215], v[112:115], v[52:55]\n"
    "v_mfma_f32_16x16x32_bf16 v[92:95], v[212:215], v[116:119], v[92:95]\n"
    "buffer_load_dwordx4 v[212:215], v0, s[40:43], s50 offen offset:1024\n"
    "s_waitcnt vmcnt(31) lgkmcnt(5)\n"
    "v_mfma_f32_16x16x32_bf16 v[16:19], v[216:219], v[120:123], v[16:19]\n"
    "s_waitcnt lgkmcnt(4)\n"
    "v_mfma_f32_16x16x32_bf16 v[56:59], v[216:219], v[124:127], v[56:59]\n"
    "buffer_load_dwordx4 v[216:219], v0, s[40:43], s50 offen offset:2048\n"
    "ds_read_b128 v[112:115], v9 offset:18432\n"
    "s_waitcnt vmcnt(31)\n"
    "v_mfma_f32_16x16x32_bf16 v[20:23], v[220:223], v[120:123], v[20:23]\n"
    "v_mfma_f32_16x16x32_bf16 v[60:63], v[220:223], v[124:127], v[60:63]\n"
    "buffer_load_dwordx4 v[220:223], v0, s[40:43], s50 offen offset:3072\n"
    "s_waitcnt vmcnt(31)\n"
    "v_mfma_f32_16x16x32_bf16 v[24:27], v[224:227], v[120:123], v[24:27]\n"
    "v_mfma_f32_16x16x32_bf16 v[64:67], v[224:227], v[124:127], v[64:67]\n"
    "s_add_u32 s50, s50, 4096\n"
    "buffer_load_dwordx4 v[224:227], v0, s[40:43], s50 offen\n"
    "s_waitcnt vmcnt(31)\n"
    "v_mfma_f32_16x16x32_bf16 v[28:31], v[228:231], v[120:123], v[28:31]\n"
    "v_mfma_f32_16x16x32_bf16 v[68:71], v[228:231], v[124:127], v[68:71]\n"
    "buffer_load_dwordx4 v[228:231], v0, s[40:43], s50 offen offset:1024\n"
    "s_waitcnt vmcnt(31)\n"
    "v_mfma_f32_16x16x32_bf16 v[32:35], v[232:235], v[120:123], v[32:35]\n"
    "v_mfma_f32_16x16x32_bf16 v[72:75], v[232:235], v[124:127], v[72:75]\n"
    "buffer_load_dwordx4 v[232:235], v0, s[40:43], s50 offen offset:2048\n"
    "s_waitcnt vmcnt(31)\n"
    "v_mfma_f32_16x16x32_bf16 v[36:39], v[236:239], v[120:123], v[36:39]\n"
    "v_mfma_f32_16x16x32_bf16 v[76:79], v[236:239], v[124:127], v[76:79]\n"
    "buffer_load_dwordx4 v[236:239], v0, s[40:43], s50 offen offset:3072\n"
    "ds_read_b128 v[116:119], v9 offset:38912\n"
    "s_waitcnt vmcnt(31)\n"
    "v_mfma_f32_16x16x32_bf16 v[40:43], v[240:243], v[120:123], v[40:43]\n"
    "v_mfma_f32_16x16x32_bf16 v[80:83], v[240:243], v[124:127], v[80:83]\n"
    "s_add_u32 s50, s50, 4096\n"
    "buffer_load_dwordx4 v[240:243], v0, s[40:43], s50 offen\n"
    "s_waitcnt vmcnt(31)\n"
    "v_mfma_f32_16x16x32_bf16 v[44:47], v[244:247], v[120:123], v[44:47]\n"
    "v_mfma_f32_16x16x32_bf16 v[84:87], v[244:247], v[124:127], v[84:87]\n"
    "buffer_load_dwordx4 v[244:247], v0, s[40:43], s50 offen offset:1024\n"
    "s_waitcnt vmcnt(31)\n"
    "v_mfma_f32_16x16x32_bf16 v[48:51], v[248:251], v[120:123], v[48:51]\n"
    "v_mfma_f32_16x16x32_bf16 v[88:91], v[248:251], v[124:127], v[88:91]\n"
    "buffer_load_dwordx4 v[248:251], v0, s[40:43], s50 offen offset:2048\n"
    "s_waitcnt vmcnt(31)\n"
    "v_mfma_f32_16x16x32_bf16 v[52:55], v[252:255], v[120:123], v[52:55]\n"
    "v_mfma_f32_16x16x32_bf16 v[92:95], v[252:255], v[124:127], v[92:95]\n"
    "buffer_load_dwordx4 v[252:255], v0, s[40:43], s50 offen offset:3072\n"
    "s_waitcnt vmcnt(31) lgkmcnt(5)\n"
    "v_mfma_f32_16x16x32_bf16 v[16:19], v[128:131], v[96:99], v[16:19]\n"
    "s_waitcnt lgkmcnt(4)\n"
    "v_mfma_f32_16x16x32_bf16 v[56:59], v[128:131], v[100:103], v[56:59]\n"
    "s_add_u32 s50, s50, 4096\n"
    "buffer_load_dwordx4 v[128:131], v0, s[40:43], s50 offen\n"
    "ds_read_b128 v[120:123], v9 offset:19456\n"
    "s_waitcnt vmcnt(31)\n"
    "v_mfma_f32_16x16x32_bf16 v[20:23], v[132:135], v[96:99], v[20:23]\n"
    "v_mfma_f32_16x16x32_bf16 v[60:63], v[132:135], v[100:103], v[60:63]\n"
    "buffer_load_dwordx4 v[132:135], v0, s[40:43], s50 offen offset:1024\n"
    "s_waitcnt vmcnt(31)\n"
    "v_mfma_f32_16x16x32_bf16 v[24:27], v[136:139], v[96:99], v[24:27]\n"
    "v_mfma_f32_16x16x32_bf16 v[64:67], v[136:139], v[100:103], v[64:67]\n"
    "buffer_load_dwordx4 v[136:139], v0, s[40:43], s50 offen offset:2048\n"
    "s_waitcnt vmcnt(31)\n"
    "v_mfma_f32_16x16x32_bf16 v[28:31], v[140:143], v[96:99], v[28:31]\n"
    "v_mfma_f32_16x16x32_bf16 v[68:71], v[140:143], v[100:103], v[68:71]\n"
    "buffer_load_dwordx4 v[140:143], v0, s[40:43], s50 offen offset:3072\n"
    "s_waitcnt vmcnt(31)\n"
    "v_mfma_f32_16x16x32_bf16 v[32:35], v[144:147], v[96:99], v[32:35]\n"
    "v_mfma_f32_16x16x32_bf16 v[72:75], v[144:147], v[100:103], v[72:75]\n"
    "s_add_u32 s50, s50, 4096\n"
    "buffer_load_dwordx4 v[144:147], v0, s[40:43], s50 offen\n"
    "s_waitcnt vmcnt(31)\n"
    "v_mfma_f32_16x16x32_bf16 v[36:39], v[148:151], v[96:99], v[36:39]\n"
    "v_mfma_f32_16x16x32_bf16 v[76:79], v[148:151], v[100:103], v[76:79]\n"
    "buffer_load_dwordx4 v[148:151], v0, s[40:43], s50 offen offset:1024\n"
    "ds_read_b128 v[124:127], v9 offset:39936\n"
    "s_waitcnt vmcnt(31)\n"
    "v_mfma_f32_16x16x32_bf16 v[40:43], v[152:155], v[96:99], v[40:43]\n"
    "v_mfma_f32_16x16x32_bf16 v[80:83], v[152:155], v[100:103], v[80:83]\n"
    "buffer_load_dwordx4 v[152:155], v0, s[40:43], s50 offen offset:2048\n"
    "s_waitcnt vmcnt(31)\n"
    "v_mfma_f32_16x16x32_bf16 v[44:47], v[156:159], v[96:99], v[44:47]\n"
    "v_mfma_f32_16x16x32_bf16 v[84:87], v[156:159], v[100:103], v[84:87]\n"
    "buffer_load_dwordx4 v[156:159], v0, s[40:43], s50 offen offset:3072\n"
    "s_waitcnt vmcnt(31)\n"
    "v_mfma_f32_16x16x32_bf16 v[48:51], v[160:163], v[96:99], v[48:51]\n"
    "v_mfma_f32_16x16x32_bf16 v[88:91], v[160:163], v[100:103], v[88:91]\n"
    "s_add_u32 s48, s28, 237445120\n"
    "buffer_load_dwordx4 v[160:163], v8, s[40:43], s48 offen sc1\n"
    "s_waitcnt vmcnt(31)\n"
    "v_mfma_f32_16x16x32_bf16 v[52:55], v[164:167], v[96:99], v[52:55]\n"
    "v_mfma_f32_16x16x32_bf16 v[92:95], v[164:167], v[100:103], v[92:95]\n"
    "s_add_u32 s48, s28, 237445120\n"
    "buffer_load_dwordx4 v[164:167], v8, s[40:43], s48 offen offset:512 sc1\n"
    "s_waitcnt vmcnt(31) lgkmcnt(5)\n"
    "v_mfma_f32_16x16x32_bf16 v[16:19], v[168:171], v[104:107], v[16:19]\n"
    "s_waitcnt lgkmcnt(4)\n"
    "v_mfma_f32_16x16x32_bf16 v[56:59], v[168:171], v[108:111], v[56:59]\n"
    "s_add_u32 s48, s28, 237477888\n"
    "buffer_load_dwordx4 v[168:171], v8, s[40:43], s48 offen sc1\n"
    "s_waitcnt vmcnt(31)\n"
    "v_mfma_f32_16x16x32_bf16 v[20:23], v[172:175], v[104:107], v[20:23]\n"
    "v_mfma_f32_16x16x32_bf16 v[60:63], v[172:175], v[108:111], v[60:63]\n"
    "s_add_u32 s48, s28, 237477888\n"
    "buffer_load_dwordx4 v[172:175], v8, s[40:43], s48 offen offset:512 sc1\n"
    "s_waitcnt vmcnt(31)\n"
    "v_mfma_f32_16x16x32_bf16 v[24:27], v[176:179], v[104:107], v[24:27]\n"
    "v_mfma_f32_16x16x32_bf16 v[64:67], v[176:179], v[108:111], v[64:67]\n"
    "s_add_u32 s48, s28, 237479936\n"
    "buffer_load_dwordx4 v[176:179], v8, s[40:43], s48 offen sc1\n"
    "s_waitcnt vmcnt(31)\n"
    "v_mfma_f32_16x16x32_bf16 v[28:31], v[180:183], v[104:107], v[28:31]\n"
    "v_mfma_f32_16x16x32_bf16 v[68:71], v[180:183], v[108:111], v[68:71]\n"
    "s_add_u32 s48, s28, 237479936\n"
    "buffer_load_dwordx4 v[180:183], v8, s[40:43], s48 offen offset:512 sc1\n"
    "s_waitcnt vmcnt(31)\n"
    "v_mfma_f32_16x16x32_bf16 v[32:35], v[184:187], v[104:107], v[32:35]\n"
    "v_mfma_f32_16x16x32_bf16 v[72:75], v[184:187], v[108:111], v[72:75]\n"
    "s_add_u32 s48, s28, 237481984\n"
    "buffer_load_dwordx4 v[184:187], v8, s[40:43], s48 offen sc1\n"
    "s_waitcnt vmcnt(31)\n"
    "v_mfma_f32_16x16x32_bf16 v[36:39], v[188:191], v[104:107], v[36:39]\n"
    "v_mfma_f32_16x16x32_bf16 v[76:79], v[188:191], v[108:111], v[76:79]\n"
    "s_add_u32 s48, s28, 237481984\n"
    "buffer_load_dwordx4 v[188:191], v8, s[40:43], s48 offen offset:512 sc1\n"
    "s_waitcnt vmcnt(31)\n"
    "v_mfma_f32_16x16x32_bf16 v[40:43], v[192:195], v[104:107], v[40:43]\n"
    "v_mfma_f32_16x16x32_bf16 v[80:83], v[192:195], v[108:111], v[80:83]\n"
    "s_add_u32 s48, s28, 237484032\n"
    "buffer_load_dwordx4 v[192:195], v8, s[40:43], s48 offen sc1\n"
    "s_waitcnt vmcnt(31)\n"
    "v_mfma_f32_16x16x32_bf16 v[44:47], v[196:199], v[104:107], v[44:47]\n"
    "v_mfma_f32_16x16x32_bf16 v[84:87], v[196:199], v[108:111], v[84:87]\n"
    "s_add_u32 s48, s28, 237484032\n"
    "buffer_load_dwordx4 v[196:199], v8, s[40:43], s48 offen offset:512 sc1\n"
    "s_waitcnt vmcnt(31)\n"
    "v_mfma_f32_16x16x32_bf16 v[48:51], v[200:203], v[104:107], v[48:51]\n"
    "v_mfma_f32_16x16x32_bf16 v[88:91], v[200:203], v[108:111], v[88:91]\n"
    "s_add_u32 s48, s28, 237486080\n"
    "buffer_load_dwordx4 v[200:203], v8, s[40:43], s48 offen sc1\n"
    "s_waitcnt vmcnt(31)\n"
    "v_mfma_f32_16x16x32_bf16 v[52:55], v[204:207], v[104:107], v[52:55]\n"
    "v_mfma_f32_16x16x32_bf16 v[92:95], v[204:207], v[108:111], v[92:95]\n"
    "s_add_u32 s48, s28, 237486080\n"
    "buffer_load_dwordx4 v[204:207], v8, s[40:43], s48 offen offset:512 sc1\n"
    "s_waitcnt vmcnt(31) lgkmcnt(3)\n"
    "v_mfma_f32_16x16x32_bf16 v[16:19], v[208:211], v[112:115], v[16:19]\n"
    "s_waitcnt lgkmcnt(2)\n"
    "v_mfma_f32_16x16x32_bf16 v[56:59], v[208:211], v[116:119], v[56:59]\n"
    "s_waitcnt vmcnt(30)\n"
    "v_mfma_f32_16x16x32_bf16 v[20:23], v[212:215], v[112:115], v[20:23]\n"
    "v_mfma_f32_16x16x32_bf16 v[60:63], v[212:215], v[116:119], v[60:63]\n"
    "s_waitcnt vmcnt(29)\n"
    "v_mfma_f32_16x16x32_bf16 v[24:27], v[216:219], v[112:115], v[24:27]\n"
    "v_mfma_f32_16x16x32_bf16 v[64:67], v[216:219], v[116:119], v[64:67]\n"
    "s_waitcnt vmcnt(28)\n"
    "v_mfma_f32_16x16x32_bf16 v[28:31], v[220:223], v[112:115], v[28:31]\n"
    "v_mfma_f32_16x16x32_bf16 v[68:71], v[220:223], v[116:119], v[68:71]\n"
    "s_waitcnt vmcnt(27)\n"
    "v_mfma_f32_16x16x32_bf16 v[32:35], v[224:227], v[112:115], v[32:35]\n"
    "v_mfma_f32_16x16x32_bf16 v[72:75], v[224:227], v[116:119], v[72:75]\n"
    "s_waitcnt vmcnt(26)\n"
    "v_mfma_f32_16x16x32_bf16 v[36:39], v[228:231], v[112:115], v[36:39]\n"
    "v_mfma_f32_16x16x32_bf16 v[76:79], v[228:231], v[116:119], v[76:79]\n"
    "s_waitcnt vmcnt(25)\n"
    "v_mfma_f32_16x16x32_bf16 v[40:43], v[232:235], v[112:115], v[40:43]\n"
    "v_mfma_f32_16x16x32_bf16 v[80:83], v[232:235], v[116:119], v[80:83]\n"
    "s_waitcnt vmcnt(24)\n"
    "v_mfma_f32_16x16x32_bf16 v[44:47], v[236:239], v[112:115], v[44:47]\n"
    "v_mfma_f32_16x16x32_bf16 v[84:87], v[236:239], v[116:119], v[84:87]\n"
    "s_waitcnt vmcnt(23)\n"
    "v_mfma_f32_16x16x32_bf16 v[48:51], v[240:243], v[112:115], v[48:51]\n"
    "v_mfma_f32_16x16x32_bf16 v[88:91], v[240:243], v[116:119], v[88:91]\n"
    "s_waitcnt vmcnt(22)\n"
    "v_mfma_f32_16x16x32_bf16 v[52:55], v[244:247], v[112:115], v[52:55]\n"
    "v_mfma_f32_16x16x32_bf16 v[92:95], v[244:247], v[116:119], v[92:95]\n"
    "s_waitcnt vmcnt(21) lgkmcnt(1)\n"
    "v_mfma_f32_16x16x32_bf16 v[16:19], v[248:251], v[120:123], v[16:19]\n"
    "s_waitcnt lgkmcnt(0)\n"
    "v_mfma_f32_16x16x32_bf16 v[56:59], v[248:251], v[124:127], v[56:59]\n"
    "s_waitcnt vmcnt(20)\n"
    "v_mfma_f32_16x16x32_bf16 v[20:23], v[252:255], v[120:123], v[20:23]\n"
    "v_mfma_f32_16x16x32_bf16 v[60:63], v[252:255], v[124:127], v[60:63]\n"
    "s_waitcnt vmcnt(19)\n"
    "v_mfma_f32_16x16x32_bf16 v[24:27], v[128:131], v[120:123], v[24:27]\n"
    "v_mfma_f32_16x16x32_bf16 v[64:67], v[128:131], v[124:127], v[64:67]\n"
    "s_add_u32 s48, s28, 237436928\n"
    "buffer_load_dwordx4 v[128:131], v8, s[40:43], s48 offen sc1\n"
    "s_waitcnt vmcnt(19)\n"
    "v_mfma_f32_16x16x32_bf16 v[28:31], v[132:135], v[120:123], v[28:31]\n"
    "v_mfma_f32_16x16x32_bf16 v[68:71], v[132:135], v[124:127], v[68:71]\n"
    "s_add_u32 s48, s28, 237436928\n"
    "buffer_load_dwordx4 v[132:135], v8, s[40:43], s48 offen offset:512 sc1\n"
    "s_waitcnt vmcnt(19)\n"
    "v_mfma_f32_16x16x32_bf16 v[32:35], v[136:139], v[120:123], v[32:35]\n"
    "v_mfma_f32_16x16x32_bf16 v[72:75], v[136:139], v[124:127], v[72:75]\n"
    "s_add_u32 s48, s28, 237438976\n"
    "buffer_load_dwordx4 v[136:139], v8, s[40:43], s48 offen sc1\n"
    "s_waitcnt vmcnt(19)\n"
    "v_mfma_f32_16x16x32_bf16 v[36:39], v[140:143], v[120:123], v[36:39]\n"
    "v_mfma_f32_16x16x32_bf16 v[76:79], v[140:143], v[124:127], v[76:79]\n"
    "s_add_u32 s48, s28, 237438976\n"
    "buffer_load_dwordx4 v[140:143], v8, s[40:43], s48 offen offset:512 sc1\n"
    "s_waitcnt vmcnt(19)\n"
    "v_mfma_f32_16x16x32_bf16 v[40:43], v[144:147], v[120:123], v[40:43]\n"
    "v_mfma_f32_16x16x32_bf16 v[80:83], v[144:147], v[124:127], v[80:83]\n"
    "s_add_u32 s48, s28, 237441024\n"
    "buffer_load_dwordx4 v[144:147], v8, s[40:43], s48 offen sc1\n"
    "s_waitcnt vmcnt(19)\n"
    "v_mfma_f32_16x16x32_bf16 v[44:47], v[148:151], v[120:123], v[44:47]\n"
    "v_mfma_f32_16x16x32_bf16 v[84:87], v[148:151], v[124:127], v[84:87]\n"
    "s_add_u32 s48, s28, 237441024\n"
    "buffer_load_dwordx4 v[148:151], v8, s[40:43], s48 offen offset:512 sc1\n"
    "s_waitcnt vmcnt(19)\n"
    "v_mfma_f32_16x16x32_bf16 v[48:51], v[152:155], v[120:123], v[48:51]\n"
    "v_mfma_f32_16x16x32_bf16 v[88:91], v[152:155], v[124:127], v[88:91]\n"
    "s_add_u32 s48, s28, 237443072\n"
    "buffer_load_dwordx4 v[152:155], v8, s[40:43], s48 offen sc1\n"
    "s_waitcnt vmcnt(19)\n"
    "v_mfma_f32_16x16x32_bf16 v[52:55], v[156:159], v[120:123], v[52:55]\n"
    "v_mfma_f32_16x16x32_bf16 v[92:95], v[156:159], v[124:127], v[92:95]\n"
    "s_add_u32 s48, s28, 237443072\n"
    "buffer_load_dwordx4 v[156:159], v8, s[40:43], s48 offen offset:512 sc1\n"
    "s_waitcnt vmcnt(7)\n"
    "v_pk_add_f32 v[128:129], v[128:129], v[16:17]\n"
    "v_pk_add_f32 v[130:131], v[130:131], v[20:21]\n"
    "s_waitcnt vmcnt(6)\n"
    "v_pk_add_f32 v[132:133], v[132:133], v[18:19]\n"
    "v_pk_add_f32 v[134:135], v[134:135], v[22:23]\n"
    "s_waitcnt vmcnt(5)\n"
    "v_pk_add_f32 v[136:137], v[136:137], v[24:25]\n"
    "v_pk_add_f32 v[138:139], v[138:139], v[28:29]\n"
    "s_waitcnt vmcnt(4)\n"
    "v_pk_add_f32 v[140:141], v[140:141], v[26:27]\n"
    "v_pk_add_f32 v[142:143], v[142:143], v[30:31]\n"
    "s_waitcnt vmcnt(3)\n"
    "v_pk_add_f32 v[144:145], v[144:145], v[32:33]\n"
    "v_pk_add_f32 v[146:147], v[146:147], v[36:37]\n"
    "s_waitcnt vmcnt(2)\n"
    "v_pk_add_f32 v[148:149], v[148:149], v[34:35]\n"
    "v_pk_add_f32 v[150:151], v[150:151], v[38:39]\n"
    "s_waitcnt vmcnt(1)\n"
    "v_pk_add_f32 v[152:153], v[152:153], v[40:41]\n"
    "v_pk_add_f32 v[154:155], v[154:155], v[44:45]\n"
    "s_waitcnt vmcnt(0)\n"
    "v_pk_add_f32 v[156:157], v[156:157], v[42:43]\n"
    "v_pk_add_f32 v[158:159], v[158:159], v[46:47]\n"
    "v_pk_add_f32 v[160:161], v[160:161], v[48:49]\n"
    "v_pk_add_f32 v[162:163], v[162:163], v[52:53]\n"
    "v_pk_add_f32 v[164:165], v[164:165], v[50:51]\n"
    "v_pk_add_f32 v[166:167], v[166:167], v[54:55]\n"
    "v_pk_add_f32 v[168:169], v[168:169], v[56:57]\n"
    "v_pk_add_f32 v[170:171], v[170:171], v[60:61]\n"
    "v_pk_add_f32 v[172:173], v[172:173], v[58:59]\n"
    "v_pk_add_f32 v[174:175], v[174:175], v[62:63]\n"
    "v_pk_add_f32 v[176:177], v[176:177], v[64:65]\n"
    "v_pk_add_f32 v[178:179], v[178:179], v[68:69]\n"
    "v_pk_add_f32 v[180:181], v[180:181], v[66:67]\n"
    "v_pk_add_f32 v[182:183], v[182:183], v[70:71]\n"
    "v_pk_add_f32 v[184:185], v[184:185], v[72:73]\n"
    "v_pk_add_f32 v[186:187], v[186:187], v[76:77]\n"
    "v_pk_add_f32 v[188:189], v[188:189], v[74:75]\n"
    "v_pk_add_f32 v[190:191], v[190:191], v[78:79]\n"
    "v_pk_add_f32 v[192:193], v[192:193], v[80:81]\n"
    "v_pk_add_f32 v[194:195], v[194:195], v[84:85]\n"
    "v_pk_add_f32 v[196:197], v[196:197], v[82:83]\n"
    "v_pk_add_f32 v[198:199], v[198:199], v[86:87]\n"
    "v_pk_add_f32 v[200:201], v[200:201], v[88:89]\n"
    "v_pk_add_f32 v[202:203], v[202:203], v[92:93]\n"
    "v_pk_add_f32 v[204:205], v[204:205], v[90:91]\n"
    "v_pk_add_f32 v[206:207], v[206:207], v[94:95]\n"
    "s_cmp_eq_u32 s21, 0\n"
    "s_nop 0\n"
    "s_cbranch_scc1 hpb2_mid_%=\n"
    "s_mov_b32 s36, s66\n"
    "s_mov_b32 s37, s67\n"
    "s_mov_b32 s38, 2621440\n"
    "s_mov_b32 s39, 0x20000\n"
    "v_and_b32 v4, 15, v15\n"
    "v_mul_u32_u24 v4, 1280, v4\n"
    "v_and_b32 v5, 32, v15\n"
    "v_add_u32 v4, v4, v5\n"
    "v_bfe_u32 v5, v15, 4, 1\n"
    "v_lshl_add_u32 v4, v5, 3, v4\n"
    "s_mul_i32 s48, s26, 40960\n"
    "s_mul_i32 s49, s45, 320\n"
    "s_add_u32 s48, s48, s49\n"
    "s_add_u32 s49, s48, 20480\n"
    "v_cvt_pk_bf16_f32 v16, v128, v129\n"
    "v_cvt_pk_bf16_f32 v17, v130, v131\n"
    "v_cvt_pk_bf16_f32 v18, v132, v133\n"
    "v_cvt_pk_bf16_f32 v19, v134, v135\n"
    "v_cvt_pk_bf16_f32 v20, v136, v137\n"
    "v_cvt_pk_bf16_f32 v21, v138, v139\n"
    "v_cvt_pk_bf16_f32 v22, v140, v141\n"
    "v_cvt_pk_bf16_f32 v23, v142, v143\n"
    "v_cvt_pk_bf16_f32 v24, v144, v145\n"
    "v_cvt_pk_bf16_f32 v25, v146, v147\n"
    "v_cvt_pk_bf16_f32 v26, v148, v149\n"
    "v_cvt_pk_bf16_f32 v27, v150, v151\n"
    "v_cvt_pk_bf16_f32 v28, v152, v153\n"
    "v_cvt_pk_bf16_f32 v29, v154, v155\n"
    "v_cvt_pk_bf16_f32 v30, v156, v157\n"
    "v_cvt_pk_bf16_f32 v31, v158, v159\n"
    "v_cvt_pk_bf16_f32 v32, v160, v161\n"
    "v_cvt_pk_bf16_f32 v33, v162, v163\n"
    "v_cvt_pk_bf16_f32 v34, v164, v165\n"
    "v_cvt_pk_bf16_f32 v35, v166, v167\n"
    "v_cvt_pk_bf16_f32 v36, v168, v169\n"
    "v_cvt_pk_bf16_f32 v37, v170, v171\n"
    "v_cvt_pk_bf16_f32 v38, v172, v173\n"
    "v_cvt_pk_bf16_f32 v39, v174, v175\n"
    "v_cvt_pk_bf16_f32 v40, v176, v177\n"
    "v_cvt_pk_bf16_f32 v41, v178, v179\n"
    "v_cvt_pk_bf16_f32 v42, v180, v181\n"
    "v_cvt_pk_bf16_f32 v43, v182, v183\n"
    "v_cvt_pk_bf16_f32 v44, v184, v185\n"
    "v_cvt_pk_bf16_f32 v45, v186, v187\n"
    "v_cvt_pk_bf16_f32 v46, v188, v189\n"
    "v_cvt_pk_bf16_f32 v47, v190, v191\n"
    "v_cvt_pk_bf16_f32 v48, v192, v193\n"
    "v_cvt_pk_bf16_f32 v49, v194, v195\n"
    "v_cvt_pk_bf16_f32 v50, v196, v197\n"
    "v_cvt_pk_bf16_f32 v51, v198, v199\n"
    "v_cvt_pk_bf16_f32 v52, v200, v201\n"
    "v_cvt_pk_bf16_f32 v53, v202, v203\n"
    "v_cvt_pk_bf16_f32 v54, v204, v205\n"
    "v_cvt_pk_bf16_f32 v55, v206, v207\n"
    "buffer_store_dwordx2 v[16:17], v4, s[36:39], s48 offen offset:0\n"
    "buffer_store_dwordx2 v[18:19], v4, s[36:39], s48 offen offset:16\n"
    "buffer_store_dwordx2 v[20:21], v4, s[36:39], s48 offen offset:64\n"
    "buffer_store_dwordx2 v[22:23], v4, s[36:39], s48 offen offset:80\n"
    "buffer_store_dwordx2 v[24:25], v4, s[36:39], s48 offen offset:128\n"
    "buffer_store_dwordx2 v[26:27], v4, s[36:39], s48 offen offset:144\n"
    "buffer_store_dwordx2 v[28:29], v4, s[36:39], s48 offen offset:192\n"
    "buffer_store_dwordx2 v[30:31], v4, s[36:39], s48 offen offset:208\n"
    "buffer_store_dwordx2 v[32:33], v4, s[36:39], s48 offen offset:256\n"
    "buffer_store_dwordx2 v[34:35], v4, s[36:39], s48 offen offset:272\n"
    "buffer_store_dwordx2 v[36:37], v4, s[36:39], s49 offen offset:0\n"
    "buffer_store_dwordx2 v[38:39], v4, s[36:39], s49 offen offset:16\n"
    "buffer_store_dwordx2 v[40:41], v4, s[36:39], s49 offen offset:64\n"
    "buffer_store_dwordx2 v[42:43], v4, s[36:39], s49 offen offset:80\n"
    "buffer_store_dwordx2 v[44:45], v4, s[36:39], s49 offen offset:128\n"
    "buffer_store_dwordx2 v[46:47], v4, s[36:39], s49 offen offset:144\n"
    "buffer_store_dwordx2 v[48:49], v4, s[36:39], s49 offen offset:192\n"
    "buffer_store_dwordx2 v[50:51], v4, s[36:39], s49 offen offset:208\n"
    "buffer_store_dwordx2 v[52:53], v4, s[36:39], s49 offen offset:256\n"
    "buffer_store_dwordx2 v[54:55], v4, s[36:39], s49 offen offset:272\n"
    "s_mov_b32 s54, 0x40000000\n"
    "s_nop 0\n"
    "s_branch hp_done_%=\n"
    "hpb2_mid_%=:\n"
    "v_and_b32 v6, 0xf0, v0\n"
    "v_add_u32 v6, 117760, v6\n"
    "s_lshl_b32 s48, s45, 2\n"
    "v_add_u32 v7, s48, v6\n"
    "v_pk_add_f32 v[10:11], v[128:129], v[130:131]\n"
    "v_pk_add_f32 v[10:11], v[10:11], v[132:133]\n"
    "v_pk_add_f32 v[10:11], v[10:11], v[134:135]\n"
    "v_pk_add_f32 v[10:11], v[10:11], v[136:137]\n"
    "v_pk_add_f32 v[10:11], v[10:11], v[138:139]\n"
    "v_pk_add_f32 v[10:11], v[10:11], v[140:141]\n"
    "v_pk_add_f32 v[10:11], v[10:11], v[142:143]\n"
    "v_pk_add_f32 v[10:11], v[10:11], v[144:145]\n"
    "v_pk_add_f32 v[10:11], v[10:11], v[146:147]\n"
    "v_pk_add_f32 v[10:11], v[10:11], v[148:149]\n"
    "v_pk_add_f32 v[10:11], v[10:11], v[150:151]\n"
    "v_pk_add_f32 v[10:11], v[10:11], v[152:153]\n"
    "v_pk_add_f32 v[10:11], v[10:11], v[154:155]\n"
    "v_pk_add_f32 v[10:11], v[10:11], v[156:157]\n"
    "v_pk_add_f32 v[10:11], v[10:11], v[158:159]\n"
    "v_pk_add_f32 v[10:11], v[10:11], v[160:161]\n"
    "v_pk_add_f32 v[10:11], v[10:11], v[162:163]\n"
    "v_pk_add_f32 v[10:11], v[10:11], v[164:165]\n"
    "v_pk_add_f32 v[10:11], v[10:11], v[166:167]\n"
    "v_pk_add_f32 v[12:13], v[168:169], v[170:171]\n"
    "v_pk_add_f32 v[12:13], v[12:13], v[172:173]\n"
    "v_pk_add_f32 v[12:13], v[12:13], v[174:175]\n"
    "v_pk_add_f32 v[12:13], v[12:13], v[176:177]\n"
    "v_pk_add_f32 v[12:13], v[12:13], v[178:179]\n"
    "v_pk_add_f32 v[12:13], v[12:13], v[180:181]\n"
    "v_pk_add_f32 v[12:13], v[12:13], v[182:183]\n"
    "v_pk_add_f32 v[12:13], v[12:13], v[184:185]\n"
    "v_pk_add_f32 v[12:13], v[12:13], v[186:187]\n"
    "v_pk_add_f32 v[12:13], v[12:13], v[188:189]\n"
    "v_pk_add_f32 v[12:13], v[12:13], v[190:191]\n"
    "v_pk_add_f32 v[12:13], v[12:13], v[192:193]\n"
    "v_pk_add_f32 v[12:13], v[12:13], v[194:195]\n"
    "v_pk_add_f32 v[12:13], v[12:13], v[196:197]\n"
    "v_pk_add_f32 v[12:13], v[12:13], v[198:199]\n"
    "v_pk_add_f32 v[12:13], v[12:13], v[200:201]\n"
    "v_pk_add_f32 v[12:13], v[12:13], v[202:203]\n"
    "v_pk_add_f32 v[12:13], v[12:13], v[204:205]\n"
    "v_pk_add_f32 v[12:13], v[12:13], v[206:207]\n"
    "v_add_f32 v10, v10, v11\n"
    "v_mov_b32 v11, v10\n"
    "s_nop 1\n"
    "v_permlane16_swap_b32 v10, v11\n"
    "v_add_f32 v10, v10, v11\n"
    "v_mov_b32 v11, v10\n"
    "s_nop 1\n"
    "v_permlane32_swap_b32 v10, v11\n"
    "v_add_f32 v10, v10, v11\n"
    "ds_write_b32 v7, v10 offset:0\n"
    "v_add_f32 v12, v12, v13\n"
    "v_mov_b32 v13, v12\n"
    "s_nop 1\n"
    "v_permlane16_swap_b32 v12, v13\n"
    "v_add_f32 v12, v12, v13\n"
    "v_mov_b32 v13, v12\n"
    "s_nop 1\n"
    "v_permlane32_swap_b32 v12, v13\n"
    "v_add_f32 v12, v12, v13\n"
    "ds_write_b32 v7, v12 offset:256\n"
    "s_waitcnt lgkmcnt(0)\n"
    "s_barrier\n"
    "ds_read_b128 v[96:99], v6 offset:0\n"
    "ds_read_b128 v[100:103], v6 offset:256\n"
    "s_waitcnt lgkmcnt(1)\n"
    "v_pk_add_f32 v[96:97], v[96:97], v[98:99]\n"
    "v_add_f32 v10, v96, v97\n"
    "v_mul_f32 v10, 0xbacccccd, v10\n"
    "s_waitcnt lgkmcnt(0)\n"
    "v_pk_add_f32 v[100:101], v[100:101], v[102:103]\n"
    "v_add_f32 v12, v100, v101\n"
    "v_mul_f32 v12, 0xbacccccd, v12\n"
    "v_pk_add_f32 v[16:17], v[128:129], v[10:11] op_sel_hi:[1,0]\n"
    "v_pk_add_f32 v[18:19], v[130:131], v[10:11] op_sel_hi:[1,0]\n"
    "v_pk_add_f32 v[20:21], v[132:133], v[10:11] op_sel_hi:[1,0]\n"
    "v_pk_add_f32 v[22:23], v[134:135], v[10:11] op_sel_hi:[1,0]\n"
    "v_pk_add_f32 v[24:25], v[136:137], v[10:11] op_sel_hi:[1,0]\n"
    "v_pk_add_f32 v[26:27], v[138:139], v[10:11] op_sel_hi:[1,0]\n"
    "v_pk_add_f32 v[28:29], v[140:141], v[10:11] op_sel_hi:[1,0]\n"
    "v_pk_add_f32 v[30:31], v[142:143], v[10:11] op_sel_hi:[1,0]\n"
    "v_pk_add_f32 v[32:33], v[144:145], v[10:11] op_sel_hi:[1,0]\n"
    "v_pk_add_f32 v[34:35], v[146:147], v[10:11] op_sel_hi:[1,0]\n"
    "v_pk_add_f32 v[36:37], v[148:149], v[10:11] op_sel_hi:[1,0]\n"
    "v_pk_add_f32 v[38:39], v[150:151], v[10:11] op_sel_hi:[1,0]\n"
    "v_pk_add_f32 v[40:41], v[152:153], v[10:11] op_sel_hi:[1,0]\n"
    "v_pk_add_f32 v[42:43], v[154:155], v[10:11] op_sel_hi:[1,0]\n"
    "v_pk_add_f32 v[44:45], v[156:157], v[10:11] op_sel_hi:[1,0]\n"
    "v_pk_add_f32 v[46:47], v[158:159], v[10:11] op_sel_hi:[1,0]\n"
    "v_pk_add_f32 v[48:49], v[160:161], v[10:11] op_sel_hi:[1,0]\n"
    "v_pk_add_f32 v[50:51], v[162:163], v[10:11] op_sel_hi:[1,0]\n"
    "v_pk_add_f32 v[52:53], v[164:165], v[10:11] op_sel_hi:[1,0]\n"
    "v_pk_add_f32 v[54:55], v[166:167], v[10:11] op_sel_hi:[1,0]\n"
    "v_pk_add_f32 v[56:57], v[168:169], v[12:13] op_sel_hi:[1,0]\n"
    "v_pk_add_f32 v[58:59], v[170:171], v[12:13] op_sel_hi:[1,0]\n"
    "v_pk_add_f32 v[60:61], v[172:173], v[12:13] op_sel_hi:[1,0]\n"
    "v_pk_add_f32 v[62:63], v[174:175], v[12:13] op_sel_hi:[1,0]\n"
    "v_pk_add_f32 v[64:65], v[176:177], v[12:13] op_sel_hi:[1,0]\n"
    "v_pk_add_f32 v[66:67], v[178:179], v[12:13] op_sel_hi:[1,0]\n"
    "v_pk_add_f32 v[68:69], v[180:181], v[12:13] op_sel_hi:[1,0]\n"
    "v_pk_add_f32 v[70:71], v[182:183], v[12:13] op_sel_hi:[1,0]\n"
    "v_pk_add_f32 v[72:73], v[184:185], v[12:13] op_sel_hi:[1,0]\n"
    "v_pk_add_f32 v[74:75], v[186:187], v[12:13] op_sel_hi:[1,0]\n"
    "v_pk_add_f32 v[76:77], v[188:189], v[12:13] op_sel_hi:[1,0]\n"
    "v_pk_add_f32 v[78:79], v[190:191], v[12:13] op_sel_hi:[1,0]\n"
    "v_pk_add_f32 v[80:81], v[192:193], v[12:13] op_sel_hi:[1,0]\n"
    "v_pk_add_f32 v[82:83], v[194:195], v[12:13] op_sel_hi:[1,0]\n"
    "v_pk_add_f32 v[84:85], v[196:197], v[12:13] op_sel_hi:[1,0]\n"
    "v_pk_add_f32 v[86:87], v[198:199], v[12:13] op_sel_hi:[1,0]\n"
    "v_pk_add_f32 v[88:89], v[200:201], v[12:13] op_sel_hi:[1,0]\n"
    "v_pk_add_f32 v[90:91], v[202:203], v[12:13] op_sel_hi:[1,0]\n"
    "v_pk_add_f32 v[92:93], v[204:205], v[12:13] op_sel_hi:[1,0]\n"
    "v_pk_add_f32 v[94:95], v[206:207], v[12:13] op_sel_hi:[1,0]\n"
    "v_pk_mul_f32 v[10:11], v[16:17], v[16:17]\n"
    "v_pk_fma_f32 v[10:11], v[18:19], v[18:19], v[10:11]\n"
    "v_pk_fma_f32 v[10:11], v[20:21], v[20:21], v[10:11]\n"
    "v_pk_fma_f32 v[10:11], v[22:23], v[22:23], v[10:11]\n"
    "v_pk_fma_f32 v[10:11], v[24:25], v[24:25], v[10:11]\n"
    "v_pk_fma_f32 v[10:11], v[26:27], v[26:27], v[10:11]\n"
    "v_pk_fma_f32 v[10:11], v[28:29], v[28:29], v[10:11]\n"
    "v_pk_fma_f32 v[10:11], v[30:31], v[30:31], v[10:11]\n"
    "v_pk_fma_f32 v[10:11], v[32:33], v[32:33], v[10:11]\n"
    "v_pk_fma_f32 v[10:11], v[34:35], v[34:35], v[10:11]\n"
    "v_pk_fma_f32 v[10:11], v[36:37], v[36:37], v[10:11]\n"
    "v_pk_fma_f32 v[10:11], v[38:39], v[38:39], v[10:11]\n"
    "v_pk_fma_f32 v[10:11], v[40:41], v[40:41], v[10:11]\n"
    "v_pk_fma_f32 v[10:11], v[42:43], v[42:43], v[10:11]\n"
    "v_pk_fma_f32 v[10:11], v[44:45], v[44:45], v[10:11]\n"
    "v_pk_fma_f32 v[10:11], v[46:47], v[46:47], v[10:11]\n"
    "v_pk_fma_f32 v[10:11], v[48:49], v[48:49], v[10:11]\n"
    "v_pk_fma_f32 v[10:11], v[50:51], v[50:51], v[10:11]\n"
    "v_pk_fma_f32 v[10:11], v[52:53], v[52:53], v[10:11]\n"
    "v_pk_fma_f32 v[10:11], v[54:55], v[54:55], v[10:11]\n"
    "v_pk_mul_f32 v[12:13], v[56:57], v[56:57]\n"
    "v_pk_fma_f32 v[12:13], v[58:59], v[58:59], v[12:13]\n"
    "v_pk_fma_f32 v[12:13], v[60:61], v[60:61], v[12:13]\n"
    "v_pk_fma_f32 v[12:13], v[62:63], v[62:63], v[12:13]\n"
    "v_pk_fma_f32 v[12:13], v[64:65], v[64:65], v[12:13]\n"
    "v_pk_fma_f32 v[12:13], v[66:67], v[66:67], v[12:13]\n"
    "v_pk_fma_f32 v[12:13], v[68:69], v[68:69], v[12:13]\n"
    "v_pk_fma_f32 v[12:13], v[70:71], v[70:71], v[12:13]\n"
    "v_pk_fma_f32 v[12:13], v[72:73], v[72:73], v[12:13]\n"
    "v_pk_fma_f32 v[12:13], v[74:75], v[74:75], v[12:13]\n"
    "v_pk_fma_f32 v[12:13], v[76:77], v[76:77], v[12:13]\n"
    "v_pk_fma_f32 v[12:13], v[78:79], v[78:79], v[12:13]\n"
    "v_pk_fma_f32 v[12:13], v[80:81], v[80:81], v[12:13]\n"
    "v_pk_fma_f32 v[12:13], v[82:83], v[82:83], v[12:13]\n"
    "v_pk_fma_f32 v[12:13], v[84:85], v[84:85], v[12:13]\n"
    "v_pk_fma_f32 v[12:13], v[86:87], v[86:87], v[12:13]\n"
    "v_pk_fma_f32 v[12:13], v[88:89], v[88:89], v[12:13]\n"
    "v_pk_fma_f32 v[12:13], v[90:91], v[90:91], v[12:13]\n"
    "v_pk_fma_f32 v[12:13], v[92:93], v[92:93], v[12:13]\n"
    "v_pk_fma_f32 v[12:13], v[94:95], v[94:95], v[12:13]\n"
    "v_add_f32 v10, v10, v11\n"
    "v_mov_b32 v11, v10\n"
    "s_nop 1\n"
    "v_permlane16_swap_b32 v10, v11\n"
    "v_add_f32 v10, v10, v11\n"
    "v_mov_b32 v11, v10\n"
    "s_nop 1\n"
    "v_permlane32_swap_b32 v10, v11\n"
    "v_add_f32 v10, v10, v11\n"
    "ds_write_b32 v7, v10 offset:512\n"
    "v_add_f32 v12, v12, v13\n"
    "v_mov_b32 v13, v12\n"
    "s_nop 1\n"
    "v_permlane16_swap_b32 v12, v13\n"
    "v_add_f32 v12, v12, v13\n"
    "v_mov_b32 v13, v12\n"
    "s_nop 1\n"
    "v_permlane32_swap_b32 v12, v13\n"
    "v_add_f32 v12, v12, v13\n"
    "ds_write_b32 v7, v12 offset:768\n"
    "s_waitcnt lgkmcnt(0)\n"
    "s_barrier\n"
    "ds_read_b128 v[96:99], v6 offset:512\n"
    "ds_read_b128 v[100:103], v6 offset:768\n"
    "s_waitcnt lgkmcnt(1)\n"
    "v_pk_add_f32 v[96:97], v[96:97], v[98:99]\n"
    "v_add_f32 v10, v96, v97\n"
    "v_mul_f32 v10, 0x3acccccd, v10\n"
    "s_waitcnt lgkmcnt(0)\n"
    "v_pk_add_f32 v[100:101], v[100:101], v[102:103]\n"
    "v_add_f32 v12, v100, v101\n"
    "v_mul_f32 v12, 0x3acccccd, v12\n"
    "v_add_f32 v10, 0x3727c5ac, v10\n"
    "v_rsq_f32 v10, v10\n"
    "v_add_f32 v12, 0x3727c5ac, v12\n"
    "v_rsq_f32 v12, v12\n"
    "v_pk_mul_f32 v[16:17], v[16:17], v[10:11] op_sel_hi:[1,0]\n"
    "v_pk_mul_f32 v[18:19], v[18:19], v[10:11] op_sel_hi:[1,0]\n"
    "v_pk_mul_f32 v[20:21], v[20:21], v[10:11] op_sel_hi:[1,0]\n"
    "v_pk_mul_f32 v[22:23], v[22:23], v[10:11] op_sel_hi:[1,0]\n"
    "v_pk_mul_f32 v[24:25], v[24:25], v[10:11] op_sel_hi:[1,0]\n"
    "v_pk_mul_f32 v[26:27], v[26:27], v[10:11] op_sel_hi:[1,0]\n"
    "v_pk_mul_f32 v[28:29], v[28:29], v[10:11] op_sel_hi:[1,0]\n"
    "v_pk_mul_f32 v[30:31], v[30:31], v[10:11] op_sel_hi:[1,0]\n"
    "v_pk_mul_f32 v[32:33], v[32:33], v[10:11] op_sel_hi:[1,0]\n"
    "v_pk_mul_f32 v[34:35], v[34:35], v[10:11] op_sel_hi:[1,0]\n"
    "v_pk_mul_f32 v[36:37], v[36:37], v[10:11] op_sel_hi:[1,0]\n"
    "v_pk_mul_f32 v[38:39], v[38:39], v[10:11] op_sel_hi:[1,0]\n"
    "v_pk_mul_f32 v[40:41], v[40:41], v[10:11] op_sel_hi:[1,0]\n"
    "v_pk_mul_f32 v[42:43], v[42:43], v[10:11] op_sel_hi:[1,0]\n"
    "v_pk_mul_f32 v[44:45], v[44:45], v[10:11] op_sel_hi:[1,0]\n"
    "v_pk_mul_f32 v[46:47], v[46:47], v[10:11] op_sel_hi:[1,0]\n"
    "v_pk_mul_f32 v[48:49], v[48:49], v[10:11] op_sel_hi:[1,0]\n"
    "v_pk_mul_f32 v[50:51], v[50:51], v[10:11] op_sel_hi:[1,0]\n"
    "v_pk_mul_f32 v[52:53], v[52:53], v[10:11] op_sel_hi:[1,0]\n"
    "v_pk_mul_f32 v[54:55], v[54:55], v[10:11] op_sel_hi:[1,0]\n"
    "v_pk_mul_f32 v[56:57], v[56:57], v[12:13] op_sel_hi:[1,0]\n"
    "v_pk_mul_f32 v[58:59], v[58:59], v[12:13] op_sel_hi:[1,0]\n"
    "v_pk_mul_f32 v[60:61], v[60:61], v[12:13] op_sel_hi:[1,0]\n"
    "v_pk_mul_f32 v[62:63], v[62:63], v[12:13] op_sel_hi:[1,0]\n"
    "v_pk_mul_f32 v[64:65], v[64:65], v[12:13] op_sel_hi:[1,0]\n"
    "v_pk_mul_f32 v[66:67], v[66:67], v[12:13] op_sel_hi:[1,0]\n"
    "v_pk_mul_f32 v[68:69], v[68:69], v[12:13] op_sel_hi:[1,0]\n"
    "v_pk_mul_f32 v[70:71], v[70:71], v[12:13] op_sel_hi:[1,0]\n"
    "v_pk_mul_f32 v[72:73], v[72:73], v[12:13] op_sel_hi:[1,0]\n"
    "v_pk_mul_f32 v[74:75], v[74:75], v[12:13] op_sel_hi:[1,0]\n"
    "v_pk_mul_f32 v[76:77], v[76:77], v[12:13] op_sel_hi:[1,0]\n"
    "v_pk_mul_f32 v[78:79], v[78:79], v[12:13] op_sel_hi:[1,0]\n"
    "v_pk_mul_f32 v[80:81], v[80:81], v[12:13] op_sel_hi:[1,0]\n"
    "v_pk_mul_f32 v[82:83], v[82:83], v[12:13] op_sel_hi:[1,0]\n"
    "v_pk_mul_f32 v[84:85], v[84:85], v[12:13] op_sel_hi:[1,0]\n"
    "v_pk_mul_f32 v[86:87], v[86:87], v[12:13] op_sel_hi:[1,0]\n"
    "v_pk_mul_f32 v[88:89], v[88:89], v[12:13] op_sel_hi:[1,0]\n"
    "v_pk_mul_f32 v[90:91], v[90:91], v[12:13] op_sel_hi:[1,0]\n"
    "v_pk_mul_f32 v[92:93], v[92:93], v[12:13] op_sel_hi:[1,0]\n"
    "v_pk_mul_f32 v[94:95], v[94:95], v[12:13] op_sel_hi:[1,0]\n"
    "s_lshr_b32 s51, s28, 1\n"
    "v_cvt_pk_bf16_f32 v208, v16, v17\n"
    "v_cvt_pk_bf16_f32 v209, v20, v21\n"
    "v_cvt_pk_bf16_f32 v210, v18, v19\n"
    "v_cvt_pk_bf16_f32 v211, v22, v23\n"
    "s_add_u32 s48, s51, 62816256\n"
    "buffer_store_dwordx4 v[208:211], v0, s[40:43], s48 offen sc1\n"
    "v_cvt_pk_bf16_f32 v212, v24, v25\n"
    "v_cvt_pk_bf16_f32 v213, v28, v29\n"
    "v_cvt_pk_bf16_f32 v214, v26, v27\n"
    "v_cvt_pk_bf16_f32 v215, v30, v31\n"
    "s_add_u32 s48, s51, 62817280\n"
    "buffer_store_dwordx4 v[212:215], v0, s[40:43], s48 offen sc1\n"
    "v_cvt_pk_bf16_f32 v216, v32, v33\n"
    "v_cvt_pk_bf16_f32 v217, v36, v37\n"
    "v_cvt_pk_bf16_f32 v218, v34, v35\n"
    "v_cvt_pk_bf16_f32 v219, v38, v39\n"
    "s_add_u32 s48, s51, 62818304\n"
    "buffer_store_dwordx4 v[216:219], v0, s[40:43], s48 offen sc1\n"
    "v_cvt_pk_bf16_f32 v220, v40, v41\n"
    "v_cvt_pk_bf16_f32 v221, v44, v45\n"
    "v_cvt_pk_bf16_f32 v222, v42, v43\n"
    "v_cvt_pk_bf16_f32 v223, v46, v47\n"
    "s_add_u32 s48, s51, 62819328\n"
    "buffer_store_dwordx4 v[220:223], v0, s[40:43], s48 offen sc1\n"
    "v_cvt_pk_bf16_f32 v224, v48, v49\n"
    "v_cvt_pk_bf16_f32 v225, v52, v53\n"
    "v_cvt_pk_bf16_f32 v226, v50, v51\n"
    "v_cvt_pk_bf16_f32 v227, v54, v55\n"
    "s_add_u32 s48, s51, 62820352\n"
    "buffer_store_dwordx4 v[224:227], v0, s[40:43], s48 offen sc1\n"
    "v_cvt_pk_bf16_f32 v228, v56, v57\n"
    "v_cvt_pk_bf16_f32 v229, v60, v61\n"
    "v_cvt_pk_bf16_f32 v230, v58, v59\n"
    "v_cvt_pk_bf16_f32 v231, v62, v63\n"
    "s_add_u32 s48, s51, 62836736\n"
    "buffer_store_dwordx4 v[228:231], v0, s[40:43], s48 offen sc1\n"
    "v_cvt_pk_bf16_f32 v232, v64, v65\n"
    "v_cvt_pk_bf16_f32 v233, v68, v69\n"
    "v_cvt_pk_bf16_f32 v234, v66, v67\n"
    "v_cvt_pk_bf16_f32 v235, v70, v71\n"
    "s_add_u32 s48, s51, 62837760\n"
    "buffer_store_dwordx4 v[232:235], v0, s[40:43], s48 offen sc1\n"
    "v_cvt_pk_bf16_f32 v236, v72, v73\n"
    "v_cvt_pk_bf16_f32 v237, v76, v77\n"
    "v_cvt_pk_bf16_f32 v238, v74, v75\n"
    "v_cvt_pk_bf16_f32 v239, v78, v79\n"
    "s_add_u32 s48, s51, 62838784\n"
    "buffer_store_dwordx4 v[236:239], v0, s[40:43], s48 offen sc1\n"
    "v_cvt_pk_bf16_f32 v240, v80, v81\n"
    "v_cvt_pk_bf16_f32 v241, v84, v85\n"
    "v_cvt_pk_bf16_f32 v242, v82, v83\n"
    "v_cvt_pk_bf16_f32 v243, v86, v87\n"
    "s_add_u32 s48, s51, 62839808\n"
    "buffer_store_dwordx4 v[240:243], v0, s[40:43], s48 offen sc1\n"
    "v_cvt_pk_bf16_f32 v244, v88, v89\n"
    "v_cvt_pk_bf16_f32 v245, v92, v93\n"
    "v_cvt_pk_bf16_f32 v246, v90, v91\n"
    "v_cvt_pk_bf16_f32 v247, v94, v95\n"
    "s_add_u32 s48, s51, 62840832\n"
    "buffer_store_dwordx4 v[244:247], v0, s[40:43], s48 offen sc1\n"
    "s_add_u32 s48, s28, 232194048\n"
    "buffer_store_dwordx4 v[128:131], v8, s[40:43], s48 offen sc1\n"
    "s_add_u32 s48, s28, 232194048\n"
    "buffer_store_dwordx4 v[132:135], v8, s[40:43], s48 offen offset:512 sc1\n"
    "s_add_u32 s48, s28, 232196096\n"
    "buffer_store_dwordx4 v[136:139], v8, s[40:43], s48 offen sc1\n"
    "s_add_u32 s48, s28, 232196096\n"
    "buffer_store_dwordx4 v[140:143], v8, s[40:43], s48 offen offset:512 sc1\n"
    "s_add_u32 s48, s28, 232198144\n"
    "buffer_store_dwordx4 v[144:147], v8, s[40:43], s48 offen sc1\n"
    "s_add_u32 s48, s28, 232198144\n"
    "buffer_store_dwordx4 v[148:151], v8, s[40:43], s48 offen offset:512 sc1\n"
    "s_add_u32 s48, s28, 232200192\n"
    "buffer_store_dwordx4 v[152:155], v8, s[40:43], s48 offen sc1\n"
    "s_add_u32 s48, s28, 232200192\n"
    "buffer_store_dwordx4 v[156:159], v8, s[40:43], s48 offen offset:512 sc1\n"
    "s_add_u32 s48, s28, 232202240\n"
    "buffer_store_dwordx4 v[160:163], v8, s[40:43], s48 offen sc1\n"
    "s_add_u32 s48, s28, 232202240\n"
    "buffer_store_dwordx4 v[164:167], v8, s[40:43], s48 offen offset:512 sc1\n"
    "s_add_u32 s48, s28, 232235008\n"
    "buffer_store_dwordx4 v[168:171], v8, s[40:43], s48 offen sc1\n"
    "s_add_u32 s48, s28, 232235008\n"
    "buffer_store_dwordx4 v[172:175], v8, s[40:43], s48 offen offset:512 sc1\n"
    "s_add_u32 s48, s28, 232237056\n"
    "buffer_store_dwordx4 v[176:179], v8, s[40:43], s48 offen sc1\n"
    "s_add_u32 s48, s28, 232237056\n"
    "buffer_store_dwordx4 v[180:183], v8, s[40:43], s48 offen offset:512 sc1\n"
    "s_add_u32 s48, s28, 232239104\n"
    "buffer_store_dwordx4 v[184:187], v8, s[40:43], s48 offen sc1\n"
    "s_add_u32 s48, s28, 232239104\n"
    "buffer_store_dwordx4 v[188:191], v8, s[40:43], s48 offen offset:512 sc1\n"
    "s_add_u32 s48, s28, 232241152\n"
    "buffer_store_dwordx4 v[192:195], v8, s[40:43], s48 offen sc1\n"
    "s_add_u32 s48, s28, 232241152\n"
    "buffer_store_dwordx4 v[196:199], v8, s[40:43], s48 offen offset:512 sc1\n"
    "s_add_u32 s48, s28, 232243200\n"
    "buffer_store_dwordx4 v[200:203], v8, s[40:43], s48 offen sc1\n"
    "s_add_u32 s48, s28, 232243200\n"
    "buffer_store_dwordx4 v[204:207], v8, s[40:43], s48 offen offset:512 sc1\n"
    "s_add_u32 s54, s26, 64\n"
    "s_lshl_b32 s54, s54, 7\n"
    "s_add_u32 s54, s54, 2140672\n"
    "hp_done_%=:\n"
    "s_cmp_eq_u32 s45, 0\n"
    "s_cselect_b32 s59, 1, 0\n"
    "v_lshlrev_b32 v2, 7, v15\n"
    "v_cmp_gt_u32 vcc, s59, v15\n"
    "v_mov_b32 v4, 2147483632\n"
    "s_nop 0\n"
    "v_cndmask_b32 v2, v4, v2, vcc\n"
    "v_mov_b32 v1, 1\n"
    "s_waitcnt vmcnt(0)\n"
    "s_waitcnt lgkmcnt(0)\n"
    "s_barrier\n"
    "buffer_atomic_add v1, v2, s[40:43], s54 offen\n"
    "s_nop 1\n"
    "s_branch hp_next_%=\n"
    "hp_ret_%=:\n"
    "s_waitcnt vmcnt(0)\n"
    "s_waitcnt lgkmcnt(0)\n"
    "s_barrier\n"
    "hp_next_%=:\n"
    "v_mov_b32 v3, 155648\n"
    "s_cmp_lg_u32 s45, 0\n"
    "s_nop 0\n"
    "s_cbranch_scc1 hp_tk_%=\n"
    "s_cmp_eq_u32 s45, 0\n"
    "s_cselect_b32 s59, 1, 0\n"
    "v_lshlrev_b32 v2, 7, v15\n"
    "v_cmp_gt_u32 vcc, s59, v15\n"
    "v_mov_b32 v4, 2147483632\n"
    "s_nop 0\n"
    "v_cndmask_b32 v2, v4, v2, vcc\n"
    "v_mov_b32 v1, 1\n"
    "s_lshl_b32 s54, s21, 7\n"
    "s_add_u32 s54, s54, 2364672\n"
    "buffer_atomic_add v1, v2, s[40:43], s54 offen sc0\n"
    "s_waitcnt vmcnt(0)\n"
    "v_readfirstlane_b32 s48, v1\n"
    "s_nop 1\n"
    "v_mov_b32 v1, s48\n"
    "ds_write_b32 v3, v1\n"
    "s_nop 0\n"
    "hp_tk_%=:\n"
    "s_waitcnt lgkmcnt(0)\n"
    "s_barrier\n"
    "ds_read_b32 v1, v3\n"
    "s_waitcnt lgkmcnt(0)\n"
    "v_readfirstlane_b32 s20, v1\n"
    "s_nop 0\n"
    "s_cmp_lt_u32 s20, 256\n"
    "s_nop 2\n"
    "s_cbranch_scc1 hp_b1_%=\n"
    "s_cmp_lt_u32 s20, 288\n"
    "s_cbranch_scc1 hp_rd_%=\n"
    "s_cmp_lt_u32 s20, 352\n"
    "s_cbranch_scc1 hp_b2_%=\n"
    "s_add_u32 s21, s21, 1\n"
    "s_cmp_lt_u32 s21, 2\n"
    "s_cbranch_scc1 hp_layer_%=\n"
    "s_branch hp_end_%=\n"
    "hp_abort_%=:\n"
    "v_mov_b32 v4, 1\n"
    "v_mov_b32 v5, 1024\n"
    "buffer_store_dword v4, v5, s[40:43], s58 offen sc1\n"
    "hp_end_%=:\n"
    "s_endpgm\n"
    :: CHAIN_INPUTS : CHAIN_CLOBBERS);
    __builtin_unreachable();
}

__global__ void __launch_bounds__(256, 1) mega(char* ws, u16* out, Tune tn) {
    extern __shared__ __attribute__((aligned(16))) char smem[];
    LAS char* lds = (LAS char*)smem;
    rsrc_t r = __builtin_amdgcn_make_buffer_rsrc(ws, 0, WS_BYTES, 0x00020000);
    const int w = __builtin_amdgcn_readfirstlane(threadIdx.x >> 6);
    const unsigned xcc = xcc_id();
    if (threadIdx.x == 0) {
        const unsigned nearg = ld4(r, 0, OFF_FLAGS + FL_NEAR) & 1u;
        const unsigned pos = ((((xcc >> 2) ^ nearg) & 1u) << 2) | (xcc & 3u);
        unsigned role = pos * 32 + ((blockIdx.x >> 3) & 31);
        unsigned* claim = (unsigned*)(ws + OFF_FLAGS + FL_CLAIM);
#pragma unroll 1
        for (int n = 0; n < 256; ++n, role = (role + 1) & 255)
            if (__hip_atomic_exchange(claim + role, 1u, __ATOMIC_RELAXED, __HIP_MEMORY_SCOPE_AGENT) == 0u) break;
        *(LAS unsigned*)lds = role;
    }
    __syncthreads();
    const int role = __builtin_amdgcn_readfirstlane(*(LAS unsigned*)lds);
    __syncthreads();
    const int pos = role >> 5, idx = role & 31;
    const unsigned lo = (unsigned)(uintptr_t)ws, hi = (unsigned)((uintptr_t)ws >> 32) & 0xffffu;
    if (pos < 4) {
        if (idx < 8) role_head(lo, hi, pos * 8 + idx, w, xcc & 3, tn);
        else if (idx < 28) role_col(lo, hi, pos * 20 + idx - 8, w, xcc & 3, tn);
        else role_tok(lo, hi, pos * 4 + idx - 28, w, 0, tn);
    } else {
        tn.p[0] = (int)(unsigned)(uintptr_t)out;
        tn.p[1] = (int)((unsigned)((uintptr_t)out >> 32) & 0xffffu);
        role_helper(lo, hi, (pos - 4) * 8 + (idx & 7), w, idx >> 3, tn);
    }
}

__global__ void __launch_bounds__(256) prep_b(const u16* __restrict__ wqkv, const u16* __restrict__ wphi, const u16* __restrict__ wo, char* ws) {
    __shared__ float red[4][16];
    const int tid = threadIdx.x, w = tid >> 6, lane = tid & 63, g = lane >> 4, m = lane & 15, b = blockIdx.x;
    if (b < 120) {
        const int hg = b / 15, t = b % 15, part = t / 5, h = 4 * hg + (m >> 2), f = 4 * (t % 5) + (m & 3);
        float ph[20], cs = 0.f;
        for (int d = 0; d < 20; ++d) ph[d] = part < 2 ? bf2f(wphi[h * 400 + d * 20 + f]) : (d == f ? 1.f : 0.f);
        for (int kk = w; kk < 20; kk += 4) {
            float v[8];
            for (int j = 0; j < 8; ++j) {
                const u16* src = wqkv + (size_t)(32 * kk + 16 * (j >> 2) + 4 * g + (j & 3)) * 1920 + part * 640 + h * 20;
                float a = 0.f;
                for (int d = 0; d < 5; ++d) { const u2 q = *(const u2*)(src + 4 * d); a += asf(q.x << 16) * ph[4 * d] + asf(q.x & 0xffff0000u) * ph[4 * d + 1] + asf(q.y << 16) * ph[4 * d + 2] + asf(q.y & 0xffff0000u) * ph[4 * d + 3]; }
                v[j] = a;
            }
            const u4 o = {pk2(v[0], v[1]), pk2(v[2], v[3]), pk2(v[4], v[5]), pk2(v[6], v[7])};
            for (int q = 0; q < 4; ++q) cs += asf(o[q] << 16) + asf(o[q] & 0xffff0000u);
            *(u4*)(ws + OFF_WB + (size_t)((hg * 20 + kk) * 15 + t) * 1024 + lane * 16) = o;
        }
        cs = xadd32(xadd16(cs));
        if (lane < 16) red[w][lane] = cs;
        __syncthreads();
        if (tid < 16) *(float*)(ws + OFF_CS + (b * 16 + tid) * 4) = (red[0][tid] + red[1][tid]) + (red[2][tid] + red[3][tid]);
    } else {
        const int ct = b - 120, c = 32 * (ct >> 1) + dperm(m >> 2, 4 * (ct & 1) + (m & 3));
        for (int kq = w; kq < 20; kq += 4) {
            unsigned v[8];
            for (int j = 0; j < 8; ++j) { const int idx = 8 * (kq % 5) + j; v[j] = wo[(size_t)((4 * (2 * (kq / 5) + idx / 20) + g) * 20 + idx % 20) * 640 + c]; }
            *(u4*)(ws + OFF_WO2 + (size_t)((ct / 10) * 200 + kq * 10 + ct % 10) * 1024 + lane * 16) = (u4){v[0] | v[1] << 16, v[2] | v[3] << 16, v[4] | v[5] << 16, v[6] | v[7] << 16};
        }
    }
}

extern "C" void kernel_launch(void* const* d_in, const int* in_sizes, int n_in, void* d_out, int out_size, void* d_ws, size_t ws_size, hipStream_t stream) {
    const u16* x = (const u16*)d_in[0];
    const u16* mem0 = (const u16*)d_in[1];
    const u16 *Wq = (const u16*)d_in[2], *Wk = (const u16*)d_in[3], *Wv = (const u16*)d_in[4], *Wo = (const u16*)d_in[5];
    const u16 *wqkv = (const u16*)d_in[8], *wphi = (const u16*)d_in[9], *wo = (const u16*)d_in[10];
    char* ws = (char*)(((uintptr_t)d_ws + 0x1FFFFF) & ~(uintptr_t)0x1FFFFF);
    (void)hipFuncSetAttribute((const void*)mega, hipFuncAttributeMaxDynamicSharedMemorySize, LDS_BYTES);
    Tune tn = {{50,123,30,72,22,50,90,20}};
    prep_w<<<960 + 192, 256, 0, stream>>>(Wq, Wk, Wv, Wo, ws);
    gemm_b<<<640, 256, 0, stream>>>(Wv, ws);
    prep_b<<<160, 256, 0, stream>>>(wqkv, wphi, wo, ws);
    ln_xf<<<128 + 65, 256, 0, stream>>>(x, mem0, ws, 0);
    mega<<<256, 256, LDS_BYTES, stream>>>(ws, (u16*)d_out, tn);
}
```

```cpp
#include <hip/hip_runtime.h>
#include <hip/hip_bf16.h>
#include <stdint.h>

typedef __bf16 bf2 __attribute__((ext_vector_type(2)));
typedef __bf16 bf8 __attribute__((ext_vector_type(8)));
typedef __bf16 bf16v __attribute__((ext_vector_type(16)));
typedef float f4 __attribute__((ext_vector_type(4)));
typedef unsigned u2 __attribute__((ext_vector_type(2)));
typedef unsigned u4 __attribute__((ext_vector_type(4)));
typedef unsigned u8v __attribute__((ext_vector_type(8)));
typedef short s4 __attribute__((ext_vector_type(4)));
typedef unsigned short u16;
#define LAS __attribute__((address_space(3)))
#define DEV __device__ __forceinline__
typedef __amdgpu_buffer_rsrc_t rsrc_t;

constexpr unsigned SENT = 0xFFFFFFFFu;
constexpr int SC1 = 16;
constexpr unsigned SPIN_CAP = 1u << 16;

constexpr unsigned RING = 4u;
constexpr unsigned MCOPY = 4u;
constexpr unsigned PSLOT = 49152u;
constexpr unsigned MSLOT = 32768u;
constexpr unsigned OFF_PRING = 0u;
constexpr unsigned OFF_MRING = 786432u;
constexpr unsigned OFF_SCHED = 1310720u;
constexpr unsigned RING_BYTES = 1441792u;
constexpr unsigned OFF_PROBE = 2031616u;
constexpr unsigned OFF_FLAGS = 2097152u;
constexpr unsigned FL_CLAIM = 0u;
constexpr unsigned FL_ABORT = 1024u;
constexpr unsigned FL_NEAR = 425856u;
constexpr unsigned FL_VX = 2048u;
constexpr unsigned FL_KM = 6144u;
constexpr unsigned FL_CK = 10240u;
constexpr unsigned FL_CU = 26880u;
constexpr unsigned FL_CX = 43520u;
constexpr unsigned FL_CT = 60416u;
constexpr unsigned FL_CD = 257024u;
constexpr unsigned FL_CB = 265216u;
constexpr unsigned FL_CR = 267264u;
constexpr unsigned FL_TK = 267520u;
constexpr unsigned FL_BG = 267776u;
constexpr unsigned FL_RP = 268800u;
constexpr unsigned FL_END = 401920u;
constexpr unsigned OFF_XF = 2523136u;
constexpr unsigned OFF_KX = 7766016u;
constexpr unsigned OFF_VX = 20348928u;
constexpr unsigned OFF_UX = 32964608u;
constexpr unsigned OFF_BT = 64421888u;
constexpr unsigned OFF_VO = 116850688u;
constexpr unsigned OFF_KM = 127336448u;
constexpr unsigned OFF_PTX = 130482176u;
constexpr unsigned OFF_PTM = 132579328u;
constexpr unsigned OFF_XA = 133103616u;
constexpr unsigned OFF_XB = 138346496u;
constexpr unsigned OFF_XR = 143589376u;
constexpr unsigned OFF_WB = 146210816u;
constexpr unsigned OFF_CS = 148668416u;
constexpr unsigned OFF_CSP = 148676608u;
constexpr unsigned OFF_WO2 = 148758528u;
constexpr unsigned OFF_PHI = 149577728u;
constexpr unsigned OFF_SLAB = 154820608u;
constexpr unsigned OFF_KVC = 161112064u;
constexpr unsigned OFF_PROF = 161177600u;
constexpr unsigned WS_BYTES = 163274752u;
constexpr unsigned OOB = 2147483632u;
constexpr unsigned NSEG = 64u;

DEV unsigned ringphys(unsigned o) { return ((o >> 13) << 14) | (o & 8191); }
DEV unsigned pk2(float a, float b) { bf2 v = {(__bf16)a, (__bf16)b}; return __builtin_bit_cast(unsigned, v); }
DEV float asf(unsigned v) { return __builtin_bit_cast(float, v); }
DEV float bf2f(u16 v) { return __builtin_bit_cast(float, (unsigned)v << 16); }
DEV f4 mfma(u4 a, u4 b, f4 c) { return __builtin_amdgcn_mfma_f32_16x16x32_bf16(__builtin_bit_cast(bf8, a), __builtin_bit_cast(bf8, b), c, 0, 0, 0); }
DEV f4 smfmac(u4 a, u8v b, f4 c, int idx) { return __builtin_amdgcn_smfmac_f32_16x16x64_bf16(__builtin_bit_cast(bf8, a), __builtin_bit_cast(bf16v, b), c, idx, 0, 0); }
DEV u4 ld16(rsrc_t r, unsigned v, unsigned s) { return __builtin_amdgcn_raw_buffer_load_b128(r, v, s, SC1); }
DEV u4 ld16p(rsrc_t r, unsigned v, unsigned s) { return __builtin_amdgcn_raw_buffer_load_b128(r, v, s, 0); }
DEV u2 ld8(rsrc_t r, unsigned v, unsigned s) { return __builtin_amdgcn_raw_buffer_load_b64(r, v, s, SC1); }
DEV unsigned ld4(rsrc_t r, unsigned v, unsigned s) { return __builtin_amdgcn_raw_buffer_load_b32(r, v, s, SC1); }
DEV void st16(u4 d, rsrc_t r, unsigned v, unsigned s) { __builtin_amdgcn_raw_buffer_store_b128(d, r, v, s, SC1); asm volatile("s_nop 1" :: "v"(d) : "memory"); }
DEV void st8(u2 d, rsrc_t r, unsigned v, unsigned s) { __builtin_amdgcn_raw_buffer_store_b64(d, r, v, s, SC1); }
DEV void st4(unsigned d, rsrc_t r, unsigned v, unsigned s) { __builtin_amdgcn_raw_buffer_store_b32(d, r, v, s, SC1); }
DEV u8v cat(u4 a, u4 b) { return __builtin_shufflevector(a, b, 0, 1, 2, 3, 4, 5, 6, 7); }
DEV unsigned umax3(unsigned a, unsigned b, unsigned c) { return max(max(a, b), c); }
DEV unsigned umax4(u4 v, unsigned m) { return umax3(umax3(v.x, v.y, v.z), v.w, m); }
DEV void memfence() { asm volatile("" ::: "memory"); __builtin_amdgcn_sched_barrier(0); }
DEV void drain() { asm volatile("s_waitcnt vmcnt(0)" ::: "memory"); }
DEV void bar() { asm volatile("s_waitcnt lgkmcnt(0)\n\ts_barrier" ::: "memory"); }
DEV unsigned xcc_id() { return (unsigned)__builtin_amdgcn_s_getreg(20 | (3 << 11)) & 0xfu; }
DEV float fold32(float x, float y) {
    auto r = __builtin_amdgcn_permlane32_swap(__builtin_bit_cast(unsigned, x), __builtin_bit_cast(unsigned, y), false, false);
    return asf(r[0]) + asf(r[1]);
}
DEV u2 trread(LAS char* p) { return __builtin_bit_cast(u2, __builtin_amdgcn_ds_read_tr16_b64_v4i16((LAS s4*)p)); }
template <int CTRL, int RM, int BM> DEV float dppf(float old, float v) {
    return __builtin_bit_cast(float, __builtin_amdgcn_update_dpp(__builtin_bit_cast(int, old), __builtin_bit_cast(int, v), CTRL, RM, BM, false));
}
DEV float xmax16(float v) { auto r = __builtin_amdgcn_permlane16_swap(__builtin_bit_cast(unsigned, v), __builtin_bit_cast(unsigned, v), false, false); return fmaxf(asf(r[0]), asf(r[1])); }
DEV float xmax32(float v) { auto r = __builtin_amdgcn_permlane32_swap(__builtin_bit_cast(unsigned, v), __builtin_bit_cast(unsigned, v), false, false); return fmaxf(asf(r[0]), asf(r[1])); }
DEV float xadd16(float v) { auto r = __builtin_amdgcn_permlane16_swap(__builtin_bit_cast(unsigned, v), __builtin_bit_cast(unsigned, v), false, false); return asf(r[0]) + asf(r[1]); }
DEV float xadd32(float v) { auto r = __builtin_amdgcn_permlane32_swap(__builtin_bit_cast(unsigned, v), __builtin_bit_cast(unsigned, v), false, false); return asf(r[0]) + asf(r[1]); }

DEV int dperm(int g, int j) { return 16 * (g >> 1) + 8 * ((j >> 1) & 1) + 4 * (g & 1) + 2 * (j >> 2) + (j & 1); }

DEV void prep_ln(const u16* __restrict__ x, char* ws, LAS char* lds, int b, int w, int lane) {
    const int m = w * 4 + (lane >> 4), k = lane & 15;
    float v[5][8], sum = 0.f, var = 0.f;
#pragma unroll
    for (int j = 0; j < 5; ++j) {
        const u4 q = *(const u4*)(x + (size_t)(b * 16 + m) * 640 + (j * 16 + k) * 8);
#pragma unroll
        for (int e = 0; e < 4; ++e) { const unsigned d = q[e]; v[j][2 * e] = asf(d << 16); v[j][2 * e + 1] = asf(d & 0xffff0000u); sum += v[j][2 * e] + v[j][2 * e + 1]; }
    }
    for (int o = 8; o; o >>= 1) sum += __shfl_xor(sum, o);
    const float mean = sum * (1.f / 640.f);
#pragma unroll
    for (int j = 0; j < 5; ++j)
#pragma unroll
        for (int e = 0; e < 8; ++e) { const float d = v[j][e] - mean; var += d * d; }
    for (int o = 8; o; o >>= 1) var += __shfl_xor(var, o);
    const float rs = rsqrtf(var * (1.f / 640.f) + 1e-5f);
#pragma unroll
    for (int j = 0; j < 5; ++j) {
        f4* xa = (f4*)(ws + OFF_XA + ((size_t)(b * 160 + (j * 16 + k) * 2) * 16 + m) * 16);
        xa[0] = (f4){v[j][0], v[j][1], v[j][2], v[j][3]}; xa[16] = (f4){v[j][4], v[j][5], v[j][6], v[j][7]};
        *(LAS u4*)(lds + m * 1296 + (j * 16 + k) * 16) = (u4){pk2((v[j][0] - mean) * rs, (v[j][1] - mean) * rs), pk2((v[j][2] - mean) * rs, (v[j][3] - mean) * rs),
                                                              pk2((v[j][4] - mean) * rs, (v[j][5] - mean) * rs), pk2((v[j][6] - mean) * rs, (v[j][7] - mean) * rs)};
    }
    bar();
    const int g = lane >> 4;
#pragma unroll
    for (int q = 0; q < 5; ++q) {
        const int kk = w * 5 + q;
        LAS char* row = lds + k * 1296 + (32 * kk + 16 * (g >> 1) + 4 * (g & 1)) * 2;
        const u2 a = *(LAS u2*)row, c = *(LAS u2*)(row + 16);
        *(u4*)(ws + OFF_XF + (size_t)(b * 20 + kk) * 1024 + lane * 16) = (u4){a.x, c.x, a.y, c.y};
    }
}
DEV void prep_wb(const u16* __restrict__ wqkv, const u16* __restrict__ wphi, char* ws, LAS char* lds, int b, int w, int lane) {
    const int q = b % 10, part = (b / 10) % 3, hg = b / 30, h = 4 * hg + w;
    LAS float* ph = (LAS float*)(lds + 16384) + w * 400;
    for (int e = lane; e < 400; e += 64) ph[e] = part < 2 ? bf2f(wphi[h * 400 + e]) : (e / 20 == e % 20 ? 1.f : 0.f);
    const u16* src = wqkv + (size_t)(64 * q + lane) * 1920 + part * 640 + h * 20;
    float in[20], o[20];
#pragma unroll
    for (int d = 0; d < 5; ++d) { const u2 t = *(const u2*)(src + 4 * d); in[4 * d] = asf(t.x << 16); in[4 * d + 1] = asf(t.x & 0xffff0000u); in[4 * d + 2] = asf(t.y << 16); in[4 * d + 3] = asf(t.y & 0xffff0000u); }
#pragma unroll
    for (int f = 0; f < 20; ++f) o[f] = 0.f;
#pragma unroll
    for (int d = 0; d < 20; ++d)
#pragma unroll
        for (int f = 0; f < 20; f += 4) { const f4 p = *(LAS f4*)(ph + d * 20 + f); o[f] += in[d] * p[0]; o[f + 1] += in[d] * p[1]; o[f + 2] += in[d] * p[2]; o[f + 3] += in[d] * p[3]; }
#pragma unroll
    for (int f = 0; f < 20; ++f) *(LAS u16*)(lds + (((f >> 2) * 16 + w * 4 + (f & 3)) * 72 + lane) * 2) = (u16)pk2(o[f], 0.f);
    bar();
    const int g = lane >> 4, m = lane & 15;
    for (int t = w; t < 5; t += 4) {
        float cs = 0.f;
#pragma unroll
        for (int u = 0; u < 2; ++u) {
            LAS char* row = lds + ((t * 16 + m) * 72 + 32 * u + 4 * g) * 2;
            const u2 a = *(LAS u2*)row, c = *(LAS u2*)(row + 32);
            cs += (asf(a.x << 16) + asf(a.x & 0xffff0000u)) + (asf(a.y << 16) + asf(a.y & 0xffff0000u)) + (asf(c.x << 16) + asf(c.x & 0xffff0000u)) + (asf(c.y << 16) + asf(c.y & 0xffff0000u));
            *(u4*)(ws + OFF_WB + (size_t)((hg * 20 + 2 * q + u) * 15 + part * 5 + t) * 1024 + lane * 16) = (u4){a.x, a.y, c.x, c.y};
        }
        cs = xadd32(xadd16(cs));
        if (lane < 16) *(float*)(ws + OFF_CSP + q * 8192 + ((hg * 15 + part * 5 + t) * 16 + lane) * 4) = cs;
    }
}
constexpr int PB_INIT = 1, PB_LN = PB_INIT + 71, PB_WB = PB_LN + 128, PB_WO = PB_WB + 240, PB_END = PB_WO + 200;
__global__ void __launch_bounds__(256, 4) prep(const u16* __restrict__ x, const u16* __restrict__ mem0, const u16* __restrict__ wqkv, const u16* __restrict__ wphi, const u16* __restrict__ wo, char* ws) {
    __shared__ __attribute__((aligned(16))) char smem[22784];
    LAS char* lds = (LAS char*)smem;
    const int tid = threadIdx.x, w = __builtin_amdgcn_readfirstlane(tid >> 6), lane = tid & 63, b = blockIdx.x;
    rsrc_t r = __builtin_amdgcn_make_buffer_rsrc(ws, 0, WS_BYTES, 0x00020000);
    if (b < PB_INIT) {
        if (w) return;
        unsigned best[2] = {1u << 30, 1u << 30};
        for (int rep = 0; rep < 6; ++rep) for (int c = 0; c < 2; ++c) {
            drain();
            const unsigned long long t0 = __builtin_amdgcn_s_memtime();
            asm volatile("s_waitcnt lgkmcnt(0)" ::: "memory");
            st16((u4){0, 0, 0, 0}, r, lane < 8 ? lane * 16 : OOB, OFF_PROBE + c * 8192 + (rep & 1) * 4096);
            drain();
            best[c] = min(best[c], (unsigned)(__builtin_amdgcn_s_memtime() - t0));
        }
        st4((xcc_id() >> 2) ^ (best[0] > best[1] ? 1u : 0u), r, lane == 0 ? 0 : OOB, OFF_FLAGS + FL_NEAR);
    } else if (b < PB_LN) {
        const int q = b - PB_INIT, n = PB_LN - PB_INIT;
        for (unsigned lo = (q * 256 + tid) * 16; lo < RING_BYTES / 2; lo += n * 4096) {
            const unsigned o = ringphys(lo), p = (lo - OFF_MRING / 2) % (MSLOT / 2);
            u4 v = {SENT, SENT, SENT, SENT};
            if (o >= OFF_MRING && o < OFF_MRING + MCOPY * MSLOT && p < 10240) v = *(const u4*)(mem0 + ((p >> 4) & 7) * 640 + (p >> 7) * 8);
            st16(v, r, o, 0);
        }
        for (unsigned o = (q * 256 + tid) * 16; o < FL_END; o += n * 4096) st16((u4){(o >= FL_CX && o < FL_CX + 64 * 128) ? 1u : 0u, 0, 0, 0}, r, o, OFF_FLAGS);
    } else if (b < PB_WB) prep_ln(x, ws, lds, b - PB_LN, w, lane);
    else if (b < PB_WO) prep_wb(wqkv, wphi, ws, lds, b - PB_WB, w, lane);
    else {
        const int f = (b - PB_WO) * 4 + w, ct = (f / 200) * 10 + f % 10, kq = (f % 200) / 10, g = lane >> 4, m = lane & 15;
        const int c = 32 * (ct >> 1) + dperm(m >> 2, 4 * (ct & 1) + (m & 3));
        unsigned v[8];
#pragma unroll
        for (int j = 0; j < 8; ++j) { const int idx = 8 * (kq % 5) + j; v[j] = wo[(size_t)((4 * (2 * (kq / 5) + idx / 20) + g) * 20 + idx % 20) * 640 + c]; }
        *(u4*)(ws + OFF_WO2 + (size_t)f * 1024 + lane * 16) = (u4){v[0] | v[1] << 16, v[2] | v[3] << 16, v[4] | v[5] << 16, v[6] | v[7] << 16};
    }
}

constexpr int LDS_BYTES = 152 * 1024 + 64;
struct Tune { int p[8]; };
DEV unsigned plo(const void* p) { return (unsigned)(uintptr_t)p; }
DEV unsigned phi(const void* p) { return (unsigned)((uintptr_t)p >> 32) & 0xffffu; }
#define CHAIN_CLOBBERS "v0", "v1", "v2", "v3", "v4", "v5", "v6", "v7", "v8", "v9", "v10", "v11", "v12", "v13", "v14", "v15", "v16", "v17", "v18", "v19", "v20", "v21", "v22", "v23", "v24", "v25", "v26", "v27", "v28", "v29", "v30", "v31", "v32", "v33", "v34", "v35", "v36", "v37", "v38", "v39", "v40", "v41", "v42", "v43", "v44", "v45", "v46", "v47", "v48", "v49", "v50", "v51", "v52", "v53", "v54", "v55", "v56", "v57", "v58", "v59", "v60", "v61", "v62", "v63", "v64", "v65", "v66", "v67", "v68", "v69", "v70", "v71", "v72", "v73", "v74", "v75", "v76", "v77", "v78", "v79", "v80", "v81", "v82", "v83", "v84", "v85", "v86", "v87", "v88", "v89", "v90", "v91", "v92", "v93", "v94", "v95", "v96", "v97", "v98", "v99", "v100", "v101", "v102", "v103", "v104", "v105", "v106", "v107", "v108", "v109", "v110", "v111", "v112", "v113", "v114", "v115", "v116", "v117", "v118", "v119", "v120", "v121", "v122", "v123", "v124", "v125", "v126", "v127", "v128", "v129", "v130", "v131", "v132", "v133", "v134", "v135", "v136", "v137", "v138", "v139", "v140", "v141", "v142", "v143", "v144", "v145", "v146", "v147", "v148", "v149", "v150", "v151", "v152", "v153", "v154", "v155", "v156", "v157", "v158", "v159", "v160", "v161", "v162", "v163", "v164", "v165", "v166", "v167", "v168", "v169", "v170", "v171", "v172", "v173", "v174", "v175", "v176", "v177", "v178", "v179", "v180", "v181", "v182", "v183", "v184", "v185", "v186", "v187", "v188", "v189", "v190", "v191", "v192", "v193", "v194", "v195", "v196", "v197", "v198", "v199", "v200", "v201", "v202", "v203", "v204", "v205", "v206", "v207", "v208", "v209", "v210", "v211", "v212", "v213", "v214", "v215", "v216", "v217", "v218", "v219", "v220", "v221", "v222", "v223", "v224", "v225", "v226", "v227", "v228", "v229", "v230", "v231", "v232", "v233", "v234", "v235", "v236", "v237", "v238", "v239", "v240", "v241", "v242", "v243", "v244", "v245", "v246", "v247", "v248", "v249", "v250", "v251", "v252", "v253", "v254", "v255", "a0", "a1", "a2", "a3", "a4", "a5", "a6", "a7", "a8", "a9", "a10", "a11", "a12", "a13", "a14", "a15", "a16", "a17", "a18", "a19", "a20", "a21", "a22", "a23", "a24", "a25", "a26", "a27", "a28", "a29", "a30", "a31", "a32", "a33", "a34", "a35", "a36", "a37", "a38", "a39", "a40", "a41", "a42", "a43", "a44", "a45", "a46", "a47", "a48", "a49", "a50", "a51", "a52", "a53", "a54", "a55", "a56", "a57", "a58", "a59", "a60", "a61", "a62", "a63", "a64", "a65", "a66", "a67", "a68", "a69", "a70", "a71", "a72", "a73", "a74", "a75", "a76", "a77", "a78", "a79", "a80", "a81", "a82", "a83", "a84", "a85", "a86", "a87", "a88", "a89", "a90", "a91", "a92", "a93", "a94", "a95", "a96", "a97", "a98", "a99", "a100", "a101", "a102", "a103", "a104", "a105", "a106", "a107", "a108", "a109", "a110", "a111", "a112", "a113", "a114", "a115", "a116", "a117", "a118", "a119", "a120", "a121", "a122", "a123", "a124", "a125", "a126", "a127", "a128", "a129", "a130", "a131", "a132", "a133", "a134", "a135", "a136", "a137", "a138", "a139", "a140", "a141", "a142", "a143", "a144", "a145", "a146", "a147", "a148", "a149", "a150", "a151", "a152", "a153", "a154", "a155", "a156", "a157", "a158", "a159", "a160", "a161", "a162", "a163", "a164", "a165", "a166", "a167", "a168", "a169", "a170", "a171", "a172", "a173", "a174", "a175", "a176", "a177", "a178", "a179", "a180", "a181", "a182", "a183", "a184", "a185", "a186", "a187", "a188", "a189", "a190", "a191", "a192", "a193", "a194", "a195", "a196", "a197", "a198", "a199", "a200", "a201", "a202", "a203", "a204", "a205", "a206", "a207", "a208", "a209", "a210", "a211", "a212", "a213", "a214", "a215", "a216", "a217", "a218", "a219", "a220", "a221", "a222", "a223", "a224", "a225", "a226", "a227", "a228", "a229", "a230", "a231", "a232", "a233", "a234", "a235", "a236", "a237", "a238", "a239", "a240", "a241", "a242", "a243", "a244", "a245", "a246", "a247", "a248", "a249", "a250", "a251", "a252", "a253", "a254", "a255", "s40", "s41", "s42", "s43", "s44", "s45", "s46", "s47", "s66", "s67", "s68", "s69", "s70", "s71", "s72", "s73", "s76", "s77", "s78", "s79", "s80", "s81", "s82", "s83", "s84", "s99", "vcc", "scc", "memory"
#define SRD_INPUTS "s"(lo), "s"(hi), "s"(WS_BYTES), "s"(0x00020000u), "s"(id), "s"(w), "s"(aux)
#define CHAIN_INPUTS SRD_INPUTS, "s"(tn.p[0]), "s"(tn.p[1]), "s"(tn.p[2]), "s"(tn.p[3]), "s"(tn.p[4]), "s"(tn.p[5]), "s"(tn.p[6]), "s"(tn.p[7]), "s"(plo(wa)), "s"(phi(wa)), "s"(plo(wb)), "s"(phi(wb)), "s"(bid)
DEV void role_head(unsigned lo, unsigned hi, int id, int w, int aux, const Tune& tn, const void* wa, const void* wb, int bid) {
    asm volatile(
    "s_mov_b32 s40, %0\n"
    "s_mov_b32 s41, %1\n"
    "s_mov_b32 s42, %2\n"
    "s_mov_b32 s43, %3\n"
    "s_mov_b32 s44, %4\n"
    "s_mov_b32 s45, %5\n"
    "s_mov_b32 s46, %6\n"
    "s_mov_b32 s66, %7\n"
    "s_mov_b32 s67, %8\n"
    "s_mov_b32 s68, %9\n"
    "s_mov_b32 s69, %10\n"
    "s_mov_b32 s70, %11\n"
    "s_mov_b32 s71, %12\n"
    "s_mov_b32 s72, %13\n"
    "s_mov_b32 s73, %14\n"
    "s_mov_b32 s76, %15\n"
    "s_mov_b32 s77, %16\n"
    "s_mov_b32 s78, 7864320\n"
    "s_mov_b32 s79, 0x20000\n"
    "s_mov_b32 s80, %17\n"
    "s_mov_b32 s81, %18\n"
    "s_mov_b32 s82, 7864320\n"
    "s_mov_b32 s83, 0x20000\n"
    "s_mov_b32 s84, %19\n"
    "s_mov_b64 exec, -1\n"
    "v_mbcnt_lo_u32_b32 v15, -1, 0\n"
    "v_mbcnt_hi_u32_b32 v15, -1, v15\n"
    "v_lshlrev_b32 v0, 4, v15\n"
    "s_mov_b32 s58, 2097152\n"
    "s_mov_b32 s92, 0\n"
    "s_lshl_b32 s48, s45, 15\n"
    "v_add_u32 v2, s48, v0\n"
    "s_mov_b32 s20, 0\n"
    "s_mov_b32 s21, 1024\n"
    "s_mov_b32 s22, 2048\n"
    "s_mov_b32 s23, 3072\n"
    "s_mov_b32 s24, 4096\n"
    "s_mov_b32 s25, 5120\n"
    "s_mov_b32 s26, 6144\n"
    "s_mov_b32 s27, 7168\n"
    "s_mov_b32 s28, 16384\n"
    "s_mov_b32 s29, 17408\n"
    "v_add_u32 v150, 0, v15\n"
    "v_mul_u32_u24 v144, 2731, v150\n"
    "v_lshrrev_b32 v144, 16, v144\n"
    "v_mul_u32_u24 v144, 11904, v144\n"
    "v_lshl_add_u32 v144, v150, 4, v144\n"
    "v_add_u32 v150, 64, v15\n"
    "v_mul_u32_u24 v145, 2731, v150\n"
    "v_lshrrev_b32 v145, 16, v145\n"
    "v_mul_u32_u24 v145, 11904, v145\n"
    "v_lshl_add_u32 v145, v150, 4, v145\n"
    "v_add_u32 v150, 128, v15\n"
    "v_mul_u32_u24 v146, 2731, v150\n"
    "v_lshrrev_b32 v146, 16, v146\n"
    "v_mul_u32_u24 v146, 11904, v146\n"
    "v_lshl_add_u32 v146, v150, 4, v146\n"
    "v_bfe_u32 v147, v15, 2, 1\n"
    "v_and_b32 v148, 8, v15\n"
    "v_add_u32 v147, v147, v148\n"
    "v_bfe_u32 v148, v15, 4, 1\n"
    "v_lshl_add_u32 v147, v148, 2, v147\n"
    "v_lshrrev_b32 v148, 5, v15\n"
    "v_lshl_add_u32 v147, v148, 4, v147\n"
    "v_mul_u32_u24 v147, 384, v147\n"
    "v_and_b32 v148, 3, v15\n"
    "v_lshl_add_u32 v147, v148, 3, v147\n"
    "s_mul_i32 s48, s45, 96\n"
    "v_add_u32 v147, s48, v147\n"
    "v_add_u32 v148, 49152, v147\n"
    "v_add_u32 v149, 98304, v147\n"
    "s_mul_i32 s50, s45, 196608\n"
    "s_mul_i32 s48, s44, 384\n"
    "s_add_u32 s50, s50, s48\n"
    "s_mul_i32 s51, s45, 6144\n"
    "s_add_u32 m0, s51, 0\n"
    "s_add_u32 s49, s50, 0\n"
    "buffer_load_dwordx4 v144, s[76:79], s49 offen lds\n"
    "s_add_u32 m0, s51, 1024\n"
    "s_add_u32 s49, s50, 0\n"
    "buffer_load_dwordx4 v145, s[76:79], s49 offen lds\n"
    "s_add_u32 m0, s51, 2048\n"
    "s_add_u32 s49, s50, 0\n"
    "buffer_load_dwordx4 v146, s[76:79], s49 offen lds\n"
    "s_add_u32 m0, s51, 3072\n"
    "s_add_u32 s49, s50, 98304\n"
    "buffer_load_dwordx4 v144, s[76:79], s49 offen lds\n"
    "s_add_u32 m0, s51, 4096\n"
    "s_add_u32 s49, s50, 98304\n"
    "buffer_load_dwordx4 v145, s[76:79], s49 offen lds\n"
    "s_add_u32 m0, s51, 5120\n"
    "s_add_u32 s49, s50, 98304\n"
    "buffer_load_dwordx4 v146, s[76:79], s49 offen lds\n"
    "s_add_u32 m0, s51, 24576\n"
    "s_add_u32 s49, s50, 0\n"
    "buffer_load_dwordx4 v144, s[80:83], s49 offen lds\n"
    "s_add_u32 m0, s51, 25600\n"
    "s_add_u32 s49, s50, 0\n"
    "buffer_load_dwordx4 v145, s[80:83], s49 offen lds\n"
    "s_add_u32 m0, s51, 26624\n"
    "s_add_u32 s49, s50, 0\n"
    "buffer_load_dwordx4 v146, s[80:83], s49 offen lds\n"
    "s_add_u32 m0, s51, 27648\n"
    "s_add_u32 s49, s50, 98304\n"
    "buffer_load_dwordx4 v144, s[80:83], s49 offen lds\n"
    "s_add_u32 m0, s51, 28672\n"
    "s_add_u32 s49, s50, 98304\n"
    "buffer_load_dwordx4 v145, s[80:83], s49 offen lds\n"
    "s_add_u32 m0, s51, 29696\n"
    "s_add_u32 s49, s50, 98304\n"
    "buffer_load_dwordx4 v146, s[80:83], s49 offen lds\n"
    "s_add_u32 m0, s51, 49152\n"
    "s_add_u32 s49, s50, 786432\n"
    "buffer_load_dwordx4 v144, s[76:79], s49 offen lds\n"
    "s_add_u32 m0, s51, 50176\n"
    "s_add_u32 s49, s50, 786432\n"
    "buffer_load_dwordx4 v145, s[76:79], s49 offen lds\n"
    "s_add_u32 m0, s51, 51200\n"
    "s_add_u32 s49, s50, 786432\n"
    "buffer_load_dwordx4 v146, s[76:79], s49 offen lds\n"
    "s_add_u32 m0, s51, 52224\n"
    "s_add_u32 s49, s50, 884736\n"
    "buffer_load_dwordx4 v144, s[76:79], s49 offen lds\n"
    "s_add_u32 m0, s51, 53248\n"
    "s_add_u32 s49, s50, 884736\n"
    "buffer_load_dwordx4 v145, s[76:79], s49 offen lds\n"
    "s_add_u32 m0, s51, 54272\n"
    "s_add_u32 s49, s50, 884736\n"
    "buffer_load_dwordx4 v146, s[76:79], s49 offen lds\n"
    "s_add_u32 m0, s51, 73728\n"
    "s_add_u32 s49, s50, 786432\n"
    "buffer_load_dwordx4 v144, s[80:83], s49 offen lds\n"
    "s_add_u32 m0, s51, 74752\n"
    "s_add_u32 s49, s50, 786432\n"
    "buffer_load_dwordx4 v145, s[80:83], s49 offen lds\n"
    "s_add_u32 m0, s51, 75776\n"
    "s_add_u32 s49, s50, 786432\n"
    "buffer_load_dwordx4 v146, s[80:83], s49 offen lds\n"
    "s_add_u32 m0, s51, 76800\n"
    "s_add_u32 s49, s50, 884736\n"
    "buffer_load_dwordx4 v144, s[80:83], s49 offen lds\n"
    "s_add_u32 m0, s51, 77824\n"
    "s_add_u32 s49, s50, 884736\n"
    "buffer_load_dwordx4 v145, s[80:83], s49 offen lds\n"
    "s_add_u32 m0, s51, 78848\n"
    "s_add_u32 s49, s50, 884736\n"
    "buffer_load_dwordx4 v146, s[80:83], s49 offen lds\n"
    "s_add_u32 m0, s51, 98304\n"
    "s_add_u32 s49, s50, 1572864\n"
    "buffer_load_dwordx4 v144, s[76:79], s49 offen lds\n"
    "s_add_u32 m0, s51, 99328\n"
    "s_add_u32 s49, s50, 1572864\n"
    "buffer_load_dwordx4 v145, s[76:79], s49 offen lds\n"
    "s_add_u32 m0, s51, 100352\n"
    "s_add_u32 s49, s50, 1572864\n"
    "buffer_load_dwordx4 v146, s[76:79], s49 offen lds\n"
    "s_add_u32 m0, s51, 101376\n"
    "s_add_u32 s49, s50, 1671168\n"
    "buffer_load_dwordx4 v144, s[76:79], s49 offen lds\n"
    "s_add_u32 m0, s51, 102400\n"
    "s_add_u32 s49, s50, 1671168\n"
    "buffer_load_dwordx4 v145, s[76:79], s49 offen lds\n"
    "s_add_u32 m0, s51, 103424\n"
    "s_add_u32 s49, s50, 1671168\n"
    "buffer_load_dwordx4 v146, s[76:79], s49 offen lds\n"
    "s_waitcnt vmcnt(24) lgkmcnt(0)\n"
    "s_barrier\n"
    "s_add_u32 m0, s51, 122880\n"
    "s_add_u32 s49, s50, 1572864\n"
    "buffer_load_dwordx4 v144, s[80:83], s49 offen lds\n"
    "s_add_u32 m0, s51, 123904\n"
    "s_add_u32 s49, s50, 1572864\n"
    "buffer_load_dwordx4 v145, s[80:83], s49 offen lds\n"
    "s_add_u32 m0, s51, 124928\n"
    "s_add_u32 s49, s50, 1572864\n"
    "buffer_load_dwordx4 v146, s[80:83], s49 offen lds\n"
    "s_add_u32 m0, s51, 125952\n"
    "s_add_u32 s49, s50, 1671168\n"
    "buffer_load_dwordx4 v144, s[80:83], s49 offen lds\n"
    "s_add_u32 m0, s51, 126976\n"
    "s_add_u32 s49, s50, 1671168\n"
    "buffer_load_dwordx4 v145, s[80:83], s49 offen lds\n"
    "s_add_u32 m0, s51, 128000\n"
    "s_add_u32 s49, s50, 1671168\n"
    "buffer_load_dwordx4 v146, s[80:83], s49 offen lds\n"
    "ds_read_b64_tr_b16 a[0:1], v147 offset:0\n"
    "ds_read_b64_tr_b16 a[2:3], v147 offset:768\n"
    "ds_read_b64_tr_b16 a[4:5], v147 offset:12288\n"
    "ds_read_b64_tr_b16 a[6:7], v147 offset:13056\n"
    "ds_read_b64_tr_b16 a[8:9], v147 offset:32\n"
    "ds_read_b64_tr_b16 a[10:11], v147 offset:800\n"
    "ds_read_b64_tr_b16 a[12:13], v147 offset:12320\n"
    "ds_read_b64_tr_b16 a[14:15], v147 offset:13088\n"
    "ds_read_b64_tr_b16 a[216:217], v147 offset:64\n"
    "ds_read_b64_tr_b16 a[218:219], v147 offset:832\n"
    "ds_read_b64_tr_b16 a[220:221], v147 offset:12352\n"
    "ds_read_b64_tr_b16 a[222:223], v147 offset:13120\n"
    "s_waitcnt vmcnt(24) lgkmcnt(0)\n"
    "s_barrier\n"
    "s_add_u32 m0, s51, 0\n"
    "s_add_u32 s49, s50, 2359296\n"
    "buffer_load_dwordx4 v144, s[76:79], s49 offen lds\n"
    "s_add_u32 m0, s51, 1024\n"
    "s_add_u32 s49, s50, 2359296\n"
    "buffer_load_dwordx4 v145, s[76:79], s49 offen lds\n"
    "s_add_u32 m0, s51, 2048\n"
    "s_add_u32 s49, s50, 2359296\n"
    "buffer_load_dwordx4 v146, s[76:79], s49 offen lds\n"
    "s_add_u32 m0, s51, 3072\n"
    "s_add_u32 s49, s50, 2457600\n"
    "buffer_load_dwordx4 v144, s[76:79], s49 offen lds\n"
    "s_add_u32 m0, s51, 4096\n"
    "s_add_u32 s49, s50, 2457600\n"
    "buffer_load_dwordx4 v145, s[76:79], s49 offen lds\n"
    "s_add_u32 m0, s51, 5120\n"
    "s_add_u32 s49, s50, 2457600\n"
    "buffer_load_dwordx4 v146, s[76:79], s49 offen lds\n"
    "ds_read_b64_tr_b16 a[16:17], v147 offset:24576\n"
    "ds_read_b64_tr_b16 a[18:19], v147 offset:25344\n"
    "ds_read_b64_tr_b16 a[20:21], v147 offset:36864\n"
    "ds_read_b64_tr_b16 a[22:23], v147 offset:37632\n"
    "ds_read_b64_tr_b16 v[16:17], v147 offset:24608\n"
    "ds_read_b64_tr_b16 v[18:19], v147 offset:25376\n"
    "ds_read_b64_tr_b16 v[20:21], v147 offset:36896\n"
    "ds_read_b64_tr_b16 v[22:23], v147 offset:37664\n"
    "ds_read_b64_tr_b16 v[120:121], v147 offset:24640\n"
    "ds_read_b64_tr_b16 v[122:123], v147 offset:25408\n"
    "ds_read_b64_tr_b16 v[124:125], v147 offset:36928\n"
    "ds_read_b64_tr_b16 v[126:127], v147 offset:37696\n"
    "s_waitcnt vmcnt(24) lgkmcnt(0)\n"
    "s_barrier\n"
    "s_add_u32 m0, s51, 24576\n"
    "s_add_u32 s49, s50, 2359296\n"
    "buffer_load_dwordx4 v144, s[80:83], s49 offen lds\n"
    "s_add_u32 m0, s51, 25600\n"
    "s_add_u32 s49, s50, 2359296\n"
    "buffer_load_dwordx4 v145, s[80:83], s49 offen lds\n"
    "s_add_u32 m0, s51, 26624\n"
    "s_add_u32 s49, s50, 2359296\n"
    "buffer_load_dwordx4 v146, s[80:83], s49 offen lds\n"
    "s_add_u32 m0, s51, 27648\n"
    "s_add_u32 s49, s50, 2457600\n"
    "buffer_load_dwordx4 v144, s[80:83], s49 offen lds\n"
    "s_add_u32 m0, s51, 28672\n"
    "s_add_u32 s49, s50, 2457600\n"
    "buffer_load_dwordx4 v145, s[80:83], s49 offen lds\n"
    "s_add_u32 m0, s51, 29696\n"
    "s_add_u32 s49, s50, 2457600\n"
    "buffer_load_dwordx4 v146, s[80:83], s49 offen lds\n"
    "ds_read_b64_tr_b16 a[24:25], v148 offset:0\n"
    "ds_read_b64_tr_b16 a[26:27], v148 offset:768\n"
    "ds_read_b64_tr_b16 a[28:29], v148 offset:12288\n"
    "ds_read_b64_tr_b16 a[30:31], v148 offset:13056\n"
    "ds_read_b64_tr_b16 v[32:33], v148 offset:32\n"
    "ds_read_b64_tr_b16 v[34:35], v148 offset:800\n"
    "ds_read_b64_tr_b16 v[36:37], v148 offset:12320\n"
    "ds_read_b64_tr_b16 v[38:39], v148 offset:13088\n"
    "ds_read_b64_tr_b16 a[224:225], v148 offset:64\n"
    "ds_read_b64_tr_b16 a[226:227], v148 offset:832\n"
    "ds_read_b64_tr_b16 a[228:229], v148 offset:12352\n"
    "ds_read_b64_tr_b16 a[230:231], v148 offset:13120\n"
    "s_waitcnt vmcnt(24) lgkmcnt(0)\n"
    "s_barrier\n"
    "s_add_u32 m0, s51, 49152\n"
    "s_add_u32 s49, s50, 3145728\n"
    "buffer_load_dwordx4 v144, s[76:79], s49 offen lds\n"
    "s_add_u32 m0, s51, 50176\n"
    "s_add_u32 s49, s50, 3145728\n"
    "buffer_load_dwordx4 v145, s[76:79], s49 offen lds\n"
    "s_add_u32 m0, s51, 51200\n"
    "s_add_u32 s49, s50, 3145728\n"
    "buffer_load_dwordx4 v146, s[76:79], s49 offen lds\n"
    "s_add_u32 m0, s51, 52224\n"
    "s_add_u32 s49, s50, 3244032\n"
    "buffer_load_dwordx4 v144, s[76:79], s49 offen lds\n"
    "s_add_u32 m0, s51, 53248\n"
    "s_add_u32 s49, s50, 3244032\n"
    "buffer_load_dwordx4 v145, s[76:79], s49 offen lds\n"
    "s_add_u32 m0, s51, 54272\n"
    "s_add_u32 s49, s50, 3244032\n"
    "buffer_load_dwordx4 v146, s[76:79], s49 offen lds\n"
    "ds_read_b64_tr_b16 a[32:33], v148 offset:24576\n"
    "ds_read_b64_tr_b16 a[34:35], v148 offset:25344\n"
    "ds_read_b64_tr_b16 a[36:37], v148 offset:36864\n"
    "ds_read_b64_tr_b16 a[38:39], v148 offset:37632\n"
    "ds_read_b64_tr_b16 v[24:25], v148 offset:24608\n"
    "ds_read_b64_tr_b16 v[26:27], v148 offset:25376\n"
    "ds_read_b64_tr_b16 v[28:29], v148 offset:36896\n"
    "ds_read_b64_tr_b16 v[30:31], v148 offset:37664\n"
    "ds_read_b64_tr_b16 v[128:129], v148 offset:24640\n"
    "ds_read_b64_tr_b16 v[130:131], v148 offset:25408\n"
    "ds_read_b64_tr_b16 v[132:133], v148 offset:36928\n"
    "ds_read_b64_tr_b16 v[134:135], v148 offset:37696\n"
    "s_waitcnt vmcnt(24) lgkmcnt(0)\n"
    "s_barrier\n"
    "s_add_u32 m0, s51, 73728\n"
    "s_add_u32 s49, s50, 3145728\n"
    "buffer_load_dwordx4 v144, s[80:83], s49 offen lds\n"
    "s_add_u32 m0, s51, 74752\n"
    "s_add_u32 s49, s50, 3145728\n"
    "buffer_load_dwordx4 v145, s[80:83], s49 offen lds\n"
    "s_add_u32 m0, s51, 75776\n"
    "s_add_u32 s49, s50, 3145728\n"
    "buffer_load_dwordx4 v146, s[80:83], s49 offen lds\n"
    "s_add_u32 m0, s51, 76800\n"
    "s_add_u32 s49, s50, 3244032\n"
    "buffer_load_dwordx4 v144, s[80:83], s49 offen lds\n"
    "s_add_u32 m0, s51, 77824\n"
    "s_add_u32 s49, s50, 3244032\n"
    "buffer_load_dwordx4 v145, s[80:83], s49 offen lds\n"
    "s_add_u32 m0, s51, 78848\n"
    "s_add_u32 s49, s50, 3244032\n"
    "buffer_load_dwordx4 v146, s[80:83], s49 offen lds\n"
    "ds_read_b64_tr_b16 a[40:41], v149 offset:0\n"
    "ds_read_b64_tr_b16 a[42:43], v149 offset:768\n"
    "ds_read_b64_tr_b16 a[44:45], v149 offset:12288\n"
    "ds_read_b64_tr_b16 a[46:47], v149 offset:13056\n"
    "ds_read_b64_tr_b16 a[48:49], v149 offset:32\n"
    "ds_read_b64_tr_b16 a[50:51], v149 offset:800\n"
    "ds_read_b64_tr_b16 a[52:53], v149 offset:12320\n"
    "ds_read_b64_tr_b16 a[54:55], v149 offset:13088\n"
    "ds_read_b64_tr_b16 a[232:233], v149 offset:64\n"
    "ds_read_b64_tr_b16 a[234:235], v149 offset:832\n"
    "ds_read_b64_tr_b16 a[236:237], v149 offset:12352\n"
    "ds_read_b64_tr_b16 a[238:239], v149 offset:13120\n"
    "s_waitcnt vmcnt(24) lgkmcnt(0)\n"
    "s_barrier\n"
    "s_add_u32 m0, s51, 98304\n"
    "s_add_u32 s49, s50, 3932160\n"
    "buffer_load_dwordx4 v144, s[76:79], s49 offen lds\n"
    "s_add_u32 m0, s51, 99328\n"
    "s_add_u32 s49, s50, 3932160\n"
    "buffer_load_dwordx4 v145, s[76:79], s49 offen lds\n"
    "s_add_u32 m0, s51, 100352\n"
    "s_add_u32 s49, s50, 3932160\n"
    "buffer_load_dwordx4 v146, s[76:79], s49 offen lds\n"
    "s_add_u32 m0, s51, 101376\n"
    "s_add_u32 s49, s50, 4030464\n"
    "buffer_load_dwordx4 v144, s[76:79], s49 offen lds\n"
    "s_add_u32 m0, s51, 102400\n"
    "s_add_u32 s49, s50, 4030464\n"
    "buffer_load_dwordx4 v145, s[76:79], s49 offen lds\n"
    "s_add_u32 m0, s51, 103424\n"
    "s_add_u32 s49, s50, 4030464\n"
    "buffer_load_dwordx4 v146, s[76:79], s49 offen lds\n"
    "ds_read_b64_tr_b16 a[56:57], v149 offset:24576\n"
    "ds_read_b64_tr_b16 a[58:59], v149 offset:25344\n"
    "ds_read_b64_tr_b16 a[60:61], v149 offset:36864\n"
    "ds_read_b64_tr_b16 a[62:63], v149 offset:37632\n"
    "ds_read_b64_tr_b16 v[40:41], v149 offset:24608\n"
    "ds_read_b64_tr_b16 v[42:43], v149 offset:25376\n"
    "ds_read_b64_tr_b16 v[44:45], v149 offset:36896\n"
    "ds_read_b64_tr_b16 v[46:47], v149 offset:37664\n"
    "ds_read_b64_tr_b16 v[136:137], v149 offset:24640\n"
    "ds_read_b64_tr_b16 v[138:139], v149 offset:25408\n"
    "ds_read_b64_tr_b16 v[140:141], v149 offset:36928\n"
    "ds_read_b64_tr_b16 v[142:143], v149 offset:37696\n"
    "s_waitcnt vmcnt(24) lgkmcnt(0)\n"
    "s_barrier\n"
    "s_add_u32 m0, s51, 122880\n"
    "s_add_u32 s49, s50, 3932160\n"
    "buffer_load_dwordx4 v144, s[80:83], s49 offen lds\n"
    "s_add_u32 m0, s51, 123904\n"
    "s_add_u32 s49, s50, 3932160\n"
    "buffer_load_dwordx4 v145, s[80:83], s49 offen lds\n"
    "s_add_u32 m0, s51, 124928\n"
    "s_add_u32 s49, s50, 3932160\n"
    "buffer_load_dwordx4 v146, s[80:83], s49 offen lds\n"
    "s_add_u32 m0, s51, 125952\n"
    "s_add_u32 s49, s50, 4030464\n"
    "buffer_load_dwordx4 v144, s[80:83], s49 offen lds\n"
    "s_add_u32 m0, s51, 126976\n"
    "s_add_u32 s49, s50, 4030464\n"
    "buffer_load_dwordx4 v145, s[80:83], s49 offen lds\n"
    "s_add_u32 m0, s51, 128000\n"
    "s_add_u32 s49, s50, 4030464\n"
    "buffer_load_dwordx4 v146, s[80:83], s49 offen lds\n"
    "ds_read_b64_tr_b16 a[64:65], v147 offset:0\n"
    "ds_read_b64_tr_b16 a[66:67], v147 offset:768\n"
    "ds_read_b64_tr_b16 a[68:69], v147 offset:12288\n"
    "ds_read_b64_tr_b16 a[70:71], v147 offset:13056\n"
    "ds_read_b64_tr_b16 a[72:73], v147 offset:32\n"
    "ds_read_b64_tr_b16 a[74:75], v147 offset:800\n"
    "ds_read_b64_tr_b16 a[76:77], v147 offset:12320\n"
    "ds_read_b64_tr_b16 a[78:79], v147 offset:13088\n"
    "ds_read_b64_tr_b16 a[240:241], v147 offset:64\n"
    "ds_read_b64_tr_b16 a[242:243], v147 offset:832\n"
    "ds_read_b64_tr_b16 a[244:245], v147 offset:12352\n"
    "ds_read_b64_tr_b16 a[246:247], v147 offset:13120\n"
    "s_waitcnt vmcnt(24) lgkmcnt(0)\n"
    "s_barrier\n"
    "s_add_u32 m0, s51, 0\n"
    "s_add_u32 s49, s50, 4718592\n"
    "buffer_load_dwordx4 v144, s[76:79], s49 offen lds\n"
    "s_add_u32 m0, s51, 1024\n"
    "s_add_u32 s49, s50, 4718592\n"
    "buffer_load_dwordx4 v145, s[76:79], s49 offen lds\n"
    "s_add_u32 m0, s51, 2048\n"
    "s_add_u32 s49, s50, 4718592\n"
    "buffer_load_dwordx4 v146, s[76:79], s49 offen lds\n"
    "s_add_u32 m0, s51, 3072\n"
    "s_add_u32 s49, s50, 4816896\n"
    "buffer_load_dwordx4 v144, s[76:79], s49 offen lds\n"
    "s_add_u32 m0, s51, 4096\n"
    "s_add_u32 s49, s50, 4816896\n"
    "buffer_load_dwordx4 v145, s[76:79], s49 offen lds\n"
    "s_add_u32 m0, s51, 5120\n"
    "s_add_u32 s49, s50, 4816896\n"
    "buffer_load_dwordx4 v146, s[76:79], s49 offen lds\n"
    "ds_read_b64_tr_b16 a[80:81], v147 offset:24576\n"
    "ds_read_b64_tr_b16 a[82:83], v147 offset:25344\n"
    "ds_read_b64_tr_b16 a[84:85], v147 offset:36864\n"
    "ds_read_b64_tr_b16 a[86:87], v147 offset:37632\n"
    "ds_read_b64_tr_b16 v[48:49], v147 offset:24608\n"
    "ds_read_b64_tr_b16 v[50:51], v147 offset:25376\n"
    "ds_read_b64_tr_b16 v[52:53], v147 offset:36896\n"
    "ds_read_b64_tr_b16 v[54:55], v147 offset:37664\n"
    "ds_read_b64_tr_b16 a[248:249], v147 offset:24640\n"
    "ds_read_b64_tr_b16 a[250:251], v147 offset:25408\n"
    "ds_read_b64_tr_b16 a[252:253], v147 offset:36928\n"
    "ds_read_b64_tr_b16 a[254:255], v147 offset:37696\n"
    "s_waitcnt vmcnt(24) lgkmcnt(0)\n"
    "s_barrier\n"
    "s_add_u32 m0, s51, 24576\n"
    "s_add_u32 s49, s50, 4718592\n"
    "buffer_load_dwordx4 v144, s[80:83], s49 offen lds\n"
    "s_add_u32 m0, s51, 25600\n"
    "s_add_u32 s49, s50, 4718592\n"
    "buffer_load_dwordx4 v145, s[80:83], s49 offen lds\n"
    "s_add_u32 m0, s51, 26624\n"
    "s_add_u32 s49, s50, 4718592\n"
    "buffer_load_dwordx4 v146, s[80:83], s49 offen lds\n"
    "s_add_u32 m0, s51, 27648\n"
    "s_add_u32 s49, s50, 4816896\n"
    "buffer_load_dwordx4 v144, s[80:83], s49 offen lds\n"
    "s_add_u32 m0, s51, 28672\n"
    "s_add_u32 s49, s50, 4816896\n"
    "buffer_load_dwordx4 v145, s[80:83], s49 offen lds\n"
    "s_add_u32 m0, s51, 29696\n"
    "s_add_u32 s49, s50, 4816896\n"
    "buffer_load_dwordx4 v146, s[80:83], s49 offen lds\n"
    "ds_read_b64_tr_b16 a[88:89], v148 offset:0\n"
    "ds_read_b64_tr_b16 a[90:91], v148 offset:768\n"
    "ds_read_b64_tr_b16 a[92:93], v148 offset:12288\n"
    "ds_read_b64_tr_b16 a[94:95], v148 offset:13056\n"
    "ds_read_b64_tr_b16 v[64:65], v148 offset:32\n"
    "ds_read_b64_tr_b16 v[66:67], v148 offset:800\n"
    "ds_read_b64_tr_b16 v[68:69], v148 offset:12320\n"
    "ds_read_b64_tr_b16 v[70:71], v148 offset:13088\n"
    "ds_read_b64_tr_b16 v[160:161], v148 offset:64\n"
    "ds_read_b64_tr_b16 v[162:163], v148 offset:832\n"
    "ds_read_b64_tr_b16 v[164:165], v148 offset:12352\n"
    "ds_read_b64_tr_b16 v[166:167], v148 offset:13120\n"
    "s_waitcnt vmcnt(24) lgkmcnt(0)\n"
    "s_barrier\n"
    "s_add_u32 m0, s51, 49152\n"
    "s_add_u32 s49, s50, 5505024\n"
    "buffer_load_dwordx4 v144, s[76:79], s49 offen lds\n"
    "s_add_u32 m0, s51, 50176\n"
    "s_add_u32 s49, s50, 5505024\n"
    "buffer_load_dwordx4 v145, s[76:79], s49 offen lds\n"
    "s_add_u32 m0, s51, 51200\n"
    "s_add_u32 s49, s50, 5505024\n"
    "buffer_load_dwordx4 v146, s[76:79], s49 offen lds\n"
    "s_add_u32 m0, s51, 52224\n"
    "s_add_u32 s49, s50, 5603328\n"
    "buffer_load_dwordx4 v144, s[76:79], s49 offen lds\n"
    "s_add_u32 m0, s51, 53248\n"
    "s_add_u32 s49, s50, 5603328\n"
    "buffer_load_dwordx4 v145, s[76:79], s49 offen lds\n"
    "s_add_u32 m0, s51, 54272\n"
    "s_add_u32 s49, s50, 5603328\n"
    "buffer_load_dwordx4 v146, s[76:79], s49 offen lds\n"
    "ds_read_b64_tr_b16 a[96:97], v148 offset:24576\n"
    "ds_read_b64_tr_b16 a[98:99], v148 offset:25344\n"
    "ds_read_b64_tr_b16 a[100:101], v148 offset:36864\n"
    "ds_read_b64_tr_b16 a[102:103], v148 offset:37632\n"
    "ds_read_b64_tr_b16 v[56:57], v148 offset:24608\n"
    "ds_read_b64_tr_b16 v[58:59], v148 offset:25376\n"
    "ds_read_b64_tr_b16 v[60:61], v148 offset:36896\n"
    "ds_read_b64_tr_b16 v[62:63], v148 offset:37664\n"
    "ds_read_b64_tr_b16 v[168:169], v148 offset:24640\n"
    "ds_read_b64_tr_b16 v[170:171], v148 offset:25408\n"
    "ds_read_b64_tr_b16 v[172:173], v148 offset:36928\n"
    "ds_read_b64_tr_b16 v[174:175], v148 offset:37696\n"
    "s_waitcnt vmcnt(24) lgkmcnt(0)\n"
    "s_barrier\n"
    "s_add_u32 m0, s51, 73728\n"
    "s_add_u32 s49, s50, 5505024\n"
    "buffer_load_dwordx4 v144, s[80:83], s49 offen lds\n"
    "s_add_u32 m0, s51, 74752\n"
    "s_add_u32 s49, s50, 5505024\n"
    "buffer_load_dwordx4 v145, s[80:83], s49 offen lds\n"
    "s_add_u32 m0, s51, 75776\n"
    "s_add_u32 s49, s50, 5505024\n"
    "buffer_load_dwordx4 v146, s[80:83], s49 offen lds\n"
    "s_add_u32 m0, s51, 76800\n"
    "s_add_u32 s49, s50, 5603328\n"
    "buffer_load_dwordx4 v144, s[80:83], s49 offen lds\n"
    "s_add_u32 m0, s51, 77824\n"
    "s_add_u32 s49, s50, 5603328\n"
    "buffer_load_dwordx4 v145, s[80:83], s49 offen lds\n"
    "s_add_u32 m0, s51, 78848\n"
    "s_add_u32 s49, s50, 5603328\n"
    "buffer_load_dwordx4 v146, s[80:83], s49 offen lds\n"
    "ds_read_b64_tr_b16 a[104:105], v149 offset:0\n"
    "ds_read_b64_tr_b16 a[106:107], v149 offset:768\n"
    "ds_read_b64_tr_b16 a[108:109], v149 offset:12288\n"
    "ds_read_b64_tr_b16 a[110:111], v149 offset:13056\n"
    "ds_read_b64_tr_b16 a[112:113], v149 offset:32\n"
    "ds_read_b64_tr_b16 a[114:115], v149 offset:800\n"
    "ds_read_b64_tr_b16 a[116:117], v149 offset:12320\n"
    "ds_read_b64_tr_b16 a[118:119], v149 offset:13088\n"
    "ds_read_b64_tr_b16 v[176:177], v149 offset:64\n"
    "ds_read_b64_tr_b16 v[178:179], v149 offset:832\n"
    "ds_read_b64_tr_b16 v[180:181], v149 offset:12352\n"
    "ds_read_b64_tr_b16 v[182:183], v149 offset:13120\n"
    "s_waitcnt vmcnt(24) lgkmcnt(0)\n"
    "s_barrier\n"
    "s_add_u32 m0, s51, 98304\n"
    "s_add_u32 s49, s50, 6291456\n"
    "buffer_load_dwordx4 v144, s[76:79], s49 offen lds\n"
    "s_add_u32 m0, s51, 99328\n"
    "s_add_u32 s49, s50, 6291456\n"
    "buffer_load_dwordx4 v145, s[76:79], s49 offen lds\n"
    "s_add_u32 m0, s51, 100352\n"
    "s_add_u32 s49, s50, 6291456\n"
    "buffer_load_dwordx4 v146, s[76:79], s49 offen lds\n"
    "s_add_u32 m0, s51, 101376\n"
    "s_add_u32 s49, s50, 6389760\n"
    "buffer_load_dwordx4 v144, s[76:79], s49 offen lds\n"
    "s_add_u32 m0, s51, 102400\n"
    "s_add_u32 s49, s50, 6389760\n"
    "buffer_load_dwordx4 v145, s[76:79], s49 offen lds\n"
    "s_add_u32 m0, s51, 103424\n"
    "s_add_u32 s49, s50, 6389760\n"
    "buffer_load_dwordx4 v146, s[76:79], s49 offen lds\n"
    "ds_read_b64_tr_b16 a[120:121], v149 offset:24576\n"
    "ds_read_b64_tr_b16 a[122:123], v149 offset:25344\n"
    "ds_read_b64_tr_b16 a[124:125], v149 offset:36864\n"
    "ds_read_b64_tr_b16 a[126:127], v149 offset:37632\n"
    "ds_read_b64_tr_b16 v[72:73], v149 offset:24608\n"
    "ds_read_b64_tr_b16 v[74:75], v149 offset:25376\n"
    "ds_read_b64_tr_b16 v[76:77], v149 offset:36896\n"
    "ds_read_b64_tr_b16 v[78:79], v149 offset:37664\n"
    "ds_read_b64_tr_b16 v[184:185], v149 offset:24640\n"
    "ds_read_b64_tr_b16 v[186:187], v149 offset:25408\n"
    "ds_read_b64_tr_b16 v[188:189], v149 offset:36928\n"
    "ds_read_b64_tr_b16 v[190:191], v149 offset:37696\n"
    "s_waitcnt vmcnt(24) lgkmcnt(0)\n"
    "s_barrier\n"
    "s_add_u32 m0, s51, 122880\n"
    "s_add_u32 s49, s50, 6291456\n"
    "buffer_load_dwordx4 v144, s[80:83], s49 offen lds\n"
    "s_add_u32 m0, s51, 123904\n"
    "s_add_u32 s49, s50, 6291456\n"
    "buffer_load_dwordx4 v145, s[80:83], s49 offen lds\n"
    "s_add_u32 m0, s51, 124928\n"
    "s_add_u32 s49, s50, 6291456\n"
    "buffer_load_dwordx4 v146, s[80:83], s49 offen lds\n"
    "s_add_u32 m0, s51, 125952\n"
    "s_add_u32 s49, s50, 6389760\n"
    "buffer_load_dwordx4 v144, s[80:83], s49 offen lds\n"
    "s_add_u32 m0, s51, 126976\n"
    "s_add_u32 s49, s50, 6389760\n"
    "buffer_load_dwordx4 v145, s[80:83], s49 offen lds\n"
    "s_add_u32 m0, s51, 128000\n"
    "s_add_u32 s49, s50, 6389760\n"
    "buffer_load_dwordx4 v146, s[80:83], s49 offen lds\n"
    "ds_read_b64_tr_b16 a[128:129], v147 offset:0\n"
    "ds_read_b64_tr_b16 a[130:131], v147 offset:768\n"
    "ds_read_b64_tr_b16 a[132:133], v147 offset:12288\n"
    "ds_read_b64_tr_b16 a[134:135], v147 offset:13056\n"
    "ds_read_b64_tr_b16 a[136:137], v147 offset:32\n"
    "ds_read_b64_tr_b16 a[138:139], v147 offset:800\n"
    "ds_read_b64_tr_b16 a[140:141], v147 offset:12320\n"
    "ds_read_b64_tr_b16 a[142:143], v147 offset:13088\n"
    "ds_read_b64_tr_b16 v[192:193], v147 offset:64\n"
    "ds_read_b64_tr_b16 v[194:195], v147 offset:832\n"
    "ds_read_b64_tr_b16 v[196:197], v147 offset:12352\n"
    "ds_read_b64_tr_b16 v[198:199], v147 offset:13120\n"
    "s_waitcnt vmcnt(24) lgkmcnt(0)\n"
    "s_barrier\n"
    "s_add_u32 m0, s51, 0\n"
    "s_add_u32 s49, s50, 7077888\n"
    "buffer_load_dwordx4 v144, s[76:79], s49 offen lds\n"
    "s_add_u32 m0, s51, 1024\n"
    "s_add_u32 s49, s50, 7077888\n"
    "buffer_load_dwordx4 v145, s[76:79], s49 offen lds\n"
    "s_add_u32 m0, s51, 2048\n"
    "s_add_u32 s49, s50, 7077888\n"
    "buffer_load_dwordx4 v146, s[76:79], s49 offen lds\n"
    "s_add_u32 m0, s51, 3072\n"
    "s_add_u32 s49, s50, 7176192\n"
    "buffer_load_dwordx4 v144, s[76:79], s49 offen lds\n"
    "s_add_u32 m0, s51, 4096\n"
    "s_add_u32 s49, s50, 7176192\n"
    "buffer_load_dwordx4 v145, s[76:79], s49 offen lds\n"
    "s_add_u32 m0, s51, 5120\n"
    "s_add_u32 s49, s50, 7176192\n"
    "buffer_load_dwordx4 v146, s[76:79], s49 offen lds\n"
    "ds_read_b64_tr_b16 a[144:145], v147 offset:24576\n"
    "ds_read_b64_tr_b16 a[146:147], v147 offset:25344\n"
    "ds_read_b64_tr_b16 a[148:149], v147 offset:36864\n"
    "ds_read_b64_tr_b16 a[150:151], v147 offset:37632\n"
    "ds_read_b64_tr_b16 v[80:81], v147 offset:24608\n"
    "ds_read_b64_tr_b16 v[82:83], v147 offset:25376\n"
    "ds_read_b64_tr_b16 v[84:85], v147 offset:36896\n"
    "ds_read_b64_tr_b16 v[86:87], v147 offset:37664\n"
    "ds_read_b64_tr_b16 v[200:201], v147 offset:24640\n"
    "ds_read_b64_tr_b16 v[202:203], v147 offset:25408\n"
    "ds_read_b64_tr_b16 v[204:205], v147 offset:36928\n"
    "ds_read_b64_tr_b16 v[206:207], v147 offset:37696\n"
    "s_waitcnt vmcnt(24) lgkmcnt(0)\n"
    "s_barrier\n"
    "s_add_u32 m0, s51, 24576\n"
    "s_add_u32 s49, s50, 7077888\n"
    "buffer_load_dwordx4 v144, s[80:83], s49 offen lds\n"
    "s_add_u32 m0, s51, 25600\n"
    "s_add_u32 s49, s50, 7077888\n"
    "buffer_load_dwordx4 v145, s[80:83], s49 offen lds\n"
    "s_add_u32 m0, s51, 26624\n"
    "s_add_u32 s49, s50, 7077888\n"
    "buffer_load_dwordx4 v146, s[80:83], s49 offen lds\n"
    "s_add_u32 m0, s51, 27648\n"
    "s_add_u32 s49, s50, 7176192\n"
    "buffer_load_dwordx4 v144, s[80:83], s49 offen lds\n"
    "s_add_u32 m0, s51, 28672\n"
    "s_add_u32 s49, s50, 7176192\n"
    "buffer_load_dwordx4 v145, s[80:83], s49 offen lds\n"
    "s_add_u32 m0, s51, 29696\n"
    "s_add_u32 s49, s50, 7176192\n"
    "buffer_load_dwordx4 v146, s[80:83], s49 offen lds\n"
    "ds_read_b64_tr_b16 a[152:153], v148 offset:0\n"
    "ds_read_b64_tr_b16 a[154:155], v148 offset:768\n"
    "ds_read_b64_tr_b16 a[156:157], v148 offset:12288\n"
    "ds_read_b64_tr_b16 a[158:159], v148 offset:13056\n"
    "ds_read_b64_tr_b16 v[96:97], v148 offset:32\n"
    "ds_read_b64_tr_b16 v[98:99], v148 offset:800\n"
    "ds_read_b64_tr_b16 v[100:101], v148 offset:12320\n"
    "ds_read_b64_tr_b16 v[102:103], v148 offset:13088\n"
    "ds_read_b64_tr_b16 v[208:209], v148 offset:64\n"
    "ds_read_b64_tr_b16 v[210:211], v148 offset:832\n"
    "ds_read_b64_tr_b16 v[212:213], v148 offset:12352\n"
    "ds_read_b64_tr_b16 v[214:215], v148 offset:13120\n"
    "s_waitcnt vmcnt(24) lgkmcnt(0)\n"
    "s_barrier\n"
    "ds_read_b64_tr_b16 a[160:161], v148 offset:24576\n"
    "ds_read_b64_tr_b16 a[162:163], v148 offset:25344\n"
    "ds_read_b64_tr_b16 a[164:165], v148 offset:36864\n"
    "ds_read_b64_tr_b16 a[166:167], v148 offset:37632\n"
    "ds_read_b64_tr_b16 v[88:89], v148 offset:24608\n"
    "ds_read_b64_tr_b16 v[90:91], v148 offset:25376\n"
    "ds_read_b64_tr_b16 v[92:93], v148 offset:36896\n"
    "ds_read_b64_tr_b16 v[94:95], v148 offset:37664\n"
    "ds_read_b64_tr_b16 v[216:217], v148 offset:24640\n"
    "ds_read_b64_tr_b16 v[218:219], v148 offset:25408\n"
    "ds_read_b64_tr_b16 v[220:221], v148 offset:36928\n"
    "ds_read_b64_tr_b16 v[222:223], v148 offset:37696\n"
    "s_waitcnt vmcnt(18) lgkmcnt(0)\n"
    "s_barrier\n"
    "ds_read_b64_tr_b16 a[168:169], v149 offset:0\n"
    "ds_read_b64_tr_b16 a[170:171], v149 offset:768\n"
    "ds_read_b64_tr_b16 a[172:173], v149 offset:12288\n"
    "ds_read_b64_tr_b16 a[174:175], v149 offset:13056\n"
    "ds_read_b64_tr_b16 a[176:177], v149 offset:32\n"
    "ds_read_b64_tr_b16 a[178:179], v149 offset:800\n"
    "ds_read_b64_tr_b16 a[180:181], v149 offset:12320\n"
    "ds_read_b64_tr_b16 a[182:183], v149 offset:13088\n"
    "ds_read_b64_tr_b16 v[224:225], v149 offset:64\n"
    "ds_read_b64_tr_b16 v[226:227], v149 offset:832\n"
    "ds_read_b64_tr_b16 v[228:229], v149 offset:12352\n"
    "ds_read_b64_tr_b16 v[230:231], v149 offset:13120\n"
    "s_waitcnt vmcnt(12) lgkmcnt(0)\n"
    "s_barrier\n"
    "ds_read_b64_tr_b16 a[184:185], v149 offset:24576\n"
    "ds_read_b64_tr_b16 a[186:187], v149 offset:25344\n"
    "ds_read_b64_tr_b16 a[188:189], v149 offset:36864\n"
    "ds_read_b64_tr_b16 a[190:191], v149 offset:37632\n"
    "ds_read_b64_tr_b16 v[104:105], v149 offset:24608\n"
    "ds_read_b64_tr_b16 v[106:107], v149 offset:25376\n"
    "ds_read_b64_tr_b16 v[108:109], v149 offset:36896\n"
    "ds_read_b64_tr_b16 v[110:111], v149 offset:37664\n"
    "ds_read_b64_tr_b16 v[232:233], v149 offset:24640\n"
    "ds_read_b64_tr_b16 v[234:235], v149 offset:25408\n"
    "ds_read_b64_tr_b16 v[236:237], v149 offset:36928\n"
    "ds_read_b64_tr_b16 v[238:239], v149 offset:37696\n"
    "s_waitcnt vmcnt(6) lgkmcnt(0)\n"
    "s_barrier\n"
    "ds_read_b64_tr_b16 a[192:193], v147 offset:0\n"
    "ds_read_b64_tr_b16 a[194:195], v147 offset:768\n"
    "ds_read_b64_tr_b16 a[196:197], v147 offset:12288\n"
    "ds_read_b64_tr_b16 a[198:199], v147 offset:13056\n"
    "ds_read_b64_tr_b16 a[200:201], v147 offset:32\n"
    "ds_read_b64_tr_b16 a[202:203], v147 offset:800\n"
    "ds_read_b64_tr_b16 a[204:205], v147 offset:12320\n"
    "ds_read_b64_tr_b16 a[206:207], v147 offset:13088\n"
    "ds_read_b64_tr_b16 v[240:241], v147 offset:64\n"
    "ds_read_b64_tr_b16 v[242:243], v147 offset:832\n"
    "ds_read_b64_tr_b16 v[244:245], v147 offset:12352\n"
    "ds_read_b64_tr_b16 v[246:247], v147 offset:13120\n"
    "s_waitcnt vmcnt(0) lgkmcnt(0)\n"
    "s_barrier\n"
    "ds_read_b64_tr_b16 a[208:209], v147 offset:24576\n"
    "ds_read_b64_tr_b16 a[210:211], v147 offset:25344\n"
    "ds_read_b64_tr_b16 a[212:213], v147 offset:36864\n"
    "ds_read_b64_tr_b16 a[214:215], v147 offset:37632\n"
    "ds_read_b64_tr_b16 v[112:113], v147 offset:24608\n"
    "ds_read_b64_tr_b16 v[114:115], v147 offset:25376\n"
    "ds_read_b64_tr_b16 v[116:117], v147 offset:36896\n"
    "ds_read_b64_tr_b16 v[118:119], v147 offset:37664\n"
    "ds_read_b64_tr_b16 v[248:249], v147 offset:24640\n"
    "ds_read_b64_tr_b16 v[250:251], v147 offset:25408\n"
    "ds_read_b64_tr_b16 v[252:253], v147 offset:36928\n"
    "ds_read_b64_tr_b16 v[254:255], v147 offset:37696\n"
    "s_waitcnt lgkmcnt(0)\n"
    "s_barrier\n"
    "ds_write_b128 v2, v[16:19] offset:0\n"
    "ds_write_b128 v2, v[20:23] offset:1024\n"
    "ds_write_b128 v2, v[24:27] offset:2048\n"
    "ds_write_b128 v2, v[28:31] offset:3072\n"
    "ds_write_b128 v2, v[32:35] offset:4096\n"
    "ds_write_b128 v2, v[36:39] offset:5120\n"
    "ds_write_b128 v2, v[40:43] offset:6144\n"
    "ds_write_b128 v2, v[44:47] offset:7168\n"
    "ds_write_b128 v2, v[48:51] offset:8192\n"
    "ds_write_b128 v2, v[52:55] offset:9216\n"
    "ds_write_b128 v2, v[56:59] offset:10240\n"
    "ds_write_b128 v2, v[60:63] offset:11264\n"
    "ds_write_b128 v2, v[64:67] offset:12288\n"
    "ds_write_b128 v2, v[68:71] offset:13312\n"
    "ds_write_b128 v2, v[72:75] offset:14336\n"
    "ds_write_b128 v2, v[76:79] offset:15360\n"
    "s_waitcnt lgkmcnt(14)\n"
    "ds_write_b128 v2, v[80:83] offset:16384\n"
    "ds_write_b128 v2, v[84:87] offset:17408\n"
    "s_waitcnt lgkmcnt(14)\n"
    "ds_write_b128 v2, v[88:91] offset:18432\n"
    "ds_write_b128 v2, v[92:95] offset:19456\n"
    "s_waitcnt lgkmcnt(14)\n"
    "ds_write_b128 v2, v[96:99] offset:20480\n"
    "ds_write_b128 v2, v[100:103] offset:21504\n"
    "s_waitcnt lgkmcnt(14)\n"
    "ds_write_b128 v2, v[104:107] offset:22528\n"
    "ds_write_b128 v2, v[108:111] offset:23552\n"
    "s_waitcnt lgkmcnt(14)\n"
    "ds_write_b128 v2, v[112:115] offset:24576\n"
    "ds_write_b128 v2, v[116:119] offset:25600\n"
    "s_waitcnt lgkmcnt(14)\n"
    "ds_write_b128 v2, v[120:123] offset:26624\n"
    "ds_write_b128 v2, v[124:127] offset:27648\n"
    "s_waitcnt lgkmcnt(14)\n"
    "ds_write_b128 v2, v[128:131] offset:28672\n"
    "ds_write_b128 v2, v[132:135] offset:29696\n"
    "s_waitcnt lgkmcnt(14)\n"
    "ds_write_b128 v2, v[136:139] offset:30720\n"
    "ds_write_b128 v2, v[140:143] offset:31744\n"
    "s_waitcnt lgkmcnt(14)\n"
    "s_waitcnt vmcnt(0) lgkmcnt(0)\n"
    "v_lshrrev_b32 v152, 4, v15\n"
    "v_and_b32 v153, 7, v15\n"
    "v_bfe_u32 v154, v15, 3, 1\n"
    "v_cmp_eq_u32 vcc, 1, v154\n"
    "v_mov_b32 v13, 0x4444\n"
    "v_mov_b32 v1, 0xeeee\n"
    "v_cndmask_b32 v1, v13, v1, vcc\n"
    "v_mov_b32 v13, 0xff800000\n"
    "v_cndmask_b32 v8, 0, v13, vcc\n"
    "v_cndmask_b32 v9, 0, v13, vcc\n"
    "v_cndmask_b32 v10, 0, v13, vcc\n"
    "v_cndmask_b32 v11, 0, v13, vcc\n"
    "v_bfe_u32 v13, v15, 2, 2\n"
    "v_xor_b32 v13, v13, v152\n"
    "v_lshlrev_b32 v13, 3, v13\n"
    "v_and_b32 v3, 15, v15\n"
    "v_lshl_add_u32 v3, v3, 5, v13\n"
    "s_mul_i32 s48, s45, 1536\n"
    "s_add_u32 s48, s48, 143360\n"
    "v_add_u32 v3, s48, v3\n"
    "v_xor_b32 v4, v15, v152\n"
    "v_lshlrev_b32 v4, 3, v4\n"
    "v_add_u32 v4, 143360, v4\n"
    "v_add_u32 v5, 131072, v0\n"
    "s_lshr_b32 s48, s44, 1\n"
    "s_lshl_b32 s48, s48, 10\n"
    "s_and_b32 s49, s44, 1\n"
    "s_lshl_b32 s49, s49, 7\n"
    "s_add_u32 s50, s48, s49\n"
    "s_lshl_b32 s51, s44, 7\n"
    "s_add_u32 s51, s51, 16384\n"
    "s_mov_b32 s47, 0\n"
    "s_mov_b32 s61, 6144\n"
    "s_cmp_lg_u32 s45, 0\n"
    "s_cbranch_scc1 hw_wx_%=\n"
    "v_lshlrev_b32 v6, 8, v152\n"
    "v_add_u32 v6, s50, v6\n"
    "v_mov_b32 v7, s51\n"
    "v_cmp_eq_u32 vcc, 0, v154\n"
    "s_nop 1\n"
    "v_cndmask_b32 v6, v7, v6, vcc\n"
    "v_lshl_add_u32 v6, v153, 4, v6\n"
    "v_and_b32 v13, 0xffffe000, v6\n"
    "v_add_u32 v6, v6, v13\n"
    "v_lshrrev_b32 v7, 3, v15\n"
    "v_cmp_eq_u32 s[56:57], 5, v7\n"
    "s_nop 0\n"
    "s_or_b64 vcc, vcc, s[56:57]\n"
    "v_mov_b32 v13, 2147483632\n"
    "v_cndmask_b32 v6, v13, v6, vcc\n"
    "v_mov_b32 v14, 0\n"
    "v_cmp_eq_u32 vcc, 0, v15\n"
    "v_mov_b32 v13, 2147483632\n"
    "s_nop 0\n"
    "v_cndmask_b32 v14, v13, v14, vcc\n"
    "s_cmp_eq_u32 s44, 0\n"
    "s_cselect_b32 s59, 4, 0\n"
    "v_lshlrev_b32 v7, 15, v15\n"
    "v_cmp_gt_u32 vcc, s59, v15\n"
    "v_mov_b32 v13, 2147483632\n"
    "s_nop 0\n"
    "v_cndmask_b32 v7, v13, v7, vcc\n"
    "s_lshl_b32 s59, s59, 1\n"
    "v_lshlrev_b32 v155, 7, v15\n"
    "v_cmp_gt_u32 vcc, s59, v15\n"
    "v_mov_b32 v13, 2147483632\n"
    "s_nop 0\n"
    "v_cndmask_b32 v155, v13, v155, vcc\n"
    "s_mov_b32 s64, 0x3dd53b95\n"
    "s_mov_b32 s65, s64\n"
    "s_lshr_b32 s48, s44, 3\n"
    "s_lshl_b32 s48, s48, 7\n"
    "v_add_u32 v159, s48, v14\n"
    "s_mul_i32 s10, s67, 23\n"
    "s_sub_u32 s10, s10, s71\n"
    "s_mul_i32 s11, s66, 23\n"
    "s_sub_u32 s11, s11, s72\n"
    "hw_loopa_%=:\n"
    "s_and_b32 s48, s47, 3\n"
    "s_mul_i32 s53, s48, 196608\n"
    "s_lshl_b32 s50, s48, 2\n"
    "s_add_u32 s50, s50, s46\n"
    "s_mul_i32 s50, s50, 32768\n"
    "s_add_u32 s50, s50, 786432\n"
    "v_add_u32 v158, s50, v0\n"
    "s_add_u32 s50, s53, 49152\n"
    "s_add_u32 s51, s53, 98304\n"
    "s_add_u32 s52, s53, 147456\n"
    "v_mov_b32 v56, 0\n"
    "v_mov_b32 v57, 0\n"
    "v_mov_b32 v58, 0\n"
    "v_mov_b32 v59, 0\n"
    "v_mov_b32 v60, 0\n"
    "v_mov_b32 v61, 0\n"
    "v_mov_b32 v62, 0\n"
    "v_mov_b32 v63, 0\n"
    "v_mov_b32 v64, 0\n"
    "v_mov_b32 v65, 0\n"
    "v_mov_b32 v66, 0\n"
    "v_mov_b32 v67, 0\n"
    "v_mov_b32 v68, 0\n"
    "v_mov_b32 v69, 0\n"
    "v_mov_b32 v70, 0\n"
    "v_mov_b32 v71, 0\n"
    "v_mov_b32 v72, 0\n"
    "v_mov_b32 v73, 0\n"
    "v_mov_b32 v74, 0\n"
    "v_mov_b32 v75, 0\n"
    "v_mov_b32 v76, 0\n"
    "v_mov_b32 v77, 0\n"
    "v_mov_b32 v78, 0\n"
    "v_mov_b32 v79, 0\n"
    "ds_read_b128 v[80:83], v2 offset:0\n"
    "ds_read_b128 v[84:87], v2 offset:1024\n"
    "ds_read_b128 v[88:91], v2 offset:2048\n"
    "ds_read_b128 v[92:95], v2 offset:3072\n"
    "ds_read_b128 v[96:99], v2 offset:4096\n"
    "ds_read_b128 v[100:103], v2 offset:5120\n"
    "s_and_b32 s59, s47, 64\n"
    "s_add_u32 s63, s47, s59\n"
    "s_lshl_b32 s63, s63, 7\n"
    "s_add_u32 s63, s63, 1310720\n"
    "s_lshl_b32 s59, s46, 15\n"
    "s_add_u32 s59, s59, s63\n"
    "s_add_u32 s14, s40, s59\n"
    "s_addc_u32 s15, s41, 0\n"
    "s_lshl_b32 s12, s47, 10\n"
    "s_add_u32 s12, s12, 2365952\n"
    "s_waitcnt lgkmcnt(0)\n"
    "s_and_b32 s59, s47, 63\n"
    "s_cbranch_scc1 hw_timed_%=\n"
    "s_add_u32 s75, s47, 6\n"
    "s_barrier\n"
    "s_memrealtime s[56:57]\n"
    "s_waitcnt lgkmcnt(0)\n"
    "s_add_u32 s62, s56, s68\n"
    "s_add_u32 s59, s62, s67\n"
    "v_mov_b32 v152, s59\n"
    "s_add_u32 s59, s59, s66\n"
    "v_mov_b32 v153, s59\n"
    "s_nop 1\n"
    "s_branch hw_go_%=\n"
    "hw_timed_%=:\n"
    "s_mov_b32 s62, s17\n"
    "s_cmp_lg_u32 s62, -1\n"
    "s_cbranch_scc1 hw_rs_ok_%=\n"
    "s_sub_u32 s55, s47, 1\n"
    "s_and_b32 s59, s55, 64\n"
    "s_add_u32 s54, s55, s59\n"
    "s_lshl_b32 s54, s54, 7\n"
    "s_add_u32 s54, s54, 1310720\n"
    "s_mov_b32 s60, 0\n"
    "hw_rs_%=:\n"
    "buffer_load_dwordx2 v[152:153], v14, s[40:43], s54 offen sc1\n"
    "s_waitcnt vmcnt(0)\n"
    "v_readfirstlane_b32 s55, v152\n"
    "v_readfirstlane_b32 s62, v153\n"
    "s_nop 0\n"
    "s_max_u32 s59, s55, s62\n"
    "s_cmp_lg_u32 s59, -1\n"
    "s_nop 1\n"
    "s_cbranch_scc1 hw_rs_ok_%=\n"
    "s_sleep 4\n"
    "s_add_u32 s60, s60, 1\n"
    "s_cmp_gt_u32 s60, 65536\n"
    "s_cbranch_scc1 hw_abort_%=\n"
    "s_and_b32 s59, s60, 63\n"
    "s_cbranch_scc1 hw_rs_%=\n"
    "v_mov_b32 v152, 1024\n"
    "buffer_load_dword v152, v152, s[40:43], s58 offen sc1\n"
    "s_waitcnt vmcnt(0)\n"
    "v_readfirstlane_b32 s59, v152\n"
    "s_nop 0\n"
    "s_cmp_lg_u32 s59, 0\n"
    "s_nop 2\n"
    "s_cbranch_scc1 hw_end_%=\n"
    "s_branch hw_rs_%=\n"
    "hw_rs_ok_%=:\n"
    "s_add_u32 s59, s62, s67\n"
    "v_mov_b32 v152, s59\n"
    "s_add_u32 s59, s59, s66\n"
    "v_mov_b32 v153, s59\n"
    "s_memrealtime s[56:57]\n"
    "s_waitcnt lgkmcnt(0)\n"
    "s_sub_u32 s59, s62, s56\n"
    "s_cmp_gt_u32 s59, 1000\n"
    "s_cbranch_scc1 hw_wu_go_%=\n"
    "hw_wu_%=:\n"
    "s_memrealtime s[56:57]\n"
    "s_waitcnt lgkmcnt(0)\n"
    "s_sub_u32 s59, s62, s56\n"
    "s_cmp_gt_i32 s59, 15\n"
    "s_cbranch_scc1 hw_wu_%=\n"
    "s_memrealtime s[30:31]\n"
    "s_nop 1\n"
    "s_memrealtime s[18:19]\n"
    "s_nop 1\n"
    "s_memrealtime s[34:35]\n"
    "s_nop 1\n"
    "s_memrealtime s[36:37]\n"
    "s_nop 1\n"
    "s_memrealtime s[38:39]\n"
    "s_nop 1\n"
    "s_memrealtime s[94:95]\n"
    "s_nop 1\n"
    "s_memrealtime s[96:97]\n"
    "s_nop 1\n"
    "s_memrealtime s[98:99]\n"
    "s_nop 1\n"
    "s_getpc_b64 s[56:57]\n"
    "hw_wu_pc_%=:\n"
    "s_waitcnt lgkmcnt(0)\n"
    "s_add_u32 s18, s18, s34\n"
    "s_add_u32 s18, s18, s36\n"
    "s_add_u32 s18, s18, s38\n"
    "s_add_u32 s18, s18, s94\n"
    "s_add_u32 s18, s18, s96\n"
    "s_add_u32 s18, s18, s98\n"
    "s_mul_i32 s34, s30, 7\n"
    "s_sub_u32 s18, s18, s34\n"
    "s_mul_i32 s18, s18, 3\n"
    "s_sub_u32 s59, s62, s30\n"
    "s_mul_i32 s59, s59, 23\n"
    "s_sub_u32 s59, s59, s18\n"
    "s_sub_u32 s59, s59, 170\n"
    "s_max_i32 s59, s59, 0\n"
    "s_min_i32 s59, s59, 252\n"
    "s_andn2_b32 s59, s59, 3\n"
    "s_sub_u32 s59, hw_wu_e_%=-hw_wu_pc_%=, s59\n"
    "s_add_u32 s56, s56, s59\n"
    "s_addc_u32 s57, s57, 0\n"
    "s_setpc_b64 s[56:57]\n"
    "s_nop 0\n"
    "s_nop 0\n"
    "s_nop 0\n"
    "s_nop 0\n"
    "s_nop 0\n"
    "s_nop 0\n"
    "s_nop 0\n"
    "s_nop 0\n"
    "s_nop 0\n"
    "s_nop 0\n"
    "s_nop 0\n"
    "s_nop 0\n"
    "s_nop 0\n"
    "s_nop 0\n"
    "s_nop 0\n"
    "s_nop 0\n"
    "s_nop 0\n"
    "s_nop 0\n"
    "s_nop 0\n"
    "s_nop 0\n"
    "s_nop 0\n"
    "s_nop 0\n"
    "s_nop 0\n"
    "s_nop 0\n"
    "s_nop 0\n"
    "s_nop 0\n"
    "s_nop 0\n"
    "s_nop 0\n"
    "s_nop 0\n"
    "s_nop 0\n"
    "s_nop 0\n"
    "s_nop 0\n"
    "s_nop 0\n"
    "s_nop 0\n"
    "s_nop 0\n"
    "s_nop 0\n"
    "s_nop 0\n"
    "s_nop 0\n"
    "s_nop 0\n"
    "s_nop 0\n"
    "s_nop 0\n"
    "s_nop 0\n"
    "s_nop 0\n"
    "s_nop 0\n"
    "s_nop 0\n"
    "s_nop 0\n"
    "s_nop 0\n"
    "s_nop 0\n"
    "s_nop 0\n"
    "s_nop 0\n"
    "s_nop 0\n"
    "s_nop 0\n"
    "s_nop 0\n"
    "s_nop 0\n"
    "s_nop 0\n"
    "s_nop 0\n"
    "s_nop 0\n"
    "s_nop 0\n"
    "s_nop 0\n"
    "s_nop 0\n"
    "s_nop 0\n"
    "s_nop 0\n"
    "s_nop 0\n"
    "hw_wu_e_%=:\n"
    "hw_wu_go_%=:\n"
    "s_barrier\n"
    "hw_go_%=:\n"
    "s_mov_b32 s60, 0\n"
    "hw_retrya_%=:\n"
    "buffer_load_dwordx4 v[16:19], v158, s[40:43], s20 offen sc1\n"
    "ds_read_b128 v[104:107], v5 offset:0\n"
    "s_nop 6\n"
    "buffer_load_dwordx4 v[20:23], v158, s[40:43], s21 offen sc1\n"
    "ds_read_b128 v[108:111], v5 offset:1024\n"
    "s_nop 6\n"
    "buffer_load_dwordx4 v[24:27], v158, s[40:43], s22 offen sc1\n"
    "ds_read_b128 v[112:115], v5 offset:2048\n"
    "s_nop 6\n"
    "buffer_load_dwordx4 v[28:31], v158, s[40:43], s23 offen sc1\n"
    "ds_read_b128 v[116:119], v5 offset:3072\n"
    "s_nop 6\n"
    "buffer_load_dwordx4 v[32:35], v158, s[40:43], s24 offen sc1\n"
    "ds_read_b128 v[120:123], v5 offset:4096\n"
    "s_nop 6\n"
    "buffer_load_dwordx4 v[36:39], v158, s[40:43], s25 offen sc1\n"
    "ds_read_b128 v[124:127], v5 offset:5120\n"
    "s_nop 6\n"
    "buffer_load_dwordx4 v[40:43], v158, s[40:43], s26 offen sc1\n"
    "ds_read_b128 v[128:131], v5 offset:6144\n"
    "s_nop 6\n"
    "buffer_load_dwordx4 v[44:47], v158, s[40:43], s27 offen sc1\n"
    "ds_read_b128 v[132:135], v5 offset:7168\n"
    "s_nop 6\n"
    "buffer_load_dwordx4 v[48:51], v158, s[40:43], s28 offen sc1\n"
    "s_nop 7\n"
    "buffer_load_dwordx4 v[52:55], v158, s[40:43], s29 offen sc1\n"
    "s_waitcnt lgkmcnt(0)\n"
    "s_waitcnt vmcnt(9)\n"
    "v_smfmac_f32_16x16x64_bf16 v[68:71], v[16:19], v[80:87], v1\n"
    "ds_read_b128 v[80:83], v2 offset:6144\n"
    "v_smfmac_f32_16x16x64_bf16 v[56:59], v[16:19], a[0:7], v1\n"
    "ds_read_b128 v[84:87], v2 offset:7168\n"
    "v_smfmac_f32_16x16x64_bf16 v[64:67], v[16:19], a[8:15], v1\n"
    "v_smfmac_f32_16x16x64_bf16 v[60:63], v[16:19], a[16:23], v1\n"
    "s_waitcnt vmcnt(8)\n"
    "v_smfmac_f32_16x16x64_bf16 v[68:71], v[20:23], v[88:95], v1\n"
    "ds_read_b128 v[88:91], v2 offset:8192\n"
    "v_smfmac_f32_16x16x64_bf16 v[56:59], v[20:23], a[24:31], v1\n"
    "ds_read_b128 v[92:95], v2 offset:9216\n"
    "v_smfmac_f32_16x16x64_bf16 v[64:67], v[20:23], v[96:103], v1\n"
    "ds_read_b128 v[96:99], v2 offset:10240\n"
    "v_smfmac_f32_16x16x64_bf16 v[60:63], v[20:23], a[32:39], v1\n"
    "ds_read_b128 v[100:103], v2 offset:11264\n"
    "s_waitcnt vmcnt(7) lgkmcnt(4)\n"
    "v_smfmac_f32_16x16x64_bf16 v[68:71], v[24:27], v[80:87], v1\n"
    "ds_read_b128 v[80:83], v2 offset:12288\n"
    "v_smfmac_f32_16x16x64_bf16 v[56:59], v[24:27], a[40:47], v1\n"
    "ds_read_b128 v[84:87], v2 offset:13312\n"
    "v_smfmac_f32_16x16x64_bf16 v[64:67], v[24:27], a[48:55], v1\n"
    "v_smfmac_f32_16x16x64_bf16 v[60:63], v[24:27], a[56:63], v1\n"
    "v_max3_u32 v12, v16, v20, v24\n"
    "s_waitcnt vmcnt(6) lgkmcnt(4)\n"
    "v_smfmac_f32_16x16x64_bf16 v[68:71], v[28:31], v[88:95], v1\n"
    "ds_read_b128 v[88:91], v2 offset:14336\n"
    "v_smfmac_f32_16x16x64_bf16 v[56:59], v[28:31], a[64:71], v1\n"
    "ds_read_b128 v[92:95], v2 offset:15360\n"
    "v_smfmac_f32_16x16x64_bf16 v[64:67], v[28:31], a[72:79], v1\n"
    "v_cmp_eq_u32 vcc, -1, v12\n"
    "v_smfmac_f32_16x16x64_bf16 v[60:63], v[28:31], a[80:87], v1\n"
    "s_cbranch_vccnz hw_polla_%=\n"
    "s_waitcnt vmcnt(5) lgkmcnt(4)\n"
    "v_smfmac_f32_16x16x64_bf16 v[68:71], v[32:35], v[96:103], v1\n"
    "ds_read_b128 v[96:99], v2 offset:16384\n"
    "v_smfmac_f32_16x16x64_bf16 v[56:59], v[32:35], a[88:95], v1\n"
    "ds_read_b128 v[100:103], v2 offset:17408\n"
    "s_waitcnt lgkmcnt(4)\n"
    "v_smfmac_f32_16x16x64_bf16 v[64:67], v[32:35], v[80:87], v1\n"
    "ds_read_b128 v[80:83], v2 offset:18432\n"
    "v_smfmac_f32_16x16x64_bf16 v[60:63], v[32:35], a[96:103], v1\n"
    "ds_read_b128 v[84:87], v2 offset:19456\n"
    "s_waitcnt vmcnt(4) lgkmcnt(4)\n"
    "v_smfmac_f32_16x16x64_bf16 v[68:71], v[36:39], v[88:95], v1\n"
    "ds_read_b128 v[88:91], v2 offset:20480\n"
    "v_smfmac_f32_16x16x64_bf16 v[56:59], v[36:39], a[104:111], v1\n"
    "ds_read_b128 v[92:95], v2 offset:21504\n"
    "v_smfmac_f32_16x16x64_bf16 v[64:67], v[36:39], a[112:119], v1\n"
    "v_smfmac_f32_16x16x64_bf16 v[60:63], v[36:39], a[120:127], v1\n"
    "s_waitcnt vmcnt(3) lgkmcnt(4)\n"
    "v_smfmac_f32_16x16x64_bf16 v[68:71], v[40:43], v[96:103], v1\n"
    "ds_read_b128 v[96:99], v2 offset:22528\n"
    "v_smfmac_f32_16x16x64_bf16 v[56:59], v[40:43], a[128:135], v1\n"
    "ds_read_b128 v[100:103], v2 offset:23552\n"
    "v_smfmac_f32_16x16x64_bf16 v[64:67], v[40:43], a[136:143], v1\n"
    "v_smfmac_f32_16x16x64_bf16 v[60:63], v[40:43], a[144:151], v1\n"
    "s_waitcnt vmcnt(2) lgkmcnt(4)\n"
    "v_smfmac_f32_16x16x64_bf16 v[68:71], v[44:47], v[80:87], v1\n"
    "ds_read_b128 v[80:83], v2 offset:24576\n"
    "v_smfmac_f32_16x16x64_bf16 v[56:59], v[44:47], a[152:159], v1\n"
    "ds_read_b128 v[84:87], v2 offset:25600\n"
    "s_waitcnt lgkmcnt(4)\n"
    "v_smfmac_f32_16x16x64_bf16 v[64:67], v[44:47], v[88:95], v1\n"
    "ds_read_b128 v[88:91], v2 offset:26624\n"
    "v_smfmac_f32_16x16x64_bf16 v[60:63], v[44:47], a[160:167], v1\n"
    "ds_read_b128 v[92:95], v2 offset:27648\n"
    "s_waitcnt vmcnt(1) lgkmcnt(4)\n"
    "v_smfmac_f32_16x16x64_bf16 v[68:71], v[48:51], v[96:103], v1\n"
    "ds_read_b128 v[96:99], v2 offset:28672\n"
    "v_smfmac_f32_16x16x64_bf16 v[56:59], v[48:51], a[168:175], v1\n"
    "ds_read_b128 v[100:103], v2 offset:29696\n"
    "v_smfmac_f32_16x16x64_bf16 v[64:67], v[48:51], a[176:183], v1\n"
    "v_smfmac_f32_16x16x64_bf16 v[60:63], v[48:51], a[184:191], v1\n"
    "s_waitcnt vmcnt(0) lgkmcnt(4)\n"
    "v_smfmac_f32_16x16x64_bf16 v[68:71], v[52:55], v[80:87], v1\n"
    "ds_read_b128 v[80:83], v2 offset:30720\n"
    "v_smfmac_f32_16x16x64_bf16 v[56:59], v[52:55], a[192:199], v1\n"
    "ds_read_b128 v[84:87], v2 offset:31744\n"
    "v_smfmac_f32_16x16x64_bf16 v[64:67], v[52:55], a[200:207], v1\n"
    "v_smfmac_f32_16x16x64_bf16 v[60:63], v[52:55], a[208:215], v1\n"
    "v_smfmac_f32_16x16x64_bf16 v[72:75], v[16:19], a[216:223], v1\n"
    "v_mov_b32 v13, v13\n"
    "v_mov_b32 v13, v13\n"
    "v_mov_b32 v13, v13\n"
    "v_mov_b32 v13, v13\n"
    "v_mov_b32 v13, v13\n"
    "v_mov_b32 v13, v13\n"
    "v_mov_b32 v13, v13\n"
    "v_permlane32_swap_b32 v56, v60\n"
    "v_permlane32_swap_b32 v57, v61\n"
    "s_waitcnt lgkmcnt(4)\n"
    "v_smfmac_f32_16x16x64_bf16 v[76:79], v[16:19], v[88:95], v1\n"
    "v_permlane32_swap_b32 v58, v62\n"
    "v_permlane32_swap_b32 v59, v63\n"
    "v_smfmac_f32_16x16x64_bf16 v[72:75], v[20:23], a[224:231], v1\n"
    "v_add_f32 v56, v56, v60\n"
    "v_add_f32 v57, v57, v61\n"
    "s_waitcnt lgkmcnt(2)\n"
    "v_smfmac_f32_16x16x64_bf16 v[76:79], v[20:23], v[96:103], v1\n"
    "v_add_f32 v58, v58, v62\n"
    "v_add_f32 v59, v59, v63\n"
    "v_smfmac_f32_16x16x64_bf16 v[72:75], v[24:27], a[232:239], v1\n"
    "v_cmp_u_f32 vcc, v56, v57\n"
    "v_cmp_u_f32 s[56:57], v58, v59\n"
    "s_waitcnt lgkmcnt(0)\n"
    "v_smfmac_f32_16x16x64_bf16 v[76:79], v[24:27], v[80:87], v1\n"
    "s_or_b64 vcc, vcc, s[56:57]\n"
    "s_cbranch_vccnz hw_faila_%=\n"
    "hw_resumea_%=:\n"
    "buffer_store_dwordx2 v[152:153], v7, s[40:43], s63 offen sc1\n"
    "v_cvt_pk_bf16_f32 v60, v56, v57\n"
    "v_smfmac_f32_16x16x64_bf16 v[72:75], v[28:31], a[240:247], v1\n"
    "v_cvt_pk_bf16_f32 v61, v58, v59\n"
    "ds_write_b64 v3, v[60:61] offset:0\n"
    "v_smfmac_f32_16x16x64_bf16 v[76:79], v[28:31], a[248:255], v1\n"
    "v_permlane32_swap_b32 v64, v68\n"
    "v_permlane32_swap_b32 v65, v69\n"
    "v_smfmac_f32_16x16x64_bf16 v[72:75], v[32:35], v[160:167], v1\n"
    "v_permlane32_swap_b32 v66, v70\n"
    "v_permlane32_swap_b32 v67, v71\n"
    "v_smfmac_f32_16x16x64_bf16 v[76:79], v[32:35], v[168:175], v1\n"
    "v_add_f32 v64, v64, v68\n"
    "v_smfmac_f32_16x16x64_bf16 v[72:75], v[36:39], v[176:183], v1\n"
    "v_add_f32 v65, v65, v69\n"
    "v_smfmac_f32_16x16x64_bf16 v[76:79], v[36:39], v[184:191], v1\n"
    "v_add_f32 v66, v66, v70\n"
    "v_smfmac_f32_16x16x64_bf16 v[72:75], v[40:43], v[192:199], v1\n"
    "v_add_f32 v67, v67, v71\n"
    "v_smfmac_f32_16x16x64_bf16 v[76:79], v[40:43], v[200:207], v1\n"
    "v_cvt_pk_bf16_f32 v68, v64, v65\n"
    "v_smfmac_f32_16x16x64_bf16 v[72:75], v[44:47], v[208:215], v1\n"
    "v_cvt_pk_bf16_f32 v69, v66, v67\n"
    "v_smfmac_f32_16x16x64_bf16 v[76:79], v[44:47], v[216:223], v1\n"
    "ds_write_b64 v3, v[68:69] offset:512\n"
    "buffer_load_dword v154, v155, s[40:43], s12 offen sc1\n"
    "v_smfmac_f32_16x16x64_bf16 v[72:75], v[48:51], v[224:231], v1\n"
    "ds_read_b128 v[16:19], v5 offset:8192\n"
    "v_smfmac_f32_16x16x64_bf16 v[76:79], v[48:51], v[232:239], v1\n"
    "ds_read_b128 v[20:23], v5 offset:9216\n"
    "v_smfmac_f32_16x16x64_bf16 v[72:75], v[52:55], v[240:247], v1\n"
    "ds_read_b128 v[24:27], v5 offset:10240\n"
    "v_smfmac_f32_16x16x64_bf16 v[76:79], v[52:55], v[248:255], v1\n"
    "ds_read_b128 v[28:31], v5 offset:11264\n"
    "s_waitcnt lgkmcnt(4)\n"
    "s_barrier\n"
    "ds_read_b64_tr_b16 v[136:137], v4 offset:0\n"
    "ds_read_b64_tr_b16 v[138:139], v4 offset:512\n"
    "v_mov_b32 v13, v13\n"
    "ds_read_b64_tr_b16 v[140:141], v4 offset:1536\n"
    "ds_read_b64_tr_b16 v[142:143], v4 offset:2048\n"
    "v_mov_b32 v13, v13\n"
    "ds_read_b64_tr_b16 v[144:145], v4 offset:3072\n"
    "ds_read_b64_tr_b16 v[146:147], v4 offset:3584\n"
    "v_mov_b32 v13, v13\n"
    "ds_read_b64_tr_b16 v[148:149], v4 offset:4608\n"
    "ds_read_b64_tr_b16 v[150:151], v4 offset:5120\n"
    "v_mov_b32 v13, v13\n"
    "v_permlane32_swap_b32 v72, v76\n"
    "v_permlane32_swap_b32 v73, v77\n"
    "v_permlane32_swap_b32 v74, v78\n"
    "v_permlane32_swap_b32 v75, v79\n"
    "v_pk_add_f32 v[72:73], v[72:73], v[76:77]\n"
    "v_pk_add_f32 v[74:75], v[74:75], v[78:79]\n"
    "v_cvt_pk_bf16_f32 v76, v72, v73\n"
    "v_cvt_pk_bf16_f32 v77, v74, v75\n"
    "ds_write_b64 v3, v[76:77] offset:1024\n"
    "s_waitcnt lgkmcnt(7)\n"
    "v_mfma_f32_16x16x32_bf16 v[56:59], v[104:107], v[136:139], v[8:11]\n"
    "v_mfma_f32_16x16x32_bf16 v[60:63], v[108:111], v[136:139], v[8:11]\n"
    "v_mfma_f32_16x16x32_bf16 v[64:67], v[136:139], v[136:139], 0\n"
    "s_waitcnt lgkmcnt(5)\n"
    "v_mfma_f32_16x16x32_bf16 v[56:59], v[112:115], v[140:143], v[56:59]\n"
    "s_waitcnt lgkmcnt(0)\n"
    "s_barrier\n"
    "ds_read_b64_tr_b16 v[32:33], v4 offset:1024\n"
    "ds_read_b64_tr_b16 v[34:35], v4 offset:2560\n"
    "ds_read_b64_tr_b16 v[36:37], v4 offset:4096\n"
    "ds_read_b64_tr_b16 v[38:39], v4 offset:5632\n"
    "v_mfma_f32_16x16x32_bf16 v[60:63], v[116:119], v[140:143], v[60:63]\n"
    "v_mfma_f32_16x16x32_bf16 v[64:67], v[140:143], v[140:143], v[64:67]\n"
    "v_mfma_f32_16x16x32_bf16 v[56:59], v[120:123], v[144:147], v[56:59]\n"
    "v_mfma_f32_16x16x32_bf16 v[60:63], v[124:127], v[144:147], v[60:63]\n"
    "v_mfma_f32_16x16x32_bf16 v[64:67], v[144:147], v[144:147], v[64:67]\n"
    "v_mfma_f32_16x16x32_bf16 v[56:59], v[128:131], v[148:151], v[56:59]\n"
    "v_mfma_f32_16x16x32_bf16 v[60:63], v[132:135], v[148:151], v[60:63]\n"
    "v_mfma_f32_16x16x32_bf16 v[64:67], v[148:151], v[148:151], v[64:67]\n"
    "s_waitcnt lgkmcnt(2)\n"
    "v_mfma_f32_16x16x32_bf16 v[64:67], v[32:35], v[32:35], v[64:67]\n"
    "s_waitcnt lgkmcnt(0)\n"
    "v_mfma_f32_16x16x32_bf16 v[64:67], v[36:39], v[36:39], v[64:67]\n"
    "v_mfma_f32_16x16x32_bf16 v[56:59], v[16:19], v[32:35], v[56:59]\n"
    "v_mfma_f32_16x16x32_bf16 v[56:59], v[24:27], v[36:39], v[56:59]\n"
    "v_mfma_f32_16x16x32_bf16 v[60:63], v[20:23], v[32:35], v[60:63]\n"
    "v_mfma_f32_16x16x32_bf16 v[60:63], v[28:31], v[36:39], v[60:63]\n"
    "s_load_dwordx2 s[16:17], s[14:15], 0x0\n"
    "v_mov_b32 v13, v13\n"
    "v_mov_b32 v13, v13\n"
    "v_mov_b32 v13, v13\n"
    "v_permlane16_swap_b32 v64, v72\n"
    "v_permlane16_swap_b32 v65, v73\n"
    "v_permlane16_swap_b32 v66, v74\n"
    "v_permlane16_swap_b32 v67, v75\n"
    "v_mov_b32_dpp v56, v64 row_shr:8 row_mask:0x4 bank_mask:0xc\n"
    "v_mov_b32_dpp v57, v65 row_shr:8 row_mask:0x4 bank_mask:0xc\n"
    "v_mov_b32_dpp v58, v66 row_shr:8 row_mask:0x4 bank_mask:0xc\n"
    "v_mov_b32_dpp v59, v67 row_shr:8 row_mask:0x4 bank_mask:0xc\n"
    "v_mov_b32_dpp v60, v72 row_shr:8 row_mask:0x4 bank_mask:0xc\n"
    "v_mov_b32_dpp v61, v73 row_shr:8 row_mask:0x4 bank_mask:0xc\n"
    "v_mov_b32_dpp v62, v74 row_shr:8 row_mask:0x4 bank_mask:0xc\n"
    "v_mov_b32_dpp v63, v75 row_shr:8 row_mask:0x4 bank_mask:0xc\n"
    "v_max3_f32 v68, v60, v61, v62\n"
    "v_max3_f32 v69, v56, v57, v58\n"
    "v_max3_f32 v68, v68, v63, v59\n"
    "v_max_f32 v68, v68, v69\n"
    "s_nop 1\n"
    "v_max_f32_dpp v68, v68, v68 row_ror:8 row_mask:0xf bank_mask:0xf\n"
    "v_mov_b32 v69, v68\n"
    "s_nop 1\n"
    "v_permlane16_swap_b32 v68, v69\n"
    "v_max_f32 v68, v68, v69\n"
    "v_mov_b32 v69, v68\n"
    "s_nop 1\n"
    "v_permlane32_swap_b32 v68, v69\n"
    "v_max_f32 v68, v68, v69\n"
    "v_mul_f32 v68, 0xbdd53b95, v68\n"
    "v_pk_fma_f32 v[56:57], v[56:57], s[64:65], v[68:69] op_sel_hi:[1,1,0]\n"
    "v_pk_fma_f32 v[58:59], v[58:59], s[64:65], v[68:69] op_sel_hi:[1,1,0]\n"
    "v_pk_fma_f32 v[60:61], v[60:61], s[64:65], v[68:69] op_sel_hi:[1,1,0]\n"
    "v_pk_fma_f32 v[62:63], v[62:63], s[64:65], v[68:69] op_sel_hi:[1,1,0]\n"
    "v_exp_f32 v56, v56\n"
    "v_exp_f32 v57, v57\n"
    "v_exp_f32 v58, v58\n"
    "v_exp_f32 v59, v59\n"
    "v_exp_f32 v60, v60\n"
    "v_exp_f32 v61, v61\n"
    "v_exp_f32 v62, v62\n"
    "v_exp_f32 v63, v63\n"
    "v_pk_add_f32 v[64:65], v[56:57], v[58:59]\n"
    "v_pk_add_f32 v[66:67], v[60:61], v[62:63]\n"
    "v_pk_add_f32 v[64:65], v[64:65], v[66:67]\n"
    "v_add_f32 v68, v64, v65\n"
    "s_nop 1\n"
    "v_add_f32_dpp v68, v68, v68 row_ror:8 row_mask:0xf bank_mask:0xf\n"
    "v_mov_b32 v69, v68\n"
    "s_nop 1\n"
    "v_permlane16_swap_b32 v68, v69\n"
    "v_add_f32 v68, v68, v69\n"
    "v_mov_b32 v69, v68\n"
    "s_nop 1\n"
    "v_permlane32_swap_b32 v68, v69\n"
    "v_add_f32 v68, v68, v69\n"
    "v_rcp_f32 v68, v68\n"
    "s_nop 0\n"
    "v_pk_mul_f32 v[56:57], v[56:57], v[68:69] op_sel_hi:[1,0]\n"
    "v_pk_mul_f32 v[58:59], v[58:59], v[68:69] op_sel_hi:[1,0]\n"
    "v_pk_mul_f32 v[60:61], v[60:61], v[68:69] op_sel_hi:[1,0]\n"
    "v_pk_mul_f32 v[62:63], v[62:63], v[68:69] op_sel_hi:[1,0]\n"
    "v_cvt_pk_bf16_f32 v64, v56, v57\n"
    "v_cvt_pk_bf16_f32 v65, v58, v59\n"
    "v_cvt_pk_bf16_f32 v66, v60, v61\n"
    "v_cvt_pk_bf16_f32 v67, v62, v63\n"
    "buffer_store_dwordx4 v[64:67], v6, s[40:43], s53 offen sc1\n"
    "buffer_store_dwordx4 v[64:67], v6, s[40:43], s50 offen sc1\n"
    "buffer_store_dwordx4 v[64:67], v6, s[40:43], s51 offen sc1\n"
    "buffer_store_dwordx4 v[64:67], v6, s[40:43], s52 offen sc1\n"
    "s_memrealtime s[30:31]\n"
    "s_nop 1\n"
    "s_memrealtime s[18:19]\n"
    "s_nop 1\n"
    "s_memrealtime s[34:35]\n"
    "s_nop 1\n"
    "s_memrealtime s[36:37]\n"
    "s_nop 1\n"
    "s_memrealtime s[38:39]\n"
    "s_nop 1\n"
    "s_memrealtime s[94:95]\n"
    "s_nop 1\n"
    "s_memrealtime s[96:97]\n"
    "s_nop 1\n"
    "s_memrealtime s[98:99]\n"
    "s_nop 1\n"
    "s_waitcnt lgkmcnt(0)\n"
    "s_add_u32 s18, s18, s34\n"
    "s_add_u32 s18, s18, s36\n"
    "s_add_u32 s18, s18, s38\n"
    "s_add_u32 s18, s18, s94\n"
    "s_add_u32 s18, s18, s96\n"
    "s_add_u32 s18, s18, s98\n"
    "s_mul_i32 s34, s30, 7\n"
    "s_sub_u32 s18, s18, s34\n"
    "s_mul_i32 s18, s18, 3\n"
    "s_sub_u32 s59, s30, s62\n"
    "s_mul_i32 s59, s59, 23\n"
    "s_add_u32 s59, s59, s18\n"
    "v_mov_b32 v13, s59\n"
    "s_lshl_b32 s34, s47, 10\n"
    "s_add_u32 s34, s34, 2368000\n"
    "s_cmp_eq_u32 s60, 0\n"
    "s_cselect_b32 s34, s34, 0x40000000\n"
    "buffer_atomic_umax v13, v159, s[40:43], s34 offen\n"
    "s_waitcnt vmcnt(5)\n"
    "v_readlane_b32 s0, v154, 0\n"
    "v_readlane_b32 s1, v154, 1\n"
    "v_readlane_b32 s2, v154, 2\n"
    "v_readlane_b32 s3, v154, 3\n"
    "v_readlane_b32 s4, v154, 4\n"
    "v_readlane_b32 s5, v154, 5\n"
    "v_readlane_b32 s6, v154, 6\n"
    "v_readlane_b32 s7, v154, 7\n"
    "s_max_u32 s0, s0, s1\n"
    "s_max_u32 s0, s0, s2\n"
    "s_max_u32 s0, s0, s3\n"
    "s_sub_u32 s59, s0, s10\n"
    "s_add_u32 s57, s59, 500\n"
    "s_cmp_lt_u32 s57, 900\n"
    "s_cselect_b32 s59, s59, 0\n"
    "s_ashr_i32 s57, s59, 4\n"
    "s_max_i32 s59, s59, s57\n"
    "s_min_i32 s59, s59, s73\n"
    "s_add_u32 s10, s10, s59\n"
    "s_add_u32 s59, s10, s71\n"
    "s_mul_i32 s59, s59, 2850\n"
    "s_lshr_b32 s67, s59, 16\n"
    "s_max_u32 s4, s4, s5\n"
    "s_max_u32 s4, s4, s6\n"
    "s_max_u32 s4, s4, s7\n"
    "s_sub_u32 s59, s4, s11\n"
    "s_add_u32 s57, s59, 500\n"
    "s_cmp_lt_u32 s57, 900\n"
    "s_cselect_b32 s59, s59, 0\n"
    "s_ashr_i32 s57, s59, 4\n"
    "s_max_i32 s59, s59, s57\n"
    "s_min_i32 s59, s59, s73\n"
    "s_add_u32 s11, s11, s59\n"
    "s_add_u32 s59, s11, s72\n"
    "s_mul_i32 s59, s59, 2850\n"
    "s_lshr_b32 s66, s59, 16\n"
    "v_add_u32 v3, s61, v3\n"
    "v_add_u32 v4, s61, v4\n"
    "s_sub_u32 s61, 0, s61\n"
    "s_add_u32 s47, s47, 1\n"
    "s_cmp_lt_u32 s47, 128\n"
    "s_cbranch_scc1 hw_loopa_%=\n"
    "s_branch hw_end_%=\n"
    "hw_faila_%=:\n"
    "v_max_u32 v12, v16, v17\n"
    "v_max3_u32 v12, v18, v19, v12\n"
    "v_max3_u32 v12, v20, v21, v12\n"
    "v_max3_u32 v12, v22, v23, v12\n"
    "v_max3_u32 v12, v24, v25, v12\n"
    "v_max3_u32 v12, v26, v27, v12\n"
    "v_max3_u32 v12, v28, v29, v12\n"
    "v_max3_u32 v12, v30, v31, v12\n"
    "v_max3_u32 v12, v32, v33, v12\n"
    "v_max3_u32 v12, v34, v35, v12\n"
    "v_max3_u32 v12, v36, v37, v12\n"
    "v_max3_u32 v12, v38, v39, v12\n"
    "v_max3_u32 v12, v40, v41, v12\n"
    "v_max3_u32 v12, v42, v43, v12\n"
    "v_max3_u32 v12, v44, v45, v12\n"
    "v_max3_u32 v12, v46, v47, v12\n"
    "v_max3_u32 v12, v48, v49, v12\n"
    "v_max3_u32 v12, v50, v51, v12\n"
    "v_max3_u32 v12, v52, v53, v12\n"
    "v_max3_u32 v12, v54, v55, v12\n"
    "v_cmp_eq_u32 vcc, -1, v12\n"
    "s_nop 4\n"
    "s_cbranch_vccz hw_resumea_%=\n"
    "hw_polla_%=:\n"
    "s_nop 7\n"
    "s_waitcnt vmcnt(0) lgkmcnt(0)\n"
    "v_mov_b32 v56, 0\n"
    "v_mov_b32 v57, 0\n"
    "v_mov_b32 v58, 0\n"
    "v_mov_b32 v59, 0\n"
    "v_mov_b32 v60, 0\n"
    "v_mov_b32 v61, 0\n"
    "v_mov_b32 v62, 0\n"
    "v_mov_b32 v63, 0\n"
    "v_mov_b32 v64, 0\n"
    "v_mov_b32 v65, 0\n"
    "v_mov_b32 v66, 0\n"
    "v_mov_b32 v67, 0\n"
    "v_mov_b32 v68, 0\n"
    "v_mov_b32 v69, 0\n"
    "v_mov_b32 v70, 0\n"
    "v_mov_b32 v71, 0\n"
    "v_mov_b32 v72, 0\n"
    "v_mov_b32 v73, 0\n"
    "v_mov_b32 v74, 0\n"
    "v_mov_b32 v75, 0\n"
    "v_mov_b32 v76, 0\n"
    "v_mov_b32 v77, 0\n"
    "v_mov_b32 v78, 0\n"
    "v_mov_b32 v79, 0\n"
    "ds_read_b128 v[80:83], v2 offset:0\n"
    "ds_read_b128 v[84:87], v2 offset:1024\n"
    "ds_read_b128 v[88:91], v2 offset:2048\n"
    "ds_read_b128 v[92:95], v2 offset:3072\n"
    "ds_read_b128 v[96:99], v2 offset:4096\n"
    "ds_read_b128 v[100:103], v2 offset:5120\n"
    "v_sub_u32 v12, v158, v0\n"
    "v_lshl_add_u32 v12, v15, 7, v12\n"
    "v_and_b32 v18, 15, v15\n"
    "v_sub_u32 v18, v18, v15\n"
    "v_lshl_add_u32 v18, v18, 7, v12\n"
    "s_movk_i32 s55, 0x4000\n"
    "s_waitcnt vmcnt(0) lgkmcnt(0)\n"
    "s_add_u32 s60, s60, 1\n"
    "s_cmp_gt_u32 s60, 65536\n"
    "s_cbranch_scc1 hw_abort_%=\n"
    "s_and_b32 s59, s60, 63\n"
    "s_cbranch_scc1 hw_againa_%=\n"
    "v_mov_b32 v13, 1024\n"
    "buffer_load_dword v13, v13, s[40:43], s58 offen sc1\n"
    "s_waitcnt vmcnt(0)\n"
    "v_readfirstlane_b32 s59, v13\n"
    "s_nop 0\n"
    "s_cmp_lg_u32 s59, 0\n"
    "s_nop 2\n"
    "s_cbranch_scc1 hw_end_%=\n"
    "s_branch hw_againa_%=\n"
    "hw_againa_%=:\n"
    "buffer_load_dword v16, v12, s[40:43], 0 offen sc1\n"
    "buffer_load_dword v17, v18, s[40:43], s55 offen sc1\n"
    "s_waitcnt vmcnt(0)\n"
    "v_max_u32 v16, v16, v17\n"
    "v_cmp_eq_u32 vcc, -1, v16\n"
    "s_nop 4\n"
    "s_cbranch_vccz hw_relocka_%=\n"
    "s_add_u32 s60, s60, 1\n"
    "s_cmp_gt_u32 s60, 65536\n"
    "s_cbranch_scc1 hw_abort_%=\n"
    "s_and_b32 s59, s60, 63\n"
    "s_cbranch_scc1 hw_againa_%=\n"
    "v_mov_b32 v13, 1024\n"
    "buffer_load_dword v13, v13, s[40:43], s58 offen sc1\n"
    "s_waitcnt vmcnt(0)\n"
    "v_readfirstlane_b32 s59, v13\n"
    "s_nop 0\n"
    "s_cmp_lg_u32 s59, 0\n"
    "s_nop 2\n"
    "s_cbranch_scc1 hw_end_%=\n"
    "s_branch hw_againa_%=\n"
    "hw_relocka_%=:\n"
    "s_memrealtime s[56:57]\n"
    "s_waitcnt lgkmcnt(0)\n"
    "s_cmp_lt_u32 s47, s75\n"
    "s_cbranch_scc1 hw_nobump_%=\n"
    "s_add_u32 s71, s71, 12\n"
    "s_add_u32 s72, s72, 12\n"
    "hw_nobump_%=:\n"
    "s_add_u32 s59, s47, 2\n"
    "s_max_u32 s75, s75, s59\n"
    "s_add_u32 s62, s56, s68\n"
    "s_add_u32 s59, s62, s67\n"
    "v_mov_b32 v152, s59\n"
    "s_add_u32 s59, s59, s66\n"
    "v_mov_b32 v153, s59\n"
    "s_nop 1\n"
    "s_branch hw_retrya_%=\n"
    "hw_wx_%=:\n"
    "v_lshrrev_b32 v6, 3, v15\n"
    "v_lshlrev_b32 v6, 8, v6\n"
    "v_add_u32 v6, s50, v6\n"
    "v_mov_b32 v13, s51\n"
    "v_cmp_gt_u32 vcc, 32, v15\n"
    "s_nop 1\n"
    "v_cndmask_b32 v6, v13, v6, vcc\n"
    "v_lshl_add_u32 v6, v153, 4, v6\n"
    "v_and_b32 v13, 0xffffe000, v6\n"
    "v_add_u32 v6, v6, v13\n"
    "v_cmp_gt_u32 vcc, 40, v15\n"
    "v_mov_b32 v13, 2147483632\n"
    "s_nop 0\n"
    "v_cndmask_b32 v6, v13, v6, vcc\n"
    "v_lshlrev_b32 v7, 7, v152\n"
    "v_lshl_add_u32 v7, v153, 4, v7\n"
    "v_cmp_eq_u32 vcc, 1, v154\n"
    "v_mov_b32 v13, 2147483632\n"
    "s_nop 0\n"
    "v_cndmask_b32 v7, v13, v7, vcc\n"
    "s_cmp_eq_u32 s45, 1\n"
    "s_cselect_b64 vcc, -1, 0\n"
    "v_cndmask_b32 v6, v6, v7, vcc\n"
    "s_cmp_eq_u32 s45, 1\n"
    "s_cselect_b32 s59, 1, 0\n"
    "v_lshlrev_b32 v133, 7, v15\n"
    "v_cmp_gt_u32 vcc, s59, v15\n"
    "v_mov_b32 v13, 2147483632\n"
    "s_nop 0\n"
    "v_cndmask_b32 v133, v13, v133, vcc\n"
    "s_lshl_b32 s64, s44, 7\n"
    "s_add_u32 s64, s64, 2103296\n"
    "s_mov_b32 s48, 0\n"
    "s_cmp_eq_u32 s45, 2\n"
    "s_cselect_b32 s48, 2056, s48\n"
    "s_cmp_eq_u32 s45, 3\n"
    "s_cselect_b32 s48, 10240, s48\n"
    "v_add_u32 v129, s48, v5\n"
    "s_mov_b32 s48, 8\n"
    "s_cmp_eq_u32 s45, 2\n"
    "s_cselect_b32 s48, 8200, s48\n"
    "s_cmp_eq_u32 s45, 3\n"
    "s_cselect_b32 s48, 6144, s48\n"
    "v_add_u32 v130, s48, v5\n"
    "s_mov_b32 s48, 8192\n"
    "s_cmp_eq_u32 s45, 2\n"
    "s_cselect_b32 s48, 4096, s48\n"
    "s_cmp_eq_u32 s45, 3\n"
    "s_cselect_b32 s48, 6152, s48\n"
    "v_add_u32 v131, s48, v5\n"
    "s_mov_b32 s48, 2048\n"
    "s_cmp_eq_u32 s45, 2\n"
    "s_cselect_b32 s48, 4104, s48\n"
    "s_cmp_eq_u32 s45, 3\n"
    "s_cselect_b32 s48, 10248, s48\n"
    "v_add_u32 v132, s48, v5\n"
    "v_mov_b32 v104, -1\n"
    "v_mov_b32 v105, -1\n"
    "v_mov_b32 v106, -1\n"
    "v_mov_b32 v107, -1\n"
    "v_mov_b32 v108, -1\n"
    "v_mov_b32 v109, -1\n"
    "v_mov_b32 v110, -1\n"
    "v_mov_b32 v111, -1\n"
    "v_mov_b32 v112, -1\n"
    "v_mov_b32 v113, -1\n"
    "v_mov_b32 v114, -1\n"
    "v_mov_b32 v115, -1\n"
    "v_mov_b32 v116, -1\n"
    "v_mov_b32 v117, -1\n"
    "v_mov_b32 v118, -1\n"
    "v_mov_b32 v119, -1\n"
    "v_mov_b32 v120, -1\n"
    "v_mov_b32 v121, -1\n"
    "v_mov_b32 v122, -1\n"
    "v_mov_b32 v123, -1\n"
    "v_mov_b32 v124, -1\n"
    "v_mov_b32 v125, -1\n"
    "v_mov_b32 v126, -1\n"
    "v_mov_b32 v127, -1\n"
    "v_mov_b32 v8, 0\n"
    "s_mov_b32 s65, 32\n"
    "s_mov_b32 s63, 0x40000000\n"
    "hw_first_%=:\n"
    "s_mul_i32 s62, s44, 12288\n"
    "s_lshl_b32 s49, s45, 12\n"
    "s_add_u32 s62, s62, s49\n"
    "s_add_u32 s62, s62, 7761920\n"
    "s_mov_b32 s31, s62\n"
    "s_lshl_b32 s49, s47, 7\n"
    "s_add_u32 s49, s49, 10240\n"
    "v_mov_b32 v128, s49\n"
    "v_mov_b32 v14, 0\n"
    "s_mov_b32 s60, 0\n"
    "v_cmp_gt_u32 vcc, s65, v14\n"
    "s_nop 4\n"
    "s_cbranch_vccz hw_kx0_ok_%=\n"
    "hw_kx0_poll_%=:\n"
    "buffer_load_dword v14, v128, s[40:43], s58 offen sc1\n"
    "s_waitcnt vmcnt(0)\n"
    "v_cmp_gt_u32 vcc, s65, v14\n"
    "s_nop 4\n"
    "s_cbranch_vccz hw_kx0_ok_%=\n"
    "s_sleep 8\n"
    "s_add_u32 s60, s60, 1\n"
    "s_cmp_gt_u32 s60, 65536\n"
    "s_cbranch_scc1 hw_abort_%=\n"
    "s_and_b32 s59, s60, 63\n"
    "s_cbranch_scc1 hw_kx0_poll_%=\n"
    "v_mov_b32 v13, 1024\n"
    "buffer_load_dword v13, v13, s[40:43], s58 offen sc1\n"
    "s_waitcnt vmcnt(0)\n"
    "v_readfirstlane_b32 s59, v13\n"
    "s_nop 0\n"
    "s_cmp_lg_u32 s59, 0\n"
    "s_nop 2\n"
    "s_cbranch_scc1 hw_end_%=\n"
    "s_branch hw_kx0_poll_%=\n"
    "hw_kx0_ok_%=:\n"
    "buffer_load_dwordx4 v[136:139], v0, s[40:43], s62 offen sc1\n"
    "buffer_load_dwordx4 v[140:143], v0, s[40:43], s62 offen offset:1024 sc1\n"
    "buffer_load_dwordx4 v[144:147], v0, s[40:43], s62 offen offset:2048 sc1\n"
    "buffer_load_dwordx4 v[148:151], v0, s[40:43], s62 offen offset:3072 sc1\n"
    "s_waitcnt vmcnt(3)\n"
    "ds_write2st64_b64 v129, v[136:137], v[138:139] offset1:2\n"
    "s_waitcnt vmcnt(2)\n"
    "ds_write2st64_b64 v130, v[140:141], v[142:143] offset1:2\n"
    "s_waitcnt vmcnt(1)\n"
    "ds_write2st64_b64 v131, v[144:145], v[146:147] offset1:2\n"
    "s_waitcnt vmcnt(0)\n"
    "ds_write2st64_b64 v132, v[148:149], v[150:151] offset1:2\n"
    "v_add_u32 v128, 128, v128\n"
    "v_mov_b32 v14, 0\n"
    "s_nop 1\n"
    "hw_loopb_%=:\n"
    "s_add_u32 s59, s47, 0\n"
    "s_and_b32 s59, s59, 63\n"
    "s_cmp_eq_u32 s59, 31\n"
    "s_cselect_b32 s59, -31, 1\n"
    "s_mul_i32 s57, s59, 393216\n"
    "s_add_u32 s31, s31, s57\n"
    "v_cmp_gt_u32 vcc, s65, v14\n"
    "s_nop 0\n"
    "s_cmp_eq_u64 vcc, 0\n"
    "s_cselect_b32 s62, s31, 0x40000000\n"
    "v_mov_b32 v134, v128\n"
    "v_add_u32 v128, 128, v128\n"
    "s_and_b32 s48, s47, 3\n"
    "s_mul_i32 s53, s48, 196608\n"
    "s_lshl_b32 s50, s48, 2\n"
    "s_add_u32 s50, s50, s46\n"
    "s_mul_i32 s50, s50, 32768\n"
    "s_add_u32 s50, s50, 786432\n"
    "v_add_u32 v158, s50, v0\n"
    "s_add_u32 s50, s53, 49152\n"
    "s_add_u32 s51, s53, 98304\n"
    "s_add_u32 s52, s53, 147456\n"
    "v_mov_b32 v56, 0\n"
    "v_mov_b32 v57, 0\n"
    "v_mov_b32 v58, 0\n"
    "v_mov_b32 v59, 0\n"
    "v_mov_b32 v60, 0\n"
    "v_mov_b32 v61, 0\n"
    "v_mov_b32 v62, 0\n"
    "v_mov_b32 v63, 0\n"
    "v_mov_b32 v64, 0\n"
    "v_mov_b32 v65, 0\n"
    "v_mov_b32 v66, 0\n"
    "v_mov_b32 v67, 0\n"
    "v_mov_b32 v68, 0\n"
    "v_mov_b32 v69, 0\n"
    "v_mov_b32 v70, 0\n"
    "v_mov_b32 v71, 0\n"
    "v_mov_b32 v72, 0\n"
    "v_mov_b32 v73, 0\n"
    "v_mov_b32 v74, 0\n"
    "v_mov_b32 v75, 0\n"
    "v_mov_b32 v76, 0\n"
    "v_mov_b32 v77, 0\n"
    "v_mov_b32 v78, 0\n"
    "v_mov_b32 v79, 0\n"
    "ds_read_b128 v[80:83], v2 offset:0\n"
    "ds_read_b128 v[84:87], v2 offset:1024\n"
    "ds_read_b128 v[88:91], v2 offset:2048\n"
    "ds_read_b128 v[92:95], v2 offset:3072\n"
    "ds_read_b128 v[96:99], v2 offset:4096\n"
    "ds_read_b128 v[100:103], v2 offset:5120\n"
    "s_xor_b32 s48, s48, 2\n"
    "s_mul_i32 s54, s48, 196608\n"
    "s_and_b32 s63, s47, 31\n"
    "s_lshl_b32 s63, s63, 5\n"
    "s_add_u32 s63, s63, s44\n"
    "s_mul_i32 s63, s63, 3072\n"
    "s_add_u32 s63, s63, 127336448\n"
    "s_cmp_eq_u32 s45, 1\n"
    "s_cselect_b32 s63, s63, 0x40000000\n"
    "s_movk_i32 s49, 512\n"
    "s_cmp_eq_u32 s45, 2\n"
    "s_cselect_b32 s48, 49152, s49\n"
    "s_cselect_b32 s34, s54, s63\n"
    "s_cselect_b32 s49, 0x40000000, 0\n"
    "s_add_u32 s35, s34, s48\n"
    "s_add_u32 s36, s35, s48\n"
    "s_add_u32 s37, s36, s48\n"
    "s_add_u32 s38, s37, s48\n"
    "s_add_u32 s39, s38, s48\n"
    "s_or_b32 s38, s38, s49\n"
    "s_or_b32 s39, s39, s49\n"
    "s_waitcnt lgkmcnt(0)\n"
    "s_barrier\n"
    "s_mov_b32 s60, 0\n"
    "hw_retryb_%=:\n"
    "buffer_load_dwordx4 v[16:19], v158, s[40:43], s20 offen sc1\n"
    "s_nop 7\n"
    "buffer_load_dwordx4 v[20:23], v158, s[40:43], s21 offen sc1\n"
    "s_nop 7\n"
    "buffer_load_dwordx4 v[24:27], v158, s[40:43], s22 offen sc1\n"
    "s_nop 7\n"
    "buffer_load_dwordx4 v[28:31], v158, s[40:43], s23 offen sc1\n"
    "s_nop 7\n"
    "buffer_load_dwordx4 v[32:35], v158, s[40:43], s24 offen sc1\n"
    "s_nop 7\n"
    "buffer_load_dwordx4 v[36:39], v158, s[40:43], s25 offen sc1\n"
    "s_nop 7\n"
    "buffer_load_dwordx4 v[40:43], v158, s[40:43], s26 offen sc1\n"
    "s_nop 7\n"
    "buffer_load_dwordx4 v[44:47], v158, s[40:43], s27 offen sc1\n"
    "s_nop 7\n"
    "buffer_load_dwordx4 v[48:51], v158, s[40:43], s28 offen sc1\n"
    "s_nop 7\n"
    "buffer_load_dwordx4 v[52:55], v158, s[40:43], s29 offen sc1\n"
    "s_waitcnt vmcnt(9)\n"
    "v_smfmac_f32_16x16x64_bf16 v[68:71], v[16:19], v[80:87], v1\n"
    "ds_read_b128 v[80:83], v2 offset:6144\n"
    "v_smfmac_f32_16x16x64_bf16 v[56:59], v[16:19], a[0:7], v1\n"
    "ds_read_b128 v[84:87], v2 offset:7168\n"
    "v_smfmac_f32_16x16x64_bf16 v[64:67], v[16:19], a[8:15], v1\n"
    "v_smfmac_f32_16x16x64_bf16 v[60:63], v[16:19], a[16:23], v1\n"
    "buffer_store_dword v8, v133, s[40:43], s64 offen sc1\n"
    "s_waitcnt vmcnt(9)\n"
    "v_smfmac_f32_16x16x64_bf16 v[68:71], v[20:23], v[88:95], v1\n"
    "ds_read_b128 v[88:91], v2 offset:8192\n"
    "v_smfmac_f32_16x16x64_bf16 v[56:59], v[20:23], a[24:31], v1\n"
    "ds_read_b128 v[92:95], v2 offset:9216\n"
    "v_smfmac_f32_16x16x64_bf16 v[64:67], v[20:23], v[96:103], v1\n"
    "ds_read_b128 v[96:99], v2 offset:10240\n"
    "v_smfmac_f32_16x16x64_bf16 v[60:63], v[20:23], a[32:39], v1\n"
    "ds_read_b128 v[100:103], v2 offset:11264\n"
    "s_waitcnt vmcnt(8) lgkmcnt(4)\n"
    "v_smfmac_f32_16x16x64_bf16 v[68:71], v[24:27], v[80:87], v1\n"
    "ds_read_b128 v[80:83], v2 offset:12288\n"
    "v_smfmac_f32_16x16x64_bf16 v[56:59], v[24:27], a[40:47], v1\n"
    "ds_read_b128 v[84:87], v2 offset:13312\n"
    "v_smfmac_f32_16x16x64_bf16 v[64:67], v[24:27], a[48:55], v1\n"
    "v_smfmac_f32_16x16x64_bf16 v[60:63], v[24:27], a[56:63], v1\n"
    "v_max3_u32 v12, v16, v20, v24\n"
    "s_waitcnt vmcnt(7) lgkmcnt(4)\n"
    "v_smfmac_f32_16x16x64_bf16 v[68:71], v[28:31], v[88:95], v1\n"
    "ds_read_b128 v[88:91], v2 offset:14336\n"
    "v_smfmac_f32_16x16x64_bf16 v[56:59], v[28:31], a[64:71], v1\n"
    "ds_read_b128 v[92:95], v2 offset:15360\n"
    "v_smfmac_f32_16x16x64_bf16 v[64:67], v[28:31], a[72:79], v1\n"
    "v_cmp_eq_u32 vcc, -1, v12\n"
    "v_smfmac_f32_16x16x64_bf16 v[60:63], v[28:31], a[80:87], v1\n"
    "s_cbranch_vccnz hw_pollb_%=\n"
    "s_waitcnt vmcnt(6) lgkmcnt(4)\n"
    "v_smfmac_f32_16x16x64_bf16 v[68:71], v[32:35], v[96:103], v1\n"
    "ds_read_b128 v[96:99], v2 offset:16384\n"
    "v_smfmac_f32_16x16x64_bf16 v[56:59], v[32:35], a[88:95], v1\n"
    "ds_read_b128 v[100:103], v2 offset:17408\n"
    "s_waitcnt lgkmcnt(4)\n"
    "v_smfmac_f32_16x16x64_bf16 v[64:67], v[32:35], v[80:87], v1\n"
    "ds_read_b128 v[80:83], v2 offset:18432\n"
    "v_smfmac_f32_16x16x64_bf16 v[60:63], v[32:35], a[96:103], v1\n"
    "ds_read_b128 v[84:87], v2 offset:19456\n"
    "s_waitcnt vmcnt(5) lgkmcnt(4)\n"
    "v_smfmac_f32_16x16x64_bf16 v[68:71], v[36:39], v[88:95], v1\n"
    "ds_read_b128 v[88:91], v2 offset:20480\n"
    "v_smfmac_f32_16x16x64_bf16 v[56:59], v[36:39], a[104:111], v1\n"
    "ds_read_b128 v[92:95], v2 offset:21504\n"
    "v_smfmac_f32_16x16x64_bf16 v[64:67], v[36:39], a[112:119], v1\n"
    "buffer_load_dwordx4 v[136:139], v0, s[40:43], s62 offen offset:0 sc1\n"
    "v_smfmac_f32_16x16x64_bf16 v[60:63], v[36:39], a[120:127], v1\n"
    "buffer_load_dwordx4 v[140:143], v0, s[40:43], s62 offen offset:1024 sc1\n"
    "s_waitcnt vmcnt(6) lgkmcnt(4)\n"
    "v_smfmac_f32_16x16x64_bf16 v[68:71], v[40:43], v[96:103], v1\n"
    "ds_read_b128 v[96:99], v2 offset:22528\n"
    "v_smfmac_f32_16x16x64_bf16 v[56:59], v[40:43], a[128:135], v1\n"
    "ds_read_b128 v[100:103], v2 offset:23552\n"
    "v_smfmac_f32_16x16x64_bf16 v[64:67], v[40:43], a[136:143], v1\n"
    "buffer_load_dwordx4 v[144:147], v0, s[40:43], s62 offen offset:2048 sc1\n"
    "v_smfmac_f32_16x16x64_bf16 v[60:63], v[40:43], a[144:151], v1\n"
    "buffer_load_dwordx4 v[148:151], v0, s[40:43], s62 offen offset:3072 sc1\n"
    "s_waitcnt vmcnt(7) lgkmcnt(4)\n"
    "v_smfmac_f32_16x16x64_bf16 v[68:71], v[44:47], v[80:87], v1\n"
    "ds_read_b128 v[80:83], v2 offset:24576\n"
    "v_smfmac_f32_16x16x64_bf16 v[56:59], v[44:47], a[152:159], v1\n"
    "ds_read_b128 v[84:87], v2 offset:25600\n"
    "s_waitcnt lgkmcnt(4)\n"
    "v_smfmac_f32_16x16x64_bf16 v[64:67], v[44:47], v[88:95], v1\n"
    "ds_read_b128 v[88:91], v2 offset:26624\n"
    "v_smfmac_f32_16x16x64_bf16 v[60:63], v[44:47], a[160:167], v1\n"
    "ds_read_b128 v[92:95], v2 offset:27648\n"
    "s_waitcnt vmcnt(6) lgkmcnt(4)\n"
    "v_smfmac_f32_16x16x64_bf16 v[68:71], v[48:51], v[96:103], v1\n"
    "ds_read_b128 v[96:99], v2 offset:28672\n"
    "v_smfmac_f32_16x16x64_bf16 v[56:59], v[48:51], a[168:175], v1\n"
    "ds_read_b128 v[100:103], v2 offset:29696\n"
    "v_smfmac_f32_16x16x64_bf16 v[64:67], v[48:51], a[176:183], v1\n"
    "buffer_load_dword v14, v128, s[40:43], s58 offen sc1\n"
    "v_smfmac_f32_16x16x64_bf16 v[60:63], v[48:51], a[184:191], v1\n"
    "s_waitcnt vmcnt(6) lgkmcnt(4)\n"
    "v_smfmac_f32_16x16x64_bf16 v[68:71], v[52:55], v[80:87], v1\n"
    "ds_read_b128 v[80:83], v2 offset:30720\n"
    "v_smfmac_f32_16x16x64_bf16 v[56:59], v[52:55], a[192:199], v1\n"
    "ds_read_b128 v[84:87], v2 offset:31744\n"
    "v_smfmac_f32_16x16x64_bf16 v[64:67], v[52:55], a[200:207], v1\n"
    "v_smfmac_f32_16x16x64_bf16 v[60:63], v[52:55], a[208:215], v1\n"
    "v_smfmac_f32_16x16x64_bf16 v[72:75], v[16:19], a[216:223], v1\n"
    "v_mov_b32 v13, v13\n"
    "v_mov_b32 v13, v13\n"
    "v_mov_b32 v13, v13\n"
    "v_mov_b32 v13, v13\n"
    "v_mov_b32 v13, v13\n"
    "v_mov_b32 v13, v13\n"
    "v_mov_b32 v13, v13\n"
    "v_permlane32_swap_b32 v56, v60\n"
    "v_permlane32_swap_b32 v57, v61\n"
    "s_waitcnt lgkmcnt(4)\n"
    "v_smfmac_f32_16x16x64_bf16 v[76:79], v[16:19], v[88:95], v1\n"
    "v_permlane32_swap_b32 v58, v62\n"
    "v_permlane32_swap_b32 v59, v63\n"
    "v_smfmac_f32_16x16x64_bf16 v[72:75], v[20:23], a[224:231], v1\n"
    "v_add_f32 v56, v56, v60\n"
    "v_add_f32 v57, v57, v61\n"
    "s_waitcnt lgkmcnt(2)\n"
    "v_smfmac_f32_16x16x64_bf16 v[76:79], v[20:23], v[96:103], v1\n"
    "v_add_f32 v58, v58, v62\n"
    "v_add_f32 v59, v59, v63\n"
    "v_smfmac_f32_16x16x64_bf16 v[72:75], v[24:27], a[232:239], v1\n"
    "v_cmp_u_f32 vcc, v56, v57\n"
    "v_cmp_u_f32 s[56:57], v58, v59\n"
    "s_waitcnt lgkmcnt(0)\n"
    "v_smfmac_f32_16x16x64_bf16 v[76:79], v[24:27], v[80:87], v1\n"
    "s_or_b64 vcc, vcc, s[56:57]\n"
    "s_cbranch_vccnz hw_failb_%=\n"
    "hw_resumeb_%=:\n"
    "v_cvt_pk_bf16_f32 v60, v56, v57\n"
    "v_smfmac_f32_16x16x64_bf16 v[72:75], v[28:31], a[240:247], v1\n"
    "v_cvt_pk_bf16_f32 v61, v58, v59\n"
    "ds_write_b64 v3, v[60:61] offset:0\n"
    "v_smfmac_f32_16x16x64_bf16 v[76:79], v[28:31], a[248:255], v1\n"
    "v_permlane32_swap_b32 v64, v68\n"
    "v_permlane32_swap_b32 v65, v69\n"
    "v_smfmac_f32_16x16x64_bf16 v[72:75], v[32:35], v[160:167], v1\n"
    "v_permlane32_swap_b32 v66, v70\n"
    "v_permlane32_swap_b32 v67, v71\n"
    "v_smfmac_f32_16x16x64_bf16 v[76:79], v[32:35], v[168:175], v1\n"
    "v_add_f32 v64, v64, v68\n"
    "v_smfmac_f32_16x16x64_bf16 v[72:75], v[36:39], v[176:183], v1\n"
    "v_add_f32 v65, v65, v69\n"
    "v_smfmac_f32_16x16x64_bf16 v[76:79], v[36:39], v[184:191], v1\n"
    "v_add_f32 v66, v66, v70\n"
    "v_smfmac_f32_16x16x64_bf16 v[72:75], v[40:43], v[192:199], v1\n"
    "v_add_f32 v67, v67, v71\n"
    "v_smfmac_f32_16x16x64_bf16 v[76:79], v[40:43], v[200:207], v1\n"
    "v_cvt_pk_bf16_f32 v68, v64, v65\n"
    "v_smfmac_f32_16x16x64_bf16 v[72:75], v[44:47], v[208:215], v1\n"
    "v_cvt_pk_bf16_f32 v69, v66, v67\n"
    "v_smfmac_f32_16x16x64_bf16 v[76:79], v[44:47], v[216:223], v1\n"
    "ds_write_b64 v3, v[68:69] offset:512\n"
    "v_smfmac_f32_16x16x64_bf16 v[72:75], v[48:51], v[224:231], v1\n"
    "v_smfmac_f32_16x16x64_bf16 v[76:79], v[48:51], v[232:239], v1\n"
    "v_smfmac_f32_16x16x64_bf16 v[72:75], v[52:55], v[240:247], v1\n"
    "v_smfmac_f32_16x16x64_bf16 v[76:79], v[52:55], v[248:255], v1\n"
    "s_waitcnt lgkmcnt(0)\n"
    "s_barrier\n"
    "v_mov_b32 v13, v13\n"
    "v_mov_b32 v13, v13\n"
    "v_mov_b32 v13, v13\n"
    "v_mov_b32 v13, v13\n"
    "v_mov_b32 v13, v13\n"
    "v_mov_b32 v13, v13\n"
    "v_permlane32_swap_b32 v72, v76\n"
    "v_permlane32_swap_b32 v73, v77\n"
    "v_permlane32_swap_b32 v74, v78\n"
    "v_permlane32_swap_b32 v75, v79\n"
    "v_pk_add_f32 v[72:73], v[72:73], v[76:77]\n"
    "v_pk_add_f32 v[74:75], v[74:75], v[78:79]\n"
    "v_cvt_pk_bf16_f32 v76, v72, v73\n"
    "v_cvt_pk_bf16_f32 v77, v74, v75\n"
    "ds_write_b64 v3, v[76:77] offset:1024\n"
    "s_waitcnt lgkmcnt(0)\n"
    "s_barrier\n"
    "s_cmp_lg_u32 s45, 1\n"
    "s_cbranch_scc1 hw_n1_%=\n"
    "ds_read_b64_tr_b16 v[104:105], v4 offset:0\n"
    "ds_read_b64_tr_b16 v[106:107], v4 offset:512\n"
    "ds_read_b64_tr_b16 v[108:109], v4 offset:1536\n"
    "ds_read_b64_tr_b16 v[110:111], v4 offset:2048\n"
    "ds_read_b64_tr_b16 v[112:113], v4 offset:3072\n"
    "ds_read_b64_tr_b16 v[114:115], v4 offset:3584\n"
    "ds_read_b64_tr_b16 v[116:117], v4 offset:4608\n"
    "ds_read_b64_tr_b16 v[118:119], v4 offset:5120\n"
    "ds_read_b64_tr_b16 v[120:121], v4 offset:1024\n"
    "ds_read_b64_tr_b16 v[122:123], v4 offset:2560\n"
    "ds_read_b64_tr_b16 v[124:125], v4 offset:4096\n"
    "ds_read_b64_tr_b16 v[126:127], v4 offset:5632\n"
    "s_waitcnt lgkmcnt(0)\n"
    "hw_n1_%=:\n"
    "buffer_store_dwordx4 v[104:107], v6, s[40:43], s34 offen sc1\n"
    "buffer_store_dwordx4 v[108:111], v6, s[40:43], s35 offen sc1\n"
    "buffer_store_dwordx4 v[112:115], v6, s[40:43], s36 offen sc1\n"
    "buffer_store_dwordx4 v[116:119], v6, s[40:43], s37 offen sc1\n"
    "buffer_store_dwordx4 v[120:123], v6, s[40:43], s38 offen sc1\n"
    "buffer_store_dwordx4 v[124:127], v6, s[40:43], s39 offen sc1\n"
    "s_waitcnt vmcnt(10)\n"
    "ds_write2st64_b64 v129, v[136:137], v[138:139] offset1:2\n"
    "s_waitcnt vmcnt(9)\n"
    "ds_write2st64_b64 v130, v[140:141], v[142:143] offset1:2\n"
    "s_waitcnt vmcnt(8)\n"
    "ds_write2st64_b64 v131, v[144:145], v[146:147] offset1:2\n"
    "s_waitcnt vmcnt(7)\n"
    "ds_write2st64_b64 v132, v[148:149], v[150:151] offset1:2\n"
    "s_add_u32 s49, s47, 1\n"
    "v_mov_b32 v8, s49\n"
    "s_waitcnt vmcnt(6)\n"
    "v_add_u32 v3, s61, v3\n"
    "v_add_u32 v4, s61, v4\n"
    "s_sub_u32 s61, 0, s61\n"
    "s_add_u32 s47, s47, 1\n"
    "s_and_b32 s59, s47, 63\n"
    "s_cbranch_scc0 hw_flush_%=\n"
    "s_cmp_eq_u32 s62, s31\n"
    "s_cbranch_scc1 hw_loopb_%=\n"
    "v_mov_b32 v135, 0\n"
    "s_mov_b32 s60, 0\n"
    "v_cmp_gt_u32 vcc, s65, v135\n"
    "s_nop 4\n"
    "s_cbranch_vccz hw_kx1_ok_%=\n"
    "hw_kx1_poll_%=:\n"
    "buffer_load_dword v135, v134, s[40:43], s58 offen sc1\n"
    "s_waitcnt vmcnt(0)\n"
    "v_cmp_gt_u32 vcc, s65, v135\n"
    "s_nop 4\n"
    "s_cbranch_vccz hw_kx1_ok_%=\n"
    "s_sleep 4\n"
    "s_add_u32 s60, s60, 1\n"
    "s_cmp_gt_u32 s60, 65536\n"
    "s_cbranch_scc1 hw_abort_%=\n"
    "s_and_b32 s59, s60, 63\n"
    "s_cbranch_scc1 hw_kx1_poll_%=\n"
    "v_mov_b32 v13, 1024\n"
    "buffer_load_dword v13, v13, s[40:43], s58 offen sc1\n"
    "s_waitcnt vmcnt(0)\n"
    "v_readfirstlane_b32 s59, v13\n"
    "s_nop 0\n"
    "s_cmp_lg_u32 s59, 0\n"
    "s_nop 2\n"
    "s_cbranch_scc1 hw_end_%=\n"
    "s_branch hw_kx1_poll_%=\n"
    "hw_kx1_ok_%=:\n"
    "buffer_load_dwordx4 v[136:139], v0, s[40:43], s31 offen sc1\n"
    "buffer_load_dwordx4 v[140:143], v0, s[40:43], s31 offen offset:1024 sc1\n"
    "buffer_load_dwordx4 v[144:147], v0, s[40:43], s31 offen offset:2048 sc1\n"
    "buffer_load_dwordx4 v[148:151], v0, s[40:43], s31 offen offset:3072 sc1\n"
    "s_waitcnt vmcnt(3)\n"
    "ds_write2st64_b64 v129, v[136:137], v[138:139] offset1:2\n"
    "s_waitcnt vmcnt(2)\n"
    "ds_write2st64_b64 v130, v[140:141], v[142:143] offset1:2\n"
    "s_waitcnt vmcnt(1)\n"
    "ds_write2st64_b64 v131, v[144:145], v[146:147] offset1:2\n"
    "s_waitcnt vmcnt(0)\n"
    "ds_write2st64_b64 v132, v[148:149], v[150:151] offset1:2\n"
    "s_branch hw_loopb_%=\n"
    "hw_flush_%=:\n"
    "s_waitcnt vmcnt(0)\n"
    "buffer_store_dword v8, v133, s[40:43], s64 offen sc1\n"
    "s_cmp_lt_u32 s47, 128\n"
    "s_nop 0\n"
    "s_cbranch_scc1 hw_first_%=\n"
    "s_branch hw_end_%=\n"
    "hw_failb_%=:\n"
    "v_max_u32 v12, v16, v17\n"
    "v_max3_u32 v12, v18, v19, v12\n"
    "v_max3_u32 v12, v20, v21, v12\n"
    "v_max3_u32 v12, v22, v23, v12\n"
    "v_max3_u32 v12, v24, v25, v12\n"
    "v_max3_u32 v12, v26, v27, v12\n"
    "v_max3_u32 v12, v28, v29, v12\n"
    "v_max3_u32 v12, v30, v31, v12\n"
    "v_max3_u32 v12, v32, v33, v12\n"
    "v_max3_u32 v12, v34, v35, v12\n"
    "v_max3_u32 v12, v36, v37, v12\n"
    "v_max3_u32 v12, v38, v39, v12\n"
    "v_max3_u32 v12, v40, v41, v12\n"
    "v_max3_u32 v12, v42, v43, v12\n"
    "v_max3_u32 v12, v44, v45, v12\n"
    "v_max3_u32 v12, v46, v47, v12\n"
    "v_max3_u32 v12, v48, v49, v12\n"
    "v_max3_u32 v12, v50, v51, v12\n"
    "v_max3_u32 v12, v52, v53, v12\n"
    "v_max3_u32 v12, v54, v55, v12\n"
    "v_cmp_eq_u32 vcc, -1, v12\n"
    "s_nop 4\n"
    "s_cbranch_vccz hw_resumeb_%=\n"
    "hw_pollb_%=:\n"
    "s_nop 7\n"
    "s_waitcnt vmcnt(0) lgkmcnt(0)\n"
    "v_mov_b32 v56, 0\n"
    "v_mov_b32 v57, 0\n"
    "v_mov_b32 v58, 0\n"
    "v_mov_b32 v59, 0\n"
    "v_mov_b32 v60, 0\n"
    "v_mov_b32 v61, 0\n"
    "v_mov_b32 v62, 0\n"
    "v_mov_b32 v63, 0\n"
    "v_mov_b32 v64, 0\n"
    "v_mov_b32 v65, 0\n"
    "v_mov_b32 v66, 0\n"
    "v_mov_b32 v67, 0\n"
    "v_mov_b32 v68, 0\n"
    "v_mov_b32 v69, 0\n"
    "v_mov_b32 v70, 0\n"
    "v_mov_b32 v71, 0\n"
    "v_mov_b32 v72, 0\n"
    "v_mov_b32 v73, 0\n"
    "v_mov_b32 v74, 0\n"
    "v_mov_b32 v75, 0\n"
    "v_mov_b32 v76, 0\n"
    "v_mov_b32 v77, 0\n"
    "v_mov_b32 v78, 0\n"
    "v_mov_b32 v79, 0\n"
    "ds_read_b128 v[80:83], v2 offset:0\n"
    "ds_read_b128 v[84:87], v2 offset:1024\n"
    "ds_read_b128 v[88:91], v2 offset:2048\n"
    "ds_read_b128 v[92:95], v2 offset:3072\n"
    "ds_read_b128 v[96:99], v2 offset:4096\n"
    "ds_read_b128 v[100:103], v2 offset:5120\n"
    "v_sub_u32 v12, v158, v0\n"
    "v_lshl_add_u32 v12, v15, 7, v12\n"
    "v_and_b32 v18, 15, v15\n"
    "v_sub_u32 v18, v18, v15\n"
    "v_lshl_add_u32 v18, v18, 7, v12\n"
    "s_movk_i32 s55, 0x4000\n"
    "s_waitcnt vmcnt(0) lgkmcnt(0)\n"
    "s_add_u32 s60, s60, 1\n"
    "s_cmp_gt_u32 s60, 65536\n"
    "s_cbranch_scc1 hw_abort_%=\n"
    "s_and_b32 s59, s60, 63\n"
    "s_cbranch_scc1 hw_againb_%=\n"
    "v_mov_b32 v13, 1024\n"
    "buffer_load_dword v13, v13, s[40:43], s58 offen sc1\n"
    "s_waitcnt vmcnt(0)\n"
    "v_readfirstlane_b32 s59, v13\n"
    "s_nop 0\n"
    "s_cmp_lg_u32 s59, 0\n"
    "s_nop 2\n"
    "s_cbranch_scc1 hw_end_%=\n"
    "s_branch hw_againb_%=\n"
    "hw_againb_%=:\n"
    "buffer_load_dword v16, v12, s[40:43], 0 offen sc1\n"
    "buffer_load_dword v17, v18, s[40:43], s55 offen sc1\n"
    "s_waitcnt vmcnt(0)\n"
    "v_max_u32 v16, v16, v17\n"
    "v_cmp_eq_u32 vcc, -1, v16\n"
    "s_nop 4\n"
    "s_cbranch_vccz hw_retryb_%=\n"
    "s_add_u32 s60, s60, 1\n"
    "s_cmp_gt_u32 s60, 65536\n"
    "s_cbranch_scc1 hw_abort_%=\n"
    "s_and_b32 s59, s60, 63\n"
    "s_cbranch_scc1 hw_againb_%=\n"
    "v_mov_b32 v13, 1024\n"
    "buffer_load_dword v13, v13, s[40:43], s58 offen sc1\n"
    "s_waitcnt vmcnt(0)\n"
    "v_readfirstlane_b32 s59, v13\n"
    "s_nop 0\n"
    "s_cmp_lg_u32 s59, 0\n"
    "s_nop 2\n"
    "s_cbranch_scc1 hw_end_%=\n"
    "s_branch hw_againb_%=\n"
    "s_branch hw_end_%=\n"
    "hw_abort_%=:\n"
    "v_mov_b32 v13, 1\n"
    "v_mov_b32 v14, 1024\n"
    "buffer_store_dword v13, v14, s[40:43], s58 offen sc1\n"
    "hw_end_%=:\n"
    "s_endpgm\n"
    :: CHAIN_INPUTS : CHAIN_CLOBBERS);
    __builtin_unreachable();
}
DEV void role_col(unsigned lo, unsigned hi, int id, int w, int aux, const Tune& tn, const void* wa, const void* wb, int bid) {
    asm volatile(
    "s_mov_b32 s40, %0\n"
    "s_mov_b32 s41, %1\n"
    "s_mov_b32 s42, %2\n"
    "s_mov_b32 s43, %3\n"
    "s_mov_b32 s44, %4\n"
    "s_mov_b32 s45, %5\n"
    "s_mov_b32 s46, %6\n"
    "s_mov_b32 s66, %7\n"
    "s_mov_b32 s67, %8\n"
    "s_mov_b32 s68, %9\n"
    "s_mov_b32 s69, %10\n"
    "s_mov_b32 s70, %11\n"
    "s_mov_b32 s71, %12\n"
    "s_mov_b32 s72, %13\n"
    "s_mov_b32 s73, %14\n"
    "s_mov_b32 s76, %15\n"
    "s_mov_b32 s77, %16\n"
    "s_mov_b32 s78, 7864320\n"
    "s_mov_b32 s79, 0x20000\n"
    "s_mov_b32 s80, %17\n"
    "s_mov_b32 s81, %18\n"
    "s_mov_b32 s82, 7864320\n"
    "s_mov_b32 s83, 0x20000\n"
    "s_mov_b32 s84, %19\n"
    "s_mov_b64 exec, -1\n"
    "v_mbcnt_lo_u32_b32 v15, -1, 0\n"
    "v_mbcnt_hi_u32_b32 v15, -1, v15\n"
    "v_lshlrev_b32 v0, 4, v15\n"
    "s_mov_b32 s58, 2097152\n"
    "s_mov_b32 s92, 0\n"
    "s_lshr_b32 s85, s84, 2\n"
    "s_and_b32 s86, s84, 3\n"
    "v_add_u32 v26, 0, v15\n"
    "v_mul_u32_u24 v27, 2731, v26\n"
    "v_lshrrev_b32 v27, 16, v27\n"
    "v_mul_u32_u24 v16, 11904, v27\n"
    "v_lshl_add_u32 v16, v26, 4, v16\n"
    "v_add_u32 v26, v26, v27\n"
    "s_mul_i32 s48, s45, 3200\n"
    "v_lshl_add_u32 v19, v26, 4, s48\n"
    "v_add_u32 v26, 64, v15\n"
    "v_mul_u32_u24 v27, 2731, v26\n"
    "v_lshrrev_b32 v27, 16, v27\n"
    "v_mul_u32_u24 v17, 11904, v27\n"
    "v_lshl_add_u32 v17, v26, 4, v17\n"
    "v_add_u32 v26, v26, v27\n"
    "s_mul_i32 s48, s45, 3200\n"
    "v_lshl_add_u32 v20, v26, 4, s48\n"
    "v_add_u32 v26, 128, v15\n"
    "v_mul_u32_u24 v27, 2731, v26\n"
    "v_lshrrev_b32 v27, 16, v27\n"
    "v_mul_u32_u24 v18, 11904, v27\n"
    "v_lshl_add_u32 v18, v26, 4, v18\n"
    "v_add_u32 v26, v26, v27\n"
    "s_mul_i32 s48, s45, 3200\n"
    "v_lshl_add_u32 v21, v26, 4, s48\n"
    "s_mul_i32 s48, s86, 160\n"
    "s_lshl3_add_u32 s48, s45, s48\n"
    "s_mul_i32 s48, s48, 12288\n"
    "s_mul_i32 s49, s85, 384\n"
    "s_add_u32 s48, s48, s49\n"
    "buffer_load_dwordx4 v[32:35], v16, s[80:83], s48 offen\n"
    "buffer_load_dwordx4 v[36:39], v17, s[80:83], s48 offen\n"
    "buffer_load_dwordx4 v[40:43], v18, s[80:83], s48 offen\n"
    "s_add_u32 s48, s48, 393216\n"
    "buffer_load_dwordx4 v[44:47], v16, s[80:83], s48 offen\n"
    "buffer_load_dwordx4 v[48:51], v17, s[80:83], s48 offen\n"
    "buffer_load_dwordx4 v[52:55], v18, s[80:83], s48 offen\n"
    "s_add_u32 s48, s48, 393216\n"
    "buffer_load_dwordx4 v[56:59], v16, s[80:83], s48 offen\n"
    "buffer_load_dwordx4 v[60:63], v17, s[80:83], s48 offen\n"
    "buffer_load_dwordx4 v[64:67], v18, s[80:83], s48 offen\n"
    "s_add_u32 s48, s48, 393216\n"
    "buffer_load_dwordx4 v[68:71], v16, s[80:83], s48 offen\n"
    "buffer_load_dwordx4 v[72:75], v17, s[80:83], s48 offen\n"
    "buffer_load_dwordx4 v[76:79], v18, s[80:83], s48 offen\n"
    "s_add_u32 s48, s48, 393216\n"
    "buffer_load_dwordx4 v[80:83], v16, s[80:83], s48 offen\n"
    "buffer_load_dwordx4 v[84:87], v17, s[80:83], s48 offen\n"
    "buffer_load_dwordx4 v[88:91], v18, s[80:83], s48 offen\n"
    "s_add_u32 s48, s48, 393216\n"
    "v_lshrrev_b32 v28, 2, v15\n"
    "v_mul_u32_u24 v28, 1280, v28\n"
    "v_and_b32 v29, 3, v15\n"
    "v_lshl_add_u32 v28, v29, 3, v28\n"
    "s_mul_i32 s48, s45, 320\n"
    "v_add_u32 v28, s48, v28\n"
    "v_add_u32 v29, 40960, v28\n"
    "v_add_u32 v30, 81920, v28\n"
    "s_mul_i32 s51, s45, 10240\n"
    "s_mul_i32 s50, s85, 245760\n"
    "s_add_u32 s50, s50, s51\n"
    "s_add_u32 m0, s51, 0\n"
    "s_add_u32 s49, s50, 0\n"
    "buffer_load_dwordx4 v0, s[76:79], s49 offen offset:0 lds\n"
    "buffer_load_dwordx4 v0, s[76:79], s49 offen offset:1024 lds\n"
    "buffer_load_dwordx4 v0, s[76:79], s49 offen offset:2048 lds\n"
    "buffer_load_dwordx4 v0, s[76:79], s49 offen offset:3072 lds\n"
    "s_add_u32 m0, s51, 4096\n"
    "s_add_u32 s49, s50, 4096\n"
    "buffer_load_dwordx4 v0, s[76:79], s49 offen offset:0 lds\n"
    "buffer_load_dwordx4 v0, s[76:79], s49 offen offset:1024 lds\n"
    "buffer_load_dwordx4 v0, s[76:79], s49 offen offset:2048 lds\n"
    "buffer_load_dwordx4 v0, s[76:79], s49 offen offset:3072 lds\n"
    "s_add_u32 m0, s51, 8192\n"
    "s_add_u32 s49, s50, 8192\n"
    "buffer_load_dwordx4 v0, s[76:79], s49 offen offset:0 lds\n"
    "buffer_load_dwordx4 v0, s[76:79], s49 offen offset:1024 lds\n"
    "s_add_u32 m0, s51, 40960\n"
    "s_add_u32 s49, s50, 40960\n"
    "buffer_load_dwordx4 v0, s[76:79], s49 offen offset:0 lds\n"
    "buffer_load_dwordx4 v0, s[76:79], s49 offen offset:1024 lds\n"
    "buffer_load_dwordx4 v0, s[76:79], s49 offen offset:2048 lds\n"
    "buffer_load_dwordx4 v0, s[76:79], s49 offen offset:3072 lds\n"
    "s_add_u32 m0, s51, 45056\n"
    "s_add_u32 s49, s50, 45056\n"
    "buffer_load_dwordx4 v0, s[76:79], s49 offen offset:0 lds\n"
    "buffer_load_dwordx4 v0, s[76:79], s49 offen offset:1024 lds\n"
    "buffer_load_dwordx4 v0, s[76:79], s49 offen offset:2048 lds\n"
    "buffer_load_dwordx4 v0, s[76:79], s49 offen offset:3072 lds\n"
    "s_add_u32 m0, s51, 49152\n"
    "s_add_u32 s49, s50, 49152\n"
    "buffer_load_dwordx4 v0, s[76:79], s49 offen offset:0 lds\n"
    "buffer_load_dwordx4 v0, s[76:79], s49 offen offset:1024 lds\n"
    "s_waitcnt vmcnt(10) lgkmcnt(0)\n"
    "s_barrier\n"
    "s_add_u32 m0, s51, 81920\n"
    "s_add_u32 s49, s50, 81920\n"
    "buffer_load_dwordx4 v0, s[76:79], s49 offen offset:0 lds\n"
    "buffer_load_dwordx4 v0, s[76:79], s49 offen offset:1024 lds\n"
    "buffer_load_dwordx4 v0, s[76:79], s49 offen offset:2048 lds\n"
    "buffer_load_dwordx4 v0, s[76:79], s49 offen offset:3072 lds\n"
    "s_add_u32 m0, s51, 86016\n"
    "s_add_u32 s49, s50, 86016\n"
    "buffer_load_dwordx4 v0, s[76:79], s49 offen offset:0 lds\n"
    "buffer_load_dwordx4 v0, s[76:79], s49 offen offset:1024 lds\n"
    "buffer_load_dwordx4 v0, s[76:79], s49 offen offset:2048 lds\n"
    "buffer_load_dwordx4 v0, s[76:79], s49 offen offset:3072 lds\n"
    "s_add_u32 m0, s51, 90112\n"
    "s_add_u32 s49, s50, 90112\n"
    "buffer_load_dwordx4 v0, s[76:79], s49 offen offset:0 lds\n"
    "buffer_load_dwordx4 v0, s[76:79], s49 offen offset:1024 lds\n"
    "ds_read_b64_tr_b16 a[0:1], v28 offset:0\n"
    "ds_read_b64_tr_b16 a[2:3], v28 offset:20480\n"
    "ds_read_b64_tr_b16 a[24:25], v28 offset:32\n"
    "ds_read_b64_tr_b16 a[26:27], v28 offset:20512\n"
    "ds_read_b64_tr_b16 a[48:49], v28 offset:64\n"
    "ds_read_b64_tr_b16 a[50:51], v28 offset:20544\n"
    "ds_read_b64_tr_b16 a[72:73], v28 offset:96\n"
    "ds_read_b64_tr_b16 a[74:75], v28 offset:20576\n"
    "ds_read_b64_tr_b16 a[96:97], v28 offset:128\n"
    "ds_read_b64_tr_b16 a[98:99], v28 offset:20608\n"
    "ds_read_b64_tr_b16 a[120:121], v28 offset:160\n"
    "ds_read_b64_tr_b16 a[122:123], v28 offset:20640\n"
    "ds_read_b64_tr_b16 a[144:145], v28 offset:192\n"
    "ds_read_b64_tr_b16 a[146:147], v28 offset:20672\n"
    "ds_read_b64_tr_b16 a[168:169], v28 offset:224\n"
    "ds_read_b64_tr_b16 a[170:171], v28 offset:20704\n"
    "s_waitcnt lgkmcnt(14)\n"
    "ds_read_b64_tr_b16 a[192:193], v28 offset:256\n"
    "ds_read_b64_tr_b16 a[194:195], v28 offset:20736\n"
    "s_waitcnt lgkmcnt(14)\n"
    "ds_read_b64_tr_b16 a[216:217], v28 offset:288\n"
    "ds_read_b64_tr_b16 a[218:219], v28 offset:20768\n"
    "s_waitcnt lgkmcnt(14)\n"
    "s_waitcnt vmcnt(10) lgkmcnt(0)\n"
    "s_barrier\n"
    "s_add_u32 m0, s51, 0\n"
    "s_add_u32 s49, s50, 122880\n"
    "buffer_load_dwordx4 v0, s[76:79], s49 offen offset:0 lds\n"
    "buffer_load_dwordx4 v0, s[76:79], s49 offen offset:1024 lds\n"
    "buffer_load_dwordx4 v0, s[76:79], s49 offen offset:2048 lds\n"
    "buffer_load_dwordx4 v0, s[76:79], s49 offen offset:3072 lds\n"
    "s_add_u32 m0, s51, 4096\n"
    "s_add_u32 s49, s50, 126976\n"
    "buffer_load_dwordx4 v0, s[76:79], s49 offen offset:0 lds\n"
    "buffer_load_dwordx4 v0, s[76:79], s49 offen offset:1024 lds\n"
    "buffer_load_dwordx4 v0, s[76:79], s49 offen offset:2048 lds\n"
    "buffer_load_dwordx4 v0, s[76:79], s49 offen offset:3072 lds\n"
    "s_add_u32 m0, s51, 8192\n"
    "s_add_u32 s49, s50, 131072\n"
    "buffer_load_dwordx4 v0, s[76:79], s49 offen offset:0 lds\n"
    "buffer_load_dwordx4 v0, s[76:79], s49 offen offset:1024 lds\n"
    "ds_read_b64_tr_b16 a[4:5], v29 offset:0\n"
    "ds_read_b64_tr_b16 a[6:7], v29 offset:20480\n"
    "ds_read_b64_tr_b16 a[28:29], v29 offset:32\n"
    "ds_read_b64_tr_b16 a[30:31], v29 offset:20512\n"
    "ds_read_b64_tr_b16 a[52:53], v29 offset:64\n"
    "ds_read_b64_tr_b16 a[54:55], v29 offset:20544\n"
    "ds_read_b64_tr_b16 a[76:77], v29 offset:96\n"
    "ds_read_b64_tr_b16 a[78:79], v29 offset:20576\n"
    "ds_read_b64_tr_b16 a[100:101], v29 offset:128\n"
    "ds_read_b64_tr_b16 a[102:103], v29 offset:20608\n"
    "ds_read_b64_tr_b16 a[124:125], v29 offset:160\n"
    "ds_read_b64_tr_b16 a[126:127], v29 offset:20640\n"
    "ds_read_b64_tr_b16 a[148:149], v29 offset:192\n"
    "ds_read_b64_tr_b16 a[150:151], v29 offset:20672\n"
    "ds_read_b64_tr_b16 a[172:173], v29 offset:224\n"
    "ds_read_b64_tr_b16 a[174:175], v29 offset:20704\n"
    "s_waitcnt lgkmcnt(14)\n"
    "ds_read_b64_tr_b16 a[196:197], v29 offset:256\n"
    "ds_read_b64_tr_b16 a[198:199], v29 offset:20736\n"
    "s_waitcnt lgkmcnt(14)\n"
    "ds_read_b64_tr_b16 a[220:221], v29 offset:288\n"
    "ds_read_b64_tr_b16 a[222:223], v29 offset:20768\n"
    "s_waitcnt lgkmcnt(14)\n"
    "s_waitcnt vmcnt(10) lgkmcnt(0)\n"
    "s_barrier\n"
    "s_add_u32 m0, s51, 40960\n"
    "s_add_u32 s49, s50, 163840\n"
    "buffer_load_dwordx4 v0, s[76:79], s49 offen offset:0 lds\n"
    "buffer_load_dwordx4 v0, s[76:79], s49 offen offset:1024 lds\n"
    "buffer_load_dwordx4 v0, s[76:79], s49 offen offset:2048 lds\n"
    "buffer_load_dwordx4 v0, s[76:79], s49 offen offset:3072 lds\n"
    "s_add_u32 m0, s51, 45056\n"
    "s_add_u32 s49, s50, 167936\n"
    "buffer_load_dwordx4 v0, s[76:79], s49 offen offset:0 lds\n"
    "buffer_load_dwordx4 v0, s[76:79], s49 offen offset:1024 lds\n"
    "buffer_load_dwordx4 v0, s[76:79], s49 offen offset:2048 lds\n"
    "buffer_load_dwordx4 v0, s[76:79], s49 offen offset:3072 lds\n"
    "s_add_u32 m0, s51, 49152\n"
    "s_add_u32 s49, s50, 172032\n"
    "buffer_load_dwordx4 v0, s[76:79], s49 offen offset:0 lds\n"
    "buffer_load_dwordx4 v0, s[76:79], s49 offen offset:1024 lds\n"
    "ds_read_b64_tr_b16 a[8:9], v30 offset:0\n"
    "ds_read_b64_tr_b16 a[10:11], v30 offset:20480\n"
    "ds_read_b64_tr_b16 a[32:33], v30 offset:32\n"
    "ds_read_b64_tr_b16 a[34:35], v30 offset:20512\n"
    "ds_read_b64_tr_b16 a[56:57], v30 offset:64\n"
    "ds_read_b64_tr_b16 a[58:59], v30 offset:20544\n"
    "ds_read_b64_tr_b16 a[80:81], v30 offset:96\n"
    "ds_read_b64_tr_b16 a[82:83], v30 offset:20576\n"
    "ds_read_b64_tr_b16 a[104:105], v30 offset:128\n"
    "ds_read_b64_tr_b16 a[106:107], v30 offset:20608\n"
    "ds_read_b64_tr_b16 a[128:129], v30 offset:160\n"
    "ds_read_b64_tr_b16 a[130:131], v30 offset:20640\n"
    "ds_read_b64_tr_b16 a[152:153], v30 offset:192\n"
    "ds_read_b64_tr_b16 a[154:155], v30 offset:20672\n"
    "ds_read_b64_tr_b16 a[176:177], v30 offset:224\n"
    "ds_read_b64_tr_b16 a[178:179], v30 offset:20704\n"
    "s_waitcnt lgkmcnt(14)\n"
    "ds_read_b64_tr_b16 a[200:201], v30 offset:256\n"
    "ds_read_b64_tr_b16 a[202:203], v30 offset:20736\n"
    "s_waitcnt lgkmcnt(14)\n"
    "ds_read_b64_tr_b16 a[224:225], v30 offset:288\n"
    "ds_read_b64_tr_b16 a[226:227], v30 offset:20768\n"
    "s_waitcnt lgkmcnt(14)\n"
    "s_waitcnt vmcnt(10) lgkmcnt(0)\n"
    "s_barrier\n"
    "s_add_u32 m0, s51, 81920\n"
    "s_add_u32 s49, s50, 204800\n"
    "buffer_load_dwordx4 v0, s[76:79], s49 offen offset:0 lds\n"
    "buffer_load_dwordx4 v0, s[76:79], s49 offen offset:1024 lds\n"
    "buffer_load_dwordx4 v0, s[76:79], s49 offen offset:2048 lds\n"
    "buffer_load_dwordx4 v0, s[76:79], s49 offen offset:3072 lds\n"
    "s_add_u32 m0, s51, 86016\n"
    "s_add_u32 s49, s50, 208896\n"
    "buffer_load_dwordx4 v0, s[76:79], s49 offen offset:0 lds\n"
    "buffer_load_dwordx4 v0, s[76:79], s49 offen offset:1024 lds\n"
    "buffer_load_dwordx4 v0, s[76:79], s49 offen offset:2048 lds\n"
    "buffer_load_dwordx4 v0, s[76:79], s49 offen offset:3072 lds\n"
    "s_add_u32 m0, s51, 90112\n"
    "s_add_u32 s49, s50, 212992\n"
    "buffer_load_dwordx4 v0, s[76:79], s49 offen offset:0 lds\n"
    "buffer_load_dwordx4 v0, s[76:79], s49 offen offset:1024 lds\n"
    "ds_read_b64_tr_b16 a[12:13], v28 offset:0\n"
    "ds_read_b64_tr_b16 a[14:15], v28 offset:20480\n"
    "ds_read_b64_tr_b16 a[36:37], v28 offset:32\n"
    "ds_read_b64_tr_b16 a[38:39], v28 offset:20512\n"
    "ds_read_b64_tr_b16 a[60:61], v28 offset:64\n"
    "ds_read_b64_tr_b16 a[62:63], v28 offset:20544\n"
    "ds_read_b64_tr_b16 a[84:85], v28 offset:96\n"
    "ds_read_b64_tr_b16 a[86:87], v28 offset:20576\n"
    "ds_read_b64_tr_b16 a[108:109], v28 offset:128\n"
    "ds_read_b64_tr_b16 a[110:111], v28 offset:20608\n"
    "ds_read_b64_tr_b16 a[132:133], v28 offset:160\n"
    "ds_read_b64_tr_b16 a[134:135], v28 offset:20640\n"
    "ds_read_b64_tr_b16 a[156:157], v28 offset:192\n"
    "ds_read_b64_tr_b16 a[158:159], v28 offset:20672\n"
    "ds_read_b64_tr_b16 a[180:181], v28 offset:224\n"
    "ds_read_b64_tr_b16 a[182:183], v28 offset:20704\n"
    "s_waitcnt lgkmcnt(14)\n"
    "ds_read_b64_tr_b16 a[204:205], v28 offset:256\n"
    "ds_read_b64_tr_b16 a[206:207], v28 offset:20736\n"
    "s_waitcnt lgkmcnt(14)\n"
    "ds_read_b64_tr_b16 a[228:229], v28 offset:288\n"
    "ds_read_b64_tr_b16 a[230:231], v28 offset:20768\n"
    "s_waitcnt lgkmcnt(14)\n"
    "s_waitcnt vmcnt(10) lgkmcnt(0)\n"
    "s_barrier\n"
    "ds_read_b64_tr_b16 a[16:17], v29 offset:0\n"
    "ds_read_b64_tr_b16 a[18:19], v29 offset:20480\n"
    "ds_read_b64_tr_b16 a[40:41], v29 offset:32\n"
    "ds_read_b64_tr_b16 a[42:43], v29 offset:20512\n"
    "ds_read_b64_tr_b16 a[64:65], v29 offset:64\n"
    "ds_read_b64_tr_b16 a[66:67], v29 offset:20544\n"
    "ds_read_b64_tr_b16 a[88:89], v29 offset:96\n"
    "ds_read_b64_tr_b16 a[90:91], v29 offset:20576\n"
    "ds_read_b64_tr_b16 a[112:113], v29 offset:128\n"
    "ds_read_b64_tr_b16 a[114:115], v29 offset:20608\n"
    "ds_read_b64_tr_b16 a[136:137], v29 offset:160\n"
    "ds_read_b64_tr_b16 a[138:139], v29 offset:20640\n"
    "ds_read_b64_tr_b16 a[160:161], v29 offset:192\n"
    "ds_read_b64_tr_b16 a[162:163], v29 offset:20672\n"
    "ds_read_b64_tr_b16 a[184:185], v29 offset:224\n"
    "ds_read_b64_tr_b16 a[186:187], v29 offset:20704\n"
    "s_waitcnt lgkmcnt(14)\n"
    "ds_read_b64_tr_b16 a[208:209], v29 offset:256\n"
    "ds_read_b64_tr_b16 a[210:211], v29 offset:20736\n"
    "s_waitcnt lgkmcnt(14)\n"
    "ds_read_b64_tr_b16 a[232:233], v29 offset:288\n"
    "ds_read_b64_tr_b16 a[234:235], v29 offset:20768\n"
    "s_waitcnt lgkmcnt(14)\n"
    "s_waitcnt vmcnt(0) lgkmcnt(0)\n"
    "s_barrier\n"
    "ds_read_b64_tr_b16 a[20:21], v30 offset:0\n"
    "ds_read_b64_tr_b16 a[22:23], v30 offset:20480\n"
    "ds_read_b64_tr_b16 a[44:45], v30 offset:32\n"
    "ds_read_b64_tr_b16 a[46:47], v30 offset:20512\n"
    "ds_read_b64_tr_b16 a[68:69], v30 offset:64\n"
    "ds_read_b64_tr_b16 a[70:71], v30 offset:20544\n"
    "ds_read_b64_tr_b16 a[92:93], v30 offset:96\n"
    "ds_read_b64_tr_b16 a[94:95], v30 offset:20576\n"
    "ds_read_b64_tr_b16 a[116:117], v30 offset:128\n"
    "ds_read_b64_tr_b16 a[118:119], v30 offset:20608\n"
    "ds_read_b64_tr_b16 a[140:141], v30 offset:160\n"
    "ds_read_b64_tr_b16 a[142:143], v30 offset:20640\n"
    "ds_read_b64_tr_b16 a[164:165], v30 offset:192\n"
    "ds_read_b64_tr_b16 a[166:167], v30 offset:20672\n"
    "ds_read_b64_tr_b16 a[188:189], v30 offset:224\n"
    "ds_read_b64_tr_b16 a[190:191], v30 offset:20704\n"
    "s_waitcnt lgkmcnt(14)\n"
    "ds_read_b64_tr_b16 a[212:213], v30 offset:256\n"
    "ds_read_b64_tr_b16 a[214:215], v30 offset:20736\n"
    "s_waitcnt lgkmcnt(14)\n"
    "ds_read_b64_tr_b16 a[236:237], v30 offset:288\n"
    "ds_read_b64_tr_b16 a[238:239], v30 offset:20768\n"
    "s_waitcnt lgkmcnt(14)\n"
    "s_waitcnt lgkmcnt(0)\n"
    "s_barrier\n"
    "ds_write_b128 v19, v[32:35] offset:0\n"
    "ds_write_b128 v20, v[36:39] offset:0\n"
    "ds_write_b128 v21, v[40:43] offset:0\n"
    "ds_write_b128 v19, v[44:47] offset:12800\n"
    "ds_write_b128 v20, v[48:51] offset:12800\n"
    "ds_write_b128 v21, v[52:55] offset:12800\n"
    "ds_write_b128 v19, v[56:59] offset:25600\n"
    "ds_write_b128 v20, v[60:63] offset:25600\n"
    "ds_write_b128 v21, v[64:67] offset:25600\n"
    "ds_write_b128 v19, v[68:71] offset:38400\n"
    "ds_write_b128 v20, v[72:75] offset:38400\n"
    "ds_write_b128 v21, v[76:79] offset:38400\n"
    "ds_write_b128 v19, v[80:83] offset:51200\n"
    "ds_write_b128 v20, v[84:87] offset:51200\n"
    "ds_write_b128 v21, v[88:91] offset:51200\n"
    "v_and_b32 v22, 1, v15\n"
    "v_and_b32 v26, 2, v15\n"
    "v_lshl_add_u32 v22, v26, 2, v22\n"
    "v_and_b32 v26, 4, v15\n"
    "v_add_u32 v22, v22, v26\n"
    "v_and_b32 v26, 8, v15\n"
    "v_lshl_add_u32 v22, v26, 1, v22\n"
    "v_mul_u32_u24 v22, 400, v22\n"
    "v_lshrrev_b32 v26, 4, v15\n"
    "v_lshl_add_u32 v22, v26, 3, v22\n"
    "v_bfe_u32 v26, v15, 3, 1\n"
    "v_mul_u32_u24 v26, 655360, v26\n"
    "v_and_b32 v24, 0x370, v0\n"
    "v_add_u32 v24, v24, v26\n"
    "s_mul_i32 s48, s45, 13107200\n"
    "s_add_u32 s48, s48, 64421888\n"
    "v_add_u32 v24, s48, v24\n"
    "s_lshr_b32 s50, s85, 1\n"
    "s_mul_i32 s50, s50, 20480\n"
    "s_and_b32 s48, s85, 1\n"
    "s_lshl_b32 s48, s48, 7\n"
    "s_add_u32 s50, s50, s48\n"
    "s_mul_i32 s48, s86, 5120\n"
    "s_add_u32 s50, s50, s48\n"
    "s_movk_i32 s52, 0x2000\n"
    "s_mov_b32 s51, 5\n"
    "s_waitcnt lgkmcnt(0)\n"
    "s_barrier\n"
    "cw_bg_%=:\n"
    "ds_read2_b64 v[96:99], v22 offset0:0 offset1:4\n"
    "ds_read2_b64 v[100:103], v22 offset0:100 offset1:104\n"
    "ds_read2_b64 v[104:107], v22 offset0:8 offset1:12\n"
    "ds_read2_b64 v[108:111], v22 offset0:108 offset1:112\n"
    "ds_read2_b64 v[112:115], v22 offset0:16 offset1:20\n"
    "ds_read2_b64 v[116:119], v22 offset0:116 offset1:120\n"
    "ds_read2_b64 v[120:123], v22 offset0:24 offset1:28\n"
    "ds_read2_b64 v[124:127], v22 offset0:124 offset1:128\n"
    "ds_read2_b64 v[128:131], v22 offset0:32 offset1:36\n"
    "ds_read2_b64 v[132:135], v22 offset0:132 offset1:136\n"
    "ds_read2_b64 v[136:139], v22 offset0:40 offset1:44\n"
    "ds_read2_b64 v[140:143], v22 offset0:140 offset1:144\n"
    "v_add_u32 v22, 12800, v22\n"
    "s_and_b32 s53, s50, 0xffffe000\n"
    "s_add_u32 s53, s53, s50\n"
    "s_waitcnt lgkmcnt(11)\n"
    "v_mfma_f32_16x16x32_bf16 v[144:147], v[96:99], a[0:3], 0\n"
    "s_waitcnt lgkmcnt(10)\n"
    "v_mfma_f32_16x16x32_bf16 v[148:151], v[100:103], a[0:3], 0\n"
    "s_waitcnt lgkmcnt(9)\n"
    "v_mfma_f32_16x16x32_bf16 v[144:147], v[104:107], a[4:7], v[144:147]\n"
    "s_waitcnt lgkmcnt(8)\n"
    "v_mfma_f32_16x16x32_bf16 v[148:151], v[108:111], a[4:7], v[148:151]\n"
    "s_waitcnt lgkmcnt(7)\n"
    "v_mfma_f32_16x16x32_bf16 v[144:147], v[112:115], a[8:11], v[144:147]\n"
    "s_waitcnt lgkmcnt(6)\n"
    "v_mfma_f32_16x16x32_bf16 v[148:151], v[116:119], a[8:11], v[148:151]\n"
    "s_waitcnt lgkmcnt(5)\n"
    "v_mfma_f32_16x16x32_bf16 v[144:147], v[120:123], a[12:15], v[144:147]\n"
    "s_waitcnt lgkmcnt(4)\n"
    "v_mfma_f32_16x16x32_bf16 v[148:151], v[124:127], a[12:15], v[148:151]\n"
    "s_waitcnt lgkmcnt(3)\n"
    "v_mfma_f32_16x16x32_bf16 v[144:147], v[128:131], a[16:19], v[144:147]\n"
    "s_waitcnt lgkmcnt(2)\n"
    "v_mfma_f32_16x16x32_bf16 v[148:151], v[132:135], a[16:19], v[148:151]\n"
    "s_waitcnt lgkmcnt(1)\n"
    "v_mfma_f32_16x16x32_bf16 v[144:147], v[136:139], a[20:23], v[144:147]\n"
    "s_waitcnt lgkmcnt(0)\n"
    "v_mfma_f32_16x16x32_bf16 v[148:151], v[140:143], a[20:23], v[148:151]\n"
    "v_mfma_f32_16x16x32_bf16 v[152:155], v[96:99], a[24:27], 0\n"
    "v_add_u32 v25, s53, v24\n"
    "v_mfma_f32_16x16x32_bf16 v[156:159], v[100:103], a[24:27], 0\n"
    "v_lshrrev_b32 v26, 8, v25\n"
    "v_mfma_f32_16x16x32_bf16 v[152:155], v[104:107], a[28:31], v[152:155]\n"
    "v_and_or_b32 v25, v26, s52, v25\n"
    "v_mfma_f32_16x16x32_bf16 v[156:159], v[108:111], a[28:31], v[156:159]\n"
    "s_add_u32 s53, s53, 1310720\n"
    "v_mfma_f32_16x16x32_bf16 v[152:155], v[112:115], a[32:35], v[152:155]\n"
    "v_mfma_f32_16x16x32_bf16 v[156:159], v[116:119], a[32:35], v[156:159]\n"
    "v_mfma_f32_16x16x32_bf16 v[152:155], v[120:123], a[36:39], v[152:155]\n"
    "v_cvt_pk_bf16_f32 v160, v144, v145\n"
    "v_mfma_f32_16x16x32_bf16 v[156:159], v[124:127], a[36:39], v[156:159]\n"
    "v_cvt_pk_bf16_f32 v161, v146, v147\n"
    "v_mfma_f32_16x16x32_bf16 v[152:155], v[128:131], a[40:43], v[152:155]\n"
    "v_cvt_pk_bf16_f32 v162, v148, v149\n"
    "v_mfma_f32_16x16x32_bf16 v[156:159], v[132:135], a[40:43], v[156:159]\n"
    "v_cvt_pk_bf16_f32 v163, v150, v151\n"
    "v_mfma_f32_16x16x32_bf16 v[152:155], v[136:139], a[44:47], v[152:155]\n"
    "buffer_store_dwordx4 v[160:163], v25, s[40:43], 0 offen sc1\n"
    "v_mfma_f32_16x16x32_bf16 v[156:159], v[140:143], a[44:47], v[156:159]\n"
    "v_mfma_f32_16x16x32_bf16 v[144:147], v[96:99], a[48:51], 0\n"
    "v_add_u32 v25, s53, v24\n"
    "v_mfma_f32_16x16x32_bf16 v[148:151], v[100:103], a[48:51], 0\n"
    "v_lshrrev_b32 v26, 8, v25\n"
    "v_mfma_f32_16x16x32_bf16 v[144:147], v[104:107], a[52:55], v[144:147]\n"
    "v_and_or_b32 v25, v26, s52, v25\n"
    "v_mfma_f32_16x16x32_bf16 v[148:151], v[108:111], a[52:55], v[148:151]\n"
    "s_add_u32 s53, s53, 1310720\n"
    "v_mfma_f32_16x16x32_bf16 v[144:147], v[112:115], a[56:59], v[144:147]\n"
    "v_mfma_f32_16x16x32_bf16 v[148:151], v[116:119], a[56:59], v[148:151]\n"
    "v_mfma_f32_16x16x32_bf16 v[144:147], v[120:123], a[60:63], v[144:147]\n"
    "v_cvt_pk_bf16_f32 v164, v152, v153\n"
    "v_mfma_f32_16x16x32_bf16 v[148:151], v[124:127], a[60:63], v[148:151]\n"
    "v_cvt_pk_bf16_f32 v165, v154, v155\n"
    "v_mfma_f32_16x16x32_bf16 v[144:147], v[128:131], a[64:67], v[144:147]\n"
    "v_cvt_pk_bf16_f32 v166, v156, v157\n"
    "v_mfma_f32_16x16x32_bf16 v[148:151], v[132:135], a[64:67], v[148:151]\n"
    "v_cvt_pk_bf16_f32 v167, v158, v159\n"
    "v_mfma_f32_16x16x32_bf16 v[144:147], v[136:139], a[68:71], v[144:147]\n"
    "buffer_store_dwordx4 v[164:167], v25, s[40:43], 0 offen sc1\n"
    "v_mfma_f32_16x16x32_bf16 v[148:151], v[140:143], a[68:71], v[148:151]\n"
    "v_mfma_f32_16x16x32_bf16 v[152:155], v[96:99], a[72:75], 0\n"
    "v_add_u32 v25, s53, v24\n"
    "v_mfma_f32_16x16x32_bf16 v[156:159], v[100:103], a[72:75], 0\n"
    "v_lshrrev_b32 v26, 8, v25\n"
    "v_mfma_f32_16x16x32_bf16 v[152:155], v[104:107], a[76:79], v[152:155]\n"
    "v_and_or_b32 v25, v26, s52, v25\n"
    "v_mfma_f32_16x16x32_bf16 v[156:159], v[108:111], a[76:79], v[156:159]\n"
    "s_add_u32 s53, s53, 1310720\n"
    "v_mfma_f32_16x16x32_bf16 v[152:155], v[112:115], a[80:83], v[152:155]\n"
    "v_mfma_f32_16x16x32_bf16 v[156:159], v[116:119], a[80:83], v[156:159]\n"
    "v_mfma_f32_16x16x32_bf16 v[152:155], v[120:123], a[84:87], v[152:155]\n"
    "v_cvt_pk_bf16_f32 v160, v144, v145\n"
    "v_mfma_f32_16x16x32_bf16 v[156:159], v[124:127], a[84:87], v[156:159]\n"
    "v_cvt_pk_bf16_f32 v161, v146, v147\n"
    "v_mfma_f32_16x16x32_bf16 v[152:155], v[128:131], a[88:91], v[152:155]\n"
    "v_cvt_pk_bf16_f32 v162, v148, v149\n"
    "v_mfma_f32_16x16x32_bf16 v[156:159], v[132:135], a[88:91], v[156:159]\n"
    "v_cvt_pk_bf16_f32 v163, v150, v151\n"
    "v_mfma_f32_16x16x32_bf16 v[152:155], v[136:139], a[92:95], v[152:155]\n"
    "buffer_store_dwordx4 v[160:163], v25, s[40:43], 0 offen sc1\n"
    "v_mfma_f32_16x16x32_bf16 v[156:159], v[140:143], a[92:95], v[156:159]\n"
    "v_mfma_f32_16x16x32_bf16 v[144:147], v[96:99], a[96:99], 0\n"
    "v_add_u32 v25, s53, v24\n"
    "v_mfma_f32_16x16x32_bf16 v[148:151], v[100:103], a[96:99], 0\n"
    "v_lshrrev_b32 v26, 8, v25\n"
    "v_mfma_f32_16x16x32_bf16 v[144:147], v[104:107], a[100:103], v[144:147]\n"
    "v_and_or_b32 v25, v26, s52, v25\n"
    "v_mfma_f32_16x16x32_bf16 v[148:151], v[108:111], a[100:103], v[148:151]\n"
    "s_add_u32 s53, s53, 1310720\n"
    "v_mfma_f32_16x16x32_bf16 v[144:147], v[112:115], a[104:107], v[144:147]\n"
    "v_mfma_f32_16x16x32_bf16 v[148:151], v[116:119], a[104:107], v[148:151]\n"
    "v_mfma_f32_16x16x32_bf16 v[144:147], v[120:123], a[108:111], v[144:147]\n"
    "v_cvt_pk_bf16_f32 v164, v152, v153\n"
    "v_mfma_f32_16x16x32_bf16 v[148:151], v[124:127], a[108:111], v[148:151]\n"
    "v_cvt_pk_bf16_f32 v165, v154, v155\n"
    "v_mfma_f32_16x16x32_bf16 v[144:147], v[128:131], a[112:115], v[144:147]\n"
    "v_cvt_pk_bf16_f32 v166, v156, v157\n"
    "v_mfma_f32_16x16x32_bf16 v[148:151], v[132:135], a[112:115], v[148:151]\n"
    "v_cvt_pk_bf16_f32 v167, v158, v159\n"
    "v_mfma_f32_16x16x32_bf16 v[144:147], v[136:139], a[116:119], v[144:147]\n"
    "buffer_store_dwordx4 v[164:167], v25, s[40:43], 0 offen sc1\n"
    "v_mfma_f32_16x16x32_bf16 v[148:151], v[140:143], a[116:119], v[148:151]\n"
    "v_mfma_f32_16x16x32_bf16 v[152:155], v[96:99], a[120:123], 0\n"
    "v_add_u32 v25, s53, v24\n"
    "v_mfma_f32_16x16x32_bf16 v[156:159], v[100:103], a[120:123], 0\n"
    "v_lshrrev_b32 v26, 8, v25\n"
    "v_mfma_f32_16x16x32_bf16 v[152:155], v[104:107], a[124:127], v[152:155]\n"
    "v_and_or_b32 v25, v26, s52, v25\n"
    "v_mfma_f32_16x16x32_bf16 v[156:159], v[108:111], a[124:127], v[156:159]\n"
    "s_add_u32 s53, s53, 1310720\n"
    "v_mfma_f32_16x16x32_bf16 v[152:155], v[112:115], a[128:131], v[152:155]\n"
    "v_mfma_f32_16x16x32_bf16 v[156:159], v[116:119], a[128:131], v[156:159]\n"
    "v_mfma_f32_16x16x32_bf16 v[152:155], v[120:123], a[132:135], v[152:155]\n"
    "v_cvt_pk_bf16_f32 v160, v144, v145\n"
    "v_mfma_f32_16x16x32_bf16 v[156:159], v[124:127], a[132:135], v[156:159]\n"
    "v_cvt_pk_bf16_f32 v161, v146, v147\n"
    "v_mfma_f32_16x16x32_bf16 v[152:155], v[128:131], a[136:139], v[152:155]\n"
    "v_cvt_pk_bf16_f32 v162, v148, v149\n"
    "v_mfma_f32_16x16x32_bf16 v[156:159], v[132:135], a[136:139], v[156:159]\n"
    "v_cvt_pk_bf16_f32 v163, v150, v151\n"
    "v_mfma_f32_16x16x32_bf16 v[152:155], v[136:139], a[140:143], v[152:155]\n"
    "buffer_store_dwordx4 v[160:163], v25, s[40:43], 0 offen sc1\n"
    "v_mfma_f32_16x16x32_bf16 v[156:159], v[140:143], a[140:143], v[156:159]\n"
    "v_mfma_f32_16x16x32_bf16 v[144:147], v[96:99], a[144:147], 0\n"
    "v_add_u32 v25, s53, v24\n"
    "v_mfma_f32_16x16x32_bf16 v[148:151], v[100:103], a[144:147], 0\n"
    "v_lshrrev_b32 v26, 8, v25\n"
    "v_mfma_f32_16x16x32_bf16 v[144:147], v[104:107], a[148:151], v[144:147]\n"
    "v_and_or_b32 v25, v26, s52, v25\n"
    "v_mfma_f32_16x16x32_bf16 v[148:151], v[108:111], a[148:151], v[148:151]\n"
    "s_add_u32 s53, s53, 1310720\n"
    "v_mfma_f32_16x16x32_bf16 v[144:147], v[112:115], a[152:155], v[144:147]\n"
    "v_mfma_f32_16x16x32_bf16 v[148:151], v[116:119], a[152:155], v[148:151]\n"
    "v_mfma_f32_16x16x32_bf16 v[144:147], v[120:123], a[156:159], v[144:147]\n"
    "v_cvt_pk_bf16_f32 v164, v152, v153\n"
    "v_mfma_f32_16x16x32_bf16 v[148:151], v[124:127], a[156:159], v[148:151]\n"
    "v_cvt_pk_bf16_f32 v165, v154, v155\n"
    "v_mfma_f32_16x16x32_bf16 v[144:147], v[128:131], a[160:163], v[144:147]\n"
    "v_cvt_pk_bf16_f32 v166, v156, v157\n"
    "v_mfma_f32_16x16x32_bf16 v[148:151], v[132:135], a[160:163], v[148:151]\n"
    "v_cvt_pk_bf16_f32 v167, v158, v159\n"
    "v_mfma_f32_16x16x32_bf16 v[144:147], v[136:139], a[164:167], v[144:147]\n"
    "buffer_store_dwordx4 v[164:167], v25, s[40:43], 0 offen sc1\n"
    "v_mfma_f32_16x16x32_bf16 v[148:151], v[140:143], a[164:167], v[148:151]\n"
    "v_mfma_f32_16x16x32_bf16 v[152:155], v[96:99], a[168:171], 0\n"
    "v_add_u32 v25, s53, v24\n"
    "v_mfma_f32_16x16x32_bf16 v[156:159], v[100:103], a[168:171], 0\n"
    "v_lshrrev_b32 v26, 8, v25\n"
    "v_mfma_f32_16x16x32_bf16 v[152:155], v[104:107], a[172:175], v[152:155]\n"
    "v_and_or_b32 v25, v26, s52, v25\n"
    "v_mfma_f32_16x16x32_bf16 v[156:159], v[108:111], a[172:175], v[156:159]\n"
    "s_add_u32 s53, s53, 1310720\n"
    "v_mfma_f32_16x16x32_bf16 v[152:155], v[112:115], a[176:179], v[152:155]\n"
    "v_mfma_f32_16x16x32_bf16 v[156:159], v[116:119], a[176:179], v[156:159]\n"
    "v_mfma_f32_16x16x32_bf16 v[152:155], v[120:123], a[180:183], v[152:155]\n"
    "v_cvt_pk_bf16_f32 v160, v144, v145\n"
    "v_mfma_f32_16x16x32_bf16 v[156:159], v[124:127], a[180:183], v[156:159]\n"
    "v_cvt_pk_bf16_f32 v161, v146, v147\n"
    "v_mfma_f32_16x16x32_bf16 v[152:155], v[128:131], a[184:187], v[152:155]\n"
    "v_cvt_pk_bf16_f32 v162, v148, v149\n"
    "v_mfma_f32_16x16x32_bf16 v[156:159], v[132:135], a[184:187], v[156:159]\n"
    "v_cvt_pk_bf16_f32 v163, v150, v151\n"
    "v_mfma_f32_16x16x32_bf16 v[152:155], v[136:139], a[188:191], v[152:155]\n"
    "buffer_store_dwordx4 v[160:163], v25, s[40:43], 0 offen sc1\n"
    "v_mfma_f32_16x16x32_bf16 v[156:159], v[140:143], a[188:191], v[156:159]\n"
    "v_mfma_f32_16x16x32_bf16 v[144:147], v[96:99], a[192:195], 0\n"
    "v_add_u32 v25, s53, v24\n"
    "v_mfma_f32_16x16x32_bf16 v[148:151], v[100:103], a[192:195], 0\n"
    "v_lshrrev_b32 v26, 8, v25\n"
    "v_mfma_f32_16x16x32_bf16 v[144:147], v[104:107], a[196:199], v[144:147]\n"
    "v_and_or_b32 v25, v26, s52, v25\n"
    "v_mfma_f32_16x16x32_bf16 v[148:151], v[108:111], a[196:199], v[148:151]\n"
    "s_add_u32 s53, s53, 1310720\n"
    "v_mfma_f32_16x16x32_bf16 v[144:147], v[112:115], a[200:203], v[144:147]\n"
    "v_mfma_f32_16x16x32_bf16 v[148:151], v[116:119], a[200:203], v[148:151]\n"
    "v_mfma_f32_16x16x32_bf16 v[144:147], v[120:123], a[204:207], v[144:147]\n"
    "v_cvt_pk_bf16_f32 v164, v152, v153\n"
    "v_mfma_f32_16x16x32_bf16 v[148:151], v[124:127], a[204:207], v[148:151]\n"
    "v_cvt_pk_bf16_f32 v165, v154, v155\n"
    "v_mfma_f32_16x16x32_bf16 v[144:147], v[128:131], a[208:211], v[144:147]\n"
    "v_cvt_pk_bf16_f32 v166, v156, v157\n"
    "v_mfma_f32_16x16x32_bf16 v[148:151], v[132:135], a[208:211], v[148:151]\n"
    "v_cvt_pk_bf16_f32 v167, v158, v159\n"
    "v_mfma_f32_16x16x32_bf16 v[144:147], v[136:139], a[212:215], v[144:147]\n"
    "buffer_store_dwordx4 v[164:167], v25, s[40:43], 0 offen sc1\n"
    "v_mfma_f32_16x16x32_bf16 v[148:151], v[140:143], a[212:215], v[148:151]\n"
    "v_mfma_f32_16x16x32_bf16 v[152:155], v[96:99], a[216:219], 0\n"
    "v_add_u32 v25, s53, v24\n"
    "v_mfma_f32_16x16x32_bf16 v[156:159], v[100:103], a[216:219], 0\n"
    "v_lshrrev_b32 v26, 8, v25\n"
    "v_mfma_f32_16x16x32_bf16 v[152:155], v[104:107], a[220:223], v[152:155]\n"
    "v_and_or_b32 v25, v26, s52, v25\n"
    "v_mfma_f32_16x16x32_bf16 v[156:159], v[108:111], a[220:223], v[156:159]\n"
    "s_add_u32 s53, s53, 1310720\n"
    "v_mfma_f32_16x16x32_bf16 v[152:155], v[112:115], a[224:227], v[152:155]\n"
    "v_mfma_f32_16x16x32_bf16 v[156:159], v[116:119], a[224:227], v[156:159]\n"
    "v_mfma_f32_16x16x32_bf16 v[152:155], v[120:123], a[228:231], v[152:155]\n"
    "v_cvt_pk_bf16_f32 v160, v144, v145\n"
    "v_mfma_f32_16x16x32_bf16 v[156:159], v[124:127], a[228:231], v[156:159]\n"
    "v_cvt_pk_bf16_f32 v161, v146, v147\n"
    "v_mfma_f32_16x16x32_bf16 v[152:155], v[128:131], a[232:235], v[152:155]\n"
    "v_cvt_pk_bf16_f32 v162, v148, v149\n"
    "v_mfma_f32_16x16x32_bf16 v[156:159], v[132:135], a[232:235], v[156:159]\n"
    "v_cvt_pk_bf16_f32 v163, v150, v151\n"
    "v_mfma_f32_16x16x32_bf16 v[152:155], v[136:139], a[236:239], v[152:155]\n"
    "buffer_store_dwordx4 v[160:163], v25, s[40:43], 0 offen sc1\n"
    "v_mfma_f32_16x16x32_bf16 v[156:159], v[140:143], a[236:239], v[156:159]\n"
    "v_add_u32 v25, s53, v24\n"
    "v_lshrrev_b32 v26, 8, v25\n"
    "v_and_or_b32 v25, v26, s52, v25\n"
    "s_add_u32 s53, s53, 1310720\n"
    "s_nop 1\n"
    "v_cvt_pk_bf16_f32 v164, v152, v153\n"
    "v_cvt_pk_bf16_f32 v165, v154, v155\n"
    "v_cvt_pk_bf16_f32 v166, v156, v157\n"
    "v_cvt_pk_bf16_f32 v167, v158, v159\n"
    "buffer_store_dwordx4 v[164:167], v25, s[40:43], 0 offen sc1\n"
    "s_add_u32 s50, s50, 1024\n"
    "s_sub_u32 s51, s51, 1\n"
    "s_cmp_lg_u32 s51, 0\n"
    "s_cbranch_scc1 cw_bg_%=\n"
    "s_cmp_eq_u32 s45, 0\n"
    "s_cselect_b32 s59, 1, 0\n"
    "v_lshlrev_b32 v26, 7, v15\n"
    "v_cmp_gt_u32 vcc, s59, v15\n"
    "v_mov_b32 v27, 2147483632\n"
    "s_nop 0\n"
    "v_cndmask_b32 v26, v27, v26, vcc\n"
    "v_mov_b32 v27, 1\n"
    "s_and_b32 s54, s84, 7\n"
    "s_lshl_b32 s54, s54, 7\n"
    "s_add_u32 s54, s54, 2364928\n"
    "s_waitcnt vmcnt(0)\n"
    "s_waitcnt lgkmcnt(0)\n"
    "s_barrier\n"
    "buffer_atomic_add v27, v26, s[40:43], s54 offen\n"
    "v_lshrrev_b32 v12, 4, v15\n"
    "v_and_b32 v13, 7, v15\n"
    "v_and_b32 v10, 8, v15\n"
    "v_cmp_eq_u32 vcc, 0, v10\n"
    "v_mov_b32 v10, 0xeeee\n"
    "v_mov_b32 v1, 0x4444\n"
    "v_cndmask_b32 v1, v10, v1, vcc\n"
    "s_lshl_b32 s48, s44, 7\n"
    "v_lshlrev_b32 v3, 4, v13\n"
    "v_add_u32 v3, s48, v3\n"
    "v_and_b32 v10, 0xffffe000, v3\n"
    "v_add_u32 v3, v3, v10\n"
    "v_lshl_add_u32 v3, v12, 15, v3\n"
    "v_mov_b32 v10, 2147483632\n"
    "v_cndmask_b32 v3, v10, v3, vcc\n"
    "s_mov_b32 s47, 0\n"
    "s_cmp_lg_u32 s45, 0\n"
    "s_cbranch_scc1 cw_wx_%=\n"
    "v_mov_b32 v5, 0\n"
    "v_cmp_eq_u32 vcc, 0, v15\n"
    "v_mov_b32 v10, 2147483632\n"
    "s_nop 0\n"
    "v_cndmask_b32 v5, v10, v5, vcc\n"
    "s_mov_b32 s65, 0\n"
    "v_and_b32 v2, 31, v15\n"
    "v_lshlrev_b32 v2, 7, v2\n"
    "v_mov_b32 v7, 0\n"
    "v_mov_b32 v8, 26880\n"
    "s_mov_b32 s55, 32\n"
    "s_mul_i32 s48, s44, 3277\n"
    "s_lshr_b32 s48, s48, 16\n"
    "s_lshl_b32 s48, s48, 7\n"
    "s_add_u32 s48, s48, 512\n"
    "v_add_u32 v4, s48, v5\n"
    "s_mov_b32 s48, 786432\n"
    "buffer_load_dwordx4 v[192:195], v3, s[40:43], s48 offen sc1\n"
    "s_waitcnt vmcnt(0)\n"
    "s_nop 0\n"
    "cw_loopa_%=:\n"
    "s_and_b32 s48, s47, 3\n"
    "s_lshl_b32 s50, s48, 2\n"
    "s_add_u32 s50, s50, s46\n"
    "s_mul_i32 s50, s50, 32768\n"
    "s_add_u32 s50, s50, 786432\n"
    "s_add_u32 s51, s50, 4096\n"
    "s_add_u32 s52, s50, 16384\n"
    "s_add_u32 s64, s65, s70\n"
    "s_and_b32 s61, s47, 31\n"
    "s_mul_i32 s61, s61, 80\n"
    "s_add_u32 s61, s61, s44\n"
    "s_lshl_b32 s61, s61, 15\n"
    "s_add_u32 s61, s61, 32964608\n"
    "s_lshr_b32 s59, s61, 8\n"
    "s_and_b32 s59, s59, 0x2000\n"
    "s_add_u32 s61, s61, s59\n"
    "s_mov_b32 s60, 0\n"
    "v_cmp_gt_u32 vcc, s55, v7\n"
    "s_nop 4\n"
    "s_cbranch_vccz cw_ux_ok_%=\n"
    "cw_ux_poll_%=:\n"
    "buffer_load_dword v7, v8, s[40:43], s58 offen sc1\n"
    "s_waitcnt vmcnt(0)\n"
    "v_cmp_gt_u32 vcc, s55, v7\n"
    "s_nop 4\n"
    "s_cbranch_vccz cw_ux_ok_%=\n"
    "s_sleep 6\n"
    "s_add_u32 s60, s60, 1\n"
    "s_cmp_gt_u32 s60, 65536\n"
    "s_cbranch_scc1 cw_abort_%=\n"
    "s_and_b32 s59, s60, 63\n"
    "s_cbranch_scc1 cw_ux_poll_%=\n"
    "v_mov_b32 v10, 1024\n"
    "buffer_load_dword v10, v10, s[40:43], s58 offen sc1\n"
    "s_waitcnt vmcnt(0)\n"
    "v_readfirstlane_b32 s59, v10\n"
    "s_nop 0\n"
    "s_cmp_lg_u32 s59, 0\n"
    "s_nop 2\n"
    "s_cbranch_scc1 cw_end_%=\n"
    "s_branch cw_ux_poll_%=\n"
    "cw_ux_ok_%=:\n"
    "v_add_u32 v8, 128, v8\n"
    "s_memrealtime s[56:57]\n"
    "s_waitcnt lgkmcnt(0)\n"
    "s_sub_u32 s59, s64, s56\n"
    "s_cmp_gt_u32 s59, 1000\n"
    "s_cbranch_scc1 cw_wm_go_%=\n"
    "cw_wm_%=:\n"
    "s_memrealtime s[56:57]\n"
    "s_waitcnt lgkmcnt(0)\n"
    "s_sub_u32 s59, s64, s56\n"
    "s_cmp_gt_i32 s59, 3\n"
    "s_cbranch_scc1 cw_wm_%=\n"
    "cw_wm_f_%=:\n"
    "s_sub_u32 s59, s59, 1\n"
    "s_cmp_ge_i32 s59, 0\n"
    "s_cbranch_scc1 cw_wm_f_%=\n"
    "cw_wm_go_%=:\n"
    "s_mov_b32 s60, 0\n"
    "buffer_load_dwordx4 v[32:35], v0, s[40:43], s50 offen sc1\n"
    "buffer_load_dwordx4 v[36:39], v0, s[40:43], s50 offen offset:1024 sc1\n"
    "buffer_load_dwordx4 v[40:43], v0, s[40:43], s50 offen offset:2048 sc1\n"
    "buffer_load_dwordx4 v[44:47], v0, s[40:43], s50 offen offset:3072 sc1\n"
    "buffer_load_dwordx4 v[48:51], v0, s[40:43], s51 offen sc1\n"
    "buffer_load_dwordx4 v[52:55], v0, s[40:43], s51 offen offset:1024 sc1\n"
    "buffer_load_dwordx4 v[56:59], v0, s[40:43], s51 offen offset:2048 sc1\n"
    "buffer_load_dwordx4 v[60:63], v0, s[40:43], s51 offen offset:3072 sc1\n"
    "buffer_load_dwordx4 v[64:67], v0, s[40:43], s52 offen sc1\n"
    "buffer_load_dwordx4 v[68:71], v0, s[40:43], s52 offen offset:1024 sc1\n"
    "buffer_load_dwordx4 v[112:115], v0, s[40:43], s61 offen sc1\n"
    "buffer_load_dwordx4 v[116:119], v0, s[40:43], s61 offen offset:1024 sc1\n"
    "buffer_load_dwordx4 v[120:123], v0, s[40:43], s61 offen offset:2048 sc1\n"
    "buffer_load_dwordx4 v[124:127], v0, s[40:43], s61 offen offset:3072 sc1\n"
    "s_add_u32 s61, s61, 4096\n"
    "buffer_load_dwordx4 v[128:131], v0, s[40:43], s61 offen sc1\n"
    "buffer_load_dwordx4 v[132:135], v0, s[40:43], s61 offen offset:1024 sc1\n"
    "buffer_load_dwordx4 v[136:139], v0, s[40:43], s61 offen offset:2048 sc1\n"
    "buffer_load_dwordx4 v[140:143], v0, s[40:43], s61 offen offset:3072 sc1\n"
    "s_add_u32 s61, s61, 12288\n"
    "buffer_load_dwordx4 v[144:147], v0, s[40:43], s61 offen sc1\n"
    "buffer_load_dwordx4 v[148:151], v0, s[40:43], s61 offen offset:1024 sc1\n"
    "buffer_load_dwordx4 v[152:155], v0, s[40:43], s61 offen offset:2048 sc1\n"
    "buffer_load_dwordx4 v[156:159], v0, s[40:43], s61 offen offset:3072 sc1\n"
    "s_add_u32 s61, s61, 4096\n"
    "buffer_load_dwordx4 v[160:163], v0, s[40:43], s61 offen sc1\n"
    "buffer_load_dwordx4 v[164:167], v0, s[40:43], s61 offen offset:1024 sc1\n"
    "buffer_load_dwordx4 v[168:171], v0, s[40:43], s61 offen offset:2048 sc1\n"
    "buffer_load_dwordx4 v[172:175], v0, s[40:43], s61 offen offset:3072 sc1\n"
    "s_add_u32 s61, s61, 4096\n"
    "buffer_load_dword v7, v8, s[40:43], s58 offen sc1\n"
    "cw_mcomp_%=:\n"
    "s_waitcnt vmcnt(26)\n"
    "ds_write_b128 v0, v[32:35] offset:4096\n"
    "s_waitcnt vmcnt(25)\n"
    "ds_write_b128 v0, v[36:39] offset:5120\n"
    "s_waitcnt vmcnt(24)\n"
    "ds_write_b128 v0, v[40:43] offset:6144\n"
    "s_waitcnt vmcnt(23)\n"
    "ds_write_b128 v0, v[44:47] offset:7168\n"
    "s_waitcnt vmcnt(22)\n"
    "ds_write_b128 v0, v[48:51] offset:8192\n"
    "s_waitcnt vmcnt(21)\n"
    "ds_write_b128 v0, v[52:55] offset:9216\n"
    "s_waitcnt vmcnt(20)\n"
    "ds_write_b128 v0, v[56:59] offset:10240\n"
    "s_waitcnt vmcnt(19)\n"
    "ds_write_b128 v0, v[60:63] offset:11264\n"
    "s_waitcnt vmcnt(18)\n"
    "ds_write_b128 v0, v[64:67] offset:12288\n"
    "s_waitcnt vmcnt(17)\n"
    "ds_write_b128 v0, v[68:71] offset:13312\n"
    "v_max_u32 v6, v32, v33\n"
    "v_max3_u32 v6, v34, v35, v6\n"
    "v_max3_u32 v6, v36, v37, v6\n"
    "v_max3_u32 v6, v38, v39, v6\n"
    "v_max3_u32 v6, v40, v41, v6\n"
    "v_max3_u32 v6, v42, v43, v6\n"
    "v_max3_u32 v6, v44, v45, v6\n"
    "v_max3_u32 v6, v46, v47, v6\n"
    "v_max3_u32 v6, v48, v49, v6\n"
    "v_max3_u32 v6, v50, v51, v6\n"
    "v_max3_u32 v6, v52, v53, v6\n"
    "v_max3_u32 v6, v54, v55, v6\n"
    "v_max3_u32 v6, v56, v57, v6\n"
    "v_max3_u32 v6, v58, v59, v6\n"
    "v_max3_u32 v6, v60, v61, v6\n"
    "v_max3_u32 v6, v62, v63, v6\n"
    "v_max3_u32 v6, v64, v65, v6\n"
    "v_max3_u32 v6, v66, v67, v6\n"
    "v_max3_u32 v6, v68, v69, v6\n"
    "v_max3_u32 v6, v70, v71, v6\n"
    "v_cmp_eq_u32 vcc, -1, v6\n"
    "s_nop 4\n"
    "s_cbranch_vccnz cw_mfail_%=\n"
    "s_waitcnt lgkmcnt(0)\n"
    "s_barrier\n"
    "s_mul_i32 s50, s48, 4\n"
    "s_and_b32 s51, s46, 3\n"
    "s_add_u32 s50, s50, s51\n"
    "s_mul_i32 s50, s50, 49152\n"
    "s_add_u32 s51, s50, 4096\n"
    "s_add_u32 s52, s50, 16384\n"
    "s_add_u32 s53, s50, 20480\n"
    "s_add_u32 s62, s50, 32768\n"
    "s_add_u32 s49, s47, 1\n"
    "s_and_b32 s49, s49, 3\n"
    "s_mul_i32 s63, s49, 131072\n"
    "s_add_u32 s63, s63, 786432\n"
    "s_and_b32 s59, s47, 63\n"
    "s_cmp_eq_u32 s59, 63\n"
    "s_cbranch_scc1 cw_last_%=\n"
    "s_add_u32 s64, s65, s69\n"
    "s_memrealtime s[56:57]\n"
    "s_waitcnt lgkmcnt(0)\n"
    "s_sub_u32 s59, s64, s56\n"
    "s_cmp_gt_u32 s59, 1000\n"
    "s_cbranch_scc1 cw_ws_go_%=\n"
    "cw_ws_%=:\n"
    "s_memrealtime s[56:57]\n"
    "s_waitcnt lgkmcnt(0)\n"
    "s_sub_u32 s59, s64, s56\n"
    "s_cmp_gt_i32 s59, 3\n"
    "s_cbranch_scc1 cw_ws_%=\n"
    "cw_ws_f_%=:\n"
    "s_sub_u32 s59, s59, 1\n"
    "s_cmp_ge_i32 s59, 0\n"
    "s_cbranch_scc1 cw_ws_f_%=\n"
    "cw_ws_go_%=:\n"
    "s_and_b32 s59, s47, 64\n"
    "s_add_u32 s54, s47, s59\n"
    "s_lshl_b32 s54, s54, 7\n"
    "s_add_u32 s54, s54, 1310720\n"
    "s_lshl_b32 s59, s46, 15\n"
    "s_add_u32 s59, s59, s54\n"
    "s_add_u32 s56, s40, s59\n"
    "s_addc_u32 s57, s41, 0\n"
    "s_load_dwordx2 s[64:65], s[56:57], 0x0\n"
    "s_waitcnt lgkmcnt(0)\n"
    "s_max_u32 s59, s64, s65\n"
    "s_cmp_lg_u32 s59, -1\n"
    "s_cbranch_scc1 cw_rs_ok_%=\n"
    "s_and_b32 s59, s47, 64\n"
    "s_add_u32 s54, s47, s59\n"
    "s_lshl_b32 s54, s54, 7\n"
    "s_add_u32 s54, s54, 1310720\n"
    "s_mov_b32 s60, 0\n"
    "cw_rs_%=:\n"
    "buffer_load_dwordx2 v[10:11], v5, s[40:43], s54 offen sc1\n"
    "s_waitcnt vmcnt(0)\n"
    "v_readfirstlane_b32 s64, v10\n"
    "v_readfirstlane_b32 s65, v11\n"
    "s_nop 0\n"
    "s_max_u32 s59, s64, s65\n"
    "s_cmp_lg_u32 s59, -1\n"
    "s_nop 1\n"
    "s_cbranch_scc1 cw_rs_ok_%=\n"
    "s_sleep 4\n"
    "s_add_u32 s60, s60, 1\n"
    "s_cmp_gt_u32 s60, 65536\n"
    "s_cbranch_scc1 cw_abort_%=\n"
    "s_and_b32 s59, s60, 63\n"
    "s_cbranch_scc1 cw_rs_%=\n"
    "v_mov_b32 v10, 1024\n"
    "buffer_load_dword v10, v10, s[40:43], s58 offen sc1\n"
    "s_waitcnt vmcnt(0)\n"
    "v_readfirstlane_b32 s59, v10\n"
    "s_nop 0\n"
    "s_cmp_lg_u32 s59, 0\n"
    "s_nop 2\n"
    "s_cbranch_scc1 cw_end_%=\n"
    "s_branch cw_rs_%=\n"
    "cw_rs_ok_%=:\n"
    "s_mov_b32 s60, 0\n"
    "s_memrealtime s[56:57]\n"
    "s_waitcnt lgkmcnt(0)\n"
    "s_sub_u32 s59, s64, s56\n"
    "s_cmp_gt_u32 s59, 1000\n"
    "s_cbranch_scc1 cw_wu_go_%=\n"
    "cw_wu_%=:\n"
    "s_memrealtime s[56:57]\n"
    "s_waitcnt lgkmcnt(0)\n"
    "s_sub_u32 s59, s64, s56\n"
    "s_cmp_gt_i32 s59, 15\n"
    "s_cbranch_scc1 cw_wu_%=\n"
    "s_memrealtime s[30:31]\n"
    "s_nop 1\n"
    "s_memrealtime s[18:19]\n"
    "s_nop 1\n"
    "s_memrealtime s[34:35]\n"
    "s_nop 1\n"
    "s_memrealtime s[36:37]\n"
    "s_nop 1\n"
    "s_memrealtime s[38:39]\n"
    "s_nop 1\n"
    "s_memrealtime s[94:95]\n"
    "s_nop 1\n"
    "s_memrealtime s[96:97]\n"
    "s_nop 1\n"
    "s_memrealtime s[98:99]\n"
    "s_nop 1\n"
    "s_getpc_b64 s[56:57]\n"
    "cw_wu_pc_%=:\n"
    "s_waitcnt lgkmcnt(0)\n"
    "s_add_u32 s18, s18, s34\n"
    "s_add_u32 s18, s18, s36\n"
    "s_add_u32 s18, s18, s38\n"
    "s_add_u32 s18, s18, s94\n"
    "s_add_u32 s18, s18, s96\n"
    "s_add_u32 s18, s18, s98\n"
    "s_mul_i32 s34, s30, 7\n"
    "s_sub_u32 s18, s18, s34\n"
    "s_mul_i32 s18, s18, 3\n"
    "s_sub_u32 s59, s64, s30\n"
    "s_mul_i32 s59, s59, 23\n"
    "s_sub_u32 s59, s59, s18\n"
    "s_sub_u32 s59, s59, 170\n"
    "s_max_i32 s59, s59, 0\n"
    "s_min_i32 s59, s59, 252\n"
    "s_andn2_b32 s59, s59, 3\n"
    "s_sub_u32 s59, cw_wu_e_%=-cw_wu_pc_%=, s59\n"
    "s_add_u32 s56, s56, s59\n"
    "s_addc_u32 s57, s57, 0\n"
    "s_setpc_b64 s[56:57]\n"
    "s_nop 0\n"
    "s_nop 0\n"
    "s_nop 0\n"
    "s_nop 0\n"
    "s_nop 0\n"
    "s_nop 0\n"
    "s_nop 0\n"
    "s_nop 0\n"
    "s_nop 0\n"
    "s_nop 0\n"
    "s_nop 0\n"
    "s_nop 0\n"
    "s_nop 0\n"
    "s_nop 0\n"
    "s_nop 0\n"
    "s_nop 0\n"
    "s_nop 0\n"
    "s_nop 0\n"
    "s_nop 0\n"
    "s_nop 0\n"
    "s_nop 0\n"
    "s_nop 0\n"
    "s_nop 0\n"
    "s_nop 0\n"
    "s_nop 0\n"
    "s_nop 0\n"
    "s_nop 0\n"
    "s_nop 0\n"
    "s_nop 0\n"
    "s_nop 0\n"
    "s_nop 0\n"
    "s_nop 0\n"
    "s_nop 0\n"
    "s_nop 0\n"
    "s_nop 0\n"
    "s_nop 0\n"
    "s_nop 0\n"
    "s_nop 0\n"
    "s_nop 0\n"
    "s_nop 0\n"
    "s_nop 0\n"
    "s_nop 0\n"
    "s_nop 0\n"
    "s_nop 0\n"
    "s_nop 0\n"
    "s_nop 0\n"
    "s_nop 0\n"
    "s_nop 0\n"
    "s_nop 0\n"
    "s_nop 0\n"
    "s_nop 0\n"
    "s_nop 0\n"
    "s_nop 0\n"
    "s_nop 0\n"
    "s_nop 0\n"
    "s_nop 0\n"
    "s_nop 0\n"
    "s_nop 0\n"
    "s_nop 0\n"
    "s_nop 0\n"
    "s_nop 0\n"
    "s_nop 0\n"
    "s_nop 0\n"
    "cw_wu_e_%=:\n"
    "cw_wu_go_%=:\n"
    "buffer_load_dwordx4 v[32:35], v0, s[40:43], s50 offen sc1\n"
    "buffer_load_dwordx4 v[36:39], v0, s[40:43], s50 offen offset:1024 sc1\n"
    "buffer_load_dwordx4 v[40:43], v0, s[40:43], s50 offen offset:2048 sc1\n"
    "buffer_load_dwordx4 v[44:47], v0, s[40:43], s50 offen offset:3072 sc1\n"
    "buffer_load_dwordx4 v[48:51], v0, s[40:43], s51 offen sc1\n"
    "buffer_load_dwordx4 v[52:55], v0, s[40:43], s51 offen offset:1024 sc1\n"
    "buffer_load_dwordx4 v[56:59], v0, s[40:43], s51 offen offset:2048 sc1\n"
    "buffer_load_dwordx4 v[60:63], v0, s[40:43], s51 offen offset:3072 sc1\n"
    "buffer_load_dwordx4 v[64:67], v0, s[40:43], s52 offen sc1\n"
    "buffer_load_dwordx4 v[68:71], v0, s[40:43], s52 offen offset:1024 sc1\n"
    "buffer_load_dwordx4 v[72:75], v0, s[40:43], s52 offen offset:2048 sc1\n"
    "buffer_load_dwordx4 v[76:79], v0, s[40:43], s52 offen offset:3072 sc1\n"
    "buffer_load_dwordx4 v[80:83], v0, s[40:43], s53 offen sc1\n"
    "buffer_load_dwordx4 v[84:87], v0, s[40:43], s53 offen offset:1024 sc1\n"
    "buffer_load_dwordx4 v[88:91], v0, s[40:43], s53 offen offset:2048 sc1\n"
    "buffer_load_dwordx4 v[92:95], v0, s[40:43], s53 offen offset:3072 sc1\n"
    "buffer_load_dwordx4 v[96:99], v0, s[40:43], s62 offen sc1\n"
    "buffer_load_dwordx4 v[100:103], v0, s[40:43], s62 offen offset:1024 sc1\n"
    "buffer_load_dwordx4 v[104:107], v0, s[40:43], s62 offen offset:2048 sc1\n"
    "buffer_load_dwordx4 v[108:111], v0, s[40:43], s62 offen offset:3072 sc1\n"
    "s_barrier\n"
    "ds_read_b128 v[176:179], v0 offset:0\n"
    "ds_read_b128 v[180:183], v0 offset:1024\n"
    "ds_read_b128 v[184:187], v0 offset:2048\n"
    "ds_read_b128 v[188:191], v0 offset:3072\n"
    "cw_pcomp_%=:\n"
    "s_waitcnt vmcnt(19)\n"
    "v_mfma_f32_16x16x32_bf16 v[24:27], v[112:115], v[32:35], 0\n"
    "s_waitcnt vmcnt(18)\n"
    "v_mfma_f32_16x16x32_bf16 v[24:27], v[116:119], v[36:39], v[24:27]\n"
    "s_waitcnt vmcnt(17)\n"
    "v_mfma_f32_16x16x32_bf16 v[24:27], v[120:123], v[40:43], v[24:27]\n"
    "s_waitcnt vmcnt(16)\n"
    "v_mfma_f32_16x16x32_bf16 v[24:27], v[124:127], v[44:47], v[24:27]\n"
    "s_waitcnt vmcnt(15)\n"
    "v_mfma_f32_16x16x32_bf16 v[24:27], v[128:131], v[48:51], v[24:27]\n"
    "s_waitcnt vmcnt(14)\n"
    "v_mfma_f32_16x16x32_bf16 v[24:27], v[132:135], v[52:55], v[24:27]\n"
    "s_waitcnt vmcnt(13)\n"
    "v_mfma_f32_16x16x32_bf16 v[24:27], v[136:139], v[56:59], v[24:27]\n"
    "s_waitcnt vmcnt(12)\n"
    "v_mfma_f32_16x16x32_bf16 v[24:27], v[140:143], v[60:63], v[24:27]\n"
    "s_waitcnt vmcnt(11)\n"
    "v_mfma_f32_16x16x32_bf16 v[24:27], v[144:147], v[64:67], v[24:27]\n"
    "s_waitcnt vmcnt(10)\n"
    "v_mfma_f32_16x16x32_bf16 v[24:27], v[148:151], v[68:71], v[24:27]\n"
    "s_waitcnt vmcnt(9)\n"
    "v_mfma_f32_16x16x32_bf16 v[24:27], v[152:155], v[72:75], v[24:27]\n"
    "s_waitcnt vmcnt(8)\n"
    "v_mfma_f32_16x16x32_bf16 v[24:27], v[156:159], v[76:79], v[24:27]\n"
    "s_waitcnt vmcnt(7)\n"
    "v_mfma_f32_16x16x32_bf16 v[24:27], v[160:163], v[80:83], v[24:27]\n"
    "s_waitcnt vmcnt(6)\n"
    "v_mfma_f32_16x16x32_bf16 v[24:27], v[164:167], v[84:87], v[24:27]\n"
    "s_waitcnt vmcnt(5)\n"
    "v_mfma_f32_16x16x32_bf16 v[24:27], v[168:171], v[88:91], v[24:27]\n"
    "s_waitcnt vmcnt(4)\n"
    "v_mfma_f32_16x16x32_bf16 v[24:27], v[172:175], v[92:95], v[24:27]\n"
    "s_waitcnt vmcnt(3) lgkmcnt(3)\n"
    "v_mfma_f32_16x16x32_bf16 v[24:27], v[176:179], v[96:99], v[24:27]\n"
    "s_waitcnt vmcnt(2) lgkmcnt(2)\n"
    "v_mfma_f32_16x16x32_bf16 v[24:27], v[180:183], v[100:103], v[24:27]\n"
    "s_waitcnt vmcnt(1) lgkmcnt(1)\n"
    "v_mfma_f32_16x16x32_bf16 v[24:27], v[184:187], v[104:107], v[24:27]\n"
    "s_waitcnt vmcnt(0) lgkmcnt(0)\n"
    "v_mfma_f32_16x16x32_bf16 v[24:27], v[188:191], v[108:111], v[24:27]\n"
    "v_mov_b32 v11, v11\n"
    "v_mov_b32 v11, v11\n"
    "v_mov_b32 v11, v11\n"
    "v_mov_b32 v11, v11\n"
    "v_mov_b32 v11, v11\n"
    "v_mov_b32 v11, v11\n"
    "v_mov_b32 v11, v11\n"
    "v_mov_b32 v11, v11\n"
    "v_cmp_u_f32 vcc, v24, v24\n"
    "v_permlane32_swap_b32 v24, v26\n"
    "v_permlane32_swap_b32 v25, v27\n"
    "s_nop 0\n"
    "v_add_f32_dpp v24, v26, v24 row_ror:8 row_mask:0xf bank_mask:0xf\n"
    "v_add_f32_dpp v25, v27, v25 row_ror:8 row_mask:0xf bank_mask:0xf\n"
    "v_cvt_pk_bf16_f32 v28, v24, v25\n"
    "v_cvt_pk_bf16_f32 v30, v24, v25\n"
    "s_nop 1\n"
    "v_permlane16_swap_b32 v28, v30\n"
    "v_mov_b32 v29, v28\n"
    "v_mov_b32 v31, v30\n"
    "s_nop 0\n"
    "v_permlane32_swap_b32 v28, v29\n"
    "v_permlane32_swap_b32 v30, v31\n"
    "s_cbranch_vccnz cw_pfail_%=\n"
    "cw_presume_%=:\n"
    "buffer_store_dwordx4 v[28:31], v3, s[40:43], s63 offen sc1\n"
    "s_memrealtime s[30:31]\n"
    "s_nop 1\n"
    "s_memrealtime s[18:19]\n"
    "s_nop 1\n"
    "s_memrealtime s[34:35]\n"
    "s_nop 1\n"
    "s_memrealtime s[36:37]\n"
    "s_nop 1\n"
    "s_memrealtime s[38:39]\n"
    "s_nop 1\n"
    "s_memrealtime s[94:95]\n"
    "s_nop 1\n"
    "s_memrealtime s[96:97]\n"
    "s_nop 1\n"
    "s_memrealtime s[98:99]\n"
    "s_nop 1\n"
    "s_waitcnt lgkmcnt(0)\n"
    "s_add_u32 s18, s18, s34\n"
    "s_add_u32 s18, s18, s36\n"
    "s_add_u32 s18, s18, s38\n"
    "s_add_u32 s18, s18, s94\n"
    "s_add_u32 s18, s18, s96\n"
    "s_add_u32 s18, s18, s98\n"
    "s_mul_i32 s34, s30, 7\n"
    "s_sub_u32 s18, s18, s34\n"
    "s_mul_i32 s18, s18, 3\n"
    "s_sub_u32 s59, s30, s64\n"
    "s_mul_i32 s59, s59, 23\n"
    "s_add_u32 s59, s59, s18\n"
    "v_mov_b32 v10, s59\n"
    "s_lshl_b32 s34, s47, 10\n"
    "s_add_u32 s34, s34, 2368000\n"
    "s_cmp_eq_u32 s60, 0\n"
    "s_cselect_b32 s34, s34, 0x40000000\n"
    "buffer_atomic_umax v10, v4, s[40:43], s34 offen\n"
    "s_add_u32 s47, s47, 1\n"
    "s_nop 0\n"
    "s_branch cw_loopa_%=\n"
    "cw_last_%=:\n"
    "s_cmp_eq_u32 s47, 127\n"
    "s_cbranch_scc1 cw_end_%=\n"
    "buffer_store_dwordx4 v[192:195], v3, s[40:43], s63 offen sc1\n"
    "s_waitcnt vmcnt(0)\n"
    "s_barrier\n"
    "s_add_u32 s47, s47, 1\n"
    "s_branch cw_loopa_%=\n"
    "cw_pagain_%=:\n"
    "buffer_load_dwordx4 v[32:35], v0, s[40:43], s50 offen sc1\n"
    "buffer_load_dwordx4 v[36:39], v0, s[40:43], s50 offen offset:1024 sc1\n"
    "buffer_load_dwordx4 v[40:43], v0, s[40:43], s50 offen offset:2048 sc1\n"
    "buffer_load_dwordx4 v[44:47], v0, s[40:43], s50 offen offset:3072 sc1\n"
    "buffer_load_dwordx4 v[48:51], v0, s[40:43], s51 offen sc1\n"
    "buffer_load_dwordx4 v[52:55], v0, s[40:43], s51 offen offset:1024 sc1\n"
    "buffer_load_dwordx4 v[56:59], v0, s[40:43], s51 offen offset:2048 sc1\n"
    "buffer_load_dwordx4 v[60:63], v0, s[40:43], s51 offen offset:3072 sc1\n"
    "buffer_load_dwordx4 v[64:67], v0, s[40:43], s52 offen sc1\n"
    "buffer_load_dwordx4 v[68:71], v0, s[40:43], s52 offen offset:1024 sc1\n"
    "buffer_load_dwordx4 v[72:75], v0, s[40:43], s52 offen offset:2048 sc1\n"
    "buffer_load_dwordx4 v[76:79], v0, s[40:43], s52 offen offset:3072 sc1\n"
    "buffer_load_dwordx4 v[80:83], v0, s[40:43], s53 offen sc1\n"
    "buffer_load_dwordx4 v[84:87], v0, s[40:43], s53 offen offset:1024 sc1\n"
    "buffer_load_dwordx4 v[88:91], v0, s[40:43], s53 offen offset:2048 sc1\n"
    "buffer_load_dwordx4 v[92:95], v0, s[40:43], s53 offen offset:3072 sc1\n"
    "buffer_load_dwordx4 v[96:99], v0, s[40:43], s62 offen sc1\n"
    "buffer_load_dwordx4 v[100:103], v0, s[40:43], s62 offen offset:1024 sc1\n"
    "buffer_load_dwordx4 v[104:107], v0, s[40:43], s62 offen offset:2048 sc1\n"
    "buffer_load_dwordx4 v[108:111], v0, s[40:43], s62 offen offset:3072 sc1\n"
    "s_nop 1\n"
    "s_branch cw_pcomp_%=\n"
    "cw_mfail_%=:\n"
    "s_sleep 6\n"
    "s_add_u32 s60, s60, 1\n"
    "s_cmp_gt_u32 s60, 65536\n"
    "s_cbranch_scc1 cw_abort_%=\n"
    "s_and_b32 s59, s60, 63\n"
    "s_cbranch_scc1 cw_mretry_%=\n"
    "v_mov_b32 v10, 1024\n"
    "buffer_load_dword v10, v10, s[40:43], s58 offen sc1\n"
    "s_waitcnt vmcnt(0)\n"
    "v_readfirstlane_b32 s59, v10\n"
    "s_nop 0\n"
    "s_cmp_lg_u32 s59, 0\n"
    "s_nop 2\n"
    "s_cbranch_scc1 cw_end_%=\n"
    "s_branch cw_mretry_%=\n"
    "cw_mretry_%=:\n"
    "buffer_load_dwordx4 v[32:35], v0, s[40:43], s50 offen sc1\n"
    "buffer_load_dwordx4 v[36:39], v0, s[40:43], s50 offen offset:1024 sc1\n"
    "buffer_load_dwordx4 v[40:43], v0, s[40:43], s50 offen offset:2048 sc1\n"
    "buffer_load_dwordx4 v[44:47], v0, s[40:43], s50 offen offset:3072 sc1\n"
    "buffer_load_dwordx4 v[48:51], v0, s[40:43], s51 offen sc1\n"
    "buffer_load_dwordx4 v[52:55], v0, s[40:43], s51 offen offset:1024 sc1\n"
    "buffer_load_dwordx4 v[56:59], v0, s[40:43], s51 offen offset:2048 sc1\n"
    "buffer_load_dwordx4 v[60:63], v0, s[40:43], s51 offen offset:3072 sc1\n"
    "buffer_load_dwordx4 v[64:67], v0, s[40:43], s52 offen sc1\n"
    "buffer_load_dwordx4 v[68:71], v0, s[40:43], s52 offen offset:1024 sc1\n"
    "s_waitcnt vmcnt(0)\n"
    "s_nop 0\n"
    "s_branch cw_mcomp_%=\n"
    "cw_pfail_%=:\n"
    "v_max_u32 v6, v32, v33\n"
    "v_max3_u32 v6, v34, v35, v6\n"
    "v_max3_u32 v6, v36, v37, v6\n"
    "v_max3_u32 v6, v38, v39, v6\n"
    "v_max3_u32 v6, v40, v41, v6\n"
    "v_max3_u32 v6, v42, v43, v6\n"
    "v_max3_u32 v6, v44, v45, v6\n"
    "v_max3_u32 v6, v46, v47, v6\n"
    "v_max3_u32 v6, v48, v49, v6\n"
    "v_max3_u32 v6, v50, v51, v6\n"
    "v_max3_u32 v6, v52, v53, v6\n"
    "v_max3_u32 v6, v54, v55, v6\n"
    "v_max3_u32 v6, v56, v57, v6\n"
    "v_max3_u32 v6, v58, v59, v6\n"
    "v_max3_u32 v6, v60, v61, v6\n"
    "v_max3_u32 v6, v62, v63, v6\n"
    "v_max3_u32 v6, v64, v65, v6\n"
    "v_max3_u32 v6, v66, v67, v6\n"
    "v_max3_u32 v6, v68, v69, v6\n"
    "v_max3_u32 v6, v70, v71, v6\n"
    "v_max3_u32 v6, v72, v73, v6\n"
    "v_max3_u32 v6, v74, v75, v6\n"
    "v_max3_u32 v6, v76, v77, v6\n"
    "v_max3_u32 v6, v78, v79, v6\n"
    "v_max3_u32 v6, v80, v81, v6\n"
    "v_max3_u32 v6, v82, v83, v6\n"
    "v_max3_u32 v6, v84, v85, v6\n"
    "v_max3_u32 v6, v86, v87, v6\n"
    "v_max3_u32 v6, v88, v89, v6\n"
    "v_max3_u32 v6, v90, v91, v6\n"
    "v_max3_u32 v6, v92, v93, v6\n"
    "v_max3_u32 v6, v94, v95, v6\n"
    "v_max3_u32 v6, v96, v97, v6\n"
    "v_max3_u32 v6, v98, v99, v6\n"
    "v_max3_u32 v6, v100, v101, v6\n"
    "v_max3_u32 v6, v102, v103, v6\n"
    "v_max3_u32 v6, v104, v105, v6\n"
    "v_max3_u32 v6, v106, v107, v6\n"
    "v_max3_u32 v6, v108, v109, v6\n"
    "v_max3_u32 v6, v110, v111, v6\n"
    "v_cmp_eq_u32 vcc, -1, v6\n"
    "s_nop 4\n"
    "s_cbranch_vccz cw_presume_%=\n"
    "s_cmp_lg_u32 s60, 0\n"
    "s_cbranch_scc1 cw_pspin_%=\n"
    "s_mov_b32 s60, 1\n"
    "s_branch cw_pagain_%=\n"
    "cw_ppoll_%=:\n"
    "buffer_load_dword v10, v2, s[40:43], s62 offen sc1\n"
    "s_waitcnt vmcnt(0)\n"
    "v_cmp_eq_u32 vcc, -1, v10\n"
    "s_nop 4\n"
    "s_cbranch_vccz cw_pagain_%=\n"
    "cw_pspin_%=:\n"
    "s_add_u32 s60, s60, 1\n"
    "s_cmp_gt_u32 s60, 65536\n"
    "s_cbranch_scc1 cw_abort_%=\n"
    "s_and_b32 s59, s60, 63\n"
    "s_cbranch_scc1 cw_ppoll_%=\n"
    "v_mov_b32 v10, 1024\n"
    "buffer_load_dword v10, v10, s[40:43], s58 offen sc1\n"
    "s_waitcnt vmcnt(0)\n"
    "v_readfirstlane_b32 s59, v10\n"
    "s_nop 0\n"
    "s_cmp_lg_u32 s59, 0\n"
    "s_nop 2\n"
    "s_cbranch_scc1 cw_end_%=\n"
    "s_branch cw_ppoll_%=\n"
    "cw_wx_%=:\n"
    "s_mul_i32 s61, s45, 6\n"
    "s_sub_u32 s61, s61, 6\n"
    "s_min_u32 s61, s61, 10\n"
    "s_lshl_b32 s48, s44, 4\n"
    "s_add_u32 s48, s48, s61\n"
    "s_mul_i32 s50, s48, 20480\n"
    "s_lshl_b32 s49, s45, 15\n"
    "s_add_u32 s49, s49, -18432\n"
    "v_add_u32 v14, s49, v0\n"
    "v_and_b32 v8, 7, v15\n"
    "v_lshlrev_b32 v8, 7, v8\n"
    "v_add_u32 v8, 267776, v8\n"
    "v_mov_b32 v7, 0\n"
    "s_mov_b32 s55, 16\n"
    "s_mov_b32 s60, 0\n"
    "v_cmp_gt_u32 vcc, s55, v7\n"
    "s_nop 4\n"
    "s_cbranch_vccz cw_bgw_ok_%=\n"
    "cw_bgw_poll_%=:\n"
    "buffer_load_dword v7, v8, s[40:43], s58 offen sc1\n"
    "s_waitcnt vmcnt(0)\n"
    "v_cmp_gt_u32 vcc, s55, v7\n"
    "s_nop 4\n"
    "s_cbranch_vccz cw_bgw_ok_%=\n"
    "s_sleep 6\n"
    "s_add_u32 s60, s60, 1\n"
    "s_cmp_gt_u32 s60, 65536\n"
    "s_cbranch_scc1 cw_abort_%=\n"
    "s_and_b32 s59, s60, 63\n"
    "s_cbranch_scc1 cw_bgw_poll_%=\n"
    "v_mov_b32 v10, 1024\n"
    "buffer_load_dword v10, v10, s[40:43], s58 offen sc1\n"
    "s_waitcnt vmcnt(0)\n"
    "v_readfirstlane_b32 s59, v10\n"
    "s_nop 0\n"
    "s_cmp_lg_u32 s59, 0\n"
    "s_nop 2\n"
    "s_cbranch_scc1 cw_end_%=\n"
    "s_branch cw_bgw_poll_%=\n"
    "cw_bgw_ok_%=:\n"
    "v_mov_b32 v13, v14\n"
    "v_mov_b32 v14, v13\n"
    "s_and_b32 s48, s50, 0xffffe000\n"
    "s_add_u32 s48, s48, s50\n"
    "s_add_u32 s48, s48, 64421888\n"
    "s_lshr_b32 s59, s48, 8\n"
    "s_and_b32 s59, s59, 0x2000\n"
    "s_add_u32 s48, s48, s59\n"
    "s_add_u32 s50, s50, 4096\n"
    "buffer_load_dwordx4 v[16:19], v0, s[40:43], s48 offen sc1\n"
    "buffer_load_dwordx4 v[20:23], v0, s[40:43], s48 offen offset:1024 sc1\n"
    "buffer_load_dwordx4 v[32:35], v0, s[40:43], s48 offen offset:2048 sc1\n"
    "buffer_load_dwordx4 v[36:39], v0, s[40:43], s48 offen offset:3072 sc1\n"
    "s_and_b32 s48, s50, 0xffffe000\n"
    "s_add_u32 s48, s48, s50\n"
    "s_add_u32 s48, s48, 64421888\n"
    "s_lshr_b32 s59, s48, 8\n"
    "s_and_b32 s59, s59, 0x2000\n"
    "s_add_u32 s48, s48, s59\n"
    "s_add_u32 s50, s50, 4096\n"
    "buffer_load_dwordx4 v[48:51], v0, s[40:43], s48 offen sc1\n"
    "buffer_load_dwordx4 v[52:55], v0, s[40:43], s48 offen offset:1024 sc1\n"
    "buffer_load_dwordx4 v[64:67], v0, s[40:43], s48 offen offset:2048 sc1\n"
    "buffer_load_dwordx4 v[68:71], v0, s[40:43], s48 offen offset:3072 sc1\n"
    "s_and_b32 s48, s50, 0xffffe000\n"
    "s_add_u32 s48, s48, s50\n"
    "s_add_u32 s48, s48, 64421888\n"
    "s_lshr_b32 s59, s48, 8\n"
    "s_and_b32 s59, s59, 0x2000\n"
    "s_add_u32 s48, s48, s59\n"
    "s_add_u32 s50, s50, 4096\n"
    "buffer_load_dwordx4 v[80:83], v0, s[40:43], s48 offen sc1\n"
    "buffer_load_dwordx4 v[84:87], v0, s[40:43], s48 offen offset:1024 sc1\n"
    "buffer_load_dwordx4 v[96:99], v0, s[40:43], s48 offen offset:2048 sc1\n"
    "buffer_load_dwordx4 v[100:103], v0, s[40:43], s48 offen offset:3072 sc1\n"
    "s_and_b32 s48, s50, 0xffffe000\n"
    "s_add_u32 s48, s48, s50\n"
    "s_add_u32 s48, s48, 64421888\n"
    "s_lshr_b32 s59, s48, 8\n"
    "s_and_b32 s59, s59, 0x2000\n"
    "s_add_u32 s48, s48, s59\n"
    "s_add_u32 s50, s50, 4096\n"
    "buffer_load_dwordx4 v[112:115], v0, s[40:43], s48 offen sc1\n"
    "buffer_load_dwordx4 v[116:119], v0, s[40:43], s48 offen offset:1024 sc1\n"
    "buffer_load_dwordx4 v[120:123], v0, s[40:43], s48 offen offset:2048 sc1\n"
    "buffer_load_dwordx4 v[124:127], v0, s[40:43], s48 offen offset:3072 sc1\n"
    "s_and_b32 s48, s50, 0xffffe000\n"
    "s_add_u32 s48, s48, s50\n"
    "s_add_u32 s48, s48, 64421888\n"
    "s_lshr_b32 s59, s48, 8\n"
    "s_and_b32 s59, s59, 0x2000\n"
    "s_add_u32 s48, s48, s59\n"
    "s_add_u32 s50, s50, 4096\n"
    "buffer_load_dwordx4 v[128:131], v0, s[40:43], s48 offen sc1\n"
    "buffer_load_dwordx4 v[132:135], v0, s[40:43], s48 offen offset:1024 sc1\n"
    "buffer_load_dwordx4 v[136:139], v0, s[40:43], s48 offen offset:2048 sc1\n"
    "buffer_load_dwordx4 v[140:143], v0, s[40:43], s48 offen offset:3072 sc1\n"
    "s_and_b32 s48, s50, 0xffffe000\n"
    "s_add_u32 s48, s48, s50\n"
    "s_add_u32 s48, s48, 64421888\n"
    "s_lshr_b32 s59, s48, 8\n"
    "s_and_b32 s59, s59, 0x2000\n"
    "s_add_u32 s48, s48, s59\n"
    "s_add_u32 s50, s50, 4096\n"
    "buffer_load_dwordx4 a[0:3], v0, s[40:43], s48 offen sc1\n"
    "buffer_load_dwordx4 a[4:7], v0, s[40:43], s48 offen offset:1024 sc1\n"
    "buffer_load_dwordx4 a[32:35], v0, s[40:43], s48 offen offset:2048 sc1\n"
    "buffer_load_dwordx4 a[36:39], v0, s[40:43], s48 offen offset:3072 sc1\n"
    "s_and_b32 s48, s50, 0xffffe000\n"
    "s_add_u32 s48, s48, s50\n"
    "s_add_u32 s48, s48, 64421888\n"
    "s_lshr_b32 s59, s48, 8\n"
    "s_and_b32 s59, s59, 0x2000\n"
    "s_add_u32 s48, s48, s59\n"
    "s_add_u32 s50, s50, 4096\n"
    "buffer_load_dwordx4 a[64:67], v0, s[40:43], s48 offen sc1\n"
    "buffer_load_dwordx4 a[68:71], v0, s[40:43], s48 offen offset:1024 sc1\n"
    "buffer_load_dwordx4 a[96:99], v0, s[40:43], s48 offen offset:2048 sc1\n"
    "buffer_load_dwordx4 a[100:103], v0, s[40:43], s48 offen offset:3072 sc1\n"
    "s_and_b32 s48, s50, 0xffffe000\n"
    "s_add_u32 s48, s48, s50\n"
    "s_add_u32 s48, s48, 64421888\n"
    "s_lshr_b32 s59, s48, 8\n"
    "s_and_b32 s59, s59, 0x2000\n"
    "s_add_u32 s48, s48, s59\n"
    "s_add_u32 s50, s50, 4096\n"
    "buffer_load_dwordx4 a[128:131], v0, s[40:43], s48 offen sc1\n"
    "buffer_load_dwordx4 a[132:135], v0, s[40:43], s48 offen offset:1024 sc1\n"
    "buffer_load_dwordx4 a[160:163], v0, s[40:43], s48 offen offset:2048 sc1\n"
    "buffer_load_dwordx4 a[164:167], v0, s[40:43], s48 offen offset:3072 sc1\n"
    "s_and_b32 s48, s50, 0xffffe000\n"
    "s_add_u32 s48, s48, s50\n"
    "s_add_u32 s48, s48, 64421888\n"
    "s_lshr_b32 s59, s48, 8\n"
    "s_and_b32 s59, s59, 0x2000\n"
    "s_add_u32 s48, s48, s59\n"
    "s_add_u32 s50, s50, 4096\n"
    "buffer_load_dwordx4 a[192:195], v0, s[40:43], s48 offen sc1\n"
    "buffer_load_dwordx4 a[196:199], v0, s[40:43], s48 offen offset:1024 sc1\n"
    "buffer_load_dwordx4 a[232:235], v0, s[40:43], s48 offen offset:2048 sc1\n"
    "buffer_load_dwordx4 a[236:239], v0, s[40:43], s48 offen offset:3072 sc1\n"
    "s_and_b32 s48, s50, 0xffffe000\n"
    "s_add_u32 s48, s48, s50\n"
    "s_add_u32 s48, s48, 64421888\n"
    "s_lshr_b32 s59, s48, 8\n"
    "s_and_b32 s59, s59, 0x2000\n"
    "s_add_u32 s48, s48, s59\n"
    "s_add_u32 s50, s50, 4096\n"
    "buffer_load_dwordx4 v[176:179], v0, s[40:43], s48 offen sc1\n"
    "buffer_load_dwordx4 v[180:183], v0, s[40:43], s48 offen offset:1024 sc1\n"
    "buffer_load_dwordx4 v[216:219], v0, s[40:43], s48 offen offset:2048 sc1\n"
    "buffer_load_dwordx4 v[220:223], v0, s[40:43], s48 offen offset:3072 sc1\n"
    "s_and_b32 s48, s50, 0xffffe000\n"
    "s_add_u32 s48, s48, s50\n"
    "s_add_u32 s48, s48, 64421888\n"
    "s_lshr_b32 s59, s48, 8\n"
    "s_and_b32 s59, s59, 0x2000\n"
    "s_add_u32 s48, s48, s59\n"
    "s_add_u32 s50, s50, 4096\n"
    "buffer_load_dwordx4 a[8:11], v0, s[40:43], s48 offen sc1\n"
    "buffer_load_dwordx4 a[12:15], v0, s[40:43], s48 offen offset:1024 sc1\n"
    "buffer_load_dwordx4 a[40:43], v0, s[40:43], s48 offen offset:2048 sc1\n"
    "buffer_load_dwordx4 a[44:47], v0, s[40:43], s48 offen offset:3072 sc1\n"
    "s_and_b32 s48, s50, 0xffffe000\n"
    "s_add_u32 s48, s48, s50\n"
    "s_add_u32 s48, s48, 64421888\n"
    "s_lshr_b32 s59, s48, 8\n"
    "s_and_b32 s59, s59, 0x2000\n"
    "s_add_u32 s48, s48, s59\n"
    "s_add_u32 s50, s50, 4096\n"
    "buffer_load_dwordx4 a[72:75], v0, s[40:43], s48 offen sc1\n"
    "buffer_load_dwordx4 a[76:79], v0, s[40:43], s48 offen offset:1024 sc1\n"
    "buffer_load_dwordx4 a[104:107], v0, s[40:43], s48 offen offset:2048 sc1\n"
    "buffer_load_dwordx4 a[108:111], v0, s[40:43], s48 offen offset:3072 sc1\n"
    "s_and_b32 s48, s50, 0xffffe000\n"
    "s_add_u32 s48, s48, s50\n"
    "s_add_u32 s48, s48, 64421888\n"
    "s_lshr_b32 s59, s48, 8\n"
    "s_and_b32 s59, s59, 0x2000\n"
    "s_add_u32 s48, s48, s59\n"
    "s_add_u32 s50, s50, 4096\n"
    "buffer_load_dwordx4 a[136:139], v0, s[40:43], s48 offen sc1\n"
    "buffer_load_dwordx4 a[140:143], v0, s[40:43], s48 offen offset:1024 sc1\n"
    "buffer_load_dwordx4 a[168:171], v0, s[40:43], s48 offen offset:2048 sc1\n"
    "buffer_load_dwordx4 a[172:175], v0, s[40:43], s48 offen offset:3072 sc1\n"
    "s_and_b32 s48, s50, 0xffffe000\n"
    "s_add_u32 s48, s48, s50\n"
    "s_add_u32 s48, s48, 64421888\n"
    "s_lshr_b32 s59, s48, 8\n"
    "s_and_b32 s59, s59, 0x2000\n"
    "s_add_u32 s48, s48, s59\n"
    "s_add_u32 s50, s50, 4096\n"
    "buffer_load_dwordx4 a[200:203], v0, s[40:43], s48 offen sc1\n"
    "buffer_load_dwordx4 a[204:207], v0, s[40:43], s48 offen offset:1024 sc1\n"
    "buffer_load_dwordx4 a[240:243], v0, s[40:43], s48 offen offset:2048 sc1\n"
    "buffer_load_dwordx4 a[244:247], v0, s[40:43], s48 offen offset:3072 sc1\n"
    "s_and_b32 s48, s50, 0xffffe000\n"
    "s_add_u32 s48, s48, s50\n"
    "s_add_u32 s48, s48, 64421888\n"
    "s_lshr_b32 s59, s48, 8\n"
    "s_and_b32 s59, s59, 0x2000\n"
    "s_add_u32 s48, s48, s59\n"
    "s_add_u32 s50, s50, 4096\n"
    "buffer_load_dwordx4 v[184:187], v0, s[40:43], s48 offen sc1\n"
    "buffer_load_dwordx4 v[188:191], v0, s[40:43], s48 offen offset:1024 sc1\n"
    "buffer_load_dwordx4 v[224:227], v0, s[40:43], s48 offen offset:2048 sc1\n"
    "buffer_load_dwordx4 v[228:231], v0, s[40:43], s48 offen offset:3072 sc1\n"
    "s_and_b32 s48, s50, 0xffffe000\n"
    "s_add_u32 s48, s48, s50\n"
    "s_add_u32 s48, s48, 64421888\n"
    "s_lshr_b32 s59, s48, 8\n"
    "s_and_b32 s59, s59, 0x2000\n"
    "s_add_u32 s48, s48, s59\n"
    "s_add_u32 s50, s50, 4096\n"
    "buffer_load_dwordx4 v[24:27], v0, s[40:43], s48 offen sc1\n"
    "buffer_load_dwordx4 v[28:31], v0, s[40:43], s48 offen offset:1024 sc1\n"
    "buffer_load_dwordx4 v[40:43], v0, s[40:43], s48 offen offset:2048 sc1\n"
    "s_waitcnt vmcnt(61)\n"
    "buffer_load_dwordx4 v[44:47], v0, s[40:43], s48 offen offset:3072 sc1\n"
    "s_and_b32 s48, s50, 0xffffe000\n"
    "s_add_u32 s48, s48, s50\n"
    "s_add_u32 s48, s48, 64421888\n"
    "s_lshr_b32 s59, s48, 8\n"
    "s_and_b32 s59, s59, 0x2000\n"
    "s_add_u32 s48, s48, s59\n"
    "s_add_u32 s50, s50, 4096\n"
    "buffer_load_dwordx4 v[56:59], v0, s[40:43], s48 offen sc1\n"
    "s_waitcnt vmcnt(61)\n"
    "buffer_load_dwordx4 v[60:63], v0, s[40:43], s48 offen offset:1024 sc1\n"
    "buffer_load_dwordx4 v[72:75], v0, s[40:43], s48 offen offset:2048 sc1\n"
    "s_waitcnt vmcnt(61)\n"
    "buffer_load_dwordx4 v[76:79], v0, s[40:43], s48 offen offset:3072 sc1\n"
    "s_and_b32 s48, s50, 0xffffe000\n"
    "s_add_u32 s48, s48, s50\n"
    "s_add_u32 s48, s48, 64421888\n"
    "s_lshr_b32 s59, s48, 8\n"
    "s_and_b32 s59, s59, 0x2000\n"
    "s_add_u32 s48, s48, s59\n"
    "s_add_u32 s50, s50, 4096\n"
    "buffer_load_dwordx4 v[88:91], v0, s[40:43], s48 offen sc1\n"
    "s_waitcnt vmcnt(61)\n"
    "buffer_load_dwordx4 v[92:95], v0, s[40:43], s48 offen offset:1024 sc1\n"
    "buffer_load_dwordx4 v[104:107], v0, s[40:43], s48 offen offset:2048 sc1\n"
    "s_waitcnt vmcnt(61)\n"
    "buffer_load_dwordx4 v[108:111], v0, s[40:43], s48 offen offset:3072 sc1\n"
    "s_and_b32 s48, s50, 0xffffe000\n"
    "s_add_u32 s48, s48, s50\n"
    "s_add_u32 s48, s48, 64421888\n"
    "s_lshr_b32 s59, s48, 8\n"
    "s_and_b32 s59, s59, 0x2000\n"
    "s_add_u32 s48, s48, s59\n"
    "s_add_u32 s50, s50, 4096\n"
    "buffer_load_dwordx4 a[208:211], v0, s[40:43], s48 offen sc1\n"
    "s_waitcnt vmcnt(61)\n"
    "buffer_load_dwordx4 a[212:215], v0, s[40:43], s48 offen offset:1024 sc1\n"
    "buffer_load_dwordx4 a[248:251], v0, s[40:43], s48 offen offset:2048 sc1\n"
    "s_waitcnt vmcnt(61)\n"
    "buffer_load_dwordx4 a[252:255], v0, s[40:43], s48 offen offset:3072 sc1\n"
    "s_and_b32 s48, s50, 0xffffe000\n"
    "s_add_u32 s48, s48, s50\n"
    "s_add_u32 s48, s48, 64421888\n"
    "s_lshr_b32 s59, s48, 8\n"
    "s_and_b32 s59, s59, 0x2000\n"
    "s_add_u32 s48, s48, s59\n"
    "s_add_u32 s50, s50, 4096\n"
    "buffer_load_dwordx4 v[192:195], v0, s[40:43], s48 offen sc1\n"
    "s_waitcnt vmcnt(61)\n"
    "buffer_load_dwordx4 v[196:199], v0, s[40:43], s48 offen offset:1024 sc1\n"
    "buffer_load_dwordx4 v[232:235], v0, s[40:43], s48 offen offset:2048 sc1\n"
    "s_waitcnt vmcnt(61)\n"
    "buffer_load_dwordx4 v[236:239], v0, s[40:43], s48 offen offset:3072 sc1\n"
    "s_and_b32 s48, s50, 0xffffe000\n"
    "s_add_u32 s48, s48, s50\n"
    "s_add_u32 s48, s48, 64421888\n"
    "s_lshr_b32 s59, s48, 8\n"
    "s_and_b32 s59, s59, 0x2000\n"
    "s_add_u32 s48, s48, s59\n"
    "s_add_u32 s50, s50, 4096\n"
    "buffer_load_dwordx4 a[16:19], v0, s[40:43], s48 offen sc1\n"
    "s_waitcnt vmcnt(61)\n"
    "buffer_load_dwordx4 a[20:23], v0, s[40:43], s48 offen offset:1024 sc1\n"
    "buffer_load_dwordx4 a[48:51], v0, s[40:43], s48 offen offset:2048 sc1\n"
    "s_waitcnt vmcnt(61)\n"
    "buffer_load_dwordx4 a[52:55], v0, s[40:43], s48 offen offset:3072 sc1\n"
    "s_and_b32 s48, s50, 0xffffe000\n"
    "s_add_u32 s48, s48, s50\n"
    "s_add_u32 s48, s48, 64421888\n"
    "s_lshr_b32 s59, s48, 8\n"
    "s_and_b32 s59, s59, 0x2000\n"
    "s_add_u32 s48, s48, s59\n"
    "s_add_u32 s50, s50, 4096\n"
    "buffer_load_dwordx4 a[80:83], v0, s[40:43], s48 offen sc1\n"
    "s_waitcnt vmcnt(61)\n"
    "buffer_load_dwordx4 a[84:87], v0, s[40:43], s48 offen offset:1024 sc1\n"
    "buffer_load_dwordx4 a[112:115], v0, s[40:43], s48 offen offset:2048 sc1\n"
    "s_waitcnt vmcnt(61)\n"
    "buffer_load_dwordx4 a[116:119], v0, s[40:43], s48 offen offset:3072 sc1\n"
    "s_and_b32 s48, s50, 0xffffe000\n"
    "s_add_u32 s48, s48, s50\n"
    "s_add_u32 s48, s48, 64421888\n"
    "s_lshr_b32 s59, s48, 8\n"
    "s_and_b32 s59, s59, 0x2000\n"
    "s_add_u32 s48, s48, s59\n"
    "s_add_u32 s50, s50, 4096\n"
    "buffer_load_dwordx4 a[144:147], v0, s[40:43], s48 offen sc1\n"
    "s_waitcnt vmcnt(61)\n"
    "buffer_load_dwordx4 a[148:151], v0, s[40:43], s48 offen offset:1024 sc1\n"
    "buffer_load_dwordx4 a[176:179], v0, s[40:43], s48 offen offset:2048 sc1\n"
    "s_waitcnt vmcnt(61)\n"
    "buffer_load_dwordx4 a[180:183], v0, s[40:43], s48 offen offset:3072 sc1\n"
    "s_and_b32 s48, s50, 0xffffe000\n"
    "s_add_u32 s48, s48, s50\n"
    "s_add_u32 s48, s48, 64421888\n"
    "s_lshr_b32 s59, s48, 8\n"
    "s_and_b32 s59, s59, 0x2000\n"
    "s_add_u32 s48, s48, s59\n"
    "s_add_u32 s50, s50, 4096\n"
    "buffer_load_dwordx4 a[216:219], v0, s[40:43], s48 offen sc1\n"
    "s_waitcnt vmcnt(61)\n"
    "buffer_load_dwordx4 a[220:223], v0, s[40:43], s48 offen offset:1024 sc1\n"
    "buffer_load_dwordx4 v[160:163], v0, s[40:43], s48 offen offset:2048 sc1\n"
    "s_waitcnt vmcnt(61)\n"
    "buffer_load_dwordx4 v[164:167], v0, s[40:43], s48 offen offset:3072 sc1\n"
    "s_and_b32 s48, s50, 0xffffe000\n"
    "s_add_u32 s48, s48, s50\n"
    "s_add_u32 s48, s48, 64421888\n"
    "s_lshr_b32 s59, s48, 8\n"
    "s_and_b32 s59, s59, 0x2000\n"
    "s_add_u32 s48, s48, s59\n"
    "s_add_u32 s50, s50, 4096\n"
    "buffer_load_dwordx4 v[200:203], v0, s[40:43], s48 offen sc1\n"
    "s_waitcnt vmcnt(61)\n"
    "buffer_load_dwordx4 v[204:207], v0, s[40:43], s48 offen offset:1024 sc1\n"
    "buffer_load_dwordx4 v[240:243], v0, s[40:43], s48 offen offset:2048 sc1\n"
    "s_waitcnt vmcnt(61)\n"
    "buffer_load_dwordx4 v[244:247], v0, s[40:43], s48 offen offset:3072 sc1\n"
    "s_and_b32 s48, s50, 0xffffe000\n"
    "s_add_u32 s48, s48, s50\n"
    "s_add_u32 s48, s48, 64421888\n"
    "s_lshr_b32 s59, s48, 8\n"
    "s_and_b32 s59, s59, 0x2000\n"
    "s_add_u32 s48, s48, s59\n"
    "s_add_u32 s50, s50, 4096\n"
    "buffer_load_dwordx4 a[24:27], v0, s[40:43], s48 offen sc1\n"
    "s_waitcnt vmcnt(61)\n"
    "buffer_load_dwordx4 a[28:31], v0, s[40:43], s48 offen offset:1024 sc1\n"
    "buffer_load_dwordx4 a[56:59], v0, s[40:43], s48 offen offset:2048 sc1\n"
    "s_waitcnt vmcnt(61)\n"
    "buffer_load_dwordx4 a[60:63], v0, s[40:43], s48 offen offset:3072 sc1\n"
    "s_and_b32 s48, s50, 0xffffe000\n"
    "s_add_u32 s48, s48, s50\n"
    "s_add_u32 s48, s48, 64421888\n"
    "s_lshr_b32 s59, s48, 8\n"
    "s_and_b32 s59, s59, 0x2000\n"
    "s_add_u32 s48, s48, s59\n"
    "s_add_u32 s50, s50, 4096\n"
    "buffer_load_dwordx4 a[88:91], v0, s[40:43], s48 offen sc1\n"
    "s_waitcnt vmcnt(61)\n"
    "buffer_load_dwordx4 a[92:95], v0, s[40:43], s48 offen offset:1024 sc1\n"
    "buffer_load_dwordx4 a[120:123], v0, s[40:43], s48 offen offset:2048 sc1\n"
    "s_waitcnt vmcnt(61)\n"
    "buffer_load_dwordx4 a[124:127], v0, s[40:43], s48 offen offset:3072 sc1\n"
    "s_and_b32 s48, s50, 0xffffe000\n"
    "s_add_u32 s48, s48, s50\n"
    "s_add_u32 s48, s48, 64421888\n"
    "s_lshr_b32 s59, s48, 8\n"
    "s_and_b32 s59, s59, 0x2000\n"
    "s_add_u32 s48, s48, s59\n"
    "s_add_u32 s50, s50, 4096\n"
    "buffer_load_dwordx4 a[152:155], v0, s[40:43], s48 offen sc1\n"
    "s_waitcnt vmcnt(61)\n"
    "buffer_load_dwordx4 a[156:159], v0, s[40:43], s48 offen offset:1024 sc1\n"
    "buffer_load_dwordx4 a[184:187], v0, s[40:43], s48 offen offset:2048 sc1\n"
    "s_waitcnt vmcnt(61)\n"
    "buffer_load_dwordx4 a[188:191], v0, s[40:43], s48 offen offset:3072 sc1\n"
    "s_and_b32 s48, s50, 0xffffe000\n"
    "s_add_u32 s48, s48, s50\n"
    "s_add_u32 s48, s48, 64421888\n"
    "s_lshr_b32 s59, s48, 8\n"
    "s_and_b32 s59, s59, 0x2000\n"
    "s_add_u32 s48, s48, s59\n"
    "s_add_u32 s50, s50, 4096\n"
    "buffer_load_dwordx4 a[224:227], v0, s[40:43], s48 offen sc1\n"
    "s_waitcnt vmcnt(61)\n"
    "buffer_load_dwordx4 a[228:231], v0, s[40:43], s48 offen offset:1024 sc1\n"
    "buffer_load_dwordx4 v[168:171], v0, s[40:43], s48 offen offset:2048 sc1\n"
    "s_waitcnt vmcnt(61)\n"
    "buffer_load_dwordx4 v[172:175], v0, s[40:43], s48 offen offset:3072 sc1\n"
    "s_and_b32 s48, s50, 0xffffe000\n"
    "s_add_u32 s48, s48, s50\n"
    "s_add_u32 s48, s48, 64421888\n"
    "s_lshr_b32 s59, s48, 8\n"
    "s_and_b32 s59, s59, 0x2000\n"
    "s_add_u32 s48, s48, s59\n"
    "s_add_u32 s50, s50, 4096\n"
    "buffer_load_dwordx4 v[208:211], v0, s[40:43], s48 offen sc1\n"
    "s_waitcnt vmcnt(61)\n"
    "buffer_load_dwordx4 v[212:215], v0, s[40:43], s48 offen offset:1024 sc1\n"
    "buffer_load_dwordx4 v[248:251], v0, s[40:43], s48 offen offset:2048 sc1\n"
    "s_waitcnt vmcnt(61)\n"
    "buffer_load_dwordx4 v[252:255], v0, s[40:43], s48 offen offset:3072 sc1\n"
    "ds_write_b128 v14, v[16:19] offset:0\n"
    "ds_write_b128 v14, v[20:23] offset:1024\n"
    "s_waitcnt vmcnt(59)\n"
    "ds_write_b128 v14, v[24:27] offset:2048\n"
    "s_waitcnt vmcnt(58)\n"
    "ds_write_b128 v14, v[28:31] offset:3072\n"
    "ds_write_b128 v14, v[32:35] offset:4096\n"
    "ds_write_b128 v14, v[36:39] offset:5120\n"
    "s_waitcnt vmcnt(57)\n"
    "ds_write_b128 v14, v[40:43] offset:6144\n"
    "s_waitcnt vmcnt(56)\n"
    "ds_write_b128 v14, v[44:47] offset:7168\n"
    "ds_write_b128 v14, v[48:51] offset:8192\n"
    "ds_write_b128 v14, v[52:55] offset:9216\n"
    "s_waitcnt vmcnt(55)\n"
    "ds_write_b128 v14, v[56:59] offset:10240\n"
    "s_waitcnt vmcnt(54)\n"
    "ds_write_b128 v14, v[60:63] offset:11264\n"
    "ds_write_b128 v14, v[64:67] offset:12288\n"
    "ds_write_b128 v14, v[68:71] offset:13312\n"
    "s_waitcnt vmcnt(53)\n"
    "ds_write_b128 v14, v[72:75] offset:14336\n"
    "s_waitcnt vmcnt(52)\n"
    "ds_write_b128 v14, v[76:79] offset:15360\n"
    "s_waitcnt lgkmcnt(14)\n"
    "ds_write_b128 v14, v[80:83] offset:16384\n"
    "ds_write_b128 v14, v[84:87] offset:17408\n"
    "s_waitcnt lgkmcnt(14)\n"
    "s_waitcnt vmcnt(51)\n"
    "ds_write_b128 v14, v[88:91] offset:18432\n"
    "s_waitcnt vmcnt(50)\n"
    "ds_write_b128 v14, v[92:95] offset:19456\n"
    "s_waitcnt lgkmcnt(14)\n"
    "ds_write_b128 v14, v[96:99] offset:20480\n"
    "ds_write_b128 v14, v[100:103] offset:21504\n"
    "s_waitcnt lgkmcnt(14)\n"
    "s_waitcnt vmcnt(49)\n"
    "ds_write_b128 v14, v[104:107] offset:22528\n"
    "s_waitcnt vmcnt(48)\n"
    "ds_write_b128 v14, v[108:111] offset:23552\n"
    "s_waitcnt lgkmcnt(14)\n"
    "ds_write_b128 v14, v[112:115] offset:24576\n"
    "ds_write_b128 v14, v[116:119] offset:25600\n"
    "s_waitcnt lgkmcnt(14)\n"
    "ds_write_b128 v14, v[120:123] offset:26624\n"
    "ds_write_b128 v14, v[124:127] offset:27648\n"
    "s_waitcnt lgkmcnt(14)\n"
    "ds_write_b128 v14, v[128:131] offset:28672\n"
    "ds_write_b128 v14, v[132:135] offset:29696\n"
    "s_waitcnt lgkmcnt(14)\n"
    "ds_write_b128 v14, v[136:139] offset:30720\n"
    "ds_write_b128 v14, v[140:143] offset:31744\n"
    "s_waitcnt lgkmcnt(14)\n"
    "s_waitcnt vmcnt(0) lgkmcnt(0)\n"
    "v_mov_b32 v134, v14\n"
    "v_mov_b32 v128, -1\n"
    "v_mov_b32 v129, -1\n"
    "v_mov_b32 v130, -1\n"
    "v_mov_b32 v131, -1\n"
    "s_cmp_eq_u32 s45, 2\n"
    "s_cselect_b32 s59, 1, 0\n"
    "v_lshlrev_b32 v132, 7, v15\n"
    "v_cmp_gt_u32 vcc, s59, v15\n"
    "v_mov_b32 v10, 2147483632\n"
    "s_nop 0\n"
    "v_cndmask_b32 v132, v10, v132, vcc\n"
    "v_mov_b32 v133, 1\n"
    "s_mul_i32 s48, s44, 3277\n"
    "s_lshr_b32 s48, s48, 16\n"
    "s_lshl_b32 s48, s48, 7\n"
    "s_add_u32 s65, s48, 2158592\n"
    "s_mov_b32 s62, 0x40000000\n"
    "s_mov_b32 s63, 0x40000000\n"
    "s_mov_b32 s64, 0x40000000\n"
    "v_and_b32 v9, 0xf0, v0\n"
    "v_lshrrev_b32 v10, 1, v12\n"
    "v_lshl_add_u32 v9, v10, 8, v9\n"
    "v_and_b32 v10, 1, v12\n"
    "v_lshl_add_u32 v9, v10, 3, v9\n"
    "s_lshl_b32 s48, s61, 8\n"
    "v_add_u32 v9, s48, v9\n"
    "cw_loopb_%=:\n"
    "v_mov_b32 v56, 0\n"
    "v_mov_b32 v57, 0\n"
    "v_mov_b32 v58, 0\n"
    "v_mov_b32 v59, 0\n"
    "v_mov_b32 v60, 0\n"
    "v_mov_b32 v61, 0\n"
    "v_mov_b32 v62, 0\n"
    "v_mov_b32 v63, 0\n"
    "v_mov_b32 v64, 0\n"
    "v_mov_b32 v65, 0\n"
    "v_mov_b32 v66, 0\n"
    "v_mov_b32 v67, 0\n"
    "v_mov_b32 v68, 0\n"
    "v_mov_b32 v69, 0\n"
    "v_mov_b32 v70, 0\n"
    "v_mov_b32 v71, 0\n"
    "v_mov_b32 v72, 0\n"
    "v_mov_b32 v73, 0\n"
    "v_mov_b32 v74, 0\n"
    "v_mov_b32 v75, 0\n"
    "v_mov_b32 v76, 0\n"
    "v_mov_b32 v77, 0\n"
    "v_mov_b32 v78, 0\n"
    "v_mov_b32 v79, 0\n"
    "ds_read_b128 v[80:83], v134 offset:0\n"
    "ds_read_b128 v[84:87], v134 offset:1024\n"
    "ds_read_b128 v[88:91], v134 offset:2048\n"
    "ds_read_b128 v[92:95], v134 offset:3072\n"
    "ds_read_b128 v[96:99], v134 offset:4096\n"
    "ds_read_b128 v[100:103], v134 offset:5120\n"
    "s_add_u32 s49, s47, 3\n"
    "s_and_b32 s49, s49, 3\n"
    "s_mul_i32 s54, s49, 131072\n"
    "s_add_u32 s54, s54, 786432\n"
    "s_cmp_eq_u32 s45, 1\n"
    "s_cselect_b32 s54, s54, 0x40000000\n"
    "s_waitcnt vmcnt(0)\n"
    "s_waitcnt lgkmcnt(0)\n"
    "s_barrier\n"
    "ds_read_b128 v[16:19], v0 offset:4096\n"
    "ds_read_b128 v[20:23], v0 offset:5120\n"
    "ds_read_b128 v[24:27], v0 offset:6144\n"
    "ds_read_b128 v[28:31], v0 offset:7168\n"
    "ds_read_b128 v[32:35], v0 offset:8192\n"
    "ds_read_b128 v[36:39], v0 offset:9216\n"
    "ds_read_b128 v[40:43], v0 offset:10240\n"
    "ds_read_b128 v[44:47], v0 offset:11264\n"
    "ds_read_b128 v[48:51], v0 offset:12288\n"
    "ds_read_b128 v[52:55], v0 offset:13312\n"
    "s_waitcnt lgkmcnt(9)\n"
    "v_smfmac_f32_16x16x64_bf16 v[56:59], v[16:19], v[80:87], v1\n"
    "ds_read_b128 v[80:83], v134 offset:6144\n"
    "v_smfmac_f32_16x16x64_bf16 v[60:63], v[16:19], a[0:7], v1\n"
    "ds_read_b128 v[84:87], v134 offset:7168\n"
    "v_smfmac_f32_16x16x64_bf16 v[64:67], v[16:19], a[8:15], v1\n"
    "buffer_store_dwordx4 v[128:131], v3, s[40:43], s54 offen sc1\n"
    "v_smfmac_f32_16x16x64_bf16 v[68:71], v[16:19], v[88:95], v1\n"
    "ds_read_b128 v[88:91], v134 offset:8192\n"
    "v_smfmac_f32_16x16x64_bf16 v[72:75], v[16:19], a[16:23], v1\n"
    "ds_read_b128 v[92:95], v134 offset:9216\n"
    "v_smfmac_f32_16x16x64_bf16 v[76:79], v[16:19], a[24:31], v1\n"
    "buffer_atomic_add v133, v132, s[40:43], s64 offen\n"
    "s_waitcnt lgkmcnt(12)\n"
    "v_smfmac_f32_16x16x64_bf16 v[56:59], v[20:23], v[96:103], v1\n"
    "ds_read_b128 v[96:99], v134 offset:10240\n"
    "v_smfmac_f32_16x16x64_bf16 v[60:63], v[20:23], a[32:39], v1\n"
    "ds_read_b128 v[100:103], v134 offset:11264\n"
    "v_smfmac_f32_16x16x64_bf16 v[64:67], v[20:23], a[40:47], v1\n"
    "buffer_store_dwordx4 v[104:107], v0, s[40:43], s62 offen offset:0 sc1\n"
    "s_waitcnt lgkmcnt(4)\n"
    "v_smfmac_f32_16x16x64_bf16 v[68:71], v[20:23], v[80:87], v1\n"
    "ds_read_b128 v[80:83], v134 offset:12288\n"
    "v_smfmac_f32_16x16x64_bf16 v[72:75], v[20:23], a[48:55], v1\n"
    "ds_read_b128 v[84:87], v134 offset:13312\n"
    "v_smfmac_f32_16x16x64_bf16 v[76:79], v[20:23], a[56:63], v1\n"
    "buffer_store_dwordx4 v[108:111], v0, s[40:43], s62 offen offset:1024 sc1\n"
    "s_waitcnt lgkmcnt(4)\n"
    "v_smfmac_f32_16x16x64_bf16 v[56:59], v[24:27], v[88:95], v1\n"
    "ds_read_b128 v[88:91], v134 offset:14336\n"
    "v_smfmac_f32_16x16x64_bf16 v[60:63], v[24:27], a[64:71], v1\n"
    "ds_read_b128 v[92:95], v134 offset:15360\n"
    "v_smfmac_f32_16x16x64_bf16 v[64:67], v[24:27], a[72:79], v1\n"
    "buffer_store_dwordx4 v[112:115], v0, s[40:43], s62 offen offset:2048 sc1\n"
    "s_waitcnt lgkmcnt(4)\n"
    "v_smfmac_f32_16x16x64_bf16 v[68:71], v[24:27], v[96:103], v1\n"
    "ds_read_b128 v[96:99], v134 offset:16384\n"
    "v_smfmac_f32_16x16x64_bf16 v[72:75], v[24:27], a[80:87], v1\n"
    "ds_read_b128 v[100:103], v134 offset:17408\n"
    "v_smfmac_f32_16x16x64_bf16 v[76:79], v[24:27], a[88:95], v1\n"
    "buffer_store_dwordx4 v[116:119], v0, s[40:43], s62 offen offset:3072 sc1\n"
    "s_waitcnt lgkmcnt(4)\n"
    "v_smfmac_f32_16x16x64_bf16 v[56:59], v[28:31], v[80:87], v1\n"
    "ds_read_b128 v[80:83], v134 offset:18432\n"
    "v_smfmac_f32_16x16x64_bf16 v[60:63], v[28:31], a[96:103], v1\n"
    "ds_read_b128 v[84:87], v134 offset:19456\n"
    "v_smfmac_f32_16x16x64_bf16 v[64:67], v[28:31], a[104:111], v1\n"
    "s_waitcnt lgkmcnt(4)\n"
    "v_smfmac_f32_16x16x64_bf16 v[68:71], v[28:31], v[88:95], v1\n"
    "ds_read_b128 v[88:91], v134 offset:20480\n"
    "v_smfmac_f32_16x16x64_bf16 v[72:75], v[28:31], a[112:119], v1\n"
    "ds_read_b128 v[92:95], v134 offset:21504\n"
    "v_smfmac_f32_16x16x64_bf16 v[76:79], v[28:31], a[120:127], v1\n"
    "s_waitcnt lgkmcnt(4)\n"
    "v_smfmac_f32_16x16x64_bf16 v[56:59], v[32:35], v[96:103], v1\n"
    "ds_read_b128 v[96:99], v134 offset:22528\n"
    "v_smfmac_f32_16x16x64_bf16 v[60:63], v[32:35], a[128:135], v1\n"
    "ds_read_b128 v[100:103], v134 offset:23552\n"
    "v_smfmac_f32_16x16x64_bf16 v[64:67], v[32:35], a[136:143], v1\n"
    "s_waitcnt lgkmcnt(4)\n"
    "v_smfmac_f32_16x16x64_bf16 v[68:71], v[32:35], v[80:87], v1\n"
    "ds_read_b128 v[80:83], v134 offset:24576\n"
    "v_smfmac_f32_16x16x64_bf16 v[72:75], v[32:35], a[144:151], v1\n"
    "ds_read_b128 v[84:87], v134 offset:25600\n"
    "v_smfmac_f32_16x16x64_bf16 v[76:79], v[32:35], a[152:159], v1\n"
    "s_waitcnt lgkmcnt(4)\n"
    "v_smfmac_f32_16x16x64_bf16 v[56:59], v[36:39], v[88:95], v1\n"
    "ds_read_b128 v[88:91], v134 offset:26624\n"
    "v_smfmac_f32_16x16x64_bf16 v[60:63], v[36:39], a[160:167], v1\n"
    "ds_read_b128 v[92:95], v134 offset:27648\n"
    "v_smfmac_f32_16x16x64_bf16 v[64:67], v[36:39], a[168:175], v1\n"
    "s_waitcnt lgkmcnt(4)\n"
    "v_smfmac_f32_16x16x64_bf16 v[68:71], v[36:39], v[96:103], v1\n"
    "ds_read_b128 v[96:99], v134 offset:28672\n"
    "v_smfmac_f32_16x16x64_bf16 v[72:75], v[36:39], a[176:183], v1\n"
    "ds_read_b128 v[100:103], v134 offset:29696\n"
    "v_smfmac_f32_16x16x64_bf16 v[76:79], v[36:39], a[184:191], v1\n"
    "s_waitcnt lgkmcnt(4)\n"
    "v_smfmac_f32_16x16x64_bf16 v[56:59], v[40:43], v[80:87], v1\n"
    "ds_read_b128 v[80:83], v134 offset:30720\n"
    "v_smfmac_f32_16x16x64_bf16 v[60:63], v[40:43], a[192:199], v1\n"
    "ds_read_b128 v[84:87], v134 offset:31744\n"
    "v_smfmac_f32_16x16x64_bf16 v[64:67], v[40:43], a[200:207], v1\n"
    "v_smfmac_f32_16x16x64_bf16 v[68:71], v[40:43], a[208:215], v1\n"
    "v_smfmac_f32_16x16x64_bf16 v[72:75], v[40:43], a[216:223], v1\n"
    "v_smfmac_f32_16x16x64_bf16 v[76:79], v[40:43], a[224:231], v1\n"
    "s_waitcnt lgkmcnt(4)\n"
    "v_smfmac_f32_16x16x64_bf16 v[56:59], v[44:47], v[88:95], v1\n"
    "v_smfmac_f32_16x16x64_bf16 v[60:63], v[44:47], a[232:239], v1\n"
    "v_smfmac_f32_16x16x64_bf16 v[64:67], v[44:47], a[240:247], v1\n"
    "v_smfmac_f32_16x16x64_bf16 v[68:71], v[44:47], a[248:255], v1\n"
    "v_smfmac_f32_16x16x64_bf16 v[72:75], v[44:47], v[160:167], v1\n"
    "v_smfmac_f32_16x16x64_bf16 v[76:79], v[44:47], v[168:175], v1\n"
    "s_waitcnt lgkmcnt(2)\n"
    "v_smfmac_f32_16x16x64_bf16 v[56:59], v[48:51], v[96:103], v1\n"
    "v_smfmac_f32_16x16x64_bf16 v[60:63], v[48:51], v[176:183], v1\n"
    "v_smfmac_f32_16x16x64_bf16 v[64:67], v[48:51], v[184:191], v1\n"
    "v_smfmac_f32_16x16x64_bf16 v[68:71], v[48:51], v[192:199], v1\n"
    "v_smfmac_f32_16x16x64_bf16 v[72:75], v[48:51], v[200:207], v1\n"
    "v_smfmac_f32_16x16x64_bf16 v[76:79], v[48:51], v[208:215], v1\n"
    "s_waitcnt lgkmcnt(0)\n"
    "v_smfmac_f32_16x16x64_bf16 v[56:59], v[52:55], v[80:87], v1\n"
    "v_smfmac_f32_16x16x64_bf16 v[60:63], v[52:55], v[216:223], v1\n"
    "v_smfmac_f32_16x16x64_bf16 v[64:67], v[52:55], v[224:231], v1\n"
    "v_smfmac_f32_16x16x64_bf16 v[68:71], v[52:55], v[232:239], v1\n"
    "v_smfmac_f32_16x16x64_bf16 v[72:75], v[52:55], v[240:247], v1\n"
    "v_smfmac_f32_16x16x64_bf16 v[76:79], v[52:55], v[248:255], v1\n"
    "v_mov_b32 v11, v11\n"
    "v_mov_b32 v11, v11\n"
    "v_mov_b32 v11, v11\n"
    "v_mov_b32 v11, v11\n"
    "v_permlane32_swap_b32 v56, v60\n"
    "v_permlane32_swap_b32 v57, v61\n"
    "v_permlane32_swap_b32 v58, v62\n"
    "v_permlane32_swap_b32 v59, v63\n"
    "v_pk_add_f32 v[56:57], v[56:57], v[60:61]\n"
    "v_pk_add_f32 v[58:59], v[58:59], v[62:63]\n"
    "v_cvt_pk_bf16_f32 v60, v56, v57\n"
    "v_cvt_pk_bf16_f32 v61, v58, v59\n"
    "ds_write_b64 v9, v[60:61] offset:0\n"
    "v_permlane32_swap_b32 v64, v68\n"
    "v_permlane32_swap_b32 v65, v69\n"
    "v_permlane32_swap_b32 v66, v70\n"
    "v_permlane32_swap_b32 v67, v71\n"
    "v_pk_add_f32 v[64:65], v[64:65], v[68:69]\n"
    "v_pk_add_f32 v[66:67], v[66:67], v[70:71]\n"
    "v_cvt_pk_bf16_f32 v68, v64, v65\n"
    "v_cvt_pk_bf16_f32 v69, v66, v67\n"
    "ds_write_b64 v9, v[68:69] offset:512\n"
    "v_permlane32_swap_b32 v72, v76\n"
    "v_permlane32_swap_b32 v73, v77\n"
    "v_permlane32_swap_b32 v74, v78\n"
    "v_permlane32_swap_b32 v75, v79\n"
    "v_pk_add_f32 v[72:73], v[72:73], v[76:77]\n"
    "v_pk_add_f32 v[74:75], v[74:75], v[78:79]\n"
    "v_cvt_pk_bf16_f32 v76, v72, v73\n"
    "v_cvt_pk_bf16_f32 v77, v74, v75\n"
    "ds_write_b64 v9, v[76:77] offset:1024\n"
    "s_waitcnt lgkmcnt(0)\n"
    "s_barrier\n"
    "ds_read_b128 v[104:107], v0 offset:0\n"
    "ds_read_b128 v[108:111], v0 offset:1024\n"
    "ds_read_b128 v[112:115], v0 offset:2048\n"
    "ds_read_b128 v[116:119], v0 offset:3072\n"
    "s_and_b32 s62, s47, 31\n"
    "s_mul_i32 s62, s62, 80\n"
    "s_add_u32 s62, s62, s44\n"
    "s_lshl_b32 s62, s62, 12\n"
    "s_add_u32 s62, s62, 116850688\n"
    "s_cmp_eq_u32 s45, 2\n"
    "s_cselect_b32 s62, s62, 0x40000000\n"
    "s_mov_b32 s64, s63\n"
    "s_mul_i32 s63, s47, 1536\n"
    "s_add_u32 s63, s63, s65\n"
    "s_waitcnt lgkmcnt(0)\n"
    "s_add_u32 s47, s47, 1\n"
    "s_and_b32 s59, s47, 63\n"
    "s_cbranch_scc1 cw_loopb_%=\n"
    "buffer_store_dwordx4 v[104:107], v0, s[40:43], s62 offen offset:0 sc1\n"
    "buffer_store_dwordx4 v[108:111], v0, s[40:43], s62 offen offset:1024 sc1\n"
    "buffer_store_dwordx4 v[112:115], v0, s[40:43], s62 offen offset:2048 sc1\n"
    "buffer_store_dwordx4 v[116:119], v0, s[40:43], s62 offen offset:3072 sc1\n"
    "s_waitcnt vmcnt(0)\n"
    "buffer_atomic_add v133, v132, s[40:43], s64 offen\n"
    "buffer_atomic_add v133, v132, s[40:43], s63 offen\n"
    "s_mov_b32 s62, 0x40000000\n"
    "s_mov_b32 s63, 0x40000000\n"
    "s_mov_b32 s64, 0x40000000\n"
    "s_cmp_lt_u32 s47, 128\n"
    "s_cbranch_scc1 cw_loopb_%=\n"
    "s_branch cw_end_%=\n"
    "cw_abort_%=:\n"
    "v_mov_b32 v10, 1\n"
    "v_mov_b32 v11, 1024\n"
    "buffer_store_dword v10, v11, s[40:43], s58 offen sc1\n"
    "cw_end_%=:\n"
    "s_endpgm\n"
    :: CHAIN_INPUTS : CHAIN_CLOBBERS);
    __builtin_unreachable();
}
DEV void role_tok(unsigned lo, unsigned hi, int id, int w, int aux, const Tune& tn, const void* wa, const void* wb, int bid) {
    asm volatile(
    "s_mov_b32 s40, %0\n"
    "s_mov_b32 s41, %1\n"
    "s_mov_b32 s42, %2\n"
    "s_mov_b32 s43, %3\n"
    "s_mov_b32 s44, %4\n"
    "s_mov_b32 s45, %5\n"
    "s_mov_b32 s46, %6\n"
    "s_mov_b32 s66, %7\n"
    "s_mov_b32 s67, %8\n"
    "s_mov_b32 s68, %9\n"
    "s_mov_b32 s69, %10\n"
    "s_mov_b32 s70, %11\n"
    "s_mov_b32 s71, %12\n"
    "s_mov_b32 s72, %13\n"
    "s_mov_b32 s73, %14\n"
    "s_mov_b32 s76, %15\n"
    "s_mov_b32 s77, %16\n"
    "s_mov_b32 s78, 7864320\n"
    "s_mov_b32 s79, 0x20000\n"
    "s_mov_b32 s80, %17\n"
    "s_mov_b32 s81, %18\n"
    "s_mov_b32 s82, 7864320\n"
    "s_mov_b32 s83, 0x20000\n"
    "s_mov_b32 s84, %19\n"
    "s_mov_b64 exec, -1\n"
    "v_mbcnt_lo_u32_b32 v15, -1, 0\n"
    "v_mbcnt_hi_u32_b32 v15, -1, v15\n"
    "v_lshlrev_b32 v0, 4, v15\n"
    "s_mov_b32 s58, 2097152\n"
    "s_mov_b32 s92, 0\n"
    "s_lshr_b32 s85, s84, 2\n"
    "s_and_b32 s86, s84, 3\n"
    "v_add_u32 v26, 0, v15\n"
    "v_mul_u32_u24 v27, 2731, v26\n"
    "v_lshrrev_b32 v27, 16, v27\n"
    "v_mul_u32_u24 v16, 11904, v27\n"
    "v_lshl_add_u32 v16, v26, 4, v16\n"
    "v_add_u32 v26, v26, v27\n"
    "s_mul_i32 s48, s45, 3200\n"
    "v_lshl_add_u32 v19, v26, 4, s48\n"
    "v_add_u32 v26, 64, v15\n"
    "v_mul_u32_u24 v27, 2731, v26\n"
    "v_lshrrev_b32 v27, 16, v27\n"
    "v_mul_u32_u24 v17, 11904, v27\n"
    "v_lshl_add_u32 v17, v26, 4, v17\n"
    "v_add_u32 v26, v26, v27\n"
    "s_mul_i32 s48, s45, 3200\n"
    "v_lshl_add_u32 v20, v26, 4, s48\n"
    "v_add_u32 v26, 128, v15\n"
    "v_mul_u32_u24 v27, 2731, v26\n"
    "v_lshrrev_b32 v27, 16, v27\n"
    "v_mul_u32_u24 v18, 11904, v27\n"
    "v_lshl_add_u32 v18, v26, 4, v18\n"
    "v_add_u32 v26, v26, v27\n"
    "s_mul_i32 s48, s45, 3200\n"
    "v_lshl_add_u32 v21, v26, 4, s48\n"
    "s_mul_i32 s48, s86, 160\n"
    "s_lshl3_add_u32 s48, s45, s48\n"
    "s_mul_i32 s48, s48, 12288\n"
    "s_mul_i32 s49, s85, 384\n"
    "s_add_u32 s48, s48, s49\n"
    "buffer_load_dwordx4 v[32:35], v16, s[80:83], s48 offen\n"
    "buffer_load_dwordx4 v[36:39], v17, s[80:83], s48 offen\n"
    "buffer_load_dwordx4 v[40:43], v18, s[80:83], s48 offen\n"
    "s_add_u32 s48, s48, 393216\n"
    "buffer_load_dwordx4 v[44:47], v16, s[80:83], s48 offen\n"
    "buffer_load_dwordx4 v[48:51], v17, s[80:83], s48 offen\n"
    "buffer_load_dwordx4 v[52:55], v18, s[80:83], s48 offen\n"
    "s_add_u32 s48, s48, 393216\n"
    "buffer_load_dwordx4 v[56:59], v16, s[80:83], s48 offen\n"
    "buffer_load_dwordx4 v[60:63], v17, s[80:83], s48 offen\n"
    "buffer_load_dwordx4 v[64:67], v18, s[80:83], s48 offen\n"
    "s_add_u32 s48, s48, 393216\n"
    "buffer_load_dwordx4 v[68:71], v16, s[80:83], s48 offen\n"
    "buffer_load_dwordx4 v[72:75], v17, s[80:83], s48 offen\n"
    "buffer_load_dwordx4 v[76:79], v18, s[80:83], s48 offen\n"
    "s_add_u32 s48, s48, 393216\n"
    "buffer_load_dwordx4 v[80:83], v16, s[80:83], s48 offen\n"
    "buffer_load_dwordx4 v[84:87], v17, s[80:83], s48 offen\n"
    "buffer_load_dwordx4 v[88:91], v18, s[80:83], s48 offen\n"
    "s_add_u32 s48, s48, 393216\n"
    "v_lshrrev_b32 v28, 2, v15\n"
    "v_mul_u32_u24 v28, 1280, v28\n"
    "v_and_b32 v29, 3, v15\n"
    "v_lshl_add_u32 v28, v29, 3, v28\n"
    "s_mul_i32 s48, s45, 320\n"
    "v_add_u32 v28, s48, v28\n"
    "v_add_u32 v29, 40960, v28\n"
    "v_add_u32 v30, 81920, v28\n"
    "s_mul_i32 s51, s45, 10240\n"
    "s_mul_i32 s50, s85, 245760\n"
    "s_add_u32 s50, s50, s51\n"
    "s_add_u32 m0, s51, 0\n"
    "s_add_u32 s49, s50, 0\n"
    "buffer_load_dwordx4 v0, s[76:79], s49 offen offset:0 lds\n"
    "buffer_load_dwordx4 v0, s[76:79], s49 offen offset:1024 lds\n"
    "buffer_load_dwordx4 v0, s[76:79], s49 offen offset:2048 lds\n"
    "buffer_load_dwordx4 v0, s[76:79], s49 offen offset:3072 lds\n"
    "s_add_u32 m0, s51, 4096\n"
    "s_add_u32 s49, s50, 4096\n"
    "buffer_load_dwordx4 v0, s[76:79], s49 offen offset:0 lds\n"
    "buffer_load_dwordx4 v0, s[76:79], s49 offen offset:1024 lds\n"
    "buffer_load_dwordx4 v0, s[76:79], s49 offen offset:2048 lds\n"
    "buffer_load_dwordx4 v0, s[76:79], s49 offen offset:3072 lds\n"
    "s_add_u32 m0, s51, 8192\n"
    "s_add_u32 s49, s50, 8192\n"
    "buffer_load_dwordx4 v0, s[76:79], s49 offen offset:0 lds\n"
    "buffer_load_dwordx4 v0, s[76:79], s49 offen offset:1024 lds\n"
    "s_add_u32 m0, s51, 40960\n"
    "s_add_u32 s49, s50, 40960\n"
    "buffer_load_dwordx4 v0, s[76:79], s49 offen offset:0 lds\n"
    "buffer_load_dwordx4 v0, s[76:79], s49 offen offset:1024 lds\n"
    "buffer_load_dwordx4 v0, s[76:79], s49 offen offset:2048 lds\n"
    "buffer_load_dwordx4 v0, s[76:79], s49 offen offset:3072 lds\n"
    "s_add_u32 m0, s51, 45056\n"
    "s_add_u32 s49, s50, 45056\n"
    "buffer_load_dwordx4 v0, s[76:79], s49 offen offset:0 lds\n"
    "buffer_load_dwordx4 v0, s[76:79], s49 offen offset:1024 lds\n"
    "buffer_load_dwordx4 v0, s[76:79], s49 offen offset:2048 lds\n"
    "buffer_load_dwordx4 v0, s[76:79], s49 offen offset:3072 lds\n"
    "s_add_u32 m0, s51, 49152\n"
    "s_add_u32 s49, s50, 49152\n"
    "buffer_load_dwordx4 v0, s[76:79], s49 offen offset:0 lds\n"
    "buffer_load_dwordx4 v0, s[76:79], s49 offen offset:1024 lds\n"
    "s_waitcnt vmcnt(10) lgkmcnt(0)\n"
    "s_barrier\n"
    "s_add_u32 m0, s51, 81920\n"
    "s_add_u32 s49, s50, 81920\n"
    "buffer_load_dwordx4 v0, s[76:79], s49 offen offset:0 lds\n"
    "buffer_load_dwordx4 v0, s[76:79], s49 offen offset:1024 lds\n"
    "buffer_load_dwordx4 v0, s[76:79], s49 offen offset:2048 lds\n"
    "buffer_load_dwordx4 v0, s[76:79], s49 offen offset:3072 lds\n"
    "s_add_u32 m0, s51, 86016\n"
    "s_add_u32 s49, s50, 86016\n"
    "buffer_load_dwordx4 v0, s[76:79], s49 offen offset:0 lds\n"
    "buffer_load_dwordx4 v0, s[76:79], s49 offen offset:1024 lds\n"
    "buffer_load_dwordx4 v0, s[76:79], s49 offen offset:2048 lds\n"
    "buffer_load_dwordx4 v0, s[76:79], s49 offen offset:3072 lds\n"
    "s_add_u32 m0, s51, 90112\n"
    "s_add_u32 s49, s50, 90112\n"
    "buffer_load_dwordx4 v0, s[76:79], s49 offen offset:0 lds\n"
    "buffer_load_dwordx4 v0, s[76:79], s49 offen offset:1024 lds\n"
    "ds_read_b64_tr_b16 a[0:1], v28 offset:0\n"
    "ds_read_b64_tr_b16 a[2:3], v28 offset:20480\n"
    "ds_read_b64_tr_b16 a[24:25], v28 offset:32\n"
    "ds_read_b64_tr_b16 a[26:27], v28 offset:20512\n"
    "ds_read_b64_tr_b16 a[48:49], v28 offset:64\n"
    "ds_read_b64_tr_b16 a[50:51], v28 offset:20544\n"
    "ds_read_b64_tr_b16 a[72:73], v28 offset:96\n"
    "ds_read_b64_tr_b16 a[74:75], v28 offset:20576\n"
    "ds_read_b64_tr_b16 a[96:97], v28 offset:128\n"
    "ds_read_b64_tr_b16 a[98:99], v28 offset:20608\n"
    "ds_read_b64_tr_b16 a[120:121], v28 offset:160\n"
    "ds_read_b64_tr_b16 a[122:123], v28 offset:20640\n"
    "ds_read_b64_tr_b16 a[144:145], v28 offset:192\n"
    "ds_read_b64_tr_b16 a[146:147], v28 offset:20672\n"
    "ds_read_b64_tr_b16 a[168:169], v28 offset:224\n"
    "ds_read_b64_tr_b16 a[170:171], v28 offset:20704\n"
    "s_waitcnt lgkmcnt(14)\n"
    "ds_read_b64_tr_b16 a[192:193], v28 offset:256\n"
    "ds_read_b64_tr_b16 a[194:195], v28 offset:20736\n"
    "s_waitcnt lgkmcnt(14)\n"
    "ds_read_b64_tr_b16 a[216:217], v28 offset:288\n"
    "ds_read_b64_tr_b16 a[218:219], v28 offset:20768\n"
    "s_waitcnt lgkmcnt(14)\n"
    "s_waitcnt vmcnt(10) lgkmcnt(0)\n"
    "s_barrier\n"
    "s_add_u32 m0, s51, 0\n"
    "s_add_u32 s49, s50, 122880\n"
    "buffer_load_dwordx4 v0, s[76:79], s49 offen offset:0 lds\n"
    "buffer_load_dwordx4 v0, s[76:79], s49 offen offset:1024 lds\n"
    "buffer_load_dwordx4 v0, s[76:79], s49 offen offset:2048 lds\n"
    "buffer_load_dwordx4 v0, s[76:79], s49 offen offset:3072 lds\n"
    "s_add_u32 m0, s51, 4096\n"
    "s_add_u32 s49, s50, 126976\n"
    "buffer_load_dwordx4 v0, s[76:79], s49 offen offset:0 lds\n"
    "buffer_load_dwordx4 v0, s[76:79], s49 offen offset:1024 lds\n"
    "buffer_load_dwordx4 v0, s[76:79], s49 offen offset:2048 lds\n"
    "buffer_load_dwordx4 v0, s[76:79], s49 offen offset:3072 lds\n"
    "s_add_u32 m0, s51, 8192\n"
    "s_add_u32 s49, s50, 131072\n"
    "buffer_load_dwordx4 v0, s[76:79], s49 offen offset:0 lds\n"
    "buffer_load_dwordx4 v0, s[76:79], s49 offen offset:1024 lds\n"
    "ds_read_b64_tr_b16 a[4:5], v29 offset:0\n"
    "ds_read_b64_tr_b16 a[6:7], v29 offset:20480\n"
    "ds_read_b64_tr_b16 a[28:29], v29 offset:32\n"
    "ds_read_b64_tr_b16 a[30:31], v29 offset:20512\n"
    "ds_read_b64_tr_b16 a[52:53], v29 offset:64\n"
    "ds_read_b64_tr_b16 a[54:55], v29 offset:20544\n"
    "ds_read_b64_tr_b16 a[76:77], v29 offset:96\n"
    "ds_read_b64_tr_b16 a[78:79], v29 offset:20576\n"
    "ds_read_b64_tr_b16 a[100:101], v29 offset:128\n"
    "ds_read_b64_tr_b16 a[102:103], v29 offset:20608\n"
    "ds_read_b64_tr_b16 a[124:125], v29 offset:160\n"
    "ds_read_b64_tr_b16 a[126:127], v29 offset:20640\n"
    "ds_read_b64_tr_b16 a[148:149], v29 offset:192\n"
    "ds_read_b64_tr_b16 a[150:151], v29 offset:20672\n"
    "ds_read_b64_tr_b16 a[172:173], v29 offset:224\n"
    "ds_read_b64_tr_b16 a[174:175], v29 offset:20704\n"
    "s_waitcnt lgkmcnt(14)\n"
    "ds_read_b64_tr_b16 a[196:197], v29 offset:256\n"
    "ds_read_b64_tr_b16 a[198:199], v29 offset:20736\n"
    "s_waitcnt lgkmcnt(14)\n"
    "ds_read_b64_tr_b16 a[220:221], v29 offset:288\n"
    "ds_read_b64_tr_b16 a[222:223], v29 offset:20768\n"
    "s_waitcnt lgkmcnt(14)\n"
    "s_waitcnt vmcnt(10) lgkmcnt(0)\n"
    "s_barrier\n"
    "s_add_u32 m0, s51, 40960\n"
    "s_add_u32 s49, s50, 163840\n"
    "buffer_load_dwordx4 v0, s[76:79], s49 offen offset:0 lds\n"
    "buffer_load_dwordx4 v0, s[76:79], s49 offen offset:1024 lds\n"
    "buffer_load_dwordx4 v0, s[76:79], s49 offen offset:2048 lds\n"
    "buffer_load_dwordx4 v0, s[76:79], s49 offen offset:3072 lds\n"
    "s_add_u32 m0, s51, 45056\n"
    "s_add_u32 s49, s50, 167936\n"
    "buffer_load_dwordx4 v0, s[76:79], s49 offen offset:0 lds\n"
    "buffer_load_dwordx4 v0, s[76:79], s49 offen offset:1024 lds\n"
    "buffer_load_dwordx4 v0, s[76:79], s49 offen offset:2048 lds\n"
    "buffer_load_dwordx4 v0, s[76:79], s49 offen offset:3072 lds\n"
    "s_add_u32 m0, s51, 49152\n"
    "s_add_u32 s49, s50, 172032\n"
    "buffer_load_dwordx4 v0, s[76:79], s49 offen offset:0 lds\n"
    "buffer_load_dwordx4 v0, s[76:79], s49 offen offset:1024 lds\n"
    "ds_read_b64_tr_b16 a[8:9], v30 offset:0\n"
    "ds_read_b64_tr_b16 a[10:11], v30 offset:20480\n"
    "ds_read_b64_tr_b16 a[32:33], v30 offset:32\n"
    "ds_read_b64_tr_b16 a[34:35], v30 offset:20512\n"
    "ds_read_b64_tr_b16 a[56:57], v30 offset:64\n"
    "ds_read_b64_tr_b16 a[58:59], v30 offset:20544\n"
    "ds_read_b64_tr_b16 a[80:81], v30 offset:96\n"
    "ds_read_b64_tr_b16 a[82:83], v30 offset:20576\n"
    "ds_read_b64_tr_b16 a[104:105], v30 offset:128\n"
    "ds_read_b64_tr_b16 a[106:107], v30 offset:20608\n"
    "ds_read_b64_tr_b16 a[128:129], v30 offset:160\n"
    "ds_read_b64_tr_b16 a[130:131], v30 offset:20640\n"
    "ds_read_b64_tr_b16 a[152:153], v30 offset:192\n"
    "ds_read_b64_tr_b16 a[154:155], v30 offset:20672\n"
    "ds_read_b64_tr_b16 a[176:177], v30 offset:224\n"
    "ds_read_b64_tr_b16 a[178:179], v30 offset:20704\n"
    "s_waitcnt lgkmcnt(14)\n"
    "ds_read_b64_tr_b16 a[200:201], v30 offset:256\n"
    "ds_read_b64_tr_b16 a[202:203], v30 offset:20736\n"
    "s_waitcnt lgkmcnt(14)\n"
    "ds_read_b64_tr_b16 a[224:225], v30 offset:288\n"
    "ds_read_b64_tr_b16 a[226:227], v30 offset:20768\n"
    "s_waitcnt lgkmcnt(14)\n"
    "s_waitcnt vmcnt(10) lgkmcnt(0)\n"
    "s_barrier\n"
    "s_add_u32 m0, s51, 81920\n"
    "s_add_u32 s49, s50, 204800\n"
    "buffer_load_dwordx4 v0, s[76:79], s49 offen offset:0 lds\n"
    "buffer_load_dwordx4 v0, s[76:79], s49 offen offset:1024 lds\n"
    "buffer_load_dwordx4 v0, s[76:79], s49 offen offset:2048 lds\n"
    "buffer_load_dwordx4 v0, s[76:79], s49 offen offset:3072 lds\n"
    "s_add_u32 m0, s51, 86016\n"
    "s_add_u32 s49, s50, 208896\n"
    "buffer_load_dwordx4 v0, s[76:79], s49 offen offset:0 lds\n"
    "buffer_load_dwordx4 v0, s[76:79], s49 offen offset:1024 lds\n"
    "buffer_load_dwordx4 v0, s[76:79], s49 offen offset:2048 lds\n"
    "buffer_load_dwordx4 v0, s[76:79], s49 offen offset:3072 lds\n"
    "s_add_u32 m0, s51, 90112\n"
    "s_add_u32 s49, s50, 212992\n"
    "buffer_load_dwordx4 v0, s[76:79], s49 offen offset:0 lds\n"
    "buffer_load_dwordx4 v0, s[76:79], s49 offen offset:1024 lds\n"
    "ds_read_b64_tr_b16 a[12:13], v28 offset:0\n"
    "ds_read_b64_tr_b16 a[14:15], v28 offset:20480\n"
    "ds_read_b64_tr_b16 a[36:37], v28 offset:32\n"
    "ds_read_b64_tr_b16 a[38:39], v28 offset:20512\n"
    "ds_read_b64_tr_b16 a[60:61], v28 offset:64\n"
    "ds_read_b64_tr_b16 a[62:63], v28 offset:20544\n"
    "ds_read_b64_tr_b16 a[84:85], v28 offset:96\n"
    "ds_read_b64_tr_b16 a[86:87], v28 offset:20576\n"
    "ds_read_b64_tr_b16 a[108:109], v28 offset:128\n"
    "ds_read_b64_tr_b16 a[110:111], v28 offset:20608\n"
    "ds_read_b64_tr_b16 a[132:133], v28 offset:160\n"
    "ds_read_b64_tr_b16 a[134:135], v28 offset:20640\n"
    "ds_read_b64_tr_b16 a[156:157], v28 offset:192\n"
    "ds_read_b64_tr_b16 a[158:159], v28 offset:20672\n"
    "ds_read_b64_tr_b16 a[180:181], v28 offset:224\n"
    "ds_read_b64_tr_b16 a[182:183], v28 offset:20704\n"
    "s_waitcnt lgkmcnt(14)\n"
    "ds_read_b64_tr_b16 a[204:205], v28 offset:256\n"
    "ds_read_b64_tr_b16 a[206:207], v28 offset:20736\n"
    "s_waitcnt lgkmcnt(14)\n"
    "ds_read_b64_tr_b16 a[228:229], v28 offset:288\n"
    "ds_read_b64_tr_b16 a[230:231], v28 offset:20768\n"
    "s_waitcnt lgkmcnt(14)\n"
    "s_waitcnt vmcnt(10) lgkmcnt(0)\n"
    "s_barrier\n"
    "ds_read_b64_tr_b16 a[16:17], v29 offset:0\n"
    "ds_read_b64_tr_b16 a[18:19], v29 offset:20480\n"
    "ds_read_b64_tr_b16 a[40:41], v29 offset:32\n"
    "ds_read_b64_tr_b16 a[42:43], v29 offset:20512\n"
    "ds_read_b64_tr_b16 a[64:65], v29 offset:64\n"
    "ds_read_b64_tr_b16 a[66:67], v29 offset:20544\n"
    "ds_read_b64_tr_b16 a[88:89], v29 offset:96\n"
    "ds_read_b64_tr_b16 a[90:91], v29 offset:20576\n"
    "ds_read_b64_tr_b16 a[112:113], v29 offset:128\n"
    "ds_read_b64_tr_b16 a[114:115], v29 offset:20608\n"
    "ds_read_b64_tr_b16 a[136:137], v29 offset:160\n"
    "ds_read_b64_tr_b16 a[138:139], v29 offset:20640\n"
    "ds_read_b64_tr_b16 a[160:161], v29 offset:192\n"
    "ds_read_b64_tr_b16 a[162:163], v29 offset:20672\n"
    "ds_read_b64_tr_b16 a[184:185], v29 offset:224\n"
    "ds_read_b64_tr_b16 a[186:187], v29 offset:20704\n"
    "s_waitcnt lgkmcnt(14)\n"
    "ds_read_b64_tr_b16 a[208:209], v29 offset:256\n"
    "ds_read_b64_tr_b16 a[210:211], v29 offset:20736\n"
    "s_waitcnt lgkmcnt(14)\n"
    "ds_read_b64_tr_b16 a[232:233], v29 offset:288\n"
    "ds_read_b64_tr_b16 a[234:235], v29 offset:20768\n"
    "s_waitcnt lgkmcnt(14)\n"
    "s_waitcnt vmcnt(0) lgkmcnt(0)\n"
    "s_barrier\n"
    "ds_read_b64_tr_b16 a[20:21], v30 offset:0\n"
    "ds_read_b64_tr_b16 a[22:23], v30 offset:20480\n"
    "ds_read_b64_tr_b16 a[44:45], v30 offset:32\n"
    "ds_read_b64_tr_b16 a[46:47], v30 offset:20512\n"
    "ds_read_b64_tr_b16 a[68:69], v30 offset:64\n"
    "ds_read_b64_tr_b16 a[70:71], v30 offset:20544\n"
    "ds_read_b64_tr_b16 a[92:93], v30 offset:96\n"
    "ds_read_b64_tr_b16 a[94:95], v30 offset:20576\n"
    "ds_read_b64_tr_b16 a[116:117], v30 offset:128\n"
    "ds_read_b64_tr_b16 a[118:119], v30 offset:20608\n"
    "ds_read_b64_tr_b16 a[140:141], v30 offset:160\n"
    "ds_read_b64_tr_b16 a[142:143], v30 offset:20640\n"
    "ds_read_b64_tr_b16 a[164:165], v30 offset:192\n"
    "ds_read_b64_tr_b16 a[166:167], v30 offset:20672\n"
    "ds_read_b64_tr_b16 a[188:189], v30 offset:224\n"
    "ds_read_b64_tr_b16 a[190:191], v30 offset:20704\n"
    "s_waitcnt lgkmcnt(14)\n"
    "ds_read_b64_tr_b16 a[212:213], v30 offset:256\n"
    "ds_read_b64_tr_b16 a[214:215], v30 offset:20736\n"
    "s_waitcnt lgkmcnt(14)\n"
    "ds_read_b64_tr_b16 a[236:237], v30 offset:288\n"
    "ds_read_b64_tr_b16 a[238:239], v30 offset:20768\n"
    "s_waitcnt lgkmcnt(14)\n"
    "s_waitcnt lgkmcnt(0)\n"
    "s_barrier\n"
    "ds_write_b128 v19, v[32:35] offset:0\n"
    "ds_write_b128 v20, v[36:39] offset:0\n"
    "ds_write_b128 v21, v[40:43] offset:0\n"
    "ds_write_b128 v19, v[44:47] offset:12800\n"
    "ds_write_b128 v20, v[48:51] offset:12800\n"
    "ds_write_b128 v21, v[52:55] offset:12800\n"
    "ds_write_b128 v19, v[56:59] offset:25600\n"
    "ds_write_b128 v20, v[60:63] offset:25600\n"
    "ds_write_b128 v21, v[64:67] offset:25600\n"
    "ds_write_b128 v19, v[68:71] offset:38400\n"
    "ds_write_b128 v20, v[72:75] offset:38400\n"
    "ds_write_b128 v21, v[76:79] offset:38400\n"
    "ds_write_b128 v19, v[80:83] offset:51200\n"
    "ds_write_b128 v20, v[84:87] offset:51200\n"
    "ds_write_b128 v21, v[88:91] offset:51200\n"
    "v_and_b32 v22, 1, v15\n"
    "v_and_b32 v26, 2, v15\n"
    "v_lshl_add_u32 v22, v26, 2, v22\n"
    "v_and_b32 v26, 4, v15\n"
    "v_add_u32 v22, v22, v26\n"
    "v_and_b32 v26, 8, v15\n"
    "v_lshl_add_u32 v22, v26, 1, v22\n"
    "v_mul_u32_u24 v22, 400, v22\n"
    "v_lshrrev_b32 v26, 4, v15\n"
    "v_lshl_add_u32 v22, v26, 3, v22\n"
    "v_bfe_u32 v26, v15, 3, 1\n"
    "v_mul_u32_u24 v26, 655360, v26\n"
    "v_and_b32 v24, 0x370, v0\n"
    "v_add_u32 v24, v24, v26\n"
    "s_mul_i32 s48, s45, 13107200\n"
    "s_add_u32 s48, s48, 64421888\n"
    "v_add_u32 v24, s48, v24\n"
    "s_lshr_b32 s50, s85, 1\n"
    "s_mul_i32 s50, s50, 20480\n"
    "s_and_b32 s48, s85, 1\n"
    "s_lshl_b32 s48, s48, 7\n"
    "s_add_u32 s50, s50, s48\n"
    "s_mul_i32 s48, s86, 5120\n"
    "s_add_u32 s50, s50, s48\n"
    "s_movk_i32 s52, 0x2000\n"
    "s_mov_b32 s51, 5\n"
    "s_waitcnt lgkmcnt(0)\n"
    "s_barrier\n"
    "tk_bg_%=:\n"
    "ds_read2_b64 v[96:99], v22 offset0:0 offset1:4\n"
    "ds_read2_b64 v[100:103], v22 offset0:100 offset1:104\n"
    "ds_read2_b64 v[104:107], v22 offset0:8 offset1:12\n"
    "ds_read2_b64 v[108:111], v22 offset0:108 offset1:112\n"
    "ds_read2_b64 v[112:115], v22 offset0:16 offset1:20\n"
    "ds_read2_b64 v[116:119], v22 offset0:116 offset1:120\n"
    "ds_read2_b64 v[120:123], v22 offset0:24 offset1:28\n"
    "ds_read2_b64 v[124:127], v22 offset0:124 offset1:128\n"
    "ds_read2_b64 v[128:131], v22 offset0:32 offset1:36\n"
    "ds_read2_b64 v[132:135], v22 offset0:132 offset1:136\n"
    "ds_read2_b64 v[136:139], v22 offset0:40 offset1:44\n"
    "ds_read2_b64 v[140:143], v22 offset0:140 offset1:144\n"
    "v_add_u32 v22, 12800, v22\n"
    "s_and_b32 s53, s50, 0xffffe000\n"
    "s_add_u32 s53, s53, s50\n"
    "s_waitcnt lgkmcnt(11)\n"
    "v_mfma_f32_16x16x32_bf16 v[144:147], v[96:99], a[0:3], 0\n"
    "s_waitcnt lgkmcnt(10)\n"
    "v_mfma_f32_16x16x32_bf16 v[148:151], v[100:103], a[0:3], 0\n"
    "s_waitcnt lgkmcnt(9)\n"
    "v_mfma_f32_16x16x32_bf16 v[144:147], v[104:107], a[4:7], v[144:147]\n"
    "s_waitcnt lgkmcnt(8)\n"
    "v_mfma_f32_16x16x32_bf16 v[148:151], v[108:111], a[4:7], v[148:151]\n"
    "s_waitcnt lgkmcnt(7)\n"
    "v_mfma_f32_16x16x32_bf16 v[144:147], v[112:115], a[8:11], v[144:147]\n"
    "s_waitcnt lgkmcnt(6)\n"
    "v_mfma_f32_16x16x32_bf16 v[148:151], v[116:119], a[8:11], v[148:151]\n"
    "s_waitcnt lgkmcnt(5)\n"
    "v_mfma_f32_16x16x32_bf16 v[144:147], v[120:123], a[12:15], v[144:147]\n"
    "s_waitcnt lgkmcnt(4)\n"
    "v_mfma_f32_16x16x32_bf16 v[148:151], v[124:127], a[12:15], v[148:151]\n"
    "s_waitcnt lgkmcnt(3)\n"
    "v_mfma_f32_16x16x32_bf16 v[144:147], v[128:131], a[16:19], v[144:147]\n"
    "s_waitcnt lgkmcnt(2)\n"
    "v_mfma_f32_16x16x32_bf16 v[148:151], v[132:135], a[16:19], v[148:151]\n"
    "s_waitcnt lgkmcnt(1)\n"
    "v_mfma_f32_16x16x32_bf16 v[144:147], v[136:139], a[20:23], v[144:147]\n"
    "s_waitcnt lgkmcnt(0)\n"
    "v_mfma_f32_16x16x32_bf16 v[148:151], v[140:143], a[20:23], v[148:151]\n"
    "v_mfma_f32_16x16x32_bf16 v[152:155], v[96:99], a[24:27], 0\n"
    "v_add_u32 v25, s53, v24\n"
    "v_mfma_f32_16x16x32_bf16 v[156:159], v[100:103], a[24:27], 0\n"
    "v_lshrrev_b32 v26, 8, v25\n"
    "v_mfma_f32_16x16x32_bf16 v[152:155], v[104:107], a[28:31], v[152:155]\n"
    "v_and_or_b32 v25, v26, s52, v25\n"
    "v_mfma_f32_16x16x32_bf16 v[156:159], v[108:111], a[28:31], v[156:159]\n"
    "s_add_u32 s53, s53, 1310720\n"
    "v_mfma_f32_16x16x32_bf16 v[152:155], v[112:115], a[32:35], v[152:155]\n"
    "v_mfma_f32_16x16x32_bf16 v[156:159], v[116:119], a[32:35], v[156:159]\n"
    "v_mfma_f32_16x16x32_bf16 v[152:155], v[120:123], a[36:39], v[152:155]\n"
    "v_cvt_pk_bf16_f32 v160, v144, v145\n"
    "v_mfma_f32_16x16x32_bf16 v[156:159], v[124:127], a[36:39], v[156:159]\n"
    "v_cvt_pk_bf16_f32 v161, v146, v147\n"
    "v_mfma_f32_16x16x32_bf16 v[152:155], v[128:131], a[40:43], v[152:155]\n"
    "v_cvt_pk_bf16_f32 v162, v148, v149\n"
    "v_mfma_f32_16x16x32_bf16 v[156:159], v[132:135], a[40:43], v[156:159]\n"
    "v_cvt_pk_bf16_f32 v163, v150, v151\n"
    "v_mfma_f32_16x16x32_bf16 v[152:155], v[136:139], a[44:47], v[152:155]\n"
    "buffer_store_dwordx4 v[160:163], v25, s[40:43], 0 offen sc1\n"
    "v_mfma_f32_16x16x32_bf16 v[156:159], v[140:143], a[44:47], v[156:159]\n"
    "v_mfma_f32_16x16x32_bf16 v[144:147], v[96:99], a[48:51], 0\n"
    "v_add_u32 v25, s53, v24\n"
    "v_mfma_f32_16x16x32_bf16 v[148:151], v[100:103], a[48:51], 0\n"
    "v_lshrrev_b32 v26, 8, v25\n"
    "v_mfma_f32_16x16x32_bf16 v[144:147], v[104:107], a[52:55], v[144:147]\n"
    "v_and_or_b32 v25, v26, s52, v25\n"
    "v_mfma_f32_16x16x32_bf16 v[148:151], v[108:111], a[52:55], v[148:151]\n"
    "s_add_u32 s53, s53, 1310720\n"
    "v_mfma_f32_16x16x32_bf16 v[144:147], v[112:115], a[56:59], v[144:147]\n"
    "v_mfma_f32_16x16x32_bf16 v[148:151], v[116:119], a[56:59], v[148:151]\n"
    "v_mfma_f32_16x16x32_bf16 v[144:147], v[120:123], a[60:63], v[144:147]\n"
    "v_cvt_pk_bf16_f32 v164, v152, v153\n"
    "v_mfma_f32_16x16x32_bf16 v[148:151], v[124:127], a[60:63], v[148:151]\n"
    "v_cvt_pk_bf16_f32 v165, v154, v155\n"
    "v_mfma_f32_16x16x32_bf16 v[144:147], v[128:131], a[64:67], v[144:147]\n"
    "v_cvt_pk_bf16_f32 v166, v156, v157\n"
    "v_mfma_f32_16x16x32_bf16 v[148:151], v[132:135], a[64:67], v[148:151]\n"
    "v_cvt_pk_bf16_f32 v167, v158, v159\n"
    "v_mfma_f32_16x16x32_bf16 v[144:147], v[136:139], a[68:71], v[144:147]\n"
    "buffer_store_dwordx4 v[164:167], v25, s[40:43], 0 offen sc1\n"
    "v_mfma_f32_16x16x32_bf16 v[148:151], v[140:143], a[68:71], v[148:151]\n"
    "v_mfma_f32_16x16x32_bf16 v[152:155], v[96:99], a[72:75], 0\n"
    "v_add_u32 v25, s53, v24\n"
    "v_mfma_f32_16x16x32_bf16 v[156:159], v[100:103], a[72:75], 0\n"
    "v_lshrrev_b32 v26, 8, v25\n"
    "v_mfma_f32_16x16x32_bf16 v[152:155], v[104:107], a[76:79], v[152:155]\n"
    "v_and_or_b32 v25, v26, s52, v25\n"
    "v_mfma_f32_16x16x32_bf16 v[156:159], v[108:111], a[76:79], v[156:159]\n"
    "s_add_u32 s53, s53, 1310720\n"
    "v_mfma_f32_16x16x32_bf16 v[152:155], v[112:115], a[80:83], v[152:155]\n"
    "v_mfma_f32_16x16x32_bf16 v[156:159], v[116:119], a[80:83], v[156:159]\n"
    "v_mfma_f32_16x16x32_bf16 v[152:155], v[120:123], a[84:87], v[152:155]\n"
    "v_cvt_pk_bf16_f32 v160, v144, v145\n"
    "v_mfma_f32_16x16x32_bf16 v[156:159], v[124:127], a[84:87], v[156:159]\n"
    "v_cvt_pk_bf16_f32 v161, v146, v147\n"
    "v_mfma_f32_16x16x32_bf16 v[152:155], v[128:131], a[88:91], v[152:155]\n"
    "v_cvt_pk_bf16_f32 v162, v148, v149\n"
    "v_mfma_f32_16x16x32_bf16 v[156:159], v[132:135], a[88:91], v[156:159]\n"
    "v_cvt_pk_bf16_f32 v163, v150, v151\n"
    "v_mfma_f32_16x16x32_bf16 v[152:155], v[136:139], a[92:95], v[152:155]\n"
    "buffer_store_dwordx4 v[160:163], v25, s[40:43], 0 offen sc1\n"
    "v_mfma_f32_16x16x32_bf16 v[156:159], v[140:143], a[92:95], v[156:159]\n"
    "v_mfma_f32_16x16x32_bf16 v[144:147], v[96:99], a[96:99], 0\n"
    "v_add_u32 v25, s53, v24\n"
    "v_mfma_f32_16x16x32_bf16 v[148:151], v[100:103], a[96:99], 0\n"
    "v_lshrrev_b32 v26, 8, v25\n"
    "v_mfma_f32_16x16x32_bf16 v[144:147], v[104:107], a[100:103], v[144:147]\n"
    "v_and_or_b32 v25, v26, s52, v25\n"
    "v_mfma_f32_16x16x32_bf16 v[148:151], v[108:111], a[100:103], v[148:151]\n"
    "s_add_u32 s53, s53, 1310720\n"
    "v_mfma_f32_16x16x32_bf16 v[144:147], v[112:115], a[104:107], v[144:147]\n"
    "v_mfma_f32_16x16x32_bf16 v[148:151], v[116:119], a[104:107], v[148:151]\n"
    "v_mfma_f32_16x16x32_bf16 v[144:147], v[120:123], a[108:111], v[144:147]\n"
    "v_cvt_pk_bf16_f32 v164, v152, v153\n"
    "v_mfma_f32_16x16x32_bf16 v[148:151], v[124:127], a[108:111], v[148:151]\n"
    "v_cvt_pk_bf16_f32 v165, v154, v155\n"
    "v_mfma_f32_16x16x32_bf16 v[144:147], v[128:131], a[112:115], v[144:147]\n"
    "v_cvt_pk_bf16_f32 v166, v156, v157\n"
    "v_mfma_f32_16x16x32_bf16 v[148:151], v[132:135], a[112:115], v[148:151]\n"
    "v_cvt_pk_bf16_f32 v167, v158, v159\n"
    "v_mfma_f32_16x16x32_bf16 v[144:147], v[136:139], a[116:119], v[144:147]\n"
    "buffer_store_dwordx4 v[164:167], v25, s[40:43], 0 offen sc1\n"
    "v_mfma_f32_16x16x32_bf16 v[148:151], v[140:143], a[116:119], v[148:151]\n"
    "v_mfma_f32_16x16x32_bf16 v[152:155], v[96:99], a[120:123], 0\n"
    "v_add_u32 v25, s53, v24\n"
    "v_mfma_f32_16x16x32_bf16 v[156:159], v[100:103], a[120:123], 0\n"
    "v_lshrrev_b32 v26, 8, v25\n"
    "v_mfma_f32_16x16x32_bf16 v[152:155], v[104:107], a[124:127], v[152:155]\n"
    "v_and_or_b32 v25, v26, s52, v25\n"
    "v_mfma_f32_16x16x32_bf16 v[156:159], v[108:111], a[124:127], v[156:159]\n"
    "s_add_u32 s53, s53, 1310720\n"
    "v_mfma_f32_16x16x32_bf16 v[152:155], v[112:115], a[128:131], v[152:155]\n"
    "v_mfma_f32_16x16x32_bf16 v[156:159], v[116:119], a[128:131], v[156:159]\n"
    "v_mfma_f32_16x16x32_bf16 v[152:155], v[120:123], a[132:135], v[152:155]\n"
    "v_cvt_pk_bf16_f32 v160, v144, v145\n"
    "v_mfma_f32_16x16x32_bf16 v[156:159], v[124:127], a[132:135], v[156:159]\n"
    "v_cvt_pk_bf16_f32 v161, v146, v147\n"
    "v_mfma_f32_16x16x32_bf16 v[152:155], v[128:131], a[136:139], v[152:155]\n"
    "v_cvt_pk_bf16_f32 v162, v148, v149\n"
    "v_mfma_f32_16x16x32_bf16 v[156:159], v[132:135], a[136:139], v[156:159]\n"
    "v_cvt_pk_bf16_f32 v163, v150, v151\n"
    "v_mfma_f32_16x16x32_bf16 v[152:155], v[136:139], a[140:143], v[152:155]\n"
    "buffer_store_dwordx4 v[160:163], v25, s[40:43], 0 offen sc1\n"
    "v_mfma_f32_16x16x32_bf16 v[156:159], v[140:143], a[140:143], v[156:159]\n"
    "v_mfma_f32_16x16x32_bf16 v[144:147], v[96:99], a[144:147], 0\n"
    "v_add_u32 v25, s53, v24\n"
    "v_mfma_f32_16x16x32_bf16 v[148:151], v[100:103], a[144:147], 0\n"
    "v_lshrrev_b32 v26, 8, v25\n"
    "v_mfma_f32_16x16x32_bf16 v[144:147], v[104:107], a[148:151], v[144:147]\n"
    "v_and_or_b32 v25, v26, s52, v25\n"
    "v_mfma_f32_16x16x32_bf16 v[148:151], v[108:111], a[148:151], v[148:151]\n"
    "s_add_u32 s53, s53, 1310720\n"
    "v_mfma_f32_16x16x32_bf16 v[144:147], v[112:115], a[152:155], v[144:147]\n"
    "v_mfma_f32_16x16x32_bf16 v[148:151], v[116:119], a[152:155], v[148:151]\n"
    "v_mfma_f32_16x16x32_bf16 v[144:147], v[120:123], a[156:159], v[144:147]\n"
    "v_cvt_pk_bf16_f32 v164, v152, v153\n"
    "v_mfma_f32_16x16x32_bf16 v[148:151], v[124:127], a[156:159], v[148:151]\n"
    "v_cvt_pk_bf16_f32 v165, v154, v155\n"
    "v_mfma_f32_16x16x32_bf16 v[144:147], v[128:131], a[160:163], v[144:147]\n"
    "v_cvt_pk_bf16_f32 v166, v156, v157\n"
    "v_mfma_f32_16x16x32_bf16 v[148:151], v[132:135], a[160:163], v[148:151]\n"
    "v_cvt_pk_bf16_f32 v167, v158, v159\n"
    "v_mfma_f32_16x16x32_bf16 v[144:147], v[136:139], a[164:167], v[144:147]\n"
    "buffer_store_dwordx4 v[164:167], v25, s[40:43], 0 offen sc1\n"
    "v_mfma_f32_16x16x32_bf16 v[148:151], v[140:143], a[164:167], v[148:151]\n"
    "v_mfma_f32_16x16x32_bf16 v[152:155], v[96:99], a[168:171], 0\n"
    "v_add_u32 v25, s53, v24\n"
    "v_mfma_f32_16x16x32_bf16 v[156:159], v[100:103], a[168:171], 0\n"
    "v_lshrrev_b32 v26, 8, v25\n"
    "v_mfma_f32_16x16x32_bf16 v[152:155], v[104:107], a[172:175], v[152:155]\n"
    "v_and_or_b32 v25, v26, s52, v25\n"
    "v_mfma_f32_16x16x32_bf16 v[156:159], v[108:111], a[172:175], v[156:159]\n"
    "s_add_u32 s53, s53, 1310720\n"
    "v_mfma_f32_16x16x32_bf16 v[152:155], v[112:115], a[176:179], v[152:155]\n"
    "v_mfma_f32_16x16x32_bf16 v[156:159], v[116:119], a[176:179], v[156:159]\n"
    "v_mfma_f32_16x16x32_bf16 v[152:155], v[120:123], a[180:183], v[152:155]\n"
    "v_cvt_pk_bf16_f32 v160, v144, v145\n"
    "v_mfma_f32_16x16x32_bf16 v[156:159], v[124:127], a[180:183], v[156:159]\n"
    "v_cvt_pk_bf16_f32 v161, v146, v147\n"
    "v_mfma_f32_16x16x32_bf16 v[152:155], v[128:131], a[184:187], v[152:155]\n"
    "v_cvt_pk_bf16_f32 v162, v148, v149\n"
    "v_mfma_f32_16x16x32_bf16 v[156:159], v[132:135], a[184:187], v[156:159]\n"
    "v_cvt_pk_bf16_f32 v163, v150, v151\n"
    "v_mfma_f32_16x16x32_bf16 v[152:155], v[136:139], a[188:191], v[152:155]\n"
    "buffer_store_dwordx4 v[160:163], v25, s[40:43], 0 offen sc1\n"
    "v_mfma_f32_16x16x32_bf16 v[156:159], v[140:143], a[188:191], v[156:159]\n"
    "v_mfma_f32_16x16x32_bf16 v[144:147], v[96:99], a[192:195], 0\n"
    "v_add_u32 v25, s53, v24\n"
    "v_mfma_f32_16x16x32_bf16 v[148:151], v[100:103], a[192:195], 0\n"
    "v_lshrrev_b32 v26, 8, v25\n"
    "v_mfma_f32_16x16x32_bf16 v[144:147], v[104:107], a[196:199], v[144:147]\n"
    "v_and_or_b32 v25, v26, s52, v25\n"
    "v_mfma_f32_16x16x32_bf16 v[148:151], v[108:111], a[196:199], v[148:151]\n"
    "s_add_u32 s53, s53, 1310720\n"
    "v_mfma_f32_16x16x32_bf16 v[144:147], v[112:115], a[200:203], v[144:147]\n"
    "v_mfma_f32_16x16x32_bf16 v[148:151], v[116:119], a[200:203], v[148:151]\n"
    "v_mfma_f32_16x16x32_bf16 v[144:147], v[120:123], a[204:207], v[144:147]\n"
    "v_cvt_pk_bf16_f32 v164, v152, v153\n"
    "v_mfma_f32_16x16x32_bf16 v[148:151], v[124:127], a[204:207], v[148:151]\n"
    "v_cvt_pk_bf16_f32 v165, v154, v155\n"
    "v_mfma_f32_16x16x32_bf16 v[144:147], v[128:131], a[208:211], v[144:147]\n"
    "v_cvt_pk_bf16_f32 v166, v156, v157\n"
    "v_mfma_f32_16x16x32_bf16 v[148:151], v[132:135], a[208:211], v[148:151]\n"
    "v_cvt_pk_bf16_f32 v167, v158, v159\n"
    "v_mfma_f32_16x16x32_bf16 v[144:147], v[136:139], a[212:215], v[144:147]\n"
    "buffer_store_dwordx4 v[164:167], v25, s[40:43], 0 offen sc1\n"
    "v_mfma_f32_16x16x32_bf16 v[148:151], v[140:143], a[212:215], v[148:151]\n"
    "v_mfma_f32_16x16x32_bf16 v[152:155], v[96:99], a[216:219], 0\n"
    "v_add_u32 v25, s53, v24\n"
    "v_mfma_f32_16x16x32_bf16 v[156:159], v[100:103], a[216:219], 0\n"
    "v_lshrrev_b32 v26, 8, v25\n"
    "v_mfma_f32_16x16x32_bf16 v[152:155], v[104:107], a[220:223], v[152:155]\n"
    "v_and_or_b32 v25, v26, s52, v25\n"
    "v_mfma_f32_16x16x32_bf16 v[156:159], v[108:111], a[220:223], v[156:159]\n"
    "s_add_u32 s53, s53, 1310720\n"
    "v_mfma_f32_16x16x32_bf16 v[152:155], v[112:115], a[224:227], v[152:155]\n"
    "v_mfma_f32_16x16x32_bf16 v[156:159], v[116:119], a[224:227], v[156:159]\n"
    "v_mfma_f32_16x16x32_bf16 v[152:155], v[120:123], a[228:231], v[152:155]\n"
    "v_cvt_pk_bf16_f32 v160, v144, v145\n"
    "v_mfma_f32_16x16x32_bf16 v[156:159], v[124:127], a[228:231], v[156:159]\n"
    "v_cvt_pk_bf16_f32 v161, v146, v147\n"
    "v_mfma_f32_16x16x32_bf16 v[152:155], v[128:131], a[232:235], v[152:155]\n"
    "v_cvt_pk_bf16_f32 v162, v148, v149\n"
    "v_mfma_f32_16x16x32_bf16 v[156:159], v[132:135], a[232:235], v[156:159]\n"
    "v_cvt_pk_bf16_f32 v163, v150, v151\n"
    "v_mfma_f32_16x16x32_bf16 v[152:155], v[136:139], a[236:239], v[152:155]\n"
    "buffer_store_dwordx4 v[160:163], v25, s[40:43], 0 offen sc1\n"
    "v_mfma_f32_16x16x32_bf16 v[156:159], v[140:143], a[236:239], v[156:159]\n"
    "v_add_u32 v25, s53, v24\n"
    "v_lshrrev_b32 v26, 8, v25\n"
    "v_and_or_b32 v25, v26, s52, v25\n"
    "s_add_u32 s53, s53, 1310720\n"
    "s_nop 1\n"
    "v_cvt_pk_bf16_f32 v164, v152, v153\n"
    "v_cvt_pk_bf16_f32 v165, v154, v155\n"
    "v_cvt_pk_bf16_f32 v166, v156, v157\n"
    "v_cvt_pk_bf16_f32 v167, v158, v159\n"
    "buffer_store_dwordx4 v[164:167], v25, s[40:43], 0 offen sc1\n"
    "s_add_u32 s50, s50, 1024\n"
    "s_sub_u32 s51, s51, 1\n"
    "s_cmp_lg_u32 s51, 0\n"
    "s_cbranch_scc1 tk_bg_%=\n"
    "s_cmp_eq_u32 s45, 0\n"
    "s_cselect_b32 s59, 1, 0\n"
    "v_lshlrev_b32 v26, 7, v15\n"
    "v_cmp_gt_u32 vcc, s59, v15\n"
    "v_mov_b32 v27, 2147483632\n"
    "s_nop 0\n"
    "v_cndmask_b32 v26, v27, v26, vcc\n"
    "v_mov_b32 v27, 1\n"
    "s_and_b32 s54, s84, 7\n"
    "s_lshl_b32 s54, s54, 7\n"
    "s_add_u32 s54, s54, 2364928\n"
    "s_waitcnt vmcnt(0)\n"
    "s_waitcnt lgkmcnt(0)\n"
    "s_barrier\n"
    "buffer_atomic_add v27, v26, s[40:43], s54 offen\n"
    "s_lshr_b32 s61, s44, 2\n"
    "s_and_b32 s47, s44, 3\n"
    "v_and_b32 v4, 0x370, v0\n"
    "v_bfe_u32 v5, v15, 3, 1\n"
    "v_lshl_add_u32 v1, v5, 15, v4\n"
    "v_lshl_add_u32 v2, v5, 12, v4\n"
    "s_cmp_eq_u32 s45, 0\n"
    "s_cselect_b32 s59, 1, 0\n"
    "v_lshlrev_b32 v3, 7, v15\n"
    "v_cmp_gt_u32 vcc, s59, v15\n"
    "v_mov_b32 v4, 2147483632\n"
    "s_nop 0\n"
    "v_cndmask_b32 v3, v4, v3, vcc\n"
    "v_mov_b32 v6, 1\n"
    "s_mul_i32 s48, s45, 20480\n"
    "v_add_u32 v10, s48, v0\n"
    "s_add_u32 s20, s45, 0\n"
    "s_min_u32 s20, s20, 9\n"
    "s_lshl_b32 s48, s20, 11\n"
    "v_add_u32 v11, s48, v0\n"
    "s_cmp_ge_u32 s20, 5\n"
    "s_cselect_b32 s48, 15, 0\n"
    "s_add_u32 s20, s20, s48\n"
    "s_mul_i32 s48, s61, 5\n"
    "s_add_u32 s20, s20, s48\n"
    "s_add_u32 s21, s45, 4\n"
    "s_min_u32 s21, s21, 9\n"
    "s_lshl_b32 s48, s21, 11\n"
    "v_add_u32 v12, s48, v0\n"
    "s_cmp_ge_u32 s21, 5\n"
    "s_cselect_b32 s48, 15, 0\n"
    "s_add_u32 s21, s21, s48\n"
    "s_mul_i32 s48, s61, 5\n"
    "s_add_u32 s21, s21, s48\n"
    "s_add_u32 s22, s45, 8\n"
    "s_min_u32 s22, s22, 9\n"
    "s_lshl_b32 s48, s22, 11\n"
    "v_add_u32 v13, s48, v0\n"
    "s_cmp_ge_u32 s22, 5\n"
    "s_cselect_b32 s48, 15, 0\n"
    "s_add_u32 s22, s22, s48\n"
    "s_mul_i32 s48, s61, 5\n"
    "s_add_u32 s22, s22, s48\n"
    "v_lshlrev_b32 v7, 7, v15\n"
    "s_lshl_b32 s48, s61, 7\n"
    "v_add_u32 v8, s48, v7\n"
    "v_cmp_eq_u32 vcc, 8, v15\n"
    "s_nop 1\n"
    "v_cndmask_b32 v7, v7, v8, vcc\n"
    "v_mov_b32 v8, 8\n"
    "v_mov_b32 v9, 20\n"
    "v_cndmask_b32 v8, v8, v9, vcc\n"
    "v_cmp_gt_u32 vcc, 9, v15\n"
    "s_nop 1\n"
    "v_cndmask_b32 v8, 0, v8, vcc\n"
    "v_mov_b32 v4, 2147483632\n"
    "v_cndmask_b32 v7, v4, v7, vcc\n"
    "v_mov_b32 v9, 32\n"
    "v_cmp_eq_u32 vcc, 0, v15\n"
    "s_nop 1\n"
    "v_cndmask_b32 v9, 0, v9, vcc\n"
    "s_mov_b32 s63, s47\n"
    "s_nop 0\n"
    "tk_first_%=:\n"
    "s_and_b32 s47, s63, 63\n"
    "s_lshl_b32 s54, s63, 7\n"
    "s_add_u32 s54, s54, 2124032\n"
    "s_cmp_lg_u32 s45, 0\n"
    "s_cbranch_scc1 tk_u0_ok_%=\n"
    "s_mov_b32 s60, 0\n"
    "tk_u0_%=:\n"
    "v_mov_b32 v4, 0\n"
    "buffer_atomic_add v4, v3, s[40:43], s54 offen sc0\n"
    "s_waitcnt vmcnt(0)\n"
    "v_cmp_gt_u32 vcc, v9, v4\n"
    "s_nop 4\n"
    "s_cbranch_vccz tk_u0_ok_%=\n"
    "s_sleep 4\n"
    "s_add_u32 s60, s60, 1\n"
    "s_cmp_gt_u32 s60, 65536\n"
    "s_cbranch_scc1 tk_abort_%=\n"
    "s_and_b32 s59, s60, 63\n"
    "s_cbranch_scc1 tk_u0_%=\n"
    "v_mov_b32 v4, 1024\n"
    "buffer_load_dword v4, v4, s[40:43], s58 offen sc1\n"
    "s_waitcnt vmcnt(0)\n"
    "v_readfirstlane_b32 s59, v4\n"
    "s_nop 0\n"
    "s_cmp_lg_u32 s59, 0\n"
    "s_nop 2\n"
    "s_cbranch_scc1 tk_end_%=\n"
    "s_branch tk_u0_%=\n"
    "tk_u0_ok_%=:\n"
    "s_barrier\n"
    "s_mov_b32 s64, 0\n"
    "s_add_u32 s50, s47, 0\n"
    "s_and_b32 s50, s50, 31\n"
    "s_mul_i32 s50, s50, 80\n"
    "s_mul_i32 s48, s61, 20\n"
    "s_add_u32 s50, s50, s48\n"
    "s_lshl_b32 s50, s50, 15\n"
    "s_lshl_b32 s48, s45, 12\n"
    "s_and_b32 s49, s48, 0x2000\n"
    "s_add_u32 s48, s48, s49\n"
    "s_add_u32 s50, s50, s48\n"
    "s_add_u32 s50, s50, 32964608\n"
    "s_or_b32 s50, s50, s64\n"
    "s_lshr_b32 s59, s50, 8\n"
    "s_and_b32 s59, s59, 0x2000\n"
    "s_add_u32 s30, s50, s59\n"
    "s_add_u32 s50, s50, 65536\n"
    "s_lshr_b32 s59, s50, 8\n"
    "s_and_b32 s59, s59, 0x2000\n"
    "s_add_u32 s31, s50, s59\n"
    "s_add_u32 s50, s50, 65536\n"
    "s_lshr_b32 s59, s50, 8\n"
    "s_and_b32 s59, s59, 0x2000\n"
    "s_add_u32 s34, s50, s59\n"
    "s_add_u32 s50, s50, 65536\n"
    "s_lshr_b32 s59, s50, 8\n"
    "s_and_b32 s59, s59, 0x2000\n"
    "s_add_u32 s35, s50, s59\n"
    "s_add_u32 s50, s50, 65536\n"
    "s_lshr_b32 s59, s50, 8\n"
    "s_and_b32 s59, s59, 0x2000\n"
    "s_add_u32 s36, s50, s59\n"
    "s_add_u32 s50, s50, 65536\n"
    "s_lshr_b32 s59, s50, 8\n"
    "s_and_b32 s59, s59, 0x2000\n"
    "s_add_u32 s37, s50, s59\n"
    "s_add_u32 s50, s50, 65536\n"
    "s_lshr_b32 s59, s50, 8\n"
    "s_and_b32 s59, s59, 0x2000\n"
    "s_add_u32 s38, s50, s59\n"
    "s_add_u32 s50, s50, 65536\n"
    "s_lshr_b32 s59, s50, 8\n"
    "s_and_b32 s59, s59, 0x2000\n"
    "s_add_u32 s39, s50, s59\n"
    "s_add_u32 s50, s50, 65536\n"
    "s_lshr_b32 s59, s50, 8\n"
    "s_and_b32 s59, s59, 0x2000\n"
    "s_add_u32 s96, s50, s59\n"
    "s_add_u32 s50, s50, 65536\n"
    "s_lshr_b32 s59, s50, 8\n"
    "s_and_b32 s59, s59, 0x2000\n"
    "s_add_u32 s97, s50, s59\n"
    "s_add_u32 s50, s50, 65536\n"
    "buffer_load_dwordx4 a[0:3], v1, s[40:43], s30 offen sc1\n"
    "buffer_load_dwordx4 a[4:7], v1, s[40:43], s31 offen sc1\n"
    "buffer_load_dwordx4 a[8:11], v1, s[40:43], s34 offen sc1\n"
    "buffer_load_dwordx4 a[12:15], v1, s[40:43], s35 offen sc1\n"
    "buffer_load_dwordx4 a[16:19], v1, s[40:43], s36 offen sc1\n"
    "buffer_load_dwordx4 a[20:23], v1, s[40:43], s37 offen sc1\n"
    "buffer_load_dwordx4 a[24:27], v1, s[40:43], s38 offen sc1\n"
    "buffer_load_dwordx4 a[28:31], v1, s[40:43], s39 offen sc1\n"
    "buffer_load_dwordx4 a[32:35], v1, s[40:43], s96 offen sc1\n"
    "buffer_load_dwordx4 a[36:39], v1, s[40:43], s97 offen sc1\n"
    "buffer_load_dwordx4 a[40:43], v1, s[40:43], s30 offen offset:128 sc1\n"
    "buffer_load_dwordx4 a[44:47], v1, s[40:43], s31 offen offset:128 sc1\n"
    "buffer_load_dwordx4 a[48:51], v1, s[40:43], s34 offen offset:128 sc1\n"
    "buffer_load_dwordx4 a[52:55], v1, s[40:43], s35 offen offset:128 sc1\n"
    "buffer_load_dwordx4 a[56:59], v1, s[40:43], s36 offen offset:128 sc1\n"
    "buffer_load_dwordx4 a[60:63], v1, s[40:43], s37 offen offset:128 sc1\n"
    "buffer_load_dwordx4 a[64:67], v1, s[40:43], s38 offen offset:128 sc1\n"
    "buffer_load_dwordx4 a[68:71], v1, s[40:43], s39 offen offset:128 sc1\n"
    "buffer_load_dwordx4 a[72:75], v1, s[40:43], s96 offen offset:128 sc1\n"
    "buffer_load_dwordx4 a[76:79], v1, s[40:43], s97 offen offset:128 sc1\n"
    "buffer_load_dwordx4 a[80:83], v1, s[40:43], s30 offen offset:1024 sc1\n"
    "buffer_load_dwordx4 a[84:87], v1, s[40:43], s31 offen offset:1024 sc1\n"
    "buffer_load_dwordx4 a[88:91], v1, s[40:43], s34 offen offset:1024 sc1\n"
    "buffer_load_dwordx4 a[92:95], v1, s[40:43], s35 offen offset:1024 sc1\n"
    "buffer_load_dwordx4 a[96:99], v1, s[40:43], s36 offen offset:1024 sc1\n"
    "buffer_load_dwordx4 a[100:103], v1, s[40:43], s37 offen offset:1024 sc1\n"
    "buffer_load_dwordx4 a[104:107], v1, s[40:43], s38 offen offset:1024 sc1\n"
    "buffer_load_dwordx4 a[108:111], v1, s[40:43], s39 offen offset:1024 sc1\n"
    "buffer_load_dwordx4 a[112:115], v1, s[40:43], s96 offen offset:1024 sc1\n"
    "buffer_load_dwordx4 a[116:119], v1, s[40:43], s97 offen offset:1024 sc1\n"
    "buffer_load_dwordx4 a[120:123], v1, s[40:43], s30 offen offset:1152 sc1\n"
    "buffer_load_dwordx4 a[124:127], v1, s[40:43], s31 offen offset:1152 sc1\n"
    "buffer_load_dwordx4 a[128:131], v1, s[40:43], s34 offen offset:1152 sc1\n"
    "buffer_load_dwordx4 a[132:135], v1, s[40:43], s35 offen offset:1152 sc1\n"
    "buffer_load_dwordx4 a[136:139], v1, s[40:43], s36 offen offset:1152 sc1\n"
    "buffer_load_dwordx4 a[140:143], v1, s[40:43], s37 offen offset:1152 sc1\n"
    "buffer_load_dwordx4 a[144:147], v1, s[40:43], s38 offen offset:1152 sc1\n"
    "buffer_load_dwordx4 a[148:151], v1, s[40:43], s39 offen offset:1152 sc1\n"
    "buffer_load_dwordx4 a[152:155], v1, s[40:43], s96 offen offset:1152 sc1\n"
    "buffer_load_dwordx4 a[156:159], v1, s[40:43], s97 offen offset:1152 sc1\n"
    "buffer_load_dwordx4 a[160:163], v1, s[40:43], s30 offen offset:2048 sc1\n"
    "buffer_load_dwordx4 a[164:167], v1, s[40:43], s31 offen offset:2048 sc1\n"
    "buffer_load_dwordx4 a[168:171], v1, s[40:43], s34 offen offset:2048 sc1\n"
    "buffer_load_dwordx4 a[172:175], v1, s[40:43], s35 offen offset:2048 sc1\n"
    "buffer_load_dwordx4 a[176:179], v1, s[40:43], s36 offen offset:2048 sc1\n"
    "buffer_load_dwordx4 a[180:183], v1, s[40:43], s37 offen offset:2048 sc1\n"
    "buffer_load_dwordx4 a[184:187], v1, s[40:43], s38 offen offset:2048 sc1\n"
    "buffer_load_dwordx4 a[188:191], v1, s[40:43], s39 offen offset:2048 sc1\n"
    "buffer_load_dwordx4 a[192:195], v1, s[40:43], s96 offen offset:2048 sc1\n"
    "buffer_load_dwordx4 a[196:199], v1, s[40:43], s97 offen offset:2048 sc1\n"
    "buffer_load_dwordx4 a[200:203], v1, s[40:43], s30 offen offset:2176 sc1\n"
    "buffer_load_dwordx4 a[204:207], v1, s[40:43], s31 offen offset:2176 sc1\n"
    "buffer_load_dwordx4 a[208:211], v1, s[40:43], s34 offen offset:2176 sc1\n"
    "buffer_load_dwordx4 a[212:215], v1, s[40:43], s35 offen offset:2176 sc1\n"
    "buffer_load_dwordx4 a[216:219], v1, s[40:43], s36 offen offset:2176 sc1\n"
    "buffer_load_dwordx4 a[220:223], v1, s[40:43], s37 offen offset:2176 sc1\n"
    "buffer_load_dwordx4 a[224:227], v1, s[40:43], s38 offen offset:2176 sc1\n"
    "buffer_load_dwordx4 a[228:231], v1, s[40:43], s39 offen offset:2176 sc1\n"
    "buffer_load_dwordx4 a[232:235], v1, s[40:43], s96 offen offset:2176 sc1\n"
    "buffer_load_dwordx4 a[236:239], v1, s[40:43], s97 offen offset:2176 sc1\n"
    "buffer_load_dwordx4 a[240:243], v1, s[40:43], s30 offen offset:3072 sc1\n"
    "buffer_load_dwordx4 a[244:247], v1, s[40:43], s31 offen offset:3072 sc1\n"
    "buffer_load_dwordx4 a[248:251], v1, s[40:43], s34 offen offset:3072 sc1\n"
    "s_waitcnt vmcnt(61)\n"
    "buffer_load_dwordx4 a[252:255], v1, s[40:43], s35 offen offset:3072 sc1\n"
    "buffer_load_dwordx4 v[192:195], v1, s[40:43], s36 offen offset:3072 sc1\n"
    "s_waitcnt vmcnt(61)\n"
    "buffer_load_dwordx4 v[196:199], v1, s[40:43], s37 offen offset:3072 sc1\n"
    "buffer_load_dwordx4 v[200:203], v1, s[40:43], s38 offen offset:3072 sc1\n"
    "s_waitcnt vmcnt(61)\n"
    "buffer_load_dwordx4 v[204:207], v1, s[40:43], s39 offen offset:3072 sc1\n"
    "buffer_load_dwordx4 v[208:211], v1, s[40:43], s96 offen offset:3072 sc1\n"
    "s_waitcnt vmcnt(61)\n"
    "buffer_load_dwordx4 v[212:215], v1, s[40:43], s97 offen offset:3072 sc1\n"
    "buffer_load_dwordx4 v[216:219], v1, s[40:43], s30 offen offset:3200 sc1\n"
    "s_waitcnt vmcnt(61)\n"
    "buffer_load_dwordx4 v[220:223], v1, s[40:43], s31 offen offset:3200 sc1\n"
    "buffer_load_dwordx4 v[224:227], v1, s[40:43], s34 offen offset:3200 sc1\n"
    "s_waitcnt vmcnt(61)\n"
    "buffer_load_dwordx4 v[228:231], v1, s[40:43], s35 offen offset:3200 sc1\n"
    "buffer_load_dwordx4 v[232:235], v1, s[40:43], s36 offen offset:3200 sc1\n"
    "s_waitcnt vmcnt(61)\n"
    "buffer_load_dwordx4 v[236:239], v1, s[40:43], s37 offen offset:3200 sc1\n"
    "buffer_load_dwordx4 v[240:243], v1, s[40:43], s38 offen offset:3200 sc1\n"
    "s_waitcnt vmcnt(61)\n"
    "buffer_load_dwordx4 v[244:247], v1, s[40:43], s39 offen offset:3200 sc1\n"
    "buffer_load_dwordx4 v[248:251], v1, s[40:43], s96 offen offset:3200 sc1\n"
    "s_waitcnt vmcnt(61)\n"
    "buffer_load_dwordx4 v[252:255], v1, s[40:43], s97 offen offset:3200 sc1\n"
    "tk_loop_%=:\n"
    "s_and_b32 s47, s63, 63\n"
    "s_cmp_lt_u32 s47, 60\n"
    "s_cselect_b32 s48, 4, 0\n"
    "s_cselect_b32 s64, 0, 0x40000000\n"
    "s_add_u32 s54, s63, s48\n"
    "s_lshl_b32 s54, s54, 7\n"
    "s_add_u32 s54, s54, 2124032\n"
    "s_cmp_lg_u32 s45, 0\n"
    "s_cbranch_scc1 tk_ux_ok_%=\n"
    "s_mov_b32 s60, 0\n"
    "tk_ux_%=:\n"
    "v_mov_b32 v4, 0\n"
    "buffer_atomic_add v4, v3, s[40:43], s54 offen sc0\n"
    "s_waitcnt vmcnt(0)\n"
    "v_cmp_gt_u32 vcc, v9, v4\n"
    "s_nop 4\n"
    "s_cbranch_vccz tk_ux_ok_%=\n"
    "s_sleep 4\n"
    "s_add_u32 s60, s60, 1\n"
    "s_cmp_gt_u32 s60, 65536\n"
    "s_cbranch_scc1 tk_abort_%=\n"
    "s_and_b32 s59, s60, 63\n"
    "s_cbranch_scc1 tk_ux_%=\n"
    "v_mov_b32 v4, 1024\n"
    "buffer_load_dword v4, v4, s[40:43], s58 offen sc1\n"
    "s_waitcnt vmcnt(0)\n"
    "v_readfirstlane_b32 s59, v4\n"
    "s_nop 0\n"
    "s_cmp_lg_u32 s59, 0\n"
    "s_nop 2\n"
    "s_cbranch_scc1 tk_end_%=\n"
    "s_branch tk_ux_%=\n"
    "tk_ux_ok_%=:\n"
    "s_barrier\n"
    "s_add_u32 s50, s47, 4\n"
    "s_and_b32 s50, s50, 31\n"
    "s_mul_i32 s50, s50, 80\n"
    "s_mul_i32 s48, s61, 20\n"
    "s_add_u32 s50, s50, s48\n"
    "s_lshl_b32 s50, s50, 15\n"
    "s_lshl_b32 s48, s45, 12\n"
    "s_and_b32 s49, s48, 0x2000\n"
    "s_add_u32 s48, s48, s49\n"
    "s_add_u32 s50, s50, s48\n"
    "s_add_u32 s50, s50, 32964608\n"
    "s_or_b32 s50, s50, s64\n"
    "s_lshr_b32 s59, s50, 8\n"
    "s_and_b32 s59, s59, 0x2000\n"
    "s_add_u32 s30, s50, s59\n"
    "s_add_u32 s50, s50, 65536\n"
    "s_lshr_b32 s59, s50, 8\n"
    "s_and_b32 s59, s59, 0x2000\n"
    "s_add_u32 s31, s50, s59\n"
    "s_add_u32 s50, s50, 65536\n"
    "s_lshr_b32 s59, s50, 8\n"
    "s_and_b32 s59, s59, 0x2000\n"
    "s_add_u32 s34, s50, s59\n"
    "s_add_u32 s50, s50, 65536\n"
    "s_lshr_b32 s59, s50, 8\n"
    "s_and_b32 s59, s59, 0x2000\n"
    "s_add_u32 s35, s50, s59\n"
    "s_add_u32 s50, s50, 65536\n"
    "s_lshr_b32 s59, s50, 8\n"
    "s_and_b32 s59, s59, 0x2000\n"
    "s_add_u32 s36, s50, s59\n"
    "s_add_u32 s50, s50, 65536\n"
    "s_lshr_b32 s59, s50, 8\n"
    "s_and_b32 s59, s59, 0x2000\n"
    "s_add_u32 s37, s50, s59\n"
    "s_add_u32 s50, s50, 65536\n"
    "s_lshr_b32 s59, s50, 8\n"
    "s_and_b32 s59, s59, 0x2000\n"
    "s_add_u32 s38, s50, s59\n"
    "s_add_u32 s50, s50, 65536\n"
    "s_lshr_b32 s59, s50, 8\n"
    "s_and_b32 s59, s59, 0x2000\n"
    "s_add_u32 s39, s50, s59\n"
    "s_add_u32 s50, s50, 65536\n"
    "s_lshr_b32 s59, s50, 8\n"
    "s_and_b32 s59, s59, 0x2000\n"
    "s_add_u32 s96, s50, s59\n"
    "s_add_u32 s50, s50, 65536\n"
    "s_lshr_b32 s59, s50, 8\n"
    "s_and_b32 s59, s59, 0x2000\n"
    "s_add_u32 s97, s50, s59\n"
    "s_add_u32 s50, s50, 65536\n"
    "s_and_b32 s65, s47, 31\n"
    "s_mul_i32 s62, s65, 80\n"
    "s_mul_i32 s48, s61, 20\n"
    "s_add_u32 s62, s62, s48\n"
    "s_mul_i32 s54, s63, 1536\n"
    "s_add_u32 s54, s54, 2157568\n"
    "s_cmp_lg_u32 s45, 0\n"
    "s_cbranch_scc1 tk_pt_ok_%=\n"
    "s_mov_b32 s60, 0\n"
    "tk_pt_%=:\n"
    "v_mov_b32 v4, 0\n"
    "buffer_atomic_add v4, v7, s[40:43], s54 offen sc0\n"
    "s_waitcnt vmcnt(0)\n"
    "v_cmp_gt_u32 vcc, v8, v4\n"
    "s_nop 4\n"
    "s_cbranch_vccz tk_pt_ok_%=\n"
    "s_sleep 4\n"
    "s_add_u32 s60, s60, 1\n"
    "s_cmp_gt_u32 s60, 65536\n"
    "s_cbranch_scc1 tk_abort_%=\n"
    "s_and_b32 s59, s60, 63\n"
    "s_cbranch_scc1 tk_pt_%=\n"
    "v_mov_b32 v4, 1024\n"
    "buffer_load_dword v4, v4, s[40:43], s58 offen sc1\n"
    "s_waitcnt vmcnt(0)\n"
    "v_readfirstlane_b32 s59, v4\n"
    "s_nop 0\n"
    "s_cmp_lg_u32 s59, 0\n"
    "s_nop 2\n"
    "s_cbranch_scc1 tk_end_%=\n"
    "s_branch tk_pt_%=\n"
    "tk_pt_ok_%=:\n"
    "s_barrier\n"
    "s_lshl_b32 s50, s65, 5\n"
    "s_lshl_b32 s48, s45, 3\n"
    "s_add_u32 s50, s50, s48\n"
    "s_lshl_b32 s50, s50, 11\n"
    "s_add_u32 s50, s50, 130482176\n"
    "buffer_load_dwordx4 v[32:35], v0, s[40:43], s50 offen sc1\n"
    "buffer_load_dwordx4 v[36:39], v0, s[40:43], s50 offen offset:1024 sc1\n"
    "buffer_load_dwordx4 v[40:43], v0, s[40:43], s50 offen offset:2048 sc1\n"
    "buffer_load_dwordx4 v[44:47], v0, s[40:43], s50 offen offset:3072 sc1\n"
    "s_add_u32 s50, s50, 4096\n"
    "buffer_load_dwordx4 v[48:51], v0, s[40:43], s50 offen sc1\n"
    "buffer_load_dwordx4 v[52:55], v0, s[40:43], s50 offen offset:1024 sc1\n"
    "buffer_load_dwordx4 v[56:59], v0, s[40:43], s50 offen offset:2048 sc1\n"
    "buffer_load_dwordx4 v[60:63], v0, s[40:43], s50 offen offset:3072 sc1\n"
    "s_add_u32 s50, s50, 4096\n"
    "buffer_load_dwordx4 v[64:67], v0, s[40:43], s50 offen sc1\n"
    "buffer_load_dwordx4 v[68:71], v0, s[40:43], s50 offen offset:1024 sc1\n"
    "buffer_load_dwordx4 v[72:75], v0, s[40:43], s50 offen offset:2048 sc1\n"
    "buffer_load_dwordx4 v[76:79], v0, s[40:43], s50 offen offset:3072 sc1\n"
    "s_add_u32 s50, s50, 4096\n"
    "buffer_load_dwordx4 v[80:83], v0, s[40:43], s50 offen sc1\n"
    "buffer_load_dwordx4 v[84:87], v0, s[40:43], s50 offen offset:1024 sc1\n"
    "buffer_load_dwordx4 v[88:91], v0, s[40:43], s50 offen offset:2048 sc1\n"
    "buffer_load_dwordx4 v[92:95], v0, s[40:43], s50 offen offset:3072 sc1\n"
    "s_lshl_b32 s50, s65, 14\n"
    "s_lshl_b32 s48, s45, 11\n"
    "s_add_u32 s50, s50, s48\n"
    "s_add_u32 s50, s50, 132579328\n"
    "s_add_u32 s51, s50, 8192\n"
    "buffer_load_dwordx4 v[96:99], v0, s[40:43], s50 offen sc1\n"
    "buffer_load_dwordx4 v[100:103], v0, s[40:43], s51 offen sc1\n"
    "buffer_load_dwordx4 v[104:107], v0, s[40:43], s50 offen offset:1024 sc1\n"
    "buffer_load_dwordx4 v[108:111], v0, s[40:43], s51 offen offset:1024 sc1\n"
    "s_lshl_b32 s50, s62, 12\n"
    "s_lshl_b32 s48, s45, 10\n"
    "s_add_u32 s50, s50, s48\n"
    "s_add_u32 s50, s50, 116850688\n"
    "s_add_u32 s76, s50, 0\n"
    "s_add_u32 s77, s50, 8192\n"
    "s_add_u32 s78, s50, 16384\n"
    "s_add_u32 s79, s50, 24576\n"
    "s_add_u32 s80, s50, 32768\n"
    "s_add_u32 s81, s50, 40960\n"
    "s_add_u32 s82, s50, 49152\n"
    "s_add_u32 s83, s50, 57344\n"
    "s_add_u32 s84, s50, 65536\n"
    "s_add_u32 s85, s50, 73728\n"
    "s_mul_i32 s48, s47, 40\n"
    "s_add_u32 s48, s48, s20\n"
    "s_lshl_b32 s24, s48, 11\n"
    "s_add_u32 s27, s24, 133103616\n"
    "s_mul_i32 s48, s47, 40\n"
    "s_add_u32 s48, s48, s21\n"
    "s_lshl_b32 s25, s48, 11\n"
    "s_add_u32 s28, s25, 133103616\n"
    "s_mul_i32 s48, s47, 40\n"
    "s_add_u32 s48, s48, s22\n"
    "s_lshl_b32 s26, s48, 11\n"
    "s_add_u32 s29, s26, 133103616\n"
    "s_waitcnt vmcnt(19)\n"
    "v_mfma_f32_16x16x32_bf16 v[112:115], a[0:3], v[32:35], 0\n"
    "s_waitcnt vmcnt(18)\n"
    "v_mfma_f32_16x16x32_bf16 v[152:155], a[0:3], v[36:39], 0\n"
    "buffer_load_dwordx4 a[0:3], v2, s[40:43], s76 offen sc1\n"
    "v_mfma_f32_16x16x32_bf16 v[116:119], a[4:7], v[32:35], 0\n"
    "v_mfma_f32_16x16x32_bf16 v[156:159], a[4:7], v[36:39], 0\n"
    "buffer_load_dwordx4 a[4:7], v2, s[40:43], s77 offen sc1\n"
    "v_mfma_f32_16x16x32_bf16 v[120:123], a[8:11], v[32:35], 0\n"
    "v_mfma_f32_16x16x32_bf16 v[160:163], a[8:11], v[36:39], 0\n"
    "buffer_load_dwordx4 a[8:11], v2, s[40:43], s78 offen sc1\n"
    "v_mfma_f32_16x16x32_bf16 v[124:127], a[12:15], v[32:35], 0\n"
    "v_mfma_f32_16x16x32_bf16 v[164:167], a[12:15], v[36:39], 0\n"
    "buffer_load_dwordx4 a[12:15], v2, s[40:43], s79 offen sc1\n"
    "v_mfma_f32_16x16x32_bf16 v[128:131], a[16:19], v[32:35], 0\n"
    "v_mfma_f32_16x16x32_bf16 v[168:171], a[16:19], v[36:39], 0\n"
    "buffer_load_dwordx4 a[16:19], v2, s[40:43], s80 offen sc1\n"
    "v_mfma_f32_16x16x32_bf16 v[132:135], a[20:23], v[32:35], 0\n"
    "v_mfma_f32_16x16x32_bf16 v[172:175], a[20:23], v[36:39], 0\n"
    "buffer_load_dwordx4 a[20:23], v2, s[40:43], s81 offen sc1\n"
    "v_mfma_f32_16x16x32_bf16 v[136:139], a[24:27], v[32:35], 0\n"
    "v_mfma_f32_16x16x32_bf16 v[176:179], a[24:27], v[36:39], 0\n"
    "buffer_load_dwordx4 a[24:27], v2, s[40:43], s82 offen sc1\n"
    "v_mfma_f32_16x16x32_bf16 v[140:143], a[28:31], v[32:35], 0\n"
    "v_mfma_f32_16x16x32_bf16 v[180:183], a[28:31], v[36:39], 0\n"
    "buffer_load_dwordx4 a[28:31], v2, s[40:43], s83 offen sc1\n"
    "v_mfma_f32_16x16x32_bf16 v[144:147], a[32:35], v[32:35], 0\n"
    "v_mfma_f32_16x16x32_bf16 v[184:187], a[32:35], v[36:39], 0\n"
    "buffer_load_dwordx4 a[32:35], v2, s[40:43], s84 offen sc1\n"
    "v_mfma_f32_16x16x32_bf16 v[148:151], a[36:39], v[32:35], 0\n"
    "v_mfma_f32_16x16x32_bf16 v[188:191], a[36:39], v[36:39], 0\n"
    "buffer_load_dwordx4 a[36:39], v2, s[40:43], s85 offen sc1\n"
    "s_waitcnt vmcnt(27)\n"
    "v_mfma_f32_16x16x32_bf16 v[112:115], a[40:43], v[40:43], v[112:115]\n"
    "s_waitcnt vmcnt(26)\n"
    "v_mfma_f32_16x16x32_bf16 v[152:155], a[40:43], v[44:47], v[152:155]\n"
    "buffer_load_dwordx4 a[40:43], v2, s[40:43], s76 offen offset:128 sc1\n"
    "v_mfma_f32_16x16x32_bf16 v[116:119], a[44:47], v[40:43], v[116:119]\n"
    "v_mfma_f32_16x16x32_bf16 v[156:159], a[44:47], v[44:47], v[156:159]\n"
    "buffer_load_dwordx4 a[44:47], v2, s[40:43], s77 offen offset:128 sc1\n"
    "v_mfma_f32_16x16x32_bf16 v[120:123], a[48:51], v[40:43], v[120:123]\n"
    "v_mfma_f32_16x16x32_bf16 v[160:163], a[48:51], v[44:47], v[160:163]\n"
    "buffer_load_dwordx4 a[48:51], v2, s[40:43], s78 offen offset:128 sc1\n"
    "v_mfma_f32_16x16x32_bf16 v[124:127], a[52:55], v[40:43], v[124:127]\n"
    "v_mfma_f32_16x16x32_bf16 v[164:167], a[52:55], v[44:47], v[164:167]\n"
    "buffer_load_dwordx4 a[52:55], v2, s[40:43], s79 offen offset:128 sc1\n"
    "v_mfma_f32_16x16x32_bf16 v[128:131], a[56:59], v[40:43], v[128:131]\n"
    "v_mfma_f32_16x16x32_bf16 v[168:171], a[56:59], v[44:47], v[168:171]\n"
    "buffer_load_dwordx4 a[56:59], v2, s[40:43], s80 offen offset:128 sc1\n"
    "v_mfma_f32_16x16x32_bf16 v[132:135], a[60:63], v[40:43], v[132:135]\n"
    "v_mfma_f32_16x16x32_bf16 v[172:175], a[60:63], v[44:47], v[172:175]\n"
    "buffer_load_dwordx4 a[60:63], v2, s[40:43], s81 offen offset:128 sc1\n"
    "v_mfma_f32_16x16x32_bf16 v[136:139], a[64:67], v[40:43], v[136:139]\n"
    "v_mfma_f32_16x16x32_bf16 v[176:179], a[64:67], v[44:47], v[176:179]\n"
    "buffer_load_dwordx4 a[64:67], v2, s[40:43], s82 offen offset:128 sc1\n"
    "v_mfma_f32_16x16x32_bf16 v[140:143], a[68:71], v[40:43], v[140:143]\n"
    "v_mfma_f32_16x16x32_bf16 v[180:183], a[68:71], v[44:47], v[180:183]\n"
    "buffer_load_dwordx4 a[68:71], v2, s[40:43], s83 offen offset:128 sc1\n"
    "v_mfma_f32_16x16x32_bf16 v[144:147], a[72:75], v[40:43], v[144:147]\n"
    "v_mfma_f32_16x16x32_bf16 v[184:187], a[72:75], v[44:47], v[184:187]\n"
    "buffer_load_dwordx4 a[72:75], v2, s[40:43], s84 offen offset:128 sc1\n"
    "v_mfma_f32_16x16x32_bf16 v[148:151], a[76:79], v[40:43], v[148:151]\n"
    "v_mfma_f32_16x16x32_bf16 v[188:191], a[76:79], v[44:47], v[188:191]\n"
    "buffer_load_dwordx4 a[76:79], v2, s[40:43], s85 offen offset:128 sc1\n"
    "s_waitcnt vmcnt(35)\n"
    "v_mfma_f32_16x16x32_bf16 v[112:115], a[80:83], v[48:51], v[112:115]\n"
    "s_waitcnt vmcnt(34)\n"
    "v_mfma_f32_16x16x32_bf16 v[152:155], a[80:83], v[52:55], v[152:155]\n"
    "v_mfma_f32_16x16x32_bf16 v[116:119], a[84:87], v[48:51], v[116:119]\n"
    "v_mfma_f32_16x16x32_bf16 v[156:159], a[84:87], v[52:55], v[156:159]\n"
    "v_mfma_f32_16x16x32_bf16 v[120:123], a[88:91], v[48:51], v[120:123]\n"
    "v_mfma_f32_16x16x32_bf16 v[160:163], a[88:91], v[52:55], v[160:163]\n"
    "v_mfma_f32_16x16x32_bf16 v[124:127], a[92:95], v[48:51], v[124:127]\n"
    "v_mfma_f32_16x16x32_bf16 v[164:167], a[92:95], v[52:55], v[164:167]\n"
    "v_mfma_f32_16x16x32_bf16 v[128:131], a[96:99], v[48:51], v[128:131]\n"
    "v_mfma_f32_16x16x32_bf16 v[168:171], a[96:99], v[52:55], v[168:171]\n"
    "v_mfma_f32_16x16x32_bf16 v[132:135], a[100:103], v[48:51], v[132:135]\n"
    "v_mfma_f32_16x16x32_bf16 v[172:175], a[100:103], v[52:55], v[172:175]\n"
    "v_mfma_f32_16x16x32_bf16 v[136:139], a[104:107], v[48:51], v[136:139]\n"
    "v_mfma_f32_16x16x32_bf16 v[176:179], a[104:107], v[52:55], v[176:179]\n"
    "v_mfma_f32_16x16x32_bf16 v[140:143], a[108:111], v[48:51], v[140:143]\n"
    "v_mfma_f32_16x16x32_bf16 v[180:183], a[108:111], v[52:55], v[180:183]\n"
    "v_mfma_f32_16x16x32_bf16 v[144:147], a[112:115], v[48:51], v[144:147]\n"
    "v_mfma_f32_16x16x32_bf16 v[184:187], a[112:115], v[52:55], v[184:187]\n"
    "v_mfma_f32_16x16x32_bf16 v[148:151], a[116:119], v[48:51], v[148:151]\n"
    "v_mfma_f32_16x16x32_bf16 v[188:191], a[116:119], v[52:55], v[188:191]\n"
    "s_waitcnt vmcnt(33)\n"
    "v_mfma_f32_16x16x32_bf16 v[112:115], a[120:123], v[56:59], v[112:115]\n"
    "s_waitcnt vmcnt(32)\n"
    "v_mfma_f32_16x16x32_bf16 v[152:155], a[120:123], v[60:63], v[152:155]\n"
    "buffer_load_dwordx4 v[32:35], v0, s[40:43], s27 offen sc1\n"
    "v_mfma_f32_16x16x32_bf16 v[116:119], a[124:127], v[56:59], v[116:119]\n"
    "v_mfma_f32_16x16x32_bf16 v[156:159], a[124:127], v[60:63], v[156:159]\n"
    "buffer_load_dwordx4 v[36:39], v0, s[40:43], s27 offen offset:1024 sc1\n"
    "v_mfma_f32_16x16x32_bf16 v[120:123], a[128:131], v[56:59], v[120:123]\n"
    "v_mfma_f32_16x16x32_bf16 v[160:163], a[128:131], v[60:63], v[160:163]\n"
    "buffer_load_dwordx4 v[40:43], v0, s[40:43], s28 offen sc1\n"
    "v_mfma_f32_16x16x32_bf16 v[124:127], a[132:135], v[56:59], v[124:127]\n"
    "v_mfma_f32_16x16x32_bf16 v[164:167], a[132:135], v[60:63], v[164:167]\n"
    "buffer_load_dwordx4 v[44:47], v0, s[40:43], s28 offen offset:1024 sc1\n"
    "v_mfma_f32_16x16x32_bf16 v[128:131], a[136:139], v[56:59], v[128:131]\n"
    "v_mfma_f32_16x16x32_bf16 v[168:171], a[136:139], v[60:63], v[168:171]\n"
    "buffer_load_dwordx4 v[48:51], v0, s[40:43], s29 offen sc1\n"
    "v_mfma_f32_16x16x32_bf16 v[132:135], a[140:143], v[56:59], v[132:135]\n"
    "v_mfma_f32_16x16x32_bf16 v[172:175], a[140:143], v[60:63], v[172:175]\n"
    "buffer_load_dwordx4 v[52:55], v0, s[40:43], s29 offen offset:1024 sc1\n"
    "v_mfma_f32_16x16x32_bf16 v[136:139], a[144:147], v[56:59], v[136:139]\n"
    "v_mfma_f32_16x16x32_bf16 v[176:179], a[144:147], v[60:63], v[176:179]\n"
    "v_mfma_f32_16x16x32_bf16 v[140:143], a[148:151], v[56:59], v[140:143]\n"
    "v_mfma_f32_16x16x32_bf16 v[180:183], a[148:151], v[60:63], v[180:183]\n"
    "v_mfma_f32_16x16x32_bf16 v[144:147], a[152:155], v[56:59], v[144:147]\n"
    "v_mfma_f32_16x16x32_bf16 v[184:187], a[152:155], v[60:63], v[184:187]\n"
    "v_mfma_f32_16x16x32_bf16 v[148:151], a[156:159], v[56:59], v[148:151]\n"
    "v_mfma_f32_16x16x32_bf16 v[188:191], a[156:159], v[60:63], v[188:191]\n"
    "s_waitcnt vmcnt(37)\n"
    "v_mfma_f32_16x16x32_bf16 v[112:115], a[160:163], v[64:67], v[112:115]\n"
    "s_waitcnt vmcnt(36)\n"
    "v_mfma_f32_16x16x32_bf16 v[152:155], a[160:163], v[68:71], v[152:155]\n"
    "buffer_load_dwordx4 a[80:83], v1, s[40:43], s30 offen offset:1024 sc1\n"
    "v_mfma_f32_16x16x32_bf16 v[116:119], a[164:167], v[64:67], v[116:119]\n"
    "v_mfma_f32_16x16x32_bf16 v[156:159], a[164:167], v[68:71], v[156:159]\n"
    "buffer_load_dwordx4 a[84:87], v1, s[40:43], s31 offen offset:1024 sc1\n"
    "v_mfma_f32_16x16x32_bf16 v[120:123], a[168:171], v[64:67], v[120:123]\n"
    "v_mfma_f32_16x16x32_bf16 v[160:163], a[168:171], v[68:71], v[160:163]\n"
    "buffer_load_dwordx4 a[88:91], v1, s[40:43], s34 offen offset:1024 sc1\n"
    "v_mfma_f32_16x16x32_bf16 v[124:127], a[172:175], v[64:67], v[124:127]\n"
    "v_mfma_f32_16x16x32_bf16 v[164:167], a[172:175], v[68:71], v[164:167]\n"
    "buffer_load_dwordx4 a[92:95], v1, s[40:43], s35 offen offset:1024 sc1\n"
    "v_mfma_f32_16x16x32_bf16 v[128:131], a[176:179], v[64:67], v[128:131]\n"
    "v_mfma_f32_16x16x32_bf16 v[168:171], a[176:179], v[68:71], v[168:171]\n"
    "buffer_load_dwordx4 a[96:99], v1, s[40:43], s36 offen offset:1024 sc1\n"
    "v_mfma_f32_16x16x32_bf16 v[132:135], a[180:183], v[64:67], v[132:135]\n"
    "v_mfma_f32_16x16x32_bf16 v[172:175], a[180:183], v[68:71], v[172:175]\n"
    "buffer_load_dwordx4 a[100:103], v1, s[40:43], s37 offen offset:1024 sc1\n"
    "v_mfma_f32_16x16x32_bf16 v[136:139], a[184:187], v[64:67], v[136:139]\n"
    "v_mfma_f32_16x16x32_bf16 v[176:179], a[184:187], v[68:71], v[176:179]\n"
    "buffer_load_dwordx4 a[104:107], v1, s[40:43], s38 offen offset:1024 sc1\n"
    "v_mfma_f32_16x16x32_bf16 v[140:143], a[188:191], v[64:67], v[140:143]\n"
    "v_mfma_f32_16x16x32_bf16 v[180:183], a[188:191], v[68:71], v[180:183]\n"
    "buffer_load_dwordx4 a[108:111], v1, s[40:43], s39 offen offset:1024 sc1\n"
    "v_mfma_f32_16x16x32_bf16 v[144:147], a[192:195], v[64:67], v[144:147]\n"
    "v_mfma_f32_16x16x32_bf16 v[184:187], a[192:195], v[68:71], v[184:187]\n"
    "buffer_load_dwordx4 a[112:115], v1, s[40:43], s96 offen offset:1024 sc1\n"
    "v_mfma_f32_16x16x32_bf16 v[148:151], a[196:199], v[64:67], v[148:151]\n"
    "v_mfma_f32_16x16x32_bf16 v[188:191], a[196:199], v[68:71], v[188:191]\n"
    "buffer_load_dwordx4 a[116:119], v1, s[40:43], s97 offen offset:1024 sc1\n"
    "s_waitcnt vmcnt(45)\n"
    "v_mfma_f32_16x16x32_bf16 v[112:115], a[200:203], v[72:75], v[112:115]\n"
    "s_waitcnt vmcnt(44)\n"
    "v_mfma_f32_16x16x32_bf16 v[152:155], a[200:203], v[76:79], v[152:155]\n"
    "buffer_load_dwordx4 a[120:123], v1, s[40:43], s30 offen offset:1152 sc1\n"
    "v_mfma_f32_16x16x32_bf16 v[116:119], a[204:207], v[72:75], v[116:119]\n"
    "v_mfma_f32_16x16x32_bf16 v[156:159], a[204:207], v[76:79], v[156:159]\n"
    "buffer_load_dwordx4 a[124:127], v1, s[40:43], s31 offen offset:1152 sc1\n"
    "v_mfma_f32_16x16x32_bf16 v[120:123], a[208:211], v[72:75], v[120:123]\n"
    "v_mfma_f32_16x16x32_bf16 v[160:163], a[208:211], v[76:79], v[160:163]\n"
    "buffer_load_dwordx4 a[128:131], v1, s[40:43], s34 offen offset:1152 sc1\n"
    "v_mfma_f32_16x16x32_bf16 v[124:127], a[212:215], v[72:75], v[124:127]\n"
    "v_mfma_f32_16x16x32_bf16 v[164:167], a[212:215], v[76:79], v[164:167]\n"
    "buffer_load_dwordx4 a[132:135], v1, s[40:43], s35 offen offset:1152 sc1\n"
    "v_mfma_f32_16x16x32_bf16 v[128:131], a[216:219], v[72:75], v[128:131]\n"
    "v_mfma_f32_16x16x32_bf16 v[168:171], a[216:219], v[76:79], v[168:171]\n"
    "buffer_load_dwordx4 a[136:139], v1, s[40:43], s36 offen offset:1152 sc1\n"
    "v_mfma_f32_16x16x32_bf16 v[132:135], a[220:223], v[72:75], v[132:135]\n"
    "v_mfma_f32_16x16x32_bf16 v[172:175], a[220:223], v[76:79], v[172:175]\n"
    "buffer_load_dwordx4 a[140:143], v1, s[40:43], s37 offen offset:1152 sc1\n"
    "v_mfma_f32_16x16x32_bf16 v[136:139], a[224:227], v[72:75], v[136:139]\n"
    "v_mfma_f32_16x16x32_bf16 v[176:179], a[224:227], v[76:79], v[176:179]\n"
    "buffer_load_dwordx4 a[144:147], v1, s[40:43], s38 offen offset:1152 sc1\n"
    "v_mfma_f32_16x16x32_bf16 v[140:143], a[228:231], v[72:75], v[140:143]\n"
    "v_mfma_f32_16x16x32_bf16 v[180:183], a[228:231], v[76:79], v[180:183]\n"
    "buffer_load_dwordx4 a[148:151], v1, s[40:43], s39 offen offset:1152 sc1\n"
    "v_mfma_f32_16x16x32_bf16 v[144:147], a[232:235], v[72:75], v[144:147]\n"
    "v_mfma_f32_16x16x32_bf16 v[184:187], a[232:235], v[76:79], v[184:187]\n"
    "buffer_load_dwordx4 a[152:155], v1, s[40:43], s96 offen offset:1152 sc1\n"
    "v_mfma_f32_16x16x32_bf16 v[148:151], a[236:239], v[72:75], v[148:151]\n"
    "v_mfma_f32_16x16x32_bf16 v[188:191], a[236:239], v[76:79], v[188:191]\n"
    "buffer_load_dwordx4 a[156:159], v1, s[40:43], s97 offen offset:1152 sc1\n"
    "s_waitcnt vmcnt(53)\n"
    "v_mfma_f32_16x16x32_bf16 v[112:115], a[240:243], v[80:83], v[112:115]\n"
    "s_waitcnt vmcnt(52)\n"
    "v_mfma_f32_16x16x32_bf16 v[152:155], a[240:243], v[84:87], v[152:155]\n"
    "buffer_load_dwordx4 a[160:163], v1, s[40:43], s30 offen offset:2048 sc1\n"
    "v_mfma_f32_16x16x32_bf16 v[116:119], a[244:247], v[80:83], v[116:119]\n"
    "v_mfma_f32_16x16x32_bf16 v[156:159], a[244:247], v[84:87], v[156:159]\n"
    "buffer_load_dwordx4 a[164:167], v1, s[40:43], s31 offen offset:2048 sc1\n"
    "v_mfma_f32_16x16x32_bf16 v[120:123], a[248:251], v[80:83], v[120:123]\n"
    "v_mfma_f32_16x16x32_bf16 v[160:163], a[248:251], v[84:87], v[160:163]\n"
    "buffer_load_dwordx4 a[168:171], v1, s[40:43], s34 offen offset:2048 sc1\n"
    "v_mfma_f32_16x16x32_bf16 v[124:127], a[252:255], v[80:83], v[124:127]\n"
    "v_mfma_f32_16x16x32_bf16 v[164:167], a[252:255], v[84:87], v[164:167]\n"
    "buffer_load_dwordx4 a[172:175], v1, s[40:43], s35 offen offset:2048 sc1\n"
    "v_mfma_f32_16x16x32_bf16 v[128:131], v[192:195], v[80:83], v[128:131]\n"
    "v_mfma_f32_16x16x32_bf16 v[168:171], v[192:195], v[84:87], v[168:171]\n"
    "buffer_load_dwordx4 a[176:179], v1, s[40:43], s36 offen offset:2048 sc1\n"
    "v_mfma_f32_16x16x32_bf16 v[132:135], v[196:199], v[80:83], v[132:135]\n"
    "v_mfma_f32_16x16x32_bf16 v[172:175], v[196:199], v[84:87], v[172:175]\n"
    "buffer_load_dwordx4 a[180:183], v1, s[40:43], s37 offen offset:2048 sc1\n"
    "v_mfma_f32_16x16x32_bf16 v[136:139], v[200:203], v[80:83], v[136:139]\n"
    "v_mfma_f32_16x16x32_bf16 v[176:179], v[200:203], v[84:87], v[176:179]\n"
    "buffer_load_dwordx4 a[184:187], v1, s[40:43], s38 offen offset:2048 sc1\n"
    "v_mfma_f32_16x16x32_bf16 v[140:143], v[204:207], v[80:83], v[140:143]\n"
    "v_mfma_f32_16x16x32_bf16 v[180:183], v[204:207], v[84:87], v[180:183]\n"
    "buffer_load_dwordx4 a[188:191], v1, s[40:43], s39 offen offset:2048 sc1\n"
    "v_mfma_f32_16x16x32_bf16 v[144:147], v[208:211], v[80:83], v[144:147]\n"
    "v_mfma_f32_16x16x32_bf16 v[184:187], v[208:211], v[84:87], v[184:187]\n"
    "buffer_load_dwordx4 a[192:195], v1, s[40:43], s96 offen offset:2048 sc1\n"
    "v_mfma_f32_16x16x32_bf16 v[148:151], v[212:215], v[80:83], v[148:151]\n"
    "v_mfma_f32_16x16x32_bf16 v[188:191], v[212:215], v[84:87], v[188:191]\n"
    "buffer_load_dwordx4 a[196:199], v1, s[40:43], s97 offen offset:2048 sc1\n"
    "s_waitcnt vmcnt(61)\n"
    "v_mfma_f32_16x16x32_bf16 v[112:115], v[216:219], v[88:91], v[112:115]\n"
    "s_waitcnt vmcnt(60)\n"
    "v_mfma_f32_16x16x32_bf16 v[152:155], v[216:219], v[92:95], v[152:155]\n"
    "buffer_load_dwordx4 a[200:203], v1, s[40:43], s30 offen offset:2176 sc1\n"
    "v_mfma_f32_16x16x32_bf16 v[116:119], v[220:223], v[88:91], v[116:119]\n"
    "v_mfma_f32_16x16x32_bf16 v[156:159], v[220:223], v[92:95], v[156:159]\n"
    "buffer_load_dwordx4 a[204:207], v1, s[40:43], s31 offen offset:2176 sc1\n"
    "v_mfma_f32_16x16x32_bf16 v[120:123], v[224:227], v[88:91], v[120:123]\n"
    "v_mfma_f32_16x16x32_bf16 v[160:163], v[224:227], v[92:95], v[160:163]\n"
    "buffer_load_dwordx4 a[208:211], v1, s[40:43], s34 offen offset:2176 sc1\n"
    "s_waitcnt vmcnt(61)\n"
    "v_mfma_f32_16x16x32_bf16 v[124:127], v[228:231], v[88:91], v[124:127]\n"
    "v_mfma_f32_16x16x32_bf16 v[164:167], v[228:231], v[92:95], v[164:167]\n"
    "buffer_load_dwordx4 a[212:215], v1, s[40:43], s35 offen offset:2176 sc1\n"
    "v_mfma_f32_16x16x32_bf16 v[128:131], v[232:235], v[88:91], v[128:131]\n"
    "v_mfma_f32_16x16x32_bf16 v[168:171], v[232:235], v[92:95], v[168:171]\n"
    "buffer_load_dwordx4 a[216:219], v1, s[40:43], s36 offen offset:2176 sc1\n"
    "s_waitcnt vmcnt(61)\n"
    "v_mfma_f32_16x16x32_bf16 v[132:135], v[236:239], v[88:91], v[132:135]\n"
    "v_mfma_f32_16x16x32_bf16 v[172:175], v[236:239], v[92:95], v[172:175]\n"
    "buffer_load_dwordx4 a[220:223], v1, s[40:43], s37 offen offset:2176 sc1\n"
    "v_mfma_f32_16x16x32_bf16 v[136:139], v[240:243], v[88:91], v[136:139]\n"
    "v_mfma_f32_16x16x32_bf16 v[176:179], v[240:243], v[92:95], v[176:179]\n"
    "buffer_load_dwordx4 a[224:227], v1, s[40:43], s38 offen offset:2176 sc1\n"
    "s_waitcnt vmcnt(61)\n"
    "v_mfma_f32_16x16x32_bf16 v[140:143], v[244:247], v[88:91], v[140:143]\n"
    "v_mfma_f32_16x16x32_bf16 v[180:183], v[244:247], v[92:95], v[180:183]\n"
    "buffer_load_dwordx4 a[228:231], v1, s[40:43], s39 offen offset:2176 sc1\n"
    "v_mfma_f32_16x16x32_bf16 v[144:147], v[248:251], v[88:91], v[144:147]\n"
    "v_mfma_f32_16x16x32_bf16 v[184:187], v[248:251], v[92:95], v[184:187]\n"
    "buffer_load_dwordx4 a[232:235], v1, s[40:43], s96 offen offset:2176 sc1\n"
    "s_waitcnt vmcnt(61)\n"
    "v_mfma_f32_16x16x32_bf16 v[148:151], v[252:255], v[88:91], v[148:151]\n"
    "v_mfma_f32_16x16x32_bf16 v[188:191], v[252:255], v[92:95], v[188:191]\n"
    "buffer_load_dwordx4 a[236:239], v1, s[40:43], s97 offen offset:2176 sc1\n"
    "v_mfma_f32_16x16x32_bf16 v[112:115], a[0:3], v[96:99], v[112:115]\n"
    "v_mfma_f32_16x16x32_bf16 v[152:155], a[0:3], v[100:103], v[152:155]\n"
    "v_mfma_f32_16x16x32_bf16 v[116:119], a[4:7], v[96:99], v[116:119]\n"
    "v_mfma_f32_16x16x32_bf16 v[156:159], a[4:7], v[100:103], v[156:159]\n"
    "v_mfma_f32_16x16x32_bf16 v[120:123], a[8:11], v[96:99], v[120:123]\n"
    "v_mfma_f32_16x16x32_bf16 v[160:163], a[8:11], v[100:103], v[160:163]\n"
    "v_mfma_f32_16x16x32_bf16 v[124:127], a[12:15], v[96:99], v[124:127]\n"
    "v_mfma_f32_16x16x32_bf16 v[164:167], a[12:15], v[100:103], v[164:167]\n"
    "s_waitcnt vmcnt(61)\n"
    "v_mfma_f32_16x16x32_bf16 v[128:131], a[16:19], v[96:99], v[128:131]\n"
    "v_mfma_f32_16x16x32_bf16 v[168:171], a[16:19], v[100:103], v[168:171]\n"
    "s_waitcnt vmcnt(60)\n"
    "v_mfma_f32_16x16x32_bf16 v[132:135], a[20:23], v[96:99], v[132:135]\n"
    "v_mfma_f32_16x16x32_bf16 v[172:175], a[20:23], v[100:103], v[172:175]\n"
    "s_waitcnt vmcnt(59)\n"
    "v_mfma_f32_16x16x32_bf16 v[136:139], a[24:27], v[96:99], v[136:139]\n"
    "v_mfma_f32_16x16x32_bf16 v[176:179], a[24:27], v[100:103], v[176:179]\n"
    "s_waitcnt vmcnt(58)\n"
    "v_mfma_f32_16x16x32_bf16 v[140:143], a[28:31], v[96:99], v[140:143]\n"
    "v_mfma_f32_16x16x32_bf16 v[180:183], a[28:31], v[100:103], v[180:183]\n"
    "s_waitcnt vmcnt(57)\n"
    "v_mfma_f32_16x16x32_bf16 v[144:147], a[32:35], v[96:99], v[144:147]\n"
    "v_mfma_f32_16x16x32_bf16 v[184:187], a[32:35], v[100:103], v[184:187]\n"
    "s_waitcnt vmcnt(56)\n"
    "v_mfma_f32_16x16x32_bf16 v[148:151], a[36:39], v[96:99], v[148:151]\n"
    "v_mfma_f32_16x16x32_bf16 v[188:191], a[36:39], v[100:103], v[188:191]\n"
    "s_waitcnt vmcnt(55)\n"
    "v_mfma_f32_16x16x32_bf16 v[112:115], a[40:43], v[104:107], v[112:115]\n"
    "v_mfma_f32_16x16x32_bf16 v[152:155], a[40:43], v[108:111], v[152:155]\n"
    "s_waitcnt vmcnt(54)\n"
    "v_mfma_f32_16x16x32_bf16 v[116:119], a[44:47], v[104:107], v[116:119]\n"
    "v_mfma_f32_16x16x32_bf16 v[156:159], a[44:47], v[108:111], v[156:159]\n"
    "s_waitcnt vmcnt(53)\n"
    "v_mfma_f32_16x16x32_bf16 v[120:123], a[48:51], v[104:107], v[120:123]\n"
    "v_mfma_f32_16x16x32_bf16 v[160:163], a[48:51], v[108:111], v[160:163]\n"
    "s_waitcnt vmcnt(52)\n"
    "v_mfma_f32_16x16x32_bf16 v[124:127], a[52:55], v[104:107], v[124:127]\n"
    "v_mfma_f32_16x16x32_bf16 v[164:167], a[52:55], v[108:111], v[164:167]\n"
    "s_waitcnt vmcnt(51)\n"
    "v_mfma_f32_16x16x32_bf16 v[128:131], a[56:59], v[104:107], v[128:131]\n"
    "v_mfma_f32_16x16x32_bf16 v[168:171], a[56:59], v[108:111], v[168:171]\n"
    "s_waitcnt vmcnt(50)\n"
    "v_mfma_f32_16x16x32_bf16 v[132:135], a[60:63], v[104:107], v[132:135]\n"
    "v_mfma_f32_16x16x32_bf16 v[172:175], a[60:63], v[108:111], v[172:175]\n"
    "s_waitcnt vmcnt(49)\n"
    "v_mfma_f32_16x16x32_bf16 v[136:139], a[64:67], v[104:107], v[136:139]\n"
    "v_mfma_f32_16x16x32_bf16 v[176:179], a[64:67], v[108:111], v[176:179]\n"
    "s_waitcnt vmcnt(48)\n"
    "v_mfma_f32_16x16x32_bf16 v[140:143], a[68:71], v[104:107], v[140:143]\n"
    "v_mfma_f32_16x16x32_bf16 v[180:183], a[68:71], v[108:111], v[180:183]\n"
    "s_waitcnt vmcnt(47)\n"
    "v_mfma_f32_16x16x32_bf16 v[144:147], a[72:75], v[104:107], v[144:147]\n"
    "v_mfma_f32_16x16x32_bf16 v[184:187], a[72:75], v[108:111], v[184:187]\n"
    "s_waitcnt vmcnt(46)\n"
    "v_mfma_f32_16x16x32_bf16 v[148:151], a[76:79], v[104:107], v[148:151]\n"
    "v_mfma_f32_16x16x32_bf16 v[188:191], a[76:79], v[108:111], v[188:191]\n"
    "ds_write_b128 v10, v[112:115] offset:0\n"
    "ds_write_b128 v10, v[116:119] offset:1024\n"
    "ds_write_b128 v10, v[120:123] offset:2048\n"
    "ds_write_b128 v10, v[124:127] offset:3072\n"
    "ds_write_b128 v10, v[128:131] offset:4096\n"
    "ds_write_b128 v10, v[132:135] offset:5120\n"
    "ds_write_b128 v10, v[136:139] offset:6144\n"
    "ds_write_b128 v10, v[140:143] offset:7168\n"
    "ds_write_b128 v10, v[144:147] offset:8192\n"
    "ds_write_b128 v10, v[148:151] offset:9216\n"
    "ds_write_b128 v10, v[152:155] offset:10240\n"
    "ds_write_b128 v10, v[156:159] offset:11264\n"
    "ds_write_b128 v10, v[160:163] offset:12288\n"
    "ds_write_b128 v10, v[164:167] offset:13312\n"
    "ds_write_b128 v10, v[168:171] offset:14336\n"
    "ds_write_b128 v10, v[172:175] offset:15360\n"
    "s_waitcnt lgkmcnt(14)\n"
    "ds_write_b128 v10, v[176:179] offset:16384\n"
    "ds_write_b128 v10, v[180:183] offset:17408\n"
    "s_waitcnt lgkmcnt(14)\n"
    "ds_write_b128 v10, v[184:187] offset:18432\n"
    "ds_write_b128 v10, v[188:191] offset:19456\n"
    "s_waitcnt lgkmcnt(14)\n"
    "s_waitcnt lgkmcnt(0)\n"
    "s_barrier\n"
    "ds_read_b128 v[112:115], v11 offset:0\n"
    "ds_read_b128 v[116:119], v11 offset:20480\n"
    "ds_read_b128 v[120:123], v11 offset:40960\n"
    "ds_read_b128 v[124:127], v11 offset:61440\n"
    "ds_read_b128 v[128:131], v11 offset:1024\n"
    "ds_read_b128 v[132:135], v11 offset:21504\n"
    "ds_read_b128 v[136:139], v11 offset:41984\n"
    "ds_read_b128 v[140:143], v11 offset:62464\n"
    "ds_read_b128 v[144:147], v12 offset:0\n"
    "ds_read_b128 v[148:151], v12 offset:20480\n"
    "ds_read_b128 v[152:155], v12 offset:40960\n"
    "ds_read_b128 v[156:159], v12 offset:61440\n"
    "ds_read_b128 v[160:163], v12 offset:1024\n"
    "ds_read_b128 v[164:167], v12 offset:21504\n"
    "ds_read_b128 v[168:171], v12 offset:41984\n"
    "ds_read_b128 v[172:175], v12 offset:62464\n"
    "s_waitcnt lgkmcnt(14)\n"
    "ds_read_b128 v[176:179], v13 offset:0\n"
    "ds_read_b128 v[180:183], v13 offset:20480\n"
    "s_waitcnt lgkmcnt(14)\n"
    "ds_read_b128 v[184:187], v13 offset:40960\n"
    "ds_read_b128 v[188:191], v13 offset:61440\n"
    "s_waitcnt lgkmcnt(14)\n"
    "ds_read_b128 v[192:195], v13 offset:1024\n"
    "ds_read_b128 v[196:199], v13 offset:21504\n"
    "s_waitcnt lgkmcnt(14)\n"
    "ds_read_b128 v[200:203], v13 offset:41984\n"
    "ds_read_b128 v[204:207], v13 offset:62464\n"
    "s_waitcnt lgkmcnt(14)\n"
    "v_pk_add_f32 v[112:113], v[112:113], v[116:117]\n"
    "v_pk_add_f32 v[120:121], v[120:121], v[124:125]\n"
    "s_waitcnt vmcnt(45)\n"
    "v_pk_add_f32 v[32:33], v[32:33], v[112:113]\n"
    "v_pk_add_f32 v[32:33], v[32:33], v[120:121]\n"
    "v_pk_add_f32 v[114:115], v[114:115], v[118:119]\n"
    "v_pk_add_f32 v[122:123], v[122:123], v[126:127]\n"
    "v_pk_add_f32 v[34:35], v[34:35], v[114:115]\n"
    "v_pk_add_f32 v[34:35], v[34:35], v[122:123]\n"
    "v_pk_add_f32 v[128:129], v[128:129], v[132:133]\n"
    "v_pk_add_f32 v[136:137], v[136:137], v[140:141]\n"
    "s_waitcnt vmcnt(44)\n"
    "v_pk_add_f32 v[36:37], v[36:37], v[128:129]\n"
    "v_pk_add_f32 v[36:37], v[36:37], v[136:137]\n"
    "v_pk_add_f32 v[130:131], v[130:131], v[134:135]\n"
    "v_pk_add_f32 v[138:139], v[138:139], v[142:143]\n"
    "v_pk_add_f32 v[38:39], v[38:39], v[130:131]\n"
    "v_pk_add_f32 v[38:39], v[38:39], v[138:139]\n"
    "s_add_u32 s48, s24, 138346496\n"
    "s_lshr_b32 s49, s24, 1\n"
    "s_add_u32 s49, s49, 143589376\n"
    "v_cvt_pk_bf16_f32 v64, v32, v33\n"
    "v_cvt_pk_bf16_f32 v65, v34, v35\n"
    "v_cvt_pk_bf16_f32 v66, v36, v37\n"
    "v_cvt_pk_bf16_f32 v67, v38, v39\n"
    "buffer_store_dwordx4 v[64:67], v0, s[40:43], s49 offen sc1\n"
    "buffer_store_dwordx4 v[32:35], v0, s[40:43], s48 offen sc1\n"
    "buffer_store_dwordx4 v[36:39], v0, s[40:43], s48 offen offset:1024 sc1\n"
    "v_pk_add_f32 v[144:145], v[144:145], v[148:149]\n"
    "s_waitcnt lgkmcnt(12)\n"
    "v_pk_add_f32 v[152:153], v[152:153], v[156:157]\n"
    "s_waitcnt vmcnt(46)\n"
    "v_pk_add_f32 v[40:41], v[40:41], v[144:145]\n"
    "v_pk_add_f32 v[40:41], v[40:41], v[152:153]\n"
    "v_pk_add_f32 v[146:147], v[146:147], v[150:151]\n"
    "v_pk_add_f32 v[154:155], v[154:155], v[158:159]\n"
    "v_pk_add_f32 v[42:43], v[42:43], v[146:147]\n"
    "v_pk_add_f32 v[42:43], v[42:43], v[154:155]\n"
    "s_waitcnt lgkmcnt(10)\n"
    "v_pk_add_f32 v[160:161], v[160:161], v[164:165]\n"
    "s_waitcnt lgkmcnt(8)\n"
    "v_pk_add_f32 v[168:169], v[168:169], v[172:173]\n"
    "s_waitcnt vmcnt(45)\n"
    "v_pk_add_f32 v[44:45], v[44:45], v[160:161]\n"
    "v_pk_add_f32 v[44:45], v[44:45], v[168:169]\n"
    "v_pk_add_f32 v[162:163], v[162:163], v[166:167]\n"
    "v_pk_add_f32 v[170:171], v[170:171], v[174:175]\n"
    "v_pk_add_f32 v[46:47], v[46:47], v[162:163]\n"
    "v_pk_add_f32 v[46:47], v[46:47], v[170:171]\n"
    "s_add_u32 s48, s25, 138346496\n"
    "s_lshr_b32 s49, s25, 1\n"
    "s_add_u32 s49, s49, 143589376\n"
    "v_cvt_pk_bf16_f32 v68, v40, v41\n"
    "v_cvt_pk_bf16_f32 v69, v42, v43\n"
    "v_cvt_pk_bf16_f32 v70, v44, v45\n"
    "v_cvt_pk_bf16_f32 v71, v46, v47\n"
    "buffer_store_dwordx4 v[68:71], v0, s[40:43], s49 offen sc1\n"
    "buffer_store_dwordx4 v[40:43], v0, s[40:43], s48 offen sc1\n"
    "buffer_store_dwordx4 v[44:47], v0, s[40:43], s48 offen offset:1024 sc1\n"
    "s_waitcnt lgkmcnt(6)\n"
    "v_pk_add_f32 v[176:177], v[176:177], v[180:181]\n"
    "s_waitcnt lgkmcnt(4)\n"
    "v_pk_add_f32 v[184:185], v[184:185], v[188:189]\n"
    "s_waitcnt vmcnt(47)\n"
    "v_pk_add_f32 v[48:49], v[48:49], v[176:177]\n"
    "v_pk_add_f32 v[48:49], v[48:49], v[184:185]\n"
    "v_pk_add_f32 v[178:179], v[178:179], v[182:183]\n"
    "v_pk_add_f32 v[186:187], v[186:187], v[190:191]\n"
    "v_pk_add_f32 v[50:51], v[50:51], v[178:179]\n"
    "v_pk_add_f32 v[50:51], v[50:51], v[186:187]\n"
    "s_waitcnt lgkmcnt(2)\n"
    "v_pk_add_f32 v[192:193], v[192:193], v[196:197]\n"
    "s_waitcnt lgkmcnt(0)\n"
    "v_pk_add_f32 v[200:201], v[200:201], v[204:205]\n"
    "s_waitcnt vmcnt(46)\n"
    "v_pk_add_f32 v[52:53], v[52:53], v[192:193]\n"
    "v_pk_add_f32 v[52:53], v[52:53], v[200:201]\n"
    "v_pk_add_f32 v[194:195], v[194:195], v[198:199]\n"
    "v_pk_add_f32 v[202:203], v[202:203], v[206:207]\n"
    "v_pk_add_f32 v[54:55], v[54:55], v[194:195]\n"
    "v_pk_add_f32 v[54:55], v[54:55], v[202:203]\n"
    "s_add_u32 s48, s26, 138346496\n"
    "s_lshr_b32 s49, s26, 1\n"
    "s_add_u32 s49, s49, 143589376\n"
    "v_cvt_pk_bf16_f32 v72, v48, v49\n"
    "v_cvt_pk_bf16_f32 v73, v50, v51\n"
    "v_cvt_pk_bf16_f32 v74, v52, v53\n"
    "v_cvt_pk_bf16_f32 v75, v54, v55\n"
    "buffer_store_dwordx4 v[72:75], v0, s[40:43], s49 offen sc1\n"
    "buffer_store_dwordx4 v[48:51], v0, s[40:43], s48 offen sc1\n"
    "buffer_store_dwordx4 v[52:55], v0, s[40:43], s48 offen offset:1024 sc1\n"
    "s_lshr_b32 s54, s63, 1\n"
    "s_lshl_b32 s54, s54, 7\n"
    "s_add_u32 s54, s54, 2354176\n"
    "buffer_load_dwordx4 a[0:3], v1, s[40:43], s30 offen sc1\n"
    "buffer_load_dwordx4 a[4:7], v1, s[40:43], s31 offen sc1\n"
    "buffer_load_dwordx4 a[8:11], v1, s[40:43], s34 offen sc1\n"
    "buffer_load_dwordx4 a[12:15], v1, s[40:43], s35 offen sc1\n"
    "buffer_load_dwordx4 a[16:19], v1, s[40:43], s36 offen sc1\n"
    "buffer_load_dwordx4 a[20:23], v1, s[40:43], s37 offen sc1\n"
    "buffer_load_dwordx4 a[24:27], v1, s[40:43], s38 offen sc1\n"
    "buffer_load_dwordx4 a[28:31], v1, s[40:43], s39 offen sc1\n"
    "buffer_load_dwordx4 a[32:35], v1, s[40:43], s96 offen sc1\n"
    "buffer_load_dwordx4 a[36:39], v1, s[40:43], s97 offen sc1\n"
    "buffer_load_dwordx4 a[40:43], v1, s[40:43], s30 offen offset:128 sc1\n"
    "buffer_load_dwordx4 a[44:47], v1, s[40:43], s31 offen offset:128 sc1\n"
    "buffer_load_dwordx4 a[48:51], v1, s[40:43], s34 offen offset:128 sc1\n"
    "buffer_load_dwordx4 a[52:55], v1, s[40:43], s35 offen offset:128 sc1\n"
    "buffer_load_dwordx4 a[56:59], v1, s[40:43], s36 offen offset:128 sc1\n"
    "buffer_load_dwordx4 a[60:63], v1, s[40:43], s37 offen offset:128 sc1\n"
    "buffer_load_dwordx4 a[64:67], v1, s[40:43], s38 offen offset:128 sc1\n"
    "buffer_load_dwordx4 a[68:71], v1, s[40:43], s39 offen offset:128 sc1\n"
    "buffer_load_dwordx4 a[72:75], v1, s[40:43], s96 offen offset:128 sc1\n"
    "buffer_load_dwordx4 a[76:79], v1, s[40:43], s97 offen offset:128 sc1\n"
    "buffer_load_dwordx4 a[240:243], v1, s[40:43], s30 offen offset:3072 sc1\n"
    "buffer_load_dwordx4 a[244:247], v1, s[40:43], s31 offen offset:3072 sc1\n"
    "buffer_load_dwordx4 a[248:251], v1, s[40:43], s34 offen offset:3072 sc1\n"
    "buffer_load_dwordx4 a[252:255], v1, s[40:43], s35 offen offset:3072 sc1\n"
    "buffer_load_dwordx4 v[192:195], v1, s[40:43], s36 offen offset:3072 sc1\n"
    "buffer_load_dwordx4 v[196:199], v1, s[40:43], s37 offen offset:3072 sc1\n"
    "buffer_load_dwordx4 v[200:203], v1, s[40:43], s38 offen offset:3072 sc1\n"
    "buffer_load_dwordx4 v[204:207], v1, s[40:43], s39 offen offset:3072 sc1\n"
    "buffer_load_dwordx4 v[208:211], v1, s[40:43], s96 offen offset:3072 sc1\n"
    "buffer_load_dwordx4 v[212:215], v1, s[40:43], s97 offen offset:3072 sc1\n"
    "buffer_load_dwordx4 v[216:219], v1, s[40:43], s30 offen offset:3200 sc1\n"
    "buffer_load_dwordx4 v[220:223], v1, s[40:43], s31 offen offset:3200 sc1\n"
    "buffer_load_dwordx4 v[224:227], v1, s[40:43], s34 offen offset:3200 sc1\n"
    "buffer_load_dwordx4 v[228:231], v1, s[40:43], s35 offen offset:3200 sc1\n"
    "buffer_load_dwordx4 v[232:235], v1, s[40:43], s36 offen offset:3200 sc1\n"
    "buffer_load_dwordx4 v[236:239], v1, s[40:43], s37 offen offset:3200 sc1\n"
    "buffer_load_dwordx4 v[240:243], v1, s[40:43], s38 offen offset:3200 sc1\n"
    "buffer_load_dwordx4 v[244:247], v1, s[40:43], s39 offen offset:3200 sc1\n"
    "buffer_load_dwordx4 v[248:251], v1, s[40:43], s96 offen offset:3200 sc1\n"
    "buffer_load_dwordx4 v[252:255], v1, s[40:43], s97 offen offset:3200 sc1\n"
    "s_waitcnt vmcnt(40)\n"
    "s_waitcnt lgkmcnt(0)\n"
    "s_barrier\n"
    "buffer_atomic_add v6, v3, s[40:43], s54 offen\n"
    "s_add_u32 s63, s63, 4\n"
    "s_cmp_lt_u32 s47, 60\n"
    "s_cbranch_scc1 tk_loop_%=\n"
    "s_cmp_lt_u32 s63, 128\n"
    "s_cbranch_scc1 tk_first_%=\n"
    "s_branch tk_end_%=\n"
    "tk_abort_%=:\n"
    "v_mov_b32 v4, 1\n"
    "v_mov_b32 v5, 1024\n"
    "buffer_store_dword v4, v5, s[40:43], s58 offen sc1\n"
    "tk_end_%=:\n"
    "s_endpgm\n"
    :: CHAIN_INPUTS : CHAIN_CLOBBERS);
    __builtin_unreachable();
}
DEV void role_helper(unsigned lo, unsigned hi, int id, int w, int aux, const Tune& tn, const void* wa, const void* wb, int bid) {
    asm volatile(
    "s_mov_b32 s40, %0\n"
    "s_mov_b32 s41, %1\n"
    "s_mov_b32 s42, %2\n"
    "s_mov_b32 s43, %3\n"
    "s_mov_b32 s44, %4\n"
    "s_mov_b32 s45, %5\n"
    "s_mov_b32 s46, %6\n"
    "s_mov_b32 s66, %7\n"
    "s_mov_b32 s67, %8\n"
    "s_mov_b32 s68, %9\n"
    "s_mov_b32 s69, %10\n"
    "s_mov_b32 s70, %11\n"
    "s_mov_b32 s71, %12\n"
    "s_mov_b32 s72, %13\n"
    "s_mov_b32 s73, %14\n"
    "s_mov_b32 s76, %15\n"
    "s_mov_b32 s77, %16\n"
    "s_mov_b32 s78, 7864320\n"
    "s_mov_b32 s79, 0x20000\n"
    "s_mov_b32 s80, %17\n"
    "s_mov_b32 s81, %18\n"
    "s_mov_b32 s82, 7864320\n"
    "s_mov_b32 s83, 0x20000\n"
    "s_mov_b32 s84, %19\n"
    "s_mov_b64 exec, -1\n"
    "v_mbcnt_lo_u32_b32 v15, -1, 0\n"
    "v_mbcnt_hi_u32_b32 v15, -1, v15\n"
    "v_lshlrev_b32 v0, 4, v15\n"
    "s_mov_b32 s58, 2097152\n"
    "s_mov_b32 s92, 0\n"
    "s_mov_b32 s21, 0\n"
    "s_mov_b32 s22, 0x3fb8aa3b\n"
    "s_mov_b32 s23, s22\n"
    "s_mov_b32 s24, 0xbfb8aa3b\n"
    "s_mov_b32 s25, s24\n"
    "s_cmp_eq_u32 s46, 3\n"
    "s_cbranch_scc1 hp_ldo_%=\n"
    "s_cmp_lg_u32 s46, 0\n"
    "s_cbranch_scc1 hp_ldw_%=\n"
    "s_lshr_b32 s85, s84, 2\n"
    "s_and_b32 s86, s84, 3\n"
    "v_add_u32 v26, 0, v15\n"
    "v_mul_u32_u24 v27, 2731, v26\n"
    "v_lshrrev_b32 v27, 16, v27\n"
    "v_mul_u32_u24 v16, 11904, v27\n"
    "v_lshl_add_u32 v16, v26, 4, v16\n"
    "v_add_u32 v26, v26, v27\n"
    "s_mul_i32 s48, s45, 3200\n"
    "v_lshl_add_u32 v19, v26, 4, s48\n"
    "v_add_u32 v26, 64, v15\n"
    "v_mul_u32_u24 v27, 2731, v26\n"
    "v_lshrrev_b32 v27, 16, v27\n"
    "v_mul_u32_u24 v17, 11904, v27\n"
    "v_lshl_add_u32 v17, v26, 4, v17\n"
    "v_add_u32 v26, v26, v27\n"
    "s_mul_i32 s48, s45, 3200\n"
    "v_lshl_add_u32 v20, v26, 4, s48\n"
    "v_add_u32 v26, 128, v15\n"
    "v_mul_u32_u24 v27, 2731, v26\n"
    "v_lshrrev_b32 v27, 16, v27\n"
    "v_mul_u32_u24 v18, 11904, v27\n"
    "v_lshl_add_u32 v18, v26, 4, v18\n"
    "v_add_u32 v26, v26, v27\n"
    "s_mul_i32 s48, s45, 3200\n"
    "v_lshl_add_u32 v21, v26, 4, s48\n"
    "s_mul_i32 s48, s86, 160\n"
    "s_lshl3_add_u32 s48, s45, s48\n"
    "s_mul_i32 s48, s48, 12288\n"
    "s_mul_i32 s49, s85, 384\n"
    "s_add_u32 s48, s48, s49\n"
    "buffer_load_dwordx4 v[32:35], v16, s[80:83], s48 offen\n"
    "buffer_load_dwordx4 v[36:39], v17, s[80:83], s48 offen\n"
    "buffer_load_dwordx4 v[40:43], v18, s[80:83], s48 offen\n"
    "s_add_u32 s48, s48, 393216\n"
    "buffer_load_dwordx4 v[44:47], v16, s[80:83], s48 offen\n"
    "buffer_load_dwordx4 v[48:51], v17, s[80:83], s48 offen\n"
    "buffer_load_dwordx4 v[52:55], v18, s[80:83], s48 offen\n"
    "s_add_u32 s48, s48, 393216\n"
    "buffer_load_dwordx4 v[56:59], v16, s[80:83], s48 offen\n"
    "buffer_load_dwordx4 v[60:63], v17, s[80:83], s48 offen\n"
    "buffer_load_dwordx4 v[64:67], v18, s[80:83], s48 offen\n"
    "s_add_u32 s48, s48, 393216\n"
    "buffer_load_dwordx4 v[68:71], v16, s[80:83], s48 offen\n"
    "buffer_load_dwordx4 v[72:75], v17, s[80:83], s48 offen\n"
    "buffer_load_dwordx4 v[76:79], v18, s[80:83], s48 offen\n"
    "s_add_u32 s48, s48, 393216\n"
    "buffer_load_dwordx4 v[80:83], v16, s[80:83], s48 offen\n"
    "buffer_load_dwordx4 v[84:87], v17, s[80:83], s48 offen\n"
    "buffer_load_dwordx4 v[88:91], v18, s[80:83], s48 offen\n"
    "s_add_u32 s48, s48, 393216\n"
    "v_lshrrev_b32 v28, 2, v15\n"
    "v_mul_u32_u24 v28, 1280, v28\n"
    "v_and_b32 v29, 3, v15\n"
    "v_lshl_add_u32 v28, v29, 3, v28\n"
    "s_mul_i32 s48, s45, 320\n"
    "v_add_u32 v28, s48, v28\n"
    "v_add_u32 v29, 40960, v28\n"
    "v_add_u32 v30, 81920, v28\n"
    "s_mul_i32 s51, s45, 10240\n"
    "s_mul_i32 s50, s85, 245760\n"
    "s_add_u32 s50, s50, s51\n"
    "s_add_u32 m0, s51, 0\n"
    "s_add_u32 s49, s50, 0\n"
    "buffer_load_dwordx4 v0, s[76:79], s49 offen offset:0 lds\n"
    "buffer_load_dwordx4 v0, s[76:79], s49 offen offset:1024 lds\n"
    "buffer_load_dwordx4 v0, s[76:79], s49 offen offset:2048 lds\n"
    "buffer_load_dwordx4 v0, s[76:79], s49 offen offset:3072 lds\n"
    "s_add_u32 m0, s51, 4096\n"
    "s_add_u32 s49, s50, 4096\n"
    "buffer_load_dwordx4 v0, s[76:79], s49 offen offset:0 lds\n"
    "buffer_load_dwordx4 v0, s[76:79], s49 offen offset:1024 lds\n"
    "buffer_load_dwordx4 v0, s[76:79], s49 offen offset:2048 lds\n"
    "buffer_load_dwordx4 v0, s[76:79], s49 offen offset:3072 lds\n"
    "s_add_u32 m0, s51, 8192\n"
    "s_add_u32 s49, s50, 8192\n"
    "buffer_load_dwordx4 v0, s[76:79], s49 offen offset:0 lds\n"
    "buffer_load_dwordx4 v0, s[76:79], s49 offen offset:1024 lds\n"
    "s_add_u32 m0, s51, 40960\n"
    "s_add_u32 s49, s50, 40960\n"
    "buffer_load_dwordx4 v0, s[76:79], s49 offen offset:0 lds\n"
    "buffer_load_dwordx4 v0, s[76:79], s49 offen offset:1024 lds\n"
    "buffer_load_dwordx4 v0, s[76:79], s49 offen offset:2048 lds\n"
    "buffer_load_dwordx4 v0, s[76:79], s49 offen offset:3072 lds\n"
    "s_add_u32 m0, s51, 45056\n"
    "s_add_u32 s49, s50, 45056\n"
    "buffer_load_dwordx4 v0, s[76:79], s49 offen offset:0 lds\n"
    "buffer_load_dwordx4 v0, s[76:79], s49 offen offset:1024 lds\n"
    "buffer_load_dwordx4 v0, s[76:79], s49 offen offset:2048 lds\n"
    "buffer_load_dwordx4 v0, s[76:79], s49 offen offset:3072 lds\n"
    "s_add_u32 m0, s51, 49152\n"
    "s_add_u32 s49, s50, 49152\n"
    "buffer_load_dwordx4 v0, s[76:79], s49 offen offset:0 lds\n"
    "buffer_load_dwordx4 v0, s[76:79], s49 offen offset:1024 lds\n"
    "s_waitcnt vmcnt(10) lgkmcnt(0)\n"
    "s_barrier\n"
    "s_add_u32 m0, s51, 81920\n"
    "s_add_u32 s49, s50, 81920\n"
    "buffer_load_dwordx4 v0, s[76:79], s49 offen offset:0 lds\n"
    "buffer_load_dwordx4 v0, s[76:79], s49 offen offset:1024 lds\n"
    "buffer_load_dwordx4 v0, s[76:79], s49 offen offset:2048 lds\n"
    "buffer_load_dwordx4 v0, s[76:79], s49 offen offset:3072 lds\n"
    "s_add_u32 m0, s51, 86016\n"
    "s_add_u32 s49, s50, 86016\n"
    "buffer_load_dwordx4 v0, s[76:79], s49 offen offset:0 lds\n"
    "buffer_load_dwordx4 v0, s[76:79], s49 offen offset:1024 lds\n"
    "buffer_load_dwordx4 v0, s[76:79], s49 offen offset:2048 lds\n"
    "buffer_load_dwordx4 v0, s[76:79], s49 offen offset:3072 lds\n"
    "s_add_u32 m0, s51, 90112\n"
    "s_add_u32 s49, s50, 90112\n"
    "buffer_load_dwordx4 v0, s[76:79], s49 offen offset:0 lds\n"
    "buffer_load_dwordx4 v0, s[76:79], s49 offen offset:1024 lds\n"
    "ds_read_b64_tr_b16 a[0:1], v28 offset:0\n"
    "ds_read_b64_tr_b16 a[2:3], v28 offset:20480\n"
    "ds_read_b64_tr_b16 a[24:25], v28 offset:32\n"
    "ds_read_b64_tr_b16 a[26:27], v28 offset:20512\n"
    "ds_read_b64_tr_b16 a[48:49], v28 offset:64\n"
    "ds_read_b64_tr_b16 a[50:51], v28 offset:20544\n"
    "ds_read_b64_tr_b16 a[72:73], v28 offset:96\n"
    "ds_read_b64_tr_b16 a[74:75], v28 offset:20576\n"
    "ds_read_b64_tr_b16 a[96:97], v28 offset:128\n"
    "ds_read_b64_tr_b16 a[98:99], v28 offset:20608\n"
    "ds_read_b64_tr_b16 a[120:121], v28 offset:160\n"
    "ds_read_b64_tr_b16 a[122:123], v28 offset:20640\n"
    "ds_read_b64_tr_b16 a[144:145], v28 offset:192\n"
    "ds_read_b64_tr_b16 a[146:147], v28 offset:20672\n"
    "ds_read_b64_tr_b16 a[168:169], v28 offset:224\n"
    "ds_read_b64_tr_b16 a[170:171], v28 offset:20704\n"
    "s_waitcnt lgkmcnt(14)\n"
    "ds_read_b64_tr_b16 a[192:193], v28 offset:256\n"
    "ds_read_b64_tr_b16 a[194:195], v28 offset:20736\n"
    "s_waitcnt lgkmcnt(14)\n"
    "ds_read_b64_tr_b16 a[216:217], v28 offset:288\n"
    "ds_read_b64_tr_b16 a[218:219], v28 offset:20768\n"
    "s_waitcnt lgkmcnt(14)\n"
    "s_waitcnt vmcnt(10) lgkmcnt(0)\n"
    "s_barrier\n"
    "s_add_u32 m0, s51, 0\n"
    "s_add_u32 s49, s50, 122880\n"
    "buffer_load_dwordx4 v0, s[76:79], s49 offen offset:0 lds\n"
    "buffer_load_dwordx4 v0, s[76:79], s49 offen offset:1024 lds\n"
    "buffer_load_dwordx4 v0, s[76:79], s49 offen offset:2048 lds\n"
    "buffer_load_dwordx4 v0, s[76:79], s49 offen offset:3072 lds\n"
    "s_add_u32 m0, s51, 4096\n"
    "s_add_u32 s49, s50, 126976\n"
    "buffer_load_dwordx4 v0, s[76:79], s49 offen offset:0 lds\n"
    "buffer_load_dwordx4 v0, s[76:79], s49 offen offset:1024 lds\n"
    "buffer_load_dwordx4 v0, s[76:79], s49 offen offset:2048 lds\n"
    "buffer_load_dwordx4 v0, s[76:79], s49 offen offset:3072 lds\n"
    "s_add_u32 m0, s51, 8192\n"
    "s_add_u32 s49, s50, 131072\n"
    "buffer_load_dwordx4 v0, s[76:79], s49 offen offset:0 lds\n"
    "buffer_load_dwordx4 v0, s[76:79], s49 offen offset:1024 lds\n"
    "ds_read_b64_tr_b16 a[4:5], v29 offset:0\n"
    "ds_read_b64_tr_b16 a[6:7], v29 offset:20480\n"
    "ds_read_b64_tr_b16 a[28:29], v29 offset:32\n"
    "ds_read_b64_tr_b16 a[30:31], v29 offset:20512\n"
    "ds_read_b64_tr_b16 a[52:53], v29 offset:64\n"
    "ds_read_b64_tr_b16 a[54:55], v29 offset:20544\n"
    "ds_read_b64_tr_b16 a[76:77], v29 offset:96\n"
    "ds_read_b64_tr_b16 a[78:79], v29 offset:20576\n"
    "ds_read_b64_tr_b16 a[100:101], v29 offset:128\n"
    "ds_read_b64_tr_b16 a[102:103], v29 offset:20608\n"
    "ds_read_b64_tr_b16 a[124:125], v29 offset:160\n"
    "ds_read_b64_tr_b16 a[126:127], v29 offset:20640\n"
    "ds_read_b64_tr_b16 a[148:149], v29 offset:192\n"
    "ds_read_b64_tr_b16 a[150:151], v29 offset:20672\n"
    "ds_read_b64_tr_b16 a[172:173], v29 offset:224\n"
    "ds_read_b64_tr_b16 a[174:175], v29 offset:20704\n"
    "s_waitcnt lgkmcnt(14)\n"
    "ds_read_b64_tr_b16 a[196:197], v29 offset:256\n"
    "ds_read_b64_tr_b16 a[198:199], v29 offset:20736\n"
    "s_waitcnt lgkmcnt(14)\n"
    "ds_read_b64_tr_b16 a[220:221], v29 offset:288\n"
    "ds_read_b64_tr_b16 a[222:223], v29 offset:20768\n"
    "s_waitcnt lgkmcnt(14)\n"
    "s_waitcnt vmcnt(10) lgkmcnt(0)\n"
    "s_barrier\n"
    "s_add_u32 m0, s51, 40960\n"
    "s_add_u32 s49, s50, 163840\n"
    "buffer_load_dwordx4 v0, s[76:79], s49 offen offset:0 lds\n"
    "buffer_load_dwordx4 v0, s[76:79], s49 offen offset:1024 lds\n"
    "buffer_load_dwordx4 v0, s[76:79], s49 offen offset:2048 lds\n"
    "buffer_load_dwordx4 v0, s[76:79], s49 offen offset:3072 lds\n"
    "s_add_u32 m0, s51, 45056\n"
    "s_add_u32 s49, s50, 167936\n"
    "buffer_load_dwordx4 v0, s[76:79], s49 offen offset:0 lds\n"
    "buffer_load_dwordx4 v0, s[76:79], s49 offen offset:1024 lds\n"
    "buffer_load_dwordx4 v0, s[76:79], s49 offen offset:2048 lds\n"
    "buffer_load_dwordx4 v0, s[76:79], s49 offen offset:3072 lds\n"
    "s_add_u32 m0, s51, 49152\n"
    "s_add_u32 s49, s50, 172032\n"
    "buffer_load_dwordx4 v0, s[76:79], s49 offen offset:0 lds\n"
    "buffer_load_dwordx4 v0, s[76:79], s49 offen offset:1024 lds\n"
    "ds_read_b64_tr_b16 a[8:9], v30 offset:0\n"
    "ds_read_b64_tr_b16 a[10:11], v30 offset:20480\n"
    "ds_read_b64_tr_b16 a[32:33], v30 offset:32\n"
    "ds_read_b64_tr_b16 a[34:35], v30 offset:20512\n"
    "ds_read_b64_tr_b16 a[56:57], v30 offset:64\n"
    "ds_read_b64_tr_b16 a[58:59], v30 offset:20544\n"
    "ds_read_b64_tr_b16 a[80:81], v30 offset:96\n"
    "ds_read_b64_tr_b16 a[82:83], v30 offset:20576\n"
    "ds_read_b64_tr_b16 a[104:105], v30 offset:128\n"
    "ds_read_b64_tr_b16 a[106:107], v30 offset:20608\n"
    "ds_read_b64_tr_b16 a[128:129], v30 offset:160\n"
    "ds_read_b64_tr_b16 a[130:131], v30 offset:20640\n"
    "ds_read_b64_tr_b16 a[152:153], v30 offset:192\n"
    "ds_read_b64_tr_b16 a[154:155], v30 offset:20672\n"
    "ds_read_b64_tr_b16 a[176:177], v30 offset:224\n"
    "ds_read_b64_tr_b16 a[178:179], v30 offset:20704\n"
    "s_waitcnt lgkmcnt(14)\n"
    "ds_read_b64_tr_b16 a[200:201], v30 offset:256\n"
    "ds_read_b64_tr_b16 a[202:203], v30 offset:20736\n"
    "s_waitcnt lgkmcnt(14)\n"
    "ds_read_b64_tr_b16 a[224:225], v30 offset:288\n"
    "ds_read_b64_tr_b16 a[226:227], v30 offset:20768\n"
    "s_waitcnt lgkmcnt(14)\n"
    "s_waitcnt vmcnt(10) lgkmcnt(0)\n"
    "s_barrier\n"
    "s_add_u32 m0, s51, 81920\n"
    "s_add_u32 s49, s50, 204800\n"
    "buffer_load_dwordx4 v0, s[76:79], s49 offen offset:0 lds\n"
    "buffer_load_dwordx4 v0, s[76:79], s49 offen offset:1024 lds\n"
    "buffer_load_dwordx4 v0, s[76:79], s49 offen offset:2048 lds\n"
    "buffer_load_dwordx4 v0, s[76:79], s49 offen offset:3072 lds\n"
    "s_add_u32 m0, s51, 86016\n"
    "s_add_u32 s49, s50, 208896\n"
    "buffer_load_dwordx4 v0, s[76:79], s49 offen offset:0 lds\n"
    "buffer_load_dwordx4 v0, s[76:79], s49 offen offset:1024 lds\n"
    "buffer_load_dwordx4 v0, s[76:79], s49 offen offset:2048 lds\n"
    "buffer_load_dwordx4 v0, s[76:79], s49 offen offset:3072 lds\n"
    "s_add_u32 m0, s51, 90112\n"
    "s_add_u32 s49, s50, 212992\n"
    "buffer_load_dwordx4 v0, s[76:79], s49 offen offset:0 lds\n"
    "buffer_load_dwordx4 v0, s[76:79], s49 offen offset:1024 lds\n"
    "ds_read_b64_tr_b16 a[12:13], v28 offset:0\n"
    "ds_read_b64_tr_b16 a[14:15], v28 offset:20480\n"
    "ds_read_b64_tr_b16 a[36:37], v28 offset:32\n"
    "ds_read_b64_tr_b16 a[38:39], v28 offset:20512\n"
    "ds_read_b64_tr_b16 a[60:61], v28 offset:64\n"
    "ds_read_b64_tr_b16 a[62:63], v28 offset:20544\n"
    "ds_read_b64_tr_b16 a[84:85], v28 offset:96\n"
    "ds_read_b64_tr_b16 a[86:87], v28 offset:20576\n"
    "ds_read_b64_tr_b16 a[108:109], v28 offset:128\n"
    "ds_read_b64_tr_b16 a[110:111], v28 offset:20608\n"
    "ds_read_b64_tr_b16 a[132:133], v28 offset:160\n"
    "ds_read_b64_tr_b16 a[134:135], v28 offset:20640\n"
    "ds_read_b64_tr_b16 a[156:157], v28 offset:192\n"
    "ds_read_b64_tr_b16 a[158:159], v28 offset:20672\n"
    "ds_read_b64_tr_b16 a[180:181], v28 offset:224\n"
    "ds_read_b64_tr_b16 a[182:183], v28 offset:20704\n"
    "s_waitcnt lgkmcnt(14)\n"
    "ds_read_b64_tr_b16 a[204:205], v28 offset:256\n"
    "ds_read_b64_tr_b16 a[206:207], v28 offset:20736\n"
    "s_waitcnt lgkmcnt(14)\n"
    "ds_read_b64_tr_b16 a[228:229], v28 offset:288\n"
    "ds_read_b64_tr_b16 a[230:231], v28 offset:20768\n"
    "s_waitcnt lgkmcnt(14)\n"
    "s_waitcnt vmcnt(10) lgkmcnt(0)\n"
    "s_barrier\n"
    "ds_read_b64_tr_b16 a[16:17], v29 offset:0\n"
    "ds_read_b64_tr_b16 a[18:19], v29 offset:20480\n"
    "ds_read_b64_tr_b16 a[40:41], v29 offset:32\n"
    "ds_read_b64_tr_b16 a[42:43], v29 offset:20512\n"
    "ds_read_b64_tr_b16 a[64:65], v29 offset:64\n"
    "ds_read_b64_tr_b16 a[66:67], v29 offset:20544\n"
    "ds_read_b64_tr_b16 a[88:89], v29 offset:96\n"
    "ds_read_b64_tr_b16 a[90:91], v29 offset:20576\n"
    "ds_read_b64_tr_b16 a[112:113], v29 offset:128\n"
    "ds_read_b64_tr_b16 a[114:115], v29 offset:20608\n"
    "ds_read_b64_tr_b16 a[136:137], v29 offset:160\n"
    "ds_read_b64_tr_b16 a[138:139], v29 offset:20640\n"
    "ds_read_b64_tr_b16 a[160:161], v29 offset:192\n"
    "ds_read_b64_tr_b16 a[162:163], v29 offset:20672\n"
    "ds_read_b64_tr_b16 a[184:185], v29 offset:224\n"
    "ds_read_b64_tr_b16 a[186:187], v29 offset:20704\n"
    "s_waitcnt lgkmcnt(14)\n"
    "ds_read_b64_tr_b16 a[208:209], v29 offset:256\n"
    "ds_read_b64_tr_b16 a[210:211], v29 offset:20736\n"
    "s_waitcnt lgkmcnt(14)\n"
    "ds_read_b64_tr_b16 a[232:233], v29 offset:288\n"
    "ds_read_b64_tr_b16 a[234:235], v29 offset:20768\n"
    "s_waitcnt lgkmcnt(14)\n"
    "s_waitcnt vmcnt(0) lgkmcnt(0)\n"
    "s_barrier\n"
    "ds_read_b64_tr_b16 a[20:21], v30 offset:0\n"
    "ds_read_b64_tr_b16 a[22:23], v30 offset:20480\n"
    "ds_read_b64_tr_b16 a[44:45], v30 offset:32\n"
    "ds_read_b64_tr_b16 a[46:47], v30 offset:20512\n"
    "ds_read_b64_tr_b16 a[68:69], v30 offset:64\n"
    "ds_read_b64_tr_b16 a[70:71], v30 offset:20544\n"
    "ds_read_b64_tr_b16 a[92:93], v30 offset:96\n"
    "ds_read_b64_tr_b16 a[94:95], v30 offset:20576\n"
    "ds_read_b64_tr_b16 a[116:117], v30 offset:128\n"
    "ds_read_b64_tr_b16 a[118:119], v30 offset:20608\n"
    "ds_read_b64_tr_b16 a[140:141], v30 offset:160\n"
    "ds_read_b64_tr_b16 a[142:143], v30 offset:20640\n"
    "ds_read_b64_tr_b16 a[164:165], v30 offset:192\n"
    "ds_read_b64_tr_b16 a[166:167], v30 offset:20672\n"
    "ds_read_b64_tr_b16 a[188:189], v30 offset:224\n"
    "ds_read_b64_tr_b16 a[190:191], v30 offset:20704\n"
    "s_waitcnt lgkmcnt(14)\n"
    "ds_read_b64_tr_b16 a[212:213], v30 offset:256\n"
    "ds_read_b64_tr_b16 a[214:215], v30 offset:20736\n"
    "s_waitcnt lgkmcnt(14)\n"
    "ds_read_b64_tr_b16 a[236:237], v30 offset:288\n"
    "ds_read_b64_tr_b16 a[238:239], v30 offset:20768\n"
    "s_waitcnt lgkmcnt(14)\n"
    "s_waitcnt lgkmcnt(0)\n"
    "s_barrier\n"
    "ds_write_b128 v19, v[32:35] offset:0\n"
    "ds_write_b128 v20, v[36:39] offset:0\n"
    "ds_write_b128 v21, v[40:43] offset:0\n"
    "ds_write_b128 v19, v[44:47] offset:12800\n"
    "ds_write_b128 v20, v[48:51] offset:12800\n"
    "ds_write_b128 v21, v[52:55] offset:12800\n"
    "ds_write_b128 v19, v[56:59] offset:25600\n"
    "ds_write_b128 v20, v[60:63] offset:25600\n"
    "ds_write_b128 v21, v[64:67] offset:25600\n"
    "ds_write_b128 v19, v[68:71] offset:38400\n"
    "ds_write_b128 v20, v[72:75] offset:38400\n"
    "ds_write_b128 v21, v[76:79] offset:38400\n"
    "ds_write_b128 v19, v[80:83] offset:51200\n"
    "ds_write_b128 v20, v[84:87] offset:51200\n"
    "ds_write_b128 v21, v[88:91] offset:51200\n"
    "v_and_b32 v22, 1, v15\n"
    "v_and_b32 v26, 2, v15\n"
    "v_lshl_add_u32 v22, v26, 2, v22\n"
    "v_and_b32 v26, 4, v15\n"
    "v_add_u32 v22, v22, v26\n"
    "v_and_b32 v26, 8, v15\n"
    "v_lshl_add_u32 v22, v26, 1, v22\n"
    "v_mul_u32_u24 v22, 400, v22\n"
    "v_lshrrev_b32 v26, 4, v15\n"
    "v_lshl_add_u32 v22, v26, 3, v22\n"
    "v_bfe_u32 v26, v15, 3, 1\n"
    "v_mul_u32_u24 v26, 655360, v26\n"
    "v_and_b32 v24, 0x370, v0\n"
    "v_add_u32 v24, v24, v26\n"
    "s_mul_i32 s48, s45, 13107200\n"
    "s_add_u32 s48, s48, 64421888\n"
    "v_add_u32 v24, s48, v24\n"
    "s_lshr_b32 s50, s85, 1\n"
    "s_mul_i32 s50, s50, 20480\n"
    "s_and_b32 s48, s85, 1\n"
    "s_lshl_b32 s48, s48, 7\n"
    "s_add_u32 s50, s50, s48\n"
    "s_mul_i32 s48, s86, 5120\n"
    "s_add_u32 s50, s50, s48\n"
    "s_movk_i32 s52, 0x2000\n"
    "s_mov_b32 s51, 5\n"
    "s_waitcnt lgkmcnt(0)\n"
    "s_barrier\n"
    "hp_bg_%=:\n"
    "ds_read2_b64 v[96:99], v22 offset0:0 offset1:4\n"
    "ds_read2_b64 v[100:103], v22 offset0:100 offset1:104\n"
    "ds_read2_b64 v[104:107], v22 offset0:8 offset1:12\n"
    "ds_read2_b64 v[108:111], v22 offset0:108 offset1:112\n"
    "ds_read2_b64 v[112:115], v22 offset0:16 offset1:20\n"
    "ds_read2_b64 v[116:119], v22 offset0:116 offset1:120\n"
    "ds_read2_b64 v[120:123], v22 offset0:24 offset1:28\n"
    "ds_read2_b64 v[124:127], v22 offset0:124 offset1:128\n"
    "ds_read2_b64 v[128:131], v22 offset0:32 offset1:36\n"
    "ds_read2_b64 v[132:135], v22 offset0:132 offset1:136\n"
    "ds_read2_b64 v[136:139], v22 offset0:40 offset1:44\n"
    "ds_read2_b64 v[140:143], v22 offset0:140 offset1:144\n"
    "v_add_u32 v22, 12800, v22\n"
    "s_and_b32 s53, s50, 0xffffe000\n"
    "s_add_u32 s53, s53, s50\n"
    "s_waitcnt lgkmcnt(11)\n"
    "v_mfma_f32_16x16x32_bf16 v[144:147], v[96:99], a[0:3], 0\n"
    "s_waitcnt lgkmcnt(10)\n"
    "v_mfma_f32_16x16x32_bf16 v[148:151], v[100:103], a[0:3], 0\n"
    "s_waitcnt lgkmcnt(9)\n"
    "v_mfma_f32_16x16x32_bf16 v[144:147], v[104:107], a[4:7], v[144:147]\n"
    "s_waitcnt lgkmcnt(8)\n"
    "v_mfma_f32_16x16x32_bf16 v[148:151], v[108:111], a[4:7], v[148:151]\n"
    "s_waitcnt lgkmcnt(7)\n"
    "v_mfma_f32_16x16x32_bf16 v[144:147], v[112:115], a[8:11], v[144:147]\n"
    "s_waitcnt lgkmcnt(6)\n"
    "v_mfma_f32_16x16x32_bf16 v[148:151], v[116:119], a[8:11], v[148:151]\n"
    "s_waitcnt lgkmcnt(5)\n"
    "v_mfma_f32_16x16x32_bf16 v[144:147], v[120:123], a[12:15], v[144:147]\n"
    "s_waitcnt lgkmcnt(4)\n"
    "v_mfma_f32_16x16x32_bf16 v[148:151], v[124:127], a[12:15], v[148:151]\n"
    "s_waitcnt lgkmcnt(3)\n"
    "v_mfma_f32_16x16x32_bf16 v[144:147], v[128:131], a[16:19], v[144:147]\n"
    "s_waitcnt lgkmcnt(2)\n"
    "v_mfma_f32_16x16x32_bf16 v[148:151], v[132:135], a[16:19], v[148:151]\n"
    "s_waitcnt lgkmcnt(1)\n"
    "v_mfma_f32_16x16x32_bf16 v[144:147], v[136:139], a[20:23], v[144:147]\n"
    "s_waitcnt lgkmcnt(0)\n"
    "v_mfma_f32_16x16x32_bf16 v[148:151], v[140:143], a[20:23], v[148:151]\n"
    "v_mfma_f32_16x16x32_bf16 v[152:155], v[96:99], a[24:27], 0\n"
    "v_add_u32 v25, s53, v24\n"
    "v_mfma_f32_16x16x32_bf16 v[156:159], v[100:103], a[24:27], 0\n"
    "v_lshrrev_b32 v26, 8, v25\n"
    "v_mfma_f32_16x16x32_bf16 v[152:155], v[104:107], a[28:31], v[152:155]\n"
    "v_and_or_b32 v25, v26, s52, v25\n"
    "v_mfma_f32_16x16x32_bf16 v[156:159], v[108:111], a[28:31], v[156:159]\n"
    "s_add_u32 s53, s53, 1310720\n"
    "v_mfma_f32_16x16x32_bf16 v[152:155], v[112:115], a[32:35], v[152:155]\n"
    "v_mfma_f32_16x16x32_bf16 v[156:159], v[116:119], a[32:35], v[156:159]\n"
    "v_mfma_f32_16x16x32_bf16 v[152:155], v[120:123], a[36:39], v[152:155]\n"
    "v_cvt_pk_bf16_f32 v160, v144, v145\n"
    "v_mfma_f32_16x16x32_bf16 v[156:159], v[124:127], a[36:39], v[156:159]\n"
    "v_cvt_pk_bf16_f32 v161, v146, v147\n"
    "v_mfma_f32_16x16x32_bf16 v[152:155], v[128:131], a[40:43], v[152:155]\n"
    "v_cvt_pk_bf16_f32 v162, v148, v149\n"
    "v_mfma_f32_16x16x32_bf16 v[156:159], v[132:135], a[40:43], v[156:159]\n"
    "v_cvt_pk_bf16_f32 v163, v150, v151\n"
    "v_mfma_f32_16x16x32_bf16 v[152:155], v[136:139], a[44:47], v[152:155]\n"
    "buffer_store_dwordx4 v[160:163], v25, s[40:43], 0 offen sc1\n"
    "v_mfma_f32_16x16x32_bf16 v[156:159], v[140:143], a[44:47], v[156:159]\n"
    "v_mfma_f32_16x16x32_bf16 v[144:147], v[96:99], a[48:51], 0\n"
    "v_add_u32 v25, s53, v24\n"
    "v_mfma_f32_16x16x32_bf16 v[148:151], v[100:103], a[48:51], 0\n"
    "v_lshrrev_b32 v26, 8, v25\n"
    "v_mfma_f32_16x16x32_bf16 v[144:147], v[104:107], a[52:55], v[144:147]\n"
    "v_and_or_b32 v25, v26, s52, v25\n"
    "v_mfma_f32_16x16x32_bf16 v[148:151], v[108:111], a[52:55], v[148:151]\n"
    "s_add_u32 s53, s53, 1310720\n"
    "v_mfma_f32_16x16x32_bf16 v[144:147], v[112:115], a[56:59], v[144:147]\n"
    "v_mfma_f32_16x16x32_bf16 v[148:151], v[116:119], a[56:59], v[148:151]\n"
    "v_mfma_f32_16x16x32_bf16 v[144:147], v[120:123], a[60:63], v[144:147]\n"
    "v_cvt_pk_bf16_f32 v164, v152, v153\n"
    "v_mfma_f32_16x16x32_bf16 v[148:151], v[124:127], a[60:63], v[148:151]\n"
    "v_cvt_pk_bf16_f32 v165, v154, v155\n"
    "v_mfma_f32_16x16x32_bf16 v[144:147], v[128:131], a[64:67], v[144:147]\n"
    "v_cvt_pk_bf16_f32 v166, v156, v157\n"
    "v_mfma_f32_16x16x32_bf16 v[148:151], v[132:135], a[64:67], v[148:151]\n"
    "v_cvt_pk_bf16_f32 v167, v158, v159\n"
    "v_mfma_f32_16x16x32_bf16 v[144:147], v[136:139], a[68:71], v[144:147]\n"
    "buffer_store_dwordx4 v[164:167], v25, s[40:43], 0 offen sc1\n"
    "v_mfma_f32_16x16x32_bf16 v[148:151], v[140:143], a[68:71], v[148:151]\n"
    "v_mfma_f32_16x16x32_bf16 v[152:155], v[96:99], a[72:75], 0\n"
    "v_add_u32 v25, s53, v24\n"
    "v_mfma_f32_16x16x32_bf16 v[156:159], v[100:103], a[72:75], 0\n"
    "v_lshrrev_b32 v26, 8, v25\n"
    "v_mfma_f32_16x16x32_bf16 v[152:155], v[104:107], a[76:79], v[152:155]\n"
    "v_and_or_b32 v25, v26, s52, v25\n"
    "v_mfma_f32_16x16x32_bf16 v[156:159], v[108:111], a[76:79], v[156:159]\n"
    "s_add_u32 s53, s53, 1310720\n"
    "v_mfma_f32_16x16x32_bf16 v[152:155], v[112:115], a[80:83], v[152:155]\n"
    "v_mfma_f32_16x16x32_bf16 v[156:159], v[116:119], a[80:83], v[156:159]\n"
    "v_mfma_f32_16x16x32_bf16 v[152:155], v[120:123], a[84:87], v[152:155]\n"
    "v_cvt_pk_bf16_f32 v160, v144, v145\n"
    "v_mfma_f32_16x16x32_bf16 v[156:159], v[124:127], a[84:87], v[156:159]\n"
    "v_cvt_pk_bf16_f32 v161, v146, v147\n"
    "v_mfma_f32_16x16x32_bf16 v[152:155], v[128:131], a[88:91], v[152:155]\n"
    "v_cvt_pk_bf16_f32 v162, v148, v149\n"
    "v_mfma_f32_16x16x32_bf16 v[156:159], v[132:135], a[88:91], v[156:159]\n"
    "v_cvt_pk_bf16_f32 v163, v150, v151\n"
    "v_mfma_f32_16x16x32_bf16 v[152:155], v[136:139], a[92:95], v[152:155]\n"
    "buffer_store_dwordx4 v[160:163], v25, s[40:43], 0 offen sc1\n"
    "v_mfma_f32_16x16x32_bf16 v[156:159], v[140:143], a[92:95], v[156:159]\n"
    "v_mfma_f32_16x16x32_bf16 v[144:147], v[96:99], a[96:99], 0\n"
    "v_add_u32 v25, s53, v24\n"
    "v_mfma_f32_16x16x32_bf16 v[148:151], v[100:103], a[96:99], 0\n"
    "v_lshrrev_b32 v26, 8, v25\n"
    "v_mfma_f32_16x16x32_bf16 v[144:147], v[104:107], a[100:103], v[144:147]\n"
    "v_and_or_b32 v25, v26, s52, v25\n"
    "v_mfma_f32_16x16x32_bf16 v[148:151], v[108:111], a[100:103], v[148:151]\n"
    "s_add_u32 s53, s53, 1310720\n"
    "v_mfma_f32_16x16x32_bf16 v[144:147], v[112:115], a[104:107], v[144:147]\n"
    "v_mfma_f32_16x16x32_bf16 v[148:151], v[116:119], a[104:107], v[148:151]\n"
    "v_mfma_f32_16x16x32_bf16 v[144:147], v[120:123], a[108:111], v[144:147]\n"
    "v_cvt_pk_bf16_f32 v164, v152, v153\n"
    "v_mfma_f32_16x16x32_bf16 v[148:151], v[124:127], a[108:111], v[148:151]\n"
    "v_cvt_pk_bf16_f32 v165, v154, v155\n"
    "v_mfma_f32_16x16x32_bf16 v[144:147], v[128:131], a[112:115], v[144:147]\n"
    "v_cvt_pk_bf16_f32 v166, v156, v157\n"
    "v_mfma_f32_16x16x32_bf16 v[148:151], v[132:135], a[112:115], v[148:151]\n"
    "v_cvt_pk_bf16_f32 v167, v158, v159\n"
    "v_mfma_f32_16x16x32_bf16 v[144:147], v[136:139], a[116:119], v[144:147]\n"
    "buffer_store_dwordx4 v[164:167], v25, s[40:43], 0 offen sc1\n"
    "v_mfma_f32_16x16x32_bf16 v[148:151], v[140:143], a[116:119], v[148:151]\n"
    "v_mfma_f32_16x16x32_bf16 v[152:155], v[96:99], a[120:123], 0\n"
    "v_add_u32 v25, s53, v24\n"
    "v_mfma_f32_16x16x32_bf16 v[156:159], v[100:103], a[120:123], 0\n"
    "v_lshrrev_b32 v26, 8, v25\n"
    "v_mfma_f32_16x16x32_bf16 v[152:155], v[104:107], a[124:127], v[152:155]\n"
    "v_and_or_b32 v25, v26, s52, v25\n"
    "v_mfma_f32_16x16x32_bf16 v[156:159], v[108:111], a[124:127], v[156:159]\n"
    "s_add_u32 s53, s53, 1310720\n"
    "v_mfma_f32_16x16x32_bf16 v[152:155], v[112:115], a[128:131], v[152:155]\n"
    "v_mfma_f32_16x16x32_bf16 v[156:159], v[116:119], a[128:131], v[156:159]\n"
    "v_mfma_f32_16x16x32_bf16 v[152:155], v[120:123], a[132:135], v[152:155]\n"
    "v_cvt_pk_bf16_f32 v160, v144, v145\n"
    "v_mfma_f32_16x16x32_bf16 v[156:159], v[124:127], a[132:135], v[156:159]\n"
    "v_cvt_pk_bf16_f32 v161, v146, v147\n"
    "v_mfma_f32_16x16x32_bf16 v[152:155], v[128:131], a[136:139], v[152:155]\n"
    "v_cvt_pk_bf16_f32 v162, v148, v149\n"
    "v_mfma_f32_16x16x32_bf16 v[156:159], v[132:135], a[136:139], v[156:159]\n"
    "v_cvt_pk_bf16_f32 v163, v150, v151\n"
    "v_mfma_f32_16x16x32_bf16 v[152:155], v[136:139], a[140:143], v[152:155]\n"
    "buffer_store_dwordx4 v[160:163], v25, s[40:43], 0 offen sc1\n"
    "v_mfma_f32_16x16x32_bf16 v[156:159], v[140:143], a[140:143], v[156:159]\n"
    "v_mfma_f32_16x16x32_bf16 v[144:147], v[96:99], a[144:147], 0\n"
    "v_add_u32 v25, s53, v24\n"
    "v_mfma_f32_16x16x32_bf16 v[148:151], v[100:103], a[144:147], 0\n"
    "v_lshrrev_b32 v26, 8, v25\n"
    "v_mfma_f32_16x16x32_bf16 v[144:147], v[104:107], a[148:151], v[144:147]\n"
    "v_and_or_b32 v25, v26, s52, v25\n"
    "v_mfma_f32_16x16x32_bf16 v[148:151], v[108:111], a[148:151], v[148:151]\n"
    "s_add_u32 s53, s53, 1310720\n"
    "v_mfma_f32_16x16x32_bf16 v[144:147], v[112:115], a[152:155], v[144:147]\n"
    "v_mfma_f32_16x16x32_bf16 v[148:151], v[116:119], a[152:155], v[148:151]\n"
    "v_mfma_f32_16x16x32_bf16 v[144:147], v[120:123], a[156:159], v[144:147]\n"
    "v_cvt_pk_bf16_f32 v164, v152, v153\n"
    "v_mfma_f32_16x16x32_bf16 v[148:151], v[124:127], a[156:159], v[148:151]\n"
    "v_cvt_pk_bf16_f32 v165, v154, v155\n"
    "v_mfma_f32_16x16x32_bf16 v[144:147], v[128:131], a[160:163], v[144:147]\n"
    "v_cvt_pk_bf16_f32 v166, v156, v157\n"
    "v_mfma_f32_16x16x32_bf16 v[148:151], v[132:135], a[160:163], v[148:151]\n"
    "v_cvt_pk_bf16_f32 v167, v158, v159\n"
    "v_mfma_f32_16x16x32_bf16 v[144:147], v[136:139], a[164:167], v[144:147]\n"
    "buffer_store_dwordx4 v[164:167], v25, s[40:43], 0 offen sc1\n"
    "v_mfma_f32_16x16x32_bf16 v[148:151], v[140:143], a[164:167], v[148:151]\n"
    "v_mfma_f32_16x16x32_bf16 v[152:155], v[96:99], a[168:171], 0\n"
    "v_add_u32 v25, s53, v24\n"
    "v_mfma_f32_16x16x32_bf16 v[156:159], v[100:103], a[168:171], 0\n"
    "v_lshrrev_b32 v26, 8, v25\n"
    "v_mfma_f32_16x16x32_bf16 v[152:155], v[104:107], a[172:175], v[152:155]\n"
    "v_and_or_b32 v25, v26, s52, v25\n"
    "v_mfma_f32_16x16x32_bf16 v[156:159], v[108:111], a[172:175], v[156:159]\n"
    "s_add_u32 s53, s53, 1310720\n"
    "v_mfma_f32_16x16x32_bf16 v[152:155], v[112:115], a[176:179], v[152:155]\n"
    "v_mfma_f32_16x16x32_bf16 v[156:159], v[116:119], a[176:179], v[156:159]\n"
    "v_mfma_f32_16x16x32_bf16 v[152:155], v[120:123], a[180:183], v[152:155]\n"
    "v_cvt_pk_bf16_f32 v160, v144, v145\n"
    "v_mfma_f32_16x16x32_bf16 v[156:159], v[124:127], a[180:183], v[156:159]\n"
    "v_cvt_pk_bf16_f32 v161, v146, v147\n"
    "v_mfma_f32_16x16x32_bf16 v[152:155], v[128:131], a[184:187], v[152:155]\n"
    "v_cvt_pk_bf16_f32 v162, v148, v149\n"
    "v_mfma_f32_16x16x32_bf16 v[156:159], v[132:135], a[184:187], v[156:159]\n"
    "v_cvt_pk_bf16_f32 v163, v150, v151\n"
    "v_mfma_f32_16x16x32_bf16 v[152:155], v[136:139], a[188:191], v[152:155]\n"
    "buffer_store_dwordx4 v[160:163], v25, s[40:43], 0 offen sc1\n"
    "v_mfma_f32_16x16x32_bf16 v[156:159], v[140:143], a[188:191], v[156:159]\n"
    "v_mfma_f32_16x16x32_bf16 v[144:147], v[96:99], a[192:195], 0\n"
    "v_add_u32 v25, s53, v24\n"
    "v_mfma_f32_16x16x32_bf16 v[148:151], v[100:103], a[192:195], 0\n"
    "v_lshrrev_b32 v26, 8, v25\n"
    "v_mfma_f32_16x16x32_bf16 v[144:147], v[104:107], a[196:199], v[144:147]\n"
    "v_and_or_b32 v25, v26, s52, v25\n"
    "v_mfma_f32_16x16x32_bf16 v[148:151], v[108:111], a[196:199], v[148:151]\n"
    "s_add_u32 s53, s53, 1310720\n"
    "v_mfma_f32_16x16x32_bf16 v[144:147], v[112:115], a[200:203], v[144:147]\n"
    "v_mfma_f32_16x16x32_bf16 v[148:151], v[116:119], a[200:203], v[148:151]\n"
    "v_mfma_f32_16x16x32_bf16 v[144:147], v[120:123], a[204:207], v[144:147]\n"
    "v_cvt_pk_bf16_f32 v164, v152, v153\n"
    "v_mfma_f32_16x16x32_bf16 v[148:151], v[124:127], a[204:207], v[148:151]\n"
    "v_cvt_pk_bf16_f32 v165, v154, v155\n"
    "v_mfma_f32_16x16x32_bf16 v[144:147], v[128:131], a[208:211], v[144:147]\n"
    "v_cvt_pk_bf16_f32 v166, v156, v157\n"
    "v_mfma_f32_16x16x32_bf16 v[148:151], v[132:135], a[208:211], v[148:151]\n"
    "v_cvt_pk_bf16_f32 v167, v158, v159\n"
    "v_mfma_f32_16x16x32_bf16 v[144:147], v[136:139], a[212:215], v[144:147]\n"
    "buffer_store_dwordx4 v[164:167], v25, s[40:43], 0 offen sc1\n"
    "v_mfma_f32_16x16x32_bf16 v[148:151], v[140:143], a[212:215], v[148:151]\n"
    "v_mfma_f32_16x16x32_bf16 v[152:155], v[96:99], a[216:219], 0\n"
    "v_add_u32 v25, s53, v24\n"
    "v_mfma_f32_16x16x32_bf16 v[156:159], v[100:103], a[216:219], 0\n"
    "v_lshrrev_b32 v26, 8, v25\n"
    "v_mfma_f32_16x16x32_bf16 v[152:155], v[104:107], a[220:223], v[152:155]\n"
    "v_and_or_b32 v25, v26, s52, v25\n"
    "v_mfma_f32_16x16x32_bf16 v[156:159], v[108:111], a[220:223], v[156:159]\n"
    "s_add_u32 s53, s53, 1310720\n"
    "v_mfma_f32_16x16x32_bf16 v[152:155], v[112:115], a[224:227], v[152:155]\n"
    "v_mfma_f32_16x16x32_bf16 v[156:159], v[116:119], a[224:227], v[156:159]\n"
    "v_mfma_f32_16x16x32_bf16 v[152:155], v[120:123], a[228:231], v[152:155]\n"
    "v_cvt_pk_bf16_f32 v160, v144, v145\n"
    "v_mfma_f32_16x16x32_bf16 v[156:159], v[124:127], a[228:231], v[156:159]\n"
    "v_cvt_pk_bf16_f32 v161, v146, v147\n"
    "v_mfma_f32_16x16x32_bf16 v[152:155], v[128:131], a[232:235], v[152:155]\n"
    "v_cvt_pk_bf16_f32 v162, v148, v149\n"
    "v_mfma_f32_16x16x32_bf16 v[156:159], v[132:135], a[232:235], v[156:159]\n"
    "v_cvt_pk_bf16_f32 v163, v150, v151\n"
    "v_mfma_f32_16x16x32_bf16 v[152:155], v[136:139], a[236:239], v[152:155]\n"
    "buffer_store_dwordx4 v[160:163], v25, s[40:43], 0 offen sc1\n"
    "v_mfma_f32_16x16x32_bf16 v[156:159], v[140:143], a[236:239], v[156:159]\n"
    "v_add_u32 v25, s53, v24\n"
    "v_lshrrev_b32 v26, 8, v25\n"
    "v_and_or_b32 v25, v26, s52, v25\n"
    "s_add_u32 s53, s53, 1310720\n"
    "s_nop 1\n"
    "v_cvt_pk_bf16_f32 v164, v152, v153\n"
    "v_cvt_pk_bf16_f32 v165, v154, v155\n"
    "v_cvt_pk_bf16_f32 v166, v156, v157\n"
    "v_cvt_pk_bf16_f32 v167, v158, v159\n"
    "buffer_store_dwordx4 v[164:167], v25, s[40:43], 0 offen sc1\n"
    "s_add_u32 s50, s50, 1024\n"
    "s_sub_u32 s51, s51, 1\n"
    "s_cmp_lg_u32 s51, 0\n"
    "s_cbranch_scc1 hp_bg_%=\n"
    "s_cmp_eq_u32 s45, 0\n"
    "s_cselect_b32 s59, 1, 0\n"
    "v_lshlrev_b32 v26, 7, v15\n"
    "v_cmp_gt_u32 vcc, s59, v15\n"
    "v_mov_b32 v27, 2147483632\n"
    "s_nop 0\n"
    "v_cndmask_b32 v26, v27, v26, vcc\n"
    "v_mov_b32 v27, 1\n"
    "s_and_b32 s54, s84, 7\n"
    "s_lshl_b32 s54, s54, 7\n"
    "s_add_u32 s54, s54, 2364928\n"
    "s_waitcnt vmcnt(0)\n"
    "s_waitcnt lgkmcnt(0)\n"
    "s_barrier\n"
    "buffer_atomic_add v27, v26, s[40:43], s54 offen\n"
    "s_mov_b32 s76, s68\n"
    "s_mov_b32 s77, s69\n"
    "hp_ldw_%=:\n"
    "v_add_u32 v22, 0, v15\n"
    "v_mul_u32_u24 v16, 2731, v22\n"
    "v_lshrrev_b32 v16, 16, v16\n"
    "v_mul_u32_u24 v16, 11904, v16\n"
    "v_lshl_add_u32 v16, v22, 4, v16\n"
    "v_add_u32 v22, 64, v15\n"
    "v_mul_u32_u24 v17, 2731, v22\n"
    "v_lshrrev_b32 v17, 16, v17\n"
    "v_mul_u32_u24 v17, 11904, v17\n"
    "v_lshl_add_u32 v17, v22, 4, v17\n"
    "v_add_u32 v22, 128, v15\n"
    "v_mul_u32_u24 v18, 2731, v22\n"
    "v_lshrrev_b32 v18, 16, v18\n"
    "v_mul_u32_u24 v18, 11904, v18\n"
    "v_lshl_add_u32 v18, v22, 4, v18\n"
    "v_bfe_u32 v19, v15, 2, 1\n"
    "v_and_b32 v20, 8, v15\n"
    "v_add_u32 v19, v19, v20\n"
    "v_bfe_u32 v20, v15, 4, 1\n"
    "v_lshl_add_u32 v19, v20, 2, v19\n"
    "v_lshrrev_b32 v20, 5, v15\n"
    "v_lshl_add_u32 v19, v20, 4, v19\n"
    "v_mul_u32_u24 v19, 384, v19\n"
    "v_and_b32 v20, 3, v15\n"
    "v_lshl_add_u32 v19, v20, 3, v19\n"
    "s_mul_i32 s48, s45, 96\n"
    "v_add_u32 v19, s48, v19\n"
    "v_add_u32 v20, 49152, v19\n"
    "v_add_u32 v21, 98304, v19\n"
    "s_mul_i32 s50, s45, 196608\n"
    "s_mul_i32 s48, s44, 384\n"
    "s_add_u32 s50, s50, s48\n"
    "s_mul_i32 s51, s45, 6144\n"
    "s_add_u32 m0, s51, 0\n"
    "s_add_u32 s49, s50, 0\n"
    "buffer_load_dwordx4 v16, s[76:79], s49 offen lds\n"
    "s_add_u32 m0, s51, 1024\n"
    "s_add_u32 s49, s50, 0\n"
    "buffer_load_dwordx4 v17, s[76:79], s49 offen lds\n"
    "s_add_u32 m0, s51, 2048\n"
    "s_add_u32 s49, s50, 0\n"
    "buffer_load_dwordx4 v18, s[76:79], s49 offen lds\n"
    "s_add_u32 m0, s51, 3072\n"
    "s_add_u32 s49, s50, 98304\n"
    "buffer_load_dwordx4 v16, s[76:79], s49 offen lds\n"
    "s_add_u32 m0, s51, 4096\n"
    "s_add_u32 s49, s50, 98304\n"
    "buffer_load_dwordx4 v17, s[76:79], s49 offen lds\n"
    "s_add_u32 m0, s51, 5120\n"
    "s_add_u32 s49, s50, 98304\n"
    "buffer_load_dwordx4 v18, s[76:79], s49 offen lds\n"
    "s_add_u32 m0, s51, 24576\n"
    "s_add_u32 s49, s50, 786432\n"
    "buffer_load_dwordx4 v16, s[76:79], s49 offen lds\n"
    "s_add_u32 m0, s51, 25600\n"
    "s_add_u32 s49, s50, 786432\n"
    "buffer_load_dwordx4 v17, s[76:79], s49 offen lds\n"
    "s_add_u32 m0, s51, 26624\n"
    "s_add_u32 s49, s50, 786432\n"
    "buffer_load_dwordx4 v18, s[76:79], s49 offen lds\n"
    "s_add_u32 m0, s51, 27648\n"
    "s_add_u32 s49, s50, 884736\n"
    "buffer_load_dwordx4 v16, s[76:79], s49 offen lds\n"
    "s_add_u32 m0, s51, 28672\n"
    "s_add_u32 s49, s50, 884736\n"
    "buffer_load_dwordx4 v17, s[76:79], s49 offen lds\n"
    "s_add_u32 m0, s51, 29696\n"
    "s_add_u32 s49, s50, 884736\n"
    "buffer_load_dwordx4 v18, s[76:79], s49 offen lds\n"
    "s_add_u32 m0, s51, 49152\n"
    "s_add_u32 s49, s50, 1572864\n"
    "buffer_load_dwordx4 v16, s[76:79], s49 offen lds\n"
    "s_add_u32 m0, s51, 50176\n"
    "s_add_u32 s49, s50, 1572864\n"
    "buffer_load_dwordx4 v17, s[76:79], s49 offen lds\n"
    "s_add_u32 m0, s51, 51200\n"
    "s_add_u32 s49, s50, 1572864\n"
    "buffer_load_dwordx4 v18, s[76:79], s49 offen lds\n"
    "s_add_u32 m0, s51, 52224\n"
    "s_add_u32 s49, s50, 1671168\n"
    "buffer_load_dwordx4 v16, s[76:79], s49 offen lds\n"
    "s_add_u32 m0, s51, 53248\n"
    "s_add_u32 s49, s50, 1671168\n"
    "buffer_load_dwordx4 v17, s[76:79], s49 offen lds\n"
    "s_add_u32 m0, s51, 54272\n"
    "s_add_u32 s49, s50, 1671168\n"
    "buffer_load_dwordx4 v18, s[76:79], s49 offen lds\n"
    "s_add_u32 m0, s51, 73728\n"
    "s_add_u32 s49, s50, 2359296\n"
    "buffer_load_dwordx4 v16, s[76:79], s49 offen lds\n"
    "s_add_u32 m0, s51, 74752\n"
    "s_add_u32 s49, s50, 2359296\n"
    "buffer_load_dwordx4 v17, s[76:79], s49 offen lds\n"
    "s_add_u32 m0, s51, 75776\n"
    "s_add_u32 s49, s50, 2359296\n"
    "buffer_load_dwordx4 v18, s[76:79], s49 offen lds\n"
    "s_add_u32 m0, s51, 76800\n"
    "s_add_u32 s49, s50, 2457600\n"
    "buffer_load_dwordx4 v16, s[76:79], s49 offen lds\n"
    "s_add_u32 m0, s51, 77824\n"
    "s_add_u32 s49, s50, 2457600\n"
    "buffer_load_dwordx4 v17, s[76:79], s49 offen lds\n"
    "s_add_u32 m0, s51, 78848\n"
    "s_add_u32 s49, s50, 2457600\n"
    "buffer_load_dwordx4 v18, s[76:79], s49 offen lds\n"
    "s_add_u32 m0, s51, 98304\n"
    "s_add_u32 s49, s50, 3145728\n"
    "buffer_load_dwordx4 v16, s[76:79], s49 offen lds\n"
    "s_add_u32 m0, s51, 99328\n"
    "s_add_u32 s49, s50, 3145728\n"
    "buffer_load_dwordx4 v17, s[76:79], s49 offen lds\n"
    "s_add_u32 m0, s51, 100352\n"
    "s_add_u32 s49, s50, 3145728\n"
    "buffer_load_dwordx4 v18, s[76:79], s49 offen lds\n"
    "s_add_u32 m0, s51, 101376\n"
    "s_add_u32 s49, s50, 3244032\n"
    "buffer_load_dwordx4 v16, s[76:79], s49 offen lds\n"
    "s_add_u32 m0, s51, 102400\n"
    "s_add_u32 s49, s50, 3244032\n"
    "buffer_load_dwordx4 v17, s[76:79], s49 offen lds\n"
    "s_add_u32 m0, s51, 103424\n"
    "s_add_u32 s49, s50, 3244032\n"
    "buffer_load_dwordx4 v18, s[76:79], s49 offen lds\n"
    "s_waitcnt vmcnt(24) lgkmcnt(0)\n"
    "s_barrier\n"
    "s_add_u32 m0, s51, 122880\n"
    "s_add_u32 s49, s50, 3932160\n"
    "buffer_load_dwordx4 v16, s[76:79], s49 offen lds\n"
    "s_add_u32 m0, s51, 123904\n"
    "s_add_u32 s49, s50, 3932160\n"
    "buffer_load_dwordx4 v17, s[76:79], s49 offen lds\n"
    "s_add_u32 m0, s51, 124928\n"
    "s_add_u32 s49, s50, 3932160\n"
    "buffer_load_dwordx4 v18, s[76:79], s49 offen lds\n"
    "s_add_u32 m0, s51, 125952\n"
    "s_add_u32 s49, s50, 4030464\n"
    "buffer_load_dwordx4 v16, s[76:79], s49 offen lds\n"
    "s_add_u32 m0, s51, 126976\n"
    "s_add_u32 s49, s50, 4030464\n"
    "buffer_load_dwordx4 v17, s[76:79], s49 offen lds\n"
    "s_add_u32 m0, s51, 128000\n"
    "s_add_u32 s49, s50, 4030464\n"
    "buffer_load_dwordx4 v18, s[76:79], s49 offen lds\n"
    "ds_read_b64_tr_b16 a[0:1], v19 offset:0\n"
    "ds_read_b64_tr_b16 a[2:3], v19 offset:768\n"
    "ds_read_b64_tr_b16 a[4:5], v19 offset:12288\n"
    "ds_read_b64_tr_b16 a[6:7], v19 offset:13056\n"
    "ds_read_b64_tr_b16 a[80:81], v19 offset:32\n"
    "ds_read_b64_tr_b16 a[82:83], v19 offset:800\n"
    "ds_read_b64_tr_b16 a[84:85], v19 offset:12320\n"
    "ds_read_b64_tr_b16 a[86:87], v19 offset:13088\n"
    "ds_read_b64_tr_b16 a[160:161], v19 offset:64\n"
    "ds_read_b64_tr_b16 a[162:163], v19 offset:832\n"
    "ds_read_b64_tr_b16 a[164:165], v19 offset:12352\n"
    "ds_read_b64_tr_b16 a[166:167], v19 offset:13120\n"
    "s_waitcnt vmcnt(24) lgkmcnt(0)\n"
    "s_barrier\n"
    "s_add_u32 m0, s51, 0\n"
    "s_add_u32 s49, s50, 4718592\n"
    "buffer_load_dwordx4 v16, s[76:79], s49 offen lds\n"
    "s_add_u32 m0, s51, 1024\n"
    "s_add_u32 s49, s50, 4718592\n"
    "buffer_load_dwordx4 v17, s[76:79], s49 offen lds\n"
    "s_add_u32 m0, s51, 2048\n"
    "s_add_u32 s49, s50, 4718592\n"
    "buffer_load_dwordx4 v18, s[76:79], s49 offen lds\n"
    "s_add_u32 m0, s51, 3072\n"
    "s_add_u32 s49, s50, 4816896\n"
    "buffer_load_dwordx4 v16, s[76:79], s49 offen lds\n"
    "s_add_u32 m0, s51, 4096\n"
    "s_add_u32 s49, s50, 4816896\n"
    "buffer_load_dwordx4 v17, s[76:79], s49 offen lds\n"
    "s_add_u32 m0, s51, 5120\n"
    "s_add_u32 s49, s50, 4816896\n"
    "buffer_load_dwordx4 v18, s[76:79], s49 offen lds\n"
    "ds_read_b64_tr_b16 a[8:9], v19 offset:24576\n"
    "ds_read_b64_tr_b16 a[10:11], v19 offset:25344\n"
    "ds_read_b64_tr_b16 a[12:13], v19 offset:36864\n"
    "ds_read_b64_tr_b16 a[14:15], v19 offset:37632\n"
    "ds_read_b64_tr_b16 a[88:89], v19 offset:24608\n"
    "ds_read_b64_tr_b16 a[90:91], v19 offset:25376\n"
    "ds_read_b64_tr_b16 a[92:93], v19 offset:36896\n"
    "ds_read_b64_tr_b16 a[94:95], v19 offset:37664\n"
    "ds_read_b64_tr_b16 a[168:169], v19 offset:24640\n"
    "ds_read_b64_tr_b16 a[170:171], v19 offset:25408\n"
    "ds_read_b64_tr_b16 a[172:173], v19 offset:36928\n"
    "ds_read_b64_tr_b16 a[174:175], v19 offset:37696\n"
    "s_waitcnt vmcnt(24) lgkmcnt(0)\n"
    "s_barrier\n"
    "s_add_u32 m0, s51, 24576\n"
    "s_add_u32 s49, s50, 5505024\n"
    "buffer_load_dwordx4 v16, s[76:79], s49 offen lds\n"
    "s_add_u32 m0, s51, 25600\n"
    "s_add_u32 s49, s50, 5505024\n"
    "buffer_load_dwordx4 v17, s[76:79], s49 offen lds\n"
    "s_add_u32 m0, s51, 26624\n"
    "s_add_u32 s49, s50, 5505024\n"
    "buffer_load_dwordx4 v18, s[76:79], s49 offen lds\n"
    "s_add_u32 m0, s51, 27648\n"
    "s_add_u32 s49, s50, 5603328\n"
    "buffer_load_dwordx4 v16, s[76:79], s49 offen lds\n"
    "s_add_u32 m0, s51, 28672\n"
    "s_add_u32 s49, s50, 5603328\n"
    "buffer_load_dwordx4 v17, s[76:79], s49 offen lds\n"
    "s_add_u32 m0, s51, 29696\n"
    "s_add_u32 s49, s50, 5603328\n"
    "buffer_load_dwordx4 v18, s[76:79], s49 offen lds\n"
    "ds_read_b64_tr_b16 a[16:17], v20 offset:0\n"
    "ds_read_b64_tr_b16 a[18:19], v20 offset:768\n"
    "ds_read_b64_tr_b16 a[20:21], v20 offset:12288\n"
    "ds_read_b64_tr_b16 a[22:23], v20 offset:13056\n"
    "ds_read_b64_tr_b16 a[96:97], v20 offset:32\n"
    "ds_read_b64_tr_b16 a[98:99], v20 offset:800\n"
    "ds_read_b64_tr_b16 a[100:101], v20 offset:12320\n"
    "ds_read_b64_tr_b16 a[102:103], v20 offset:13088\n"
    "ds_read_b64_tr_b16 a[176:177], v20 offset:64\n"
    "ds_read_b64_tr_b16 a[178:179], v20 offset:832\n"
    "ds_read_b64_tr_b16 a[180:181], v20 offset:12352\n"
    "ds_read_b64_tr_b16 a[182:183], v20 offset:13120\n"
    "s_waitcnt vmcnt(24) lgkmcnt(0)\n"
    "s_barrier\n"
    "s_add_u32 m0, s51, 49152\n"
    "s_add_u32 s49, s50, 6291456\n"
    "buffer_load_dwordx4 v16, s[76:79], s49 offen lds\n"
    "s_add_u32 m0, s51, 50176\n"
    "s_add_u32 s49, s50, 6291456\n"
    "buffer_load_dwordx4 v17, s[76:79], s49 offen lds\n"
    "s_add_u32 m0, s51, 51200\n"
    "s_add_u32 s49, s50, 6291456\n"
    "buffer_load_dwordx4 v18, s[76:79], s49 offen lds\n"
    "s_add_u32 m0, s51, 52224\n"
    "s_add_u32 s49, s50, 6389760\n"
    "buffer_load_dwordx4 v16, s[76:79], s49 offen lds\n"
    "s_add_u32 m0, s51, 53248\n"
    "s_add_u32 s49, s50, 6389760\n"
    "buffer_load_dwordx4 v17, s[76:79], s49 offen lds\n"
    "s_add_u32 m0, s51, 54272\n"
    "s_add_u32 s49, s50, 6389760\n"
    "buffer_load_dwordx4 v18, s[76:79], s49 offen lds\n"
    "ds_read_b64_tr_b16 a[24:25], v20 offset:24576\n"
    "ds_read_b64_tr_b16 a[26:27], v20 offset:25344\n"
    "ds_read_b64_tr_b16 a[28:29], v20 offset:36864\n"
    "ds_read_b64_tr_b16 a[30:31], v20 offset:37632\n"
    "ds_read_b64_tr_b16 a[104:105], v20 offset:24608\n"
    "ds_read_b64_tr_b16 a[106:107], v20 offset:25376\n"
    "ds_read_b64_tr_b16 a[108:109], v20 offset:36896\n"
    "ds_read_b64_tr_b16 a[110:111], v20 offset:37664\n"
    "ds_read_b64_tr_b16 a[184:185], v20 offset:24640\n"
    "ds_read_b64_tr_b16 a[186:187], v20 offset:25408\n"
    "ds_read_b64_tr_b16 a[188:189], v20 offset:36928\n"
    "ds_read_b64_tr_b16 a[190:191], v20 offset:37696\n"
    "s_waitcnt vmcnt(24) lgkmcnt(0)\n"
    "s_barrier\n"
    "s_add_u32 m0, s51, 73728\n"
    "s_add_u32 s49, s50, 7077888\n"
    "buffer_load_dwordx4 v16, s[76:79], s49 offen lds\n"
    "s_add_u32 m0, s51, 74752\n"
    "s_add_u32 s49, s50, 7077888\n"
    "buffer_load_dwordx4 v17, s[76:79], s49 offen lds\n"
    "s_add_u32 m0, s51, 75776\n"
    "s_add_u32 s49, s50, 7077888\n"
    "buffer_load_dwordx4 v18, s[76:79], s49 offen lds\n"
    "s_add_u32 m0, s51, 76800\n"
    "s_add_u32 s49, s50, 7176192\n"
    "buffer_load_dwordx4 v16, s[76:79], s49 offen lds\n"
    "s_add_u32 m0, s51, 77824\n"
    "s_add_u32 s49, s50, 7176192\n"
    "buffer_load_dwordx4 v17, s[76:79], s49 offen lds\n"
    "s_add_u32 m0, s51, 78848\n"
    "s_add_u32 s49, s50, 7176192\n"
    "buffer_load_dwordx4 v18, s[76:79], s49 offen lds\n"
    "ds_read_b64_tr_b16 a[32:33], v21 offset:0\n"
    "ds_read_b64_tr_b16 a[34:35], v21 offset:768\n"
    "ds_read_b64_tr_b16 a[36:37], v21 offset:12288\n"
    "ds_read_b64_tr_b16 a[38:39], v21 offset:13056\n"
    "ds_read_b64_tr_b16 a[112:113], v21 offset:32\n"
    "ds_read_b64_tr_b16 a[114:115], v21 offset:800\n"
    "ds_read_b64_tr_b16 a[116:117], v21 offset:12320\n"
    "ds_read_b64_tr_b16 a[118:119], v21 offset:13088\n"
    "ds_read_b64_tr_b16 a[192:193], v21 offset:64\n"
    "ds_read_b64_tr_b16 a[194:195], v21 offset:832\n"
    "ds_read_b64_tr_b16 a[196:197], v21 offset:12352\n"
    "ds_read_b64_tr_b16 a[198:199], v21 offset:13120\n"
    "s_waitcnt vmcnt(24) lgkmcnt(0)\n"
    "s_barrier\n"
    "ds_read_b64_tr_b16 a[40:41], v21 offset:24576\n"
    "ds_read_b64_tr_b16 a[42:43], v21 offset:25344\n"
    "ds_read_b64_tr_b16 a[44:45], v21 offset:36864\n"
    "ds_read_b64_tr_b16 a[46:47], v21 offset:37632\n"
    "ds_read_b64_tr_b16 a[120:121], v21 offset:24608\n"
    "ds_read_b64_tr_b16 a[122:123], v21 offset:25376\n"
    "ds_read_b64_tr_b16 a[124:125], v21 offset:36896\n"
    "ds_read_b64_tr_b16 a[126:127], v21 offset:37664\n"
    "ds_read_b64_tr_b16 a[200:201], v21 offset:24640\n"
    "ds_read_b64_tr_b16 a[202:203], v21 offset:25408\n"
    "ds_read_b64_tr_b16 a[204:205], v21 offset:36928\n"
    "ds_read_b64_tr_b16 a[206:207], v21 offset:37696\n"
    "s_waitcnt vmcnt(18) lgkmcnt(0)\n"
    "s_barrier\n"
    "ds_read_b64_tr_b16 a[48:49], v19 offset:0\n"
    "ds_read_b64_tr_b16 a[50:51], v19 offset:768\n"
    "ds_read_b64_tr_b16 a[52:53], v19 offset:12288\n"
    "ds_read_b64_tr_b16 a[54:55], v19 offset:13056\n"
    "ds_read_b64_tr_b16 a[128:129], v19 offset:32\n"
    "ds_read_b64_tr_b16 a[130:131], v19 offset:800\n"
    "ds_read_b64_tr_b16 a[132:133], v19 offset:12320\n"
    "ds_read_b64_tr_b16 a[134:135], v19 offset:13088\n"
    "ds_read_b64_tr_b16 a[208:209], v19 offset:64\n"
    "ds_read_b64_tr_b16 a[210:211], v19 offset:832\n"
    "ds_read_b64_tr_b16 a[212:213], v19 offset:12352\n"
    "ds_read_b64_tr_b16 a[214:215], v19 offset:13120\n"
    "s_waitcnt vmcnt(12) lgkmcnt(0)\n"
    "s_barrier\n"
    "ds_read_b64_tr_b16 a[56:57], v19 offset:24576\n"
    "ds_read_b64_tr_b16 a[58:59], v19 offset:25344\n"
    "ds_read_b64_tr_b16 a[60:61], v19 offset:36864\n"
    "ds_read_b64_tr_b16 a[62:63], v19 offset:37632\n"
    "ds_read_b64_tr_b16 a[136:137], v19 offset:24608\n"
    "ds_read_b64_tr_b16 a[138:139], v19 offset:25376\n"
    "ds_read_b64_tr_b16 a[140:141], v19 offset:36896\n"
    "ds_read_b64_tr_b16 a[142:143], v19 offset:37664\n"
    "ds_read_b64_tr_b16 a[216:217], v19 offset:24640\n"
    "ds_read_b64_tr_b16 a[218:219], v19 offset:25408\n"
    "ds_read_b64_tr_b16 a[220:221], v19 offset:36928\n"
    "ds_read_b64_tr_b16 a[222:223], v19 offset:37696\n"
    "s_waitcnt vmcnt(6) lgkmcnt(0)\n"
    "s_barrier\n"
    "ds_read_b64_tr_b16 a[64:65], v20 offset:0\n"
    "ds_read_b64_tr_b16 a[66:67], v20 offset:768\n"
    "ds_read_b64_tr_b16 a[68:69], v20 offset:12288\n"
    "ds_read_b64_tr_b16 a[70:71], v20 offset:13056\n"
    "ds_read_b64_tr_b16 a[144:145], v20 offset:32\n"
    "ds_read_b64_tr_b16 a[146:147], v20 offset:800\n"
    "ds_read_b64_tr_b16 a[148:149], v20 offset:12320\n"
    "ds_read_b64_tr_b16 a[150:151], v20 offset:13088\n"
    "ds_read_b64_tr_b16 a[224:225], v20 offset:64\n"
    "ds_read_b64_tr_b16 a[226:227], v20 offset:832\n"
    "ds_read_b64_tr_b16 a[228:229], v20 offset:12352\n"
    "ds_read_b64_tr_b16 a[230:231], v20 offset:13120\n"
    "s_waitcnt vmcnt(0) lgkmcnt(0)\n"
    "s_barrier\n"
    "ds_read_b64_tr_b16 a[72:73], v20 offset:24576\n"
    "ds_read_b64_tr_b16 a[74:75], v20 offset:25344\n"
    "ds_read_b64_tr_b16 a[76:77], v20 offset:36864\n"
    "ds_read_b64_tr_b16 a[78:79], v20 offset:37632\n"
    "ds_read_b64_tr_b16 a[152:153], v20 offset:24608\n"
    "ds_read_b64_tr_b16 a[154:155], v20 offset:25376\n"
    "ds_read_b64_tr_b16 a[156:157], v20 offset:36896\n"
    "ds_read_b64_tr_b16 a[158:159], v20 offset:37664\n"
    "ds_read_b64_tr_b16 a[232:233], v20 offset:24640\n"
    "ds_read_b64_tr_b16 a[234:235], v20 offset:25408\n"
    "ds_read_b64_tr_b16 a[236:237], v20 offset:36928\n"
    "ds_read_b64_tr_b16 a[238:239], v20 offset:37696\n"
    "s_waitcnt lgkmcnt(0)\n"
    "s_barrier\n"
    "s_cmp_eq_u32 s46, 0\n"
    "s_cbranch_scc1 hp_layer_%=\n"
    "s_branch hp_layer_%=\n"
    "hp_ldo_%=:\n"
    "v_lshrrev_b32 v16, 2, v15\n"
    "v_mul_u32_u24 v16, 1280, v16\n"
    "v_and_b32 v17, 3, v15\n"
    "v_lshl_add_u32 v16, v17, 3, v16\n"
    "s_mul_i32 s48, s45, 320\n"
    "v_add_u32 v16, s48, v16\n"
    "v_add_u32 v17, 40960, v16\n"
    "v_add_u32 v18, 81920, v16\n"
    "s_mul_i32 s51, s45, 10240\n"
    "s_mul_i32 s50, s44, 245760\n"
    "s_add_u32 s50, s50, s51\n"
    "s_add_u32 m0, s51, 0\n"
    "s_add_u32 s49, s50, 0\n"
    "buffer_load_dwordx4 v0, s[76:79], s49 offen offset:0 lds\n"
    "buffer_load_dwordx4 v0, s[76:79], s49 offen offset:1024 lds\n"
    "buffer_load_dwordx4 v0, s[76:79], s49 offen offset:2048 lds\n"
    "buffer_load_dwordx4 v0, s[76:79], s49 offen offset:3072 lds\n"
    "s_add_u32 m0, s51, 4096\n"
    "s_add_u32 s49, s50, 4096\n"
    "buffer_load_dwordx4 v0, s[76:79], s49 offen offset:0 lds\n"
    "buffer_load_dwordx4 v0, s[76:79], s49 offen offset:1024 lds\n"
    "buffer_load_dwordx4 v0, s[76:79], s49 offen offset:2048 lds\n"
    "buffer_load_dwordx4 v0, s[76:79], s49 offen offset:3072 lds\n"
    "s_add_u32 m0, s51, 8192\n"
    "s_add_u32 s49, s50, 8192\n"
    "buffer_load_dwordx4 v0, s[76:79], s49 offen offset:0 lds\n"
    "buffer_load_dwordx4 v0, s[76:79], s49 offen offset:1024 lds\n"
    "s_add_u32 m0, s51, 40960\n"
    "s_add_u32 s49, s50, 40960\n"
    "buffer_load_dwordx4 v0, s[76:79], s49 offen offset:0 lds\n"
    "buffer_load_dwordx4 v0, s[76:79], s49 offen offset:1024 lds\n"
    "buffer_load_dwordx4 v0, s[76:79], s49 offen offset:2048 lds\n"
    "buffer_load_dwordx4 v0, s[76:79], s49 offen offset:3072 lds\n"
    "s_add_u32 m0, s51, 45056\n"
    "s_add_u32 s49, s50, 45056\n"
    "buffer_load_dwordx4 v0, s[76:79], s49 offen offset:0 lds\n"
    "buffer_load_dwordx4 v0, s[76:79], s49 offen offset:1024 lds\n"
    "buffer_load_dwordx4 v0, s[76:79], s49 offen offset:2048 lds\n"
    "buffer_load_dwordx4 v0, s[76:79], s49 offen offset:3072 lds\n"
    "s_add_u32 m0, s51, 49152\n"
    "s_add_u32 s49, s50, 49152\n"
    "buffer_load_dwordx4 v0, s[76:79], s49 offen offset:0 lds\n"
    "buffer_load_dwordx4 v0, s[76:79], s49 offen offset:1024 lds\n"
    "s_waitcnt vmcnt(10) lgkmcnt(0)\n"
    "s_barrier\n"
    "s_add_u32 m0, s51, 81920\n"
    "s_add_u32 s49, s50, 81920\n"
    "buffer_load_dwordx4 v0, s[76:79], s49 offen offset:0 lds\n"
    "buffer_load_dwordx4 v0, s[76:79], s49 offen offset:1024 lds\n"
    "buffer_load_dwordx4 v0, s[76:79], s49 offen offset:2048 lds\n"
    "buffer_load_dwordx4 v0, s[76:79], s49 offen offset:3072 lds\n"
    "s_add_u32 m0, s51, 86016\n"
    "s_add_u32 s49, s50, 86016\n"
    "buffer_load_dwordx4 v0, s[76:79], s49 offen offset:0 lds\n"
    "buffer_load_dwordx4 v0, s[76:79], s49 offen offset:1024 lds\n"
    "buffer_load_dwordx4 v0, s[76:79], s49 offen offset:2048 lds\n"
    "buffer_load_dwordx4 v0, s[76:79], s49 offen offset:3072 lds\n"
    "s_add_u32 m0, s51, 90112\n"
    "s_add_u32 s49, s50, 90112\n"
    "buffer_load_dwordx4 v0, s[76:79], s49 offen offset:0 lds\n"
    "buffer_load_dwordx4 v0, s[76:79], s49 offen offset:1024 lds\n"
    "ds_read_b64_tr_b16 a[0:1], v16 offset:0\n"
    "ds_read_b64_tr_b16 a[2:3], v16 offset:20480\n"
    "ds_read_b64_tr_b16 a[24:25], v16 offset:32\n"
    "ds_read_b64_tr_b16 a[26:27], v16 offset:20512\n"
    "ds_read_b64_tr_b16 a[48:49], v16 offset:64\n"
    "ds_read_b64_tr_b16 a[50:51], v16 offset:20544\n"
    "ds_read_b64_tr_b16 a[72:73], v16 offset:96\n"
    "ds_read_b64_tr_b16 a[74:75], v16 offset:20576\n"
    "ds_read_b64_tr_b16 a[96:97], v16 offset:128\n"
    "ds_read_b64_tr_b16 a[98:99], v16 offset:20608\n"
    "ds_read_b64_tr_b16 a[120:121], v16 offset:160\n"
    "ds_read_b64_tr_b16 a[122:123], v16 offset:20640\n"
    "ds_read_b64_tr_b16 a[144:145], v16 offset:192\n"
    "ds_read_b64_tr_b16 a[146:147], v16 offset:20672\n"
    "ds_read_b64_tr_b16 a[168:169], v16 offset:224\n"
    "ds_read_b64_tr_b16 a[170:171], v16 offset:20704\n"
    "s_waitcnt lgkmcnt(14)\n"
    "ds_read_b64_tr_b16 a[192:193], v16 offset:256\n"
    "ds_read_b64_tr_b16 a[194:195], v16 offset:20736\n"
    "s_waitcnt lgkmcnt(14)\n"
    "ds_read_b64_tr_b16 a[216:217], v16 offset:288\n"
    "ds_read_b64_tr_b16 a[218:219], v16 offset:20768\n"
    "s_waitcnt lgkmcnt(14)\n"
    "s_waitcnt vmcnt(10) lgkmcnt(0)\n"
    "s_barrier\n"
    "s_add_u32 m0, s51, 0\n"
    "s_add_u32 s49, s50, 122880\n"
    "buffer_load_dwordx4 v0, s[76:79], s49 offen offset:0 lds\n"
    "buffer_load_dwordx4 v0, s[76:79], s49 offen offset:1024 lds\n"
    "buffer_load_dwordx4 v0, s[76:79], s49 offen offset:2048 lds\n"
    "buffer_load_dwordx4 v0, s[76:79], s49 offen offset:3072 lds\n"
    "s_add_u32 m0, s51, 4096\n"
    "s_add_u32 s49, s50, 126976\n"
    "buffer_load_dwordx4 v0, s[76:79], s49 offen offset:0 lds\n"
    "buffer_load_dwordx4 v0, s[76:79], s49 offen offset:1024 lds\n"
    "buffer_load_dwordx4 v0, s[76:79], s49 offen offset:2048 lds\n"
    "buffer_load_dwordx4 v0, s[76:79], s49 offen offset:3072 lds\n"
    "s_add_u32 m0, s51, 8192\n"
    "s_add_u32 s49, s50, 131072\n"
    "buffer_load_dwordx4 v0, s[76:79], s49 offen offset:0 lds\n"
    "buffer_load_dwordx4 v0, s[76:79], s49 offen offset:1024 lds\n"
    "ds_read_b64_tr_b16 a[4:5], v17 offset:0\n"
    "ds_read_b64_tr_b16 a[6:7], v17 offset:20480\n"
    "ds_read_b64_tr_b16 a[28:29], v17 offset:32\n"
    "ds_read_b64_tr_b16 a[30:31], v17 offset:20512\n"
    "ds_read_b64_tr_b16 a[52:53], v17 offset:64\n"
    "ds_read_b64_tr_b16 a[54:55], v17 offset:20544\n"
    "ds_read_b64_tr_b16 a[76:77], v17 offset:96\n"
    "ds_read_b64_tr_b16 a[78:79], v17 offset:20576\n"
    "ds_read_b64_tr_b16 a[100:101], v17 offset:128\n"
    "ds_read_b64_tr_b16 a[102:103], v17 offset:20608\n"
    "ds_read_b64_tr_b16 a[124:125], v17 offset:160\n"
    "ds_read_b64_tr_b16 a[126:127], v17 offset:20640\n"
    "ds_read_b64_tr_b16 a[148:149], v17 offset:192\n"
    "ds_read_b64_tr_b16 a[150:151], v17 offset:20672\n"
    "ds_read_b64_tr_b16 a[172:173], v17 offset:224\n"
    "ds_read_b64_tr_b16 a[174:175], v17 offset:20704\n"
    "s_waitcnt lgkmcnt(14)\n"
    "ds_read_b64_tr_b16 a[196:197], v17 offset:256\n"
    "ds_read_b64_tr_b16 a[198:199], v17 offset:20736\n"
    "s_waitcnt lgkmcnt(14)\n"
    "ds_read_b64_tr_b16 a[220:221], v17 offset:288\n"
    "ds_read_b64_tr_b16 a[222:223], v17 offset:20768\n"
    "s_waitcnt lgkmcnt(14)\n"
    "s_waitcnt vmcnt(10) lgkmcnt(0)\n"
    "s_barrier\n"
    "s_add_u32 m0, s51, 40960\n"
    "s_add_u32 s49, s50, 163840\n"
    "buffer_load_dwordx4 v0, s[76:79], s49 offen offset:0 lds\n"
    "buffer_load_dwordx4 v0, s[76:79], s49 offen offset:1024 lds\n"
    "buffer_load_dwordx4 v0, s[76:79], s49 offen offset:2048 lds\n"
    "buffer_load_dwordx4 v0, s[76:79], s49 offen offset:3072 lds\n"
    "s_add_u32 m0, s51, 45056\n"
    "s_add_u32 s49, s50, 167936\n"
    "buffer_load_dwordx4 v0, s[76:79], s49 offen offset:0 lds\n"
    "buffer_load_dwordx4 v0, s[76:79], s49 offen offset:1024 lds\n"
    "buffer_load_dwordx4 v0, s[76:79], s49 offen offset:2048 lds\n"
    "buffer_load_dwordx4 v0, s[76:79], s49 offen offset:3072 lds\n"
    "s_add_u32 m0, s51, 49152\n"
    "s_add_u32 s49, s50, 172032\n"
    "buffer_load_dwordx4 v0, s[76:79], s49 offen offset:0 lds\n"
    "buffer_load_dwordx4 v0, s[76:79], s49 offen offset:1024 lds\n"
    "ds_read_b64_tr_b16 a[8:9], v18 offset:0\n"
    "ds_read_b64_tr_b16 a[10:11], v18 offset:20480\n"
    "ds_read_b64_tr_b16 a[32:33], v18 offset:32\n"
    "ds_read_b64_tr_b16 a[34:35], v18 offset:20512\n"
    "ds_read_b64_tr_b16 a[56:57], v18 offset:64\n"
    "ds_read_b64_tr_b16 a[58:59], v18 offset:20544\n"
    "ds_read_b64_tr_b16 a[80:81], v18 offset:96\n"
    "ds_read_b64_tr_b16 a[82:83], v18 offset:20576\n"
    "ds_read_b64_tr_b16 a[104:105], v18 offset:128\n"
    "ds_read_b64_tr_b16 a[106:107], v18 offset:20608\n"
    "ds_read_b64_tr_b16 a[128:129], v18 offset:160\n"
    "ds_read_b64_tr_b16 a[130:131], v18 offset:20640\n"
    "ds_read_b64_tr_b16 a[152:153], v18 offset:192\n"
    "ds_read_b64_tr_b16 a[154:155], v18 offset:20672\n"
    "ds_read_b64_tr_b16 a[176:177], v18 offset:224\n"
    "ds_read_b64_tr_b16 a[178:179], v18 offset:20704\n"
    "s_waitcnt lgkmcnt(14)\n"
    "ds_read_b64_tr_b16 a[200:201], v18 offset:256\n"
    "ds_read_b64_tr_b16 a[202:203], v18 offset:20736\n"
    "s_waitcnt lgkmcnt(14)\n"
    "ds_read_b64_tr_b16 a[224:225], v18 offset:288\n"
    "ds_read_b64_tr_b16 a[226:227], v18 offset:20768\n"
    "s_waitcnt lgkmcnt(14)\n"
    "s_waitcnt vmcnt(10) lgkmcnt(0)\n"
    "s_barrier\n"
    "s_add_u32 m0, s51, 81920\n"
    "s_add_u32 s49, s50, 204800\n"
    "buffer_load_dwordx4 v0, s[76:79], s49 offen offset:0 lds\n"
    "buffer_load_dwordx4 v0, s[76:79], s49 offen offset:1024 lds\n"
    "buffer_load_dwordx4 v0, s[76:79], s49 offen offset:2048 lds\n"
    "buffer_load_dwordx4 v0, s[76:79], s49 offen offset:3072 lds\n"
    "s_add_u32 m0, s51, 86016\n"
    "s_add_u32 s49, s50, 208896\n"
    "buffer_load_dwordx4 v0, s[76:79], s49 offen offset:0 lds\n"
    "buffer_load_dwordx4 v0, s[76:79], s49 offen offset:1024 lds\n"
    "buffer_load_dwordx4 v0, s[76:79], s49 offen offset:2048 lds\n"
    "buffer_load_dwordx4 v0, s[76:79], s49 offen offset:3072 lds\n"
    "s_add_u32 m0, s51, 90112\n"
    "s_add_u32 s49, s50, 212992\n"
    "buffer_load_dwordx4 v0, s[76:79], s49 offen offset:0 lds\n"
    "buffer_load_dwordx4 v0, s[76:79], s49 offen offset:1024 lds\n"
    "ds_read_b64_tr_b16 a[12:13], v16 offset:0\n"
    "ds_read_b64_tr_b16 a[14:15], v16 offset:20480\n"
    "ds_read_b64_tr_b16 a[36:37], v16 offset:32\n"
    "ds_read_b64_tr_b16 a[38:39], v16 offset:20512\n"
    "ds_read_b64_tr_b16 a[60:61], v16 offset:64\n"
    "ds_read_b64_tr_b16 a[62:63], v16 offset:20544\n"
    "ds_read_b64_tr_b16 a[84:85], v16 offset:96\n"
    "ds_read_b64_tr_b16 a[86:87], v16 offset:20576\n"
    "ds_read_b64_tr_b16 a[108:109], v16 offset:128\n"
    "ds_read_b64_tr_b16 a[110:111], v16 offset:20608\n"
    "ds_read_b64_tr_b16 a[132:133], v16 offset:160\n"
    "ds_read_b64_tr_b16 a[134:135], v16 offset:20640\n"
    "ds_read_b64_tr_b16 a[156:157], v16 offset:192\n"
    "ds_read_b64_tr_b16 a[158:159], v16 offset:20672\n"
    "ds_read_b64_tr_b16 a[180:181], v16 offset:224\n"
    "ds_read_b64_tr_b16 a[182:183], v16 offset:20704\n"
    "s_waitcnt lgkmcnt(14)\n"
    "ds_read_b64_tr_b16 a[204:205], v16 offset:256\n"
    "ds_read_b64_tr_b16 a[206:207], v16 offset:20736\n"
    "s_waitcnt lgkmcnt(14)\n"
    "ds_read_b64_tr_b16 a[228:229], v16 offset:288\n"
    "ds_read_b64_tr_b16 a[230:231], v16 offset:20768\n"
    "s_waitcnt lgkmcnt(14)\n"
    "s_waitcnt vmcnt(10) lgkmcnt(0)\n"
    "s_barrier\n"
    "ds_read_b64_tr_b16 a[16:17], v17 offset:0\n"
    "ds_read_b64_tr_b16 a[18:19], v17 offset:20480\n"
    "ds_read_b64_tr_b16 a[40:41], v17 offset:32\n"
    "ds_read_b64_tr_b16 a[42:43], v17 offset:20512\n"
    "ds_read_b64_tr_b16 a[64:65], v17 offset:64\n"
    "ds_read_b64_tr_b16 a[66:67], v17 offset:20544\n"
    "ds_read_b64_tr_b16 a[88:89], v17 offset:96\n"
    "ds_read_b64_tr_b16 a[90:91], v17 offset:20576\n"
    "ds_read_b64_tr_b16 a[112:113], v17 offset:128\n"
    "ds_read_b64_tr_b16 a[114:115], v17 offset:20608\n"
    "ds_read_b64_tr_b16 a[136:137], v17 offset:160\n"
    "ds_read_b64_tr_b16 a[138:139], v17 offset:20640\n"
    "ds_read_b64_tr_b16 a[160:161], v17 offset:192\n"
    "ds_read_b64_tr_b16 a[162:163], v17 offset:20672\n"
    "ds_read_b64_tr_b16 a[184:185], v17 offset:224\n"
    "ds_read_b64_tr_b16 a[186:187], v17 offset:20704\n"
    "s_waitcnt lgkmcnt(14)\n"
    "ds_read_b64_tr_b16 a[208:209], v17 offset:256\n"
    "ds_read_b64_tr_b16 a[210:211], v17 offset:20736\n"
    "s_waitcnt lgkmcnt(14)\n"
    "ds_read_b64_tr_b16 a[232:233], v17 offset:288\n"
    "ds_read_b64_tr_b16 a[234:235], v17 offset:20768\n"
    "s_waitcnt lgkmcnt(14)\n"
    "s_waitcnt vmcnt(0) lgkmcnt(0)\n"
    "s_barrier\n"
    "ds_read_b64_tr_b16 a[20:21], v18 offset:0\n"
    "ds_read_b64_tr_b16 a[22:23], v18 offset:20480\n"
    "ds_read_b64_tr_b16 a[44:45], v18 offset:32\n"
    "ds_read_b64_tr_b16 a[46:47], v18 offset:20512\n"
    "ds_read_b64_tr_b16 a[68:69], v18 offset:64\n"
    "ds_read_b64_tr_b16 a[70:71], v18 offset:20544\n"
    "ds_read_b64_tr_b16 a[92:93], v18 offset:96\n"
    "ds_read_b64_tr_b16 a[94:95], v18 offset:20576\n"
    "ds_read_b64_tr_b16 a[116:117], v18 offset:128\n"
    "ds_read_b64_tr_b16 a[118:119], v18 offset:20608\n"
    "ds_read_b64_tr_b16 a[140:141], v18 offset:160\n"
    "ds_read_b64_tr_b16 a[142:143], v18 offset:20640\n"
    "ds_read_b64_tr_b16 a[164:165], v18 offset:192\n"
    "ds_read_b64_tr_b16 a[166:167], v18 offset:20672\n"
    "ds_read_b64_tr_b16 a[188:189], v18 offset:224\n"
    "ds_read_b64_tr_b16 a[190:191], v18 offset:20704\n"
    "s_waitcnt lgkmcnt(14)\n"
    "ds_read_b64_tr_b16 a[212:213], v18 offset:256\n"
    "ds_read_b64_tr_b16 a[214:215], v18 offset:20736\n"
    "s_waitcnt lgkmcnt(14)\n"
    "ds_read_b64_tr_b16 a[236:237], v18 offset:288\n"
    "ds_read_b64_tr_b16 a[238:239], v18 offset:20768\n"
    "s_waitcnt lgkmcnt(14)\n"
    "s_waitcnt lgkmcnt(0)\n"
    "s_barrier\n"
    "hp_layer_%=:\n"
    "s_lshl_b32 s47, s21, 6\n"
    "s_cmp_eq_u32 s46, 0\n"
    "s_cbranch_scc1 hp_roleq_%=\n"
    "s_cmp_eq_u32 s46, 3\n"
    "s_cbranch_scc1 hp_roleo_%=\n"
    "s_mul_i32 s62, s45, 10240\n"
    "s_mul_i32 s50, s47, 40960\n"
    "s_add_u32 s50, s50, s62\n"
    "s_add_u32 s50, s50, 2523136\n"
    "s_mov_b32 s61, s62\n"
    "s_lshl_b32 s48, s47, 7\n"
    "s_add_u32 s48, s48, 43520\n"
    "v_mov_b32 v225, s48\n"
    "s_mov_b32 s48, 1\n"
    "v_mov_b32 v224, 0\n"
    "s_mov_b32 s60, 0\n"
    "v_cmp_gt_u32 vcc, s48, v224\n"
    "s_nop 4\n"
    "s_cbranch_vccz hpk_xfp0_ok_%=\n"
    "hpk_xfp0_poll_%=:\n"
    "buffer_load_dword v224, v225, s[40:43], s58 offen sc1\n"
    "s_waitcnt vmcnt(0)\n"
    "v_cmp_gt_u32 vcc, s48, v224\n"
    "s_nop 4\n"
    "s_cbranch_vccz hpk_xfp0_ok_%=\n"
    "s_sleep 2\n"
    "s_add_u32 s60, s60, 1\n"
    "s_cmp_gt_u32 s60, 65536\n"
    "s_cbranch_scc1 hp_abort_%=\n"
    "s_and_b32 s59, s60, 63\n"
    "s_cbranch_scc1 hpk_xfp0_poll_%=\n"
    "v_mov_b32 v4, 1024\n"
    "buffer_load_dword v4, v4, s[40:43], s58 offen sc1\n"
    "s_waitcnt vmcnt(0)\n"
    "v_readfirstlane_b32 s59, v4\n"
    "s_nop 0\n"
    "s_cmp_lg_u32 s59, 0\n"
    "s_nop 2\n"
    "s_cbranch_scc1 hp_end_%=\n"
    "s_branch hpk_xfp0_poll_%=\n"
    "hpk_xfp0_ok_%=:\n"
    "v_add_u32 v225, 128, v225\n"
    "s_add_u32 m0, s61, 0\n"
    "s_add_u32 s51, s50, 0\n"
    "buffer_load_dwordx4 v0, s[40:43], s51 offen offset:0 sc1 lds\n"
    "buffer_load_dwordx4 v0, s[40:43], s51 offen offset:1024 sc1 lds\n"
    "buffer_load_dwordx4 v0, s[40:43], s51 offen offset:2048 sc1 lds\n"
    "buffer_load_dwordx4 v0, s[40:43], s51 offen offset:3072 sc1 lds\n"
    "s_add_u32 m0, s61, 4096\n"
    "s_add_u32 s51, s50, 4096\n"
    "buffer_load_dwordx4 v0, s[40:43], s51 offen offset:0 sc1 lds\n"
    "buffer_load_dwordx4 v0, s[40:43], s51 offen offset:1024 sc1 lds\n"
    "buffer_load_dwordx4 v0, s[40:43], s51 offen offset:2048 sc1 lds\n"
    "buffer_load_dwordx4 v0, s[40:43], s51 offen offset:3072 sc1 lds\n"
    "s_add_u32 m0, s61, 8192\n"
    "s_add_u32 s51, s50, 8192\n"
    "buffer_load_dwordx4 v0, s[40:43], s51 offen offset:0 sc1 lds\n"
    "buffer_load_dwordx4 v0, s[40:43], s51 offen offset:1024 sc1 lds\n"
    "s_add_u32 s50, s50, 40960\n"
    "s_add_u32 s61, s61, 40960\n"
    "s_cmp_ge_u32 s61, 122880\n"
    "s_cselect_b32 s51, 122880, 0\n"
    "s_sub_u32 s61, s61, s51\n"
    "v_mov_b32 v224, 0\n"
    "s_mov_b32 s60, 0\n"
    "v_cmp_gt_u32 vcc, s48, v224\n"
    "s_nop 4\n"
    "s_cbranch_vccz hpk_xfp1_ok_%=\n"
    "hpk_xfp1_poll_%=:\n"
    "buffer_load_dword v224, v225, s[40:43], s58 offen sc1\n"
    "s_waitcnt vmcnt(0)\n"
    "v_cmp_gt_u32 vcc, s48, v224\n"
    "s_nop 4\n"
    "s_cbranch_vccz hpk_xfp1_ok_%=\n"
    "s_sleep 2\n"
    "s_add_u32 s60, s60, 1\n"
    "s_cmp_gt_u32 s60, 65536\n"
    "s_cbranch_scc1 hp_abort_%=\n"
    "s_and_b32 s59, s60, 63\n"
    "s_cbranch_scc1 hpk_xfp1_poll_%=\n"
    "v_mov_b32 v4, 1024\n"
    "buffer_load_dword v4, v4, s[40:43], s58 offen sc1\n"
    "s_waitcnt vmcnt(0)\n"
    "v_readfirstlane_b32 s59, v4\n"
    "s_nop 0\n"
    "s_cmp_lg_u32 s59, 0\n"
    "s_nop 2\n"
    "s_cbranch_scc1 hp_end_%=\n"
    "s_branch hpk_xfp1_poll_%=\n"
    "hpk_xfp1_ok_%=:\n"
    "v_add_u32 v225, 128, v225\n"
    "s_add_u32 m0, s61, 0\n"
    "s_add_u32 s51, s50, 0\n"
    "buffer_load_dwordx4 v0, s[40:43], s51 offen offset:0 sc1 lds\n"
    "buffer_load_dwordx4 v0, s[40:43], s51 offen offset:1024 sc1 lds\n"
    "buffer_load_dwordx4 v0, s[40:43], s51 offen offset:2048 sc1 lds\n"
    "buffer_load_dwordx4 v0, s[40:43], s51 offen offset:3072 sc1 lds\n"
    "s_add_u32 m0, s61, 4096\n"
    "s_add_u32 s51, s50, 4096\n"
    "buffer_load_dwordx4 v0, s[40:43], s51 offen offset:0 sc1 lds\n"
    "buffer_load_dwordx4 v0, s[40:43], s51 offen offset:1024 sc1 lds\n"
    "buffer_load_dwordx4 v0, s[40:43], s51 offen offset:2048 sc1 lds\n"
    "buffer_load_dwordx4 v0, s[40:43], s51 offen offset:3072 sc1 lds\n"
    "s_add_u32 m0, s61, 8192\n"
    "s_add_u32 s51, s50, 8192\n"
    "buffer_load_dwordx4 v0, s[40:43], s51 offen offset:0 sc1 lds\n"
    "buffer_load_dwordx4 v0, s[40:43], s51 offen offset:1024 sc1 lds\n"
    "s_add_u32 s50, s50, 40960\n"
    "s_add_u32 s61, s61, 40960\n"
    "s_cmp_ge_u32 s61, 122880\n"
    "s_cselect_b32 s51, 122880, 0\n"
    "s_sub_u32 s61, s61, s51\n"
    "v_mov_b32 v224, 0\n"
    "v_mov_b32 v226, 1\n"
    "v_mov_b32 v1, v0\n"
    "s_mov_b32 s64, 0\n"
    "s_lshl_b32 s48, s44, 7\n"
    "s_cmp_eq_u32 s45, 0\n"
    "s_cselect_b32 s59, 1, 0\n"
    "v_lshlrev_b32 v3, 7, v15\n"
    "v_cmp_gt_u32 vcc, s59, v15\n"
    "v_mov_b32 v4, 2147483632\n"
    "s_nop 0\n"
    "v_cndmask_b32 v3, v4, v3, vcc\n"
    "s_lshr_b32 s49, s47, 1\n"
    "s_lshl_b32 s49, s49, 7\n"
    "s_add_u32 s49, s49, 254976\n"
    "v_mov_b32 v10, s49\n"
    "v_mov_b32 v9, 0\n"
    "s_add_u32 s53, s48, 2099200\n"
    "s_lshl_b32 s54, s47, 7\n"
    "s_add_u32 s54, s54, 2107392\n"
    "s_mov_b32 s63, 20348928\n"
    "s_cmp_eq_u32 s46, 1\n"
    "s_cselect_b32 s53, s54, s53\n"
    "s_cselect_b32 s54, 128, 0\n"
    "s_cselect_b32 s63, 7766016, s63\n"
    "s_mul_i32 s49, s44, 12288\n"
    "s_add_u32 s63, s63, s49\n"
    "s_mul_i32 s49, s45, 3072\n"
    "s_add_u32 s63, s63, s49\n"
    "s_mov_b32 s52, 0x40000000\n"
    "s_waitcnt vmcnt(0)\n"
    "s_waitcnt lgkmcnt(0)\n"
    "s_barrier\n"
    "s_mov_b32 s65, 0x40000000\n"
    "ds_read_b128 v[80:83], v1 offset:0\n"
    "ds_read_b128 v[84:87], v1 offset:20480\n"
    "ds_read_b128 v[88:91], v1 offset:1024\n"
    "ds_read_b128 v[92:95], v1 offset:21504\n"
    "ds_read_b128 v[96:99], v1 offset:2048\n"
    "hpk_loop_%=:\n"
    "s_bitcmp1_b32 s47, 5\n"
    "s_cselect_b32 s48, 8, 0\n"
    "s_mov_b32 s60, 0\n"
    "v_cmp_gt_u32 vcc, s48, v9\n"
    "s_nop 4\n"
    "s_cbranch_vccz hpk_free_ok_%=\n"
    "hpk_free_poll_%=:\n"
    "buffer_load_dword v9, v10, s[40:43], s58 offen sc1\n"
    "s_waitcnt vmcnt(0)\n"
    "v_cmp_gt_u32 vcc, s48, v9\n"
    "s_nop 4\n"
    "s_cbranch_vccz hpk_free_ok_%=\n"
    "s_sleep 8\n"
    "s_add_u32 s60, s60, 1\n"
    "s_cmp_gt_u32 s60, 65536\n"
    "s_cbranch_scc1 hp_abort_%=\n"
    "s_and_b32 s59, s60, 63\n"
    "s_cbranch_scc1 hpk_free_poll_%=\n"
    "v_mov_b32 v4, 1024\n"
    "buffer_load_dword v4, v4, s[40:43], s58 offen sc1\n"
    "s_waitcnt vmcnt(0)\n"
    "v_readfirstlane_b32 s59, v4\n"
    "s_nop 0\n"
    "s_cmp_lg_u32 s59, 0\n"
    "s_nop 2\n"
    "s_cbranch_scc1 hp_end_%=\n"
    "s_branch hpk_free_poll_%=\n"
    "hpk_free_ok_%=:\n"
    "v_add_u32 v10, 128, v10\n"
    "s_waitcnt lgkmcnt(4)\n"
    "v_mfma_f32_16x16x32_bf16 v[16:19], a[0:3], v[80:83], 0\n"
    "v_mfma_f32_16x16x32_bf16 v[24:27], a[80:83], v[80:83], 0\n"
    "v_mfma_f32_16x16x32_bf16 v[32:35], a[160:163], v[80:83], 0\n"
    "ds_read_b128 v[80:83], v1 offset:22528\n"
    "s_waitcnt lgkmcnt(4)\n"
    "v_mfma_f32_16x16x32_bf16 v[20:23], a[0:3], v[84:87], 0\n"
    "v_mfma_f32_16x16x32_bf16 v[28:31], a[80:83], v[84:87], 0\n"
    "v_mfma_f32_16x16x32_bf16 v[36:39], a[160:163], v[84:87], 0\n"
    "ds_read_b128 v[84:87], v1 offset:3072\n"
    "s_waitcnt lgkmcnt(4)\n"
    "v_mfma_f32_16x16x32_bf16 v[16:19], a[4:7], v[88:91], v[16:19]\n"
    "v_mfma_f32_16x16x32_bf16 v[24:27], a[84:87], v[88:91], v[24:27]\n"
    "v_mfma_f32_16x16x32_bf16 v[32:35], a[164:167], v[88:91], v[32:35]\n"
    "ds_read_b128 v[88:91], v1 offset:23552\n"
    "s_waitcnt lgkmcnt(4)\n"
    "v_mfma_f32_16x16x32_bf16 v[20:23], a[4:7], v[92:95], v[20:23]\n"
    "v_cvt_pk_bf16_f32 v104, v40, v41\n"
    "v_mfma_f32_16x16x32_bf16 v[28:31], a[84:87], v[92:95], v[28:31]\n"
    "v_cvt_pk_bf16_f32 v105, v42, v43\n"
    "v_mfma_f32_16x16x32_bf16 v[36:39], a[164:167], v[92:95], v[36:39]\n"
    "ds_read_b128 v[92:95], v1 offset:4096\n"
    "s_waitcnt lgkmcnt(4)\n"
    "v_mfma_f32_16x16x32_bf16 v[16:19], a[8:11], v[96:99], v[16:19]\n"
    "v_cvt_pk_bf16_f32 v106, v44, v45\n"
    "v_mfma_f32_16x16x32_bf16 v[24:27], a[88:91], v[96:99], v[24:27]\n"
    "v_cvt_pk_bf16_f32 v107, v46, v47\n"
    "v_mfma_f32_16x16x32_bf16 v[32:35], a[168:171], v[96:99], v[32:35]\n"
    "ds_read_b128 v[96:99], v1 offset:24576\n"
    "s_waitcnt lgkmcnt(4)\n"
    "v_mfma_f32_16x16x32_bf16 v[20:23], a[8:11], v[80:83], v[20:23]\n"
    "buffer_store_dwordx4 v[104:107], v0, s[40:43], s65 offen offset:0 sc1\n"
    "v_mfma_f32_16x16x32_bf16 v[28:31], a[88:91], v[80:83], v[28:31]\n"
    "v_cvt_pk_bf16_f32 v108, v48, v49\n"
    "v_mfma_f32_16x16x32_bf16 v[36:39], a[168:171], v[80:83], v[36:39]\n"
    "ds_read_b128 v[80:83], v1 offset:5120\n"
    "s_waitcnt lgkmcnt(4)\n"
    "v_mfma_f32_16x16x32_bf16 v[16:19], a[12:15], v[84:87], v[16:19]\n"
    "v_cvt_pk_bf16_f32 v109, v50, v51\n"
    "v_mfma_f32_16x16x32_bf16 v[24:27], a[92:95], v[84:87], v[24:27]\n"
    "v_cvt_pk_bf16_f32 v110, v52, v53\n"
    "v_mfma_f32_16x16x32_bf16 v[32:35], a[172:175], v[84:87], v[32:35]\n"
    "ds_read_b128 v[84:87], v1 offset:25600\n"
    "s_waitcnt lgkmcnt(4)\n"
    "v_mfma_f32_16x16x32_bf16 v[20:23], a[12:15], v[88:91], v[20:23]\n"
    "v_cvt_pk_bf16_f32 v111, v54, v55\n"
    "v_mfma_f32_16x16x32_bf16 v[28:31], a[92:95], v[88:91], v[28:31]\n"
    "buffer_store_dwordx4 v[108:111], v0, s[40:43], s65 offen offset:1024 sc1\n"
    "v_mfma_f32_16x16x32_bf16 v[36:39], a[172:175], v[88:91], v[36:39]\n"
    "ds_read_b128 v[88:91], v1 offset:6144\n"
    "s_waitcnt lgkmcnt(4)\n"
    "v_mfma_f32_16x16x32_bf16 v[16:19], a[16:19], v[92:95], v[16:19]\n"
    "v_cvt_pk_bf16_f32 v112, v56, v57\n"
    "v_mfma_f32_16x16x32_bf16 v[24:27], a[96:99], v[92:95], v[24:27]\n"
    "v_cvt_pk_bf16_f32 v113, v58, v59\n"
    "v_mfma_f32_16x16x32_bf16 v[32:35], a[176:179], v[92:95], v[32:35]\n"
    "ds_read_b128 v[92:95], v1 offset:26624\n"
    "s_waitcnt lgkmcnt(4)\n"
    "v_mfma_f32_16x16x32_bf16 v[20:23], a[16:19], v[96:99], v[20:23]\n"
    "v_cvt_pk_bf16_f32 v114, v60, v61\n"
    "v_mfma_f32_16x16x32_bf16 v[28:31], a[96:99], v[96:99], v[28:31]\n"
    "v_cvt_pk_bf16_f32 v115, v62, v63\n"
    "v_mfma_f32_16x16x32_bf16 v[36:39], a[176:179], v[96:99], v[36:39]\n"
    "ds_read_b128 v[96:99], v1 offset:7168\n"
    "s_waitcnt lgkmcnt(4)\n"
    "v_mfma_f32_16x16x32_bf16 v[16:19], a[20:23], v[80:83], v[16:19]\n"
    "buffer_store_dwordx4 v[112:115], v0, s[40:43], s65 offen offset:2048 sc1\n"
    "v_mfma_f32_16x16x32_bf16 v[24:27], a[100:103], v[80:83], v[24:27]\n"
    "buffer_load_dword v224, v225, s[40:43], s58 offen sc1\n"
    "v_mfma_f32_16x16x32_bf16 v[32:35], a[180:183], v[80:83], v[32:35]\n"
    "ds_read_b128 v[80:83], v1 offset:27648\n"
    "s_waitcnt lgkmcnt(4)\n"
    "v_mfma_f32_16x16x32_bf16 v[20:23], a[20:23], v[84:87], v[20:23]\n"
    "v_mfma_f32_16x16x32_bf16 v[28:31], a[100:103], v[84:87], v[28:31]\n"
    "v_mfma_f32_16x16x32_bf16 v[36:39], a[180:183], v[84:87], v[36:39]\n"
    "ds_read_b128 v[84:87], v1 offset:8192\n"
    "s_waitcnt lgkmcnt(4)\n"
    "v_mfma_f32_16x16x32_bf16 v[16:19], a[24:27], v[88:91], v[16:19]\n"
    "v_mfma_f32_16x16x32_bf16 v[24:27], a[104:107], v[88:91], v[24:27]\n"
    "v_mfma_f32_16x16x32_bf16 v[32:35], a[184:187], v[88:91], v[32:35]\n"
    "ds_read_b128 v[88:91], v1 offset:28672\n"
    "s_waitcnt lgkmcnt(4)\n"
    "v_mfma_f32_16x16x32_bf16 v[20:23], a[24:27], v[92:95], v[20:23]\n"
    "v_mfma_f32_16x16x32_bf16 v[28:31], a[104:107], v[92:95], v[28:31]\n"
    "v_mfma_f32_16x16x32_bf16 v[36:39], a[184:187], v[92:95], v[36:39]\n"
    "ds_read_b128 v[92:95], v1 offset:9216\n"
    "s_waitcnt lgkmcnt(4)\n"
    "v_mfma_f32_16x16x32_bf16 v[16:19], a[28:31], v[96:99], v[16:19]\n"
    "v_mfma_f32_16x16x32_bf16 v[24:27], a[108:111], v[96:99], v[24:27]\n"
    "v_mfma_f32_16x16x32_bf16 v[32:35], a[188:191], v[96:99], v[32:35]\n"
    "ds_read_b128 v[96:99], v1 offset:29696\n"
    "s_waitcnt lgkmcnt(4)\n"
    "v_mfma_f32_16x16x32_bf16 v[20:23], a[28:31], v[80:83], v[20:23]\n"
    "v_mfma_f32_16x16x32_bf16 v[28:31], a[108:111], v[80:83], v[28:31]\n"
    "v_mfma_f32_16x16x32_bf16 v[36:39], a[188:191], v[80:83], v[36:39]\n"
    "ds_read_b128 v[80:83], v1 offset:10240\n"
    "s_waitcnt lgkmcnt(4)\n"
    "v_mfma_f32_16x16x32_bf16 v[16:19], a[32:35], v[84:87], v[16:19]\n"
    "v_mfma_f32_16x16x32_bf16 v[24:27], a[112:115], v[84:87], v[24:27]\n"
    "v_mfma_f32_16x16x32_bf16 v[32:35], a[192:195], v[84:87], v[32:35]\n"
    "ds_read_b128 v[84:87], v1 offset:30720\n"
    "s_waitcnt lgkmcnt(4)\n"
    "v_mfma_f32_16x16x32_bf16 v[20:23], a[32:35], v[88:91], v[20:23]\n"
    "v_mfma_f32_16x16x32_bf16 v[28:31], a[112:115], v[88:91], v[28:31]\n"
    "v_mfma_f32_16x16x32_bf16 v[36:39], a[192:195], v[88:91], v[36:39]\n"
    "ds_read_b128 v[88:91], v1 offset:11264\n"
    "s_waitcnt lgkmcnt(4)\n"
    "v_mfma_f32_16x16x32_bf16 v[16:19], a[36:39], v[92:95], v[16:19]\n"
    "v_mfma_f32_16x16x32_bf16 v[24:27], a[116:119], v[92:95], v[24:27]\n"
    "v_mfma_f32_16x16x32_bf16 v[32:35], a[196:199], v[92:95], v[32:35]\n"
    "ds_read_b128 v[92:95], v1 offset:31744\n"
    "s_waitcnt lgkmcnt(4)\n"
    "v_mfma_f32_16x16x32_bf16 v[20:23], a[36:39], v[96:99], v[20:23]\n"
    "v_mfma_f32_16x16x32_bf16 v[28:31], a[116:119], v[96:99], v[28:31]\n"
    "v_mfma_f32_16x16x32_bf16 v[36:39], a[196:199], v[96:99], v[36:39]\n"
    "ds_read_b128 v[96:99], v1 offset:12288\n"
    "s_waitcnt lgkmcnt(4)\n"
    "v_mfma_f32_16x16x32_bf16 v[16:19], a[40:43], v[80:83], v[16:19]\n"
    "v_mfma_f32_16x16x32_bf16 v[24:27], a[120:123], v[80:83], v[24:27]\n"
    "v_mfma_f32_16x16x32_bf16 v[32:35], a[200:203], v[80:83], v[32:35]\n"
    "ds_read_b128 v[80:83], v1 offset:32768\n"
    "s_waitcnt lgkmcnt(4)\n"
    "v_mfma_f32_16x16x32_bf16 v[20:23], a[40:43], v[84:87], v[20:23]\n"
    "v_mfma_f32_16x16x32_bf16 v[28:31], a[120:123], v[84:87], v[28:31]\n"
    "v_mfma_f32_16x16x32_bf16 v[36:39], a[200:203], v[84:87], v[36:39]\n"
    "ds_read_b128 v[84:87], v1 offset:13312\n"
    "s_waitcnt lgkmcnt(4)\n"
    "v_mfma_f32_16x16x32_bf16 v[16:19], a[44:47], v[88:91], v[16:19]\n"
    "v_mfma_f32_16x16x32_bf16 v[24:27], a[124:127], v[88:91], v[24:27]\n"
    "v_mfma_f32_16x16x32_bf16 v[32:35], a[204:207], v[88:91], v[32:35]\n"
    "ds_read_b128 v[88:91], v1 offset:33792\n"
    "s_waitcnt lgkmcnt(4)\n"
    "v_mfma_f32_16x16x32_bf16 v[20:23], a[44:47], v[92:95], v[20:23]\n"
    "v_mfma_f32_16x16x32_bf16 v[28:31], a[124:127], v[92:95], v[28:31]\n"
    "v_mfma_f32_16x16x32_bf16 v[36:39], a[204:207], v[92:95], v[36:39]\n"
    "ds_read_b128 v[92:95], v1 offset:14336\n"
    "s_waitcnt lgkmcnt(4)\n"
    "v_mfma_f32_16x16x32_bf16 v[16:19], a[48:51], v[96:99], v[16:19]\n"
    "v_mfma_f32_16x16x32_bf16 v[24:27], a[128:131], v[96:99], v[24:27]\n"
    "v_mfma_f32_16x16x32_bf16 v[32:35], a[208:211], v[96:99], v[32:35]\n"
    "ds_read_b128 v[96:99], v1 offset:34816\n"
    "s_waitcnt lgkmcnt(4)\n"
    "v_mfma_f32_16x16x32_bf16 v[20:23], a[48:51], v[80:83], v[20:23]\n"
    "v_mfma_f32_16x16x32_bf16 v[28:31], a[128:131], v[80:83], v[28:31]\n"
    "v_mfma_f32_16x16x32_bf16 v[36:39], a[208:211], v[80:83], v[36:39]\n"
    "ds_read_b128 v[80:83], v1 offset:15360\n"
    "s_waitcnt lgkmcnt(4)\n"
    "v_mfma_f32_16x16x32_bf16 v[16:19], a[52:55], v[84:87], v[16:19]\n"
    "v_mfma_f32_16x16x32_bf16 v[24:27], a[132:135], v[84:87], v[24:27]\n"
    "v_mfma_f32_16x16x32_bf16 v[32:35], a[212:215], v[84:87], v[32:35]\n"
    "ds_read_b128 v[84:87], v1 offset:35840\n"
    "s_waitcnt lgkmcnt(4)\n"
    "v_mfma_f32_16x16x32_bf16 v[20:23], a[52:55], v[88:91], v[20:23]\n"
    "v_mfma_f32_16x16x32_bf16 v[28:31], a[132:135], v[88:91], v[28:31]\n"
    "v_mfma_f32_16x16x32_bf16 v[36:39], a[212:215], v[88:91], v[36:39]\n"
    "ds_read_b128 v[88:91], v1 offset:16384\n"
    "s_waitcnt lgkmcnt(4)\n"
    "v_mfma_f32_16x16x32_bf16 v[16:19], a[56:59], v[92:95], v[16:19]\n"
    "v_mfma_f32_16x16x32_bf16 v[24:27], a[136:139], v[92:95], v[24:27]\n"
    "v_mfma_f32_16x16x32_bf16 v[32:35], a[216:219], v[92:95], v[32:35]\n"
    "ds_read_b128 v[92:95], v1 offset:36864\n"
    "s_waitcnt lgkmcnt(4)\n"
    "v_mfma_f32_16x16x32_bf16 v[20:23], a[56:59], v[96:99], v[20:23]\n"
    "v_mfma_f32_16x16x32_bf16 v[28:31], a[136:139], v[96:99], v[28:31]\n"
    "v_mfma_f32_16x16x32_bf16 v[36:39], a[216:219], v[96:99], v[36:39]\n"
    "s_waitcnt vmcnt(0)\n"
    "s_barrier\n"
    "buffer_atomic_add v226, v3, s[40:43], s52 offen\n"
    "s_and_b32 s48, s47, 63\n"
    "s_cmp_lt_u32 s48, 62\n"
    "s_cselect_b32 s48, 1, 0\n"
    "s_mov_b32 s60, 0\n"
    "v_cmp_gt_u32 vcc, s48, v224\n"
    "s_nop 4\n"
    "s_cbranch_vccz hpk_xf0_ok_%=\n"
    "hpk_xf0_poll_%=:\n"
    "buffer_load_dword v224, v225, s[40:43], s58 offen sc1\n"
    "s_waitcnt vmcnt(0)\n"
    "v_cmp_gt_u32 vcc, s48, v224\n"
    "s_nop 4\n"
    "s_cbranch_vccz hpk_xf0_ok_%=\n"
    "s_sleep 2\n"
    "s_add_u32 s60, s60, 1\n"
    "s_cmp_gt_u32 s60, 65536\n"
    "s_cbranch_scc1 hp_abort_%=\n"
    "s_and_b32 s59, s60, 63\n"
    "s_cbranch_scc1 hpk_xf0_poll_%=\n"
    "v_mov_b32 v4, 1024\n"
    "buffer_load_dword v4, v4, s[40:43], s58 offen sc1\n"
    "s_waitcnt vmcnt(0)\n"
    "v_readfirstlane_b32 s59, v4\n"
    "s_nop 0\n"
    "s_cmp_lg_u32 s59, 0\n"
    "s_nop 2\n"
    "s_cbranch_scc1 hp_end_%=\n"
    "s_branch hpk_xf0_poll_%=\n"
    "hpk_xf0_ok_%=:\n"
    "v_add_u32 v225, 128, v225\n"
    "ds_read_b128 v[96:99], v1 offset:17408\n"
    "s_waitcnt lgkmcnt(4)\n"
    "v_mfma_f32_16x16x32_bf16 v[16:19], a[60:63], v[80:83], v[16:19]\n"
    "s_add_u32 m0, s61, 0\n"
    "s_add_u32 s51, s50, 0\n"
    "buffer_load_dwordx4 v0, s[40:43], s51 offen offset:0 sc1 lds\n"
    "v_mfma_f32_16x16x32_bf16 v[24:27], a[140:143], v[80:83], v[24:27]\n"
    "v_mfma_f32_16x16x32_bf16 v[32:35], a[220:223], v[80:83], v[32:35]\n"
    "ds_read_b128 v[80:83], v1 offset:37888\n"
    "s_waitcnt lgkmcnt(4)\n"
    "v_mfma_f32_16x16x32_bf16 v[20:23], a[60:63], v[84:87], v[20:23]\n"
    "buffer_load_dwordx4 v0, s[40:43], s51 offen offset:1024 sc1 lds\n"
    "v_mfma_f32_16x16x32_bf16 v[28:31], a[140:143], v[84:87], v[28:31]\n"
    "v_mfma_f32_16x16x32_bf16 v[36:39], a[220:223], v[84:87], v[36:39]\n"
    "ds_read_b128 v[84:87], v1 offset:18432\n"
    "s_waitcnt lgkmcnt(4)\n"
    "v_mfma_f32_16x16x32_bf16 v[16:19], a[64:67], v[88:91], v[16:19]\n"
    "buffer_load_dwordx4 v0, s[40:43], s51 offen offset:2048 sc1 lds\n"
    "v_mfma_f32_16x16x32_bf16 v[24:27], a[144:147], v[88:91], v[24:27]\n"
    "v_mfma_f32_16x16x32_bf16 v[32:35], a[224:227], v[88:91], v[32:35]\n"
    "ds_read_b128 v[88:91], v1 offset:38912\n"
    "s_waitcnt lgkmcnt(4)\n"
    "v_mfma_f32_16x16x32_bf16 v[20:23], a[64:67], v[92:95], v[20:23]\n"
    "buffer_load_dwordx4 v0, s[40:43], s51 offen offset:3072 sc1 lds\n"
    "v_mfma_f32_16x16x32_bf16 v[28:31], a[144:147], v[92:95], v[28:31]\n"
    "v_mfma_f32_16x16x32_bf16 v[36:39], a[224:227], v[92:95], v[36:39]\n"
    "ds_read_b128 v[92:95], v1 offset:19456\n"
    "s_waitcnt lgkmcnt(4)\n"
    "v_mfma_f32_16x16x32_bf16 v[16:19], a[68:71], v[96:99], v[16:19]\n"
    "s_add_u32 m0, s61, 4096\n"
    "s_add_u32 s51, s50, 4096\n"
    "buffer_load_dwordx4 v0, s[40:43], s51 offen offset:0 sc1 lds\n"
    "v_mfma_f32_16x16x32_bf16 v[24:27], a[148:151], v[96:99], v[24:27]\n"
    "v_mfma_f32_16x16x32_bf16 v[32:35], a[228:231], v[96:99], v[32:35]\n"
    "ds_read_b128 v[96:99], v1 offset:39936\n"
    "s_waitcnt lgkmcnt(4)\n"
    "v_mfma_f32_16x16x32_bf16 v[20:23], a[68:71], v[80:83], v[20:23]\n"
    "buffer_load_dwordx4 v0, s[40:43], s51 offen offset:1024 sc1 lds\n"
    "v_mfma_f32_16x16x32_bf16 v[28:31], a[148:151], v[80:83], v[28:31]\n"
    "v_mfma_f32_16x16x32_bf16 v[36:39], a[228:231], v[80:83], v[36:39]\n"
    "s_add_u32 s64, s64, 40960\n"
    "s_cmp_ge_u32 s64, 122880\n"
    "s_cselect_b32 s64, 0, s64\n"
    "v_add_u32 v1, s64, v0\n"
    "ds_read_b128 v[80:83], v1 offset:0\n"
    "s_waitcnt lgkmcnt(4)\n"
    "v_mfma_f32_16x16x32_bf16 v[16:19], a[72:75], v[84:87], v[16:19]\n"
    "buffer_load_dwordx4 v0, s[40:43], s51 offen offset:2048 sc1 lds\n"
    "v_mfma_f32_16x16x32_bf16 v[24:27], a[152:155], v[84:87], v[24:27]\n"
    "v_mfma_f32_16x16x32_bf16 v[32:35], a[232:235], v[84:87], v[32:35]\n"
    "ds_read_b128 v[84:87], v1 offset:20480\n"
    "s_waitcnt lgkmcnt(4)\n"
    "v_mfma_f32_16x16x32_bf16 v[20:23], a[72:75], v[88:91], v[20:23]\n"
    "buffer_load_dwordx4 v0, s[40:43], s51 offen offset:3072 sc1 lds\n"
    "v_mfma_f32_16x16x32_bf16 v[28:31], a[152:155], v[88:91], v[28:31]\n"
    "v_mfma_f32_16x16x32_bf16 v[36:39], a[232:235], v[88:91], v[36:39]\n"
    "ds_read_b128 v[88:91], v1 offset:1024\n"
    "s_waitcnt lgkmcnt(4)\n"
    "v_mfma_f32_16x16x32_bf16 v[16:19], a[76:79], v[92:95], v[16:19]\n"
    "s_add_u32 m0, s61, 8192\n"
    "s_add_u32 s51, s50, 8192\n"
    "buffer_load_dwordx4 v0, s[40:43], s51 offen offset:0 sc1 lds\n"
    "v_mfma_f32_16x16x32_bf16 v[24:27], a[156:159], v[92:95], v[24:27]\n"
    "v_mfma_f32_16x16x32_bf16 v[32:35], a[236:239], v[92:95], v[32:35]\n"
    "ds_read_b128 v[92:95], v1 offset:21504\n"
    "s_waitcnt lgkmcnt(4)\n"
    "v_mfma_f32_16x16x32_bf16 v[20:23], a[76:79], v[96:99], v[20:23]\n"
    "buffer_load_dwordx4 v0, s[40:43], s51 offen offset:1024 sc1 lds\n"
    "s_add_u32 s50, s50, 40960\n"
    "s_add_u32 s61, s61, 40960\n"
    "s_cmp_ge_u32 s61, 122880\n"
    "s_cselect_b32 s51, 122880, 0\n"
    "s_sub_u32 s61, s61, s51\n"
    "v_mfma_f32_16x16x32_bf16 v[28:31], a[156:159], v[96:99], v[28:31]\n"
    "v_mfma_f32_16x16x32_bf16 v[36:39], a[236:239], v[96:99], v[36:39]\n"
    "ds_read_b128 v[96:99], v1 offset:2048\n"
    "s_mov_b32 s65, s63\n"
    "s_add_u32 s59, s47, 0\n"
    "s_and_b32 s59, s59, 63\n"
    "s_cmp_eq_u32 s59, 31\n"
    "s_cselect_b32 s59, -31, 1\n"
    "s_mul_i32 s57, s59, 393216\n"
    "s_add_u32 s63, s63, s57\n"
    "s_mov_b32 s52, s53\n"
    "s_add_u32 s53, s53, s54\n"
    "s_and_b32 s59, s47, 63\n"
    "s_cbranch_scc1 hpk_late_%=\n"
    "v_cvt_pk_bf16_f32 v104, v16, v17\n"
    "v_cvt_pk_bf16_f32 v105, v18, v19\n"
    "v_cvt_pk_bf16_f32 v106, v20, v21\n"
    "v_cvt_pk_bf16_f32 v107, v22, v23\n"
    "buffer_store_dwordx4 v[104:107], v0, s[40:43], s65 offen offset:0 sc1\n"
    "v_cvt_pk_bf16_f32 v108, v24, v25\n"
    "v_cvt_pk_bf16_f32 v109, v26, v27\n"
    "v_cvt_pk_bf16_f32 v110, v28, v29\n"
    "v_cvt_pk_bf16_f32 v111, v30, v31\n"
    "buffer_store_dwordx4 v[108:111], v0, s[40:43], s65 offen offset:1024 sc1\n"
    "v_cvt_pk_bf16_f32 v112, v32, v33\n"
    "v_cvt_pk_bf16_f32 v113, v34, v35\n"
    "v_cvt_pk_bf16_f32 v114, v36, v37\n"
    "v_cvt_pk_bf16_f32 v115, v38, v39\n"
    "buffer_store_dwordx4 v[112:115], v0, s[40:43], s65 offen offset:2048 sc1\n"
    "s_waitcnt vmcnt(0)\n"
    "s_barrier\n"
    "buffer_atomic_add v226, v3, s[40:43], s52 offen\n"
    "s_mov_b32 s52, 0x40000000\n"
    "s_nop 0\n"
    "hpk_late_%=:\n"
    "s_add_u32 s47, s47, 1\n"
    "s_waitcnt lgkmcnt(4)\n"
    "v_mfma_f32_16x16x32_bf16 v[40:43], a[0:3], v[80:83], 0\n"
    "v_mfma_f32_16x16x32_bf16 v[48:51], a[80:83], v[80:83], 0\n"
    "v_mfma_f32_16x16x32_bf16 v[56:59], a[160:163], v[80:83], 0\n"
    "ds_read_b128 v[80:83], v1 offset:22528\n"
    "s_waitcnt lgkmcnt(4)\n"
    "v_mfma_f32_16x16x32_bf16 v[44:47], a[0:3], v[84:87], 0\n"
    "v_mfma_f32_16x16x32_bf16 v[52:55], a[80:83], v[84:87], 0\n"
    "v_mfma_f32_16x16x32_bf16 v[60:63], a[160:163], v[84:87], 0\n"
    "ds_read_b128 v[84:87], v1 offset:3072\n"
    "s_waitcnt lgkmcnt(4)\n"
    "v_mfma_f32_16x16x32_bf16 v[40:43], a[4:7], v[88:91], v[40:43]\n"
    "v_mfma_f32_16x16x32_bf16 v[48:51], a[84:87], v[88:91], v[48:51]\n"
    "v_mfma_f32_16x16x32_bf16 v[56:59], a[164:167], v[88:91], v[56:59]\n"
    "ds_read_b128 v[88:91], v1 offset:23552\n"
    "s_waitcnt lgkmcnt(4)\n"
    "v_mfma_f32_16x16x32_bf16 v[44:47], a[4:7], v[92:95], v[44:47]\n"
    "v_cvt_pk_bf16_f32 v104, v16, v17\n"
    "v_mfma_f32_16x16x32_bf16 v[52:55], a[84:87], v[92:95], v[52:55]\n"
    "v_cvt_pk_bf16_f32 v105, v18, v19\n"
    "v_mfma_f32_16x16x32_bf16 v[60:63], a[164:167], v[92:95], v[60:63]\n"
    "ds_read_b128 v[92:95], v1 offset:4096\n"
    "s_waitcnt lgkmcnt(4)\n"
    "v_mfma_f32_16x16x32_bf16 v[40:43], a[8:11], v[96:99], v[40:43]\n"
    "v_cvt_pk_bf16_f32 v106, v20, v21\n"
    "v_mfma_f32_16x16x32_bf16 v[48:51], a[88:91], v[96:99], v[48:51]\n"
    "v_cvt_pk_bf16_f32 v107, v22, v23\n"
    "v_mfma_f32_16x16x32_bf16 v[56:59], a[168:171], v[96:99], v[56:59]\n"
    "ds_read_b128 v[96:99], v1 offset:24576\n"
    "s_waitcnt lgkmcnt(4)\n"
    "v_mfma_f32_16x16x32_bf16 v[44:47], a[8:11], v[80:83], v[44:47]\n"
    "buffer_store_dwordx4 v[104:107], v0, s[40:43], s65 offen offset:0 sc1\n"
    "v_mfma_f32_16x16x32_bf16 v[52:55], a[88:91], v[80:83], v[52:55]\n"
    "v_cvt_pk_bf16_f32 v108, v24, v25\n"
    "v_mfma_f32_16x16x32_bf16 v[60:63], a[168:171], v[80:83], v[60:63]\n"
    "ds_read_b128 v[80:83], v1 offset:5120\n"
    "s_waitcnt lgkmcnt(4)\n"
    "v_mfma_f32_16x16x32_bf16 v[40:43], a[12:15], v[84:87], v[40:43]\n"
    "v_cvt_pk_bf16_f32 v109, v26, v27\n"
    "v_mfma_f32_16x16x32_bf16 v[48:51], a[92:95], v[84:87], v[48:51]\n"
    "v_cvt_pk_bf16_f32 v110, v28, v29\n"
    "v_mfma_f32_16x16x32_bf16 v[56:59], a[172:175], v[84:87], v[56:59]\n"
    "ds_read_b128 v[84:87], v1 offset:25600\n"
    "s_waitcnt lgkmcnt(4)\n"
    "v_mfma_f32_16x16x32_bf16 v[44:47], a[12:15], v[88:91], v[44:47]\n"
    "v_cvt_pk_bf16_f32 v111, v30, v31\n"
    "v_mfma_f32_16x16x32_bf16 v[52:55], a[92:95], v[88:91], v[52:55]\n"
    "buffer_store_dwordx4 v[108:111], v0, s[40:43], s65 offen offset:1024 sc1\n"
    "v_mfma_f32_16x16x32_bf16 v[60:63], a[172:175], v[88:91], v[60:63]\n"
    "ds_read_b128 v[88:91], v1 offset:6144\n"
    "s_waitcnt lgkmcnt(4)\n"
    "v_mfma_f32_16x16x32_bf16 v[40:43], a[16:19], v[92:95], v[40:43]\n"
    "v_cvt_pk_bf16_f32 v112, v32, v33\n"
    "v_mfma_f32_16x16x32_bf16 v[48:51], a[96:99], v[92:95], v[48:51]\n"
    "v_cvt_pk_bf16_f32 v113, v34, v35\n"
    "v_mfma_f32_16x16x32_bf16 v[56:59], a[176:179], v[92:95], v[56:59]\n"
    "ds_read_b128 v[92:95], v1 offset:26624\n"
    "s_waitcnt lgkmcnt(4)\n"
    "v_mfma_f32_16x16x32_bf16 v[44:47], a[16:19], v[96:99], v[44:47]\n"
    "v_cvt_pk_bf16_f32 v114, v36, v37\n"
    "v_mfma_f32_16x16x32_bf16 v[52:55], a[96:99], v[96:99], v[52:55]\n"
    "v_cvt_pk_bf16_f32 v115, v38, v39\n"
    "v_mfma_f32_16x16x32_bf16 v[60:63], a[176:179], v[96:99], v[60:63]\n"
    "ds_read_b128 v[96:99], v1 offset:7168\n"
    "s_waitcnt lgkmcnt(4)\n"
    "v_mfma_f32_16x16x32_bf16 v[40:43], a[20:23], v[80:83], v[40:43]\n"
    "buffer_store_dwordx4 v[112:115], v0, s[40:43], s65 offen offset:2048 sc1\n"
    "v_mfma_f32_16x16x32_bf16 v[48:51], a[100:103], v[80:83], v[48:51]\n"
    "buffer_load_dword v224, v225, s[40:43], s58 offen sc1\n"
    "v_mfma_f32_16x16x32_bf16 v[56:59], a[180:183], v[80:83], v[56:59]\n"
    "ds_read_b128 v[80:83], v1 offset:27648\n"
    "s_waitcnt lgkmcnt(4)\n"
    "v_mfma_f32_16x16x32_bf16 v[44:47], a[20:23], v[84:87], v[44:47]\n"
    "buffer_load_dword v9, v10, s[40:43], s58 offen sc1\n"
    "v_mfma_f32_16x16x32_bf16 v[52:55], a[100:103], v[84:87], v[52:55]\n"
    "v_mfma_f32_16x16x32_bf16 v[60:63], a[180:183], v[84:87], v[60:63]\n"
    "ds_read_b128 v[84:87], v1 offset:8192\n"
    "s_waitcnt lgkmcnt(4)\n"
    "v_mfma_f32_16x16x32_bf16 v[40:43], a[24:27], v[88:91], v[40:43]\n"
    "v_mfma_f32_16x16x32_bf16 v[48:51], a[104:107], v[88:91], v[48:51]\n"
    "v_mfma_f32_16x16x32_bf16 v[56:59], a[184:187], v[88:91], v[56:59]\n"
    "ds_read_b128 v[88:91], v1 offset:28672\n"
    "s_waitcnt lgkmcnt(4)\n"
    "v_mfma_f32_16x16x32_bf16 v[44:47], a[24:27], v[92:95], v[44:47]\n"
    "v_mfma_f32_16x16x32_bf16 v[52:55], a[104:107], v[92:95], v[52:55]\n"
    "v_mfma_f32_16x16x32_bf16 v[60:63], a[184:187], v[92:95], v[60:63]\n"
    "ds_read_b128 v[92:95], v1 offset:9216\n"
    "s_waitcnt lgkmcnt(4)\n"
    "v_mfma_f32_16x16x32_bf16 v[40:43], a[28:31], v[96:99], v[40:43]\n"
    "v_mfma_f32_16x16x32_bf16 v[48:51], a[108:111], v[96:99], v[48:51]\n"
    "v_mfma_f32_16x16x32_bf16 v[56:59], a[188:191], v[96:99], v[56:59]\n"
    "ds_read_b128 v[96:99], v1 offset:29696\n"
    "s_waitcnt lgkmcnt(4)\n"
    "v_mfma_f32_16x16x32_bf16 v[44:47], a[28:31], v[80:83], v[44:47]\n"
    "v_mfma_f32_16x16x32_bf16 v[52:55], a[108:111], v[80:83], v[52:55]\n"
    "v_mfma_f32_16x16x32_bf16 v[60:63], a[188:191], v[80:83], v[60:63]\n"
    "ds_read_b128 v[80:83], v1 offset:10240\n"
    "s_waitcnt lgkmcnt(4)\n"
    "v_mfma_f32_16x16x32_bf16 v[40:43], a[32:35], v[84:87], v[40:43]\n"
    "v_mfma_f32_16x16x32_bf16 v[48:51], a[112:115], v[84:87], v[48:51]\n"
    "v_mfma_f32_16x16x32_bf16 v[56:59], a[192:195], v[84:87], v[56:59]\n"
    "ds_read_b128 v[84:87], v1 offset:30720\n"
    "s_waitcnt lgkmcnt(4)\n"
    "v_mfma_f32_16x16x32_bf16 v[44:47], a[32:35], v[88:91], v[44:47]\n"
    "v_mfma_f32_16x16x32_bf16 v[52:55], a[112:115], v[88:91], v[52:55]\n"
    "v_mfma_f32_16x16x32_bf16 v[60:63], a[192:195], v[88:91], v[60:63]\n"
    "ds_read_b128 v[88:91], v1 offset:11264\n"
    "s_waitcnt lgkmcnt(4)\n"
    "v_mfma_f32_16x16x32_bf16 v[40:43], a[36:39], v[92:95], v[40:43]\n"
    "v_mfma_f32_16x16x32_bf16 v[48:51], a[116:119], v[92:95], v[48:51]\n"
    "v_mfma_f32_16x16x32_bf16 v[56:59], a[196:199], v[92:95], v[56:59]\n"
    "ds_read_b128 v[92:95], v1 offset:31744\n"
    "s_waitcnt lgkmcnt(4)\n"
    "v_mfma_f32_16x16x32_bf16 v[44:47], a[36:39], v[96:99], v[44:47]\n"
    "v_mfma_f32_16x16x32_bf16 v[52:55], a[116:119], v[96:99], v[52:55]\n"
    "v_mfma_f32_16x16x32_bf16 v[60:63], a[196:199], v[96:99], v[60:63]\n"
    "ds_read_b128 v[96:99], v1 offset:12288\n"
    "s_waitcnt lgkmcnt(4)\n"
    "v_mfma_f32_16x16x32_bf16 v[40:43], a[40:43], v[80:83], v[40:43]\n"
    "v_mfma_f32_16x16x32_bf16 v[48:51], a[120:123], v[80:83], v[48:51]\n"
    "v_mfma_f32_16x16x32_bf16 v[56:59], a[200:203], v[80:83], v[56:59]\n"
    "ds_read_b128 v[80:83], v1 offset:32768\n"
    "s_waitcnt lgkmcnt(4)\n"
    "v_mfma_f32_16x16x32_bf16 v[44:47], a[40:43], v[84:87], v[44:47]\n"
    "v_mfma_f32_16x16x32_bf16 v[52:55], a[120:123], v[84:87], v[52:55]\n"
    "v_mfma_f32_16x16x32_bf16 v[60:63], a[200:203], v[84:87], v[60:63]\n"
    "ds_read_b128 v[84:87], v1 offset:13312\n"
    "s_waitcnt lgkmcnt(4)\n"
    "v_mfma_f32_16x16x32_bf16 v[40:43], a[44:47], v[88:91], v[40:43]\n"
    "v_mfma_f32_16x16x32_bf16 v[48:51], a[124:127], v[88:91], v[48:51]\n"
    "v_mfma_f32_16x16x32_bf16 v[56:59], a[204:207], v[88:91], v[56:59]\n"
    "ds_read_b128 v[88:91], v1 offset:33792\n"
    "s_waitcnt lgkmcnt(4)\n"
    "v_mfma_f32_16x16x32_bf16 v[44:47], a[44:47], v[92:95], v[44:47]\n"
    "v_mfma_f32_16x16x32_bf16 v[52:55], a[124:127], v[92:95], v[52:55]\n"
    "v_mfma_f32_16x16x32_bf16 v[60:63], a[204:207], v[92:95], v[60:63]\n"
    "ds_read_b128 v[92:95], v1 offset:14336\n"
    "s_waitcnt lgkmcnt(4)\n"
    "v_mfma_f32_16x16x32_bf16 v[40:43], a[48:51], v[96:99], v[40:43]\n"
    "v_mfma_f32_16x16x32_bf16 v[48:51], a[128:131], v[96:99], v[48:51]\n"
    "v_mfma_f32_16x16x32_bf16 v[56:59], a[208:211], v[96:99], v[56:59]\n"
    "ds_read_b128 v[96:99], v1 offset:34816\n"
    "s_waitcnt lgkmcnt(4)\n"
    "v_mfma_f32_16x16x32_bf16 v[44:47], a[48:51], v[80:83], v[44:47]\n"
    "v_mfma_f32_16x16x32_bf16 v[52:55], a[128:131], v[80:83], v[52:55]\n"
    "v_mfma_f32_16x16x32_bf16 v[60:63], a[208:211], v[80:83], v[60:63]\n"
    "ds_read_b128 v[80:83], v1 offset:15360\n"
    "s_waitcnt lgkmcnt(4)\n"
    "v_mfma_f32_16x16x32_bf16 v[40:43], a[52:55], v[84:87], v[40:43]\n"
    "v_mfma_f32_16x16x32_bf16 v[48:51], a[132:135], v[84:87], v[48:51]\n"
    "v_mfma_f32_16x16x32_bf16 v[56:59], a[212:215], v[84:87], v[56:59]\n"
    "ds_read_b128 v[84:87], v1 offset:35840\n"
    "s_waitcnt lgkmcnt(4)\n"
    "v_mfma_f32_16x16x32_bf16 v[44:47], a[52:55], v[88:91], v[44:47]\n"
    "v_mfma_f32_16x16x32_bf16 v[52:55], a[132:135], v[88:91], v[52:55]\n"
    "v_mfma_f32_16x16x32_bf16 v[60:63], a[212:215], v[88:91], v[60:63]\n"
    "ds_read_b128 v[88:91], v1 offset:16384\n"
    "s_waitcnt lgkmcnt(4)\n"
    "v_mfma_f32_16x16x32_bf16 v[40:43], a[56:59], v[92:95], v[40:43]\n"
    "v_mfma_f32_16x16x32_bf16 v[48:51], a[136:139], v[92:95], v[48:51]\n"
    "v_mfma_f32_16x16x32_bf16 v[56:59], a[216:219], v[92:95], v[56:59]\n"
    "ds_read_b128 v[92:95], v1 offset:36864\n"
    "s_waitcnt lgkmcnt(4)\n"
    "v_mfma_f32_16x16x32_bf16 v[44:47], a[56:59], v[96:99], v[44:47]\n"
    "v_mfma_f32_16x16x32_bf16 v[52:55], a[136:139], v[96:99], v[52:55]\n"
    "v_mfma_f32_16x16x32_bf16 v[60:63], a[216:219], v[96:99], v[60:63]\n"
    "s_waitcnt vmcnt(0)\n"
    "s_barrier\n"
    "buffer_atomic_add v226, v3, s[40:43], s52 offen\n"
    "s_and_b32 s48, s47, 63\n"
    "s_cmp_lt_u32 s48, 62\n"
    "s_cselect_b32 s48, 1, 0\n"
    "s_mov_b32 s60, 0\n"
    "v_cmp_gt_u32 vcc, s48, v224\n"
    "s_nop 4\n"
    "s_cbranch_vccz hpk_xf1_ok_%=\n"
    "hpk_xf1_poll_%=:\n"
    "buffer_load_dword v224, v225, s[40:43], s58 offen sc1\n"
    "s_waitcnt vmcnt(0)\n"
    "v_cmp_gt_u32 vcc, s48, v224\n"
    "s_nop 4\n"
    "s_cbranch_vccz hpk_xf1_ok_%=\n"
    "s_sleep 2\n"
    "s_add_u32 s60, s60, 1\n"
    "s_cmp_gt_u32 s60, 65536\n"
    "s_cbranch_scc1 hp_abort_%=\n"
    "s_and_b32 s59, s60, 63\n"
    "s_cbranch_scc1 hpk_xf1_poll_%=\n"
    "v_mov_b32 v4, 1024\n"
    "buffer_load_dword v4, v4, s[40:43], s58 offen sc1\n"
    "s_waitcnt vmcnt(0)\n"
    "v_readfirstlane_b32 s59, v4\n"
    "s_nop 0\n"
    "s_cmp_lg_u32 s59, 0\n"
    "s_nop 2\n"
    "s_cbranch_scc1 hp_end_%=\n"
    "s_branch hpk_xf1_poll_%=\n"
    "hpk_xf1_ok_%=:\n"
    "v_add_u32 v225, 128, v225\n"
    "ds_read_b128 v[96:99], v1 offset:17408\n"
    "s_waitcnt lgkmcnt(4)\n"
    "v_mfma_f32_16x16x32_bf16 v[40:43], a[60:63], v[80:83], v[40:43]\n"
    "s_add_u32 m0, s61, 0\n"
    "s_add_u32 s51, s50, 0\n"
    "buffer_load_dwordx4 v0, s[40:43], s51 offen offset:0 sc1 lds\n"
    "v_mfma_f32_16x16x32_bf16 v[48:51], a[140:143], v[80:83], v[48:51]\n"
    "v_mfma_f32_16x16x32_bf16 v[56:59], a[220:223], v[80:83], v[56:59]\n"
    "ds_read_b128 v[80:83], v1 offset:37888\n"
    "s_waitcnt lgkmcnt(4)\n"
    "v_mfma_f32_16x16x32_bf16 v[44:47], a[60:63], v[84:87], v[44:47]\n"
    "buffer_load_dwordx4 v0, s[40:43], s51 offen offset:1024 sc1 lds\n"
    "v_mfma_f32_16x16x32_bf16 v[52:55], a[140:143], v[84:87], v[52:55]\n"
    "v_mfma_f32_16x16x32_bf16 v[60:63], a[220:223], v[84:87], v[60:63]\n"
    "ds_read_b128 v[84:87], v1 offset:18432\n"
    "s_waitcnt lgkmcnt(4)\n"
    "v_mfma_f32_16x16x32_bf16 v[40:43], a[64:67], v[88:91], v[40:43]\n"
    "buffer_load_dwordx4 v0, s[40:43], s51 offen offset:2048 sc1 lds\n"
    "v_mfma_f32_16x16x32_bf16 v[48:51], a[144:147], v[88:91], v[48:51]\n"
    "v_mfma_f32_16x16x32_bf16 v[56:59], a[224:227], v[88:91], v[56:59]\n"
    "ds_read_b128 v[88:91], v1 offset:38912\n"
    "s_waitcnt lgkmcnt(4)\n"
    "v_mfma_f32_16x16x32_bf16 v[44:47], a[64:67], v[92:95], v[44:47]\n"
    "buffer_load_dwordx4 v0, s[40:43], s51 offen offset:3072 sc1 lds\n"
    "v_mfma_f32_16x16x32_bf16 v[52:55], a[144:147], v[92:95], v[52:55]\n"
    "v_mfma_f32_16x16x32_bf16 v[60:63], a[224:227], v[92:95], v[60:63]\n"
    "ds_read_b128 v[92:95], v1 offset:19456\n"
    "s_waitcnt lgkmcnt(4)\n"
    "v_mfma_f32_16x16x32_bf16 v[40:43], a[68:71], v[96:99], v[40:43]\n"
    "s_add_u32 m0, s61, 4096\n"
    "s_add_u32 s51, s50, 4096\n"
    "buffer_load_dwordx4 v0, s[40:43], s51 offen offset:0 sc1 lds\n"
    "v_mfma_f32_16x16x32_bf16 v[48:51], a[148:151], v[96:99], v[48:51]\n"
    "v_mfma_f32_16x16x32_bf16 v[56:59], a[228:231], v[96:99], v[56:59]\n"
    "ds_read_b128 v[96:99], v1 offset:39936\n"
    "s_waitcnt lgkmcnt(4)\n"
    "v_mfma_f32_16x16x32_bf16 v[44:47], a[68:71], v[80:83], v[44:47]\n"
    "buffer_load_dwordx4 v0, s[40:43], s51 offen offset:1024 sc1 lds\n"
    "v_mfma_f32_16x16x32_bf16 v[52:55], a[148:151], v[80:83], v[52:55]\n"
    "v_mfma_f32_16x16x32_bf16 v[60:63], a[228:231], v[80:83], v[60:63]\n"
    "s_add_u32 s64, s64, 40960\n"
    "s_cmp_ge_u32 s64, 122880\n"
    "s_cselect_b32 s64, 0, s64\n"
    "v_add_u32 v1, s64, v0\n"
    "ds_read_b128 v[80:83], v1 offset:0\n"
    "s_waitcnt lgkmcnt(4)\n"
    "v_mfma_f32_16x16x32_bf16 v[40:43], a[72:75], v[84:87], v[40:43]\n"
    "buffer_load_dwordx4 v0, s[40:43], s51 offen offset:2048 sc1 lds\n"
    "v_mfma_f32_16x16x32_bf16 v[48:51], a[152:155], v[84:87], v[48:51]\n"
    "v_mfma_f32_16x16x32_bf16 v[56:59], a[232:235], v[84:87], v[56:59]\n"
    "ds_read_b128 v[84:87], v1 offset:20480\n"
    "s_waitcnt lgkmcnt(4)\n"
    "v_mfma_f32_16x16x32_bf16 v[44:47], a[72:75], v[88:91], v[44:47]\n"
    "buffer_load_dwordx4 v0, s[40:43], s51 offen offset:3072 sc1 lds\n"
    "v_mfma_f32_16x16x32_bf16 v[52:55], a[152:155], v[88:91], v[52:55]\n"
    "v_mfma_f32_16x16x32_bf16 v[60:63], a[232:235], v[88:91], v[60:63]\n"
    "ds_read_b128 v[88:91], v1 offset:1024\n"
    "s_waitcnt lgkmcnt(4)\n"
    "v_mfma_f32_16x16x32_bf16 v[40:43], a[76:79], v[92:95], v[40:43]\n"
    "s_add_u32 m0, s61, 8192\n"
    "s_add_u32 s51, s50, 8192\n"
    "buffer_load_dwordx4 v0, s[40:43], s51 offen offset:0 sc1 lds\n"
    "v_mfma_f32_16x16x32_bf16 v[48:51], a[156:159], v[92:95], v[48:51]\n"
    "v_mfma_f32_16x16x32_bf16 v[56:59], a[236:239], v[92:95], v[56:59]\n"
    "ds_read_b128 v[92:95], v1 offset:21504\n"
    "s_waitcnt lgkmcnt(4)\n"
    "v_mfma_f32_16x16x32_bf16 v[44:47], a[76:79], v[96:99], v[44:47]\n"
    "buffer_load_dwordx4 v0, s[40:43], s51 offen offset:1024 sc1 lds\n"
    "s_add_u32 s50, s50, 40960\n"
    "s_add_u32 s61, s61, 40960\n"
    "s_cmp_ge_u32 s61, 122880\n"
    "s_cselect_b32 s51, 122880, 0\n"
    "s_sub_u32 s61, s61, s51\n"
    "v_mfma_f32_16x16x32_bf16 v[52:55], a[156:159], v[96:99], v[52:55]\n"
    "v_mfma_f32_16x16x32_bf16 v[60:63], a[236:239], v[96:99], v[60:63]\n"
    "ds_read_b128 v[96:99], v1 offset:2048\n"
    "s_mov_b32 s65, s63\n"
    "s_add_u32 s59, s47, 0\n"
    "s_and_b32 s59, s59, 63\n"
    "s_cmp_eq_u32 s59, 31\n"
    "s_cselect_b32 s59, -31, 1\n"
    "s_mul_i32 s57, s59, 393216\n"
    "s_add_u32 s63, s63, s57\n"
    "s_mov_b32 s52, s53\n"
    "s_add_u32 s53, s53, s54\n"
    "s_add_u32 s47, s47, 1\n"
    "s_and_b32 s59, s47, 63\n"
    "s_cbranch_scc1 hpk_loop_%=\n"
    "s_waitcnt lgkmcnt(0)\n"
    "v_cvt_pk_bf16_f32 v104, v40, v41\n"
    "v_cvt_pk_bf16_f32 v105, v42, v43\n"
    "v_cvt_pk_bf16_f32 v106, v44, v45\n"
    "v_cvt_pk_bf16_f32 v107, v46, v47\n"
    "buffer_store_dwordx4 v[104:107], v0, s[40:43], s65 offen offset:0 sc1\n"
    "v_cvt_pk_bf16_f32 v108, v48, v49\n"
    "v_cvt_pk_bf16_f32 v109, v50, v51\n"
    "v_cvt_pk_bf16_f32 v110, v52, v53\n"
    "v_cvt_pk_bf16_f32 v111, v54, v55\n"
    "buffer_store_dwordx4 v[108:111], v0, s[40:43], s65 offen offset:1024 sc1\n"
    "v_cvt_pk_bf16_f32 v112, v56, v57\n"
    "v_cvt_pk_bf16_f32 v113, v58, v59\n"
    "v_cvt_pk_bf16_f32 v114, v60, v61\n"
    "v_cvt_pk_bf16_f32 v115, v62, v63\n"
    "buffer_store_dwordx4 v[112:115], v0, s[40:43], s65 offen offset:2048 sc1\n"
    "s_waitcnt vmcnt(0)\n"
    "s_waitcnt lgkmcnt(0)\n"
    "s_barrier\n"
    "buffer_atomic_add v226, v3, s[40:43], s52 offen\n"
    "s_nop 1\n"
    "s_branch hp_ret_%=\n"
    "hp_roleq_%=:\n"
    "s_mul_i32 s62, s45, 10240\n"
    "s_mul_i32 s50, s47, 40960\n"
    "s_add_u32 s50, s50, s62\n"
    "s_add_u32 s50, s50, 2523136\n"
    "s_mov_b32 s61, s62\n"
    "s_lshl_b32 s48, s47, 7\n"
    "s_add_u32 s48, s48, 43520\n"
    "v_mov_b32 v225, s48\n"
    "s_mov_b32 s48, 1\n"
    "v_mov_b32 v224, 0\n"
    "s_mov_b32 s60, 0\n"
    "v_cmp_gt_u32 vcc, s48, v224\n"
    "s_nop 4\n"
    "s_cbranch_vccz hpq_xfp0_ok_%=\n"
    "hpq_xfp0_poll_%=:\n"
    "buffer_load_dword v224, v225, s[40:43], s58 offen sc1\n"
    "s_waitcnt vmcnt(0)\n"
    "v_cmp_gt_u32 vcc, s48, v224\n"
    "s_nop 4\n"
    "s_cbranch_vccz hpq_xfp0_ok_%=\n"
    "s_sleep 2\n"
    "s_add_u32 s60, s60, 1\n"
    "s_cmp_gt_u32 s60, 65536\n"
    "s_cbranch_scc1 hp_abort_%=\n"
    "s_and_b32 s59, s60, 63\n"
    "s_cbranch_scc1 hpq_xfp0_poll_%=\n"
    "v_mov_b32 v4, 1024\n"
    "buffer_load_dword v4, v4, s[40:43], s58 offen sc1\n"
    "s_waitcnt vmcnt(0)\n"
    "v_readfirstlane_b32 s59, v4\n"
    "s_nop 0\n"
    "s_cmp_lg_u32 s59, 0\n"
    "s_nop 2\n"
    "s_cbranch_scc1 hp_end_%=\n"
    "s_branch hpq_xfp0_poll_%=\n"
    "hpq_xfp0_ok_%=:\n"
    "v_add_u32 v225, 128, v225\n"
    "s_add_u32 m0, s61, 0\n"
    "s_add_u32 s51, s50, 0\n"
    "buffer_load_dwordx4 v0, s[40:43], s51 offen offset:0 sc1 lds\n"
    "buffer_load_dwordx4 v0, s[40:43], s51 offen offset:1024 sc1 lds\n"
    "buffer_load_dwordx4 v0, s[40:43], s51 offen offset:2048 sc1 lds\n"
    "buffer_load_dwordx4 v0, s[40:43], s51 offen offset:3072 sc1 lds\n"
    "s_add_u32 m0, s61, 4096\n"
    "s_add_u32 s51, s50, 4096\n"
    "buffer_load_dwordx4 v0, s[40:43], s51 offen offset:0 sc1 lds\n"
    "buffer_load_dwordx4 v0, s[40:43], s51 offen offset:1024 sc1 lds\n"
    "buffer_load_dwordx4 v0, s[40:43], s51 offen offset:2048 sc1 lds\n"
    "buffer_load_dwordx4 v0, s[40:43], s51 offen offset:3072 sc1 lds\n"
    "s_add_u32 m0, s61, 8192\n"
    "s_add_u32 s51, s50, 8192\n"
    "buffer_load_dwordx4 v0, s[40:43], s51 offen offset:0 sc1 lds\n"
    "buffer_load_dwordx4 v0, s[40:43], s51 offen offset:1024 sc1 lds\n"
    "s_add_u32 s50, s50, 40960\n"
    "s_add_u32 s61, s61, 40960\n"
    "s_cmp_ge_u32 s61, 122880\n"
    "s_cselect_b32 s51, 122880, 0\n"
    "s_sub_u32 s61, s61, s51\n"
    "v_mov_b32 v224, 0\n"
    "s_mov_b32 s60, 0\n"
    "v_cmp_gt_u32 vcc, s48, v224\n"
    "s_nop 4\n"
    "s_cbranch_vccz hpq_xfp1_ok_%=\n"
    "hpq_xfp1_poll_%=:\n"
    "buffer_load_dword v224, v225, s[40:43], s58 offen sc1\n"
    "s_waitcnt vmcnt(0)\n"
    "v_cmp_gt_u32 vcc, s48, v224\n"
    "s_nop 4\n"
    "s_cbranch_vccz hpq_xfp1_ok_%=\n"
    "s_sleep 2\n"
    "s_add_u32 s60, s60, 1\n"
    "s_cmp_gt_u32 s60, 65536\n"
    "s_cbranch_scc1 hp_abort_%=\n"
    "s_and_b32 s59, s60, 63\n"
    "s_cbranch_scc1 hpq_xfp1_poll_%=\n"
    "v_mov_b32 v4, 1024\n"
    "buffer_load_dword v4, v4, s[40:43], s58 offen sc1\n"
    "s_waitcnt vmcnt(0)\n"
    "v_readfirstlane_b32 s59, v4\n"
    "s_nop 0\n"
    "s_cmp_lg_u32 s59, 0\n"
    "s_nop 2\n"
    "s_cbranch_scc1 hp_end_%=\n"
    "s_branch hpq_xfp1_poll_%=\n"
    "hpq_xfp1_ok_%=:\n"
    "v_add_u32 v225, 128, v225\n"
    "s_add_u32 m0, s61, 0\n"
    "s_add_u32 s51, s50, 0\n"
    "buffer_load_dwordx4 v0, s[40:43], s51 offen offset:0 sc1 lds\n"
    "buffer_load_dwordx4 v0, s[40:43], s51 offen offset:1024 sc1 lds\n"
    "buffer_load_dwordx4 v0, s[40:43], s51 offen offset:2048 sc1 lds\n"
    "buffer_load_dwordx4 v0, s[40:43], s51 offen offset:3072 sc1 lds\n"
    "s_add_u32 m0, s61, 4096\n"
    "s_add_u32 s51, s50, 4096\n"
    "buffer_load_dwordx4 v0, s[40:43], s51 offen offset:0 sc1 lds\n"
    "buffer_load_dwordx4 v0, s[40:43], s51 offen offset:1024 sc1 lds\n"
    "buffer_load_dwordx4 v0, s[40:43], s51 offen offset:2048 sc1 lds\n"
    "buffer_load_dwordx4 v0, s[40:43], s51 offen offset:3072 sc1 lds\n"
    "s_add_u32 m0, s61, 8192\n"
    "s_add_u32 s51, s50, 8192\n"
    "buffer_load_dwordx4 v0, s[40:43], s51 offen offset:0 sc1 lds\n"
    "buffer_load_dwordx4 v0, s[40:43], s51 offen offset:1024 sc1 lds\n"
    "s_add_u32 s50, s50, 40960\n"
    "s_add_u32 s61, s61, 40960\n"
    "s_cmp_ge_u32 s61, 122880\n"
    "s_cselect_b32 s51, 122880, 0\n"
    "s_sub_u32 s61, s61, s51\n"
    "v_mov_b32 v224, 0\n"
    "v_mov_b32 v226, 1\n"
    "v_mov_b32 v1, v0\n"
    "s_mov_b32 s64, 0\n"
    "s_lshl_b32 s48, s44, 7\n"
    "s_lshr_b32 s49, s44, 2\n"
    "s_lshl_b32 s49, s49, 7\n"
    "v_mov_b32 v3, s49\n"
    "v_cmp_eq_u32 vcc, 0, v15\n"
    "v_mov_b32 v4, 2147483632\n"
    "s_nop 0\n"
    "v_cndmask_b32 v3, v4, v3, vcc\n"
    "s_mov_b32 s52, 0x40000000\n"
    "v_lshrrev_b32 v13, 4, v15\n"
    "v_and_b32 v14, 15, v15\n"
    "s_lshl_b32 s49, s45, 11\n"
    "s_add_u32 s49, s49, 122880\n"
    "v_add_u32 v6, s49, v0\n"
    "s_lshr_b32 s49, s45, 1\n"
    "s_lshl_b32 s49, s49, 11\n"
    "s_and_b32 s53, s45, 1\n"
    "s_lshl_b32 s53, s53, 3\n"
    "s_add_u32 s49, s49, s53\n"
    "s_add_u32 s49, s49, 131072\n"
    "v_add_u32 v7, s49, v0\n"
    "s_lshl_b32 s49, s45, 10\n"
    "s_add_u32 s49, s49, 122880\n"
    "v_add_u32 v8, s49, v0\n"
    "v_mov_b32 v9, 0\n"
    "s_add_u32 s49, s48, 6144\n"
    "v_mov_b32 v10, s49\n"
    "v_and_b32 v11, 7, v15\n"
    "v_lshlrev_b32 v11, 4, v11\n"
    "v_lshl_add_u32 v11, v13, 7, v11\n"
    "s_bfe_u32 s49, s44, 0x20001\n"
    "s_lshl_b32 s49, s49, 8\n"
    "v_add_u32 v12, s49, v0\n"
    "v_cmp_gt_u32 vcc, 16, v15\n"
    "v_mov_b32 v4, 2147483632\n"
    "s_nop 0\n"
    "v_cndmask_b32 v12, v4, v12, vcc\n"
    "s_lshl_b32 s49, s45, 4\n"
    "v_add_u32 v14, s49, v14\n"
    "v_lshlrev_b32 v13, 2, v13\n"
    "v_sub_u32 v14, v14, v13\n"
    "v_mov_b32 v4, 0xff800000\n"
    "v_cmp_gt_i32 vcc, 0, v14\n"
    "s_nop 1\n"
    "v_cndmask_b32 v212, 0, v4, vcc\n"
    "v_cmp_gt_i32 vcc, 1, v14\n"
    "s_nop 1\n"
    "v_cndmask_b32 v213, 0, v4, vcc\n"
    "v_cmp_gt_i32 vcc, 2, v14\n"
    "s_nop 1\n"
    "v_cndmask_b32 v214, 0, v4, vcc\n"
    "v_cmp_gt_i32 vcc, 3, v14\n"
    "s_nop 1\n"
    "v_cndmask_b32 v215, 0, v4, vcc\n"
    "v_cmp_gt_i32 vcc, 16, v14\n"
    "s_nop 1\n"
    "v_cndmask_b32 v216, 0, v4, vcc\n"
    "v_cmp_gt_i32 vcc, 17, v14\n"
    "s_nop 1\n"
    "v_cndmask_b32 v217, 0, v4, vcc\n"
    "v_cmp_gt_i32 vcc, 18, v14\n"
    "s_nop 1\n"
    "v_cndmask_b32 v218, 0, v4, vcc\n"
    "v_cmp_gt_i32 vcc, 19, v14\n"
    "s_nop 1\n"
    "v_cndmask_b32 v219, 0, v4, vcc\n"
    "v_cmp_lt_u32 vcc, 31, v15\n"
    "s_nop 1\n"
    "v_cndmask_b32 v220, 0, v4, vcc\n"
    "v_cndmask_b32 v221, 0, v4, vcc\n"
    "v_cndmask_b32 v222, 0, v4, vcc\n"
    "v_cndmask_b32 v223, 0, v4, vcc\n"
    "s_waitcnt vmcnt(0)\n"
    "s_waitcnt lgkmcnt(0)\n"
    "s_barrier\n"
    "s_mov_b32 s65, s47\n"
    "s_sub_u32 s63, s47, 2\n"
    "s_cmp_gt_u32 s45, 1\n"
    "s_cselect_b32 s30, 0x40000000, 0\n"
    "s_mul_i32 s34, s44, 3072\n"
    "s_add_u32 s34, s34, 127139840\n"
    "s_mul_i32 s35, s63, 1536\n"
    "s_add_u32 s35, s35, 2157568\n"
    "s_lshl1_add_u32 s36, s44, s45\n"
    "s_lshl_b32 s36, s36, 10\n"
    "s_add_u32 s36, s36, 130351104\n"
    "s_lshr_b32 s37, s44, 3\n"
    "s_lshl_b32 s37, s37, 1\n"
    "s_and_b32 s48, s44, 1\n"
    "s_add_u32 s37, s37, s48\n"
    "s_lshl3_add_u32 s37, s45, s37\n"
    "s_lshl_b32 s37, s37, 10\n"
    "s_add_u32 s37, s37, 132546560\n"
    "s_mul_i32 s54, s44, 12288\n"
    "s_add_u32 s54, s54, 7372800\n"
    "s_or_b32 s54, s54, s30\n"
    "s_add_u32 s55, s54, 4096\n"
    "s_add_u32 s56, s54, 8192\n"
    "s_lshl_b32 s48, s47, 7\n"
    "s_add_u32 s48, s48, 10240\n"
    "v_mov_b32 v227, s48\n"
    "v_mov_b32 v236, 0\n"
    "s_mov_b32 s48, 32\n"
    "v_add_u32 v227, -128, v227\n"
    "v_mov_b32 v236, 32\n"
    "v_add_u32 v2, 8, v6\n"
    "ds_read_b128 v[80:83], v1 offset:0\n"
    "ds_read_b128 v[84:87], v1 offset:20480\n"
    "ds_read_b128 v[88:91], v1 offset:1024\n"
    "ds_read_b128 v[92:95], v1 offset:21504\n"
    "ds_read_b128 v[96:99], v1 offset:2048\n"
    "hpq_loop_%=:\n"
    "s_mov_b32 s29, s52\n"
    "s_waitcnt vmcnt(25)\n"
    "s_add_u32 s48, s63, 1\n"
    "s_cmp_ge_i32 s63, s65\n"
    "s_cselect_b32 s48, s48, 0\n"
    "s_cselect_b32 s49, s30, 0x40000000\n"
    "v_cmp_gt_u32 vcc, s48, v9\n"
    "s_cbranch_vccz hpq_kma0_ok_%=\n"
    "s_waitcnt vmcnt(0)\n"
    "buffer_atomic_add v226, v3, s[40:43], s29 offen\n"
    "s_mov_b32 s29, 0x40000000\n"
    "s_mov_b32 s60, 0\n"
    "v_cmp_gt_u32 vcc, s48, v9\n"
    "s_nop 4\n"
    "s_cbranch_vccz hpq_kma0_ok_%=\n"
    "hpq_kma0_poll_%=:\n"
    "buffer_load_dword v9, v10, s[40:43], s58 offen sc1\n"
    "s_waitcnt vmcnt(0)\n"
    "v_cmp_gt_u32 vcc, s48, v9\n"
    "s_nop 4\n"
    "s_cbranch_vccz hpq_kma0_ok_%=\n"
    "s_sleep 2\n"
    "s_add_u32 s60, s60, 1\n"
    "s_cmp_gt_u32 s60, 65536\n"
    "s_cbranch_scc1 hp_abort_%=\n"
    "s_and_b32 s59, s60, 63\n"
    "s_cbranch_scc1 hpq_kma0_poll_%=\n"
    "v_mov_b32 v4, 1024\n"
    "buffer_load_dword v4, v4, s[40:43], s58 offen sc1\n"
    "s_waitcnt vmcnt(0)\n"
    "v_readfirstlane_b32 s59, v4\n"
    "s_nop 0\n"
    "s_cmp_lg_u32 s59, 0\n"
    "s_nop 2\n"
    "s_cbranch_scc1 hp_end_%=\n"
    "s_branch hpq_kma0_poll_%=\n"
    "hpq_kma0_ok_%=:\n"
    "s_movk_i32 s48, 32\n"
    "s_mov_b32 s60, 0\n"
    "v_cmp_gt_u32 vcc, s48, v236\n"
    "s_nop 4\n"
    "s_cbranch_vccz hpq_kxa0_ok_%=\n"
    "hpq_kxa0_poll_%=:\n"
    "buffer_load_dword v236, v227, s[40:43], s58 offen sc1\n"
    "s_waitcnt vmcnt(0)\n"
    "v_cmp_gt_u32 vcc, s48, v236\n"
    "s_nop 4\n"
    "s_cbranch_vccz hpq_kxa0_ok_%=\n"
    "s_sleep 2\n"
    "s_add_u32 s60, s60, 1\n"
    "s_cmp_gt_u32 s60, 65536\n"
    "s_cbranch_scc1 hp_abort_%=\n"
    "s_and_b32 s59, s60, 63\n"
    "s_cbranch_scc1 hpq_kxa0_poll_%=\n"
    "v_mov_b32 v4, 1024\n"
    "buffer_load_dword v4, v4, s[40:43], s58 offen sc1\n"
    "s_waitcnt vmcnt(0)\n"
    "v_readfirstlane_b32 s59, v4\n"
    "s_nop 0\n"
    "s_cmp_lg_u32 s59, 0\n"
    "s_nop 2\n"
    "s_cbranch_scc1 hp_end_%=\n"
    "s_branch hpq_kxa0_poll_%=\n"
    "hpq_kxa0_ok_%=:\n"
    "v_add_u32 v227, 128, v227\n"
    "s_or_b32 s57, s34, s49\n"
    "s_or_b32 s52, s35, s49\n"
    "s_or_b32 s26, s36, s49\n"
    "s_or_b32 s27, s37, s49\n"
    "s_add_u32 s59, s63, 0\n"
    "s_and_b32 s59, s59, 63\n"
    "s_cmp_eq_u32 s59, 31\n"
    "s_cselect_b32 s59, -31, 1\n"
    "s_mul_i32 s48, s59, 98304\n"
    "s_add_u32 s34, s34, s48\n"
    "s_mul_i32 s48, s59, 65536\n"
    "s_add_u32 s36, s36, s48\n"
    "s_mul_i32 s48, s59, 16384\n"
    "s_add_u32 s37, s37, s48\n"
    "s_add_u32 s35, s35, 1536\n"
    "s_add_u32 s63, s63, 1\n"
    "s_waitcnt lgkmcnt(4)\n"
    "v_mfma_f32_16x16x32_bf16 v[16:19], a[0:3], v[80:83], 0\n"
    "buffer_load_dwordx4 v[164:167], v11, s[40:43], s57 offen sc1\n"
    "v_mfma_f32_16x16x32_bf16 v[24:27], a[80:83], v[80:83], 0\n"
    "buffer_load_dwordx4 v[168:171], v11, s[40:43], s57 offen offset:512 sc1\n"
    "v_mfma_f32_16x16x32_bf16 v[32:35], a[160:163], v[80:83], 0\n"
    "buffer_load_dwordx4 v[172:175], v11, s[40:43], s57 offen offset:1024 sc1\n"
    "ds_read_b128 v[80:83], v1 offset:22528\n"
    "s_waitcnt lgkmcnt(4)\n"
    "v_mfma_f32_16x16x32_bf16 v[20:23], a[0:3], v[84:87], 0\n"
    "buffer_load_dwordx4 v[176:179], v11, s[40:43], s57 offen offset:1536 sc1\n"
    "v_mfma_f32_16x16x32_bf16 v[28:31], a[80:83], v[84:87], 0\n"
    "buffer_load_dwordx4 v[180:183], v11, s[40:43], s57 offen offset:2048 sc1\n"
    "v_mfma_f32_16x16x32_bf16 v[36:39], a[160:163], v[84:87], 0\n"
    "buffer_load_dwordx4 v[184:187], v11, s[40:43], s57 offen offset:2560 sc1\n"
    "ds_read_b128 v[84:87], v1 offset:3072\n"
    "s_waitcnt lgkmcnt(4)\n"
    "v_mfma_f32_16x16x32_bf16 v[16:19], a[4:7], v[88:91], v[16:19]\n"
    "buffer_load_dword v236, v227, s[40:43], s58 offen sc1\n"
    "v_mfma_f32_16x16x32_bf16 v[24:27], a[84:87], v[88:91], v[24:27]\n"
    "v_mfma_f32_16x16x32_bf16 v[32:35], a[164:167], v[88:91], v[32:35]\n"
    "ds_read_b128 v[88:91], v1 offset:23552\n"
    "s_waitcnt lgkmcnt(4)\n"
    "v_mfma_f32_16x16x32_bf16 v[20:23], a[4:7], v[92:95], v[20:23]\n"
    "v_cvt_pk_bf16_f32 v104, v40, v41\n"
    "v_mfma_f32_16x16x32_bf16 v[28:31], a[84:87], v[92:95], v[28:31]\n"
    "v_cvt_pk_bf16_f32 v105, v42, v43\n"
    "v_mfma_f32_16x16x32_bf16 v[36:39], a[164:167], v[92:95], v[36:39]\n"
    "ds_read_b128 v[92:95], v1 offset:4096\n"
    "s_waitcnt lgkmcnt(4)\n"
    "v_mfma_f32_16x16x32_bf16 v[16:19], a[8:11], v[96:99], v[16:19]\n"
    "v_cvt_pk_bf16_f32 v106, v44, v45\n"
    "v_mfma_f32_16x16x32_bf16 v[24:27], a[88:91], v[96:99], v[24:27]\n"
    "v_cvt_pk_bf16_f32 v107, v46, v47\n"
    "v_mfma_f32_16x16x32_bf16 v[32:35], a[168:171], v[96:99], v[32:35]\n"
    "ds_read_b128 v[96:99], v1 offset:24576\n"
    "s_waitcnt lgkmcnt(4)\n"
    "v_mfma_f32_16x16x32_bf16 v[20:23], a[8:11], v[80:83], v[20:23]\n"
    "ds_write2st64_b64 v6, v[104:105], v[106:107] offset0:24 offset1:26\n"
    "v_mfma_f32_16x16x32_bf16 v[28:31], a[88:91], v[80:83], v[28:31]\n"
    "v_cvt_pk_bf16_f32 v108, v48, v49\n"
    "v_mfma_f32_16x16x32_bf16 v[36:39], a[168:171], v[80:83], v[36:39]\n"
    "ds_read_b128 v[80:83], v1 offset:5120\n"
    "s_waitcnt lgkmcnt(5)\n"
    "v_mfma_f32_16x16x32_bf16 v[16:19], a[12:15], v[84:87], v[16:19]\n"
    "v_cvt_pk_bf16_f32 v109, v50, v51\n"
    "v_mfma_f32_16x16x32_bf16 v[24:27], a[92:95], v[84:87], v[24:27]\n"
    "v_cvt_pk_bf16_f32 v110, v52, v53\n"
    "v_mfma_f32_16x16x32_bf16 v[32:35], a[172:175], v[84:87], v[32:35]\n"
    "ds_read_b128 v[84:87], v1 offset:25600\n"
    "s_waitcnt lgkmcnt(5)\n"
    "v_mfma_f32_16x16x32_bf16 v[20:23], a[12:15], v[88:91], v[20:23]\n"
    "v_cvt_pk_bf16_f32 v111, v54, v55\n"
    "v_mfma_f32_16x16x32_bf16 v[28:31], a[92:95], v[88:91], v[28:31]\n"
    "ds_write2st64_b64 v2, v[108:109], v[110:111] offset0:24 offset1:26\n"
    "v_mfma_f32_16x16x32_bf16 v[36:39], a[172:175], v[88:91], v[36:39]\n"
    "ds_read_b128 v[88:91], v1 offset:6144\n"
    "s_waitcnt lgkmcnt(6)\n"
    "v_mfma_f32_16x16x32_bf16 v[16:19], a[16:19], v[92:95], v[16:19]\n"
    "v_cvt_pk_bf16_f32 v112, v56, v57\n"
    "v_mfma_f32_16x16x32_bf16 v[24:27], a[96:99], v[92:95], v[24:27]\n"
    "v_cvt_pk_bf16_f32 v113, v58, v59\n"
    "v_mfma_f32_16x16x32_bf16 v[32:35], a[176:179], v[92:95], v[32:35]\n"
    "s_waitcnt vmcnt(20)\n"
    "ds_read_b128 v[92:95], v1 offset:26624\n"
    "s_waitcnt lgkmcnt(6)\n"
    "v_mfma_f32_16x16x32_bf16 v[20:23], a[16:19], v[96:99], v[20:23]\n"
    "v_cvt_pk_bf16_f32 v114, v60, v61\n"
    "v_mfma_f32_16x16x32_bf16 v[28:31], a[96:99], v[96:99], v[28:31]\n"
    "v_cvt_pk_bf16_f32 v115, v62, v63\n"
    "v_mfma_f32_16x16x32_bf16 v[36:39], a[176:179], v[96:99], v[36:39]\n"
    "v_swap_b32 v118, v120\n"
    "ds_read_b128 v[96:99], v1 offset:7168\n"
    "s_waitcnt lgkmcnt(5)\n"
    "v_mfma_f32_16x16x32_bf16 v[16:19], a[20:23], v[80:83], v[16:19]\n"
    "ds_write2st64_b64 v7, v[112:113], v[114:115] offset0:24 offset1:26\n"
    "v_mfma_f32_16x16x32_bf16 v[24:27], a[100:103], v[80:83], v[24:27]\n"
    "buffer_load_dword v224, v225, s[40:43], s58 offen sc1\n"
    "v_mfma_f32_16x16x32_bf16 v[32:35], a[180:183], v[80:83], v[32:35]\n"
    "v_swap_b32 v119, v121\n"
    "ds_read_b128 v[80:83], v1 offset:27648\n"
    "s_waitcnt lgkmcnt(6)\n"
    "v_mfma_f32_16x16x32_bf16 v[20:23], a[20:23], v[84:87], v[20:23]\n"
    "v_swap_b32 v126, v128\n"
    "v_mfma_f32_16x16x32_bf16 v[28:31], a[100:103], v[84:87], v[28:31]\n"
    "v_swap_b32 v127, v129\n"
    "v_mfma_f32_16x16x32_bf16 v[36:39], a[180:183], v[84:87], v[36:39]\n"
    "v_swap_b32 v134, v136\n"
    "ds_read_b128 v[84:87], v1 offset:8192\n"
    "s_waitcnt lgkmcnt(5)\n"
    "v_mfma_f32_16x16x32_bf16 v[16:19], a[24:27], v[88:91], v[16:19]\n"
    "v_swap_b32 v135, v137\n"
    "v_mfma_f32_16x16x32_bf16 v[24:27], a[104:107], v[88:91], v[24:27]\n"
    "v_swap_b32 v142, v144\n"
    "v_mfma_f32_16x16x32_bf16 v[32:35], a[184:187], v[88:91], v[32:35]\n"
    "v_swap_b32 v143, v145\n"
    "ds_read_b128 v[88:91], v1 offset:28672\n"
    "s_waitcnt lgkmcnt(5)\n"
    "v_mfma_f32_16x16x32_bf16 v[20:23], a[24:27], v[92:95], v[20:23]\n"
    "v_swap_b32 v150, v152\n"
    "v_mfma_f32_16x16x32_bf16 v[28:31], a[104:107], v[92:95], v[28:31]\n"
    "v_swap_b32 v151, v153\n"
    "v_mfma_f32_16x16x32_bf16 v[36:39], a[184:187], v[92:95], v[36:39]\n"
    "v_swap_b32 v158, v160\n"
    "ds_read_b128 v[92:95], v1 offset:9216\n"
    "s_waitcnt lgkmcnt(5)\n"
    "v_mfma_f32_16x16x32_bf16 v[16:19], a[28:31], v[96:99], v[16:19]\n"
    "v_swap_b32 v159, v161\n"
    "v_mfma_f32_16x16x32_bf16 v[24:27], a[108:111], v[96:99], v[24:27]\n"
    "v_mfma_f32_16x16x32_bf16 v[32:35], a[188:191], v[96:99], v[32:35]\n"
    "ds_read_b128 v[96:99], v1 offset:29696\n"
    "s_waitcnt lgkmcnt(4)\n"
    "v_mfma_f32_16x16x32_bf16 v[20:23], a[28:31], v[80:83], v[20:23]\n"
    "v_mfma_f32_16x16x32_bf16 v[64:67], v[116:119], v[188:191], v[212:215]\n"
    "v_mfma_f32_16x16x32_bf16 v[68:71], v[120:123], v[188:191], v[216:219]\n"
    "s_waitcnt vmcnt(7)\n"
    "v_mfma_f32_16x16x32_bf16 v[72:75], v[164:167], v[188:191], v[220:223]\n"
    "v_mfma_f32_16x16x32_bf16 v[64:67], v[124:127], v[192:195], v[64:67]\n"
    "v_mfma_f32_16x16x32_bf16 v[68:71], v[128:131], v[192:195], v[68:71]\n"
    "s_waitcnt vmcnt(6)\n"
    "v_mfma_f32_16x16x32_bf16 v[72:75], v[168:171], v[192:195], v[72:75]\n"
    "v_mfma_f32_16x16x32_bf16 v[64:67], v[132:135], v[196:199], v[64:67]\n"
    "v_mfma_f32_16x16x32_bf16 v[68:71], v[136:139], v[196:199], v[68:71]\n"
    "s_waitcnt vmcnt(5)\n"
    "v_mfma_f32_16x16x32_bf16 v[72:75], v[172:175], v[196:199], v[72:75]\n"
    "v_mfma_f32_16x16x32_bf16 v[64:67], v[140:143], v[200:203], v[64:67]\n"
    "v_mfma_f32_16x16x32_bf16 v[68:71], v[144:147], v[200:203], v[68:71]\n"
    "s_waitcnt vmcnt(4)\n"
    "v_mfma_f32_16x16x32_bf16 v[72:75], v[176:179], v[200:203], v[72:75]\n"
    "v_mfma_f32_16x16x32_bf16 v[64:67], v[148:151], v[204:207], v[64:67]\n"
    "v_mfma_f32_16x16x32_bf16 v[68:71], v[152:155], v[204:207], v[68:71]\n"
    "s_waitcnt vmcnt(3)\n"
    "v_mfma_f32_16x16x32_bf16 v[72:75], v[180:183], v[204:207], v[72:75]\n"
    "v_mfma_f32_16x16x32_bf16 v[64:67], v[156:159], v[208:211], v[64:67]\n"
    "v_mfma_f32_16x16x32_bf16 v[68:71], v[160:163], v[208:211], v[68:71]\n"
    "s_waitcnt vmcnt(2)\n"
    "v_mfma_f32_16x16x32_bf16 v[72:75], v[184:187], v[208:211], v[72:75]\n"
    "v_mfma_f32_16x16x32_bf16 v[28:31], a[108:111], v[80:83], v[28:31]\n"
    "v_mfma_f32_16x16x32_bf16 v[36:39], a[188:191], v[80:83], v[36:39]\n"
    "ds_read_b128 v[80:83], v1 offset:10240\n"
    "s_waitcnt lgkmcnt(4)\n"
    "v_mfma_f32_16x16x32_bf16 v[16:19], a[32:35], v[84:87], v[16:19]\n"
    "v_mfma_f32_16x16x32_bf16 v[24:27], a[112:115], v[84:87], v[24:27]\n"
    "v_max3_f32 v76, v64, v65, v66\n"
    "v_mfma_f32_16x16x32_bf16 v[32:35], a[192:195], v[84:87], v[32:35]\n"
    "buffer_load_dword v9, v10, s[40:43], s58 offen sc1\n"
    "ds_read_b128 v[84:87], v1 offset:30720\n"
    "s_waitcnt lgkmcnt(4)\n"
    "v_mfma_f32_16x16x32_bf16 v[20:23], a[32:35], v[88:91], v[20:23]\n"
    "v_max3_f32 v76, v76, v67, v68\n"
    "v_max3_f32 v76, v76, v69, v70\n"
    "v_mfma_f32_16x16x32_bf16 v[28:31], a[112:115], v[88:91], v[28:31]\n"
    "v_max3_f32 v76, v76, v71, v72\n"
    "v_max3_f32 v76, v76, v73, v74\n"
    "v_mfma_f32_16x16x32_bf16 v[36:39], a[192:195], v[88:91], v[36:39]\n"
    "buffer_load_dwordx4 v[116:119], v0, s[40:43], s54 offen sc1\n"
    "ds_read_b128 v[88:91], v1 offset:11264\n"
    "s_waitcnt lgkmcnt(4)\n"
    "v_mfma_f32_16x16x32_bf16 v[16:19], a[36:39], v[92:95], v[16:19]\n"
    "v_max_f32 v76, v76, v75\n"
    "v_mov_b32 v77, v76\n"
    "v_mfma_f32_16x16x32_bf16 v[24:27], a[116:119], v[92:95], v[24:27]\n"
    "s_nop 0\n"
    "v_permlane16_swap_b32 v76, v77\n"
    "v_mfma_f32_16x16x32_bf16 v[32:35], a[196:199], v[92:95], v[32:35]\n"
    "buffer_load_dwordx4 v[120:123], v0, s[40:43], s54 offen offset:1024 sc1\n"
    "ds_read_b128 v[92:95], v1 offset:31744\n"
    "s_waitcnt lgkmcnt(4)\n"
    "v_mfma_f32_16x16x32_bf16 v[20:23], a[36:39], v[96:99], v[20:23]\n"
    "v_max_f32 v76, v76, v77\n"
    "v_mov_b32 v77, v76\n"
    "v_mfma_f32_16x16x32_bf16 v[28:31], a[116:119], v[96:99], v[28:31]\n"
    "s_nop 0\n"
    "v_permlane32_swap_b32 v76, v77\n"
    "v_mfma_f32_16x16x32_bf16 v[36:39], a[196:199], v[96:99], v[36:39]\n"
    "buffer_load_dwordx4 v[124:127], v0, s[40:43], s54 offen offset:3072 sc1\n"
    "ds_read_b128 v[96:99], v1 offset:12288\n"
    "s_waitcnt lgkmcnt(4)\n"
    "v_mfma_f32_16x16x32_bf16 v[16:19], a[40:43], v[80:83], v[16:19]\n"
    "v_max_f32 v76, v76, v77\n"
    "v_mul_f32 v76, 0xbdd53b95, v76\n"
    "v_mfma_f32_16x16x32_bf16 v[24:27], a[120:123], v[80:83], v[24:27]\n"
    "v_fmamk_f32 v64, v64, 0x3dd53b95, v76\n"
    "v_fmamk_f32 v65, v65, 0x3dd53b95, v76\n"
    "v_mfma_f32_16x16x32_bf16 v[32:35], a[200:203], v[80:83], v[32:35]\n"
    "buffer_load_dwordx4 v[128:131], v0, s[40:43], s55 offen sc1\n"
    "ds_read_b128 v[80:83], v1 offset:32768\n"
    "s_waitcnt lgkmcnt(4)\n"
    "v_mfma_f32_16x16x32_bf16 v[20:23], a[40:43], v[84:87], v[20:23]\n"
    "v_fmamk_f32 v66, v66, 0x3dd53b95, v76\n"
    "v_fmamk_f32 v67, v67, 0x3dd53b95, v76\n"
    "v_mfma_f32_16x16x32_bf16 v[28:31], a[120:123], v[84:87], v[28:31]\n"
    "v_fmamk_f32 v68, v68, 0x3dd53b95, v76\n"
    "v_fmamk_f32 v69, v69, 0x3dd53b95, v76\n"
    "v_mfma_f32_16x16x32_bf16 v[36:39], a[200:203], v[84:87], v[36:39]\n"
    "buffer_load_dwordx4 v[132:135], v0, s[40:43], s55 offen offset:2048 sc1\n"
    "ds_read_b128 v[84:87], v1 offset:13312\n"
    "s_waitcnt lgkmcnt(4)\n"
    "v_mfma_f32_16x16x32_bf16 v[16:19], a[44:47], v[88:91], v[16:19]\n"
    "v_fmamk_f32 v70, v70, 0x3dd53b95, v76\n"
    "v_fmamk_f32 v71, v71, 0x3dd53b95, v76\n"
    "v_mfma_f32_16x16x32_bf16 v[24:27], a[124:127], v[88:91], v[24:27]\n"
    "v_fmamk_f32 v72, v72, 0x3dd53b95, v76\n"
    "v_fmamk_f32 v73, v73, 0x3dd53b95, v76\n"
    "v_mfma_f32_16x16x32_bf16 v[32:35], a[204:207], v[88:91], v[32:35]\n"
    "buffer_load_dwordx4 v[136:139], v0, s[40:43], s55 offen offset:3072 sc1\n"
    "ds_read_b128 v[88:91], v1 offset:33792\n"
    "s_waitcnt lgkmcnt(4)\n"
    "v_mfma_f32_16x16x32_bf16 v[20:23], a[44:47], v[92:95], v[20:23]\n"
    "v_fmamk_f32 v74, v74, 0x3dd53b95, v76\n"
    "v_fmamk_f32 v75, v75, 0x3dd53b95, v76\n"
    "v_mfma_f32_16x16x32_bf16 v[28:31], a[124:127], v[92:95], v[28:31]\n"
    "v_exp_f32 v64, v64\n"
    "v_mfma_f32_16x16x32_bf16 v[36:39], a[204:207], v[92:95], v[36:39]\n"
    "buffer_load_dwordx4 v[140:143], v0, s[40:43], s56 offen offset:1024 sc1\n"
    "ds_read_b128 v[92:95], v1 offset:14336\n"
    "s_waitcnt lgkmcnt(4)\n"
    "v_mfma_f32_16x16x32_bf16 v[16:19], a[48:51], v[96:99], v[16:19]\n"
    "v_exp_f32 v65, v65\n"
    "v_mfma_f32_16x16x32_bf16 v[24:27], a[128:131], v[96:99], v[24:27]\n"
    "v_exp_f32 v66, v66\n"
    "v_mfma_f32_16x16x32_bf16 v[32:35], a[208:211], v[96:99], v[32:35]\n"
    "buffer_load_dwordx4 v[144:147], v0, s[40:43], s56 offen offset:2048 sc1\n"
    "ds_read_b128 v[96:99], v1 offset:34816\n"
    "s_waitcnt lgkmcnt(4)\n"
    "v_mfma_f32_16x16x32_bf16 v[20:23], a[48:51], v[80:83], v[20:23]\n"
    "v_exp_f32 v67, v67\n"
    "v_mfma_f32_16x16x32_bf16 v[28:31], a[128:131], v[80:83], v[28:31]\n"
    "v_exp_f32 v68, v68\n"
    "v_mfma_f32_16x16x32_bf16 v[36:39], a[208:211], v[80:83], v[36:39]\n"
    "buffer_load_dwordx4 v[148:151], v0, s[40:43], s54 offen offset:2048 sc1\n"
    "ds_read_b128 v[80:83], v1 offset:15360\n"
    "s_waitcnt lgkmcnt(4)\n"
    "v_mfma_f32_16x16x32_bf16 v[16:19], a[52:55], v[84:87], v[16:19]\n"
    "v_exp_f32 v69, v69\n"
    "v_mfma_f32_16x16x32_bf16 v[24:27], a[132:135], v[84:87], v[24:27]\n"
    "v_exp_f32 v70, v70\n"
    "v_mfma_f32_16x16x32_bf16 v[32:35], a[212:215], v[84:87], v[32:35]\n"
    "buffer_load_dwordx4 v[152:155], v0, s[40:43], s55 offen offset:1024 sc1\n"
    "ds_read_b128 v[84:87], v1 offset:35840\n"
    "s_waitcnt lgkmcnt(4)\n"
    "v_mfma_f32_16x16x32_bf16 v[20:23], a[52:55], v[88:91], v[20:23]\n"
    "v_exp_f32 v71, v71\n"
    "v_mfma_f32_16x16x32_bf16 v[28:31], a[132:135], v[88:91], v[28:31]\n"
    "v_exp_f32 v72, v72\n"
    "v_mfma_f32_16x16x32_bf16 v[36:39], a[212:215], v[88:91], v[36:39]\n"
    "buffer_load_dwordx4 v[156:159], v0, s[40:43], s56 offen sc1\n"
    "ds_read_b128 v[88:91], v1 offset:16384\n"
    "s_waitcnt lgkmcnt(4)\n"
    "v_mfma_f32_16x16x32_bf16 v[16:19], a[56:59], v[92:95], v[16:19]\n"
    "v_exp_f32 v73, v73\n"
    "v_mfma_f32_16x16x32_bf16 v[24:27], a[136:139], v[92:95], v[24:27]\n"
    "v_exp_f32 v74, v74\n"
    "v_mfma_f32_16x16x32_bf16 v[32:35], a[216:219], v[92:95], v[32:35]\n"
    "buffer_load_dwordx4 v[160:163], v0, s[40:43], s56 offen offset:3072 sc1\n"
    "s_add_u32 s59, s63, 0\n"
    "s_and_b32 s59, s59, 63\n"
    "s_cmp_eq_u32 s59, 31\n"
    "s_cselect_b32 s59, -31, 1\n"
    "s_mul_i32 s48, s59, 393216\n"
    "s_add_u32 s54, s54, s48\n"
    "s_add_u32 s55, s55, s48\n"
    "s_add_u32 s56, s56, s48\n"
    "ds_read_b128 v[92:95], v1 offset:36864\n"
    "s_waitcnt lgkmcnt(4)\n"
    "v_mfma_f32_16x16x32_bf16 v[20:23], a[56:59], v[96:99], v[20:23]\n"
    "v_exp_f32 v75, v75\n"
    "v_mfma_f32_16x16x32_bf16 v[28:31], a[136:139], v[96:99], v[28:31]\n"
    "v_add_f32 v76, v64, v65\n"
    "v_add_f32 v76, v76, v66\n"
    "v_mfma_f32_16x16x32_bf16 v[36:39], a[216:219], v[96:99], v[36:39]\n"
    "s_waitcnt vmcnt(13) lgkmcnt(4)\n"
    "s_barrier\n"
    "buffer_atomic_add v226, v3, s[40:43], s29 offen\n"
    "s_and_b32 s48, s47, 63\n"
    "s_cmp_lt_u32 s48, 62\n"
    "s_cselect_b32 s48, 1, 0\n"
    "s_mov_b32 s60, 0\n"
    "v_cmp_gt_u32 vcc, s48, v224\n"
    "s_nop 4\n"
    "s_cbranch_vccz hpq_xf0_ok_%=\n"
    "hpq_xf0_poll_%=:\n"
    "buffer_load_dword v224, v225, s[40:43], s58 offen sc1\n"
    "s_waitcnt vmcnt(0)\n"
    "v_cmp_gt_u32 vcc, s48, v224\n"
    "s_nop 4\n"
    "s_cbranch_vccz hpq_xf0_ok_%=\n"
    "s_sleep 2\n"
    "s_add_u32 s60, s60, 1\n"
    "s_cmp_gt_u32 s60, 65536\n"
    "s_cbranch_scc1 hp_abort_%=\n"
    "s_and_b32 s59, s60, 63\n"
    "s_cbranch_scc1 hpq_xf0_poll_%=\n"
    "v_mov_b32 v4, 1024\n"
    "buffer_load_dword v4, v4, s[40:43], s58 offen sc1\n"
    "s_waitcnt vmcnt(0)\n"
    "v_readfirstlane_b32 s59, v4\n"
    "s_nop 0\n"
    "s_cmp_lg_u32 s59, 0\n"
    "s_nop 2\n"
    "s_cbranch_scc1 hp_end_%=\n"
    "s_branch hpq_xf0_poll_%=\n"
    "hpq_xf0_ok_%=:\n"
    "v_add_u32 v225, 128, v225\n"
    "ds_read_b128 v[96:99], v1 offset:17408\n"
    "s_waitcnt lgkmcnt(4)\n"
    "v_mfma_f32_16x16x32_bf16 v[16:19], a[60:63], v[80:83], v[16:19]\n"
    "ds_read_b128 v[188:191], v8 offset:12288\n"
    "v_add_f32 v76, v76, v67\n"
    "s_add_u32 m0, s61, 0\n"
    "s_add_u32 s51, s50, 0\n"
    "buffer_load_dwordx4 v0, s[40:43], s51 offen offset:0 sc1 lds\n"
    "v_mfma_f32_16x16x32_bf16 v[24:27], a[140:143], v[80:83], v[24:27]\n"
    "ds_read_b128 v[192:195], v8 offset:14336\n"
    "v_add_f32 v76, v76, v68\n"
    "v_add_f32 v76, v76, v69\n"
    "v_mfma_f32_16x16x32_bf16 v[32:35], a[220:223], v[80:83], v[32:35]\n"
    "v_add_f32 v76, v76, v70\n"
    "ds_read_b128 v[80:83], v1 offset:37888\n"
    "s_waitcnt lgkmcnt(6)\n"
    "v_mfma_f32_16x16x32_bf16 v[20:23], a[60:63], v[84:87], v[20:23]\n"
    "ds_read_b128 v[196:199], v8 offset:16384\n"
    "v_add_f32 v76, v76, v71\n"
    "buffer_load_dwordx4 v0, s[40:43], s51 offen offset:1024 sc1 lds\n"
    "v_mfma_f32_16x16x32_bf16 v[28:31], a[140:143], v[84:87], v[28:31]\n"
    "ds_read_b128 v[200:203], v8 offset:18432\n"
    "v_add_f32 v76, v76, v72\n"
    "v_add_f32 v76, v76, v73\n"
    "v_mfma_f32_16x16x32_bf16 v[36:39], a[220:223], v[84:87], v[36:39]\n"
    "v_add_f32 v76, v76, v74\n"
    "ds_read_b128 v[84:87], v1 offset:18432\n"
    "s_waitcnt lgkmcnt(8)\n"
    "v_mfma_f32_16x16x32_bf16 v[16:19], a[64:67], v[88:91], v[16:19]\n"
    "ds_read_b128 v[204:207], v8 offset:20480\n"
    "v_add_f32 v76, v76, v75\n"
    "buffer_load_dwordx4 v0, s[40:43], s51 offen offset:2048 sc1 lds\n"
    "v_mfma_f32_16x16x32_bf16 v[24:27], a[144:147], v[88:91], v[24:27]\n"
    "ds_read_b128 v[208:211], v8 offset:22528\n"
    "v_mov_b32 v77, v76\n"
    "s_nop 1\n"
    "v_permlane16_swap_b32 v76, v77\n"
    "v_mfma_f32_16x16x32_bf16 v[32:35], a[224:227], v[88:91], v[32:35]\n"
    "v_add_f32 v76, v76, v77\n"
    "ds_read_b128 v[88:91], v1 offset:38912\n"
    "s_waitcnt lgkmcnt(10)\n"
    "v_mfma_f32_16x16x32_bf16 v[20:23], a[64:67], v[92:95], v[20:23]\n"
    "v_mov_b32 v77, v76\n"
    "buffer_load_dwordx4 v0, s[40:43], s51 offen offset:3072 sc1 lds\n"
    "v_mfma_f32_16x16x32_bf16 v[28:31], a[144:147], v[92:95], v[28:31]\n"
    "v_permlane32_swap_b32 v76, v77\n"
    "v_mfma_f32_16x16x32_bf16 v[36:39], a[224:227], v[92:95], v[36:39]\n"
    "v_add_f32 v76, v76, v77\n"
    "ds_read_b128 v[92:95], v1 offset:19456\n"
    "s_waitcnt lgkmcnt(10)\n"
    "v_mfma_f32_16x16x32_bf16 v[16:19], a[68:71], v[96:99], v[16:19]\n"
    "v_rcp_f32 v76, v76\n"
    "s_add_u32 m0, s61, 4096\n"
    "s_add_u32 s51, s50, 4096\n"
    "buffer_load_dwordx4 v0, s[40:43], s51 offen offset:0 sc1 lds\n"
    "v_mfma_f32_16x16x32_bf16 v[24:27], a[148:151], v[96:99], v[24:27]\n"
    "v_mul_f32 v64, v64, v76\n"
    "v_mul_f32 v65, v65, v76\n"
    "v_mfma_f32_16x16x32_bf16 v[32:35], a[228:231], v[96:99], v[32:35]\n"
    "v_mul_f32 v66, v66, v76\n"
    "ds_read_b128 v[96:99], v1 offset:39936\n"
    "s_waitcnt lgkmcnt(8)\n"
    "v_mfma_f32_16x16x32_bf16 v[20:23], a[68:71], v[80:83], v[20:23]\n"
    "v_mul_f32 v67, v67, v76\n"
    "buffer_load_dwordx4 v0, s[40:43], s51 offen offset:1024 sc1 lds\n"
    "v_mfma_f32_16x16x32_bf16 v[28:31], a[148:151], v[80:83], v[28:31]\n"
    "v_mul_f32 v68, v68, v76\n"
    "v_mul_f32 v69, v69, v76\n"
    "v_mfma_f32_16x16x32_bf16 v[36:39], a[228:231], v[80:83], v[36:39]\n"
    "v_mul_f32 v70, v70, v76\n"
    "s_add_u32 s64, s64, 40960\n"
    "s_cmp_ge_u32 s64, 122880\n"
    "s_cselect_b32 s64, 0, s64\n"
    "v_add_u32 v1, s64, v0\n"
    "ds_read_b128 v[80:83], v1 offset:0\n"
    "s_waitcnt lgkmcnt(6)\n"
    "v_mfma_f32_16x16x32_bf16 v[16:19], a[72:75], v[84:87], v[16:19]\n"
    "v_mul_f32 v71, v71, v76\n"
    "buffer_load_dwordx4 v0, s[40:43], s51 offen offset:2048 sc1 lds\n"
    "v_mfma_f32_16x16x32_bf16 v[24:27], a[152:155], v[84:87], v[24:27]\n"
    "v_mul_f32 v72, v72, v76\n"
    "v_mul_f32 v73, v73, v76\n"
    "v_mfma_f32_16x16x32_bf16 v[32:35], a[232:235], v[84:87], v[32:35]\n"
    "v_mul_f32 v74, v74, v76\n"
    "ds_read_b128 v[84:87], v1 offset:20480\n"
    "s_waitcnt lgkmcnt(4)\n"
    "v_mfma_f32_16x16x32_bf16 v[20:23], a[72:75], v[88:91], v[20:23]\n"
    "v_mul_f32 v75, v75, v76\n"
    "buffer_load_dwordx4 v0, s[40:43], s51 offen offset:3072 sc1 lds\n"
    "v_mfma_f32_16x16x32_bf16 v[28:31], a[152:155], v[88:91], v[28:31]\n"
    "v_cvt_pk_bf16_f32 v228, v64, v65\n"
    "v_cvt_pk_bf16_f32 v229, v66, v67\n"
    "v_mfma_f32_16x16x32_bf16 v[36:39], a[232:235], v[88:91], v[36:39]\n"
    "v_cvt_pk_bf16_f32 v230, v68, v69\n"
    "ds_read_b128 v[88:91], v1 offset:1024\n"
    "s_waitcnt lgkmcnt(4)\n"
    "v_mfma_f32_16x16x32_bf16 v[16:19], a[76:79], v[92:95], v[16:19]\n"
    "v_cvt_pk_bf16_f32 v231, v70, v71\n"
    "s_add_u32 m0, s61, 8192\n"
    "s_add_u32 s51, s50, 8192\n"
    "buffer_load_dwordx4 v0, s[40:43], s51 offen offset:0 sc1 lds\n"
    "v_mfma_f32_16x16x32_bf16 v[24:27], a[156:159], v[92:95], v[24:27]\n"
    "v_cvt_pk_bf16_f32 v232, v72, v73\n"
    "v_cvt_pk_bf16_f32 v233, v74, v75\n"
    "v_mfma_f32_16x16x32_bf16 v[32:35], a[236:239], v[92:95], v[32:35]\n"
    "v_mov_b32 v234, v232\n"
    "ds_read_b128 v[92:95], v1 offset:21504\n"
    "s_waitcnt lgkmcnt(4)\n"
    "v_mfma_f32_16x16x32_bf16 v[20:23], a[76:79], v[96:99], v[20:23]\n"
    "v_mov_b32 v235, v233\n"
    "buffer_load_dwordx4 v0, s[40:43], s51 offen offset:1024 sc1 lds\n"
    "s_add_u32 s50, s50, 40960\n"
    "s_add_u32 s61, s61, 40960\n"
    "s_cmp_ge_u32 s61, 122880\n"
    "s_cselect_b32 s51, 122880, 0\n"
    "s_sub_u32 s61, s61, s51\n"
    "v_mfma_f32_16x16x32_bf16 v[28:31], a[156:159], v[96:99], v[28:31]\n"
    "buffer_store_dwordx4 v[228:231], v0, s[40:43], s26 offen sc1\n"
    "v_permlane16_swap_b32 v232, v234\n"
    "v_mfma_f32_16x16x32_bf16 v[36:39], a[236:239], v[96:99], v[36:39]\n"
    "v_permlane16_swap_b32 v233, v235\n"
    "ds_read_b128 v[96:99], v1 offset:2048\n"
    "buffer_store_dwordx4 v[232:235], v12, s[40:43], s27 offen sc1\n"
    "s_add_u32 s47, s47, 1\n"
    "s_mov_b32 s29, s52\n"
    "s_waitcnt vmcnt(25)\n"
    "s_add_u32 s48, s63, 1\n"
    "s_cmp_ge_i32 s63, s65\n"
    "s_cselect_b32 s48, s48, 0\n"
    "s_cselect_b32 s49, s30, 0x40000000\n"
    "v_cmp_gt_u32 vcc, s48, v9\n"
    "s_cbranch_vccz hpq_kma1_ok_%=\n"
    "s_waitcnt vmcnt(0)\n"
    "buffer_atomic_add v226, v3, s[40:43], s29 offen\n"
    "s_mov_b32 s29, 0x40000000\n"
    "s_mov_b32 s60, 0\n"
    "v_cmp_gt_u32 vcc, s48, v9\n"
    "s_nop 4\n"
    "s_cbranch_vccz hpq_kma1_ok_%=\n"
    "hpq_kma1_poll_%=:\n"
    "buffer_load_dword v9, v10, s[40:43], s58 offen sc1\n"
    "s_waitcnt vmcnt(0)\n"
    "v_cmp_gt_u32 vcc, s48, v9\n"
    "s_nop 4\n"
    "s_cbranch_vccz hpq_kma1_ok_%=\n"
    "s_sleep 2\n"
    "s_add_u32 s60, s60, 1\n"
    "s_cmp_gt_u32 s60, 65536\n"
    "s_cbranch_scc1 hp_abort_%=\n"
    "s_and_b32 s59, s60, 63\n"
    "s_cbranch_scc1 hpq_kma1_poll_%=\n"
    "v_mov_b32 v4, 1024\n"
    "buffer_load_dword v4, v4, s[40:43], s58 offen sc1\n"
    "s_waitcnt vmcnt(0)\n"
    "v_readfirstlane_b32 s59, v4\n"
    "s_nop 0\n"
    "s_cmp_lg_u32 s59, 0\n"
    "s_nop 2\n"
    "s_cbranch_scc1 hp_end_%=\n"
    "s_branch hpq_kma1_poll_%=\n"
    "hpq_kma1_ok_%=:\n"
    "s_movk_i32 s48, 32\n"
    "s_mov_b32 s60, 0\n"
    "v_cmp_gt_u32 vcc, s48, v236\n"
    "s_nop 4\n"
    "s_cbranch_vccz hpq_kxa1_ok_%=\n"
    "hpq_kxa1_poll_%=:\n"
    "buffer_load_dword v236, v227, s[40:43], s58 offen sc1\n"
    "s_waitcnt vmcnt(0)\n"
    "v_cmp_gt_u32 vcc, s48, v236\n"
    "s_nop 4\n"
    "s_cbranch_vccz hpq_kxa1_ok_%=\n"
    "s_sleep 2\n"
    "s_add_u32 s60, s60, 1\n"
    "s_cmp_gt_u32 s60, 65536\n"
    "s_cbranch_scc1 hp_abort_%=\n"
    "s_and_b32 s59, s60, 63\n"
    "s_cbranch_scc1 hpq_kxa1_poll_%=\n"
    "v_mov_b32 v4, 1024\n"
    "buffer_load_dword v4, v4, s[40:43], s58 offen sc1\n"
    "s_waitcnt vmcnt(0)\n"
    "v_readfirstlane_b32 s59, v4\n"
    "s_nop 0\n"
    "s_cmp_lg_u32 s59, 0\n"
    "s_nop 2\n"
    "s_cbranch_scc1 hp_end_%=\n"
    "s_branch hpq_kxa1_poll_%=\n"
    "hpq_kxa1_ok_%=:\n"
    "v_add_u32 v227, 128, v227\n"
    "s_or_b32 s57, s34, s49\n"
    "s_or_b32 s52, s35, s49\n"
    "s_or_b32 s26, s36, s49\n"
    "s_or_b32 s27, s37, s49\n"
    "s_add_u32 s59, s63, 0\n"
    "s_and_b32 s59, s59, 63\n"
    "s_cmp_eq_u32 s59, 31\n"
    "s_cselect_b32 s59, -31, 1\n"
    "s_mul_i32 s48, s59, 98304\n"
    "s_add_u32 s34, s34, s48\n"
    "s_mul_i32 s48, s59, 65536\n"
    "s_add_u32 s36, s36, s48\n"
    "s_mul_i32 s48, s59, 16384\n"
    "s_add_u32 s37, s37, s48\n"
    "s_add_u32 s35, s35, 1536\n"
    "s_add_u32 s63, s63, 1\n"
    "s_waitcnt lgkmcnt(4)\n"
    "v_mfma_f32_16x16x32_bf16 v[40:43], a[0:3], v[80:83], 0\n"
    "buffer_load_dwordx4 v[164:167], v11, s[40:43], s57 offen sc1\n"
    "v_mfma_f32_16x16x32_bf16 v[48:51], a[80:83], v[80:83], 0\n"
    "buffer_load_dwordx4 v[168:171], v11, s[40:43], s57 offen offset:512 sc1\n"
    "v_mfma_f32_16x16x32_bf16 v[56:59], a[160:163], v[80:83], 0\n"
    "buffer_load_dwordx4 v[172:175], v11, s[40:43], s57 offen offset:1024 sc1\n"
    "ds_read_b128 v[80:83], v1 offset:22528\n"
    "s_waitcnt lgkmcnt(4)\n"
    "v_mfma_f32_16x16x32_bf16 v[44:47], a[0:3], v[84:87], 0\n"
    "buffer_load_dwordx4 v[176:179], v11, s[40:43], s57 offen offset:1536 sc1\n"
    "v_mfma_f32_16x16x32_bf16 v[52:55], a[80:83], v[84:87], 0\n"
    "buffer_load_dwordx4 v[180:183], v11, s[40:43], s57 offen offset:2048 sc1\n"
    "v_mfma_f32_16x16x32_bf16 v[60:63], a[160:163], v[84:87], 0\n"
    "buffer_load_dwordx4 v[184:187], v11, s[40:43], s57 offen offset:2560 sc1\n"
    "ds_read_b128 v[84:87], v1 offset:3072\n"
    "s_waitcnt lgkmcnt(4)\n"
    "v_mfma_f32_16x16x32_bf16 v[40:43], a[4:7], v[88:91], v[40:43]\n"
    "buffer_load_dword v236, v227, s[40:43], s58 offen sc1\n"
    "v_mfma_f32_16x16x32_bf16 v[48:51], a[84:87], v[88:91], v[48:51]\n"
    "v_mfma_f32_16x16x32_bf16 v[56:59], a[164:167], v[88:91], v[56:59]\n"
    "ds_read_b128 v[88:91], v1 offset:23552\n"
    "s_waitcnt lgkmcnt(4)\n"
    "v_mfma_f32_16x16x32_bf16 v[44:47], a[4:7], v[92:95], v[44:47]\n"
    "v_cvt_pk_bf16_f32 v104, v16, v17\n"
    "v_mfma_f32_16x16x32_bf16 v[52:55], a[84:87], v[92:95], v[52:55]\n"
    "v_cvt_pk_bf16_f32 v105, v18, v19\n"
    "v_mfma_f32_16x16x32_bf16 v[60:63], a[164:167], v[92:95], v[60:63]\n"
    "ds_read_b128 v[92:95], v1 offset:4096\n"
    "s_waitcnt lgkmcnt(4)\n"
    "v_mfma_f32_16x16x32_bf16 v[40:43], a[8:11], v[96:99], v[40:43]\n"
    "v_cvt_pk_bf16_f32 v106, v20, v21\n"
    "v_mfma_f32_16x16x32_bf16 v[48:51], a[88:91], v[96:99], v[48:51]\n"
    "v_cvt_pk_bf16_f32 v107, v22, v23\n"
    "v_mfma_f32_16x16x32_bf16 v[56:59], a[168:171], v[96:99], v[56:59]\n"
    "ds_read_b128 v[96:99], v1 offset:24576\n"
    "s_waitcnt lgkmcnt(4)\n"
    "v_mfma_f32_16x16x32_bf16 v[44:47], a[8:11], v[80:83], v[44:47]\n"
    "ds_write2st64_b64 v6, v[104:105], v[106:107] offset0:0 offset1:2\n"
    "v_mfma_f32_16x16x32_bf16 v[52:55], a[88:91], v[80:83], v[52:55]\n"
    "v_cvt_pk_bf16_f32 v108, v24, v25\n"
    "v_mfma_f32_16x16x32_bf16 v[60:63], a[168:171], v[80:83], v[60:63]\n"
    "ds_read_b128 v[80:83], v1 offset:5120\n"
    "s_waitcnt lgkmcnt(5)\n"
    "v_mfma_f32_16x16x32_bf16 v[40:43], a[12:15], v[84:87], v[40:43]\n"
    "v_cvt_pk_bf16_f32 v109, v26, v27\n"
    "v_mfma_f32_16x16x32_bf16 v[48:51], a[92:95], v[84:87], v[48:51]\n"
    "v_cvt_pk_bf16_f32 v110, v28, v29\n"
    "v_mfma_f32_16x16x32_bf16 v[56:59], a[172:175], v[84:87], v[56:59]\n"
    "ds_read_b128 v[84:87], v1 offset:25600\n"
    "s_waitcnt lgkmcnt(5)\n"
    "v_mfma_f32_16x16x32_bf16 v[44:47], a[12:15], v[88:91], v[44:47]\n"
    "v_cvt_pk_bf16_f32 v111, v30, v31\n"
    "v_mfma_f32_16x16x32_bf16 v[52:55], a[92:95], v[88:91], v[52:55]\n"
    "ds_write2st64_b64 v2, v[108:109], v[110:111] offset0:0 offset1:2\n"
    "v_mfma_f32_16x16x32_bf16 v[60:63], a[172:175], v[88:91], v[60:63]\n"
    "ds_read_b128 v[88:91], v1 offset:6144\n"
    "s_waitcnt lgkmcnt(6)\n"
    "v_mfma_f32_16x16x32_bf16 v[40:43], a[16:19], v[92:95], v[40:43]\n"
    "v_cvt_pk_bf16_f32 v112, v32, v33\n"
    "v_mfma_f32_16x16x32_bf16 v[48:51], a[96:99], v[92:95], v[48:51]\n"
    "v_cvt_pk_bf16_f32 v113, v34, v35\n"
    "v_mfma_f32_16x16x32_bf16 v[56:59], a[176:179], v[92:95], v[56:59]\n"
    "s_waitcnt vmcnt(20)\n"
    "ds_read_b128 v[92:95], v1 offset:26624\n"
    "s_waitcnt lgkmcnt(6)\n"
    "v_mfma_f32_16x16x32_bf16 v[44:47], a[16:19], v[96:99], v[44:47]\n"
    "v_cvt_pk_bf16_f32 v114, v36, v37\n"
    "v_mfma_f32_16x16x32_bf16 v[52:55], a[96:99], v[96:99], v[52:55]\n"
    "v_cvt_pk_bf16_f32 v115, v38, v39\n"
    "v_mfma_f32_16x16x32_bf16 v[60:63], a[176:179], v[96:99], v[60:63]\n"
    "v_swap_b32 v118, v120\n"
    "ds_read_b128 v[96:99], v1 offset:7168\n"
    "s_waitcnt lgkmcnt(5)\n"
    "v_mfma_f32_16x16x32_bf16 v[40:43], a[20:23], v[80:83], v[40:43]\n"
    "ds_write2st64_b64 v7, v[112:113], v[114:115] offset0:0 offset1:2\n"
    "v_mfma_f32_16x16x32_bf16 v[48:51], a[100:103], v[80:83], v[48:51]\n"
    "buffer_load_dword v224, v225, s[40:43], s58 offen sc1\n"
    "v_mfma_f32_16x16x32_bf16 v[56:59], a[180:183], v[80:83], v[56:59]\n"
    "v_swap_b32 v119, v121\n"
    "ds_read_b128 v[80:83], v1 offset:27648\n"
    "s_waitcnt lgkmcnt(6)\n"
    "v_mfma_f32_16x16x32_bf16 v[44:47], a[20:23], v[84:87], v[44:47]\n"
    "v_swap_b32 v126, v128\n"
    "v_mfma_f32_16x16x32_bf16 v[52:55], a[100:103], v[84:87], v[52:55]\n"
    "v_swap_b32 v127, v129\n"
    "v_mfma_f32_16x16x32_bf16 v[60:63], a[180:183], v[84:87], v[60:63]\n"
    "v_swap_b32 v134, v136\n"
    "ds_read_b128 v[84:87], v1 offset:8192\n"
    "s_waitcnt lgkmcnt(5)\n"
    "v_mfma_f32_16x16x32_bf16 v[40:43], a[24:27], v[88:91], v[40:43]\n"
    "v_swap_b32 v135, v137\n"
    "v_mfma_f32_16x16x32_bf16 v[48:51], a[104:107], v[88:91], v[48:51]\n"
    "v_swap_b32 v142, v144\n"
    "v_mfma_f32_16x16x32_bf16 v[56:59], a[184:187], v[88:91], v[56:59]\n"
    "v_swap_b32 v143, v145\n"
    "ds_read_b128 v[88:91], v1 offset:28672\n"
    "s_waitcnt lgkmcnt(5)\n"
    "v_mfma_f32_16x16x32_bf16 v[44:47], a[24:27], v[92:95], v[44:47]\n"
    "v_swap_b32 v150, v152\n"
    "v_mfma_f32_16x16x32_bf16 v[52:55], a[104:107], v[92:95], v[52:55]\n"
    "v_swap_b32 v151, v153\n"
    "v_mfma_f32_16x16x32_bf16 v[60:63], a[184:187], v[92:95], v[60:63]\n"
    "v_swap_b32 v158, v160\n"
    "ds_read_b128 v[92:95], v1 offset:9216\n"
    "s_waitcnt lgkmcnt(5)\n"
    "v_mfma_f32_16x16x32_bf16 v[40:43], a[28:31], v[96:99], v[40:43]\n"
    "v_swap_b32 v159, v161\n"
    "v_mfma_f32_16x16x32_bf16 v[48:51], a[108:111], v[96:99], v[48:51]\n"
    "v_mfma_f32_16x16x32_bf16 v[56:59], a[188:191], v[96:99], v[56:59]\n"
    "ds_read_b128 v[96:99], v1 offset:29696\n"
    "s_waitcnt lgkmcnt(4)\n"
    "v_mfma_f32_16x16x32_bf16 v[44:47], a[28:31], v[80:83], v[44:47]\n"
    "v_mfma_f32_16x16x32_bf16 v[64:67], v[116:119], v[188:191], v[212:215]\n"
    "v_mfma_f32_16x16x32_bf16 v[68:71], v[120:123], v[188:191], v[216:219]\n"
    "s_waitcnt vmcnt(7)\n"
    "v_mfma_f32_16x16x32_bf16 v[72:75], v[164:167], v[188:191], v[220:223]\n"
    "v_mfma_f32_16x16x32_bf16 v[64:67], v[124:127], v[192:195], v[64:67]\n"
    "v_mfma_f32_16x16x32_bf16 v[68:71], v[128:131], v[192:195], v[68:71]\n"
    "s_waitcnt vmcnt(6)\n"
    "v_mfma_f32_16x16x32_bf16 v[72:75], v[168:171], v[192:195], v[72:75]\n"
    "v_mfma_f32_16x16x32_bf16 v[64:67], v[132:135], v[196:199], v[64:67]\n"
    "v_mfma_f32_16x16x32_bf16 v[68:71], v[136:139], v[196:199], v[68:71]\n"
    "s_waitcnt vmcnt(5)\n"
    "v_mfma_f32_16x16x32_bf16 v[72:75], v[172:175], v[196:199], v[72:75]\n"
    "v_mfma_f32_16x16x32_bf16 v[64:67], v[140:143], v[200:203], v[64:67]\n"
    "v_mfma_f32_16x16x32_bf16 v[68:71], v[144:147], v[200:203], v[68:71]\n"
    "s_waitcnt vmcnt(4)\n"
    "v_mfma_f32_16x16x32_bf16 v[72:75], v[176:179], v[200:203], v[72:75]\n"
    "v_mfma_f32_16x16x32_bf16 v[64:67], v[148:151], v[204:207], v[64:67]\n"
    "v_mfma_f32_16x16x32_bf16 v[68:71], v[152:155], v[204:207], v[68:71]\n"
    "s_waitcnt vmcnt(3)\n"
    "v_mfma_f32_16x16x32_bf16 v[72:75], v[180:183], v[204:207], v[72:75]\n"
    "v_mfma_f32_16x16x32_bf16 v[64:67], v[156:159], v[208:211], v[64:67]\n"
    "v_mfma_f32_16x16x32_bf16 v[68:71], v[160:163], v[208:211], v[68:71]\n"
    "s_waitcnt vmcnt(2)\n"
    "v_mfma_f32_16x16x32_bf16 v[72:75], v[184:187], v[208:211], v[72:75]\n"
    "v_mfma_f32_16x16x32_bf16 v[52:55], a[108:111], v[80:83], v[52:55]\n"
    "v_mfma_f32_16x16x32_bf16 v[60:63], a[188:191], v[80:83], v[60:63]\n"
    "ds_read_b128 v[80:83], v1 offset:10240\n"
    "s_waitcnt lgkmcnt(4)\n"
    "v_mfma_f32_16x16x32_bf16 v[40:43], a[32:35], v[84:87], v[40:43]\n"
    "v_mfma_f32_16x16x32_bf16 v[48:51], a[112:115], v[84:87], v[48:51]\n"
    "v_max3_f32 v76, v64, v65, v66\n"
    "v_mfma_f32_16x16x32_bf16 v[56:59], a[192:195], v[84:87], v[56:59]\n"
    "buffer_load_dword v9, v10, s[40:43], s58 offen sc1\n"
    "ds_read_b128 v[84:87], v1 offset:30720\n"
    "s_waitcnt lgkmcnt(4)\n"
    "v_mfma_f32_16x16x32_bf16 v[44:47], a[32:35], v[88:91], v[44:47]\n"
    "v_max3_f32 v76, v76, v67, v68\n"
    "v_max3_f32 v76, v76, v69, v70\n"
    "v_mfma_f32_16x16x32_bf16 v[52:55], a[112:115], v[88:91], v[52:55]\n"
    "v_max3_f32 v76, v76, v71, v72\n"
    "v_max3_f32 v76, v76, v73, v74\n"
    "v_mfma_f32_16x16x32_bf16 v[60:63], a[192:195], v[88:91], v[60:63]\n"
    "buffer_load_dwordx4 v[116:119], v0, s[40:43], s54 offen sc1\n"
    "ds_read_b128 v[88:91], v1 offset:11264\n"
    "s_waitcnt lgkmcnt(4)\n"
    "v_mfma_f32_16x16x32_bf16 v[40:43], a[36:39], v[92:95], v[40:43]\n"
    "v_max_f32 v76, v76, v75\n"
    "v_mov_b32 v77, v76\n"
    "v_mfma_f32_16x16x32_bf16 v[48:51], a[116:119], v[92:95], v[48:51]\n"
    "s_nop 0\n"
    "v_permlane16_swap_b32 v76, v77\n"
    "v_mfma_f32_16x16x32_bf16 v[56:59], a[196:199], v[92:95], v[56:59]\n"
    "buffer_load_dwordx4 v[120:123], v0, s[40:43], s54 offen offset:1024 sc1\n"
    "ds_read_b128 v[92:95], v1 offset:31744\n"
    "s_waitcnt lgkmcnt(4)\n"
    "v_mfma_f32_16x16x32_bf16 v[44:47], a[36:39], v[96:99], v[44:47]\n"
    "v_max_f32 v76, v76, v77\n"
    "v_mov_b32 v77, v76\n"
    "v_mfma_f32_16x16x32_bf16 v[52:55], a[116:119], v[96:99], v[52:55]\n"
    "s_nop 0\n"
    "v_permlane32_swap_b32 v76, v77\n"
    "v_mfma_f32_16x16x32_bf16 v[60:63], a[196:199], v[96:99], v[60:63]\n"
    "buffer_load_dwordx4 v[124:127], v0, s[40:43], s54 offen offset:3072 sc1\n"
    "ds_read_b128 v[96:99], v1 offset:12288\n"
    "s_waitcnt lgkmcnt(4)\n"
    "v_mfma_f32_16x16x32_bf16 v[40:43], a[40:43], v[80:83], v[40:43]\n"
    "v_max_f32 v76, v76, v77\n"
    "v_mul_f32 v76, 0xbdd53b95, v76\n"
    "v_mfma_f32_16x16x32_bf16 v[48:51], a[120:123], v[80:83], v[48:51]\n"
    "v_fmamk_f32 v64, v64, 0x3dd53b95, v76\n"
    "v_fmamk_f32 v65, v65, 0x3dd53b95, v76\n"
    "v_mfma_f32_16x16x32_bf16 v[56:59], a[200:203], v[80:83], v[56:59]\n"
    "buffer_load_dwordx4 v[128:131], v0, s[40:43], s55 offen sc1\n"
    "ds_read_b128 v[80:83], v1 offset:32768\n"
    "s_waitcnt lgkmcnt(4)\n"
    "v_mfma_f32_16x16x32_bf16 v[44:47], a[40:43], v[84:87], v[44:47]\n"
    "v_fmamk_f32 v66, v66, 0x3dd53b95, v76\n"
    "v_fmamk_f32 v67, v67, 0x3dd53b95, v76\n"
    "v_mfma_f32_16x16x32_bf16 v[52:55], a[120:123], v[84:87], v[52:55]\n"
    "v_fmamk_f32 v68, v68, 0x3dd53b95, v76\n"
    "v_fmamk_f32 v69, v69, 0x3dd53b95, v76\n"
    "v_mfma_f32_16x16x32_bf16 v[60:63], a[200:203], v[84:87], v[60:63]\n"
    "buffer_load_dwordx4 v[132:135], v0, s[40:43], s55 offen offset:2048 sc1\n"
    "ds_read_b128 v[84:87], v1 offset:13312\n"
    "s_waitcnt lgkmcnt(4)\n"
    "v_mfma_f32_16x16x32_bf16 v[40:43], a[44:47], v[88:91], v[40:43]\n"
    "v_fmamk_f32 v70, v70, 0x3dd53b95, v76\n"
    "v_fmamk_f32 v71, v71, 0x3dd53b95, v76\n"
    "v_mfma_f32_16x16x32_bf16 v[48:51], a[124:127], v[88:91], v[48:51]\n"
    "v_fmamk_f32 v72, v72, 0x3dd53b95, v76\n"
    "v_fmamk_f32 v73, v73, 0x3dd53b95, v76\n"
    "v_mfma_f32_16x16x32_bf16 v[56:59], a[204:207], v[88:91], v[56:59]\n"
    "buffer_load_dwordx4 v[136:139], v0, s[40:43], s55 offen offset:3072 sc1\n"
    "ds_read_b128 v[88:91], v1 offset:33792\n"
    "s_waitcnt lgkmcnt(4)\n"
    "v_mfma_f32_16x16x32_bf16 v[44:47], a[44:47], v[92:95], v[44:47]\n"
    "v_fmamk_f32 v74, v74, 0x3dd53b95, v76\n"
    "v_fmamk_f32 v75, v75, 0x3dd53b95, v76\n"
    "v_mfma_f32_16x16x32_bf16 v[52:55], a[124:127], v[92:95], v[52:55]\n"
    "v_exp_f32 v64, v64\n"
    "v_mfma_f32_16x16x32_bf16 v[60:63], a[204:207], v[92:95], v[60:63]\n"
    "buffer_load_dwordx4 v[140:143], v0, s[40:43], s56 offen offset:1024 sc1\n"
    "ds_read_b128 v[92:95], v1 offset:14336\n"
    "s_waitcnt lgkmcnt(4)\n"
    "v_mfma_f32_16x16x32_bf16 v[40:43], a[48:51], v[96:99], v[40:43]\n"
    "v_exp_f32 v65, v65\n"
    "v_mfma_f32_16x16x32_bf16 v[48:51], a[128:131], v[96:99], v[48:51]\n"
    "v_exp_f32 v66, v66\n"
    "v_mfma_f32_16x16x32_bf16 v[56:59], a[208:211], v[96:99], v[56:59]\n"
    "buffer_load_dwordx4 v[144:147], v0, s[40:43], s56 offen offset:2048 sc1\n"
    "ds_read_b128 v[96:99], v1 offset:34816\n"
    "s_waitcnt lgkmcnt(4)\n"
    "v_mfma_f32_16x16x32_bf16 v[44:47], a[48:51], v[80:83], v[44:47]\n"
    "v_exp_f32 v67, v67\n"
    "v_mfma_f32_16x16x32_bf16 v[52:55], a[128:131], v[80:83], v[52:55]\n"
    "v_exp_f32 v68, v68\n"
    "v_mfma_f32_16x16x32_bf16 v[60:63], a[208:211], v[80:83], v[60:63]\n"
    "buffer_load_dwordx4 v[148:151], v0, s[40:43], s54 offen offset:2048 sc1\n"
    "ds_read_b128 v[80:83], v1 offset:15360\n"
    "s_waitcnt lgkmcnt(4)\n"
    "v_mfma_f32_16x16x32_bf16 v[40:43], a[52:55], v[84:87], v[40:43]\n"
    "v_exp_f32 v69, v69\n"
    "v_mfma_f32_16x16x32_bf16 v[48:51], a[132:135], v[84:87], v[48:51]\n"
    "v_exp_f32 v70, v70\n"
    "v_mfma_f32_16x16x32_bf16 v[56:59], a[212:215], v[84:87], v[56:59]\n"
    "buffer_load_dwordx4 v[152:155], v0, s[40:43], s55 offen offset:1024 sc1\n"
    "ds_read_b128 v[84:87], v1 offset:35840\n"
    "s_waitcnt lgkmcnt(4)\n"
    "v_mfma_f32_16x16x32_bf16 v[44:47], a[52:55], v[88:91], v[44:47]\n"
    "v_exp_f32 v71, v71\n"
    "v_mfma_f32_16x16x32_bf16 v[52:55], a[132:135], v[88:91], v[52:55]\n"
    "v_exp_f32 v72, v72\n"
    "v_mfma_f32_16x16x32_bf16 v[60:63], a[212:215], v[88:91], v[60:63]\n"
    "buffer_load_dwordx4 v[156:159], v0, s[40:43], s56 offen sc1\n"
    "ds_read_b128 v[88:91], v1 offset:16384\n"
    "s_waitcnt lgkmcnt(4)\n"
    "v_mfma_f32_16x16x32_bf16 v[40:43], a[56:59], v[92:95], v[40:43]\n"
    "v_exp_f32 v73, v73\n"
    "v_mfma_f32_16x16x32_bf16 v[48:51], a[136:139], v[92:95], v[48:51]\n"
    "v_exp_f32 v74, v74\n"
    "v_mfma_f32_16x16x32_bf16 v[56:59], a[216:219], v[92:95], v[56:59]\n"
    "buffer_load_dwordx4 v[160:163], v0, s[40:43], s56 offen offset:3072 sc1\n"
    "s_add_u32 s59, s63, 0\n"
    "s_and_b32 s59, s59, 63\n"
    "s_cmp_eq_u32 s59, 31\n"
    "s_cselect_b32 s59, -31, 1\n"
    "s_mul_i32 s48, s59, 393216\n"
    "s_add_u32 s54, s54, s48\n"
    "s_add_u32 s55, s55, s48\n"
    "s_add_u32 s56, s56, s48\n"
    "ds_read_b128 v[92:95], v1 offset:36864\n"
    "s_waitcnt lgkmcnt(4)\n"
    "v_mfma_f32_16x16x32_bf16 v[44:47], a[56:59], v[96:99], v[44:47]\n"
    "v_exp_f32 v75, v75\n"
    "v_mfma_f32_16x16x32_bf16 v[52:55], a[136:139], v[96:99], v[52:55]\n"
    "v_add_f32 v76, v64, v65\n"
    "v_add_f32 v76, v76, v66\n"
    "v_mfma_f32_16x16x32_bf16 v[60:63], a[216:219], v[96:99], v[60:63]\n"
    "s_waitcnt vmcnt(13) lgkmcnt(4)\n"
    "s_barrier\n"
    "buffer_atomic_add v226, v3, s[40:43], s29 offen\n"
    "s_and_b32 s48, s47, 63\n"
    "s_cmp_lt_u32 s48, 62\n"
    "s_cselect_b32 s48, 1, 0\n"
    "s_mov_b32 s60, 0\n"
    "v_cmp_gt_u32 vcc, s48, v224\n"
    "s_nop 4\n"
    "s_cbranch_vccz hpq_xf1_ok_%=\n"
    "hpq_xf1_poll_%=:\n"
    "buffer_load_dword v224, v225, s[40:43], s58 offen sc1\n"
    "s_waitcnt vmcnt(0)\n"
    "v_cmp_gt_u32 vcc, s48, v224\n"
    "s_nop 4\n"
    "s_cbranch_vccz hpq_xf1_ok_%=\n"
    "s_sleep 2\n"
    "s_add_u32 s60, s60, 1\n"
    "s_cmp_gt_u32 s60, 65536\n"
    "s_cbranch_scc1 hp_abort_%=\n"
    "s_and_b32 s59, s60, 63\n"
    "s_cbranch_scc1 hpq_xf1_poll_%=\n"
    "v_mov_b32 v4, 1024\n"
    "buffer_load_dword v4, v4, s[40:43], s58 offen sc1\n"
    "s_waitcnt vmcnt(0)\n"
    "v_readfirstlane_b32 s59, v4\n"
    "s_nop 0\n"
    "s_cmp_lg_u32 s59, 0\n"
    "s_nop 2\n"
    "s_cbranch_scc1 hp_end_%=\n"
    "s_branch hpq_xf1_poll_%=\n"
    "hpq_xf1_ok_%=:\n"
    "v_add_u32 v225, 128, v225\n"
    "ds_read_b128 v[96:99], v1 offset:17408\n"
    "s_waitcnt lgkmcnt(4)\n"
    "v_mfma_f32_16x16x32_bf16 v[40:43], a[60:63], v[80:83], v[40:43]\n"
    "ds_read_b128 v[188:191], v8 offset:0\n"
    "v_add_f32 v76, v76, v67\n"
    "s_add_u32 m0, s61, 0\n"
    "s_add_u32 s51, s50, 0\n"
    "buffer_load_dwordx4 v0, s[40:43], s51 offen offset:0 sc1 lds\n"
    "v_mfma_f32_16x16x32_bf16 v[48:51], a[140:143], v[80:83], v[48:51]\n"
    "ds_read_b128 v[192:195], v8 offset:2048\n"
    "v_add_f32 v76, v76, v68\n"
    "v_add_f32 v76, v76, v69\n"
    "v_mfma_f32_16x16x32_bf16 v[56:59], a[220:223], v[80:83], v[56:59]\n"
    "v_add_f32 v76, v76, v70\n"
    "ds_read_b128 v[80:83], v1 offset:37888\n"
    "s_waitcnt lgkmcnt(6)\n"
    "v_mfma_f32_16x16x32_bf16 v[44:47], a[60:63], v[84:87], v[44:47]\n"
    "ds_read_b128 v[196:199], v8 offset:4096\n"
    "v_add_f32 v76, v76, v71\n"
    "buffer_load_dwordx4 v0, s[40:43], s51 offen offset:1024 sc1 lds\n"
    "v_mfma_f32_16x16x32_bf16 v[52:55], a[140:143], v[84:87], v[52:55]\n"
    "ds_read_b128 v[200:203], v8 offset:6144\n"
    "v_add_f32 v76, v76, v72\n"
    "v_add_f32 v76, v76, v73\n"
    "v_mfma_f32_16x16x32_bf16 v[60:63], a[220:223], v[84:87], v[60:63]\n"
    "v_add_f32 v76, v76, v74\n"
    "ds_read_b128 v[84:87], v1 offset:18432\n"
    "s_waitcnt lgkmcnt(8)\n"
    "v_mfma_f32_16x16x32_bf16 v[40:43], a[64:67], v[88:91], v[40:43]\n"
    "ds_read_b128 v[204:207], v8 offset:8192\n"
    "v_add_f32 v76, v76, v75\n"
    "buffer_load_dwordx4 v0, s[40:43], s51 offen offset:2048 sc1 lds\n"
    "v_mfma_f32_16x16x32_bf16 v[48:51], a[144:147], v[88:91], v[48:51]\n"
    "ds_read_b128 v[208:211], v8 offset:10240\n"
    "v_mov_b32 v77, v76\n"
    "s_nop 1\n"
    "v_permlane16_swap_b32 v76, v77\n"
    "v_mfma_f32_16x16x32_bf16 v[56:59], a[224:227], v[88:91], v[56:59]\n"
    "v_add_f32 v76, v76, v77\n"
    "ds_read_b128 v[88:91], v1 offset:38912\n"
    "s_waitcnt lgkmcnt(10)\n"
    "v_mfma_f32_16x16x32_bf16 v[44:47], a[64:67], v[92:95], v[44:47]\n"
    "v_mov_b32 v77, v76\n"
    "buffer_load_dwordx4 v0, s[40:43], s51 offen offset:3072 sc1 lds\n"
    "v_mfma_f32_16x16x32_bf16 v[52:55], a[144:147], v[92:95], v[52:55]\n"
    "v_permlane32_swap_b32 v76, v77\n"
    "v_mfma_f32_16x16x32_bf16 v[60:63], a[224:227], v[92:95], v[60:63]\n"
    "v_add_f32 v76, v76, v77\n"
    "ds_read_b128 v[92:95], v1 offset:19456\n"
    "s_waitcnt lgkmcnt(10)\n"
    "v_mfma_f32_16x16x32_bf16 v[40:43], a[68:71], v[96:99], v[40:43]\n"
    "v_rcp_f32 v76, v76\n"
    "s_add_u32 m0, s61, 4096\n"
    "s_add_u32 s51, s50, 4096\n"
    "buffer_load_dwordx4 v0, s[40:43], s51 offen offset:0 sc1 lds\n"
    "v_mfma_f32_16x16x32_bf16 v[48:51], a[148:151], v[96:99], v[48:51]\n"
    "v_mul_f32 v64, v64, v76\n"
    "v_mul_f32 v65, v65, v76\n"
    "v_mfma_f32_16x16x32_bf16 v[56:59], a[228:231], v[96:99], v[56:59]\n"
    "v_mul_f32 v66, v66, v76\n"
    "ds_read_b128 v[96:99], v1 offset:39936\n"
    "s_waitcnt lgkmcnt(8)\n"
    "v_mfma_f32_16x16x32_bf16 v[44:47], a[68:71], v[80:83], v[44:47]\n"
    "v_mul_f32 v67, v67, v76\n"
    "buffer_load_dwordx4 v0, s[40:43], s51 offen offset:1024 sc1 lds\n"
    "v_mfma_f32_16x16x32_bf16 v[52:55], a[148:151], v[80:83], v[52:55]\n"
    "v_mul_f32 v68, v68, v76\n"
    "v_mul_f32 v69, v69, v76\n"
    "v_mfma_f32_16x16x32_bf16 v[60:63], a[228:231], v[80:83], v[60:63]\n"
    "v_mul_f32 v70, v70, v76\n"
    "s_add_u32 s64, s64, 40960\n"
    "s_cmp_ge_u32 s64, 122880\n"
    "s_cselect_b32 s64, 0, s64\n"
    "v_add_u32 v1, s64, v0\n"
    "ds_read_b128 v[80:83], v1 offset:0\n"
    "s_waitcnt lgkmcnt(6)\n"
    "v_mfma_f32_16x16x32_bf16 v[40:43], a[72:75], v[84:87], v[40:43]\n"
    "v_mul_f32 v71, v71, v76\n"
    "buffer_load_dwordx4 v0, s[40:43], s51 offen offset:2048 sc1 lds\n"
    "v_mfma_f32_16x16x32_bf16 v[48:51], a[152:155], v[84:87], v[48:51]\n"
    "v_mul_f32 v72, v72, v76\n"
    "v_mul_f32 v73, v73, v76\n"
    "v_mfma_f32_16x16x32_bf16 v[56:59], a[232:235], v[84:87], v[56:59]\n"
    "v_mul_f32 v74, v74, v76\n"
    "ds_read_b128 v[84:87], v1 offset:20480\n"
    "s_waitcnt lgkmcnt(4)\n"
    "v_mfma_f32_16x16x32_bf16 v[44:47], a[72:75], v[88:91], v[44:47]\n"
    "v_mul_f32 v75, v75, v76\n"
    "buffer_load_dwordx4 v0, s[40:43], s51 offen offset:3072 sc1 lds\n"
    "v_mfma_f32_16x16x32_bf16 v[52:55], a[152:155], v[88:91], v[52:55]\n"
    "v_cvt_pk_bf16_f32 v228, v64, v65\n"
    "v_cvt_pk_bf16_f32 v229, v66, v67\n"
    "v_mfma_f32_16x16x32_bf16 v[60:63], a[232:235], v[88:91], v[60:63]\n"
    "v_cvt_pk_bf16_f32 v230, v68, v69\n"
    "ds_read_b128 v[88:91], v1 offset:1024\n"
    "s_waitcnt lgkmcnt(4)\n"
    "v_mfma_f32_16x16x32_bf16 v[40:43], a[76:79], v[92:95], v[40:43]\n"
    "v_cvt_pk_bf16_f32 v231, v70, v71\n"
    "s_add_u32 m0, s61, 8192\n"
    "s_add_u32 s51, s50, 8192\n"
    "buffer_load_dwordx4 v0, s[40:43], s51 offen offset:0 sc1 lds\n"
    "v_mfma_f32_16x16x32_bf16 v[48:51], a[156:159], v[92:95], v[48:51]\n"
    "v_cvt_pk_bf16_f32 v232, v72, v73\n"
    "v_cvt_pk_bf16_f32 v233, v74, v75\n"
    "v_mfma_f32_16x16x32_bf16 v[56:59], a[236:239], v[92:95], v[56:59]\n"
    "v_mov_b32 v234, v232\n"
    "ds_read_b128 v[92:95], v1 offset:21504\n"
    "s_waitcnt lgkmcnt(4)\n"
    "v_mfma_f32_16x16x32_bf16 v[44:47], a[76:79], v[96:99], v[44:47]\n"
    "v_mov_b32 v235, v233\n"
    "buffer_load_dwordx4 v0, s[40:43], s51 offen offset:1024 sc1 lds\n"
    "s_add_u32 s50, s50, 40960\n"
    "s_add_u32 s61, s61, 40960\n"
    "s_cmp_ge_u32 s61, 122880\n"
    "s_cselect_b32 s51, 122880, 0\n"
    "s_sub_u32 s61, s61, s51\n"
    "v_mfma_f32_16x16x32_bf16 v[52:55], a[156:159], v[96:99], v[52:55]\n"
    "buffer_store_dwordx4 v[228:231], v0, s[40:43], s26 offen sc1\n"
    "v_permlane16_swap_b32 v232, v234\n"
    "v_mfma_f32_16x16x32_bf16 v[60:63], a[236:239], v[96:99], v[60:63]\n"
    "v_permlane16_swap_b32 v233, v235\n"
    "ds_read_b128 v[96:99], v1 offset:2048\n"
    "buffer_store_dwordx4 v[232:235], v12, s[40:43], s27 offen sc1\n"
    "s_add_u32 s47, s47, 1\n"
    "s_and_b32 s59, s47, 63\n"
    "s_cbranch_scc1 hpq_loop_%=\n"
    "s_waitcnt lgkmcnt(0)\n"
    "v_cvt_pk_bf16_f32 v104, v40, v41\n"
    "v_cvt_pk_bf16_f32 v105, v42, v43\n"
    "v_cvt_pk_bf16_f32 v106, v44, v45\n"
    "v_cvt_pk_bf16_f32 v107, v46, v47\n"
    "ds_write2st64_b64 v6, v[104:105], v[106:107] offset0:24 offset1:26\n"
    "v_cvt_pk_bf16_f32 v108, v48, v49\n"
    "v_cvt_pk_bf16_f32 v109, v50, v51\n"
    "v_cvt_pk_bf16_f32 v110, v52, v53\n"
    "v_cvt_pk_bf16_f32 v111, v54, v55\n"
    "ds_write2st64_b64 v2, v[108:109], v[110:111] offset0:24 offset1:26\n"
    "v_cvt_pk_bf16_f32 v112, v56, v57\n"
    "v_cvt_pk_bf16_f32 v113, v58, v59\n"
    "v_cvt_pk_bf16_f32 v114, v60, v61\n"
    "v_cvt_pk_bf16_f32 v115, v62, v63\n"
    "ds_write2st64_b64 v7, v[112:113], v[114:115] offset0:24 offset1:26\n"
    "s_waitcnt lgkmcnt(0)\n"
    "s_barrier\n"
    "s_cmp_gt_u32 s45, 1\n"
    "s_cbranch_scc1 hp_ret_%=\n"
    "s_waitcnt vmcnt(13)\n"
    "s_mov_b32 s29, s52\n"
    "s_waitcnt vmcnt(25)\n"
    "s_add_u32 s48, s63, 1\n"
    "s_cmp_ge_i32 s63, s65\n"
    "s_cselect_b32 s48, s48, 0\n"
    "s_cselect_b32 s49, s30, 0x40000000\n"
    "v_cmp_gt_u32 vcc, s48, v9\n"
    "s_cbranch_vccz hpq_kme0_ok_%=\n"
    "s_waitcnt vmcnt(0)\n"
    "buffer_atomic_add v226, v3, s[40:43], s29 offen\n"
    "s_mov_b32 s29, 0x40000000\n"
    "s_mov_b32 s60, 0\n"
    "v_cmp_gt_u32 vcc, s48, v9\n"
    "s_nop 4\n"
    "s_cbranch_vccz hpq_kme0_ok_%=\n"
    "hpq_kme0_poll_%=:\n"
    "buffer_load_dword v9, v10, s[40:43], s58 offen sc1\n"
    "s_waitcnt vmcnt(0)\n"
    "v_cmp_gt_u32 vcc, s48, v9\n"
    "s_nop 4\n"
    "s_cbranch_vccz hpq_kme0_ok_%=\n"
    "s_sleep 2\n"
    "s_add_u32 s60, s60, 1\n"
    "s_cmp_gt_u32 s60, 65536\n"
    "s_cbranch_scc1 hp_abort_%=\n"
    "s_and_b32 s59, s60, 63\n"
    "s_cbranch_scc1 hpq_kme0_poll_%=\n"
    "v_mov_b32 v4, 1024\n"
    "buffer_load_dword v4, v4, s[40:43], s58 offen sc1\n"
    "s_waitcnt vmcnt(0)\n"
    "v_readfirstlane_b32 s59, v4\n"
    "s_nop 0\n"
    "s_cmp_lg_u32 s59, 0\n"
    "s_nop 2\n"
    "s_cbranch_scc1 hp_end_%=\n"
    "s_branch hpq_kme0_poll_%=\n"
    "hpq_kme0_ok_%=:\n"
    "s_movk_i32 s48, 32\n"
    "s_mov_b32 s60, 0\n"
    "v_cmp_gt_u32 vcc, s48, v236\n"
    "s_nop 4\n"
    "s_cbranch_vccz hpq_kxe0_ok_%=\n"
    "hpq_kxe0_poll_%=:\n"
    "buffer_load_dword v236, v227, s[40:43], s58 offen sc1\n"
    "s_waitcnt vmcnt(0)\n"
    "v_cmp_gt_u32 vcc, s48, v236\n"
    "s_nop 4\n"
    "s_cbranch_vccz hpq_kxe0_ok_%=\n"
    "s_sleep 2\n"
    "s_add_u32 s60, s60, 1\n"
    "s_cmp_gt_u32 s60, 65536\n"
    "s_cbranch_scc1 hp_abort_%=\n"
    "s_and_b32 s59, s60, 63\n"
    "s_cbranch_scc1 hpq_kxe0_poll_%=\n"
    "v_mov_b32 v4, 1024\n"
    "buffer_load_dword v4, v4, s[40:43], s58 offen sc1\n"
    "s_waitcnt vmcnt(0)\n"
    "v_readfirstlane_b32 s59, v4\n"
    "s_nop 0\n"
    "s_cmp_lg_u32 s59, 0\n"
    "s_nop 2\n"
    "s_cbranch_scc1 hp_end_%=\n"
    "s_branch hpq_kxe0_poll_%=\n"
    "hpq_kxe0_ok_%=:\n"
    "v_add_u32 v227, 128, v227\n"
    "s_or_b32 s57, s34, s49\n"
    "s_or_b32 s52, s35, s49\n"
    "s_or_b32 s26, s36, s49\n"
    "s_or_b32 s27, s37, s49\n"
    "s_add_u32 s59, s63, 0\n"
    "s_and_b32 s59, s59, 63\n"
    "s_cmp_eq_u32 s59, 31\n"
    "s_cselect_b32 s59, -31, 1\n"
    "s_mul_i32 s48, s59, 98304\n"
    "s_add_u32 s34, s34, s48\n"
    "s_mul_i32 s48, s59, 65536\n"
    "s_add_u32 s36, s36, s48\n"
    "s_mul_i32 s48, s59, 16384\n"
    "s_add_u32 s37, s37, s48\n"
    "s_add_u32 s35, s35, 1536\n"
    "s_add_u32 s63, s63, 1\n"
    "buffer_load_dwordx4 v[164:167], v11, s[40:43], s57 offen sc1\n"
    "buffer_load_dwordx4 v[168:171], v11, s[40:43], s57 offen offset:512 sc1\n"
    "buffer_load_dwordx4 v[172:175], v11, s[40:43], s57 offen offset:1024 sc1\n"
    "buffer_load_dwordx4 v[176:179], v11, s[40:43], s57 offen offset:1536 sc1\n"
    "buffer_load_dwordx4 v[180:183], v11, s[40:43], s57 offen offset:2048 sc1\n"
    "buffer_load_dwordx4 v[184:187], v11, s[40:43], s57 offen offset:2560 sc1\n"
    "v_swap_b32 v118, v120\n"
    "v_swap_b32 v119, v121\n"
    "v_swap_b32 v126, v128\n"
    "v_swap_b32 v127, v129\n"
    "v_swap_b32 v134, v136\n"
    "v_swap_b32 v135, v137\n"
    "v_swap_b32 v142, v144\n"
    "v_swap_b32 v143, v145\n"
    "v_swap_b32 v150, v152\n"
    "v_swap_b32 v151, v153\n"
    "v_swap_b32 v158, v160\n"
    "v_swap_b32 v159, v161\n"
    "v_mfma_f32_16x16x32_bf16 v[64:67], v[116:119], v[188:191], v[212:215]\n"
    "v_mfma_f32_16x16x32_bf16 v[68:71], v[120:123], v[188:191], v[216:219]\n"
    "s_waitcnt vmcnt(5)\n"
    "v_mfma_f32_16x16x32_bf16 v[72:75], v[164:167], v[188:191], v[220:223]\n"
    "v_mfma_f32_16x16x32_bf16 v[64:67], v[124:127], v[192:195], v[64:67]\n"
    "v_mfma_f32_16x16x32_bf16 v[68:71], v[128:131], v[192:195], v[68:71]\n"
    "s_waitcnt vmcnt(4)\n"
    "v_mfma_f32_16x16x32_bf16 v[72:75], v[168:171], v[192:195], v[72:75]\n"
    "v_mfma_f32_16x16x32_bf16 v[64:67], v[132:135], v[196:199], v[64:67]\n"
    "v_mfma_f32_16x16x32_bf16 v[68:71], v[136:139], v[196:199], v[68:71]\n"
    "s_waitcnt vmcnt(3)\n"
    "v_mfma_f32_16x16x32_bf16 v[72:75], v[172:175], v[196:199], v[72:75]\n"
    "v_mfma_f32_16x16x32_bf16 v[64:67], v[140:143], v[200:203], v[64:67]\n"
    "v_mfma_f32_16x16x32_bf16 v[68:71], v[144:147], v[200:203], v[68:71]\n"
    "s_waitcnt vmcnt(2)\n"
    "v_mfma_f32_16x16x32_bf16 v[72:75], v[176:179], v[200:203], v[72:75]\n"
    "v_mfma_f32_16x16x32_bf16 v[64:67], v[148:151], v[204:207], v[64:67]\n"
    "v_mfma_f32_16x16x32_bf16 v[68:71], v[152:155], v[204:207], v[68:71]\n"
    "s_waitcnt vmcnt(1)\n"
    "v_mfma_f32_16x16x32_bf16 v[72:75], v[180:183], v[204:207], v[72:75]\n"
    "v_mfma_f32_16x16x32_bf16 v[64:67], v[156:159], v[208:211], v[64:67]\n"
    "v_mfma_f32_16x16x32_bf16 v[68:71], v[160:163], v[208:211], v[68:71]\n"
    "s_waitcnt vmcnt(0)\n"
    "v_mfma_f32_16x16x32_bf16 v[72:75], v[184:187], v[208:211], v[72:75]\n"
    "buffer_load_dwordx4 v[116:119], v0, s[40:43], s54 offen sc1\n"
    "buffer_load_dwordx4 v[120:123], v0, s[40:43], s54 offen offset:1024 sc1\n"
    "buffer_load_dwordx4 v[124:127], v0, s[40:43], s54 offen offset:3072 sc1\n"
    "buffer_load_dwordx4 v[128:131], v0, s[40:43], s55 offen sc1\n"
    "buffer_load_dwordx4 v[132:135], v0, s[40:43], s55 offen offset:2048 sc1\n"
    "buffer_load_dwordx4 v[136:139], v0, s[40:43], s55 offen offset:3072 sc1\n"
    "buffer_load_dwordx4 v[140:143], v0, s[40:43], s56 offen offset:1024 sc1\n"
    "buffer_load_dwordx4 v[144:147], v0, s[40:43], s56 offen offset:2048 sc1\n"
    "buffer_load_dwordx4 v[148:151], v0, s[40:43], s54 offen offset:2048 sc1\n"
    "buffer_load_dwordx4 v[152:155], v0, s[40:43], s55 offen offset:1024 sc1\n"
    "buffer_load_dwordx4 v[156:159], v0, s[40:43], s56 offen sc1\n"
    "buffer_load_dwordx4 v[160:163], v0, s[40:43], s56 offen offset:3072 sc1\n"
    "s_add_u32 s59, s63, 0\n"
    "s_and_b32 s59, s59, 63\n"
    "s_cmp_eq_u32 s59, 31\n"
    "s_cselect_b32 s59, -31, 1\n"
    "s_mul_i32 s48, s59, 393216\n"
    "s_add_u32 s54, s54, s48\n"
    "s_add_u32 s55, s55, s48\n"
    "s_add_u32 s56, s56, s48\n"
    "v_max3_f32 v76, v64, v65, v66\n"
    "v_max3_f32 v76, v76, v67, v68\n"
    "v_max3_f32 v76, v76, v69, v70\n"
    "v_max3_f32 v76, v76, v71, v72\n"
    "v_max3_f32 v76, v76, v73, v74\n"
    "v_max_f32 v76, v76, v75\n"
    "v_mov_b32 v77, v76\n"
    "s_nop 1\n"
    "v_permlane16_swap_b32 v76, v77\n"
    "v_max_f32 v76, v76, v77\n"
    "v_mov_b32 v77, v76\n"
    "s_nop 1\n"
    "v_permlane32_swap_b32 v76, v77\n"
    "v_max_f32 v76, v76, v77\n"
    "v_mul_f32 v76, 0xbdd53b95, v76\n"
    "v_fmamk_f32 v64, v64, 0x3dd53b95, v76\n"
    "v_fmamk_f32 v65, v65, 0x3dd53b95, v76\n"
    "v_fmamk_f32 v66, v66, 0x3dd53b95, v76\n"
    "v_fmamk_f32 v67, v67, 0x3dd53b95, v76\n"
    "v_fmamk_f32 v68, v68, 0x3dd53b95, v76\n"
    "v_fmamk_f32 v69, v69, 0x3dd53b95, v76\n"
    "v_fmamk_f32 v70, v70, 0x3dd53b95, v76\n"
    "v_fmamk_f32 v71, v71, 0x3dd53b95, v76\n"
    "v_fmamk_f32 v72, v72, 0x3dd53b95, v76\n"
    "v_fmamk_f32 v73, v73, 0x3dd53b95, v76\n"
    "v_fmamk_f32 v74, v74, 0x3dd53b95, v76\n"
    "v_fmamk_f32 v75, v75, 0x3dd53b95, v76\n"
    "v_exp_f32 v64, v64\n"
    "v_exp_f32 v65, v65\n"
    "v_exp_f32 v66, v66\n"
    "v_exp_f32 v67, v67\n"
    "v_exp_f32 v68, v68\n"
    "v_exp_f32 v69, v69\n"
    "v_exp_f32 v70, v70\n"
    "v_exp_f32 v71, v71\n"
    "v_exp_f32 v72, v72\n"
    "v_exp_f32 v73, v73\n"
    "v_exp_f32 v74, v74\n"
    "v_exp_f32 v75, v75\n"
    "v_add_f32 v76, v64, v65\n"
    "v_add_f32 v76, v76, v66\n"
    "v_add_f32 v76, v76, v67\n"
    "v_add_f32 v76, v76, v68\n"
    "v_add_f32 v76, v76, v69\n"
    "v_add_f32 v76, v76, v70\n"
    "v_add_f32 v76, v76, v71\n"
    "v_add_f32 v76, v76, v72\n"
    "v_add_f32 v76, v76, v73\n"
    "v_add_f32 v76, v76, v74\n"
    "v_add_f32 v76, v76, v75\n"
    "v_mov_b32 v77, v76\n"
    "s_nop 1\n"
    "v_permlane16_swap_b32 v76, v77\n"
    "v_add_f32 v76, v76, v77\n"
    "v_mov_b32 v77, v76\n"
    "s_nop 1\n"
    "v_permlane32_swap_b32 v76, v77\n"
    "v_add_f32 v76, v76, v77\n"
    "v_rcp_f32 v76, v76\n"
    "s_nop 0\n"
    "v_mul_f32 v64, v64, v76\n"
    "v_mul_f32 v65, v65, v76\n"
    "v_mul_f32 v66, v66, v76\n"
    "v_mul_f32 v67, v67, v76\n"
    "v_mul_f32 v68, v68, v76\n"
    "v_mul_f32 v69, v69, v76\n"
    "v_mul_f32 v70, v70, v76\n"
    "v_mul_f32 v71, v71, v76\n"
    "v_mul_f32 v72, v72, v76\n"
    "v_mul_f32 v73, v73, v76\n"
    "v_mul_f32 v74, v74, v76\n"
    "v_mul_f32 v75, v75, v76\n"
    "v_cvt_pk_bf16_f32 v228, v64, v65\n"
    "v_cvt_pk_bf16_f32 v229, v66, v67\n"
    "v_cvt_pk_bf16_f32 v230, v68, v69\n"
    "v_cvt_pk_bf16_f32 v231, v70, v71\n"
    "v_cvt_pk_bf16_f32 v232, v72, v73\n"
    "v_cvt_pk_bf16_f32 v233, v74, v75\n"
    "v_mov_b32 v234, v232\n"
    "v_mov_b32 v235, v233\n"
    "buffer_store_dwordx4 v[228:231], v0, s[40:43], s26 offen sc1\n"
    "v_permlane16_swap_b32 v232, v234\n"
    "v_permlane16_swap_b32 v233, v235\n"
    "buffer_store_dwordx4 v[232:235], v12, s[40:43], s27 offen sc1\n"
    "s_waitcnt vmcnt(2)\n"
    "buffer_atomic_add v226, v3, s[40:43], s29 offen\n"
    "s_mov_b32 s29, s52\n"
    "s_waitcnt vmcnt(25)\n"
    "s_add_u32 s48, s63, 1\n"
    "s_cmp_ge_i32 s63, s65\n"
    "s_cselect_b32 s48, s48, 0\n"
    "s_cselect_b32 s49, s30, 0x40000000\n"
    "v_cmp_gt_u32 vcc, s48, v9\n"
    "s_cbranch_vccz hpq_kme1_ok_%=\n"
    "s_waitcnt vmcnt(0)\n"
    "buffer_atomic_add v226, v3, s[40:43], s29 offen\n"
    "s_mov_b32 s29, 0x40000000\n"
    "s_mov_b32 s60, 0\n"
    "v_cmp_gt_u32 vcc, s48, v9\n"
    "s_nop 4\n"
    "s_cbranch_vccz hpq_kme1_ok_%=\n"
    "hpq_kme1_poll_%=:\n"
    "buffer_load_dword v9, v10, s[40:43], s58 offen sc1\n"
    "s_waitcnt vmcnt(0)\n"
    "v_cmp_gt_u32 vcc, s48, v9\n"
    "s_nop 4\n"
    "s_cbranch_vccz hpq_kme1_ok_%=\n"
    "s_sleep 2\n"
    "s_add_u32 s60, s60, 1\n"
    "s_cmp_gt_u32 s60, 65536\n"
    "s_cbranch_scc1 hp_abort_%=\n"
    "s_and_b32 s59, s60, 63\n"
    "s_cbranch_scc1 hpq_kme1_poll_%=\n"
    "v_mov_b32 v4, 1024\n"
    "buffer_load_dword v4, v4, s[40:43], s58 offen sc1\n"
    "s_waitcnt vmcnt(0)\n"
    "v_readfirstlane_b32 s59, v4\n"
    "s_nop 0\n"
    "s_cmp_lg_u32 s59, 0\n"
    "s_nop 2\n"
    "s_cbranch_scc1 hp_end_%=\n"
    "s_branch hpq_kme1_poll_%=\n"
    "hpq_kme1_ok_%=:\n"
    "s_or_b32 s57, s34, s49\n"
    "s_or_b32 s52, s35, s49\n"
    "s_or_b32 s26, s36, s49\n"
    "s_or_b32 s27, s37, s49\n"
    "s_add_u32 s59, s63, 0\n"
    "s_and_b32 s59, s59, 63\n"
    "s_cmp_eq_u32 s59, 31\n"
    "s_cselect_b32 s59, -31, 1\n"
    "s_mul_i32 s48, s59, 98304\n"
    "s_add_u32 s34, s34, s48\n"
    "s_mul_i32 s48, s59, 65536\n"
    "s_add_u32 s36, s36, s48\n"
    "s_mul_i32 s48, s59, 16384\n"
    "s_add_u32 s37, s37, s48\n"
    "s_add_u32 s35, s35, 1536\n"
    "s_add_u32 s63, s63, 1\n"
    "buffer_load_dwordx4 v[164:167], v11, s[40:43], s57 offen sc1\n"
    "buffer_load_dwordx4 v[168:171], v11, s[40:43], s57 offen offset:512 sc1\n"
    "buffer_load_dwordx4 v[172:175], v11, s[40:43], s57 offen offset:1024 sc1\n"
    "buffer_load_dwordx4 v[176:179], v11, s[40:43], s57 offen offset:1536 sc1\n"
    "buffer_load_dwordx4 v[180:183], v11, s[40:43], s57 offen offset:2048 sc1\n"
    "buffer_load_dwordx4 v[184:187], v11, s[40:43], s57 offen offset:2560 sc1\n"
    "ds_read_b128 v[188:191], v8 offset:12288\n"
    "ds_read_b128 v[192:195], v8 offset:14336\n"
    "ds_read_b128 v[196:199], v8 offset:16384\n"
    "ds_read_b128 v[200:203], v8 offset:18432\n"
    "ds_read_b128 v[204:207], v8 offset:20480\n"
    "ds_read_b128 v[208:211], v8 offset:22528\n"
    "v_swap_b32 v118, v120\n"
    "v_swap_b32 v119, v121\n"
    "v_swap_b32 v126, v128\n"
    "v_swap_b32 v127, v129\n"
    "v_swap_b32 v134, v136\n"
    "v_swap_b32 v135, v137\n"
    "v_swap_b32 v142, v144\n"
    "v_swap_b32 v143, v145\n"
    "v_swap_b32 v150, v152\n"
    "v_swap_b32 v151, v153\n"
    "v_swap_b32 v158, v160\n"
    "v_swap_b32 v159, v161\n"
    "s_waitcnt lgkmcnt(5)\n"
    "v_mfma_f32_16x16x32_bf16 v[64:67], v[116:119], v[188:191], v[212:215]\n"
    "v_mfma_f32_16x16x32_bf16 v[68:71], v[120:123], v[188:191], v[216:219]\n"
    "s_waitcnt vmcnt(5)\n"
    "v_mfma_f32_16x16x32_bf16 v[72:75], v[164:167], v[188:191], v[220:223]\n"
    "s_waitcnt lgkmcnt(4)\n"
    "v_mfma_f32_16x16x32_bf16 v[64:67], v[124:127], v[192:195], v[64:67]\n"
    "v_mfma_f32_16x16x32_bf16 v[68:71], v[128:131], v[192:195], v[68:71]\n"
    "s_waitcnt vmcnt(4)\n"
    "v_mfma_f32_16x16x32_bf16 v[72:75], v[168:171], v[192:195], v[72:75]\n"
    "s_waitcnt lgkmcnt(3)\n"
    "v_mfma_f32_16x16x32_bf16 v[64:67], v[132:135], v[196:199], v[64:67]\n"
    "v_mfma_f32_16x16x32_bf16 v[68:71], v[136:139], v[196:199], v[68:71]\n"
    "s_waitcnt vmcnt(3)\n"
    "v_mfma_f32_16x16x32_bf16 v[72:75], v[172:175], v[196:199], v[72:75]\n"
    "s_waitcnt lgkmcnt(2)\n"
    "v_mfma_f32_16x16x32_bf16 v[64:67], v[140:143], v[200:203], v[64:67]\n"
    "v_mfma_f32_16x16x32_bf16 v[68:71], v[144:147], v[200:203], v[68:71]\n"
    "s_waitcnt vmcnt(2)\n"
    "v_mfma_f32_16x16x32_bf16 v[72:75], v[176:179], v[200:203], v[72:75]\n"
    "s_waitcnt lgkmcnt(1)\n"
    "v_mfma_f32_16x16x32_bf16 v[64:67], v[148:151], v[204:207], v[64:67]\n"
    "v_mfma_f32_16x16x32_bf16 v[68:71], v[152:155], v[204:207], v[68:71]\n"
    "s_waitcnt vmcnt(1)\n"
    "v_mfma_f32_16x16x32_bf16 v[72:75], v[180:183], v[204:207], v[72:75]\n"
    "s_waitcnt lgkmcnt(0)\n"
    "v_mfma_f32_16x16x32_bf16 v[64:67], v[156:159], v[208:211], v[64:67]\n"
    "v_mfma_f32_16x16x32_bf16 v[68:71], v[160:163], v[208:211], v[68:71]\n"
    "s_waitcnt vmcnt(0)\n"
    "v_mfma_f32_16x16x32_bf16 v[72:75], v[184:187], v[208:211], v[72:75]\n"
    "s_nop 4\n"
    "v_max3_f32 v76, v64, v65, v66\n"
    "v_max3_f32 v76, v76, v67, v68\n"
    "v_max3_f32 v76, v76, v69, v70\n"
    "v_max3_f32 v76, v76, v71, v72\n"
    "v_max3_f32 v76, v76, v73, v74\n"
    "v_max_f32 v76, v76, v75\n"
    "v_mov_b32 v77, v76\n"
    "s_nop 1\n"
    "v_permlane16_swap_b32 v76, v77\n"
    "v_max_f32 v76, v76, v77\n"
    "v_mov_b32 v77, v76\n"
    "s_nop 1\n"
    "v_permlane32_swap_b32 v76, v77\n"
    "v_max_f32 v76, v76, v77\n"
    "v_mul_f32 v76, 0xbdd53b95, v76\n"
    "v_fmamk_f32 v64, v64, 0x3dd53b95, v76\n"
    "v_fmamk_f32 v65, v65, 0x3dd53b95, v76\n"
    "v_fmamk_f32 v66, v66, 0x3dd53b95, v76\n"
    "v_fmamk_f32 v67, v67, 0x3dd53b95, v76\n"
    "v_fmamk_f32 v68, v68, 0x3dd53b95, v76\n"
    "v_fmamk_f32 v69, v69, 0x3dd53b95, v76\n"
    "v_fmamk_f32 v70, v70, 0x3dd53b95, v76\n"
    "v_fmamk_f32 v71, v71, 0x3dd53b95, v76\n"
    "v_fmamk_f32 v72, v72, 0x3dd53b95, v76\n"
    "v_fmamk_f32 v73, v73, 0x3dd53b95, v76\n"
    "v_fmamk_f32 v74, v74, 0x3dd53b95, v76\n"
    "v_fmamk_f32 v75, v75, 0x3dd53b95, v76\n"
    "v_exp_f32 v64, v64\n"
    "v_exp_f32 v65, v65\n"
    "v_exp_f32 v66, v66\n"
    "v_exp_f32 v67, v67\n"
    "v_exp_f32 v68, v68\n"
    "v_exp_f32 v69, v69\n"
    "v_exp_f32 v70, v70\n"
    "v_exp_f32 v71, v71\n"
    "v_exp_f32 v72, v72\n"
    "v_exp_f32 v73, v73\n"
    "v_exp_f32 v74, v74\n"
    "v_exp_f32 v75, v75\n"
    "v_add_f32 v76, v64, v65\n"
    "v_add_f32 v76, v76, v66\n"
    "v_add_f32 v76, v76, v67\n"
    "v_add_f32 v76, v76, v68\n"
    "v_add_f32 v76, v76, v69\n"
    "v_add_f32 v76, v76, v70\n"
    "v_add_f32 v76, v76, v71\n"
    "v_add_f32 v76, v76, v72\n"
    "v_add_f32 v76, v76, v73\n"
    "v_add_f32 v76, v76, v74\n"
    "v_add_f32 v76, v76, v75\n"
    "v_mov_b32 v77, v76\n"
    "s_nop 1\n"
    "v_permlane16_swap_b32 v76, v77\n"
    "v_add_f32 v76, v76, v77\n"
    "v_mov_b32 v77, v76\n"
    "s_nop 1\n"
    "v_permlane32_swap_b32 v76, v77\n"
    "v_add_f32 v76, v76, v77\n"
    "v_rcp_f32 v76, v76\n"
    "s_nop 0\n"
    "v_mul_f32 v64, v64, v76\n"
    "v_mul_f32 v65, v65, v76\n"
    "v_mul_f32 v66, v66, v76\n"
    "v_mul_f32 v67, v67, v76\n"
    "v_mul_f32 v68, v68, v76\n"
    "v_mul_f32 v69, v69, v76\n"
    "v_mul_f32 v70, v70, v76\n"
    "v_mul_f32 v71, v71, v76\n"
    "v_mul_f32 v72, v72, v76\n"
    "v_mul_f32 v73, v73, v76\n"
    "v_mul_f32 v74, v74, v76\n"
    "v_mul_f32 v75, v75, v76\n"
    "v_cvt_pk_bf16_f32 v228, v64, v65\n"
    "v_cvt_pk_bf16_f32 v229, v66, v67\n"
    "v_cvt_pk_bf16_f32 v230, v68, v69\n"
    "v_cvt_pk_bf16_f32 v231, v70, v71\n"
    "v_cvt_pk_bf16_f32 v232, v72, v73\n"
    "v_cvt_pk_bf16_f32 v233, v74, v75\n"
    "v_mov_b32 v234, v232\n"
    "v_mov_b32 v235, v233\n"
    "buffer_store_dwordx4 v[228:231], v0, s[40:43], s26 offen sc1\n"
    "v_permlane16_swap_b32 v232, v234\n"
    "v_permlane16_swap_b32 v233, v235\n"
    "buffer_store_dwordx4 v[232:235], v12, s[40:43], s27 offen sc1\n"
    "s_waitcnt vmcnt(2)\n"
    "buffer_atomic_add v226, v3, s[40:43], s29 offen\n"
    "s_waitcnt vmcnt(0)\n"
    "buffer_atomic_add v226, v3, s[40:43], s52 offen\n"
    "s_nop 1\n"
    "s_branch hp_ret_%=\n"
    "hp_roleo_%=:\n"
    "v_and_b32 v1, 0x370, v0\n"
    "v_bfe_u32 v4, v15, 3, 1\n"
    "v_lshl_add_u32 v1, v4, 15, v1\n"
    "s_lshr_b32 s48, s44, 1\n"
    "s_lshl_b32 s48, s48, 10\n"
    "s_and_b32 s49, s48, 0x2000\n"
    "s_add_u32 s48, s48, s49\n"
    "s_and_b32 s49, s44, 1\n"
    "s_lshl_b32 s49, s49, 7\n"
    "s_add_u32 s48, s48, s49\n"
    "v_add_u32 v1, s48, v1\n"
    "s_mul_i32 s62, s45, 655360\n"
    "s_add_u32 s62, s62, 32964608\n"
    "s_mov_b32 s63, s62\n"
    "s_lshr_b32 s48, s47, 1\n"
    "s_lshl_b32 s48, s48, 7\n"
    "s_add_u32 s48, s48, 254976\n"
    "v_mov_b32 v13, s48\n"
    "v_mov_b32 v14, 0\n"
    "s_mul_i32 s48, s45, 3\n"
    "s_add_u32 s48, s48, 0\n"
    "s_and_b32 s49, s48, 1\n"
    "s_lshr_b32 s48, s48, 1\n"
    "s_lshl_b32 s48, s48, 11\n"
    "s_lshl3_add_u32 s48, s49, s48\n"
    "v_add_u32 v8, s48, v0\n"
    "s_mul_i32 s48, s45, 3\n"
    "s_add_u32 s48, s48, 1\n"
    "s_and_b32 s49, s48, 1\n"
    "s_lshr_b32 s48, s48, 1\n"
    "s_lshl_b32 s48, s48, 11\n"
    "s_lshl3_add_u32 s48, s49, s48\n"
    "v_add_u32 v9, s48, v0\n"
    "s_mul_i32 s48, s45, 3\n"
    "s_add_u32 s48, s48, 2\n"
    "s_and_b32 s49, s48, 1\n"
    "s_lshr_b32 s48, s48, 1\n"
    "s_lshl_b32 s48, s48, 11\n"
    "s_lshl3_add_u32 s48, s49, s48\n"
    "v_add_u32 v10, s48, v0\n"
    "s_mul_i32 s49, s45, 3072\n"
    "s_mul_i32 s50, s44, 12288\n"
    "s_add_u32 s50, s50, 20348928\n"
    "s_add_u32 s50, s50, s49\n"
    "s_lshl_b32 s48, s44, 7\n"
    "s_add_u32 s49, s48, 2048\n"
    "v_mov_b32 v7, s49\n"
    "v_mov_b32 v6, 0\n"
    "s_cmp_eq_u32 s45, 0\n"
    "s_cselect_b32 s59, 1, 0\n"
    "v_lshlrev_b32 v2, 7, v15\n"
    "v_cmp_gt_u32 vcc, s59, v15\n"
    "v_mov_b32 v4, 2147483632\n"
    "s_nop 0\n"
    "v_cndmask_b32 v2, v4, v2, vcc\n"
    "v_mov_b32 v12, 1\n"
    "s_add_u32 s61, s47, 64\n"
    "s_waitcnt vmcnt(0)\n"
    "s_add_u32 s55, s47, 1\n"
    "s_mov_b32 s64, 0x40000000\n"
    "s_mov_b32 s60, 0\n"
    "v_cmp_gt_u32 vcc, s55, v6\n"
    "s_nop 4\n"
    "s_cbranch_vccz hpo_v0_ok_%=\n"
    "hpo_v0_poll_%=:\n"
    "buffer_load_dword v6, v7, s[40:43], s58 offen sc1\n"
    "s_waitcnt vmcnt(0)\n"
    "v_cmp_gt_u32 vcc, s55, v6\n"
    "s_nop 4\n"
    "s_cbranch_vccz hpo_v0_ok_%=\n"
    "s_sleep 2\n"
    "s_add_u32 s60, s60, 1\n"
    "s_cmp_gt_u32 s60, 65536\n"
    "s_cbranch_scc1 hp_abort_%=\n"
    "s_and_b32 s59, s60, 63\n"
    "s_cbranch_scc1 hpo_v0_poll_%=\n"
    "v_mov_b32 v4, 1024\n"
    "buffer_load_dword v4, v4, s[40:43], s58 offen sc1\n"
    "s_waitcnt vmcnt(0)\n"
    "v_readfirstlane_b32 s59, v4\n"
    "s_nop 0\n"
    "s_cmp_lg_u32 s59, 0\n"
    "s_nop 2\n"
    "s_cbranch_scc1 hp_end_%=\n"
    "s_branch hpo_v0_poll_%=\n"
    "hpo_v0_ok_%=:\n"
    "buffer_load_dwordx4 v[144:147], v0, s[40:43], s50 offen offset:0 sc1\n"
    "buffer_load_dwordx4 v[148:151], v0, s[40:43], s50 offen offset:1024 sc1\n"
    "buffer_load_dwordx4 v[152:155], v0, s[40:43], s50 offen offset:2048 sc1\n"
    "s_waitcnt vmcnt(2)\n"
    "ds_write2st64_b64 v8, v[144:145], v[146:147] offset0:0 offset1:2\n"
    "s_waitcnt vmcnt(1)\n"
    "ds_write2st64_b64 v9, v[148:149], v[150:151] offset0:0 offset1:2\n"
    "s_waitcnt vmcnt(0)\n"
    "ds_write2st64_b64 v10, v[152:153], v[154:155] offset0:0 offset1:2\n"
    "s_waitcnt lgkmcnt(0)\n"
    "s_barrier\n"
    "ds_read_b128 v[16:19], v0 offset:0\n"
    "ds_read_b128 v[20:23], v0 offset:1024\n"
    "ds_read_b128 v[24:27], v0 offset:2048\n"
    "ds_read_b128 v[28:31], v0 offset:3072\n"
    "ds_read_b128 v[32:35], v0 offset:4096\n"
    "ds_read_b128 v[36:39], v0 offset:5120\n"
    "ds_read_b128 v[40:43], v0 offset:6144\n"
    "ds_read_b128 v[44:47], v0 offset:7168\n"
    "ds_read_b128 v[48:51], v0 offset:8192\n"
    "ds_read_b128 v[52:55], v0 offset:9216\n"
    "ds_read_b128 v[56:59], v0 offset:10240\n"
    "ds_read_b128 v[60:63], v0 offset:11264\n"
    "s_waitcnt lgkmcnt(0)\n"
    "s_mov_b32 s53, 0x40000000\n"
    "hpo_loop_%=:\n"
    "s_bitcmp1_b32 s47, 5\n"
    "s_cselect_b32 s48, 8, 0\n"
    "s_mov_b32 s60, 0\n"
    "v_cmp_gt_u32 vcc, s48, v14\n"
    "s_nop 4\n"
    "s_cbranch_vccz hpo_free_ok_%=\n"
    "hpo_free_poll_%=:\n"
    "buffer_load_dword v14, v13, s[40:43], s58 offen sc1\n"
    "s_waitcnt vmcnt(0)\n"
    "v_cmp_gt_u32 vcc, s48, v14\n"
    "s_nop 4\n"
    "s_cbranch_vccz hpo_free_ok_%=\n"
    "s_sleep 8\n"
    "s_add_u32 s60, s60, 1\n"
    "s_cmp_gt_u32 s60, 65536\n"
    "s_cbranch_scc1 hp_abort_%=\n"
    "s_and_b32 s59, s60, 63\n"
    "s_cbranch_scc1 hpo_free_poll_%=\n"
    "v_mov_b32 v4, 1024\n"
    "buffer_load_dword v4, v4, s[40:43], s58 offen sc1\n"
    "s_waitcnt vmcnt(0)\n"
    "v_readfirstlane_b32 s59, v4\n"
    "s_nop 0\n"
    "s_cmp_lg_u32 s59, 0\n"
    "s_nop 2\n"
    "s_cbranch_scc1 hp_end_%=\n"
    "s_branch hpo_free_poll_%=\n"
    "hpo_free_ok_%=:\n"
    "v_add_u32 v13, 128, v13\n"
    "s_cmp_lg_u32 s47, 12\n"
    "s_cbranch_scc1 hpo_gate_ok_%=\n"
    "v_add_u32 v3, 4096, v7\n"
    "v_mov_b32 v11, 0\n"
    "s_mov_b32 s48, 2\n"
    "s_mov_b32 s60, 0\n"
    "v_cmp_gt_u32 vcc, s48, v11\n"
    "s_nop 4\n"
    "s_cbranch_vccz hpo_gate_ok_%=\n"
    "hpo_gate_poll_%=:\n"
    "buffer_load_dword v11, v3, s[40:43], s58 offen sc1\n"
    "s_waitcnt vmcnt(0)\n"
    "v_cmp_gt_u32 vcc, s48, v11\n"
    "s_nop 4\n"
    "s_cbranch_vccz hpo_gate_ok_%=\n"
    "s_sleep 8\n"
    "s_add_u32 s60, s60, 1\n"
    "s_cmp_gt_u32 s60, 65536\n"
    "s_cbranch_scc1 hp_abort_%=\n"
    "s_and_b32 s59, s60, 63\n"
    "s_cbranch_scc1 hpo_gate_poll_%=\n"
    "v_mov_b32 v4, 1024\n"
    "buffer_load_dword v4, v4, s[40:43], s58 offen sc1\n"
    "s_waitcnt vmcnt(0)\n"
    "v_readfirstlane_b32 s59, v4\n"
    "s_nop 0\n"
    "s_cmp_lg_u32 s59, 0\n"
    "s_nop 2\n"
    "s_cbranch_scc1 hp_end_%=\n"
    "s_branch hpo_gate_poll_%=\n"
    "hpo_gate_ok_%=:\n"
    "s_add_u32 s55, s47, 2\n"
    "s_min_u32 s55, s55, s61\n"
    "s_add_u32 s59, s47, 0\n"
    "s_and_b32 s59, s59, 63\n"
    "s_cmp_eq_u32 s59, 31\n"
    "s_cselect_b32 s59, -31, 1\n"
    "s_mul_i32 s57, s59, 393216\n"
    "s_add_u32 s50, s50, s57\n"
    "v_cmp_gt_u32 vcc, s55, v6\n"
    "s_nop 0\n"
    "s_cmp_eq_u64 vcc, 0\n"
    "s_cselect_b32 s56, s50, 0x40000000\n"
    "v_mfma_f32_16x16x32_bf16 v[112:115], v[16:19], a[0:3], 0\n"
    "buffer_load_dwordx4 v[144:147], v0, s[40:43], s56 offen offset:0 sc1\n"
    "v_mfma_f32_16x16x32_bf16 v[116:119], v[20:23], a[0:3], 0\n"
    "buffer_load_dwordx4 v[148:151], v0, s[40:43], s56 offen offset:1024 sc1\n"
    "v_mfma_f32_16x16x32_bf16 v[112:115], v[24:27], a[4:7], v[112:115]\n"
    "buffer_load_dwordx4 v[152:155], v0, s[40:43], s56 offen offset:2048 sc1\n"
    "v_mfma_f32_16x16x32_bf16 v[116:119], v[28:31], a[4:7], v[116:119]\n"
    "buffer_load_dword v6, v7, s[40:43], s58 offen sc1\n"
    "v_mfma_f32_16x16x32_bf16 v[112:115], v[32:35], a[8:11], v[112:115]\n"
    "v_mfma_f32_16x16x32_bf16 v[116:119], v[36:39], a[8:11], v[116:119]\n"
    "v_mfma_f32_16x16x32_bf16 v[112:115], v[40:43], a[12:15], v[112:115]\n"
    "v_cvt_pk_bf16_f32 v140, v120, v121\n"
    "v_mfma_f32_16x16x32_bf16 v[116:119], v[44:47], a[12:15], v[116:119]\n"
    "v_cvt_pk_bf16_f32 v141, v122, v123\n"
    "v_mfma_f32_16x16x32_bf16 v[112:115], v[48:51], a[16:19], v[112:115]\n"
    "v_cvt_pk_bf16_f32 v142, v124, v125\n"
    "v_mfma_f32_16x16x32_bf16 v[116:119], v[52:55], a[16:19], v[116:119]\n"
    "v_cvt_pk_bf16_f32 v143, v126, v127\n"
    "v_mfma_f32_16x16x32_bf16 v[112:115], v[56:59], a[20:23], v[112:115]\n"
    "buffer_store_dwordx4 v[140:143], v1, s[40:43], s53 offen sc1\n"
    "v_mfma_f32_16x16x32_bf16 v[116:119], v[60:63], a[20:23], v[116:119]\n"
    "s_mov_b32 s52, s63\n"
    "s_lshr_b32 s59, s52, 8\n"
    "s_and_b32 s59, s59, 0x2000\n"
    "s_add_u32 s53, s52, s59\n"
    "v_mfma_f32_16x16x32_bf16 v[120:123], v[16:19], a[24:27], 0\n"
    "v_mfma_f32_16x16x32_bf16 v[124:127], v[20:23], a[24:27], 0\n"
    "v_mfma_f32_16x16x32_bf16 v[120:123], v[24:27], a[28:31], v[120:123]\n"
    "v_mfma_f32_16x16x32_bf16 v[124:127], v[28:31], a[28:31], v[124:127]\n"
    "v_mfma_f32_16x16x32_bf16 v[120:123], v[32:35], a[32:35], v[120:123]\n"
    "v_mfma_f32_16x16x32_bf16 v[124:127], v[36:39], a[32:35], v[124:127]\n"
    "v_mfma_f32_16x16x32_bf16 v[120:123], v[40:43], a[36:39], v[120:123]\n"
    "v_cvt_pk_bf16_f32 v136, v112, v113\n"
    "v_mfma_f32_16x16x32_bf16 v[124:127], v[44:47], a[36:39], v[124:127]\n"
    "v_cvt_pk_bf16_f32 v137, v114, v115\n"
    "v_mfma_f32_16x16x32_bf16 v[120:123], v[48:51], a[40:43], v[120:123]\n"
    "v_cvt_pk_bf16_f32 v138, v116, v117\n"
    "v_mfma_f32_16x16x32_bf16 v[124:127], v[52:55], a[40:43], v[124:127]\n"
    "v_cvt_pk_bf16_f32 v139, v118, v119\n"
    "v_mfma_f32_16x16x32_bf16 v[120:123], v[56:59], a[44:47], v[120:123]\n"
    "buffer_store_dwordx4 v[136:139], v1, s[40:43], s53 offen sc1\n"
    "v_mfma_f32_16x16x32_bf16 v[124:127], v[60:63], a[44:47], v[124:127]\n"
    "s_add_u32 s52, s52, 65536\n"
    "s_lshr_b32 s59, s52, 8\n"
    "s_and_b32 s59, s59, 0x2000\n"
    "s_add_u32 s53, s52, s59\n"
    "v_mfma_f32_16x16x32_bf16 v[112:115], v[16:19], a[48:51], 0\n"
    "v_mfma_f32_16x16x32_bf16 v[116:119], v[20:23], a[48:51], 0\n"
    "v_mfma_f32_16x16x32_bf16 v[112:115], v[24:27], a[52:55], v[112:115]\n"
    "v_mfma_f32_16x16x32_bf16 v[116:119], v[28:31], a[52:55], v[116:119]\n"
    "v_mfma_f32_16x16x32_bf16 v[112:115], v[32:35], a[56:59], v[112:115]\n"
    "v_mfma_f32_16x16x32_bf16 v[116:119], v[36:39], a[56:59], v[116:119]\n"
    "v_mfma_f32_16x16x32_bf16 v[112:115], v[40:43], a[60:63], v[112:115]\n"
    "v_cvt_pk_bf16_f32 v140, v120, v121\n"
    "v_mfma_f32_16x16x32_bf16 v[116:119], v[44:47], a[60:63], v[116:119]\n"
    "v_cvt_pk_bf16_f32 v141, v122, v123\n"
    "v_mfma_f32_16x16x32_bf16 v[112:115], v[48:51], a[64:67], v[112:115]\n"
    "v_cvt_pk_bf16_f32 v142, v124, v125\n"
    "v_mfma_f32_16x16x32_bf16 v[116:119], v[52:55], a[64:67], v[116:119]\n"
    "v_cvt_pk_bf16_f32 v143, v126, v127\n"
    "v_mfma_f32_16x16x32_bf16 v[112:115], v[56:59], a[68:71], v[112:115]\n"
    "buffer_store_dwordx4 v[140:143], v1, s[40:43], s53 offen sc1\n"
    "v_mfma_f32_16x16x32_bf16 v[116:119], v[60:63], a[68:71], v[116:119]\n"
    "s_add_u32 s52, s52, 65536\n"
    "s_lshr_b32 s59, s52, 8\n"
    "s_and_b32 s59, s59, 0x2000\n"
    "s_add_u32 s53, s52, s59\n"
    "v_mfma_f32_16x16x32_bf16 v[120:123], v[16:19], a[72:75], 0\n"
    "v_mfma_f32_16x16x32_bf16 v[124:127], v[20:23], a[72:75], 0\n"
    "v_mfma_f32_16x16x32_bf16 v[120:123], v[24:27], a[76:79], v[120:123]\n"
    "v_mfma_f32_16x16x32_bf16 v[124:127], v[28:31], a[76:79], v[124:127]\n"
    "v_mfma_f32_16x16x32_bf16 v[120:123], v[32:35], a[80:83], v[120:123]\n"
    "v_mfma_f32_16x16x32_bf16 v[124:127], v[36:39], a[80:83], v[124:127]\n"
    "v_mfma_f32_16x16x32_bf16 v[120:123], v[40:43], a[84:87], v[120:123]\n"
    "v_cvt_pk_bf16_f32 v136, v112, v113\n"
    "v_mfma_f32_16x16x32_bf16 v[124:127], v[44:47], a[84:87], v[124:127]\n"
    "v_cvt_pk_bf16_f32 v137, v114, v115\n"
    "v_mfma_f32_16x16x32_bf16 v[120:123], v[48:51], a[88:91], v[120:123]\n"
    "v_cvt_pk_bf16_f32 v138, v116, v117\n"
    "v_mfma_f32_16x16x32_bf16 v[124:127], v[52:55], a[88:91], v[124:127]\n"
    "v_cvt_pk_bf16_f32 v139, v118, v119\n"
    "v_mfma_f32_16x16x32_bf16 v[120:123], v[56:59], a[92:95], v[120:123]\n"
    "buffer_store_dwordx4 v[136:139], v1, s[40:43], s53 offen sc1\n"
    "v_mfma_f32_16x16x32_bf16 v[124:127], v[60:63], a[92:95], v[124:127]\n"
    "s_add_u32 s52, s52, 65536\n"
    "s_lshr_b32 s59, s52, 8\n"
    "s_and_b32 s59, s59, 0x2000\n"
    "s_add_u32 s53, s52, s59\n"
    "v_mfma_f32_16x16x32_bf16 v[112:115], v[16:19], a[96:99], 0\n"
    "v_mfma_f32_16x16x32_bf16 v[116:119], v[20:23], a[96:99], 0\n"
    "v_mfma_f32_16x16x32_bf16 v[112:115], v[24:27], a[100:103], v[112:115]\n"
    "v_mfma_f32_16x16x32_bf16 v[116:119], v[28:31], a[100:103], v[116:119]\n"
    "v_mfma_f32_16x16x32_bf16 v[112:115], v[32:35], a[104:107], v[112:115]\n"
    "v_mfma_f32_16x16x32_bf16 v[116:119], v[36:39], a[104:107], v[116:119]\n"
    "v_mfma_f32_16x16x32_bf16 v[112:115], v[40:43], a[108:111], v[112:115]\n"
    "v_cvt_pk_bf16_f32 v140, v120, v121\n"
    "v_mfma_f32_16x16x32_bf16 v[116:119], v[44:47], a[108:111], v[116:119]\n"
    "v_cvt_pk_bf16_f32 v141, v122, v123\n"
    "v_mfma_f32_16x16x32_bf16 v[112:115], v[48:51], a[112:115], v[112:115]\n"
    "v_cvt_pk_bf16_f32 v142, v124, v125\n"
    "v_mfma_f32_16x16x32_bf16 v[116:119], v[52:55], a[112:115], v[116:119]\n"
    "v_cvt_pk_bf16_f32 v143, v126, v127\n"
    "v_mfma_f32_16x16x32_bf16 v[112:115], v[56:59], a[116:119], v[112:115]\n"
    "buffer_store_dwordx4 v[140:143], v1, s[40:43], s53 offen sc1\n"
    "v_mfma_f32_16x16x32_bf16 v[116:119], v[60:63], a[116:119], v[116:119]\n"
    "s_add_u32 s52, s52, 65536\n"
    "s_lshr_b32 s59, s52, 8\n"
    "s_and_b32 s59, s59, 0x2000\n"
    "s_add_u32 s53, s52, s59\n"
    "v_mfma_f32_16x16x32_bf16 v[120:123], v[16:19], a[120:123], 0\n"
    "v_mfma_f32_16x16x32_bf16 v[124:127], v[20:23], a[120:123], 0\n"
    "v_mfma_f32_16x16x32_bf16 v[120:123], v[24:27], a[124:127], v[120:123]\n"
    "v_mfma_f32_16x16x32_bf16 v[124:127], v[28:31], a[124:127], v[124:127]\n"
    "v_mfma_f32_16x16x32_bf16 v[120:123], v[32:35], a[128:131], v[120:123]\n"
    "v_mfma_f32_16x16x32_bf16 v[124:127], v[36:39], a[128:131], v[124:127]\n"
    "v_mfma_f32_16x16x32_bf16 v[120:123], v[40:43], a[132:135], v[120:123]\n"
    "v_cvt_pk_bf16_f32 v136, v112, v113\n"
    "v_mfma_f32_16x16x32_bf16 v[124:127], v[44:47], a[132:135], v[124:127]\n"
    "v_cvt_pk_bf16_f32 v137, v114, v115\n"
    "v_mfma_f32_16x16x32_bf16 v[120:123], v[48:51], a[136:139], v[120:123]\n"
    "v_cvt_pk_bf16_f32 v138, v116, v117\n"
    "v_mfma_f32_16x16x32_bf16 v[124:127], v[52:55], a[136:139], v[124:127]\n"
    "v_cvt_pk_bf16_f32 v139, v118, v119\n"
    "v_mfma_f32_16x16x32_bf16 v[120:123], v[56:59], a[140:143], v[120:123]\n"
    "buffer_store_dwordx4 v[136:139], v1, s[40:43], s53 offen sc1\n"
    "v_mfma_f32_16x16x32_bf16 v[124:127], v[60:63], a[140:143], v[124:127]\n"
    "s_add_u32 s52, s52, 65536\n"
    "s_lshr_b32 s59, s52, 8\n"
    "s_and_b32 s59, s59, 0x2000\n"
    "s_add_u32 s53, s52, s59\n"
    "v_mfma_f32_16x16x32_bf16 v[112:115], v[16:19], a[144:147], 0\n"
    "v_mfma_f32_16x16x32_bf16 v[116:119], v[20:23], a[144:147], 0\n"
    "v_mfma_f32_16x16x32_bf16 v[112:115], v[24:27], a[148:151], v[112:115]\n"
    "v_mfma_f32_16x16x32_bf16 v[116:119], v[28:31], a[148:151], v[116:119]\n"
    "v_mfma_f32_16x16x32_bf16 v[112:115], v[32:35], a[152:155], v[112:115]\n"
    "v_mfma_f32_16x16x32_bf16 v[116:119], v[36:39], a[152:155], v[116:119]\n"
    "v_mfma_f32_16x16x32_bf16 v[112:115], v[40:43], a[156:159], v[112:115]\n"
    "v_cvt_pk_bf16_f32 v140, v120, v121\n"
    "v_mfma_f32_16x16x32_bf16 v[116:119], v[44:47], a[156:159], v[116:119]\n"
    "v_cvt_pk_bf16_f32 v141, v122, v123\n"
    "v_mfma_f32_16x16x32_bf16 v[112:115], v[48:51], a[160:163], v[112:115]\n"
    "v_cvt_pk_bf16_f32 v142, v124, v125\n"
    "v_mfma_f32_16x16x32_bf16 v[116:119], v[52:55], a[160:163], v[116:119]\n"
    "v_cvt_pk_bf16_f32 v143, v126, v127\n"
    "v_mfma_f32_16x16x32_bf16 v[112:115], v[56:59], a[164:167], v[112:115]\n"
    "buffer_store_dwordx4 v[140:143], v1, s[40:43], s53 offen sc1\n"
    "v_mfma_f32_16x16x32_bf16 v[116:119], v[60:63], a[164:167], v[116:119]\n"
    "s_add_u32 s52, s52, 65536\n"
    "s_lshr_b32 s59, s52, 8\n"
    "s_and_b32 s59, s59, 0x2000\n"
    "s_add_u32 s53, s52, s59\n"
    "s_waitcnt vmcnt(6)\n"
    "s_cmp_eq_u32 s56, s50\n"
    "s_cbranch_scc1 hpo_have0_%=\n"
    "s_mov_b32 s60, 0\n"
    "v_cmp_gt_u32 vcc, s55, v6\n"
    "s_nop 4\n"
    "s_cbranch_vccz hpo_v10_ok_%=\n"
    "hpo_v10_poll_%=:\n"
    "buffer_load_dword v6, v7, s[40:43], s58 offen sc1\n"
    "s_waitcnt vmcnt(0)\n"
    "v_cmp_gt_u32 vcc, s55, v6\n"
    "s_nop 4\n"
    "s_cbranch_vccz hpo_v10_ok_%=\n"
    "s_sleep 2\n"
    "s_add_u32 s60, s60, 1\n"
    "s_cmp_gt_u32 s60, 65536\n"
    "s_cbranch_scc1 hp_abort_%=\n"
    "s_and_b32 s59, s60, 63\n"
    "s_cbranch_scc1 hpo_v10_poll_%=\n"
    "v_mov_b32 v4, 1024\n"
    "buffer_load_dword v4, v4, s[40:43], s58 offen sc1\n"
    "s_waitcnt vmcnt(0)\n"
    "v_readfirstlane_b32 s59, v4\n"
    "s_nop 0\n"
    "s_cmp_lg_u32 s59, 0\n"
    "s_nop 2\n"
    "s_cbranch_scc1 hp_end_%=\n"
    "s_branch hpo_v10_poll_%=\n"
    "hpo_v10_ok_%=:\n"
    "buffer_load_dwordx4 v[144:147], v0, s[40:43], s50 offen offset:0 sc1\n"
    "buffer_load_dwordx4 v[148:151], v0, s[40:43], s50 offen offset:1024 sc1\n"
    "buffer_load_dwordx4 v[152:155], v0, s[40:43], s50 offen offset:2048 sc1\n"
    "s_waitcnt vmcnt(0)\n"
    "hpo_have0_%=:\n"
    "v_mfma_f32_16x16x32_bf16 v[120:123], v[16:19], a[168:171], 0\n"
    "ds_write2st64_b64 v8, v[144:145], v[146:147] offset0:24 offset1:26\n"
    "v_mfma_f32_16x16x32_bf16 v[124:127], v[20:23], a[168:171], 0\n"
    "ds_write2st64_b64 v9, v[148:149], v[150:151] offset0:24 offset1:26\n"
    "v_mfma_f32_16x16x32_bf16 v[120:123], v[24:27], a[172:175], v[120:123]\n"
    "ds_write2st64_b64 v10, v[152:153], v[154:155] offset0:24 offset1:26\n"
    "v_mfma_f32_16x16x32_bf16 v[124:127], v[28:31], a[172:175], v[124:127]\n"
    "v_mfma_f32_16x16x32_bf16 v[120:123], v[32:35], a[176:179], v[120:123]\n"
    "v_mfma_f32_16x16x32_bf16 v[124:127], v[36:39], a[176:179], v[124:127]\n"
    "v_mfma_f32_16x16x32_bf16 v[120:123], v[40:43], a[180:183], v[120:123]\n"
    "v_cvt_pk_bf16_f32 v136, v112, v113\n"
    "v_mfma_f32_16x16x32_bf16 v[124:127], v[44:47], a[180:183], v[124:127]\n"
    "v_cvt_pk_bf16_f32 v137, v114, v115\n"
    "v_mfma_f32_16x16x32_bf16 v[120:123], v[48:51], a[184:187], v[120:123]\n"
    "v_cvt_pk_bf16_f32 v138, v116, v117\n"
    "v_mfma_f32_16x16x32_bf16 v[124:127], v[52:55], a[184:187], v[124:127]\n"
    "v_cvt_pk_bf16_f32 v139, v118, v119\n"
    "v_mfma_f32_16x16x32_bf16 v[120:123], v[56:59], a[188:191], v[120:123]\n"
    "buffer_store_dwordx4 v[136:139], v1, s[40:43], s53 offen sc1\n"
    "v_mfma_f32_16x16x32_bf16 v[124:127], v[60:63], a[188:191], v[124:127]\n"
    "s_add_u32 s52, s52, 65536\n"
    "s_lshr_b32 s59, s52, 8\n"
    "s_and_b32 s59, s59, 0x2000\n"
    "s_add_u32 s53, s52, s59\n"
    "s_waitcnt lgkmcnt(0)\n"
    "s_barrier\n"
    "buffer_atomic_add v12, v2, s[40:43], s64 offen\n"
    "v_mfma_f32_16x16x32_bf16 v[112:115], v[16:19], a[192:195], 0\n"
    "ds_read_b128 v[64:67], v0 offset:12288\n"
    "v_mfma_f32_16x16x32_bf16 v[116:119], v[20:23], a[192:195], 0\n"
    "ds_read_b128 v[68:71], v0 offset:13312\n"
    "v_mfma_f32_16x16x32_bf16 v[112:115], v[24:27], a[196:199], v[112:115]\n"
    "ds_read_b128 v[72:75], v0 offset:14336\n"
    "v_mfma_f32_16x16x32_bf16 v[116:119], v[28:31], a[196:199], v[116:119]\n"
    "ds_read_b128 v[76:79], v0 offset:15360\n"
    "v_mfma_f32_16x16x32_bf16 v[112:115], v[32:35], a[200:203], v[112:115]\n"
    "ds_read_b128 v[80:83], v0 offset:16384\n"
    "v_mfma_f32_16x16x32_bf16 v[116:119], v[36:39], a[200:203], v[116:119]\n"
    "ds_read_b128 v[84:87], v0 offset:17408\n"
    "v_mfma_f32_16x16x32_bf16 v[112:115], v[40:43], a[204:207], v[112:115]\n"
    "v_cvt_pk_bf16_f32 v140, v120, v121\n"
    "v_mfma_f32_16x16x32_bf16 v[116:119], v[44:47], a[204:207], v[116:119]\n"
    "v_cvt_pk_bf16_f32 v141, v122, v123\n"
    "v_mfma_f32_16x16x32_bf16 v[112:115], v[48:51], a[208:211], v[112:115]\n"
    "v_cvt_pk_bf16_f32 v142, v124, v125\n"
    "v_mfma_f32_16x16x32_bf16 v[116:119], v[52:55], a[208:211], v[116:119]\n"
    "v_cvt_pk_bf16_f32 v143, v126, v127\n"
    "v_mfma_f32_16x16x32_bf16 v[112:115], v[56:59], a[212:215], v[112:115]\n"
    "buffer_store_dwordx4 v[140:143], v1, s[40:43], s53 offen sc1\n"
    "v_mfma_f32_16x16x32_bf16 v[116:119], v[60:63], a[212:215], v[116:119]\n"
    "s_add_u32 s52, s52, 65536\n"
    "s_lshr_b32 s59, s52, 8\n"
    "s_and_b32 s59, s59, 0x2000\n"
    "s_add_u32 s53, s52, s59\n"
    "v_mfma_f32_16x16x32_bf16 v[120:123], v[16:19], a[216:219], 0\n"
    "ds_read_b128 v[88:91], v0 offset:18432\n"
    "v_mfma_f32_16x16x32_bf16 v[124:127], v[20:23], a[216:219], 0\n"
    "ds_read_b128 v[92:95], v0 offset:19456\n"
    "v_mfma_f32_16x16x32_bf16 v[120:123], v[24:27], a[220:223], v[120:123]\n"
    "ds_read_b128 v[96:99], v0 offset:20480\n"
    "v_mfma_f32_16x16x32_bf16 v[124:127], v[28:31], a[220:223], v[124:127]\n"
    "ds_read_b128 v[100:103], v0 offset:21504\n"
    "v_mfma_f32_16x16x32_bf16 v[120:123], v[32:35], a[224:227], v[120:123]\n"
    "ds_read_b128 v[104:107], v0 offset:22528\n"
    "v_mfma_f32_16x16x32_bf16 v[124:127], v[36:39], a[224:227], v[124:127]\n"
    "ds_read_b128 v[108:111], v0 offset:23552\n"
    "v_mfma_f32_16x16x32_bf16 v[120:123], v[40:43], a[228:231], v[120:123]\n"
    "v_cvt_pk_bf16_f32 v136, v112, v113\n"
    "v_mfma_f32_16x16x32_bf16 v[124:127], v[44:47], a[228:231], v[124:127]\n"
    "v_cvt_pk_bf16_f32 v137, v114, v115\n"
    "v_mfma_f32_16x16x32_bf16 v[120:123], v[48:51], a[232:235], v[120:123]\n"
    "v_cvt_pk_bf16_f32 v138, v116, v117\n"
    "v_mfma_f32_16x16x32_bf16 v[124:127], v[52:55], a[232:235], v[124:127]\n"
    "v_cvt_pk_bf16_f32 v139, v118, v119\n"
    "v_mfma_f32_16x16x32_bf16 v[120:123], v[56:59], a[236:239], v[120:123]\n"
    "buffer_store_dwordx4 v[136:139], v1, s[40:43], s53 offen sc1\n"
    "v_mfma_f32_16x16x32_bf16 v[124:127], v[60:63], a[236:239], v[124:127]\n"
    "s_add_u32 s52, s52, 65536\n"
    "s_lshr_b32 s59, s52, 8\n"
    "s_and_b32 s59, s59, 0x2000\n"
    "s_add_u32 s53, s52, s59\n"
    "s_waitcnt lgkmcnt(0)\n"
    "s_add_u32 s63, s63, 2621440\n"
    "s_and_b32 s59, s47, 31\n"
    "s_cmp_eq_u32 s59, 31\n"
    "s_cselect_b32 s63, s62, s63\n"
    "s_lshl_b32 s64, s47, 7\n"
    "s_add_u32 s64, s64, 2124032\n"
    "s_and_b32 s57, s47, 63\n"
    "s_sub_u32 s59, s57, 2\n"
    "s_cmp_lt_u32 s59, 61\n"
    "s_cbranch_scc1 hpo_late0_%=\n"
    "v_cvt_pk_bf16_f32 v140, v120, v121\n"
    "v_cvt_pk_bf16_f32 v141, v122, v123\n"
    "v_cvt_pk_bf16_f32 v142, v124, v125\n"
    "v_cvt_pk_bf16_f32 v143, v126, v127\n"
    "buffer_store_dwordx4 v[140:143], v1, s[40:43], s53 offen sc1\n"
    "s_waitcnt vmcnt(0)\n"
    "s_barrier\n"
    "buffer_atomic_add v12, v2, s[40:43], s64 offen\n"
    "s_mov_b32 s64, 0x40000000\n"
    "s_cmp_eq_u32 s57, 63\n"
    "s_cbranch_scc1 hp_ret_%=\n"
    "hpo_late0_%=:\n"
    "s_add_u32 s47, s47, 1\n"
    "s_add_u32 s55, s47, 2\n"
    "s_min_u32 s55, s55, s61\n"
    "s_add_u32 s59, s47, 0\n"
    "s_and_b32 s59, s59, 63\n"
    "s_cmp_eq_u32 s59, 31\n"
    "s_cselect_b32 s59, -31, 1\n"
    "s_mul_i32 s57, s59, 393216\n"
    "s_add_u32 s50, s50, s57\n"
    "v_cmp_gt_u32 vcc, s55, v6\n"
    "s_nop 0\n"
    "s_cmp_eq_u64 vcc, 0\n"
    "s_cselect_b32 s56, s50, 0x40000000\n"
    "v_mfma_f32_16x16x32_bf16 v[112:115], v[64:67], a[0:3], 0\n"
    "buffer_load_dwordx4 v[144:147], v0, s[40:43], s56 offen offset:0 sc1\n"
    "v_mfma_f32_16x16x32_bf16 v[116:119], v[68:71], a[0:3], 0\n"
    "buffer_load_dwordx4 v[148:151], v0, s[40:43], s56 offen offset:1024 sc1\n"
    "v_mfma_f32_16x16x32_bf16 v[112:115], v[72:75], a[4:7], v[112:115]\n"
    "buffer_load_dwordx4 v[152:155], v0, s[40:43], s56 offen offset:2048 sc1\n"
    "v_mfma_f32_16x16x32_bf16 v[116:119], v[76:79], a[4:7], v[116:119]\n"
    "buffer_load_dword v6, v7, s[40:43], s58 offen sc1\n"
    "v_mfma_f32_16x16x32_bf16 v[112:115], v[80:83], a[8:11], v[112:115]\n"
    "buffer_load_dword v14, v13, s[40:43], s58 offen sc1\n"
    "v_mfma_f32_16x16x32_bf16 v[116:119], v[84:87], a[8:11], v[116:119]\n"
    "v_mfma_f32_16x16x32_bf16 v[112:115], v[88:91], a[12:15], v[112:115]\n"
    "v_cvt_pk_bf16_f32 v140, v120, v121\n"
    "v_mfma_f32_16x16x32_bf16 v[116:119], v[92:95], a[12:15], v[116:119]\n"
    "v_cvt_pk_bf16_f32 v141, v122, v123\n"
    "v_mfma_f32_16x16x32_bf16 v[112:115], v[96:99], a[16:19], v[112:115]\n"
    "v_cvt_pk_bf16_f32 v142, v124, v125\n"
    "v_mfma_f32_16x16x32_bf16 v[116:119], v[100:103], a[16:19], v[116:119]\n"
    "v_cvt_pk_bf16_f32 v143, v126, v127\n"
    "v_mfma_f32_16x16x32_bf16 v[112:115], v[104:107], a[20:23], v[112:115]\n"
    "buffer_store_dwordx4 v[140:143], v1, s[40:43], s53 offen sc1\n"
    "v_mfma_f32_16x16x32_bf16 v[116:119], v[108:111], a[20:23], v[116:119]\n"
    "s_mov_b32 s52, s63\n"
    "s_lshr_b32 s59, s52, 8\n"
    "s_and_b32 s59, s59, 0x2000\n"
    "s_add_u32 s53, s52, s59\n"
    "v_mfma_f32_16x16x32_bf16 v[120:123], v[64:67], a[24:27], 0\n"
    "v_mfma_f32_16x16x32_bf16 v[124:127], v[68:71], a[24:27], 0\n"
    "v_mfma_f32_16x16x32_bf16 v[120:123], v[72:75], a[28:31], v[120:123]\n"
    "v_mfma_f32_16x16x32_bf16 v[124:127], v[76:79], a[28:31], v[124:127]\n"
    "v_mfma_f32_16x16x32_bf16 v[120:123], v[80:83], a[32:35], v[120:123]\n"
    "v_mfma_f32_16x16x32_bf16 v[124:127], v[84:87], a[32:35], v[124:127]\n"
    "v_mfma_f32_16x16x32_bf16 v[120:123], v[88:91], a[36:39], v[120:123]\n"
    "v_cvt_pk_bf16_f32 v136, v112, v113\n"
    "v_mfma_f32_16x16x32_bf16 v[124:127], v[92:95], a[36:39], v[124:127]\n"
    "v_cvt_pk_bf16_f32 v137, v114, v115\n"
    "v_mfma_f32_16x16x32_bf16 v[120:123], v[96:99], a[40:43], v[120:123]\n"
    "v_cvt_pk_bf16_f32 v138, v116, v117\n"
    "v_mfma_f32_16x16x32_bf16 v[124:127], v[100:103], a[40:43], v[124:127]\n"
    "v_cvt_pk_bf16_f32 v139, v118, v119\n"
    "v_mfma_f32_16x16x32_bf16 v[120:123], v[104:107], a[44:47], v[120:123]\n"
    "buffer_store_dwordx4 v[136:139], v1, s[40:43], s53 offen sc1\n"
    "v_mfma_f32_16x16x32_bf16 v[124:127], v[108:111], a[44:47], v[124:127]\n"
    "s_add_u32 s52, s52, 65536\n"
    "s_lshr_b32 s59, s52, 8\n"
    "s_and_b32 s59, s59, 0x2000\n"
    "s_add_u32 s53, s52, s59\n"
    "v_mfma_f32_16x16x32_bf16 v[112:115], v[64:67], a[48:51], 0\n"
    "v_mfma_f32_16x16x32_bf16 v[116:119], v[68:71], a[48:51], 0\n"
    "v_mfma_f32_16x16x32_bf16 v[112:115], v[72:75], a[52:55], v[112:115]\n"
    "v_mfma_f32_16x16x32_bf16 v[116:119], v[76:79], a[52:55], v[116:119]\n"
    "v_mfma_f32_16x16x32_bf16 v[112:115], v[80:83], a[56:59], v[112:115]\n"
    "v_mfma_f32_16x16x32_bf16 v[116:119], v[84:87], a[56:59], v[116:119]\n"
    "v_mfma_f32_16x16x32_bf16 v[112:115], v[88:91], a[60:63], v[112:115]\n"
    "v_cvt_pk_bf16_f32 v140, v120, v121\n"
    "v_mfma_f32_16x16x32_bf16 v[116:119], v[92:95], a[60:63], v[116:119]\n"
    "v_cvt_pk_bf16_f32 v141, v122, v123\n"
    "v_mfma_f32_16x16x32_bf16 v[112:115], v[96:99], a[64:67], v[112:115]\n"
    "v_cvt_pk_bf16_f32 v142, v124, v125\n"
    "v_mfma_f32_16x16x32_bf16 v[116:119], v[100:103], a[64:67], v[116:119]\n"
    "v_cvt_pk_bf16_f32 v143, v126, v127\n"
    "v_mfma_f32_16x16x32_bf16 v[112:115], v[104:107], a[68:71], v[112:115]\n"
    "buffer_store_dwordx4 v[140:143], v1, s[40:43], s53 offen sc1\n"
    "v_mfma_f32_16x16x32_bf16 v[116:119], v[108:111], a[68:71], v[116:119]\n"
    "s_add_u32 s52, s52, 65536\n"
    "s_lshr_b32 s59, s52, 8\n"
    "s_and_b32 s59, s59, 0x2000\n"
    "s_add_u32 s53, s52, s59\n"
    "v_mfma_f32_16x16x32_bf16 v[120:123], v[64:67], a[72:75], 0\n"
    "v_mfma_f32_16x16x32_bf16 v[124:127], v[68:71], a[72:75], 0\n"
    "v_mfma_f32_16x16x32_bf16 v[120:123], v[72:75], a[76:79], v[120:123]\n"
    "v_mfma_f32_16x16x32_bf16 v[124:127], v[76:79], a[76:79], v[124:127]\n"
    "v_mfma_f32_16x16x32_bf16 v[120:123], v[80:83], a[80:83], v[120:123]\n"
    "v_mfma_f32_16x16x32_bf16 v[124:127], v[84:87], a[80:83], v[124:127]\n"
    "v_mfma_f32_16x16x32_bf16 v[120:123], v[88:91], a[84:87], v[120:123]\n"
    "v_cvt_pk_bf16_f32 v136, v112, v113\n"
    "v_mfma_f32_16x16x32_bf16 v[124:127], v[92:95], a[84:87], v[124:127]\n"
    "v_cvt_pk_bf16_f32 v137, v114, v115\n"
    "v_mfma_f32_16x16x32_bf16 v[120:123], v[96:99], a[88:91], v[120:123]\n"
    "v_cvt_pk_bf16_f32 v138, v116, v117\n"
    "v_mfma_f32_16x16x32_bf16 v[124:127], v[100:103], a[88:91], v[124:127]\n"
    "v_cvt_pk_bf16_f32 v139, v118, v119\n"
    "v_mfma_f32_16x16x32_bf16 v[120:123], v[104:107], a[92:95], v[120:123]\n"
    "buffer_store_dwordx4 v[136:139], v1, s[40:43], s53 offen sc1\n"
    "v_mfma_f32_16x16x32_bf16 v[124:127], v[108:111], a[92:95], v[124:127]\n"
    "s_add_u32 s52, s52, 65536\n"
    "s_lshr_b32 s59, s52, 8\n"
    "s_and_b32 s59, s59, 0x2000\n"
    "s_add_u32 s53, s52, s59\n"
    "v_mfma_f32_16x16x32_bf16 v[112:115], v[64:67], a[96:99], 0\n"
    "v_mfma_f32_16x16x32_bf16 v[116:119], v[68:71], a[96:99], 0\n"
    "v_mfma_f32_16x16x32_bf16 v[112:115], v[72:75], a[100:103], v[112:115]\n"
    "v_mfma_f32_16x16x32_bf16 v[116:119], v[76:79], a[100:103], v[116:119]\n"
    "v_mfma_f32_16x16x32_bf16 v[112:115], v[80:83], a[104:107], v[112:115]\n"
    "v_mfma_f32_16x16x32_bf16 v[116:119], v[84:87], a[104:107], v[116:119]\n"
    "v_mfma_f32_16x16x32_bf16 v[112:115], v[88:91], a[108:111], v[112:115]\n"
    "v_cvt_pk_bf16_f32 v140, v120, v121\n"
    "v_mfma_f32_16x16x32_bf16 v[116:119], v[92:95], a[108:111], v[116:119]\n"
    "v_cvt_pk_bf16_f32 v141, v122, v123\n"
    "v_mfma_f32_16x16x32_bf16 v[112:115], v[96:99], a[112:115], v[112:115]\n"
    "v_cvt_pk_bf16_f32 v142, v124, v125\n"
    "v_mfma_f32_16x16x32_bf16 v[116:119], v[100:103], a[112:115], v[116:119]\n"
    "v_cvt_pk_bf16_f32 v143, v126, v127\n"
    "v_mfma_f32_16x16x32_bf16 v[112:115], v[104:107], a[116:119], v[112:115]\n"
    "buffer_store_dwordx4 v[140:143], v1, s[40:43], s53 offen sc1\n"
    "v_mfma_f32_16x16x32_bf16 v[116:119], v[108:111], a[116:119], v[116:119]\n"
    "s_add_u32 s52, s52, 65536\n"
    "s_lshr_b32 s59, s52, 8\n"
    "s_and_b32 s59, s59, 0x2000\n"
    "s_add_u32 s53, s52, s59\n"
    "v_mfma_f32_16x16x32_bf16 v[120:123], v[64:67], a[120:123], 0\n"
    "v_mfma_f32_16x16x32_bf16 v[124:127], v[68:71], a[120:123], 0\n"
    "v_mfma_f32_16x16x32_bf16 v[120:123], v[72:75], a[124:127], v[120:123]\n"
    "v_mfma_f32_16x16x32_bf16 v[124:127], v[76:79], a[124:127], v[124:127]\n"
    "v_mfma_f32_16x16x32_bf16 v[120:123], v[80:83], a[128:131], v[120:123]\n"
    "v_mfma_f32_16x16x32_bf16 v[124:127], v[84:87], a[128:131], v[124:127]\n"
    "v_mfma_f32_16x16x32_bf16 v[120:123], v[88:91], a[132:135], v[120:123]\n"
    "v_cvt_pk_bf16_f32 v136, v112, v113\n"
    "v_mfma_f32_16x16x32_bf16 v[124:127], v[92:95], a[132:135], v[124:127]\n"
    "v_cvt_pk_bf16_f32 v137, v114, v115\n"
    "v_mfma_f32_16x16x32_bf16 v[120:123], v[96:99], a[136:139], v[120:123]\n"
    "v_cvt_pk_bf16_f32 v138, v116, v117\n"
    "v_mfma_f32_16x16x32_bf16 v[124:127], v[100:103], a[136:139], v[124:127]\n"
    "v_cvt_pk_bf16_f32 v139, v118, v119\n"
    "v_mfma_f32_16x16x32_bf16 v[120:123], v[104:107], a[140:143], v[120:123]\n"
    "buffer_store_dwordx4 v[136:139], v1, s[40:43], s53 offen sc1\n"
    "v_mfma_f32_16x16x32_bf16 v[124:127], v[108:111], a[140:143], v[124:127]\n"
    "s_add_u32 s52, s52, 65536\n"
    "s_lshr_b32 s59, s52, 8\n"
    "s_and_b32 s59, s59, 0x2000\n"
    "s_add_u32 s53, s52, s59\n"
    "v_mfma_f32_16x16x32_bf16 v[112:115], v[64:67], a[144:147], 0\n"
    "v_mfma_f32_16x16x32_bf16 v[116:119], v[68:71], a[144:147], 0\n"
    "v_mfma_f32_16x16x32_bf16 v[112:115], v[72:75], a[148:151], v[112:115]\n"
    "v_mfma_f32_16x16x32_bf16 v[116:119], v[76:79], a[148:151], v[116:119]\n"
    "v_mfma_f32_16x16x32_bf16 v[112:115], v[80:83], a[152:155], v[112:115]\n"
    "v_mfma_f32_16x16x32_bf16 v[116:119], v[84:87], a[152:155], v[116:119]\n"
    "v_mfma_f32_16x16x32_bf16 v[112:115], v[88:91], a[156:159], v[112:115]\n"
    "v_cvt_pk_bf16_f32 v140, v120, v121\n"
    "v_mfma_f32_16x16x32_bf16 v[116:119], v[92:95], a[156:159], v[116:119]\n"
    "v_cvt_pk_bf16_f32 v141, v122, v123\n"
    "v_mfma_f32_16x16x32_bf16 v[112:115], v[96:99], a[160:163], v[112:115]\n"
    "v_cvt_pk_bf16_f32 v142, v124, v125\n"
    "v_mfma_f32_16x16x32_bf16 v[116:119], v[100:103], a[160:163], v[116:119]\n"
    "v_cvt_pk_bf16_f32 v143, v126, v127\n"
    "v_mfma_f32_16x16x32_bf16 v[112:115], v[104:107], a[164:167], v[112:115]\n"
    "buffer_store_dwordx4 v[140:143], v1, s[40:43], s53 offen sc1\n"
    "v_mfma_f32_16x16x32_bf16 v[116:119], v[108:111], a[164:167], v[116:119]\n"
    "s_add_u32 s52, s52, 65536\n"
    "s_lshr_b32 s59, s52, 8\n"
    "s_and_b32 s59, s59, 0x2000\n"
    "s_add_u32 s53, s52, s59\n"
    "s_waitcnt vmcnt(6)\n"
    "s_cmp_eq_u32 s56, s50\n"
    "s_cbranch_scc1 hpo_have1_%=\n"
    "s_mov_b32 s60, 0\n"
    "v_cmp_gt_u32 vcc, s55, v6\n"
    "s_nop 4\n"
    "s_cbranch_vccz hpo_v11_ok_%=\n"
    "hpo_v11_poll_%=:\n"
    "buffer_load_dword v6, v7, s[40:43], s58 offen sc1\n"
    "s_waitcnt vmcnt(0)\n"
    "v_cmp_gt_u32 vcc, s55, v6\n"
    "s_nop 4\n"
    "s_cbranch_vccz hpo_v11_ok_%=\n"
    "s_sleep 2\n"
    "s_add_u32 s60, s60, 1\n"
    "s_cmp_gt_u32 s60, 65536\n"
    "s_cbranch_scc1 hp_abort_%=\n"
    "s_and_b32 s59, s60, 63\n"
    "s_cbranch_scc1 hpo_v11_poll_%=\n"
    "v_mov_b32 v4, 1024\n"
    "buffer_load_dword v4, v4, s[40:43], s58 offen sc1\n"
    "s_waitcnt vmcnt(0)\n"
    "v_readfirstlane_b32 s59, v4\n"
    "s_nop 0\n"
    "s_cmp_lg_u32 s59, 0\n"
    "s_nop 2\n"
    "s_cbranch_scc1 hp_end_%=\n"
    "s_branch hpo_v11_poll_%=\n"
    "hpo_v11_ok_%=:\n"
    "buffer_load_dwordx4 v[144:147], v0, s[40:43], s50 offen offset:0 sc1\n"
    "buffer_load_dwordx4 v[148:151], v0, s[40:43], s50 offen offset:1024 sc1\n"
    "buffer_load_dwordx4 v[152:155], v0, s[40:43], s50 offen offset:2048 sc1\n"
    "s_waitcnt vmcnt(0)\n"
    "hpo_have1_%=:\n"
    "v_mfma_f32_16x16x32_bf16 v[120:123], v[64:67], a[168:171], 0\n"
    "ds_write2st64_b64 v8, v[144:145], v[146:147] offset0:0 offset1:2\n"
    "v_mfma_f32_16x16x32_bf16 v[124:127], v[68:71], a[168:171], 0\n"
    "ds_write2st64_b64 v9, v[148:149], v[150:151] offset0:0 offset1:2\n"
    "v_mfma_f32_16x16x32_bf16 v[120:123], v[72:75], a[172:175], v[120:123]\n"
    "ds_write2st64_b64 v10, v[152:153], v[154:155] offset0:0 offset1:2\n"
    "v_mfma_f32_16x16x32_bf16 v[124:127], v[76:79], a[172:175], v[124:127]\n"
    "v_mfma_f32_16x16x32_bf16 v[120:123], v[80:83], a[176:179], v[120:123]\n"
    "v_mfma_f32_16x16x32_bf16 v[124:127], v[84:87], a[176:179], v[124:127]\n"
    "v_mfma_f32_16x16x32_bf16 v[120:123], v[88:91], a[180:183], v[120:123]\n"
    "v_cvt_pk_bf16_f32 v136, v112, v113\n"
    "v_mfma_f32_16x16x32_bf16 v[124:127], v[92:95], a[180:183], v[124:127]\n"
    "v_cvt_pk_bf16_f32 v137, v114, v115\n"
    "v_mfma_f32_16x16x32_bf16 v[120:123], v[96:99], a[184:187], v[120:123]\n"
    "v_cvt_pk_bf16_f32 v138, v116, v117\n"
    "v_mfma_f32_16x16x32_bf16 v[124:127], v[100:103], a[184:187], v[124:127]\n"
    "v_cvt_pk_bf16_f32 v139, v118, v119\n"
    "v_mfma_f32_16x16x32_bf16 v[120:123], v[104:107], a[188:191], v[120:123]\n"
    "buffer_store_dwordx4 v[136:139], v1, s[40:43], s53 offen sc1\n"
    "v_mfma_f32_16x16x32_bf16 v[124:127], v[108:111], a[188:191], v[124:127]\n"
    "s_add_u32 s52, s52, 65536\n"
    "s_lshr_b32 s59, s52, 8\n"
    "s_and_b32 s59, s59, 0x2000\n"
    "s_add_u32 s53, s52, s59\n"
    "s_waitcnt lgkmcnt(0)\n"
    "s_barrier\n"
    "buffer_atomic_add v12, v2, s[40:43], s64 offen\n"
    "v_mfma_f32_16x16x32_bf16 v[112:115], v[64:67], a[192:195], 0\n"
    "ds_read_b128 v[16:19], v0 offset:0\n"
    "v_mfma_f32_16x16x32_bf16 v[116:119], v[68:71], a[192:195], 0\n"
    "ds_read_b128 v[20:23], v0 offset:1024\n"
    "v_mfma_f32_16x16x32_bf16 v[112:115], v[72:75], a[196:199], v[112:115]\n"
    "ds_read_b128 v[24:27], v0 offset:2048\n"
    "v_mfma_f32_16x16x32_bf16 v[116:119], v[76:79], a[196:199], v[116:119]\n"
    "ds_read_b128 v[28:31], v0 offset:3072\n"
    "v_mfma_f32_16x16x32_bf16 v[112:115], v[80:83], a[200:203], v[112:115]\n"
    "ds_read_b128 v[32:35], v0 offset:4096\n"
    "v_mfma_f32_16x16x32_bf16 v[116:119], v[84:87], a[200:203], v[116:119]\n"
    "ds_read_b128 v[36:39], v0 offset:5120\n"
    "v_mfma_f32_16x16x32_bf16 v[112:115], v[88:91], a[204:207], v[112:115]\n"
    "v_cvt_pk_bf16_f32 v140, v120, v121\n"
    "v_mfma_f32_16x16x32_bf16 v[116:119], v[92:95], a[204:207], v[116:119]\n"
    "v_cvt_pk_bf16_f32 v141, v122, v123\n"
    "v_mfma_f32_16x16x32_bf16 v[112:115], v[96:99], a[208:211], v[112:115]\n"
    "v_cvt_pk_bf16_f32 v142, v124, v125\n"
    "v_mfma_f32_16x16x32_bf16 v[116:119], v[100:103], a[208:211], v[116:119]\n"
    "v_cvt_pk_bf16_f32 v143, v126, v127\n"
    "v_mfma_f32_16x16x32_bf16 v[112:115], v[104:107], a[212:215], v[112:115]\n"
    "buffer_store_dwordx4 v[140:143], v1, s[40:43], s53 offen sc1\n"
    "v_mfma_f32_16x16x32_bf16 v[116:119], v[108:111], a[212:215], v[116:119]\n"
    "s_add_u32 s52, s52, 65536\n"
    "s_lshr_b32 s59, s52, 8\n"
    "s_and_b32 s59, s59, 0x2000\n"
    "s_add_u32 s53, s52, s59\n"
    "v_mfma_f32_16x16x32_bf16 v[120:123], v[64:67], a[216:219], 0\n"
    "ds_read_b128 v[40:43], v0 offset:6144\n"
    "v_mfma_f32_16x16x32_bf16 v[124:127], v[68:71], a[216:219], 0\n"
    "ds_read_b128 v[44:47], v0 offset:7168\n"
    "v_mfma_f32_16x16x32_bf16 v[120:123], v[72:75], a[220:223], v[120:123]\n"
    "ds_read_b128 v[48:51], v0 offset:8192\n"
    "v_mfma_f32_16x16x32_bf16 v[124:127], v[76:79], a[220:223], v[124:127]\n"
    "ds_read_b128 v[52:55], v0 offset:9216\n"
    "v_mfma_f32_16x16x32_bf16 v[120:123], v[80:83], a[224:227], v[120:123]\n"
    "ds_read_b128 v[56:59], v0 offset:10240\n"
    "v_mfma_f32_16x16x32_bf16 v[124:127], v[84:87], a[224:227], v[124:127]\n"
    "ds_read_b128 v[60:63], v0 offset:11264\n"
    "v_mfma_f32_16x16x32_bf16 v[120:123], v[88:91], a[228:231], v[120:123]\n"
    "v_cvt_pk_bf16_f32 v136, v112, v113\n"
    "v_mfma_f32_16x16x32_bf16 v[124:127], v[92:95], a[228:231], v[124:127]\n"
    "v_cvt_pk_bf16_f32 v137, v114, v115\n"
    "v_mfma_f32_16x16x32_bf16 v[120:123], v[96:99], a[232:235], v[120:123]\n"
    "v_cvt_pk_bf16_f32 v138, v116, v117\n"
    "v_mfma_f32_16x16x32_bf16 v[124:127], v[100:103], a[232:235], v[124:127]\n"
    "v_cvt_pk_bf16_f32 v139, v118, v119\n"
    "v_mfma_f32_16x16x32_bf16 v[120:123], v[104:107], a[236:239], v[120:123]\n"
    "buffer_store_dwordx4 v[136:139], v1, s[40:43], s53 offen sc1\n"
    "v_mfma_f32_16x16x32_bf16 v[124:127], v[108:111], a[236:239], v[124:127]\n"
    "s_add_u32 s52, s52, 65536\n"
    "s_lshr_b32 s59, s52, 8\n"
    "s_and_b32 s59, s59, 0x2000\n"
    "s_add_u32 s53, s52, s59\n"
    "s_waitcnt lgkmcnt(0)\n"
    "s_add_u32 s63, s63, 2621440\n"
    "s_and_b32 s59, s47, 31\n"
    "s_cmp_eq_u32 s59, 31\n"
    "s_cselect_b32 s63, s62, s63\n"
    "s_lshl_b32 s64, s47, 7\n"
    "s_add_u32 s64, s64, 2124032\n"
    "s_and_b32 s57, s47, 63\n"
    "s_sub_u32 s59, s57, 2\n"
    "s_cmp_lt_u32 s59, 61\n"
    "s_cbranch_scc1 hpo_late1_%=\n"
    "v_cvt_pk_bf16_f32 v140, v120, v121\n"
    "v_cvt_pk_bf16_f32 v141, v122, v123\n"
    "v_cvt_pk_bf16_f32 v142, v124, v125\n"
    "v_cvt_pk_bf16_f32 v143, v126, v127\n"
    "buffer_store_dwordx4 v[140:143], v1, s[40:43], s53 offen sc1\n"
    "s_waitcnt vmcnt(0)\n"
    "s_barrier\n"
    "buffer_atomic_add v12, v2, s[40:43], s64 offen\n"
    "s_mov_b32 s64, 0x40000000\n"
    "s_cmp_eq_u32 s57, 63\n"
    "s_cbranch_scc1 hp_ret_%=\n"
    "hpo_late1_%=:\n"
    "s_add_u32 s47, s47, 1\n"
    "s_branch hpo_loop_%=\n"
    "s_branch hp_ret_%=\n"
    "hp_b1_%=:\n"
    "s_and_b32 s26, s20, 7\n"
    "s_lshr_b32 s27, s20, 3\n"
    "s_lshl2_add_u32 s28, s27, s45\n"
    "s_lshl_b32 s48, s45, 3\n"
    "s_min_u32 s48, s48, 22\n"
    "s_lshl_b32 s61, s48, 10\n"
    "s_mul_i32 s50, s26, 307200\n"
    "s_add_u32 s50, s50, s61\n"
    "s_add_u32 s50, s50, 146210816\n"
    "s_add_u32 m0, s61, 0\n"
    "s_add_u32 s51, s50, 0\n"
    "buffer_load_dwordx4 v0, s[40:43], s51 offen offset:0 lds\n"
    "buffer_load_dwordx4 v0, s[40:43], s51 offen offset:1024 lds\n"
    "buffer_load_dwordx4 v0, s[40:43], s51 offen offset:2048 lds\n"
    "buffer_load_dwordx4 v0, s[40:43], s51 offen offset:3072 lds\n"
    "s_add_u32 m0, s61, 4096\n"
    "s_add_u32 s51, s50, 4096\n"
    "buffer_load_dwordx4 v0, s[40:43], s51 offen offset:0 lds\n"
    "buffer_load_dwordx4 v0, s[40:43], s51 offen offset:1024 lds\n"
    "buffer_load_dwordx4 v0, s[40:43], s51 offen offset:2048 lds\n"
    "buffer_load_dwordx4 v0, s[40:43], s51 offen offset:3072 lds\n"
    "s_add_u32 m0, s61, 30720\n"
    "s_add_u32 s51, s50, 30720\n"
    "buffer_load_dwordx4 v0, s[40:43], s51 offen offset:0 lds\n"
    "buffer_load_dwordx4 v0, s[40:43], s51 offen offset:1024 lds\n"
    "buffer_load_dwordx4 v0, s[40:43], s51 offen offset:2048 lds\n"
    "buffer_load_dwordx4 v0, s[40:43], s51 offen offset:3072 lds\n"
    "s_add_u32 m0, s61, 34816\n"
    "s_add_u32 s51, s50, 34816\n"
    "buffer_load_dwordx4 v0, s[40:43], s51 offen offset:0 lds\n"
    "buffer_load_dwordx4 v0, s[40:43], s51 offen offset:1024 lds\n"
    "buffer_load_dwordx4 v0, s[40:43], s51 offen offset:2048 lds\n"
    "buffer_load_dwordx4 v0, s[40:43], s51 offen offset:3072 lds\n"
    "s_add_u32 m0, s61, 61440\n"
    "s_add_u32 s51, s50, 61440\n"
    "buffer_load_dwordx4 v0, s[40:43], s51 offen offset:0 lds\n"
    "buffer_load_dwordx4 v0, s[40:43], s51 offen offset:1024 lds\n"
    "buffer_load_dwordx4 v0, s[40:43], s51 offen offset:2048 lds\n"
    "buffer_load_dwordx4 v0, s[40:43], s51 offen offset:3072 lds\n"
    "s_add_u32 m0, s61, 65536\n"
    "s_add_u32 s51, s50, 65536\n"
    "buffer_load_dwordx4 v0, s[40:43], s51 offen offset:0 lds\n"
    "buffer_load_dwordx4 v0, s[40:43], s51 offen offset:1024 lds\n"
    "buffer_load_dwordx4 v0, s[40:43], s51 offen offset:2048 lds\n"
    "buffer_load_dwordx4 v0, s[40:43], s51 offen offset:3072 lds\n"
    "s_add_u32 m0, s61, 92160\n"
    "s_add_u32 s51, s50, 92160\n"
    "buffer_load_dwordx4 v0, s[40:43], s51 offen offset:0 lds\n"
    "buffer_load_dwordx4 v0, s[40:43], s51 offen offset:1024 lds\n"
    "buffer_load_dwordx4 v0, s[40:43], s51 offen offset:2048 lds\n"
    "buffer_load_dwordx4 v0, s[40:43], s51 offen offset:3072 lds\n"
    "s_add_u32 m0, s61, 96256\n"
    "s_add_u32 s51, s50, 96256\n"
    "buffer_load_dwordx4 v0, s[40:43], s51 offen offset:0 lds\n"
    "buffer_load_dwordx4 v0, s[40:43], s51 offen offset:1024 lds\n"
    "buffer_load_dwordx4 v0, s[40:43], s51 offen offset:2048 lds\n"
    "buffer_load_dwordx4 v0, s[40:43], s51 offen offset:3072 lds\n"
    "s_add_u32 m0, s61, 122880\n"
    "s_add_u32 s51, s50, 122880\n"
    "buffer_load_dwordx4 v0, s[40:43], s51 offen offset:0 lds\n"
    "buffer_load_dwordx4 v0, s[40:43], s51 offen offset:1024 lds\n"
    "buffer_load_dwordx4 v0, s[40:43], s51 offen offset:2048 lds\n"
    "buffer_load_dwordx4 v0, s[40:43], s51 offen offset:3072 lds\n"
    "s_add_u32 m0, s61, 126976\n"
    "s_add_u32 s51, s50, 126976\n"
    "buffer_load_dwordx4 v0, s[40:43], s51 offen offset:0 lds\n"
    "buffer_load_dwordx4 v0, s[40:43], s51 offen offset:1024 lds\n"
    "buffer_load_dwordx4 v0, s[40:43], s51 offen offset:2048 lds\n"
    "buffer_load_dwordx4 v0, s[40:43], s51 offen offset:3072 lds\n"
    "v_mov_b32 v188, 0x3f803f80\n"
    "v_mov_b32 v189, 0x3f803f80\n"
    "v_mov_b32 v190, 0x3f803f80\n"
    "v_mov_b32 v191, 0x3f803f80\n"
    "v_add_u32 v6, 61440, v0\n"
    "v_add_u32 v7, 122880, v0\n"
    "s_lshl_b32 s54, s21, 5\n"
    "s_add_u32 s54, s54, s27\n"
    "s_lshl_b32 s54, s54, 7\n"
    "s_add_u32 s54, s54, 2354176\n"
    "s_waitcnt vmcnt(0)\n"
    "s_cmp_lg_u32 s45, 0\n"
    "s_cbranch_scc1 hpb1_dep_ok_%=\n"
    "s_mov_b32 s60, 0\n"
    "hpb1_dep_%=:\n"
    "buffer_load_dword v4, off, s[40:43], s54 sc1\n"
    "s_waitcnt vmcnt(0)\n"
    "v_readfirstlane_b32 s59, v4\n"
    "s_nop 0\n"
    "s_cmp_ge_u32 s59, 8\n"
    "s_nop 2\n"
    "s_cbranch_scc1 hpb1_dep_ok_%=\n"
    "s_sleep 3\n"
    "s_add_u32 s60, s60, 1\n"
    "s_cmp_gt_u32 s60, 65536\n"
    "s_cbranch_scc1 hp_abort_%=\n"
    "s_and_b32 s59, s60, 63\n"
    "s_cbranch_scc1 hpb1_dep_%=\n"
    "v_mov_b32 v4, 1024\n"
    "buffer_load_dword v4, v4, s[40:43], s58 offen sc1\n"
    "s_waitcnt vmcnt(0)\n"
    "v_readfirstlane_b32 s59, v4\n"
    "s_nop 0\n"
    "s_cmp_lg_u32 s59, 0\n"
    "s_nop 2\n"
    "s_cbranch_scc1 hp_end_%=\n"
    "s_branch hpb1_dep_%=\n"
    "hpb1_dep_ok_%=:\n"
    "s_barrier\n"
    "s_mul_i32 s52, s28, 20480\n"
    "s_add_u32 s52, s52, 143589376\n"
    "buffer_load_dwordx4 v[16:19], v0, s[40:43], s52 offen sc1\n"
    "buffer_load_dwordx4 v[20:23], v0, s[40:43], s52 offen offset:1024 sc1\n"
    "buffer_load_dwordx4 v[24:27], v0, s[40:43], s52 offen offset:2048 sc1\n"
    "buffer_load_dwordx4 v[28:31], v0, s[40:43], s52 offen offset:3072 sc1\n"
    "s_add_u32 s52, s52, 4096\n"
    "buffer_load_dwordx4 v[32:35], v0, s[40:43], s52 offen sc1\n"
    "buffer_load_dwordx4 v[36:39], v0, s[40:43], s52 offen offset:1024 sc1\n"
    "buffer_load_dwordx4 v[40:43], v0, s[40:43], s52 offen offset:2048 sc1\n"
    "buffer_load_dwordx4 v[44:47], v0, s[40:43], s52 offen offset:3072 sc1\n"
    "s_add_u32 s52, s52, 4096\n"
    "buffer_load_dwordx4 v[48:51], v0, s[40:43], s52 offen sc1\n"
    "buffer_load_dwordx4 v[52:55], v0, s[40:43], s52 offen offset:1024 sc1\n"
    "buffer_load_dwordx4 v[56:59], v0, s[40:43], s52 offen offset:2048 sc1\n"
    "buffer_load_dwordx4 v[60:63], v0, s[40:43], s52 offen offset:3072 sc1\n"
    "s_add_u32 s52, s52, 4096\n"
    "buffer_load_dwordx4 v[64:67], v0, s[40:43], s52 offen sc1\n"
    "buffer_load_dwordx4 v[68:71], v0, s[40:43], s52 offen offset:1024 sc1\n"
    "buffer_load_dwordx4 v[72:75], v0, s[40:43], s52 offen offset:2048 sc1\n"
    "buffer_load_dwordx4 v[76:79], v0, s[40:43], s52 offen offset:3072 sc1\n"
    "s_add_u32 s52, s52, 4096\n"
    "buffer_load_dwordx4 v[80:83], v0, s[40:43], s52 offen sc1\n"
    "buffer_load_dwordx4 v[84:87], v0, s[40:43], s52 offen offset:1024 sc1\n"
    "buffer_load_dwordx4 v[88:91], v0, s[40:43], s52 offen offset:2048 sc1\n"
    "buffer_load_dwordx4 v[92:95], v0, s[40:43], s52 offen offset:3072 sc1\n"
    "ds_read_b128 v[164:167], v0 offset:0\n"
    "ds_read_b128 v[168:171], v0 offset:1024\n"
    "ds_read_b128 v[172:175], v0 offset:2048\n"
    "ds_read_b128 v[176:179], v0 offset:3072\n"
    "ds_read_b128 v[180:183], v0 offset:4096\n"
    "ds_read_b128 v[184:187], v0 offset:5120\n"
    "s_waitcnt vmcnt(19) lgkmcnt(5)\n"
    "v_mfma_f32_16x16x32_bf16 v[96:99], v[164:167], v[16:19], 0\n"
    "ds_read_b128 v[164:167], v0 offset:6144\n"
    "s_waitcnt lgkmcnt(5)\n"
    "v_mfma_f32_16x16x32_bf16 v[100:103], v[168:171], v[16:19], 0\n"
    "ds_read_b128 v[168:171], v0 offset:7168\n"
    "s_waitcnt lgkmcnt(5)\n"
    "v_mfma_f32_16x16x32_bf16 v[104:107], v[172:175], v[16:19], 0\n"
    "ds_read_b128 v[172:175], v0 offset:8192\n"
    "s_waitcnt lgkmcnt(5)\n"
    "v_mfma_f32_16x16x32_bf16 v[108:111], v[176:179], v[16:19], 0\n"
    "ds_read_b128 v[176:179], v0 offset:9216\n"
    "s_waitcnt lgkmcnt(5)\n"
    "v_mfma_f32_16x16x32_bf16 v[112:115], v[180:183], v[16:19], 0\n"
    "ds_read_b128 v[180:183], v0 offset:10240\n"
    "s_waitcnt lgkmcnt(5)\n"
    "v_mfma_f32_16x16x32_bf16 v[116:119], v[184:187], v[16:19], 0\n"
    "ds_read_b128 v[184:187], v0 offset:11264\n"
    "s_waitcnt lgkmcnt(5)\n"
    "v_mfma_f32_16x16x32_bf16 v[120:123], v[164:167], v[16:19], 0\n"
    "ds_read_b128 v[164:167], v0 offset:12288\n"
    "s_waitcnt lgkmcnt(5)\n"
    "v_mfma_f32_16x16x32_bf16 v[124:127], v[168:171], v[16:19], 0\n"
    "ds_read_b128 v[168:171], v0 offset:13312\n"
    "v_mfma_f32_16x16x32_bf16 v[156:159], v[188:191], v[16:19], 0\n"
    "s_waitcnt lgkmcnt(5)\n"
    "v_mfma_f32_16x16x32_bf16 v[128:131], v[172:175], v[16:19], 0\n"
    "ds_read_b128 v[172:175], v0 offset:14336\n"
    "s_waitcnt lgkmcnt(5)\n"
    "v_mfma_f32_16x16x32_bf16 v[132:135], v[176:179], v[16:19], 0\n"
    "ds_read_b128 v[176:179], v0 offset:15360\n"
    "s_waitcnt lgkmcnt(5)\n"
    "v_mfma_f32_16x16x32_bf16 v[136:139], v[180:183], v[16:19], 0\n"
    "ds_read_b128 v[180:183], v0 offset:16384\n"
    "s_waitcnt lgkmcnt(5)\n"
    "v_mfma_f32_16x16x32_bf16 v[140:143], v[184:187], v[16:19], 0\n"
    "ds_read_b128 v[184:187], v0 offset:17408\n"
    "s_waitcnt lgkmcnt(5)\n"
    "v_mfma_f32_16x16x32_bf16 v[144:147], v[164:167], v[16:19], 0\n"
    "ds_read_b128 v[164:167], v0 offset:18432\n"
    "s_waitcnt lgkmcnt(5)\n"
    "v_mfma_f32_16x16x32_bf16 v[148:151], v[168:171], v[16:19], 0\n"
    "ds_read_b128 v[168:171], v0 offset:19456\n"
    "s_waitcnt lgkmcnt(5)\n"
    "v_mfma_f32_16x16x32_bf16 v[152:155], v[172:175], v[16:19], 0\n"
    "ds_read_b128 v[172:175], v0 offset:20480\n"
    "v_mfma_f32_16x16x32_bf16 v[160:163], v[16:19], v[16:19], 0\n"
    "s_waitcnt vmcnt(18) lgkmcnt(5)\n"
    "v_mfma_f32_16x16x32_bf16 v[96:99], v[176:179], v[20:23], v[96:99]\n"
    "ds_read_b128 v[176:179], v0 offset:21504\n"
    "s_waitcnt lgkmcnt(5)\n"
    "v_mfma_f32_16x16x32_bf16 v[100:103], v[180:183], v[20:23], v[100:103]\n"
    "ds_read_b128 v[180:183], v0 offset:22528\n"
    "s_waitcnt lgkmcnt(5)\n"
    "v_mfma_f32_16x16x32_bf16 v[104:107], v[184:187], v[20:23], v[104:107]\n"
    "ds_read_b128 v[184:187], v0 offset:23552\n"
    "s_waitcnt lgkmcnt(5)\n"
    "v_mfma_f32_16x16x32_bf16 v[108:111], v[164:167], v[20:23], v[108:111]\n"
    "ds_read_b128 v[164:167], v0 offset:24576\n"
    "s_waitcnt lgkmcnt(5)\n"
    "v_mfma_f32_16x16x32_bf16 v[112:115], v[168:171], v[20:23], v[112:115]\n"
    "ds_read_b128 v[168:171], v0 offset:25600\n"
    "s_waitcnt lgkmcnt(5)\n"
    "v_mfma_f32_16x16x32_bf16 v[116:119], v[172:175], v[20:23], v[116:119]\n"
    "ds_read_b128 v[172:175], v0 offset:26624\n"
    "s_waitcnt lgkmcnt(5)\n"
    "v_mfma_f32_16x16x32_bf16 v[120:123], v[176:179], v[20:23], v[120:123]\n"
    "ds_read_b128 v[176:179], v0 offset:27648\n"
    "s_waitcnt lgkmcnt(5)\n"
    "v_mfma_f32_16x16x32_bf16 v[124:127], v[180:183], v[20:23], v[124:127]\n"
    "ds_read_b128 v[180:183], v0 offset:28672\n"
    "v_mfma_f32_16x16x32_bf16 v[156:159], v[188:191], v[20:23], v[156:159]\n"
    "s_waitcnt lgkmcnt(5)\n"
    "v_mfma_f32_16x16x32_bf16 v[128:131], v[184:187], v[20:23], v[128:131]\n"
    "ds_read_b128 v[184:187], v0 offset:29696\n"
    "s_waitcnt lgkmcnt(5)\n"
    "v_mfma_f32_16x16x32_bf16 v[132:135], v[164:167], v[20:23], v[132:135]\n"
    "ds_read_b128 v[164:167], v0 offset:30720\n"
    "s_waitcnt lgkmcnt(5)\n"
    "v_mfma_f32_16x16x32_bf16 v[136:139], v[168:171], v[20:23], v[136:139]\n"
    "ds_read_b128 v[168:171], v0 offset:31744\n"
    "s_waitcnt lgkmcnt(5)\n"
    "v_mfma_f32_16x16x32_bf16 v[140:143], v[172:175], v[20:23], v[140:143]\n"
    "ds_read_b128 v[172:175], v0 offset:32768\n"
    "s_waitcnt lgkmcnt(5)\n"
    "v_mfma_f32_16x16x32_bf16 v[144:147], v[176:179], v[20:23], v[144:147]\n"
    "ds_read_b128 v[176:179], v0 offset:33792\n"
    "s_waitcnt lgkmcnt(5)\n"
    "v_mfma_f32_16x16x32_bf16 v[148:151], v[180:183], v[20:23], v[148:151]\n"
    "ds_read_b128 v[180:183], v0 offset:34816\n"
    "s_waitcnt lgkmcnt(5)\n"
    "v_mfma_f32_16x16x32_bf16 v[152:155], v[184:187], v[20:23], v[152:155]\n"
    "ds_read_b128 v[184:187], v0 offset:35840\n"
    "v_mfma_f32_16x16x32_bf16 v[160:163], v[20:23], v[20:23], v[160:163]\n"
    "s_waitcnt vmcnt(17) lgkmcnt(5)\n"
    "v_mfma_f32_16x16x32_bf16 v[96:99], v[164:167], v[24:27], v[96:99]\n"
    "s_barrier\n"
    "ds_read_b128 v[164:167], v0 offset:36864\n"
    "s_waitcnt lgkmcnt(5)\n"
    "v_mfma_f32_16x16x32_bf16 v[100:103], v[168:171], v[24:27], v[100:103]\n"
    "ds_read_b128 v[168:171], v0 offset:37888\n"
    "s_add_u32 m0, s61, 0\n"
    "s_add_u32 s51, s50, 153600\n"
    "buffer_load_dwordx4 v0, s[40:43], s51 offen offset:0 lds\n"
    "s_waitcnt lgkmcnt(5)\n"
    "v_mfma_f32_16x16x32_bf16 v[104:107], v[172:175], v[24:27], v[104:107]\n"
    "ds_read_b128 v[172:175], v0 offset:38912\n"
    "s_waitcnt lgkmcnt(5)\n"
    "v_mfma_f32_16x16x32_bf16 v[108:111], v[176:179], v[24:27], v[108:111]\n"
    "ds_read_b128 v[176:179], v0 offset:39936\n"
    "s_waitcnt lgkmcnt(5)\n"
    "v_mfma_f32_16x16x32_bf16 v[112:115], v[180:183], v[24:27], v[112:115]\n"
    "ds_read_b128 v[180:183], v0 offset:40960\n"
    "buffer_load_dwordx4 v0, s[40:43], s51 offen offset:1024 lds\n"
    "s_waitcnt lgkmcnt(5)\n"
    "v_mfma_f32_16x16x32_bf16 v[116:119], v[184:187], v[24:27], v[116:119]\n"
    "ds_read_b128 v[184:187], v0 offset:41984\n"
    "s_waitcnt lgkmcnt(5)\n"
    "v_mfma_f32_16x16x32_bf16 v[120:123], v[164:167], v[24:27], v[120:123]\n"
    "ds_read_b128 v[164:167], v0 offset:43008\n"
    "s_waitcnt lgkmcnt(5)\n"
    "v_mfma_f32_16x16x32_bf16 v[124:127], v[168:171], v[24:27], v[124:127]\n"
    "ds_read_b128 v[168:171], v0 offset:44032\n"
    "buffer_load_dwordx4 v0, s[40:43], s51 offen offset:2048 lds\n"
    "v_mfma_f32_16x16x32_bf16 v[156:159], v[188:191], v[24:27], v[156:159]\n"
    "s_waitcnt lgkmcnt(5)\n"
    "v_mfma_f32_16x16x32_bf16 v[128:131], v[172:175], v[24:27], v[128:131]\n"
    "ds_read_b128 v[172:175], v0 offset:45056\n"
    "s_waitcnt lgkmcnt(5)\n"
    "v_mfma_f32_16x16x32_bf16 v[132:135], v[176:179], v[24:27], v[132:135]\n"
    "ds_read_b128 v[176:179], v0 offset:46080\n"
    "s_waitcnt lgkmcnt(5)\n"
    "v_mfma_f32_16x16x32_bf16 v[136:139], v[180:183], v[24:27], v[136:139]\n"
    "ds_read_b128 v[180:183], v0 offset:47104\n"
    "buffer_load_dwordx4 v0, s[40:43], s51 offen offset:3072 lds\n"
    "s_waitcnt lgkmcnt(5)\n"
    "v_mfma_f32_16x16x32_bf16 v[140:143], v[184:187], v[24:27], v[140:143]\n"
    "ds_read_b128 v[184:187], v0 offset:48128\n"
    "s_waitcnt lgkmcnt(5)\n"
    "v_mfma_f32_16x16x32_bf16 v[144:147], v[164:167], v[24:27], v[144:147]\n"
    "ds_read_b128 v[164:167], v0 offset:49152\n"
    "s_waitcnt lgkmcnt(5)\n"
    "v_mfma_f32_16x16x32_bf16 v[148:151], v[168:171], v[24:27], v[148:151]\n"
    "ds_read_b128 v[168:171], v0 offset:50176\n"
    "s_add_u32 m0, s61, 4096\n"
    "s_add_u32 s51, s50, 157696\n"
    "buffer_load_dwordx4 v0, s[40:43], s51 offen offset:0 lds\n"
    "s_waitcnt lgkmcnt(5)\n"
    "v_mfma_f32_16x16x32_bf16 v[152:155], v[172:175], v[24:27], v[152:155]\n"
    "ds_read_b128 v[172:175], v0 offset:51200\n"
    "v_mfma_f32_16x16x32_bf16 v[160:163], v[24:27], v[24:27], v[160:163]\n"
    "s_waitcnt vmcnt(21) lgkmcnt(5)\n"
    "v_mfma_f32_16x16x32_bf16 v[96:99], v[176:179], v[28:31], v[96:99]\n"
    "ds_read_b128 v[176:179], v0 offset:52224\n"
    "s_waitcnt lgkmcnt(5)\n"
    "v_mfma_f32_16x16x32_bf16 v[100:103], v[180:183], v[28:31], v[100:103]\n"
    "ds_read_b128 v[180:183], v0 offset:53248\n"
    "buffer_load_dwordx4 v0, s[40:43], s51 offen offset:1024 lds\n"
    "s_waitcnt lgkmcnt(5)\n"
    "v_mfma_f32_16x16x32_bf16 v[104:107], v[184:187], v[28:31], v[104:107]\n"
    "ds_read_b128 v[184:187], v0 offset:54272\n"
    "s_waitcnt lgkmcnt(5)\n"
    "v_mfma_f32_16x16x32_bf16 v[108:111], v[164:167], v[28:31], v[108:111]\n"
    "ds_read_b128 v[164:167], v0 offset:55296\n"
    "s_waitcnt lgkmcnt(5)\n"
    "v_mfma_f32_16x16x32_bf16 v[112:115], v[168:171], v[28:31], v[112:115]\n"
    "ds_read_b128 v[168:171], v0 offset:56320\n"
    "buffer_load_dwordx4 v0, s[40:43], s51 offen offset:2048 lds\n"
    "s_waitcnt lgkmcnt(5)\n"
    "v_mfma_f32_16x16x32_bf16 v[116:119], v[172:175], v[28:31], v[116:119]\n"
    "ds_read_b128 v[172:175], v0 offset:57344\n"
    "s_waitcnt lgkmcnt(5)\n"
    "v_mfma_f32_16x16x32_bf16 v[120:123], v[176:179], v[28:31], v[120:123]\n"
    "ds_read_b128 v[176:179], v0 offset:58368\n"
    "s_waitcnt lgkmcnt(5)\n"
    "v_mfma_f32_16x16x32_bf16 v[124:127], v[180:183], v[28:31], v[124:127]\n"
    "ds_read_b128 v[180:183], v0 offset:59392\n"
    "buffer_load_dwordx4 v0, s[40:43], s51 offen offset:3072 lds\n"
    "v_mfma_f32_16x16x32_bf16 v[156:159], v[188:191], v[28:31], v[156:159]\n"
    "s_waitcnt lgkmcnt(5)\n"
    "v_mfma_f32_16x16x32_bf16 v[128:131], v[184:187], v[28:31], v[128:131]\n"
    "ds_read_b128 v[184:187], v0 offset:60416\n"
    "s_waitcnt lgkmcnt(5)\n"
    "v_mfma_f32_16x16x32_bf16 v[132:135], v[164:167], v[28:31], v[132:135]\n"
    "ds_read_b128 v[164:167], v6 offset:0\n"
    "s_waitcnt lgkmcnt(5)\n"
    "v_mfma_f32_16x16x32_bf16 v[136:139], v[168:171], v[28:31], v[136:139]\n"
    "ds_read_b128 v[168:171], v6 offset:1024\n"
    "s_waitcnt lgkmcnt(5)\n"
    "v_mfma_f32_16x16x32_bf16 v[140:143], v[172:175], v[28:31], v[140:143]\n"
    "ds_read_b128 v[172:175], v6 offset:2048\n"
    "s_waitcnt lgkmcnt(5)\n"
    "v_mfma_f32_16x16x32_bf16 v[144:147], v[176:179], v[28:31], v[144:147]\n"
    "ds_read_b128 v[176:179], v6 offset:3072\n"
    "s_waitcnt lgkmcnt(5)\n"
    "v_mfma_f32_16x16x32_bf16 v[148:151], v[180:183], v[28:31], v[148:151]\n"
    "ds_read_b128 v[180:183], v6 offset:4096\n"
    "s_waitcnt lgkmcnt(5)\n"
    "v_mfma_f32_16x16x32_bf16 v[152:155], v[184:187], v[28:31], v[152:155]\n"
    "ds_read_b128 v[184:187], v6 offset:5120\n"
    "v_mfma_f32_16x16x32_bf16 v[160:163], v[28:31], v[28:31], v[160:163]\n"
    "s_waitcnt vmcnt(23) lgkmcnt(5)\n"
    "v_mfma_f32_16x16x32_bf16 v[96:99], v[164:167], v[32:35], v[96:99]\n"
    "s_barrier\n"
    "ds_read_b128 v[164:167], v6 offset:6144\n"
    "s_waitcnt lgkmcnt(5)\n"
    "v_mfma_f32_16x16x32_bf16 v[100:103], v[168:171], v[32:35], v[100:103]\n"
    "ds_read_b128 v[168:171], v6 offset:7168\n"
    "s_add_u32 m0, s61, 30720\n"
    "s_add_u32 s51, s50, 184320\n"
    "buffer_load_dwordx4 v0, s[40:43], s51 offen offset:0 lds\n"
    "s_waitcnt lgkmcnt(5)\n"
    "v_mfma_f32_16x16x32_bf16 v[104:107], v[172:175], v[32:35], v[104:107]\n"
    "ds_read_b128 v[172:175], v6 offset:8192\n"
    "s_waitcnt lgkmcnt(5)\n"
    "v_mfma_f32_16x16x32_bf16 v[108:111], v[176:179], v[32:35], v[108:111]\n"
    "ds_read_b128 v[176:179], v6 offset:9216\n"
    "s_waitcnt lgkmcnt(5)\n"
    "v_mfma_f32_16x16x32_bf16 v[112:115], v[180:183], v[32:35], v[112:115]\n"
    "ds_read_b128 v[180:183], v6 offset:10240\n"
    "buffer_load_dwordx4 v0, s[40:43], s51 offen offset:1024 lds\n"
    "s_waitcnt lgkmcnt(5)\n"
    "v_mfma_f32_16x16x32_bf16 v[116:119], v[184:187], v[32:35], v[116:119]\n"
    "ds_read_b128 v[184:187], v6 offset:11264\n"
    "s_waitcnt lgkmcnt(5)\n"
    "v_mfma_f32_16x16x32_bf16 v[120:123], v[164:167], v[32:35], v[120:123]\n"
    "ds_read_b128 v[164:167], v6 offset:12288\n"
    "s_waitcnt lgkmcnt(5)\n"
    "v_mfma_f32_16x16x32_bf16 v[124:127], v[168:171], v[32:35], v[124:127]\n"
    "ds_read_b128 v[168:171], v6 offset:13312\n"
    "buffer_load_dwordx4 v0, s[40:43], s51 offen offset:2048 lds\n"
    "v_mfma_f32_16x16x32_bf16 v[156:159], v[188:191], v[32:35], v[156:159]\n"
    "s_waitcnt lgkmcnt(5)\n"
    "v_mfma_f32_16x16x32_bf16 v[128:131], v[172:175], v[32:35], v[128:131]\n"
    "ds_read_b128 v[172:175], v6 offset:14336\n"
    "s_waitcnt lgkmcnt(5)\n"
    "v_mfma_f32_16x16x32_bf16 v[132:135], v[176:179], v[32:35], v[132:135]\n"
    "ds_read_b128 v[176:179], v6 offset:15360\n"
    "s_waitcnt lgkmcnt(5)\n"
    "v_mfma_f32_16x16x32_bf16 v[136:139], v[180:183], v[32:35], v[136:139]\n"
    "ds_read_b128 v[180:183], v6 offset:16384\n"
    "buffer_load_dwordx4 v0, s[40:43], s51 offen offset:3072 lds\n"
    "s_waitcnt lgkmcnt(5)\n"
    "v_mfma_f32_16x16x32_bf16 v[140:143], v[184:187], v[32:35], v[140:143]\n"
    "ds_read_b128 v[184:187], v6 offset:17408\n"
    "s_waitcnt lgkmcnt(5)\n"
    "v_mfma_f32_16x16x32_bf16 v[144:147], v[164:167], v[32:35], v[144:147]\n"
    "ds_read_b128 v[164:167], v6 offset:18432\n"
    "s_waitcnt lgkmcnt(5)\n"
    "v_mfma_f32_16x16x32_bf16 v[148:151], v[168:171], v[32:35], v[148:151]\n"
    "ds_read_b128 v[168:171], v6 offset:19456\n"
    "s_add_u32 m0, s61, 34816\n"
    "s_add_u32 s51, s50, 188416\n"
    "buffer_load_dwordx4 v0, s[40:43], s51 offen offset:0 lds\n"
    "s_waitcnt lgkmcnt(5)\n"
    "v_mfma_f32_16x16x32_bf16 v[152:155], v[172:175], v[32:35], v[152:155]\n"
    "ds_read_b128 v[172:175], v6 offset:20480\n"
    "v_mfma_f32_16x16x32_bf16 v[160:163], v[32:35], v[32:35], v[160:163]\n"
    "s_waitcnt vmcnt(27) lgkmcnt(5)\n"
    "v_mfma_f32_16x16x32_bf16 v[96:99], v[176:179], v[36:39], v[96:99]\n"
    "ds_read_b128 v[176:179], v6 offset:21504\n"
    "s_waitcnt lgkmcnt(5)\n"
    "v_mfma_f32_16x16x32_bf16 v[100:103], v[180:183], v[36:39], v[100:103]\n"
    "ds_read_b128 v[180:183], v6 offset:22528\n"
    "buffer_load_dwordx4 v0, s[40:43], s51 offen offset:1024 lds\n"
    "s_waitcnt lgkmcnt(5)\n"
    "v_mfma_f32_16x16x32_bf16 v[104:107], v[184:187], v[36:39], v[104:107]\n"
    "ds_read_b128 v[184:187], v6 offset:23552\n"
    "s_waitcnt lgkmcnt(5)\n"
    "v_mfma_f32_16x16x32_bf16 v[108:111], v[164:167], v[36:39], v[108:111]\n"
    "ds_read_b128 v[164:167], v6 offset:24576\n"
    "s_waitcnt lgkmcnt(5)\n"
    "v_mfma_f32_16x16x32_bf16 v[112:115], v[168:171], v[36:39], v[112:115]\n"
    "ds_read_b128 v[168:171], v6 offset:25600\n"
    "buffer_load_dwordx4 v0, s[40:43], s51 offen offset:2048 lds\n"
    "s_waitcnt lgkmcnt(5)\n"
    "v_mfma_f32_16x16x32_bf16 v[116:119], v[172:175], v[36:39], v[116:119]\n"
    "ds_read_b128 v[172:175], v6 offset:26624\n"
    "s_waitcnt lgkmcnt(5)\n"
    "v_mfma_f32_16x16x32_bf16 v[120:123], v[176:179], v[36:39], v[120:123]\n"
    "ds_read_b128 v[176:179], v6 offset:27648\n"
    "s_waitcnt lgkmcnt(5)\n"
    "v_mfma_f32_16x16x32_bf16 v[124:127], v[180:183], v[36:39], v[124:127]\n"
    "ds_read_b128 v[180:183], v6 offset:28672\n"
    "buffer_load_dwordx4 v0, s[40:43], s51 offen offset:3072 lds\n"
    "v_mfma_f32_16x16x32_bf16 v[156:159], v[188:191], v[36:39], v[156:159]\n"
    "s_waitcnt lgkmcnt(5)\n"
    "v_mfma_f32_16x16x32_bf16 v[128:131], v[184:187], v[36:39], v[128:131]\n"
    "ds_read_b128 v[184:187], v6 offset:29696\n"
    "s_waitcnt lgkmcnt(5)\n"
    "v_mfma_f32_16x16x32_bf16 v[132:135], v[164:167], v[36:39], v[132:135]\n"
    "ds_read_b128 v[164:167], v6 offset:30720\n"
    "s_waitcnt lgkmcnt(5)\n"
    "v_mfma_f32_16x16x32_bf16 v[136:139], v[168:171], v[36:39], v[136:139]\n"
    "ds_read_b128 v[168:171], v6 offset:31744\n"
    "s_waitcnt lgkmcnt(5)\n"
    "v_mfma_f32_16x16x32_bf16 v[140:143], v[172:175], v[36:39], v[140:143]\n"
    "ds_read_b128 v[172:175], v6 offset:32768\n"
    "s_waitcnt lgkmcnt(5)\n"
    "v_mfma_f32_16x16x32_bf16 v[144:147], v[176:179], v[36:39], v[144:147]\n"
    "ds_read_b128 v[176:179], v6 offset:33792\n"
    "s_waitcnt lgkmcnt(5)\n"
    "v_mfma_f32_16x16x32_bf16 v[148:151], v[180:183], v[36:39], v[148:151]\n"
    "ds_read_b128 v[180:183], v6 offset:34816\n"
    "s_waitcnt lgkmcnt(5)\n"
    "v_mfma_f32_16x16x32_bf16 v[152:155], v[184:187], v[36:39], v[152:155]\n"
    "ds_read_b128 v[184:187], v6 offset:35840\n"
    "v_mfma_f32_16x16x32_bf16 v[160:163], v[36:39], v[36:39], v[160:163]\n"
    "s_waitcnt vmcnt(29) lgkmcnt(5)\n"
    "v_mfma_f32_16x16x32_bf16 v[96:99], v[164:167], v[40:43], v[96:99]\n"
    "s_barrier\n"
    "ds_read_b128 v[164:167], v6 offset:36864\n"
    "s_waitcnt lgkmcnt(5)\n"
    "v_mfma_f32_16x16x32_bf16 v[100:103], v[168:171], v[40:43], v[100:103]\n"
    "ds_read_b128 v[168:171], v6 offset:37888\n"
    "s_add_u32 m0, s61, 61440\n"
    "s_add_u32 s51, s50, 215040\n"
    "buffer_load_dwordx4 v0, s[40:43], s51 offen offset:0 lds\n"
    "s_waitcnt lgkmcnt(5)\n"
    "v_mfma_f32_16x16x32_bf16 v[104:107], v[172:175], v[40:43], v[104:107]\n"
    "ds_read_b128 v[172:175], v6 offset:38912\n"
    "s_waitcnt lgkmcnt(5)\n"
    "v_mfma_f32_16x16x32_bf16 v[108:111], v[176:179], v[40:43], v[108:111]\n"
    "ds_read_b128 v[176:179], v6 offset:39936\n"
    "s_waitcnt lgkmcnt(5)\n"
    "v_mfma_f32_16x16x32_bf16 v[112:115], v[180:183], v[40:43], v[112:115]\n"
    "ds_read_b128 v[180:183], v6 offset:40960\n"
    "buffer_load_dwordx4 v0, s[40:43], s51 offen offset:1024 lds\n"
    "s_waitcnt lgkmcnt(5)\n"
    "v_mfma_f32_16x16x32_bf16 v[116:119], v[184:187], v[40:43], v[116:119]\n"
    "ds_read_b128 v[184:187], v6 offset:41984\n"
    "s_waitcnt lgkmcnt(5)\n"
    "v_mfma_f32_16x16x32_bf16 v[120:123], v[164:167], v[40:43], v[120:123]\n"
    "ds_read_b128 v[164:167], v6 offset:43008\n"
    "s_waitcnt lgkmcnt(5)\n"
    "v_mfma_f32_16x16x32_bf16 v[124:127], v[168:171], v[40:43], v[124:127]\n"
    "ds_read_b128 v[168:171], v6 offset:44032\n"
    "buffer_load_dwordx4 v0, s[40:43], s51 offen offset:2048 lds\n"
    "v_mfma_f32_16x16x32_bf16 v[156:159], v[188:191], v[40:43], v[156:159]\n"
    "s_waitcnt lgkmcnt(5)\n"
    "v_mfma_f32_16x16x32_bf16 v[128:131], v[172:175], v[40:43], v[128:131]\n"
    "ds_read_b128 v[172:175], v6 offset:45056\n"
    "s_waitcnt lgkmcnt(5)\n"
    "v_mfma_f32_16x16x32_bf16 v[132:135], v[176:179], v[40:43], v[132:135]\n"
    "ds_read_b128 v[176:179], v6 offset:46080\n"
    "s_waitcnt lgkmcnt(5)\n"
    "v_mfma_f32_16x16x32_bf16 v[136:139], v[180:183], v[40:43], v[136:139]\n"
    "ds_read_b128 v[180:183], v6 offset:47104\n"
    "buffer_load_dwordx4 v0, s[40:43], s51 offen offset:3072 lds\n"
    "s_waitcnt lgkmcnt(5)\n"
    "v_mfma_f32_16x16x32_bf16 v[140:143], v[184:187], v[40:43], v[140:143]\n"
    "ds_read_b128 v[184:187], v6 offset:48128\n"
    "s_waitcnt lgkmcnt(5)\n"
    "v_mfma_f32_16x16x32_bf16 v[144:147], v[164:167], v[40:43], v[144:147]\n"
    "ds_read_b128 v[164:167], v6 offset:49152\n"
    "s_waitcnt lgkmcnt(5)\n"
    "v_mfma_f32_16x16x32_bf16 v[148:151], v[168:171], v[40:43], v[148:151]\n"
    "ds_read_b128 v[168:171], v6 offset:50176\n"
    "s_add_u32 m0, s61, 65536\n"
    "s_add_u32 s51, s50, 219136\n"
    "buffer_load_dwordx4 v0, s[40:43], s51 offen offset:0 lds\n"
    "s_waitcnt lgkmcnt(5)\n"
    "v_mfma_f32_16x16x32_bf16 v[152:155], v[172:175], v[40:43], v[152:155]\n"
    "ds_read_b128 v[172:175], v6 offset:51200\n"
    "v_mfma_f32_16x16x32_bf16 v[160:163], v[40:43], v[40:43], v[160:163]\n"
    "s_waitcnt vmcnt(33) lgkmcnt(5)\n"
    "v_mfma_f32_16x16x32_bf16 v[96:99], v[176:179], v[44:47], v[96:99]\n"
    "ds_read_b128 v[176:179], v6 offset:52224\n"
    "s_waitcnt lgkmcnt(5)\n"
    "v_mfma_f32_16x16x32_bf16 v[100:103], v[180:183], v[44:47], v[100:103]\n"
    "ds_read_b128 v[180:183], v6 offset:53248\n"
    "buffer_load_dwordx4 v0, s[40:43], s51 offen offset:1024 lds\n"
    "s_waitcnt lgkmcnt(5)\n"
    "v_mfma_f32_16x16x32_bf16 v[104:107], v[184:187], v[44:47], v[104:107]\n"
    "ds_read_b128 v[184:187], v6 offset:54272\n"
    "s_waitcnt lgkmcnt(5)\n"
    "v_mfma_f32_16x16x32_bf16 v[108:111], v[164:167], v[44:47], v[108:111]\n"
    "ds_read_b128 v[164:167], v6 offset:55296\n"
    "s_waitcnt lgkmcnt(5)\n"
    "v_mfma_f32_16x16x32_bf16 v[112:115], v[168:171], v[44:47], v[112:115]\n"
    "ds_read_b128 v[168:171], v6 offset:56320\n"
    "buffer_load_dwordx4 v0, s[40:43], s51 offen offset:2048 lds\n"
    "s_waitcnt lgkmcnt(5)\n"
    "v_mfma_f32_16x16x32_bf16 v[116:119], v[172:175], v[44:47], v[116:119]\n"
    "ds_read_b128 v[172:175], v6 offset:57344\n"
    "s_waitcnt lgkmcnt(5)\n"
    "v_mfma_f32_16x16x32_bf16 v[120:123], v[176:179], v[44:47], v[120:123]\n"
    "ds_read_b128 v[176:179], v6 offset:58368\n"
    "s_waitcnt lgkmcnt(5)\n"
    "v_mfma_f32_16x16x32_bf16 v[124:127], v[180:183], v[44:47], v[124:127]\n"
    "ds_read_b128 v[180:183], v6 offset:59392\n"
    "buffer_load_dwordx4 v0, s[40:43], s51 offen offset:3072 lds\n"
    "v_mfma_f32_16x16x32_bf16 v[156:159], v[188:191], v[44:47], v[156:159]\n"
    "s_waitcnt lgkmcnt(5)\n"
    "v_mfma_f32_16x16x32_bf16 v[128:131], v[184:187], v[44:47], v[128:131]\n"
    "ds_read_b128 v[184:187], v6 offset:60416\n"
    "s_waitcnt lgkmcnt(5)\n"
    "v_mfma_f32_16x16x32_bf16 v[132:135], v[164:167], v[44:47], v[132:135]\n"
    "ds_read_b128 v[164:167], v7 offset:0\n"
    "s_waitcnt lgkmcnt(5)\n"
    "v_mfma_f32_16x16x32_bf16 v[136:139], v[168:171], v[44:47], v[136:139]\n"
    "ds_read_b128 v[168:171], v7 offset:1024\n"
    "s_waitcnt lgkmcnt(5)\n"
    "v_mfma_f32_16x16x32_bf16 v[140:143], v[172:175], v[44:47], v[140:143]\n"
    "ds_read_b128 v[172:175], v7 offset:2048\n"
    "s_waitcnt lgkmcnt(5)\n"
    "v_mfma_f32_16x16x32_bf16 v[144:147], v[176:179], v[44:47], v[144:147]\n"
    "ds_read_b128 v[176:179], v7 offset:3072\n"
    "s_waitcnt lgkmcnt(5)\n"
    "v_mfma_f32_16x16x32_bf16 v[148:151], v[180:183], v[44:47], v[148:151]\n"
    "ds_read_b128 v[180:183], v7 offset:4096\n"
    "s_waitcnt lgkmcnt(5)\n"
    "v_mfma_f32_16x16x32_bf16 v[152:155], v[184:187], v[44:47], v[152:155]\n"
    "ds_read_b128 v[184:187], v7 offset:5120\n"
    "v_mfma_f32_16x16x32_bf16 v[160:163], v[44:47], v[44:47], v[160:163]\n"
    "s_waitcnt vmcnt(35) lgkmcnt(5)\n"
    "v_mfma_f32_16x16x32_bf16 v[96:99], v[164:167], v[48:51], v[96:99]\n"
    "s_barrier\n"
    "ds_read_b128 v[164:167], v7 offset:6144\n"
    "s_waitcnt lgkmcnt(5)\n"
    "v_mfma_f32_16x16x32_bf16 v[100:103], v[168:171], v[48:51], v[100:103]\n"
    "ds_read_b128 v[168:171], v7 offset:7168\n"
    "s_add_u32 m0, s61, 92160\n"
    "s_add_u32 s51, s50, 245760\n"
    "buffer_load_dwordx4 v0, s[40:43], s51 offen offset:0 lds\n"
    "s_waitcnt lgkmcnt(5)\n"
    "v_mfma_f32_16x16x32_bf16 v[104:107], v[172:175], v[48:51], v[104:107]\n"
    "ds_read_b128 v[172:175], v7 offset:8192\n"
    "s_waitcnt lgkmcnt(5)\n"
    "v_mfma_f32_16x16x32_bf16 v[108:111], v[176:179], v[48:51], v[108:111]\n"
    "ds_read_b128 v[176:179], v7 offset:9216\n"
    "s_waitcnt lgkmcnt(5)\n"
    "v_mfma_f32_16x16x32_bf16 v[112:115], v[180:183], v[48:51], v[112:115]\n"
    "ds_read_b128 v[180:183], v7 offset:10240\n"
    "buffer_load_dwordx4 v0, s[40:43], s51 offen offset:1024 lds\n"
    "s_waitcnt lgkmcnt(5)\n"
    "v_mfma_f32_16x16x32_bf16 v[116:119], v[184:187], v[48:51], v[116:119]\n"
    "ds_read_b128 v[184:187], v7 offset:11264\n"
    "s_waitcnt lgkmcnt(5)\n"
    "v_mfma_f32_16x16x32_bf16 v[120:123], v[164:167], v[48:51], v[120:123]\n"
    "ds_read_b128 v[164:167], v7 offset:12288\n"
    "s_waitcnt lgkmcnt(5)\n"
    "v_mfma_f32_16x16x32_bf16 v[124:127], v[168:171], v[48:51], v[124:127]\n"
    "ds_read_b128 v[168:171], v7 offset:13312\n"
    "buffer_load_dwordx4 v0, s[40:43], s51 offen offset:2048 lds\n"
    "v_mfma_f32_16x16x32_bf16 v[156:159], v[188:191], v[48:51], v[156:159]\n"
    "s_waitcnt lgkmcnt(5)\n"
    "v_mfma_f32_16x16x32_bf16 v[128:131], v[172:175], v[48:51], v[128:131]\n"
    "ds_read_b128 v[172:175], v7 offset:14336\n"
    "s_waitcnt lgkmcnt(5)\n"
    "v_mfma_f32_16x16x32_bf16 v[132:135], v[176:179], v[48:51], v[132:135]\n"
    "ds_read_b128 v[176:179], v7 offset:15360\n"
    "s_waitcnt lgkmcnt(5)\n"
    "v_mfma_f32_16x16x32_bf16 v[136:139], v[180:183], v[48:51], v[136:139]\n"
    "ds_read_b128 v[180:183], v7 offset:16384\n"
    "buffer_load_dwordx4 v0, s[40:43], s51 offen offset:3072 lds\n"
    "s_waitcnt lgkmcnt(5)\n"
    "v_mfma_f32_16x16x32_bf16 v[140:143], v[184:187], v[48:51], v[140:143]\n"
    "ds_read_b128 v[184:187], v7 offset:17408\n"
    "s_waitcnt lgkmcnt(5)\n"
    "v_mfma_f32_16x16x32_bf16 v[144:147], v[164:167], v[48:51], v[144:147]\n"
    "ds_read_b128 v[164:167], v7 offset:18432\n"
    "s_waitcnt lgkmcnt(5)\n"
    "v_mfma_f32_16x16x32_bf16 v[148:151], v[168:171], v[48:51], v[148:151]\n"
    "ds_read_b128 v[168:171], v7 offset:19456\n"
    "s_add_u32 m0, s61, 96256\n"
    "s_add_u32 s51, s50, 249856\n"
    "buffer_load_dwordx4 v0, s[40:43], s51 offen offset:0 lds\n"
    "s_waitcnt lgkmcnt(5)\n"
    "v_mfma_f32_16x16x32_bf16 v[152:155], v[172:175], v[48:51], v[152:155]\n"
    "ds_read_b128 v[172:175], v7 offset:20480\n"
    "v_mfma_f32_16x16x32_bf16 v[160:163], v[48:51], v[48:51], v[160:163]\n"
    "s_waitcnt vmcnt(39) lgkmcnt(5)\n"
    "v_mfma_f32_16x16x32_bf16 v[96:99], v[176:179], v[52:55], v[96:99]\n"
    "ds_read_b128 v[176:179], v7 offset:21504\n"
    "s_waitcnt lgkmcnt(5)\n"
    "v_mfma_f32_16x16x32_bf16 v[100:103], v[180:183], v[52:55], v[100:103]\n"
    "ds_read_b128 v[180:183], v7 offset:22528\n"
    "buffer_load_dwordx4 v0, s[40:43], s51 offen offset:1024 lds\n"
    "s_waitcnt lgkmcnt(5)\n"
    "v_mfma_f32_16x16x32_bf16 v[104:107], v[184:187], v[52:55], v[104:107]\n"
    "ds_read_b128 v[184:187], v7 offset:23552\n"
    "s_waitcnt lgkmcnt(5)\n"
    "v_mfma_f32_16x16x32_bf16 v[108:111], v[164:167], v[52:55], v[108:111]\n"
    "ds_read_b128 v[164:167], v7 offset:24576\n"
    "s_waitcnt lgkmcnt(5)\n"
    "v_mfma_f32_16x16x32_bf16 v[112:115], v[168:171], v[52:55], v[112:115]\n"
    "ds_read_b128 v[168:171], v7 offset:25600\n"
    "buffer_load_dwordx4 v0, s[40:43], s51 offen offset:2048 lds\n"
    "s_waitcnt lgkmcnt(5)\n"
    "v_mfma_f32_16x16x32_bf16 v[116:119], v[172:175], v[52:55], v[116:119]\n"
    "ds_read_b128 v[172:175], v7 offset:26624\n"
    "s_waitcnt lgkmcnt(5)\n"
    "v_mfma_f32_16x16x32_bf16 v[120:123], v[176:179], v[52:55], v[120:123]\n"
    "ds_read_b128 v[176:179], v7 offset:27648\n"
    "s_waitcnt lgkmcnt(5)\n"
    "v_mfma_f32_16x16x32_bf16 v[124:127], v[180:183], v[52:55], v[124:127]\n"
    "ds_read_b128 v[180:183], v7 offset:28672\n"
    "buffer_load_dwordx4 v0, s[40:43], s51 offen offset:3072 lds\n"
    "v_mfma_f32_16x16x32_bf16 v[156:159], v[188:191], v[52:55], v[156:159]\n"
    "s_waitcnt lgkmcnt(5)\n"
    "v_mfma_f32_16x16x32_bf16 v[128:131], v[184:187], v[52:55], v[128:131]\n"
    "ds_read_b128 v[184:187], v7 offset:29696\n"
    "s_waitcnt lgkmcnt(5)\n"
    "v_mfma_f32_16x16x32_bf16 v[132:135], v[164:167], v[52:55], v[132:135]\n"
    "s_waitcnt vmcnt(16)\n"
    "s_barrier\n"
    "ds_read_b128 v[164:167], v0 offset:0\n"
    "s_waitcnt lgkmcnt(5)\n"
    "v_mfma_f32_16x16x32_bf16 v[136:139], v[168:171], v[52:55], v[136:139]\n"
    "ds_read_b128 v[168:171], v0 offset:1024\n"
    "s_waitcnt lgkmcnt(5)\n"
    "v_mfma_f32_16x16x32_bf16 v[140:143], v[172:175], v[52:55], v[140:143]\n"
    "ds_read_b128 v[172:175], v0 offset:2048\n"
    "s_waitcnt lgkmcnt(5)\n"
    "v_mfma_f32_16x16x32_bf16 v[144:147], v[176:179], v[52:55], v[144:147]\n"
    "ds_read_b128 v[176:179], v0 offset:3072\n"
    "s_waitcnt lgkmcnt(5)\n"
    "v_mfma_f32_16x16x32_bf16 v[148:151], v[180:183], v[52:55], v[148:151]\n"
    "ds_read_b128 v[180:183], v0 offset:4096\n"
    "s_waitcnt lgkmcnt(5)\n"
    "v_mfma_f32_16x16x32_bf16 v[152:155], v[184:187], v[52:55], v[152:155]\n"
    "ds_read_b128 v[184:187], v0 offset:5120\n"
    "v_mfma_f32_16x16x32_bf16 v[160:163], v[52:55], v[52:55], v[160:163]\n"
    "s_waitcnt lgkmcnt(5)\n"
    "v_mfma_f32_16x16x32_bf16 v[96:99], v[164:167], v[56:59], v[96:99]\n"
    "s_barrier\n"
    "ds_read_b128 v[164:167], v0 offset:6144\n"
    "s_waitcnt lgkmcnt(5)\n"
    "v_mfma_f32_16x16x32_bf16 v[100:103], v[168:171], v[56:59], v[100:103]\n"
    "ds_read_b128 v[168:171], v0 offset:7168\n"
    "s_add_u32 m0, s61, 122880\n"
    "s_add_u32 s51, s50, 276480\n"
    "buffer_load_dwordx4 v0, s[40:43], s51 offen offset:0 lds\n"
    "s_waitcnt lgkmcnt(5)\n"
    "v_mfma_f32_16x16x32_bf16 v[104:107], v[172:175], v[56:59], v[104:107]\n"
    "ds_read_b128 v[172:175], v0 offset:8192\n"
    "s_waitcnt lgkmcnt(5)\n"
    "v_mfma_f32_16x16x32_bf16 v[108:111], v[176:179], v[56:59], v[108:111]\n"
    "ds_read_b128 v[176:179], v0 offset:9216\n"
    "s_waitcnt lgkmcnt(5)\n"
    "v_mfma_f32_16x16x32_bf16 v[112:115], v[180:183], v[56:59], v[112:115]\n"
    "ds_read_b128 v[180:183], v0 offset:10240\n"
    "buffer_load_dwordx4 v0, s[40:43], s51 offen offset:1024 lds\n"
    "s_waitcnt lgkmcnt(5)\n"
    "v_mfma_f32_16x16x32_bf16 v[116:119], v[184:187], v[56:59], v[116:119]\n"
    "ds_read_b128 v[184:187], v0 offset:11264\n"
    "s_waitcnt lgkmcnt(5)\n"
    "v_mfma_f32_16x16x32_bf16 v[120:123], v[164:167], v[56:59], v[120:123]\n"
    "ds_read_b128 v[164:167], v0 offset:12288\n"
    "s_waitcnt lgkmcnt(5)\n"
    "v_mfma_f32_16x16x32_bf16 v[124:127], v[168:171], v[56:59], v[124:127]\n"
    "ds_read_b128 v[168:171], v0 offset:13312\n"
    "buffer_load_dwordx4 v0, s[40:43], s51 offen offset:2048 lds\n"
    "v_mfma_f32_16x16x32_bf16 v[156:159], v[188:191], v[56:59], v[156:159]\n"
    "s_waitcnt lgkmcnt(5)\n"
    "v_mfma_f32_16x16x32_bf16 v[128:131], v[172:175], v[56:59], v[128:131]\n"
    "ds_read_b128 v[172:175], v0 offset:14336\n"
    "s_waitcnt lgkmcnt(5)\n"
    "v_mfma_f32_16x16x32_bf16 v[132:135], v[176:179], v[56:59], v[132:135]\n"
    "ds_read_b128 v[176:179], v0 offset:15360\n"
    "s_waitcnt lgkmcnt(5)\n"
    "v_mfma_f32_16x16x32_bf16 v[136:139], v[180:183], v[56:59], v[136:139]\n"
    "ds_read_b128 v[180:183], v0 offset:16384\n"
    "buffer_load_dwordx4 v0, s[40:43], s51 offen offset:3072 lds\n"
    "s_waitcnt lgkmcnt(5)\n"
    "v_mfma_f32_16x16x32_bf16 v[140:143], v[184:187], v[56:59], v[140:143]\n"
    "ds_read_b128 v[184:187], v0 offset:17408\n"
    "s_waitcnt lgkmcnt(5)\n"
    "v_mfma_f32_16x16x32_bf16 v[144:147], v[164:167], v[56:59], v[144:147]\n"
    "ds_read_b128 v[164:167], v0 offset:18432\n"
    "s_waitcnt lgkmcnt(5)\n"
    "v_mfma_f32_16x16x32_bf16 v[148:151], v[168:171], v[56:59], v[148:151]\n"
    "ds_read_b128 v[168:171], v0 offset:19456\n"
    "s_add_u32 m0, s61, 126976\n"
    "s_add_u32 s51, s50, 280576\n"
    "buffer_load_dwordx4 v0, s[40:43], s51 offen offset:0 lds\n"
    "s_waitcnt lgkmcnt(5)\n"
    "v_mfma_f32_16x16x32_bf16 v[152:155], v[172:175], v[56:59], v[152:155]\n"
    "ds_read_b128 v[172:175], v0 offset:20480\n"
    "v_mfma_f32_16x16x32_bf16 v[160:163], v[56:59], v[56:59], v[160:163]\n"
    "s_waitcnt lgkmcnt(5)\n"
    "v_mfma_f32_16x16x32_bf16 v[96:99], v[176:179], v[60:63], v[96:99]\n"
    "ds_read_b128 v[176:179], v0 offset:21504\n"
    "s_waitcnt lgkmcnt(5)\n"
    "v_mfma_f32_16x16x32_bf16 v[100:103], v[180:183], v[60:63], v[100:103]\n"
    "ds_read_b128 v[180:183], v0 offset:22528\n"
    "buffer_load_dwordx4 v0, s[40:43], s51 offen offset:1024 lds\n"
    "s_waitcnt lgkmcnt(5)\n"
    "v_mfma_f32_16x16x32_bf16 v[104:107], v[184:187], v[60:63], v[104:107]\n"
    "ds_read_b128 v[184:187], v0 offset:23552\n"
    "s_waitcnt lgkmcnt(5)\n"
    "v_mfma_f32_16x16x32_bf16 v[108:111], v[164:167], v[60:63], v[108:111]\n"
    "ds_read_b128 v[164:167], v0 offset:24576\n"
    "s_waitcnt lgkmcnt(5)\n"
    "v_mfma_f32_16x16x32_bf16 v[112:115], v[168:171], v[60:63], v[112:115]\n"
    "ds_read_b128 v[168:171], v0 offset:25600\n"
    "buffer_load_dwordx4 v0, s[40:43], s51 offen offset:2048 lds\n"
    "s_waitcnt lgkmcnt(5)\n"
    "v_mfma_f32_16x16x32_bf16 v[116:119], v[172:175], v[60:63], v[116:119]\n"
    "ds_read_b128 v[172:175], v0 offset:26624\n"
    "s_waitcnt lgkmcnt(5)\n"
    "v_mfma_f32_16x16x32_bf16 v[120:123], v[176:179], v[60:63], v[120:123]\n"
    "ds_read_b128 v[176:179], v0 offset:27648\n"
    "s_waitcnt lgkmcnt(5)\n"
    "v_mfma_f32_16x16x32_bf16 v[124:127], v[180:183], v[60:63], v[124:127]\n"
    "ds_read_b128 v[180:183], v0 offset:28672\n"
    "buffer_load_dwordx4 v0, s[40:43], s51 offen offset:3072 lds\n"
    "v_mfma_f32_16x16x32_bf16 v[156:159], v[188:191], v[60:63], v[156:159]\n"
    "s_waitcnt lgkmcnt(5)\n"
    "v_mfma_f32_16x16x32_bf16 v[128:131], v[184:187], v[60:63], v[128:131]\n"
    "ds_read_b128 v[184:187], v0 offset:29696\n"
    "s_waitcnt lgkmcnt(5)\n"
    "v_mfma_f32_16x16x32_bf16 v[132:135], v[164:167], v[60:63], v[132:135]\n"
    "s_waitcnt vmcnt(16)\n"
    "s_barrier\n"
    "ds_read_b128 v[164:167], v0 offset:30720\n"
    "s_waitcnt lgkmcnt(5)\n"
    "v_mfma_f32_16x16x32_bf16 v[136:139], v[168:171], v[60:63], v[136:139]\n"
    "ds_read_b128 v[168:171], v0 offset:31744\n"
    "s_waitcnt lgkmcnt(5)\n"
    "v_mfma_f32_16x16x32_bf16 v[140:143], v[172:175], v[60:63], v[140:143]\n"
    "ds_read_b128 v[172:175], v0 offset:32768\n"
    "s_waitcnt lgkmcnt(5)\n"
    "v_mfma_f32_16x16x32_bf16 v[144:147], v[176:179], v[60:63], v[144:147]\n"
    "ds_read_b128 v[176:179], v0 offset:33792\n"
    "s_waitcnt lgkmcnt(5)\n"
    "v_mfma_f32_16x16x32_bf16 v[148:151], v[180:183], v[60:63], v[148:151]\n"
    "ds_read_b128 v[180:183], v0 offset:34816\n"
    "s_waitcnt lgkmcnt(5)\n"
    "v_mfma_f32_16x16x32_bf16 v[152:155], v[184:187], v[60:63], v[152:155]\n"
    "ds_read_b128 v[184:187], v0 offset:35840\n"
    "v_mfma_f32_16x16x32_bf16 v[160:163], v[60:63], v[60:63], v[160:163]\n"
    "s_waitcnt lgkmcnt(5)\n"
    "v_mfma_f32_16x16x32_bf16 v[96:99], v[164:167], v[64:67], v[96:99]\n"
    "ds_read_b128 v[164:167], v0 offset:36864\n"
    "s_waitcnt lgkmcnt(5)\n"
    "v_mfma_f32_16x16x32_bf16 v[100:103], v[168:171], v[64:67], v[100:103]\n"
    "ds_read_b128 v[168:171], v0 offset:37888\n"
    "s_waitcnt lgkmcnt(5)\n"
    "v_mfma_f32_16x16x32_bf16 v[104:107], v[172:175], v[64:67], v[104:107]\n"
    "ds_read_b128 v[172:175], v0 offset:38912\n"
    "s_waitcnt lgkmcnt(5)\n"
    "v_mfma_f32_16x16x32_bf16 v[108:111], v[176:179], v[64:67], v[108:111]\n"
    "ds_read_b128 v[176:179], v0 offset:39936\n"
    "s_waitcnt lgkmcnt(5)\n"
    "v_mfma_f32_16x16x32_bf16 v[112:115], v[180:183], v[64:67], v[112:115]\n"
    "ds_read_b128 v[180:183], v0 offset:40960\n"
    "s_waitcnt lgkmcnt(5)\n"
    "v_mfma_f32_16x16x32_bf16 v[116:119], v[184:187], v[64:67], v[116:119]\n"
    "ds_read_b128 v[184:187], v0 offset:41984\n"
    "s_waitcnt lgkmcnt(5)\n"
    "v_mfma_f32_16x16x32_bf16 v[120:123], v[164:167], v[64:67], v[120:123]\n"
    "ds_read_b128 v[164:167], v0 offset:43008\n"
    "s_waitcnt lgkmcnt(5)\n"
    "v_mfma_f32_16x16x32_bf16 v[124:127], v[168:171], v[64:67], v[124:127]\n"
    "ds_read_b128 v[168:171], v0 offset:44032\n"
    "v_mfma_f32_16x16x32_bf16 v[156:159], v[188:191], v[64:67], v[156:159]\n"
    "s_waitcnt lgkmcnt(5)\n"
    "v_mfma_f32_16x16x32_bf16 v[128:131], v[172:175], v[64:67], v[128:131]\n"
    "ds_read_b128 v[172:175], v0 offset:45056\n"
    "s_waitcnt lgkmcnt(5)\n"
    "v_mfma_f32_16x16x32_bf16 v[132:135], v[176:179], v[64:67], v[132:135]\n"
    "ds_read_b128 v[176:179], v0 offset:46080\n"
    "s_waitcnt lgkmcnt(5)\n"
    "v_mfma_f32_16x16x32_bf16 v[136:139], v[180:183], v[64:67], v[136:139]\n"
    "ds_read_b128 v[180:183], v0 offset:47104\n"
    "s_waitcnt lgkmcnt(5)\n"
    "v_mfma_f32_16x16x32_bf16 v[140:143], v[184:187], v[64:67], v[140:143]\n"
    "ds_read_b128 v[184:187], v0 offset:48128\n"
    "s_waitcnt lgkmcnt(5)\n"
    "v_mfma_f32_16x16x32_bf16 v[144:147], v[164:167], v[64:67], v[144:147]\n"
    "ds_read_b128 v[164:167], v0 offset:49152\n"
    "s_waitcnt lgkmcnt(5)\n"
    "v_mfma_f32_16x16x32_bf16 v[148:151], v[168:171], v[64:67], v[148:151]\n"
    "ds_read_b128 v[168:171], v0 offset:50176\n"
    "s_waitcnt lgkmcnt(5)\n"
    "v_mfma_f32_16x16x32_bf16 v[152:155], v[172:175], v[64:67], v[152:155]\n"
    "ds_read_b128 v[172:175], v0 offset:51200\n"
    "v_mfma_f32_16x16x32_bf16 v[160:163], v[64:67], v[64:67], v[160:163]\n"
    "s_waitcnt lgkmcnt(5)\n"
    "v_mfma_f32_16x16x32_bf16 v[96:99], v[176:179], v[68:71], v[96:99]\n"
    "ds_read_b128 v[176:179], v0 offset:52224\n"
    "s_waitcnt lgkmcnt(5)\n"
    "v_mfma_f32_16x16x32_bf16 v[100:103], v[180:183], v[68:71], v[100:103]\n"
    "ds_read_b128 v[180:183], v0 offset:53248\n"
    "s_waitcnt lgkmcnt(5)\n"
    "v_mfma_f32_16x16x32_bf16 v[104:107], v[184:187], v[68:71], v[104:107]\n"
    "ds_read_b128 v[184:187], v0 offset:54272\n"
    "s_waitcnt lgkmcnt(5)\n"
    "v_mfma_f32_16x16x32_bf16 v[108:111], v[164:167], v[68:71], v[108:111]\n"
    "ds_read_b128 v[164:167], v0 offset:55296\n"
    "s_waitcnt lgkmcnt(5)\n"
    "v_mfma_f32_16x16x32_bf16 v[112:115], v[168:171], v[68:71], v[112:115]\n"
    "ds_read_b128 v[168:171], v0 offset:56320\n"
    "s_waitcnt lgkmcnt(5)\n"
    "v_mfma_f32_16x16x32_bf16 v[116:119], v[172:175], v[68:71], v[116:119]\n"
    "ds_read_b128 v[172:175], v0 offset:57344\n"
    "s_waitcnt lgkmcnt(5)\n"
    "v_mfma_f32_16x16x32_bf16 v[120:123], v[176:179], v[68:71], v[120:123]\n"
    "ds_read_b128 v[176:179], v0 offset:58368\n"
    "s_waitcnt lgkmcnt(5)\n"
    "v_mfma_f32_16x16x32_bf16 v[124:127], v[180:183], v[68:71], v[124:127]\n"
    "ds_read_b128 v[180:183], v0 offset:59392\n"
    "v_mfma_f32_16x16x32_bf16 v[156:159], v[188:191], v[68:71], v[156:159]\n"
    "s_waitcnt lgkmcnt(5)\n"
    "v_mfma_f32_16x16x32_bf16 v[128:131], v[184:187], v[68:71], v[128:131]\n"
    "ds_read_b128 v[184:187], v0 offset:60416\n"
    "s_waitcnt lgkmcnt(5)\n"
    "v_mfma_f32_16x16x32_bf16 v[132:135], v[164:167], v[68:71], v[132:135]\n"
    "s_waitcnt vmcnt(16)\n"
    "s_barrier\n"
    "ds_read_b128 v[164:167], v6 offset:0\n"
    "s_waitcnt lgkmcnt(5)\n"
    "v_mfma_f32_16x16x32_bf16 v[136:139], v[168:171], v[68:71], v[136:139]\n"
    "ds_read_b128 v[168:171], v6 offset:1024\n"
    "s_waitcnt lgkmcnt(5)\n"
    "v_mfma_f32_16x16x32_bf16 v[140:143], v[172:175], v[68:71], v[140:143]\n"
    "ds_read_b128 v[172:175], v6 offset:2048\n"
    "s_waitcnt lgkmcnt(5)\n"
    "v_mfma_f32_16x16x32_bf16 v[144:147], v[176:179], v[68:71], v[144:147]\n"
    "ds_read_b128 v[176:179], v6 offset:3072\n"
    "s_waitcnt lgkmcnt(5)\n"
    "v_mfma_f32_16x16x32_bf16 v[148:151], v[180:183], v[68:71], v[148:151]\n"
    "ds_read_b128 v[180:183], v6 offset:4096\n"
    "s_waitcnt lgkmcnt(5)\n"
    "v_mfma_f32_16x16x32_bf16 v[152:155], v[184:187], v[68:71], v[152:155]\n"
    "ds_read_b128 v[184:187], v6 offset:5120\n"
    "v_mfma_f32_16x16x32_bf16 v[160:163], v[68:71], v[68:71], v[160:163]\n"
    "s_waitcnt lgkmcnt(5)\n"
    "v_mfma_f32_16x16x32_bf16 v[96:99], v[164:167], v[72:75], v[96:99]\n"
    "ds_read_b128 v[164:167], v6 offset:6144\n"
    "s_waitcnt lgkmcnt(5)\n"
    "v_mfma_f32_16x16x32_bf16 v[100:103], v[168:171], v[72:75], v[100:103]\n"
    "ds_read_b128 v[168:171], v6 offset:7168\n"
    "s_waitcnt lgkmcnt(5)\n"
    "v_mfma_f32_16x16x32_bf16 v[104:107], v[172:175], v[72:75], v[104:107]\n"
    "ds_read_b128 v[172:175], v6 offset:8192\n"
    "s_waitcnt lgkmcnt(5)\n"
    "v_mfma_f32_16x16x32_bf16 v[108:111], v[176:179], v[72:75], v[108:111]\n"
    "ds_read_b128 v[176:179], v6 offset:9216\n"
    "s_waitcnt lgkmcnt(5)\n"
    "v_mfma_f32_16x16x32_bf16 v[112:115], v[180:183], v[72:75], v[112:115]\n"
    "ds_read_b128 v[180:183], v6 offset:10240\n"
    "s_waitcnt lgkmcnt(5)\n"
    "v_mfma_f32_16x16x32_bf16 v[116:119], v[184:187], v[72:75], v[116:119]\n"
    "ds_read_b128 v[184:187], v6 offset:11264\n"
    "s_waitcnt lgkmcnt(5)\n"
    "v_mfma_f32_16x16x32_bf16 v[120:123], v[164:167], v[72:75], v[120:123]\n"
    "ds_read_b128 v[164:167], v6 offset:12288\n"
    "s_waitcnt lgkmcnt(5)\n"
    "v_mfma_f32_16x16x32_bf16 v[124:127], v[168:171], v[72:75], v[124:127]\n"
    "ds_read_b128 v[168:171], v6 offset:13312\n"
    "v_mfma_f32_16x16x32_bf16 v[156:159], v[188:191], v[72:75], v[156:159]\n"
    "s_waitcnt lgkmcnt(5)\n"
    "v_mfma_f32_16x16x32_bf16 v[128:131], v[172:175], v[72:75], v[128:131]\n"
    "ds_read_b128 v[172:175], v6 offset:14336\n"
    "s_waitcnt lgkmcnt(5)\n"
    "v_mfma_f32_16x16x32_bf16 v[132:135], v[176:179], v[72:75], v[132:135]\n"
    "ds_read_b128 v[176:179], v6 offset:15360\n"
    "s_waitcnt lgkmcnt(5)\n"
    "v_mfma_f32_16x16x32_bf16 v[136:139], v[180:183], v[72:75], v[136:139]\n"
    "ds_read_b128 v[180:183], v6 offset:16384\n"
    "s_waitcnt lgkmcnt(5)\n"
    "v_mfma_f32_16x16x32_bf16 v[140:143], v[184:187], v[72:75], v[140:143]\n"
    "ds_read_b128 v[184:187], v6 offset:17408\n"
    "s_waitcnt lgkmcnt(5)\n"
    "v_mfma_f32_16x16x32_bf16 v[144:147], v[164:167], v[72:75], v[144:147]\n"
    "ds_read_b128 v[164:167], v6 offset:18432\n"
    "s_waitcnt lgkmcnt(5)\n"
    "v_mfma_f32_16x16x32_bf16 v[148:151], v[168:171], v[72:75], v[148:151]\n"
    "ds_read_b128 v[168:171], v6 offset:19456\n"
    "s_waitcnt lgkmcnt(5)\n"
    "v_mfma_f32_16x16x32_bf16 v[152:155], v[172:175], v[72:75], v[152:155]\n"
    "ds_read_b128 v[172:175], v6 offset:20480\n"
    "v_mfma_f32_16x16x32_bf16 v[160:163], v[72:75], v[72:75], v[160:163]\n"
    "s_waitcnt lgkmcnt(5)\n"
    "v_mfma_f32_16x16x32_bf16 v[96:99], v[176:179], v[76:79], v[96:99]\n"
    "ds_read_b128 v[176:179], v6 offset:21504\n"
    "s_waitcnt lgkmcnt(5)\n"
    "v_mfma_f32_16x16x32_bf16 v[100:103], v[180:183], v[76:79], v[100:103]\n"
    "ds_read_b128 v[180:183], v6 offset:22528\n"
    "s_waitcnt lgkmcnt(5)\n"
    "v_mfma_f32_16x16x32_bf16 v[104:107], v[184:187], v[76:79], v[104:107]\n"
    "ds_read_b128 v[184:187], v6 offset:23552\n"
    "s_waitcnt lgkmcnt(5)\n"
    "v_mfma_f32_16x16x32_bf16 v[108:111], v[164:167], v[76:79], v[108:111]\n"
    "ds_read_b128 v[164:167], v6 offset:24576\n"
    "s_waitcnt lgkmcnt(5)\n"
    "v_mfma_f32_16x16x32_bf16 v[112:115], v[168:171], v[76:79], v[112:115]\n"
    "ds_read_b128 v[168:171], v6 offset:25600\n"
    "s_waitcnt lgkmcnt(5)\n"
    "v_mfma_f32_16x16x32_bf16 v[116:119], v[172:175], v[76:79], v[116:119]\n"
    "ds_read_b128 v[172:175], v6 offset:26624\n"
    "s_waitcnt lgkmcnt(5)\n"
    "v_mfma_f32_16x16x32_bf16 v[120:123], v[176:179], v[76:79], v[120:123]\n"
    "ds_read_b128 v[176:179], v6 offset:27648\n"
    "s_waitcnt lgkmcnt(5)\n"
    "v_mfma_f32_16x16x32_bf16 v[124:127], v[180:183], v[76:79], v[124:127]\n"
    "ds_read_b128 v[180:183], v6 offset:28672\n"
    "v_mfma_f32_16x16x32_bf16 v[156:159], v[188:191], v[76:79], v[156:159]\n"
    "s_waitcnt lgkmcnt(5)\n"
    "v_mfma_f32_16x16x32_bf16 v[128:131], v[184:187], v[76:79], v[128:131]\n"
    "ds_read_b128 v[184:187], v6 offset:29696\n"
    "s_waitcnt lgkmcnt(5)\n"
    "v_mfma_f32_16x16x32_bf16 v[132:135], v[164:167], v[76:79], v[132:135]\n"
    "s_waitcnt vmcnt(8)\n"
    "s_barrier\n"
    "ds_read_b128 v[164:167], v6 offset:30720\n"
    "s_waitcnt lgkmcnt(5)\n"
    "v_mfma_f32_16x16x32_bf16 v[136:139], v[168:171], v[76:79], v[136:139]\n"
    "ds_read_b128 v[168:171], v6 offset:31744\n"
    "s_waitcnt lgkmcnt(5)\n"
    "v_mfma_f32_16x16x32_bf16 v[140:143], v[172:175], v[76:79], v[140:143]\n"
    "ds_read_b128 v[172:175], v6 offset:32768\n"
    "s_waitcnt lgkmcnt(5)\n"
    "v_mfma_f32_16x16x32_bf16 v[144:147], v[176:179], v[76:79], v[144:147]\n"
    "ds_read_b128 v[176:179], v6 offset:33792\n"
    "s_waitcnt lgkmcnt(5)\n"
    "v_mfma_f32_16x16x32_bf16 v[148:151], v[180:183], v[76:79], v[148:151]\n"
    "ds_read_b128 v[180:183], v6 offset:34816\n"
    "s_waitcnt lgkmcnt(5)\n"
    "v_mfma_f32_16x16x32_bf16 v[152:155], v[184:187], v[76:79], v[152:155]\n"
    "ds_read_b128 v[184:187], v6 offset:35840\n"
    "v_mfma_f32_16x16x32_bf16 v[160:163], v[76:79], v[76:79], v[160:163]\n"
    "s_waitcnt lgkmcnt(5)\n"
    "v_mfma_f32_16x16x32_bf16 v[96:99], v[164:167], v[80:83], v[96:99]\n"
    "ds_read_b128 v[164:167], v6 offset:36864\n"
    "v_and_b32 v4, 48, v15\n"
    "s_mul_i32 s52, s26, 960\n"
    "s_add_u32 s52, s52, 148668416\n"
    "s_waitcnt lgkmcnt(5)\n"
    "v_mfma_f32_16x16x32_bf16 v[100:103], v[168:171], v[80:83], v[100:103]\n"
    "ds_read_b128 v[168:171], v6 offset:37888\n"
    "buffer_load_dwordx4 v[16:19], v4, s[40:43], s52 offen sc1\n"
    "s_waitcnt lgkmcnt(5)\n"
    "v_mfma_f32_16x16x32_bf16 v[104:107], v[172:175], v[80:83], v[104:107]\n"
    "ds_read_b128 v[172:175], v6 offset:38912\n"
    "buffer_load_dwordx4 v[20:23], v4, s[40:43], s52 offen offset:64 sc1\n"
    "s_waitcnt lgkmcnt(5)\n"
    "v_mfma_f32_16x16x32_bf16 v[108:111], v[176:179], v[80:83], v[108:111]\n"
    "ds_read_b128 v[176:179], v6 offset:39936\n"
    "buffer_load_dwordx4 v[24:27], v4, s[40:43], s52 offen offset:128 sc1\n"
    "s_waitcnt lgkmcnt(5)\n"
    "v_mfma_f32_16x16x32_bf16 v[112:115], v[180:183], v[80:83], v[112:115]\n"
    "ds_read_b128 v[180:183], v6 offset:40960\n"
    "buffer_load_dwordx4 v[28:31], v4, s[40:43], s52 offen offset:192 sc1\n"
    "s_waitcnt lgkmcnt(5)\n"
    "v_mfma_f32_16x16x32_bf16 v[116:119], v[184:187], v[80:83], v[116:119]\n"
    "ds_read_b128 v[184:187], v6 offset:41984\n"
    "buffer_load_dwordx4 v[32:35], v4, s[40:43], s52 offen offset:256 sc1\n"
    "s_waitcnt lgkmcnt(5)\n"
    "v_mfma_f32_16x16x32_bf16 v[120:123], v[164:167], v[80:83], v[120:123]\n"
    "ds_read_b128 v[164:167], v6 offset:43008\n"
    "buffer_load_dwordx4 v[36:39], v4, s[40:43], s52 offen offset:320 sc1\n"
    "s_waitcnt lgkmcnt(5)\n"
    "v_mfma_f32_16x16x32_bf16 v[124:127], v[168:171], v[80:83], v[124:127]\n"
    "ds_read_b128 v[168:171], v6 offset:44032\n"
    "v_mfma_f32_16x16x32_bf16 v[156:159], v[188:191], v[80:83], v[156:159]\n"
    "buffer_load_dwordx4 v[40:43], v4, s[40:43], s52 offen offset:384 sc1\n"
    "s_waitcnt lgkmcnt(5)\n"
    "v_mfma_f32_16x16x32_bf16 v[128:131], v[172:175], v[80:83], v[128:131]\n"
    "ds_read_b128 v[172:175], v6 offset:45056\n"
    "buffer_load_dwordx4 v[44:47], v4, s[40:43], s52 offen offset:448 sc1\n"
    "s_waitcnt lgkmcnt(5)\n"
    "v_mfma_f32_16x16x32_bf16 v[132:135], v[176:179], v[80:83], v[132:135]\n"
    "ds_read_b128 v[176:179], v6 offset:46080\n"
    "buffer_load_dwordx4 v[48:51], v4, s[40:43], s52 offen offset:512 sc1\n"
    "s_waitcnt lgkmcnt(5)\n"
    "v_mfma_f32_16x16x32_bf16 v[136:139], v[180:183], v[80:83], v[136:139]\n"
    "ds_read_b128 v[180:183], v6 offset:47104\n"
    "buffer_load_dwordx4 v[52:55], v4, s[40:43], s52 offen offset:576 sc1\n"
    "s_waitcnt lgkmcnt(5)\n"
    "v_mfma_f32_16x16x32_bf16 v[140:143], v[184:187], v[80:83], v[140:143]\n"
    "ds_read_b128 v[184:187], v6 offset:48128\n"
    "buffer_load_dwordx4 v[56:59], v4, s[40:43], s52 offen offset:640 sc1\n"
    "s_waitcnt lgkmcnt(5)\n"
    "v_mfma_f32_16x16x32_bf16 v[144:147], v[164:167], v[80:83], v[144:147]\n"
    "ds_read_b128 v[164:167], v6 offset:49152\n"
    "buffer_load_dwordx4 v[60:63], v4, s[40:43], s52 offen offset:704 sc1\n"
    "s_waitcnt lgkmcnt(5)\n"
    "v_mfma_f32_16x16x32_bf16 v[148:151], v[168:171], v[80:83], v[148:151]\n"
    "ds_read_b128 v[168:171], v6 offset:50176\n"
    "buffer_load_dwordx4 v[64:67], v4, s[40:43], s52 offen offset:768 sc1\n"
    "s_waitcnt lgkmcnt(5)\n"
    "v_mfma_f32_16x16x32_bf16 v[152:155], v[172:175], v[80:83], v[152:155]\n"
    "ds_read_b128 v[172:175], v6 offset:51200\n"
    "v_mfma_f32_16x16x32_bf16 v[160:163], v[80:83], v[80:83], v[160:163]\n"
    "buffer_load_dwordx4 v[68:71], v4, s[40:43], s52 offen offset:832 sc1\n"
    "s_waitcnt lgkmcnt(5)\n"
    "v_mfma_f32_16x16x32_bf16 v[96:99], v[176:179], v[84:87], v[96:99]\n"
    "ds_read_b128 v[176:179], v6 offset:52224\n"
    "buffer_load_dwordx4 v[72:75], v4, s[40:43], s52 offen offset:896 sc1\n"
    "s_waitcnt lgkmcnt(5)\n"
    "v_mfma_f32_16x16x32_bf16 v[100:103], v[180:183], v[84:87], v[100:103]\n"
    "ds_read_b128 v[180:183], v6 offset:53248\n"
    "s_waitcnt lgkmcnt(5)\n"
    "v_mfma_f32_16x16x32_bf16 v[104:107], v[184:187], v[84:87], v[104:107]\n"
    "ds_read_b128 v[184:187], v6 offset:54272\n"
    "s_waitcnt lgkmcnt(5)\n"
    "v_mfma_f32_16x16x32_bf16 v[108:111], v[164:167], v[84:87], v[108:111]\n"
    "ds_read_b128 v[164:167], v6 offset:55296\n"
    "s_waitcnt lgkmcnt(5)\n"
    "v_mfma_f32_16x16x32_bf16 v[112:115], v[168:171], v[84:87], v[112:115]\n"
    "ds_read_b128 v[168:171], v6 offset:56320\n"
    "s_waitcnt lgkmcnt(5)\n"
    "v_mfma_f32_16x16x32_bf16 v[116:119], v[172:175], v[84:87], v[116:119]\n"
    "ds_read_b128 v[172:175], v6 offset:57344\n"
    "s_waitcnt lgkmcnt(5)\n"
    "v_mfma_f32_16x16x32_bf16 v[120:123], v[176:179], v[84:87], v[120:123]\n"
    "ds_read_b128 v[176:179], v6 offset:58368\n"
    "s_waitcnt lgkmcnt(5)\n"
    "v_mfma_f32_16x16x32_bf16 v[124:127], v[180:183], v[84:87], v[124:127]\n"
    "ds_read_b128 v[180:183], v6 offset:59392\n"
    "v_mfma_f32_16x16x32_bf16 v[156:159], v[188:191], v[84:87], v[156:159]\n"
    "s_waitcnt lgkmcnt(5)\n"
    "v_mfma_f32_16x16x32_bf16 v[128:131], v[184:187], v[84:87], v[128:131]\n"
    "ds_read_b128 v[184:187], v6 offset:60416\n"
    "s_waitcnt lgkmcnt(5)\n"
    "v_mfma_f32_16x16x32_bf16 v[132:135], v[164:167], v[84:87], v[132:135]\n"
    "s_waitcnt vmcnt(0)\n"
    "s_barrier\n"
    "ds_read_b128 v[164:167], v7 offset:0\n"
    "s_waitcnt lgkmcnt(5)\n"
    "v_mfma_f32_16x16x32_bf16 v[136:139], v[168:171], v[84:87], v[136:139]\n"
    "ds_read_b128 v[168:171], v7 offset:1024\n"
    "s_waitcnt lgkmcnt(5)\n"
    "v_mfma_f32_16x16x32_bf16 v[140:143], v[172:175], v[84:87], v[140:143]\n"
    "ds_read_b128 v[172:175], v7 offset:2048\n"
    "s_waitcnt lgkmcnt(5)\n"
    "v_mfma_f32_16x16x32_bf16 v[144:147], v[176:179], v[84:87], v[144:147]\n"
    "ds_read_b128 v[176:179], v7 offset:3072\n"
    "s_waitcnt lgkmcnt(5)\n"
    "v_mfma_f32_16x16x32_bf16 v[148:151], v[180:183], v[84:87], v[148:151]\n"
    "ds_read_b128 v[180:183], v7 offset:4096\n"
    "s_waitcnt lgkmcnt(5)\n"
    "v_mfma_f32_16x16x32_bf16 v[152:155], v[184:187], v[84:87], v[152:155]\n"
    "ds_read_b128 v[184:187], v7 offset:5120\n"
    "v_mfma_f32_16x16x32_bf16 v[160:163], v[84:87], v[84:87], v[160:163]\n"
    "s_waitcnt lgkmcnt(5)\n"
    "v_mfma_f32_16x16x32_bf16 v[96:99], v[164:167], v[88:91], v[96:99]\n"
    "ds_read_b128 v[164:167], v7 offset:6144\n"
    "s_waitcnt lgkmcnt(5)\n"
    "v_mfma_f32_16x16x32_bf16 v[100:103], v[168:171], v[88:91], v[100:103]\n"
    "ds_read_b128 v[168:171], v7 offset:7168\n"
    "s_waitcnt lgkmcnt(5)\n"
    "v_mfma_f32_16x16x32_bf16 v[104:107], v[172:175], v[88:91], v[104:107]\n"
    "ds_read_b128 v[172:175], v7 offset:8192\n"
    "s_waitcnt lgkmcnt(5)\n"
    "v_mfma_f32_16x16x32_bf16 v[108:111], v[176:179], v[88:91], v[108:111]\n"
    "ds_read_b128 v[176:179], v7 offset:9216\n"
    "s_waitcnt lgkmcnt(5)\n"
    "v_mfma_f32_16x16x32_bf16 v[112:115], v[180:183], v[88:91], v[112:115]\n"
    "ds_read_b128 v[180:183], v7 offset:10240\n"
    "s_waitcnt lgkmcnt(5)\n"
    "v_mfma_f32_16x16x32_bf16 v[116:119], v[184:187], v[88:91], v[116:119]\n"
    "ds_read_b128 v[184:187], v7 offset:11264\n"
    "s_waitcnt lgkmcnt(5)\n"
    "v_mfma_f32_16x16x32_bf16 v[120:123], v[164:167], v[88:91], v[120:123]\n"
    "ds_read_b128 v[164:167], v7 offset:12288\n"
    "s_waitcnt lgkmcnt(5)\n"
    "v_mfma_f32_16x16x32_bf16 v[124:127], v[168:171], v[88:91], v[124:127]\n"
    "ds_read_b128 v[168:171], v7 offset:13312\n"
    "v_mfma_f32_16x16x32_bf16 v[156:159], v[188:191], v[88:91], v[156:159]\n"
    "s_waitcnt lgkmcnt(5)\n"
    "v_mfma_f32_16x16x32_bf16 v[128:131], v[172:175], v[88:91], v[128:131]\n"
    "ds_read_b128 v[172:175], v7 offset:14336\n"
    "s_waitcnt lgkmcnt(5)\n"
    "v_mfma_f32_16x16x32_bf16 v[132:135], v[176:179], v[88:91], v[132:135]\n"
    "ds_read_b128 v[176:179], v7 offset:15360\n"
    "s_waitcnt lgkmcnt(5)\n"
    "v_mfma_f32_16x16x32_bf16 v[136:139], v[180:183], v[88:91], v[136:139]\n"
    "ds_read_b128 v[180:183], v7 offset:16384\n"
    "s_waitcnt lgkmcnt(5)\n"
    "v_mfma_f32_16x16x32_bf16 v[140:143], v[184:187], v[88:91], v[140:143]\n"
    "ds_read_b128 v[184:187], v7 offset:17408\n"
    "s_waitcnt lgkmcnt(5)\n"
    "v_mfma_f32_16x16x32_bf16 v[144:147], v[164:167], v[88:91], v[144:147]\n"
    "ds_read_b128 v[164:167], v7 offset:18432\n"
    "s_waitcnt lgkmcnt(5)\n"
    "v_mfma_f32_16x16x32_bf16 v[148:151], v[168:171], v[88:91], v[148:151]\n"
    "ds_read_b128 v[168:171], v7 offset:19456\n"
    "s_waitcnt lgkmcnt(5)\n"
    "v_mfma_f32_16x16x32_bf16 v[152:155], v[172:175], v[88:91], v[152:155]\n"
    "ds_read_b128 v[172:175], v7 offset:20480\n"
    "v_mfma_f32_16x16x32_bf16 v[160:163], v[88:91], v[88:91], v[160:163]\n"
    "s_waitcnt lgkmcnt(5)\n"
    "v_mfma_f32_16x16x32_bf16 v[96:99], v[176:179], v[92:95], v[96:99]\n"
    "ds_read_b128 v[176:179], v7 offset:21504\n"
    "s_waitcnt lgkmcnt(5)\n"
    "v_mfma_f32_16x16x32_bf16 v[100:103], v[180:183], v[92:95], v[100:103]\n"
    "ds_read_b128 v[180:183], v7 offset:22528\n"
    "s_waitcnt lgkmcnt(5)\n"
    "v_mfma_f32_16x16x32_bf16 v[104:107], v[184:187], v[92:95], v[104:107]\n"
    "ds_read_b128 v[184:187], v7 offset:23552\n"
    "s_waitcnt lgkmcnt(5)\n"
    "v_mfma_f32_16x16x32_bf16 v[108:111], v[164:167], v[92:95], v[108:111]\n"
    "ds_read_b128 v[164:167], v7 offset:24576\n"
    "s_waitcnt lgkmcnt(5)\n"
    "v_mfma_f32_16x16x32_bf16 v[112:115], v[168:171], v[92:95], v[112:115]\n"
    "ds_read_b128 v[168:171], v7 offset:25600\n"
    "s_waitcnt lgkmcnt(5)\n"
    "v_mfma_f32_16x16x32_bf16 v[116:119], v[172:175], v[92:95], v[116:119]\n"
    "ds_read_b128 v[172:175], v7 offset:26624\n"
    "s_waitcnt lgkmcnt(5)\n"
    "v_mfma_f32_16x16x32_bf16 v[120:123], v[176:179], v[92:95], v[120:123]\n"
    "ds_read_b128 v[176:179], v7 offset:27648\n"
    "s_waitcnt lgkmcnt(5)\n"
    "v_mfma_f32_16x16x32_bf16 v[124:127], v[180:183], v[92:95], v[124:127]\n"
    "ds_read_b128 v[180:183], v7 offset:28672\n"
    "v_mfma_f32_16x16x32_bf16 v[156:159], v[188:191], v[92:95], v[156:159]\n"
    "s_waitcnt lgkmcnt(5)\n"
    "v_mfma_f32_16x16x32_bf16 v[128:131], v[184:187], v[92:95], v[128:131]\n"
    "ds_read_b128 v[184:187], v7 offset:29696\n"
    "s_waitcnt lgkmcnt(5)\n"
    "v_mfma_f32_16x16x32_bf16 v[132:135], v[164:167], v[92:95], v[132:135]\n"
    "s_waitcnt lgkmcnt(4)\n"
    "v_mfma_f32_16x16x32_bf16 v[136:139], v[168:171], v[92:95], v[136:139]\n"
    "s_waitcnt lgkmcnt(3)\n"
    "v_mfma_f32_16x16x32_bf16 v[140:143], v[172:175], v[92:95], v[140:143]\n"
    "s_waitcnt lgkmcnt(2)\n"
    "v_mfma_f32_16x16x32_bf16 v[144:147], v[176:179], v[92:95], v[144:147]\n"
    "s_waitcnt lgkmcnt(1)\n"
    "v_mfma_f32_16x16x32_bf16 v[148:151], v[180:183], v[92:95], v[148:151]\n"
    "s_waitcnt lgkmcnt(0)\n"
    "v_mfma_f32_16x16x32_bf16 v[152:155], v[184:187], v[92:95], v[152:155]\n"
    "v_mfma_f32_16x16x32_bf16 v[160:163], v[92:95], v[92:95], v[160:163]\n"
    "v_and_b32 v4, 1, v15\n"
    "v_cmp_eq_u32 vcc, 1, v4\n"
    "s_nop 5\n"
    "v_cndmask_b32 v160, v160, v161, vcc\n"
    "v_cndmask_b32 v162, v162, v163, vcc\n"
    "v_and_b32 v4, 2, v15\n"
    "v_cmp_eq_u32 vcc, 2, v4\n"
    "s_nop 1\n"
    "v_cndmask_b32 v160, v160, v162, vcc\n"
    "v_bfe_u32 v4, v15, 2, 2\n"
    "v_lshrrev_b32 v5, 4, v15\n"
    "v_cmp_eq_u32 vcc, v4, v5\n"
    "s_nop 1\n"
    "v_cndmask_b32 v160, 0, v160, vcc\n"
    "v_mov_b32 v161, v160\n"
    "s_nop 1\n"
    "v_permlane16_swap_b32 v160, v161\n"
    "v_add_f32 v160, v160, v161\n"
    "v_mov_b32 v161, v160\n"
    "s_nop 1\n"
    "v_permlane32_swap_b32 v160, v161\n"
    "v_add_f32 v160, v160, v161\n"
    "v_mul_f32 v8, 0x3acccccd, v156\n"
    "v_mul_f32 v160, 0x3acccccd, v160\n"
    "v_fma_f32 v160, -v8, v8, v160\n"
    "v_max_f32 v160, 0, v160\n"
    "v_add_f32 v160, 0x3727c5ac, v160\n"
    "v_rsq_f32 v10, v160\n"
    "v_sub_f32 v8, 0, v8\n"
    "v_pk_fma_f32 v[96:97], v[16:17], v[8:9], v[96:97] op_sel_hi:[1,0,1]\n"
    "v_pk_mul_f32 v[96:97], v[96:97], v[10:11] op_sel_hi:[1,0]\n"
    "v_pk_fma_f32 v[98:99], v[18:19], v[8:9], v[98:99] op_sel_hi:[1,0,1]\n"
    "v_pk_mul_f32 v[98:99], v[98:99], v[10:11] op_sel_hi:[1,0]\n"
    "v_pk_fma_f32 v[100:101], v[20:21], v[8:9], v[100:101] op_sel_hi:[1,0,1]\n"
    "v_pk_mul_f32 v[100:101], v[100:101], v[10:11] op_sel_hi:[1,0]\n"
    "v_pk_fma_f32 v[102:103], v[22:23], v[8:9], v[102:103] op_sel_hi:[1,0,1]\n"
    "v_pk_mul_f32 v[102:103], v[102:103], v[10:11] op_sel_hi:[1,0]\n"
    "v_pk_fma_f32 v[104:105], v[24:25], v[8:9], v[104:105] op_sel_hi:[1,0,1]\n"
    "v_pk_mul_f32 v[104:105], v[104:105], v[10:11] op_sel_hi:[1,0]\n"
    "v_pk_fma_f32 v[106:107], v[26:27], v[8:9], v[106:107] op_sel_hi:[1,0,1]\n"
    "v_pk_mul_f32 v[106:107], v[106:107], v[10:11] op_sel_hi:[1,0]\n"
    "v_pk_fma_f32 v[108:109], v[28:29], v[8:9], v[108:109] op_sel_hi:[1,0,1]\n"
    "v_pk_mul_f32 v[108:109], v[108:109], v[10:11] op_sel_hi:[1,0]\n"
    "v_pk_fma_f32 v[110:111], v[30:31], v[8:9], v[110:111] op_sel_hi:[1,0,1]\n"
    "v_pk_mul_f32 v[110:111], v[110:111], v[10:11] op_sel_hi:[1,0]\n"
    "v_pk_fma_f32 v[112:113], v[32:33], v[8:9], v[112:113] op_sel_hi:[1,0,1]\n"
    "v_pk_mul_f32 v[112:113], v[112:113], v[10:11] op_sel_hi:[1,0]\n"
    "v_pk_fma_f32 v[114:115], v[34:35], v[8:9], v[114:115] op_sel_hi:[1,0,1]\n"
    "v_pk_mul_f32 v[114:115], v[114:115], v[10:11] op_sel_hi:[1,0]\n"
    "v_pk_fma_f32 v[116:117], v[36:37], v[8:9], v[116:117] op_sel_hi:[1,0,1]\n"
    "v_pk_mul_f32 v[116:117], v[116:117], v[10:11] op_sel_hi:[1,0]\n"
    "v_pk_fma_f32 v[118:119], v[38:39], v[8:9], v[118:119] op_sel_hi:[1,0,1]\n"
    "v_pk_mul_f32 v[118:119], v[118:119], v[10:11] op_sel_hi:[1,0]\n"
    "v_pk_fma_f32 v[120:121], v[40:41], v[8:9], v[120:121] op_sel_hi:[1,0,1]\n"
    "v_pk_mul_f32 v[120:121], v[120:121], v[10:11] op_sel_hi:[1,0]\n"
    "v_pk_fma_f32 v[122:123], v[42:43], v[8:9], v[122:123] op_sel_hi:[1,0,1]\n"
    "v_pk_mul_f32 v[122:123], v[122:123], v[10:11] op_sel_hi:[1,0]\n"
    "v_pk_fma_f32 v[124:125], v[44:45], v[8:9], v[124:125] op_sel_hi:[1,0,1]\n"
    "v_pk_mul_f32 v[124:125], v[124:125], v[10:11] op_sel_hi:[1,0]\n"
    "v_pk_fma_f32 v[126:127], v[46:47], v[8:9], v[126:127] op_sel_hi:[1,0,1]\n"
    "v_pk_mul_f32 v[126:127], v[126:127], v[10:11] op_sel_hi:[1,0]\n"
    "v_pk_fma_f32 v[128:129], v[48:49], v[8:9], v[128:129] op_sel_hi:[1,0,1]\n"
    "v_pk_mul_f32 v[128:129], v[128:129], v[10:11] op_sel_hi:[1,0]\n"
    "v_pk_fma_f32 v[130:131], v[50:51], v[8:9], v[130:131] op_sel_hi:[1,0,1]\n"
    "v_pk_mul_f32 v[130:131], v[130:131], v[10:11] op_sel_hi:[1,0]\n"
    "v_pk_fma_f32 v[132:133], v[52:53], v[8:9], v[132:133] op_sel_hi:[1,0,1]\n"
    "v_pk_mul_f32 v[132:133], v[132:133], v[10:11] op_sel_hi:[1,0]\n"
    "v_pk_fma_f32 v[134:135], v[54:55], v[8:9], v[134:135] op_sel_hi:[1,0,1]\n"
    "v_pk_mul_f32 v[134:135], v[134:135], v[10:11] op_sel_hi:[1,0]\n"
    "v_pk_fma_f32 v[136:137], v[56:57], v[8:9], v[136:137] op_sel_hi:[1,0,1]\n"
    "v_pk_mul_f32 v[136:137], v[136:137], v[10:11] op_sel_hi:[1,0]\n"
    "v_pk_fma_f32 v[138:139], v[58:59], v[8:9], v[138:139] op_sel_hi:[1,0,1]\n"
    "v_pk_mul_f32 v[138:139], v[138:139], v[10:11] op_sel_hi:[1,0]\n"
    "v_pk_fma_f32 v[140:141], v[60:61], v[8:9], v[140:141] op_sel_hi:[1,0,1]\n"
    "v_pk_mul_f32 v[140:141], v[140:141], v[10:11] op_sel_hi:[1,0]\n"
    "v_pk_fma_f32 v[142:143], v[62:63], v[8:9], v[142:143] op_sel_hi:[1,0,1]\n"
    "v_pk_mul_f32 v[142:143], v[142:143], v[10:11] op_sel_hi:[1,0]\n"
    "v_pk_fma_f32 v[144:145], v[64:65], v[8:9], v[144:145] op_sel_hi:[1,0,1]\n"
    "v_pk_mul_f32 v[144:145], v[144:145], v[10:11] op_sel_hi:[1,0]\n"
    "v_pk_fma_f32 v[146:147], v[66:67], v[8:9], v[146:147] op_sel_hi:[1,0,1]\n"
    "v_pk_mul_f32 v[146:147], v[146:147], v[10:11] op_sel_hi:[1,0]\n"
    "v_pk_fma_f32 v[148:149], v[68:69], v[8:9], v[148:149] op_sel_hi:[1,0,1]\n"
    "v_pk_mul_f32 v[148:149], v[148:149], v[10:11] op_sel_hi:[1,0]\n"
    "v_pk_fma_f32 v[150:151], v[70:71], v[8:9], v[150:151] op_sel_hi:[1,0,1]\n"
    "v_pk_mul_f32 v[150:151], v[150:151], v[10:11] op_sel_hi:[1,0]\n"
    "v_pk_fma_f32 v[152:153], v[72:73], v[8:9], v[152:153] op_sel_hi:[1,0,1]\n"
    "v_pk_mul_f32 v[152:153], v[152:153], v[10:11] op_sel_hi:[1,0]\n"
    "v_pk_fma_f32 v[154:155], v[74:75], v[8:9], v[154:155] op_sel_hi:[1,0,1]\n"
    "v_pk_mul_f32 v[154:155], v[154:155], v[10:11] op_sel_hi:[1,0]\n"
    "v_max3_f32 v8, v116, v117, v118\n"
    "v_max3_f32 v8, v8, v119, v120\n"
    "v_max3_f32 v8, v8, v121, v122\n"
    "v_max3_f32 v8, v8, v123, v124\n"
    "v_max3_f32 v8, v8, v125, v126\n"
    "v_max3_f32 v8, v8, v127, v128\n"
    "v_max3_f32 v8, v8, v129, v130\n"
    "v_max3_f32 v8, v8, v131, v132\n"
    "v_max3_f32 v8, v8, v133, v134\n"
    "v_max_f32 v8, v8, v135\n"
    "v_min3_f32 v9, v116, v117, v118\n"
    "v_min3_f32 v9, v9, v119, v120\n"
    "v_min3_f32 v9, v9, v121, v122\n"
    "v_min3_f32 v9, v9, v123, v124\n"
    "v_min3_f32 v9, v9, v125, v126\n"
    "v_min3_f32 v9, v9, v127, v128\n"
    "v_min3_f32 v9, v9, v129, v130\n"
    "v_min3_f32 v9, v9, v131, v132\n"
    "v_min3_f32 v9, v9, v133, v134\n"
    "v_min_f32 v9, v9, v135\n"
    "v_mul_f32 v8, 0xbfb8aa3b, v8\n"
    "v_mul_f32 v10, 0x3fb8aa3b, v9\n"
    "v_pk_fma_f32 v[16:17], v[116:117], s[22:23], v[8:9] op_sel_hi:[1,1,0]\n"
    "v_pk_fma_f32 v[36:37], v[116:117], s[24:25], v[10:11] op_sel_hi:[1,1,0]\n"
    "v_pk_fma_f32 v[18:19], v[118:119], s[22:23], v[8:9] op_sel_hi:[1,1,0]\n"
    "v_pk_fma_f32 v[38:39], v[118:119], s[24:25], v[10:11] op_sel_hi:[1,1,0]\n"
    "v_pk_fma_f32 v[20:21], v[120:121], s[22:23], v[8:9] op_sel_hi:[1,1,0]\n"
    "v_pk_fma_f32 v[40:41], v[120:121], s[24:25], v[10:11] op_sel_hi:[1,1,0]\n"
    "v_pk_fma_f32 v[22:23], v[122:123], s[22:23], v[8:9] op_sel_hi:[1,1,0]\n"
    "v_pk_fma_f32 v[42:43], v[122:123], s[24:25], v[10:11] op_sel_hi:[1,1,0]\n"
    "v_pk_fma_f32 v[24:25], v[124:125], s[22:23], v[8:9] op_sel_hi:[1,1,0]\n"
    "v_pk_fma_f32 v[44:45], v[124:125], s[24:25], v[10:11] op_sel_hi:[1,1,0]\n"
    "v_pk_fma_f32 v[26:27], v[126:127], s[22:23], v[8:9] op_sel_hi:[1,1,0]\n"
    "v_pk_fma_f32 v[46:47], v[126:127], s[24:25], v[10:11] op_sel_hi:[1,1,0]\n"
    "v_pk_fma_f32 v[28:29], v[128:129], s[22:23], v[8:9] op_sel_hi:[1,1,0]\n"
    "v_pk_fma_f32 v[48:49], v[128:129], s[24:25], v[10:11] op_sel_hi:[1,1,0]\n"
    "v_pk_fma_f32 v[30:31], v[130:131], s[22:23], v[8:9] op_sel_hi:[1,1,0]\n"
    "v_pk_fma_f32 v[50:51], v[130:131], s[24:25], v[10:11] op_sel_hi:[1,1,0]\n"
    "v_pk_fma_f32 v[32:33], v[132:133], s[22:23], v[8:9] op_sel_hi:[1,1,0]\n"
    "v_pk_fma_f32 v[52:53], v[132:133], s[24:25], v[10:11] op_sel_hi:[1,1,0]\n"
    "v_pk_fma_f32 v[34:35], v[134:135], s[22:23], v[8:9] op_sel_hi:[1,1,0]\n"
    "v_pk_fma_f32 v[54:55], v[134:135], s[24:25], v[10:11] op_sel_hi:[1,1,0]\n"
    "v_exp_f32 v16, v16\n"
    "v_exp_f32 v17, v17\n"
    "v_exp_f32 v18, v18\n"
    "v_exp_f32 v19, v19\n"
    "v_exp_f32 v20, v20\n"
    "v_exp_f32 v21, v21\n"
    "v_exp_f32 v22, v22\n"
    "v_exp_f32 v23, v23\n"
    "v_exp_f32 v24, v24\n"
    "v_exp_f32 v25, v25\n"
    "v_exp_f32 v26, v26\n"
    "v_exp_f32 v27, v27\n"
    "v_exp_f32 v28, v28\n"
    "v_exp_f32 v29, v29\n"
    "v_exp_f32 v30, v30\n"
    "v_exp_f32 v31, v31\n"
    "v_exp_f32 v32, v32\n"
    "v_exp_f32 v33, v33\n"
    "v_exp_f32 v34, v34\n"
    "v_exp_f32 v35, v35\n"
    "v_exp_f32 v36, v36\n"
    "v_exp_f32 v37, v37\n"
    "v_exp_f32 v38, v38\n"
    "v_exp_f32 v39, v39\n"
    "v_exp_f32 v40, v40\n"
    "v_exp_f32 v41, v41\n"
    "v_exp_f32 v42, v42\n"
    "v_exp_f32 v43, v43\n"
    "v_exp_f32 v44, v44\n"
    "v_exp_f32 v45, v45\n"
    "v_exp_f32 v46, v46\n"
    "v_exp_f32 v47, v47\n"
    "v_exp_f32 v48, v48\n"
    "v_exp_f32 v49, v49\n"
    "v_exp_f32 v50, v50\n"
    "v_exp_f32 v51, v51\n"
    "v_exp_f32 v52, v52\n"
    "v_exp_f32 v53, v53\n"
    "v_exp_f32 v54, v54\n"
    "v_exp_f32 v55, v55\n"
    "v_pk_add_f32 v[8:9], v[16:17], v[18:19]\n"
    "v_pk_add_f32 v[8:9], v[8:9], v[20:21]\n"
    "v_pk_add_f32 v[8:9], v[8:9], v[22:23]\n"
    "v_pk_add_f32 v[8:9], v[8:9], v[24:25]\n"
    "v_pk_add_f32 v[8:9], v[8:9], v[26:27]\n"
    "v_pk_add_f32 v[8:9], v[8:9], v[28:29]\n"
    "v_pk_add_f32 v[8:9], v[8:9], v[30:31]\n"
    "v_pk_add_f32 v[8:9], v[8:9], v[32:33]\n"
    "v_pk_add_f32 v[8:9], v[8:9], v[34:35]\n"
    "v_add_f32 v8, v8, v9\n"
    "v_pk_add_f32 v[10:11], v[36:37], v[38:39]\n"
    "v_pk_add_f32 v[10:11], v[10:11], v[40:41]\n"
    "v_pk_add_f32 v[10:11], v[10:11], v[42:43]\n"
    "v_pk_add_f32 v[10:11], v[10:11], v[44:45]\n"
    "v_pk_add_f32 v[10:11], v[10:11], v[46:47]\n"
    "v_pk_add_f32 v[10:11], v[10:11], v[48:49]\n"
    "v_pk_add_f32 v[10:11], v[10:11], v[50:51]\n"
    "v_pk_add_f32 v[10:11], v[10:11], v[52:53]\n"
    "v_pk_add_f32 v[10:11], v[10:11], v[54:55]\n"
    "v_add_f32 v10, v10, v11\n"
    "v_rcp_f32 v8, v8\n"
    "v_rcp_f32 v10, v10\n"
    "v_pk_mul_f32 v[16:17], v[16:17], v[8:9] op_sel_hi:[1,0]\n"
    "v_pk_mul_f32 v[18:19], v[18:19], v[8:9] op_sel_hi:[1,0]\n"
    "v_pk_mul_f32 v[20:21], v[20:21], v[8:9] op_sel_hi:[1,0]\n"
    "v_pk_mul_f32 v[22:23], v[22:23], v[8:9] op_sel_hi:[1,0]\n"
    "v_pk_mul_f32 v[24:25], v[24:25], v[8:9] op_sel_hi:[1,0]\n"
    "v_pk_mul_f32 v[26:27], v[26:27], v[8:9] op_sel_hi:[1,0]\n"
    "v_pk_mul_f32 v[28:29], v[28:29], v[8:9] op_sel_hi:[1,0]\n"
    "v_pk_mul_f32 v[30:31], v[30:31], v[8:9] op_sel_hi:[1,0]\n"
    "v_pk_mul_f32 v[32:33], v[32:33], v[8:9] op_sel_hi:[1,0]\n"
    "v_pk_mul_f32 v[34:35], v[34:35], v[8:9] op_sel_hi:[1,0]\n"
    "v_pk_mul_f32 v[36:37], v[36:37], v[10:11] op_sel_hi:[1,0]\n"
    "v_pk_mul_f32 v[38:39], v[38:39], v[10:11] op_sel_hi:[1,0]\n"
    "v_pk_mul_f32 v[40:41], v[40:41], v[10:11] op_sel_hi:[1,0]\n"
    "v_pk_mul_f32 v[42:43], v[42:43], v[10:11] op_sel_hi:[1,0]\n"
    "v_pk_mul_f32 v[44:45], v[44:45], v[10:11] op_sel_hi:[1,0]\n"
    "v_pk_mul_f32 v[46:47], v[46:47], v[10:11] op_sel_hi:[1,0]\n"
    "v_pk_mul_f32 v[48:49], v[48:49], v[10:11] op_sel_hi:[1,0]\n"
    "v_pk_mul_f32 v[50:51], v[50:51], v[10:11] op_sel_hi:[1,0]\n"
    "v_pk_mul_f32 v[52:53], v[52:53], v[10:11] op_sel_hi:[1,0]\n"
    "v_pk_mul_f32 v[54:55], v[54:55], v[10:11] op_sel_hi:[1,0]\n"
    "v_cvt_pk_bf16_f32 v56, v16, v17\n"
    "v_cvt_pk_bf16_f32 v57, v18, v19\n"
    "v_cvt_pk_bf16_f32 v58, v20, v21\n"
    "v_cvt_pk_bf16_f32 v59, v22, v23\n"
    "v_cvt_pk_bf16_f32 v60, v24, v25\n"
    "v_cvt_pk_bf16_f32 v61, v26, v27\n"
    "v_cvt_pk_bf16_f32 v62, v28, v29\n"
    "v_cvt_pk_bf16_f32 v63, v30, v31\n"
    "v_cvt_pk_bf16_f32 v64, v32, v33\n"
    "v_cvt_pk_bf16_f32 v65, v34, v35\n"
    "v_cvt_pk_bf16_f32 v66, v36, v37\n"
    "v_cvt_pk_bf16_f32 v67, v38, v39\n"
    "v_cvt_pk_bf16_f32 v68, v40, v41\n"
    "v_cvt_pk_bf16_f32 v69, v42, v43\n"
    "v_cvt_pk_bf16_f32 v70, v44, v45\n"
    "v_cvt_pk_bf16_f32 v71, v46, v47\n"
    "v_cvt_pk_bf16_f32 v72, v48, v49\n"
    "v_cvt_pk_bf16_f32 v73, v50, v51\n"
    "v_cvt_pk_bf16_f32 v74, v52, v53\n"
    "v_cvt_pk_bf16_f32 v75, v54, v55\n"
    "v_mov_b32 v76, 0\n"
    "v_mov_b32 v77, 0\n"
    "v_mov_b32 v78, 0\n"
    "v_mov_b32 v79, 0\n"
    "v_cvt_pk_bf16_f32 v80, v136, v137\n"
    "v_cvt_pk_bf16_f32 v81, v138, v139\n"
    "v_cvt_pk_bf16_f32 v82, v140, v141\n"
    "v_cvt_pk_bf16_f32 v83, v142, v143\n"
    "v_cvt_pk_bf16_f32 v84, v144, v145\n"
    "v_cvt_pk_bf16_f32 v85, v146, v147\n"
    "v_cvt_pk_bf16_f32 v86, v148, v149\n"
    "v_cvt_pk_bf16_f32 v87, v150, v151\n"
    "v_cvt_pk_bf16_f32 v88, v152, v153\n"
    "v_cvt_pk_bf16_f32 v89, v154, v155\n"
    "v_mov_b32 v90, 0x3f80\n"
    "v_mov_b32 v91, 0\n"
    "v_and_b32 v4, 15, v15\n"
    "v_lshl_add_u32 v4, v5, 6, v4\n"
    "s_lshl_b32 s48, s45, 4\n"
    "v_add_u32 v4, s48, v4\n"
    "v_mul_u32_u24 v4, 176, v4\n"
    "ds_write_b128 v4, v[56:59] offset:0\n"
    "ds_write_b128 v4, v[60:63] offset:16\n"
    "ds_write_b128 v4, v[64:67] offset:32\n"
    "ds_write_b128 v4, v[68:71] offset:48\n"
    "ds_write_b128 v4, v[72:75] offset:64\n"
    "ds_write_b128 v4, v[76:79] offset:80\n"
    "ds_write_b128 v4, v[80:83] offset:96\n"
    "ds_write_b128 v4, v[84:87] offset:112\n"
    "ds_write_b128 v4, v[88:91] offset:128\n"
    "ds_write_b128 v4, v[76:79] offset:144\n"
    "v_lshrrev_b32 v5, 2, v15\n"
    "s_lshl_b32 s48, s45, 6\n"
    "v_add_u32 v5, s48, v5\n"
    "v_mul_u32_u24 v5, 176, v5\n"
    "v_and_b32 v6, 3, v15\n"
    "v_lshl_add_u32 v6, v6, 3, v5\n"
    "s_waitcnt lgkmcnt(0)\n"
    "s_barrier\n"
    "ds_read_b64_tr_b16 v[116:117], v6 offset:0\n"
    "ds_read_b64_tr_b16 v[118:119], v6 offset:2816\n"
    "ds_read_b64_tr_b16 v[120:121], v6 offset:32\n"
    "ds_read_b64_tr_b16 v[122:123], v6 offset:2848\n"
    "ds_read_b64_tr_b16 v[124:125], v6 offset:64\n"
    "ds_read_b64_tr_b16 v[126:127], v6 offset:2880\n"
    "ds_read_b64_tr_b16 v[128:129], v6 offset:96\n"
    "ds_read_b64_tr_b16 v[130:131], v6 offset:2912\n"
    "ds_read_b64_tr_b16 v[132:133], v6 offset:128\n"
    "ds_read_b64_tr_b16 v[134:135], v6 offset:2944\n"
    "ds_read_b64_tr_b16 v[136:137], v6 offset:5632\n"
    "ds_read_b64_tr_b16 v[138:139], v6 offset:8448\n"
    "ds_read_b64_tr_b16 v[140:141], v6 offset:5664\n"
    "ds_read_b64_tr_b16 v[142:143], v6 offset:8480\n"
    "ds_read_b64_tr_b16 v[144:145], v6 offset:5696\n"
    "ds_read_b64_tr_b16 v[146:147], v6 offset:8512\n"
    "s_waitcnt lgkmcnt(14)\n"
    "ds_read_b64_tr_b16 v[148:149], v6 offset:5728\n"
    "ds_read_b64_tr_b16 v[150:151], v6 offset:8544\n"
    "s_waitcnt lgkmcnt(14)\n"
    "ds_read_b64_tr_b16 v[152:153], v6 offset:5760\n"
    "ds_read_b64_tr_b16 v[154:155], v6 offset:8576\n"
    "s_waitcnt lgkmcnt(14)\n"
    "s_waitcnt lgkmcnt(12)\n"
    "v_mfma_f32_16x16x32_bf16 v[192:195], v[116:119], v[128:131], 0\n"
    "s_waitcnt lgkmcnt(10)\n"
    "v_mfma_f32_16x16x32_bf16 v[196:199], v[116:119], v[132:135], 0\n"
    "v_mfma_f32_16x16x32_bf16 v[200:203], v[120:123], v[128:131], 0\n"
    "v_mfma_f32_16x16x32_bf16 v[204:207], v[120:123], v[132:135], 0\n"
    "v_mfma_f32_16x16x32_bf16 v[208:211], v[124:127], v[128:131], 0\n"
    "v_mfma_f32_16x16x32_bf16 v[212:215], v[124:127], v[132:135], 0\n"
    "s_waitcnt lgkmcnt(2)\n"
    "v_mfma_f32_16x16x32_bf16 v[192:195], v[136:139], v[148:151], v[192:195]\n"
    "s_waitcnt lgkmcnt(0)\n"
    "v_mfma_f32_16x16x32_bf16 v[196:199], v[136:139], v[152:155], v[196:199]\n"
    "v_mfma_f32_16x16x32_bf16 v[200:203], v[140:143], v[148:151], v[200:203]\n"
    "v_mfma_f32_16x16x32_bf16 v[204:207], v[140:143], v[152:155], v[204:207]\n"
    "v_mfma_f32_16x16x32_bf16 v[208:211], v[144:147], v[148:151], v[208:211]\n"
    "v_mfma_f32_16x16x32_bf16 v[212:215], v[144:147], v[152:155], v[212:215]\n"
    "s_lshl_b32 s48, s27, 5\n"
    "s_lshl2_add_u32 s48, s26, s48\n"
    "s_add_u32 s48, s48, s45\n"
    "s_mul_i32 s48, s48, 6144\n"
    "s_add_u32 s48, s48, 154820608\n"
    "s_add_u32 s49, s48, 4096\n"
    "buffer_store_dwordx4 v[192:195], v0, s[40:43], s48 offen sc1\n"
    "buffer_store_dwordx4 v[196:199], v0, s[40:43], s48 offen offset:1024 sc1\n"
    "buffer_store_dwordx4 v[200:203], v0, s[40:43], s48 offen offset:2048 sc1\n"
    "buffer_store_dwordx4 v[204:207], v0, s[40:43], s48 offen offset:3072 sc1\n"
    "buffer_store_dwordx4 v[208:211], v0, s[40:43], s49 offen sc1\n"
    "buffer_store_dwordx4 v[212:215], v0, s[40:43], s49 offen offset:1024 sc1\n"
    "v_max3_f32 v8, v96, v97, v98\n"
    "v_max3_f32 v8, v8, v99, v100\n"
    "v_max3_f32 v8, v8, v101, v102\n"
    "v_max3_f32 v8, v8, v103, v104\n"
    "v_max3_f32 v8, v8, v105, v106\n"
    "v_max3_f32 v8, v8, v107, v108\n"
    "v_max3_f32 v8, v8, v109, v110\n"
    "v_max3_f32 v8, v8, v111, v112\n"
    "v_max3_f32 v8, v8, v113, v114\n"
    "v_max_f32 v8, v8, v115\n"
    "v_min3_f32 v9, v96, v97, v98\n"
    "v_min3_f32 v9, v9, v99, v100\n"
    "v_min3_f32 v9, v9, v101, v102\n"
    "v_min3_f32 v9, v9, v103, v104\n"
    "v_min3_f32 v9, v9, v105, v106\n"
    "v_min3_f32 v9, v9, v107, v108\n"
    "v_min3_f32 v9, v9, v109, v110\n"
    "v_min3_f32 v9, v9, v111, v112\n"
    "v_min3_f32 v9, v9, v113, v114\n"
    "v_min_f32 v9, v9, v115\n"
    "v_mul_f32 v8, 0xbfb8aa3b, v8\n"
    "v_mul_f32 v10, 0x3fb8aa3b, v9\n"
    "v_pk_fma_f32 v[16:17], v[96:97], s[22:23], v[8:9] op_sel_hi:[1,1,0]\n"
    "v_pk_fma_f32 v[36:37], v[96:97], s[24:25], v[10:11] op_sel_hi:[1,1,0]\n"
    "v_pk_fma_f32 v[18:19], v[98:99], s[22:23], v[8:9] op_sel_hi:[1,1,0]\n"
    "v_pk_fma_f32 v[38:39], v[98:99], s[24:25], v[10:11] op_sel_hi:[1,1,0]\n"
    "v_pk_fma_f32 v[20:21], v[100:101], s[22:23], v[8:9] op_sel_hi:[1,1,0]\n"
    "v_pk_fma_f32 v[40:41], v[100:101], s[24:25], v[10:11] op_sel_hi:[1,1,0]\n"
    "v_pk_fma_f32 v[22:23], v[102:103], s[22:23], v[8:9] op_sel_hi:[1,1,0]\n"
    "v_pk_fma_f32 v[42:43], v[102:103], s[24:25], v[10:11] op_sel_hi:[1,1,0]\n"
    "v_pk_fma_f32 v[24:25], v[104:105], s[22:23], v[8:9] op_sel_hi:[1,1,0]\n"
    "v_pk_fma_f32 v[44:45], v[104:105], s[24:25], v[10:11] op_sel_hi:[1,1,0]\n"
    "v_pk_fma_f32 v[26:27], v[106:107], s[22:23], v[8:9] op_sel_hi:[1,1,0]\n"
    "v_pk_fma_f32 v[46:47], v[106:107], s[24:25], v[10:11] op_sel_hi:[1,1,0]\n"
    "v_pk_fma_f32 v[28:29], v[108:109], s[22:23], v[8:9] op_sel_hi:[1,1,0]\n"
    "v_pk_fma_f32 v[48:49], v[108:109], s[24:25], v[10:11] op_sel_hi:[1,1,0]\n"
    "v_pk_fma_f32 v[30:31], v[110:111], s[22:23], v[8:9] op_sel_hi:[1,1,0]\n"
    "v_pk_fma_f32 v[50:51], v[110:111], s[24:25], v[10:11] op_sel_hi:[1,1,0]\n"
    "v_pk_fma_f32 v[32:33], v[112:113], s[22:23], v[8:9] op_sel_hi:[1,1,0]\n"
    "v_pk_fma_f32 v[52:53], v[112:113], s[24:25], v[10:11] op_sel_hi:[1,1,0]\n"
    "v_pk_fma_f32 v[34:35], v[114:115], s[22:23], v[8:9] op_sel_hi:[1,1,0]\n"
    "v_pk_fma_f32 v[54:55], v[114:115], s[24:25], v[10:11] op_sel_hi:[1,1,0]\n"
    "v_exp_f32 v16, v16\n"
    "v_exp_f32 v17, v17\n"
    "v_exp_f32 v18, v18\n"
    "v_exp_f32 v19, v19\n"
    "v_exp_f32 v20, v20\n"
    "v_exp_f32 v21, v21\n"
    "v_exp_f32 v22, v22\n"
    "v_exp_f32 v23, v23\n"
    "v_exp_f32 v24, v24\n"
    "v_exp_f32 v25, v25\n"
    "v_exp_f32 v26, v26\n"
    "v_exp_f32 v27, v27\n"
    "v_exp_f32 v28, v28\n"
    "v_exp_f32 v29, v29\n"
    "v_exp_f32 v30, v30\n"
    "v_exp_f32 v31, v31\n"
    "v_exp_f32 v32, v32\n"
    "v_exp_f32 v33, v33\n"
    "v_exp_f32 v34, v34\n"
    "v_exp_f32 v35, v35\n"
    "v_exp_f32 v36, v36\n"
    "v_exp_f32 v37, v37\n"
    "v_exp_f32 v38, v38\n"
    "v_exp_f32 v39, v39\n"
    "v_exp_f32 v40, v40\n"
    "v_exp_f32 v41, v41\n"
    "v_exp_f32 v42, v42\n"
    "v_exp_f32 v43, v43\n"
    "v_exp_f32 v44, v44\n"
    "v_exp_f32 v45, v45\n"
    "v_exp_f32 v46, v46\n"
    "v_exp_f32 v47, v47\n"
    "v_exp_f32 v48, v48\n"
    "v_exp_f32 v49, v49\n"
    "v_exp_f32 v50, v50\n"
    "v_exp_f32 v51, v51\n"
    "v_exp_f32 v52, v52\n"
    "v_exp_f32 v53, v53\n"
    "v_exp_f32 v54, v54\n"
    "v_exp_f32 v55, v55\n"
    "v_pk_add_f32 v[8:9], v[16:17], v[18:19]\n"
    "v_pk_add_f32 v[8:9], v[8:9], v[20:21]\n"
    "v_pk_add_f32 v[8:9], v[8:9], v[22:23]\n"
    "v_pk_add_f32 v[8:9], v[8:9], v[24:25]\n"
    "v_pk_add_f32 v[8:9], v[8:9], v[26:27]\n"
    "v_pk_add_f32 v[8:9], v[8:9], v[28:29]\n"
    "v_pk_add_f32 v[8:9], v[8:9], v[30:31]\n"
    "v_pk_add_f32 v[8:9], v[8:9], v[32:33]\n"
    "v_pk_add_f32 v[8:9], v[8:9], v[34:35]\n"
    "v_add_f32 v8, v8, v9\n"
    "v_pk_add_f32 v[10:11], v[36:37], v[38:39]\n"
    "v_pk_add_f32 v[10:11], v[10:11], v[40:41]\n"
    "v_pk_add_f32 v[10:11], v[10:11], v[42:43]\n"
    "v_pk_add_f32 v[10:11], v[10:11], v[44:45]\n"
    "v_pk_add_f32 v[10:11], v[10:11], v[46:47]\n"
    "v_pk_add_f32 v[10:11], v[10:11], v[48:49]\n"
    "v_pk_add_f32 v[10:11], v[10:11], v[50:51]\n"
    "v_pk_add_f32 v[10:11], v[10:11], v[52:53]\n"
    "v_pk_add_f32 v[10:11], v[10:11], v[54:55]\n"
    "v_add_f32 v10, v10, v11\n"
    "v_rcp_f32 v8, v8\n"
    "v_rcp_f32 v10, v10\n"
    "v_pk_mul_f32 v[16:17], v[16:17], v[8:9] op_sel_hi:[1,0]\n"
    "v_pk_mul_f32 v[18:19], v[18:19], v[8:9] op_sel_hi:[1,0]\n"
    "v_pk_mul_f32 v[20:21], v[20:21], v[8:9] op_sel_hi:[1,0]\n"
    "v_pk_mul_f32 v[22:23], v[22:23], v[8:9] op_sel_hi:[1,0]\n"
    "v_pk_mul_f32 v[24:25], v[24:25], v[8:9] op_sel_hi:[1,0]\n"
    "v_pk_mul_f32 v[26:27], v[26:27], v[8:9] op_sel_hi:[1,0]\n"
    "v_pk_mul_f32 v[28:29], v[28:29], v[8:9] op_sel_hi:[1,0]\n"
    "v_pk_mul_f32 v[30:31], v[30:31], v[8:9] op_sel_hi:[1,0]\n"
    "v_pk_mul_f32 v[32:33], v[32:33], v[8:9] op_sel_hi:[1,0]\n"
    "v_pk_mul_f32 v[34:35], v[34:35], v[8:9] op_sel_hi:[1,0]\n"
    "v_pk_mul_f32 v[36:37], v[36:37], v[10:11] op_sel_hi:[1,0]\n"
    "v_pk_mul_f32 v[38:39], v[38:39], v[10:11] op_sel_hi:[1,0]\n"
    "v_pk_mul_f32 v[40:41], v[40:41], v[10:11] op_sel_hi:[1,0]\n"
    "v_pk_mul_f32 v[42:43], v[42:43], v[10:11] op_sel_hi:[1,0]\n"
    "v_pk_mul_f32 v[44:45], v[44:45], v[10:11] op_sel_hi:[1,0]\n"
    "v_pk_mul_f32 v[46:47], v[46:47], v[10:11] op_sel_hi:[1,0]\n"
    "v_pk_mul_f32 v[48:49], v[48:49], v[10:11] op_sel_hi:[1,0]\n"
    "v_pk_mul_f32 v[50:51], v[50:51], v[10:11] op_sel_hi:[1,0]\n"
    "v_pk_mul_f32 v[52:53], v[52:53], v[10:11] op_sel_hi:[1,0]\n"
    "v_pk_mul_f32 v[54:55], v[54:55], v[10:11] op_sel_hi:[1,0]\n"
    "v_cvt_pk_bf16_f32 v56, v16, v17\n"
    "v_cvt_pk_bf16_f32 v57, v18, v19\n"
    "v_cvt_pk_bf16_f32 v58, v20, v21\n"
    "v_cvt_pk_bf16_f32 v59, v22, v23\n"
    "v_cvt_pk_bf16_f32 v60, v24, v25\n"
    "v_cvt_pk_bf16_f32 v61, v26, v27\n"
    "v_cvt_pk_bf16_f32 v62, v28, v29\n"
    "v_cvt_pk_bf16_f32 v63, v30, v31\n"
    "v_cvt_pk_bf16_f32 v64, v32, v33\n"
    "v_cvt_pk_bf16_f32 v65, v34, v35\n"
    "v_cvt_pk_bf16_f32 v66, v36, v37\n"
    "v_cvt_pk_bf16_f32 v67, v38, v39\n"
    "v_cvt_pk_bf16_f32 v68, v40, v41\n"
    "v_cvt_pk_bf16_f32 v69, v42, v43\n"
    "v_cvt_pk_bf16_f32 v70, v44, v45\n"
    "v_cvt_pk_bf16_f32 v71, v46, v47\n"
    "v_cvt_pk_bf16_f32 v72, v48, v49\n"
    "v_cvt_pk_bf16_f32 v73, v50, v51\n"
    "v_cvt_pk_bf16_f32 v74, v52, v53\n"
    "v_cvt_pk_bf16_f32 v75, v54, v55\n"
    "s_lshl3_add_u32 s48, s28, s26\n"
    "s_mul_i32 s48, s48, 5120\n"
    "s_add_u32 s48, s48, 149577728\n"
    "s_add_u32 s49, s48, 4096\n"
    "buffer_store_dwordx4 v[56:59], v0, s[40:43], s48 offen sc1\n"
    "buffer_store_dwordx4 v[60:63], v0, s[40:43], s48 offen offset:1024 sc1\n"
    "buffer_store_dwordx4 v[64:67], v0, s[40:43], s48 offen offset:2048 sc1\n"
    "buffer_store_dwordx4 v[68:71], v0, s[40:43], s48 offen offset:3072 sc1\n"
    "buffer_store_dwordx4 v[72:75], v0, s[40:43], s49 offen sc1\n"
    "s_lshl3_add_u32 s54, s21, s26\n"
    "s_lshl_b32 s54, s54, 7\n"
    "s_add_u32 s54, s54, 2362368\n"
    "s_branch hp_done_%=\n"
    "hp_rd_%=:\n"
    "s_sub_u32 s26, s20, 256\n"
    "s_lshr_b32 s27, s26, 2\n"
    "s_lshl3_add_u32 s54, s21, s27\n"
    "s_lshl_b32 s54, s54, 7\n"
    "s_add_u32 s54, s54, 2362368\n"
    "s_cmp_lg_u32 s45, 0\n"
    "s_cbranch_scc1 hprd_dep_ok_%=\n"
    "s_mov_b32 s60, 0\n"
    "hprd_dep_%=:\n"
    "buffer_load_dword v4, off, s[40:43], s54 sc1\n"
    "s_waitcnt vmcnt(0)\n"
    "v_readfirstlane_b32 s59, v4\n"
    "s_nop 0\n"
    "s_cmp_ge_u32 s59, 32\n"
    "s_nop 2\n"
    "s_cbranch_scc1 hprd_dep_ok_%=\n"
    "s_sleep 3\n"
    "s_add_u32 s60, s60, 1\n"
    "s_cmp_gt_u32 s60, 65536\n"
    "s_cbranch_scc1 hp_abort_%=\n"
    "s_and_b32 s59, s60, 63\n"
    "s_cbranch_scc1 hprd_dep_%=\n"
    "v_mov_b32 v4, 1024\n"
    "buffer_load_dword v4, v4, s[40:43], s58 offen sc1\n"
    "s_waitcnt vmcnt(0)\n"
    "v_readfirstlane_b32 s59, v4\n"
    "s_nop 0\n"
    "s_cmp_lg_u32 s59, 0\n"
    "s_nop 2\n"
    "s_cbranch_scc1 hp_end_%=\n"
    "s_branch hprd_dep_%=\n"
    "hprd_dep_ok_%=:\n"
    "s_barrier\n"
    "s_lshl_b32 s48, s45, 8\n"
    "s_add_u32 s48, s48, s26\n"
    "s_mul_i32 s48, s48, 6144\n"
    "s_add_u32 s48, s48, 154820608\n"
    "s_add_u32 s49, s48, 4096\n"
    "buffer_load_dwordx4 v[16:19], v0, s[40:43], s48 offen sc1\n"
    "buffer_load_dwordx4 v[20:23], v0, s[40:43], s48 offen offset:1024 sc1\n"
    "buffer_load_dwordx4 v[24:27], v0, s[40:43], s48 offen offset:2048 sc1\n"
    "buffer_load_dwordx4 v[28:31], v0, s[40:43], s48 offen offset:3072 sc1\n"
    "buffer_load_dwordx4 v[32:35], v0, s[40:43], s49 offen sc1\n"
    "buffer_load_dwordx4 v[36:39], v0, s[40:43], s49 offen offset:1024 sc1\n"
    "s_add_u32 s48, s48, 196608\n"
    "s_add_u32 s49, s48, 4096\n"
    "buffer_load_dwordx4 v[40:43], v0, s[40:43], s48 offen sc1\n"
    "buffer_load_dwordx4 v[44:47], v0, s[40:43], s48 offen offset:1024 sc1\n"
    "buffer_load_dwordx4 v[48:51], v0, s[40:43], s48 offen offset:2048 sc1\n"
    "buffer_load_dwordx4 v[52:55], v0, s[40:43], s48 offen offset:3072 sc1\n"
    "buffer_load_dwordx4 v[56:59], v0, s[40:43], s49 offen sc1\n"
    "buffer_load_dwordx4 v[60:63], v0, s[40:43], s49 offen offset:1024 sc1\n"
    "s_add_u32 s48, s48, 196608\n"
    "s_add_u32 s49, s48, 4096\n"
    "buffer_load_dwordx4 v[64:67], v0, s[40:43], s48 offen sc1\n"
    "buffer_load_dwordx4 v[68:71], v0, s[40:43], s48 offen offset:1024 sc1\n"
    "buffer_load_dwordx4 v[72:75], v0, s[40:43], s48 offen offset:2048 sc1\n"
    "buffer_load_dwordx4 v[76:79], v0, s[40:43], s48 offen offset:3072 sc1\n"
    "buffer_load_dwordx4 v[80:83], v0, s[40:43], s49 offen sc1\n"
    "buffer_load_dwordx4 v[84:87], v0, s[40:43], s49 offen offset:1024 sc1\n"
    "s_add_u32 s48, s48, 196608\n"
    "s_add_u32 s49, s48, 4096\n"
    "buffer_load_dwordx4 v[88:91], v0, s[40:43], s48 offen sc1\n"
    "buffer_load_dwordx4 v[92:95], v0, s[40:43], s48 offen offset:1024 sc1\n"
    "buffer_load_dwordx4 v[96:99], v0, s[40:43], s48 offen offset:2048 sc1\n"
    "buffer_load_dwordx4 v[100:103], v0, s[40:43], s48 offen offset:3072 sc1\n"
    "buffer_load_dwordx4 v[104:107], v0, s[40:43], s49 offen sc1\n"
    "buffer_load_dwordx4 v[108:111], v0, s[40:43], s49 offen offset:1024 sc1\n"
    "s_add_u32 s48, s48, 196608\n"
    "s_add_u32 s49, s48, 4096\n"
    "buffer_load_dwordx4 v[112:115], v0, s[40:43], s48 offen sc1\n"
    "buffer_load_dwordx4 v[116:119], v0, s[40:43], s48 offen offset:1024 sc1\n"
    "buffer_load_dwordx4 v[120:123], v0, s[40:43], s48 offen offset:2048 sc1\n"
    "buffer_load_dwordx4 v[124:127], v0, s[40:43], s48 offen offset:3072 sc1\n"
    "buffer_load_dwordx4 v[128:131], v0, s[40:43], s49 offen sc1\n"
    "buffer_load_dwordx4 v[132:135], v0, s[40:43], s49 offen offset:1024 sc1\n"
    "s_add_u32 s48, s48, 196608\n"
    "s_add_u32 s49, s48, 4096\n"
    "buffer_load_dwordx4 v[136:139], v0, s[40:43], s48 offen sc1\n"
    "buffer_load_dwordx4 v[140:143], v0, s[40:43], s48 offen offset:1024 sc1\n"
    "buffer_load_dwordx4 v[144:147], v0, s[40:43], s48 offen offset:2048 sc1\n"
    "buffer_load_dwordx4 v[148:151], v0, s[40:43], s48 offen offset:3072 sc1\n"
    "buffer_load_dwordx4 v[152:155], v0, s[40:43], s49 offen sc1\n"
    "buffer_load_dwordx4 v[156:159], v0, s[40:43], s49 offen offset:1024 sc1\n"
    "s_add_u32 s48, s48, 196608\n"
    "s_add_u32 s49, s48, 4096\n"
    "buffer_load_dwordx4 v[160:163], v0, s[40:43], s48 offen sc1\n"
    "buffer_load_dwordx4 v[164:167], v0, s[40:43], s48 offen offset:1024 sc1\n"
    "buffer_load_dwordx4 v[168:171], v0, s[40:43], s48 offen offset:2048 sc1\n"
    "buffer_load_dwordx4 v[172:175], v0, s[40:43], s48 offen offset:3072 sc1\n"
    "buffer_load_dwordx4 v[176:179], v0, s[40:43], s49 offen sc1\n"
    "buffer_load_dwordx4 v[180:183], v0, s[40:43], s49 offen offset:1024 sc1\n"
    "s_add_u32 s48, s48, 196608\n"
    "s_add_u32 s49, s48, 4096\n"
    "buffer_load_dwordx4 v[184:187], v0, s[40:43], s48 offen sc1\n"
    "buffer_load_dwordx4 v[188:191], v0, s[40:43], s48 offen offset:1024 sc1\n"
    "buffer_load_dwordx4 v[192:195], v0, s[40:43], s48 offen offset:2048 sc1\n"
    "buffer_load_dwordx4 v[196:199], v0, s[40:43], s48 offen offset:3072 sc1\n"
    "buffer_load_dwordx4 v[200:203], v0, s[40:43], s49 offen sc1\n"
    "buffer_load_dwordx4 v[204:207], v0, s[40:43], s49 offen offset:1024 sc1\n"
    "s_add_u32 s48, s48, 196608\n"
    "s_mul_i32 s48, s45, 6144\n"
    "v_add_u32 v6, s48, v0\n"
    "s_waitcnt vmcnt(41)\n"
    "v_pk_add_f32 v[16:17], v[16:17], v[40:41]\n"
    "v_pk_add_f32 v[18:19], v[18:19], v[42:43]\n"
    "s_waitcnt vmcnt(40)\n"
    "v_pk_add_f32 v[20:21], v[20:21], v[44:45]\n"
    "v_pk_add_f32 v[22:23], v[22:23], v[46:47]\n"
    "s_waitcnt vmcnt(39)\n"
    "v_pk_add_f32 v[24:25], v[24:25], v[48:49]\n"
    "v_pk_add_f32 v[26:27], v[26:27], v[50:51]\n"
    "s_waitcnt vmcnt(38)\n"
    "v_pk_add_f32 v[28:29], v[28:29], v[52:53]\n"
    "v_pk_add_f32 v[30:31], v[30:31], v[54:55]\n"
    "s_waitcnt vmcnt(37)\n"
    "v_pk_add_f32 v[32:33], v[32:33], v[56:57]\n"
    "v_pk_add_f32 v[34:35], v[34:35], v[58:59]\n"
    "s_waitcnt vmcnt(36)\n"
    "v_pk_add_f32 v[36:37], v[36:37], v[60:61]\n"
    "v_pk_add_f32 v[38:39], v[38:39], v[62:63]\n"
    "s_waitcnt vmcnt(35)\n"
    "v_pk_add_f32 v[16:17], v[16:17], v[64:65]\n"
    "v_pk_add_f32 v[18:19], v[18:19], v[66:67]\n"
    "s_waitcnt vmcnt(34)\n"
    "v_pk_add_f32 v[20:21], v[20:21], v[68:69]\n"
    "v_pk_add_f32 v[22:23], v[22:23], v[70:71]\n"
    "s_waitcnt vmcnt(33)\n"
    "v_pk_add_f32 v[24:25], v[24:25], v[72:73]\n"
    "v_pk_add_f32 v[26:27], v[26:27], v[74:75]\n"
    "s_waitcnt vmcnt(32)\n"
    "v_pk_add_f32 v[28:29], v[28:29], v[76:77]\n"
    "v_pk_add_f32 v[30:31], v[30:31], v[78:79]\n"
    "s_waitcnt vmcnt(31)\n"
    "v_pk_add_f32 v[32:33], v[32:33], v[80:81]\n"
    "v_pk_add_f32 v[34:35], v[34:35], v[82:83]\n"
    "s_waitcnt vmcnt(30)\n"
    "v_pk_add_f32 v[36:37], v[36:37], v[84:85]\n"
    "v_pk_add_f32 v[38:39], v[38:39], v[86:87]\n"
    "s_waitcnt vmcnt(29)\n"
    "v_pk_add_f32 v[16:17], v[16:17], v[88:89]\n"
    "v_pk_add_f32 v[18:19], v[18:19], v[90:91]\n"
    "s_waitcnt vmcnt(28)\n"
    "v_pk_add_f32 v[20:21], v[20:21], v[92:93]\n"
    "v_pk_add_f32 v[22:23], v[22:23], v[94:95]\n"
    "s_waitcnt vmcnt(27)\n"
    "v_pk_add_f32 v[24:25], v[24:25], v[96:97]\n"
    "v_pk_add_f32 v[26:27], v[26:27], v[98:99]\n"
    "s_waitcnt vmcnt(26)\n"
    "v_pk_add_f32 v[28:29], v[28:29], v[100:101]\n"
    "v_pk_add_f32 v[30:31], v[30:31], v[102:103]\n"
    "s_waitcnt vmcnt(25)\n"
    "v_pk_add_f32 v[32:33], v[32:33], v[104:105]\n"
    "v_pk_add_f32 v[34:35], v[34:35], v[106:107]\n"
    "s_waitcnt vmcnt(24)\n"
    "v_pk_add_f32 v[36:37], v[36:37], v[108:109]\n"
    "v_pk_add_f32 v[38:39], v[38:39], v[110:111]\n"
    "s_waitcnt vmcnt(23)\n"
    "v_pk_add_f32 v[16:17], v[16:17], v[112:113]\n"
    "v_pk_add_f32 v[18:19], v[18:19], v[114:115]\n"
    "s_waitcnt vmcnt(22)\n"
    "v_pk_add_f32 v[20:21], v[20:21], v[116:117]\n"
    "v_pk_add_f32 v[22:23], v[22:23], v[118:119]\n"
    "s_waitcnt vmcnt(21)\n"
    "v_pk_add_f32 v[24:25], v[24:25], v[120:121]\n"
    "v_pk_add_f32 v[26:27], v[26:27], v[122:123]\n"
    "s_waitcnt vmcnt(20)\n"
    "v_pk_add_f32 v[28:29], v[28:29], v[124:125]\n"
    "v_pk_add_f32 v[30:31], v[30:31], v[126:127]\n"
    "s_waitcnt vmcnt(19)\n"
    "v_pk_add_f32 v[32:33], v[32:33], v[128:129]\n"
    "v_pk_add_f32 v[34:35], v[34:35], v[130:131]\n"
    "s_waitcnt vmcnt(18)\n"
    "v_pk_add_f32 v[36:37], v[36:37], v[132:133]\n"
    "v_pk_add_f32 v[38:39], v[38:39], v[134:135]\n"
    "s_waitcnt vmcnt(17)\n"
    "v_pk_add_f32 v[16:17], v[16:17], v[136:137]\n"
    "v_pk_add_f32 v[18:19], v[18:19], v[138:139]\n"
    "s_waitcnt vmcnt(16)\n"
    "v_pk_add_f32 v[20:21], v[20:21], v[140:141]\n"
    "v_pk_add_f32 v[22:23], v[22:23], v[142:143]\n"
    "s_waitcnt vmcnt(15)\n"
    "v_pk_add_f32 v[24:25], v[24:25], v[144:145]\n"
    "v_pk_add_f32 v[26:27], v[26:27], v[146:147]\n"
    "s_waitcnt vmcnt(14)\n"
    "v_pk_add_f32 v[28:29], v[28:29], v[148:149]\n"
    "v_pk_add_f32 v[30:31], v[30:31], v[150:151]\n"
    "s_waitcnt vmcnt(13)\n"
    "v_pk_add_f32 v[32:33], v[32:33], v[152:153]\n"
    "v_pk_add_f32 v[34:35], v[34:35], v[154:155]\n"
    "s_waitcnt vmcnt(12)\n"
    "v_pk_add_f32 v[36:37], v[36:37], v[156:157]\n"
    "v_pk_add_f32 v[38:39], v[38:39], v[158:159]\n"
    "s_waitcnt vmcnt(11)\n"
    "v_pk_add_f32 v[16:17], v[16:17], v[160:161]\n"
    "v_pk_add_f32 v[18:19], v[18:19], v[162:163]\n"
    "s_waitcnt vmcnt(10)\n"
    "v_pk_add_f32 v[20:21], v[20:21], v[164:165]\n"
    "v_pk_add_f32 v[22:23], v[22:23], v[166:167]\n"
    "s_waitcnt vmcnt(9)\n"
    "v_pk_add_f32 v[24:25], v[24:25], v[168:169]\n"
    "v_pk_add_f32 v[26:27], v[26:27], v[170:171]\n"
    "s_waitcnt vmcnt(8)\n"
    "v_pk_add_f32 v[28:29], v[28:29], v[172:173]\n"
    "v_pk_add_f32 v[30:31], v[30:31], v[174:175]\n"
    "s_waitcnt vmcnt(7)\n"
    "v_pk_add_f32 v[32:33], v[32:33], v[176:177]\n"
    "v_pk_add_f32 v[34:35], v[34:35], v[178:179]\n"
    "s_waitcnt vmcnt(6)\n"
    "v_pk_add_f32 v[36:37], v[36:37], v[180:181]\n"
    "v_pk_add_f32 v[38:39], v[38:39], v[182:183]\n"
    "s_waitcnt vmcnt(5)\n"
    "v_pk_add_f32 v[16:17], v[16:17], v[184:185]\n"
    "v_pk_add_f32 v[18:19], v[18:19], v[186:187]\n"
    "s_waitcnt vmcnt(4)\n"
    "v_pk_add_f32 v[20:21], v[20:21], v[188:189]\n"
    "v_pk_add_f32 v[22:23], v[22:23], v[190:191]\n"
    "s_waitcnt vmcnt(3)\n"
    "v_pk_add_f32 v[24:25], v[24:25], v[192:193]\n"
    "v_pk_add_f32 v[26:27], v[26:27], v[194:195]\n"
    "s_waitcnt vmcnt(2)\n"
    "v_pk_add_f32 v[28:29], v[28:29], v[196:197]\n"
    "v_pk_add_f32 v[30:31], v[30:31], v[198:199]\n"
    "s_waitcnt vmcnt(1)\n"
    "v_pk_add_f32 v[32:33], v[32:33], v[200:201]\n"
    "v_pk_add_f32 v[34:35], v[34:35], v[202:203]\n"
    "s_waitcnt vmcnt(0)\n"
    "v_pk_add_f32 v[36:37], v[36:37], v[204:205]\n"
    "v_pk_add_f32 v[38:39], v[38:39], v[206:207]\n"
    "ds_write_b128 v6, v[16:19] offset:0\n"
    "ds_write_b128 v6, v[20:23] offset:1024\n"
    "ds_write_b128 v6, v[24:27] offset:2048\n"
    "ds_write_b128 v6, v[28:31] offset:3072\n"
    "ds_write_b128 v6, v[32:35] offset:4096\n"
    "ds_write_b128 v6, v[36:39] offset:5120\n"
    "s_waitcnt lgkmcnt(0)\n"
    "s_barrier\n"
    "s_mul_i32 s50, s27, 7680\n"
    "s_add_u32 s50, s50, 161112064\n"
    "s_and_b32 s48, s26, 3\n"
    "s_lshl_b32 s48, s48, 6\n"
    "s_add_u32 s50, s50, s48\n"
    "s_add_u32 s48, s45, 0\n"
    "s_min_u32 s48, s48, 5\n"
    "s_lshl_b32 s49, s48, 10\n"
    "v_add_u32 v6, s49, v0\n"
    "ds_read_b128 v[16:19], v6 offset:0\n"
    "ds_read_b128 v[20:23], v6 offset:6144\n"
    "ds_read_b128 v[24:27], v6 offset:12288\n"
    "ds_read_b128 v[28:31], v6 offset:18432\n"
    "s_lshr_b32 s49, s48, 1\n"
    "s_lshl_b32 s49, s49, 2\n"
    "v_lshrrev_b32 v8, 4, v15\n"
    "v_add_u32 v8, s49, v8\n"
    "s_and_b32 s49, s48, 1\n"
    "s_lshl_b32 s49, s49, 4\n"
    "v_and_b32 v9, 15, v15\n"
    "v_add_u32 v9, s49, v9\n"
    "v_lshrrev_b32 v4, 2, v9\n"
    "v_mul_u32_u24 v4, 5, v4\n"
    "v_lshrrev_b32 v5, 1, v8\n"
    "v_add_u32 v4, v4, v5\n"
    "v_lshlrev_b32 v4, 8, v4\n"
    "v_and_b32 v5, 3, v9\n"
    "v_lshl_add_u32 v4, v5, 4, v4\n"
    "v_and_b32 v5, 1, v8\n"
    "v_lshl_add_u32 v10, v5, 3, v4\n"
    "v_cmp_gt_u32 vcc, 10, v8\n"
    "v_cmp_gt_u32 s[56:57], 24, v9\n"
    "s_nop 0\n"
    "s_and_b64 vcc, vcc, s[56:57]\n"
    "v_mov_b32 v4, 2147483632\n"
    "v_cndmask_b32 v10, v4, v10, vcc\n"
    "s_waitcnt lgkmcnt(2)\n"
    "v_pk_add_f32 v[16:17], v[16:17], v[20:21]\n"
    "s_waitcnt lgkmcnt(0)\n"
    "v_pk_add_f32 v[24:25], v[24:25], v[28:29]\n"
    "v_pk_add_f32 v[16:17], v[16:17], v[24:25]\n"
    "v_pk_add_f32 v[18:19], v[18:19], v[22:23]\n"
    "v_pk_add_f32 v[26:27], v[26:27], v[30:31]\n"
    "v_pk_add_f32 v[18:19], v[18:19], v[26:27]\n"
    "v_cvt_pk_bf16_f32 v32, v16, v17\n"
    "v_cvt_pk_bf16_f32 v33, v18, v19\n"
    "buffer_store_dwordx2 v[32:33], v10, s[40:43], s50 offen sc1\n"
    "s_add_u32 s48, s45, 4\n"
    "s_min_u32 s48, s48, 5\n"
    "s_lshl_b32 s49, s48, 10\n"
    "v_add_u32 v6, s49, v0\n"
    "ds_read_b128 v[48:51], v6 offset:0\n"
    "ds_read_b128 v[52:55], v6 offset:6144\n"
    "ds_read_b128 v[56:59], v6 offset:12288\n"
    "ds_read_b128 v[60:63], v6 offset:18432\n"
    "s_lshr_b32 s49, s48, 1\n"
    "s_lshl_b32 s49, s49, 2\n"
    "v_lshrrev_b32 v8, 4, v15\n"
    "v_add_u32 v8, s49, v8\n"
    "s_and_b32 s49, s48, 1\n"
    "s_lshl_b32 s49, s49, 4\n"
    "v_and_b32 v9, 15, v15\n"
    "v_add_u32 v9, s49, v9\n"
    "v_lshrrev_b32 v4, 2, v9\n"
    "v_mul_u32_u24 v4, 5, v4\n"
    "v_lshrrev_b32 v5, 1, v8\n"
    "v_add_u32 v4, v4, v5\n"
    "v_lshlrev_b32 v4, 8, v4\n"
    "v_and_b32 v5, 3, v9\n"
    "v_lshl_add_u32 v4, v5, 4, v4\n"
    "v_and_b32 v5, 1, v8\n"
    "v_lshl_add_u32 v11, v5, 3, v4\n"
    "v_cmp_gt_u32 vcc, 10, v8\n"
    "v_cmp_gt_u32 s[56:57], 24, v9\n"
    "s_nop 0\n"
    "s_and_b64 vcc, vcc, s[56:57]\n"
    "v_mov_b32 v4, 2147483632\n"
    "v_cndmask_b32 v11, v4, v11, vcc\n"
    "s_waitcnt lgkmcnt(2)\n"
    "v_pk_add_f32 v[48:49], v[48:49], v[52:53]\n"
    "s_waitcnt lgkmcnt(0)\n"
    "v_pk_add_f32 v[56:57], v[56:57], v[60:61]\n"
    "v_pk_add_f32 v[48:49], v[48:49], v[56:57]\n"
    "v_pk_add_f32 v[50:51], v[50:51], v[54:55]\n"
    "v_pk_add_f32 v[58:59], v[58:59], v[62:63]\n"
    "v_pk_add_f32 v[50:51], v[50:51], v[58:59]\n"
    "v_cvt_pk_bf16_f32 v64, v48, v49\n"
    "v_cvt_pk_bf16_f32 v65, v50, v51\n"
    "buffer_store_dwordx2 v[64:65], v11, s[40:43], s50 offen sc1\n"
    "s_lshl_b32 s54, s21, 7\n"
    "s_add_u32 s54, s54, 2364416\n"
    "s_branch hp_done_%=\n"
    "hp_b2_%=:\n"
    "s_sub_u32 s26, s20, 288\n"
    "s_mul_i32 s50, s45, 204800\n"
    "s_add_u32 s50, s50, 148758528\n"
    "buffer_load_dwordx4 v[128:131], v0, s[40:43], s50 offen\n"
    "buffer_load_dwordx4 v[132:135], v0, s[40:43], s50 offen offset:1024\n"
    "buffer_load_dwordx4 v[136:139], v0, s[40:43], s50 offen offset:2048\n"
    "buffer_load_dwordx4 v[140:143], v0, s[40:43], s50 offen offset:3072\n"
    "s_add_u32 s50, s50, 4096\n"
    "buffer_load_dwordx4 v[144:147], v0, s[40:43], s50 offen\n"
    "buffer_load_dwordx4 v[148:151], v0, s[40:43], s50 offen offset:1024\n"
    "buffer_load_dwordx4 v[152:155], v0, s[40:43], s50 offen offset:2048\n"
    "buffer_load_dwordx4 v[156:159], v0, s[40:43], s50 offen offset:3072\n"
    "s_add_u32 s50, s50, 4096\n"
    "buffer_load_dwordx4 v[160:163], v0, s[40:43], s50 offen\n"
    "buffer_load_dwordx4 v[164:167], v0, s[40:43], s50 offen offset:1024\n"
    "buffer_load_dwordx4 v[168:171], v0, s[40:43], s50 offen offset:2048\n"
    "buffer_load_dwordx4 v[172:175], v0, s[40:43], s50 offen offset:3072\n"
    "s_add_u32 s50, s50, 4096\n"
    "buffer_load_dwordx4 v[176:179], v0, s[40:43], s50 offen\n"
    "buffer_load_dwordx4 v[180:183], v0, s[40:43], s50 offen offset:1024\n"
    "buffer_load_dwordx4 v[184:187], v0, s[40:43], s50 offen offset:2048\n"
    "buffer_load_dwordx4 v[188:191], v0, s[40:43], s50 offen offset:3072\n"
    "s_add_u32 s50, s50, 4096\n"
    "buffer_load_dwordx4 v[192:195], v0, s[40:43], s50 offen\n"
    "buffer_load_dwordx4 v[196:199], v0, s[40:43], s50 offen offset:1024\n"
    "buffer_load_dwordx4 v[200:203], v0, s[40:43], s50 offen offset:2048\n"
    "buffer_load_dwordx4 v[204:207], v0, s[40:43], s50 offen offset:3072\n"
    "s_add_u32 s50, s50, 4096\n"
    "buffer_load_dwordx4 v[208:211], v0, s[40:43], s50 offen\n"
    "buffer_load_dwordx4 v[212:215], v0, s[40:43], s50 offen offset:1024\n"
    "buffer_load_dwordx4 v[216:219], v0, s[40:43], s50 offen offset:2048\n"
    "buffer_load_dwordx4 v[220:223], v0, s[40:43], s50 offen offset:3072\n"
    "s_add_u32 s50, s50, 4096\n"
    "buffer_load_dwordx4 v[224:227], v0, s[40:43], s50 offen\n"
    "buffer_load_dwordx4 v[228:231], v0, s[40:43], s50 offen offset:1024\n"
    "buffer_load_dwordx4 v[232:235], v0, s[40:43], s50 offen offset:2048\n"
    "buffer_load_dwordx4 v[236:239], v0, s[40:43], s50 offen offset:3072\n"
    "s_add_u32 s50, s50, 4096\n"
    "buffer_load_dwordx4 v[240:243], v0, s[40:43], s50 offen\n"
    "buffer_load_dwordx4 v[244:247], v0, s[40:43], s50 offen offset:1024\n"
    "buffer_load_dwordx4 v[248:251], v0, s[40:43], s50 offen offset:2048\n"
    "buffer_load_dwordx4 v[252:255], v0, s[40:43], s50 offen offset:3072\n"
    "v_lshlrev_b32 v4, 7, v15\n"
    "s_add_u32 s48, s50, 4096\n"
    "buffer_load_dword v24, v4, s[40:43], s48 offen\n"
    "s_add_u32 s48, s48, 8192\n"
    "buffer_load_dword v25, v4, s[40:43], s48 offen\n"
    "s_add_u32 s48, s48, 8192\n"
    "buffer_load_dword v26, v4, s[40:43], s48 offen\n"
    "s_add_u32 s48, s48, 8192\n"
    "buffer_load_dword v27, v4, s[40:43], s48 offen\n"
    "s_add_u32 s48, s48, 8192\n"
    "buffer_load_dword v28, v4, s[40:43], s48 offen\n"
    "s_add_u32 s48, s48, 8192\n"
    "buffer_load_dword v29, v4, s[40:43], s48 offen\n"
    "s_add_u32 s48, s48, 8192\n"
    "buffer_load_dword v30, v4, s[40:43], s48 offen\n"
    "s_add_u32 s48, s48, 8192\n"
    "buffer_load_dword v31, v4, s[40:43], s48 offen\n"
    "s_add_u32 s48, s48, 8192\n"
    "buffer_load_dword v32, v4, s[40:43], s48 offen\n"
    "s_add_u32 s48, s48, 8192\n"
    "buffer_load_dword v33, v4, s[40:43], s48 offen\n"
    "s_add_u32 s48, s48, 8192\n"
    "buffer_load_dword v34, v4, s[40:43], s48 offen\n"
    "s_add_u32 s48, s48, 8192\n"
    "buffer_load_dword v35, v4, s[40:43], s48 offen\n"
    "s_add_u32 s48, s48, 8192\n"
    "buffer_load_dword v36, v4, s[40:43], s48 offen\n"
    "s_add_u32 s48, s48, 8192\n"
    "buffer_load_dword v37, v4, s[40:43], s48 offen\n"
    "s_add_u32 s48, s48, 8192\n"
    "buffer_load_dword v38, v4, s[40:43], s48 offen\n"
    "s_add_u32 s48, s48, 8192\n"
    "buffer_load_dword v39, v4, s[40:43], s48 offen\n"
    "s_add_u32 s48, s48, 8192\n"
    "buffer_load_dword v40, v4, s[40:43], s48 offen\n"
    "s_add_u32 s48, s48, 8192\n"
    "buffer_load_dword v41, v4, s[40:43], s48 offen\n"
    "s_add_u32 s48, s48, 8192\n"
    "buffer_load_dword v42, v4, s[40:43], s48 offen\n"
    "s_add_u32 s48, s48, 8192\n"
    "buffer_load_dword v43, v4, s[40:43], s48 offen\n"
    "s_add_u32 s48, s48, 8192\n"
    "buffer_load_dword v44, v4, s[40:43], s48 offen\n"
    "s_add_u32 s48, s48, 8192\n"
    "v_mov_b32 v16, 0\n"
    "v_mov_b32 v17, 0\n"
    "v_mov_b32 v18, 0\n"
    "v_mov_b32 v19, 0\n"
    "s_mul_i32 s48, s45, 3840\n"
    "s_add_u32 s48, s48, 61440\n"
    "v_add_u32 v4, s48, v0\n"
    "ds_write_b128 v4, v[16:19] offset:0\n"
    "ds_write_b128 v4, v[16:19] offset:1024\n"
    "ds_write_b128 v4, v[16:19] offset:2048\n"
    "ds_write_b128 v4, v[16:19] offset:2816\n"
    "s_mul_i32 s27, s45, 15360\n"
    "v_add_u32 v6, s27, v0\n"
    "v_and_b32 v7, 0xf0, v0\n"
    "v_add_u32 v7, s27, v7\n"
    "v_mov_b32 v8, 61440\n"
    "v_bfe_u32 v4, v15, 2, 2\n"
    "v_lshrrev_b32 v5, 4, v15\n"
    "v_cmp_eq_u32 vcc, v4, v5\n"
    "s_nop 1\n"
    "v_cndmask_b32 v7, v8, v7, vcc\n"
    "v_and_b32 v8, 0x1f0, v0\n"
    "v_lshrrev_b32 v4, 5, v15\n"
    "v_lshl_add_u32 v8, v4, 10, v8\n"
    "s_mul_i32 s48, s45, 5120\n"
    "s_add_u32 s48, s48, 76800\n"
    "v_add_u32 v9, s48, v0\n"
    "s_lshl_b32 s54, s21, 7\n"
    "s_add_u32 s54, s54, 2364416\n"
    "s_waitcnt lgkmcnt(0)\n"
    "s_cmp_lg_u32 s45, 0\n"
    "s_cbranch_scc1 hpb2_dep_ok_%=\n"
    "s_mov_b32 s60, 0\n"
    "hpb2_dep_%=:\n"
    "buffer_load_dword v4, off, s[40:43], s54 sc1\n"
    "s_waitcnt vmcnt(0)\n"
    "v_readfirstlane_b32 s59, v4\n"
    "s_nop 0\n"
    "s_cmp_ge_u32 s59, 32\n"
    "s_nop 2\n"
    "s_cbranch_scc1 hpb2_dep_ok_%=\n"
    "s_sleep 3\n"
    "s_add_u32 s60, s60, 1\n"
    "s_cmp_gt_u32 s60, 65536\n"
    "s_cbranch_scc1 hp_abort_%=\n"
    "s_and_b32 s59, s60, 63\n"
    "s_cbranch_scc1 hpb2_dep_%=\n"
    "v_mov_b32 v4, 1024\n"
    "buffer_load_dword v4, v4, s[40:43], s58 offen sc1\n"
    "s_waitcnt vmcnt(0)\n"
    "v_readfirstlane_b32 s59, v4\n"
    "s_nop 0\n"
    "s_cmp_lg_u32 s59, 0\n"
    "s_nop 2\n"
    "s_cbranch_scc1 hp_end_%=\n"
    "s_branch hpb2_dep_%=\n"
    "hpb2_dep_ok_%=:\n"
    "s_barrier\n"
    "s_add_u32 s48, s27, 161112064\n"
    "buffer_load_dwordx4 v[56:59], v0, s[40:43], s48 offen sc1\n"
    "buffer_load_dwordx4 v[60:63], v0, s[40:43], s48 offen offset:1024 sc1\n"
    "buffer_load_dwordx4 v[64:67], v0, s[40:43], s48 offen offset:2048 sc1\n"
    "buffer_load_dwordx4 v[68:71], v0, s[40:43], s48 offen offset:3072 sc1\n"
    "s_add_u32 s48, s48, 4096\n"
    "buffer_load_dwordx4 v[72:75], v0, s[40:43], s48 offen sc1\n"
    "buffer_load_dwordx4 v[76:79], v0, s[40:43], s48 offen offset:1024 sc1\n"
    "buffer_load_dwordx4 v[80:83], v0, s[40:43], s48 offen offset:2048 sc1\n"
    "buffer_load_dwordx4 v[84:87], v0, s[40:43], s48 offen offset:3072 sc1\n"
    "s_add_u32 s48, s48, 4096\n"
    "buffer_load_dwordx4 v[88:91], v0, s[40:43], s48 offen sc1\n"
    "buffer_load_dwordx4 v[92:95], v0, s[40:43], s48 offen offset:1024 sc1\n"
    "s_waitcnt vmcnt(61)\n"
    "buffer_load_dwordx4 v[96:99], v0, s[40:43], s48 offen offset:2048 sc1\n"
    "buffer_load_dwordx4 v[100:103], v0, s[40:43], s48 offen offset:3072 sc1\n"
    "s_waitcnt vmcnt(61)\n"
    "s_add_u32 s48, s48, 4096\n"
    "buffer_load_dwordx4 v[104:107], v0, s[40:43], s48 offen sc1\n"
    "buffer_load_dwordx4 v[108:111], v0, s[40:43], s48 offen offset:1024 sc1\n"
    "s_waitcnt vmcnt(61)\n"
    "buffer_load_dwordx4 v[112:115], v0, s[40:43], s48 offen offset:2048 sc1\n"
    "s_lshl3_add_u32 s28, s26, s45\n"
    "s_mul_i32 s28, s28, 10240\n"
    "s_add_u32 s48, s28, 149577728\n"
    "s_add_u32 s49, s48, 4096\n"
    "buffer_load_dwordx4 v[16:19], v0, s[40:43], s48 offen sc1\n"
    "s_waitcnt vmcnt(61)\n"
    "buffer_load_dwordx4 v[20:23], v0, s[40:43], s48 offen offset:1024 sc1\n"
    "s_waitcnt vmcnt(34)\n"
    "buffer_load_dwordx4 v[24:27], v0, s[40:43], s48 offen offset:2048 sc1\n"
    "s_waitcnt vmcnt(31)\n"
    "buffer_load_dwordx4 v[28:31], v0, s[40:43], s48 offen offset:3072 sc1\n"
    "s_waitcnt vmcnt(28)\n"
    "buffer_load_dwordx4 v[32:35], v0, s[40:43], s49 offen sc1\n"
    "s_add_u32 s48, s28, 149582848\n"
    "s_add_u32 s49, s48, 4096\n"
    "s_waitcnt vmcnt(25)\n"
    "buffer_load_dwordx4 v[36:39], v0, s[40:43], s48 offen sc1\n"
    "s_waitcnt vmcnt(22)\n"
    "buffer_load_dwordx4 v[40:43], v0, s[40:43], s48 offen offset:1024 sc1\n"
    "s_waitcnt vmcnt(22)\n"
    "buffer_load_dwordx4 v[44:47], v0, s[40:43], s48 offen offset:2048 sc1\n"
    "buffer_load_dwordx4 v[48:51], v0, s[40:43], s48 offen offset:3072 sc1\n"
    "buffer_load_dwordx4 v[52:55], v0, s[40:43], s49 offen sc1\n"
    "s_waitcnt vmcnt(24)\n"
    "ds_write_b128 v6, v[56:59] offset:0\n"
    "s_waitcnt vmcnt(23)\n"
    "ds_write_b128 v6, v[60:63] offset:1024\n"
    "s_waitcnt vmcnt(22)\n"
    "ds_write_b128 v6, v[64:67] offset:2048\n"
    "s_waitcnt vmcnt(21)\n"
    "ds_write_b128 v6, v[68:71] offset:3072\n"
    "s_waitcnt vmcnt(20)\n"
    "ds_write_b128 v6, v[72:75] offset:4096\n"
    "s_waitcnt vmcnt(19)\n"
    "ds_write_b128 v6, v[76:79] offset:5120\n"
    "s_waitcnt vmcnt(18)\n"
    "ds_write_b128 v6, v[80:83] offset:6144\n"
    "s_waitcnt vmcnt(17)\n"
    "ds_write_b128 v6, v[84:87] offset:7168\n"
    "s_waitcnt vmcnt(16)\n"
    "ds_write_b128 v6, v[88:91] offset:8192\n"
    "s_waitcnt vmcnt(15)\n"
    "ds_write_b128 v6, v[92:95] offset:9216\n"
    "s_waitcnt vmcnt(14)\n"
    "ds_write_b128 v6, v[96:99] offset:10240\n"
    "s_waitcnt vmcnt(13)\n"
    "ds_write_b128 v6, v[100:103] offset:11264\n"
    "s_waitcnt vmcnt(12)\n"
    "ds_write_b128 v6, v[104:107] offset:12288\n"
    "s_waitcnt vmcnt(11)\n"
    "ds_write_b128 v6, v[108:111] offset:13312\n"
    "s_waitcnt vmcnt(10)\n"
    "ds_write_b128 v6, v[112:115] offset:14336\n"
    "s_waitcnt lgkmcnt(0)\n"
    "ds_read_b128 v[80:83], v7 offset:0\n"
    "ds_read_b128 v[84:87], v7 offset:256\n"
    "ds_read_b128 v[88:91], v7 offset:512\n"
    "ds_read_b128 v[92:95], v7 offset:768\n"
    "ds_read_b128 v[96:99], v7 offset:1024\n"
    "s_waitcnt vmcnt(9) lgkmcnt(4)\n"
    "v_mfma_f32_16x16x32_bf16 v[56:59], v[80:83], v[16:19], 0\n"
    "ds_read_b128 v[80:83], v7 offset:1280\n"
    "s_waitcnt vmcnt(8) lgkmcnt(4)\n"
    "v_mfma_f32_16x16x32_bf16 v[56:59], v[84:87], v[20:23], v[56:59]\n"
    "ds_read_b128 v[84:87], v7 offset:1536\n"
    "s_waitcnt vmcnt(7) lgkmcnt(4)\n"
    "v_mfma_f32_16x16x32_bf16 v[56:59], v[88:91], v[24:27], v[56:59]\n"
    "ds_read_b128 v[88:91], v7 offset:1792\n"
    "s_waitcnt vmcnt(6) lgkmcnt(4)\n"
    "v_mfma_f32_16x16x32_bf16 v[56:59], v[92:95], v[28:31], v[56:59]\n"
    "ds_read_b128 v[92:95], v7 offset:2048\n"
    "s_waitcnt vmcnt(5) lgkmcnt(4)\n"
    "v_mfma_f32_16x16x32_bf16 v[56:59], v[96:99], v[32:35], v[56:59]\n"
    "ds_read_b128 v[96:99], v7 offset:2304\n"
    "s_waitcnt lgkmcnt(4)\n"
    "v_mfma_f32_16x16x32_bf16 v[60:63], v[80:83], v[16:19], 0\n"
    "ds_read_b128 v[80:83], v7 offset:2560\n"
    "s_waitcnt lgkmcnt(4)\n"
    "v_mfma_f32_16x16x32_bf16 v[60:63], v[84:87], v[20:23], v[60:63]\n"
    "ds_read_b128 v[84:87], v7 offset:2816\n"
    "s_waitcnt lgkmcnt(4)\n"
    "v_mfma_f32_16x16x32_bf16 v[60:63], v[88:91], v[24:27], v[60:63]\n"
    "ds_read_b128 v[88:91], v7 offset:3072\n"
    "s_waitcnt lgkmcnt(4)\n"
    "v_mfma_f32_16x16x32_bf16 v[60:63], v[92:95], v[28:31], v[60:63]\n"
    "ds_read_b128 v[92:95], v7 offset:3328\n"
    "s_waitcnt lgkmcnt(4)\n"
    "v_mfma_f32_16x16x32_bf16 v[60:63], v[96:99], v[32:35], v[60:63]\n"
    "ds_read_b128 v[96:99], v7 offset:3584\n"
    "s_waitcnt lgkmcnt(4)\n"
    "v_mfma_f32_16x16x32_bf16 v[64:67], v[80:83], v[16:19], 0\n"
    "ds_read_b128 v[80:83], v7 offset:3840\n"
    "s_waitcnt lgkmcnt(4)\n"
    "v_mfma_f32_16x16x32_bf16 v[64:67], v[84:87], v[20:23], v[64:67]\n"
    "ds_read_b128 v[84:87], v7 offset:4096\n"
    "s_waitcnt lgkmcnt(4)\n"
    "v_mfma_f32_16x16x32_bf16 v[64:67], v[88:91], v[24:27], v[64:67]\n"
    "ds_read_b128 v[88:91], v7 offset:4352\n"
    "s_waitcnt lgkmcnt(4)\n"
    "v_mfma_f32_16x16x32_bf16 v[64:67], v[92:95], v[28:31], v[64:67]\n"
    "ds_read_b128 v[92:95], v7 offset:4608\n"
    "s_waitcnt lgkmcnt(4)\n"
    "v_mfma_f32_16x16x32_bf16 v[64:67], v[96:99], v[32:35], v[64:67]\n"
    "ds_read_b128 v[96:99], v7 offset:4864\n"
    "s_waitcnt lgkmcnt(4)\n"
    "v_mfma_f32_16x16x32_bf16 v[68:71], v[80:83], v[16:19], 0\n"
    "ds_read_b128 v[80:83], v7 offset:5120\n"
    "s_waitcnt lgkmcnt(4)\n"
    "v_mfma_f32_16x16x32_bf16 v[68:71], v[84:87], v[20:23], v[68:71]\n"
    "ds_read_b128 v[84:87], v7 offset:5376\n"
    "s_waitcnt lgkmcnt(4)\n"
    "v_mfma_f32_16x16x32_bf16 v[68:71], v[88:91], v[24:27], v[68:71]\n"
    "ds_read_b128 v[88:91], v7 offset:5632\n"
    "s_waitcnt lgkmcnt(4)\n"
    "v_mfma_f32_16x16x32_bf16 v[68:71], v[92:95], v[28:31], v[68:71]\n"
    "ds_read_b128 v[92:95], v7 offset:5888\n"
    "s_waitcnt lgkmcnt(4)\n"
    "v_mfma_f32_16x16x32_bf16 v[68:71], v[96:99], v[32:35], v[68:71]\n"
    "ds_read_b128 v[96:99], v7 offset:6144\n"
    "s_waitcnt lgkmcnt(4)\n"
    "v_mfma_f32_16x16x32_bf16 v[72:75], v[80:83], v[16:19], 0\n"
    "ds_read_b128 v[80:83], v7 offset:6400\n"
    "s_waitcnt lgkmcnt(4)\n"
    "v_mfma_f32_16x16x32_bf16 v[72:75], v[84:87], v[20:23], v[72:75]\n"
    "ds_read_b128 v[84:87], v7 offset:6656\n"
    "s_waitcnt lgkmcnt(4)\n"
    "v_mfma_f32_16x16x32_bf16 v[72:75], v[88:91], v[24:27], v[72:75]\n"
    "ds_read_b128 v[88:91], v7 offset:6912\n"
    "s_waitcnt lgkmcnt(4)\n"
    "v_mfma_f32_16x16x32_bf16 v[72:75], v[92:95], v[28:31], v[72:75]\n"
    "ds_read_b128 v[92:95], v7 offset:7168\n"
    "s_waitcnt lgkmcnt(4)\n"
    "v_mfma_f32_16x16x32_bf16 v[72:75], v[96:99], v[32:35], v[72:75]\n"
    "ds_read_b128 v[96:99], v7 offset:7424\n"
    "s_waitcnt lgkmcnt(4)\n"
    "v_mfma_f32_16x16x32_bf16 v[76:79], v[80:83], v[16:19], 0\n"
    "ds_read_b128 v[80:83], v7 offset:7680\n"
    "s_waitcnt lgkmcnt(4)\n"
    "v_mfma_f32_16x16x32_bf16 v[76:79], v[84:87], v[20:23], v[76:79]\n"
    "ds_read_b128 v[84:87], v7 offset:7936\n"
    "s_waitcnt lgkmcnt(4)\n"
    "v_mfma_f32_16x16x32_bf16 v[76:79], v[88:91], v[24:27], v[76:79]\n"
    "ds_read_b128 v[88:91], v7 offset:8192\n"
    "s_waitcnt lgkmcnt(4)\n"
    "v_mfma_f32_16x16x32_bf16 v[76:79], v[92:95], v[28:31], v[76:79]\n"
    "ds_read_b128 v[92:95], v7 offset:8448\n"
    "s_waitcnt lgkmcnt(4)\n"
    "v_mfma_f32_16x16x32_bf16 v[76:79], v[96:99], v[32:35], v[76:79]\n"
    "ds_read_b128 v[96:99], v7 offset:8704\n"
    "s_add_u32 s48, s28, 149618688\n"
    "s_add_u32 s49, s48, 4096\n"
    "buffer_load_dwordx4 v[16:19], v0, s[40:43], s48 offen sc1\n"
    "buffer_load_dwordx4 v[20:23], v0, s[40:43], s48 offen offset:1024 sc1\n"
    "buffer_load_dwordx4 v[24:27], v0, s[40:43], s48 offen offset:2048 sc1\n"
    "buffer_load_dwordx4 v[28:31], v0, s[40:43], s48 offen offset:3072 sc1\n"
    "buffer_load_dwordx4 v[32:35], v0, s[40:43], s49 offen sc1\n"
    "v_add_f32 v76, 0x358637bd, v76\n"
    "v_rcp_f32 v76, v76\n"
    "s_nop 0\n"
    "v_pk_mul_f32 v[56:57], v[56:57], v[76:77] op_sel_hi:[1,0]\n"
    "v_pk_mul_f32 v[58:59], v[58:59], v[76:77] op_sel_hi:[1,0]\n"
    "v_pk_mul_f32 v[60:61], v[60:61], v[76:77] op_sel_hi:[1,0]\n"
    "v_pk_mul_f32 v[62:63], v[62:63], v[76:77] op_sel_hi:[1,0]\n"
    "v_pk_mul_f32 v[64:65], v[64:65], v[76:77] op_sel_hi:[1,0]\n"
    "v_pk_mul_f32 v[66:67], v[66:67], v[76:77] op_sel_hi:[1,0]\n"
    "v_pk_mul_f32 v[68:69], v[68:69], v[76:77] op_sel_hi:[1,0]\n"
    "v_pk_mul_f32 v[70:71], v[70:71], v[76:77] op_sel_hi:[1,0]\n"
    "v_pk_mul_f32 v[72:73], v[72:73], v[76:77] op_sel_hi:[1,0]\n"
    "v_pk_mul_f32 v[74:75], v[74:75], v[76:77] op_sel_hi:[1,0]\n"
    "v_cvt_pk_bf16_f32 v100, v56, v57\n"
    "v_cvt_pk_bf16_f32 v101, v58, v59\n"
    "v_cvt_pk_bf16_f32 v102, v60, v61\n"
    "v_cvt_pk_bf16_f32 v103, v62, v63\n"
    "v_cvt_pk_bf16_f32 v104, v64, v65\n"
    "v_cvt_pk_bf16_f32 v105, v66, v67\n"
    "v_cvt_pk_bf16_f32 v106, v68, v69\n"
    "v_cvt_pk_bf16_f32 v107, v70, v71\n"
    "v_cvt_pk_bf16_f32 v108, v72, v73\n"
    "v_cvt_pk_bf16_f32 v109, v74, v75\n"
    "s_waitcnt vmcnt(9) lgkmcnt(4)\n"
    "v_mfma_f32_16x16x32_bf16 v[56:59], v[80:83], v[36:39], 0\n"
    "ds_read_b128 v[80:83], v7 offset:8960\n"
    "s_waitcnt vmcnt(8) lgkmcnt(4)\n"
    "v_mfma_f32_16x16x32_bf16 v[56:59], v[84:87], v[40:43], v[56:59]\n"
    "ds_read_b128 v[84:87], v7 offset:9216\n"
    "s_waitcnt vmcnt(7) lgkmcnt(4)\n"
    "v_mfma_f32_16x16x32_bf16 v[56:59], v[88:91], v[44:47], v[56:59]\n"
    "ds_read_b128 v[88:91], v7 offset:9472\n"
    "s_waitcnt vmcnt(6) lgkmcnt(4)\n"
    "v_mfma_f32_16x16x32_bf16 v[56:59], v[92:95], v[48:51], v[56:59]\n"
    "ds_read_b128 v[92:95], v7 offset:9728\n"
    "s_waitcnt vmcnt(5) lgkmcnt(4)\n"
    "v_mfma_f32_16x16x32_bf16 v[56:59], v[96:99], v[52:55], v[56:59]\n"
    "ds_read_b128 v[96:99], v7 offset:9984\n"
    "s_waitcnt lgkmcnt(4)\n"
    "v_mfma_f32_16x16x32_bf16 v[60:63], v[80:83], v[36:39], 0\n"
    "ds_read_b128 v[80:83], v7 offset:10240\n"
    "s_waitcnt lgkmcnt(4)\n"
    "v_mfma_f32_16x16x32_bf16 v[60:63], v[84:87], v[40:43], v[60:63]\n"
    "ds_read_b128 v[84:87], v7 offset:10496\n"
    "s_waitcnt lgkmcnt(4)\n"
    "v_mfma_f32_16x16x32_bf16 v[60:63], v[88:91], v[44:47], v[60:63]\n"
    "ds_read_b128 v[88:91], v7 offset:10752\n"
    "s_waitcnt lgkmcnt(4)\n"
    "v_mfma_f32_16x16x32_bf16 v[60:63], v[92:95], v[48:51], v[60:63]\n"
    "ds_read_b128 v[92:95], v7 offset:11008\n"
    "s_waitcnt lgkmcnt(4)\n"
    "v_mfma_f32_16x16x32_bf16 v[60:63], v[96:99], v[52:55], v[60:63]\n"
    "ds_read_b128 v[96:99], v7 offset:11264\n"
    "s_waitcnt lgkmcnt(4)\n"
    "v_mfma_f32_16x16x32_bf16 v[64:67], v[80:83], v[36:39], 0\n"
    "ds_read_b128 v[80:83], v7 offset:11520\n"
    "s_waitcnt lgkmcnt(4)\n"
    "v_mfma_f32_16x16x32_bf16 v[64:67], v[84:87], v[40:43], v[64:67]\n"
    "ds_read_b128 v[84:87], v7 offset:11776\n"
    "s_waitcnt lgkmcnt(4)\n"
    "v_mfma_f32_16x16x32_bf16 v[64:67], v[88:91], v[44:47], v[64:67]\n"
    "ds_read_b128 v[88:91], v7 offset:12032\n"
    "s_waitcnt lgkmcnt(4)\n"
    "v_mfma_f32_16x16x32_bf16 v[64:67], v[92:95], v[48:51], v[64:67]\n"
    "ds_read_b128 v[92:95], v7 offset:12288\n"
    "s_waitcnt lgkmcnt(4)\n"
    "v_mfma_f32_16x16x32_bf16 v[64:67], v[96:99], v[52:55], v[64:67]\n"
    "ds_read_b128 v[96:99], v7 offset:12544\n"
    "s_waitcnt lgkmcnt(4)\n"
    "v_mfma_f32_16x16x32_bf16 v[68:71], v[80:83], v[36:39], 0\n"
    "ds_read_b128 v[80:83], v7 offset:12800\n"
    "s_waitcnt lgkmcnt(4)\n"
    "v_mfma_f32_16x16x32_bf16 v[68:71], v[84:87], v[40:43], v[68:71]\n"
    "ds_read_b128 v[84:87], v7 offset:13056\n"
    "s_waitcnt lgkmcnt(4)\n"
    "v_mfma_f32_16x16x32_bf16 v[68:71], v[88:91], v[44:47], v[68:71]\n"
    "ds_read_b128 v[88:91], v7 offset:13312\n"
    "s_waitcnt lgkmcnt(4)\n"
    "v_mfma_f32_16x16x32_bf16 v[68:71], v[92:95], v[48:51], v[68:71]\n"
    "ds_read_b128 v[92:95], v7 offset:13568\n"
    "s_waitcnt lgkmcnt(4)\n"
    "v_mfma_f32_16x16x32_bf16 v[68:71], v[96:99], v[52:55], v[68:71]\n"
    "ds_read_b128 v[96:99], v7 offset:13824\n"
    "s_waitcnt lgkmcnt(4)\n"
    "v_mfma_f32_16x16x32_bf16 v[72:75], v[80:83], v[36:39], 0\n"
    "ds_read_b128 v[80:83], v7 offset:14080\n"
    "s_waitcnt lgkmcnt(4)\n"
    "v_mfma_f32_16x16x32_bf16 v[72:75], v[84:87], v[40:43], v[72:75]\n"
    "ds_read_b128 v[84:87], v7 offset:14336\n"
    "s_waitcnt lgkmcnt(4)\n"
    "v_mfma_f32_16x16x32_bf16 v[72:75], v[88:91], v[44:47], v[72:75]\n"
    "ds_read_b128 v[88:91], v7 offset:14592\n"
    "s_waitcnt lgkmcnt(4)\n"
    "v_mfma_f32_16x16x32_bf16 v[72:75], v[92:95], v[48:51], v[72:75]\n"
    "ds_read_b128 v[92:95], v7 offset:14848\n"
    "s_waitcnt lgkmcnt(4)\n"
    "v_mfma_f32_16x16x32_bf16 v[72:75], v[96:99], v[52:55], v[72:75]\n"
    "ds_read_b128 v[96:99], v7 offset:15104\n"
    "s_waitcnt lgkmcnt(4)\n"
    "v_mfma_f32_16x16x32_bf16 v[76:79], v[80:83], v[36:39], 0\n"
    "ds_read_b128 v[80:83], v7 offset:0\n"
    "s_waitcnt lgkmcnt(4)\n"
    "v_mfma_f32_16x16x32_bf16 v[76:79], v[84:87], v[40:43], v[76:79]\n"
    "ds_read_b128 v[84:87], v7 offset:256\n"
    "s_waitcnt lgkmcnt(4)\n"
    "v_mfma_f32_16x16x32_bf16 v[76:79], v[88:91], v[44:47], v[76:79]\n"
    "ds_read_b128 v[88:91], v7 offset:512\n"
    "s_waitcnt lgkmcnt(4)\n"
    "v_mfma_f32_16x16x32_bf16 v[76:79], v[92:95], v[48:51], v[76:79]\n"
    "ds_read_b128 v[92:95], v7 offset:768\n"
    "s_waitcnt lgkmcnt(4)\n"
    "v_mfma_f32_16x16x32_bf16 v[76:79], v[96:99], v[52:55], v[76:79]\n"
    "ds_read_b128 v[96:99], v7 offset:1024\n"
    "s_add_u32 s48, s28, 149623808\n"
    "s_add_u32 s49, s48, 4096\n"
    "buffer_load_dwordx4 v[36:39], v0, s[40:43], s48 offen sc1\n"
    "buffer_load_dwordx4 v[40:43], v0, s[40:43], s48 offen offset:1024 sc1\n"
    "buffer_load_dwordx4 v[44:47], v0, s[40:43], s48 offen offset:2048 sc1\n"
    "buffer_load_dwordx4 v[48:51], v0, s[40:43], s48 offen offset:3072 sc1\n"
    "buffer_load_dwordx4 v[52:55], v0, s[40:43], s49 offen sc1\n"
    "v_add_f32 v76, 0x358637bd, v76\n"
    "v_rcp_f32 v76, v76\n"
    "s_nop 0\n"
    "v_pk_mul_f32 v[56:57], v[56:57], v[76:77] op_sel_hi:[1,0]\n"
    "v_pk_mul_f32 v[58:59], v[58:59], v[76:77] op_sel_hi:[1,0]\n"
    "v_pk_mul_f32 v[60:61], v[60:61], v[76:77] op_sel_hi:[1,0]\n"
    "v_pk_mul_f32 v[62:63], v[62:63], v[76:77] op_sel_hi:[1,0]\n"
    "v_pk_mul_f32 v[64:65], v[64:65], v[76:77] op_sel_hi:[1,0]\n"
    "v_pk_mul_f32 v[66:67], v[66:67], v[76:77] op_sel_hi:[1,0]\n"
    "v_pk_mul_f32 v[68:69], v[68:69], v[76:77] op_sel_hi:[1,0]\n"
    "v_pk_mul_f32 v[70:71], v[70:71], v[76:77] op_sel_hi:[1,0]\n"
    "v_pk_mul_f32 v[72:73], v[72:73], v[76:77] op_sel_hi:[1,0]\n"
    "v_pk_mul_f32 v[74:75], v[74:75], v[76:77] op_sel_hi:[1,0]\n"
    "v_cvt_pk_bf16_f32 v110, v56, v57\n"
    "v_cvt_pk_bf16_f32 v111, v58, v59\n"
    "v_cvt_pk_bf16_f32 v112, v60, v61\n"
    "v_cvt_pk_bf16_f32 v113, v62, v63\n"
    "v_cvt_pk_bf16_f32 v114, v64, v65\n"
    "v_cvt_pk_bf16_f32 v115, v66, v67\n"
    "v_cvt_pk_bf16_f32 v116, v68, v69\n"
    "v_cvt_pk_bf16_f32 v117, v70, v71\n"
    "v_cvt_pk_bf16_f32 v118, v72, v73\n"
    "v_cvt_pk_bf16_f32 v119, v74, v75\n"
    "ds_write_b128 v9, v[100:103] offset:0\n"
    "ds_write_b128 v9, v[104:107] offset:1024\n"
    "ds_write_b128 v9, v[108:111] offset:2048\n"
    "ds_write_b128 v9, v[112:115] offset:3072\n"
    "ds_write_b128 v9, v[116:119] offset:4096\n"
    "s_waitcnt vmcnt(9) lgkmcnt(9)\n"
    "v_mfma_f32_16x16x32_bf16 v[56:59], v[80:83], v[16:19], 0\n"
    "ds_read_b128 v[80:83], v7 offset:1280\n"
    "s_waitcnt vmcnt(8) lgkmcnt(9)\n"
    "v_mfma_f32_16x16x32_bf16 v[56:59], v[84:87], v[20:23], v[56:59]\n"
    "ds_read_b128 v[84:87], v7 offset:1536\n"
    "s_waitcnt vmcnt(7) lgkmcnt(9)\n"
    "v_mfma_f32_16x16x32_bf16 v[56:59], v[88:91], v[24:27], v[56:59]\n"
    "ds_read_b128 v[88:91], v7 offset:1792\n"
    "s_waitcnt vmcnt(6) lgkmcnt(9)\n"
    "v_mfma_f32_16x16x32_bf16 v[56:59], v[92:95], v[28:31], v[56:59]\n"
    "ds_read_b128 v[92:95], v7 offset:2048\n"
    "s_waitcnt vmcnt(5) lgkmcnt(9)\n"
    "v_mfma_f32_16x16x32_bf16 v[56:59], v[96:99], v[32:35], v[56:59]\n"
    "ds_read_b128 v[96:99], v7 offset:2304\n"
    "s_waitcnt lgkmcnt(4)\n"
    "v_mfma_f32_16x16x32_bf16 v[60:63], v[80:83], v[16:19], 0\n"
    "ds_read_b128 v[80:83], v7 offset:2560\n"
    "s_waitcnt lgkmcnt(4)\n"
    "v_mfma_f32_16x16x32_bf16 v[60:63], v[84:87], v[20:23], v[60:63]\n"
    "ds_read_b128 v[84:87], v7 offset:2816\n"
    "s_waitcnt lgkmcnt(4)\n"
    "v_mfma_f32_16x16x32_bf16 v[60:63], v[88:91], v[24:27], v[60:63]\n"
    "ds_read_b128 v[88:91], v7 offset:3072\n"
    "s_waitcnt lgkmcnt(4)\n"
    "v_mfma_f32_16x16x32_bf16 v[60:63], v[92:95], v[28:31], v[60:63]\n"
    "ds_read_b128 v[92:95], v7 offset:3328\n"
    "s_waitcnt lgkmcnt(4)\n"
    "v_mfma_f32_16x16x32_bf16 v[60:63], v[96:99], v[32:35], v[60:63]\n"
    "ds_read_b128 v[96:99], v7 offset:3584\n"
    "s_waitcnt lgkmcnt(4)\n"
    "v_mfma_f32_16x16x32_bf16 v[64:67], v[80:83], v[16:19], 0\n"
    "ds_read_b128 v[80:83], v7 offset:3840\n"
    "s_waitcnt lgkmcnt(4)\n"
    "v_mfma_f32_16x16x32_bf16 v[64:67], v[84:87], v[20:23], v[64:67]\n"
    "ds_read_b128 v[84:87], v7 offset:4096\n"
    "s_waitcnt lgkmcnt(4)\n"
    "v_mfma_f32_16x16x32_bf16 v[64:67], v[88:91], v[24:27], v[64:67]\n"
    "ds_read_b128 v[88:91], v7 offset:4352\n"
    "s_waitcnt lgkmcnt(4)\n"
    "v_mfma_f32_16x16x32_bf16 v[64:67], v[92:95], v[28:31], v[64:67]\n"
    "ds_read_b128 v[92:95], v7 offset:4608\n"
    "s_waitcnt lgkmcnt(4)\n"
    "v_mfma_f32_16x16x32_bf16 v[64:67], v[96:99], v[32:35], v[64:67]\n"
    "ds_read_b128 v[96:99], v7 offset:4864\n"
    "s_waitcnt lgkmcnt(4)\n"
    "v_mfma_f32_16x16x32_bf16 v[68:71], v[80:83], v[16:19], 0\n"
    "ds_read_b128 v[80:83], v7 offset:5120\n"
    "s_waitcnt lgkmcnt(4)\n"
    "v_mfma_f32_16x16x32_bf16 v[68:71], v[84:87], v[20:23], v[68:71]\n"
    "ds_read_b128 v[84:87], v7 offset:5376\n"
    "s_waitcnt lgkmcnt(4)\n"
    "v_mfma_f32_16x16x32_bf16 v[68:71], v[88:91], v[24:27], v[68:71]\n"
    "ds_read_b128 v[88:91], v7 offset:5632\n"
    "s_waitcnt lgkmcnt(4)\n"
    "v_mfma_f32_16x16x32_bf16 v[68:71], v[92:95], v[28:31], v[68:71]\n"
    "ds_read_b128 v[92:95], v7 offset:5888\n"
    "s_waitcnt lgkmcnt(4)\n"
    "v_mfma_f32_16x16x32_bf16 v[68:71], v[96:99], v[32:35], v[68:71]\n"
    "ds_read_b128 v[96:99], v7 offset:6144\n"
    "s_waitcnt lgkmcnt(4)\n"
    "v_mfma_f32_16x16x32_bf16 v[72:75], v[80:83], v[16:19], 0\n"
    "ds_read_b128 v[80:83], v7 offset:6400\n"
    "s_waitcnt lgkmcnt(4)\n"
    "v_mfma_f32_16x16x32_bf16 v[72:75], v[84:87], v[20:23], v[72:75]\n"
    "ds_read_b128 v[84:87], v7 offset:6656\n"
    "s_waitcnt lgkmcnt(4)\n"
    "v_mfma_f32_16x16x32_bf16 v[72:75], v[88:91], v[24:27], v[72:75]\n"
    "ds_read_b128 v[88:91], v7 offset:6912\n"
    "s_waitcnt lgkmcnt(4)\n"
    "v_mfma_f32_16x16x32_bf16 v[72:75], v[92:95], v[28:31], v[72:75]\n"
    "ds_read_b128 v[92:95], v7 offset:7168\n"
    "s_waitcnt lgkmcnt(4)\n"
    "v_mfma_f32_16x16x32_bf16 v[72:75], v[96:99], v[32:35], v[72:75]\n"
    "ds_read_b128 v[96:99], v7 offset:7424\n"
    "s_waitcnt lgkmcnt(4)\n"
    "v_mfma_f32_16x16x32_bf16 v[76:79], v[80:83], v[16:19], 0\n"
    "ds_read_b128 v[80:83], v7 offset:7680\n"
    "s_waitcnt lgkmcnt(4)\n"
    "v_mfma_f32_16x16x32_bf16 v[76:79], v[84:87], v[20:23], v[76:79]\n"
    "ds_read_b128 v[84:87], v7 offset:7936\n"
    "s_waitcnt lgkmcnt(4)\n"
    "v_mfma_f32_16x16x32_bf16 v[76:79], v[88:91], v[24:27], v[76:79]\n"
    "ds_read_b128 v[88:91], v7 offset:8192\n"
    "s_waitcnt lgkmcnt(4)\n"
    "v_mfma_f32_16x16x32_bf16 v[76:79], v[92:95], v[28:31], v[76:79]\n"
    "ds_read_b128 v[92:95], v7 offset:8448\n"
    "s_waitcnt lgkmcnt(4)\n"
    "v_mfma_f32_16x16x32_bf16 v[76:79], v[96:99], v[32:35], v[76:79]\n"
    "ds_read_b128 v[96:99], v7 offset:8704\n"
    "s_nop 6\n"
    "v_add_f32 v76, 0x358637bd, v76\n"
    "v_rcp_f32 v76, v76\n"
    "s_nop 0\n"
    "v_pk_mul_f32 v[56:57], v[56:57], v[76:77] op_sel_hi:[1,0]\n"
    "v_pk_mul_f32 v[58:59], v[58:59], v[76:77] op_sel_hi:[1,0]\n"
    "v_pk_mul_f32 v[60:61], v[60:61], v[76:77] op_sel_hi:[1,0]\n"
    "v_pk_mul_f32 v[62:63], v[62:63], v[76:77] op_sel_hi:[1,0]\n"
    "v_pk_mul_f32 v[64:65], v[64:65], v[76:77] op_sel_hi:[1,0]\n"
    "v_pk_mul_f32 v[66:67], v[66:67], v[76:77] op_sel_hi:[1,0]\n"
    "v_pk_mul_f32 v[68:69], v[68:69], v[76:77] op_sel_hi:[1,0]\n"
    "v_pk_mul_f32 v[70:71], v[70:71], v[76:77] op_sel_hi:[1,0]\n"
    "v_pk_mul_f32 v[72:73], v[72:73], v[76:77] op_sel_hi:[1,0]\n"
    "v_pk_mul_f32 v[74:75], v[74:75], v[76:77] op_sel_hi:[1,0]\n"
    "v_cvt_pk_bf16_f32 v100, v56, v57\n"
    "v_cvt_pk_bf16_f32 v101, v58, v59\n"
    "v_cvt_pk_bf16_f32 v102, v60, v61\n"
    "v_cvt_pk_bf16_f32 v103, v62, v63\n"
    "v_cvt_pk_bf16_f32 v104, v64, v65\n"
    "v_cvt_pk_bf16_f32 v105, v66, v67\n"
    "v_cvt_pk_bf16_f32 v106, v68, v69\n"
    "v_cvt_pk_bf16_f32 v107, v70, v71\n"
    "v_cvt_pk_bf16_f32 v108, v72, v73\n"
    "v_cvt_pk_bf16_f32 v109, v74, v75\n"
    "s_waitcnt vmcnt(4) lgkmcnt(4)\n"
    "v_mfma_f32_16x16x32_bf16 v[56:59], v[80:83], v[36:39], 0\n"
    "ds_read_b128 v[80:83], v7 offset:8960\n"
    "s_waitcnt vmcnt(3) lgkmcnt(4)\n"
    "v_mfma_f32_16x16x32_bf16 v[56:59], v[84:87], v[40:43], v[56:59]\n"
    "ds_read_b128 v[84:87], v7 offset:9216\n"
    "s_waitcnt vmcnt(2) lgkmcnt(4)\n"
    "v_mfma_f32_16x16x32_bf16 v[56:59], v[88:91], v[44:47], v[56:59]\n"
    "ds_read_b128 v[88:91], v7 offset:9472\n"
    "s_waitcnt vmcnt(1) lgkmcnt(4)\n"
    "v_mfma_f32_16x16x32_bf16 v[56:59], v[92:95], v[48:51], v[56:59]\n"
    "ds_read_b128 v[92:95], v7 offset:9728\n"
    "s_waitcnt vmcnt(0) lgkmcnt(4)\n"
    "v_mfma_f32_16x16x32_bf16 v[56:59], v[96:99], v[52:55], v[56:59]\n"
    "ds_read_b128 v[96:99], v7 offset:9984\n"
    "s_waitcnt lgkmcnt(4)\n"
    "v_mfma_f32_16x16x32_bf16 v[60:63], v[80:83], v[36:39], 0\n"
    "ds_read_b128 v[80:83], v7 offset:10240\n"
    "s_waitcnt lgkmcnt(4)\n"
    "v_mfma_f32_16x16x32_bf16 v[60:63], v[84:87], v[40:43], v[60:63]\n"
    "ds_read_b128 v[84:87], v7 offset:10496\n"
    "s_waitcnt lgkmcnt(4)\n"
    "v_mfma_f32_16x16x32_bf16 v[60:63], v[88:91], v[44:47], v[60:63]\n"
    "ds_read_b128 v[88:91], v7 offset:10752\n"
    "s_waitcnt lgkmcnt(4)\n"
    "v_mfma_f32_16x16x32_bf16 v[60:63], v[92:95], v[48:51], v[60:63]\n"
    "ds_read_b128 v[92:95], v7 offset:11008\n"
    "s_waitcnt lgkmcnt(4)\n"
    "v_mfma_f32_16x16x32_bf16 v[60:63], v[96:99], v[52:55], v[60:63]\n"
    "ds_read_b128 v[96:99], v7 offset:11264\n"
    "s_waitcnt lgkmcnt(4)\n"
    "v_mfma_f32_16x16x32_bf16 v[64:67], v[80:83], v[36:39], 0\n"
    "ds_read_b128 v[80:83], v7 offset:11520\n"
    "s_waitcnt lgkmcnt(4)\n"
    "v_mfma_f32_16x16x32_bf16 v[64:67], v[84:87], v[40:43], v[64:67]\n"
    "ds_read_b128 v[84:87], v7 offset:11776\n"
    "s_waitcnt lgkmcnt(4)\n"
    "v_mfma_f32_16x16x32_bf16 v[64:67], v[88:91], v[44:47], v[64:67]\n"
    "ds_read_b128 v[88:91], v7 offset:12032\n"
    "s_waitcnt lgkmcnt(4)\n"
    "v_mfma_f32_16x16x32_bf16 v[64:67], v[92:95], v[48:51], v[64:67]\n"
    "ds_read_b128 v[92:95], v7 offset:12288\n"
    "s_waitcnt lgkmcnt(4)\n"
    "v_mfma_f32_16x16x32_bf16 v[64:67], v[96:99], v[52:55], v[64:67]\n"
    "ds_read_b128 v[96:99], v7 offset:12544\n"
    "s_waitcnt lgkmcnt(4)\n"
    "v_mfma_f32_16x16x32_bf16 v[68:71], v[80:83], v[36:39], 0\n"
    "ds_read_b128 v[80:83], v7 offset:12800\n"
    "s_waitcnt lgkmcnt(4)\n"
    "v_mfma_f32_16x16x32_bf16 v[68:71], v[84:87], v[40:43], v[68:71]\n"
    "ds_read_b128 v[84:87], v7 offset:13056\n"
    "s_waitcnt lgkmcnt(4)\n"
    "v_mfma_f32_16x16x32_bf16 v[68:71], v[88:91], v[44:47], v[68:71]\n"
    "ds_read_b128 v[88:91], v7 offset:13312\n"
    "s_waitcnt lgkmcnt(4)\n"
    "v_mfma_f32_16x16x32_bf16 v[68:71], v[92:95], v[48:51], v[68:71]\n"
    "ds_read_b128 v[92:95], v7 offset:13568\n"
    "s_waitcnt lgkmcnt(4)\n"
    "v_mfma_f32_16x16x32_bf16 v[68:71], v[96:99], v[52:55], v[68:71]\n"
    "ds_read_b128 v[96:99], v7 offset:13824\n"
    "s_waitcnt lgkmcnt(4)\n"
    "v_mfma_f32_16x16x32_bf16 v[72:75], v[80:83], v[36:39], 0\n"
    "ds_read_b128 v[80:83], v7 offset:14080\n"
    "s_waitcnt lgkmcnt(4)\n"
    "v_mfma_f32_16x16x32_bf16 v[72:75], v[84:87], v[40:43], v[72:75]\n"
    "ds_read_b128 v[84:87], v7 offset:14336\n"
    "s_waitcnt lgkmcnt(4)\n"
    "v_mfma_f32_16x16x32_bf16 v[72:75], v[88:91], v[44:47], v[72:75]\n"
    "ds_read_b128 v[88:91], v7 offset:14592\n"
    "s_waitcnt lgkmcnt(4)\n"
    "v_mfma_f32_16x16x32_bf16 v[72:75], v[92:95], v[48:51], v[72:75]\n"
    "ds_read_b128 v[92:95], v7 offset:14848\n"
    "s_waitcnt lgkmcnt(4)\n"
    "v_mfma_f32_16x16x32_bf16 v[72:75], v[96:99], v[52:55], v[72:75]\n"
    "ds_read_b128 v[96:99], v7 offset:15104\n"
    "s_waitcnt lgkmcnt(4)\n"
    "v_mfma_f32_16x16x32_bf16 v[76:79], v[80:83], v[36:39], 0\n"
    "s_waitcnt lgkmcnt(3)\n"
    "v_mfma_f32_16x16x32_bf16 v[76:79], v[84:87], v[40:43], v[76:79]\n"
    "s_waitcnt lgkmcnt(2)\n"
    "v_mfma_f32_16x16x32_bf16 v[76:79], v[88:91], v[44:47], v[76:79]\n"
    "s_waitcnt lgkmcnt(1)\n"
    "v_mfma_f32_16x16x32_bf16 v[76:79], v[92:95], v[48:51], v[76:79]\n"
    "s_waitcnt lgkmcnt(0)\n"
    "v_mfma_f32_16x16x32_bf16 v[76:79], v[96:99], v[52:55], v[76:79]\n"
    "s_nop 7\n"
    "v_add_f32 v76, 0x358637bd, v76\n"
    "v_rcp_f32 v76, v76\n"
    "s_nop 0\n"
    "v_pk_mul_f32 v[56:57], v[56:57], v[76:77] op_sel_hi:[1,0]\n"
    "v_pk_mul_f32 v[58:59], v[58:59], v[76:77] op_sel_hi:[1,0]\n"
    "v_pk_mul_f32 v[60:61], v[60:61], v[76:77] op_sel_hi:[1,0]\n"
    "v_pk_mul_f32 v[62:63], v[62:63], v[76:77] op_sel_hi:[1,0]\n"
    "v_pk_mul_f32 v[64:65], v[64:65], v[76:77] op_sel_hi:[1,0]\n"
    "v_pk_mul_f32 v[66:67], v[66:67], v[76:77] op_sel_hi:[1,0]\n"
    "v_pk_mul_f32 v[68:69], v[68:69], v[76:77] op_sel_hi:[1,0]\n"
    "v_pk_mul_f32 v[70:71], v[70:71], v[76:77] op_sel_hi:[1,0]\n"
    "v_pk_mul_f32 v[72:73], v[72:73], v[76:77] op_sel_hi:[1,0]\n"
    "v_pk_mul_f32 v[74:75], v[74:75], v[76:77] op_sel_hi:[1,0]\n"
    "v_cvt_pk_bf16_f32 v110, v56, v57\n"
    "v_cvt_pk_bf16_f32 v111, v58, v59\n"
    "v_cvt_pk_bf16_f32 v112, v60, v61\n"
    "v_cvt_pk_bf16_f32 v113, v62, v63\n"
    "v_cvt_pk_bf16_f32 v114, v64, v65\n"
    "v_cvt_pk_bf16_f32 v115, v66, v67\n"
    "v_cvt_pk_bf16_f32 v116, v68, v69\n"
    "v_cvt_pk_bf16_f32 v117, v70, v71\n"
    "v_cvt_pk_bf16_f32 v118, v72, v73\n"
    "v_cvt_pk_bf16_f32 v119, v74, v75\n"
    "ds_write_b128 v9, v[100:103] offset:20480\n"
    "ds_write_b128 v9, v[104:107] offset:21504\n"
    "ds_write_b128 v9, v[108:111] offset:22528\n"
    "ds_write_b128 v9, v[112:115] offset:23552\n"
    "ds_write_b128 v9, v[116:119] offset:24576\n"
    "v_add_u32 v9, 76800, v0\n"
    "s_waitcnt lgkmcnt(0)\n"
    "s_barrier\n"
    "ds_read_b128 v[96:99], v9 offset:0\n"
    "ds_read_b128 v[100:103], v9 offset:20480\n"
    "ds_read_b128 v[104:107], v9 offset:1024\n"
    "ds_read_b128 v[108:111], v9 offset:21504\n"
    "ds_read_b128 v[112:115], v9 offset:2048\n"
    "ds_read_b128 v[116:119], v9 offset:22528\n"
    "s_waitcnt lgkmcnt(5)\n"
    "v_mfma_f32_16x16x32_bf16 v[16:19], v[128:131], v[96:99], 0\n"
    "s_waitcnt lgkmcnt(4)\n"
    "v_mfma_f32_16x16x32_bf16 v[56:59], v[128:131], v[100:103], 0\n"
    "s_add_u32 s50, s50, 4096\n"
    "buffer_load_dwordx4 v[128:131], v0, s[40:43], s50 offen\n"
    "ds_read_b128 v[120:123], v9 offset:3072\n"
    "v_mfma_f32_16x16x32_bf16 v[20:23], v[132:135], v[96:99], 0\n"
    "v_mfma_f32_16x16x32_bf16 v[60:63], v[132:135], v[100:103], 0\n"
    "buffer_load_dwordx4 v[132:135], v0, s[40:43], s50 offen offset:1024\n"
    "v_mfma_f32_16x16x32_bf16 v[24:27], v[136:139], v[96:99], 0\n"
    "v_mfma_f32_16x16x32_bf16 v[64:67], v[136:139], v[100:103], 0\n"
    "buffer_load_dwordx4 v[136:139], v0, s[40:43], s50 offen offset:2048\n"
    "v_mfma_f32_16x16x32_bf16 v[28:31], v[140:143], v[96:99], 0\n"
    "v_mfma_f32_16x16x32_bf16 v[68:71], v[140:143], v[100:103], 0\n"
    "buffer_load_dwordx4 v[140:143], v0, s[40:43], s50 offen offset:3072\n"
    "v_mfma_f32_16x16x32_bf16 v[32:35], v[144:147], v[96:99], 0\n"
    "v_mfma_f32_16x16x32_bf16 v[72:75], v[144:147], v[100:103], 0\n"
    "s_add_u32 s50, s50, 4096\n"
    "buffer_load_dwordx4 v[144:147], v0, s[40:43], s50 offen\n"
    "v_mfma_f32_16x16x32_bf16 v[36:39], v[148:151], v[96:99], 0\n"
    "v_mfma_f32_16x16x32_bf16 v[76:79], v[148:151], v[100:103], 0\n"
    "buffer_load_dwordx4 v[148:151], v0, s[40:43], s50 offen offset:1024\n"
    "ds_read_b128 v[124:127], v9 offset:23552\n"
    "v_mfma_f32_16x16x32_bf16 v[40:43], v[152:155], v[96:99], 0\n"
    "v_mfma_f32_16x16x32_bf16 v[80:83], v[152:155], v[100:103], 0\n"
    "buffer_load_dwordx4 v[152:155], v0, s[40:43], s50 offen offset:2048\n"
    "v_mfma_f32_16x16x32_bf16 v[44:47], v[156:159], v[96:99], 0\n"
    "v_mfma_f32_16x16x32_bf16 v[84:87], v[156:159], v[100:103], 0\n"
    "buffer_load_dwordx4 v[156:159], v0, s[40:43], s50 offen offset:3072\n"
    "v_mfma_f32_16x16x32_bf16 v[48:51], v[160:163], v[96:99], 0\n"
    "v_mfma_f32_16x16x32_bf16 v[88:91], v[160:163], v[100:103], 0\n"
    "s_add_u32 s50, s50, 4096\n"
    "buffer_load_dwordx4 v[160:163], v0, s[40:43], s50 offen\n"
    "v_mfma_f32_16x16x32_bf16 v[52:55], v[164:167], v[96:99], 0\n"
    "v_mfma_f32_16x16x32_bf16 v[92:95], v[164:167], v[100:103], 0\n"
    "buffer_load_dwordx4 v[164:167], v0, s[40:43], s50 offen offset:1024\n"
    "s_waitcnt lgkmcnt(5)\n"
    "v_mfma_f32_16x16x32_bf16 v[16:19], v[168:171], v[104:107], v[16:19]\n"
    "s_waitcnt lgkmcnt(4)\n"
    "v_mfma_f32_16x16x32_bf16 v[56:59], v[168:171], v[108:111], v[56:59]\n"
    "buffer_load_dwordx4 v[168:171], v0, s[40:43], s50 offen offset:2048\n"
    "ds_read_b128 v[96:99], v9 offset:4096\n"
    "v_mfma_f32_16x16x32_bf16 v[20:23], v[172:175], v[104:107], v[20:23]\n"
    "v_mfma_f32_16x16x32_bf16 v[60:63], v[172:175], v[108:111], v[60:63]\n"
    "buffer_load_dwordx4 v[172:175], v0, s[40:43], s50 offen offset:3072\n"
    "v_mfma_f32_16x16x32_bf16 v[24:27], v[176:179], v[104:107], v[24:27]\n"
    "v_mfma_f32_16x16x32_bf16 v[64:67], v[176:179], v[108:111], v[64:67]\n"
    "s_add_u32 s50, s50, 4096\n"
    "buffer_load_dwordx4 v[176:179], v0, s[40:43], s50 offen\n"
    "v_mfma_f32_16x16x32_bf16 v[28:31], v[180:183], v[104:107], v[28:31]\n"
    "v_mfma_f32_16x16x32_bf16 v[68:71], v[180:183], v[108:111], v[68:71]\n"
    "buffer_load_dwordx4 v[180:183], v0, s[40:43], s50 offen offset:1024\n"
    "v_mfma_f32_16x16x32_bf16 v[32:35], v[184:187], v[104:107], v[32:35]\n"
    "v_mfma_f32_16x16x32_bf16 v[72:75], v[184:187], v[108:111], v[72:75]\n"
    "buffer_load_dwordx4 v[184:187], v0, s[40:43], s50 offen offset:2048\n"
    "v_mfma_f32_16x16x32_bf16 v[36:39], v[188:191], v[104:107], v[36:39]\n"
    "v_mfma_f32_16x16x32_bf16 v[76:79], v[188:191], v[108:111], v[76:79]\n"
    "buffer_load_dwordx4 v[188:191], v0, s[40:43], s50 offen offset:3072\n"
    "ds_read_b128 v[100:103], v9 offset:24576\n"
    "v_mfma_f32_16x16x32_bf16 v[40:43], v[192:195], v[104:107], v[40:43]\n"
    "v_mfma_f32_16x16x32_bf16 v[80:83], v[192:195], v[108:111], v[80:83]\n"
    "s_add_u32 s50, s50, 4096\n"
    "buffer_load_dwordx4 v[192:195], v0, s[40:43], s50 offen\n"
    "v_mfma_f32_16x16x32_bf16 v[44:47], v[196:199], v[104:107], v[44:47]\n"
    "v_mfma_f32_16x16x32_bf16 v[84:87], v[196:199], v[108:111], v[84:87]\n"
    "buffer_load_dwordx4 v[196:199], v0, s[40:43], s50 offen offset:1024\n"
    "v_mfma_f32_16x16x32_bf16 v[48:51], v[200:203], v[104:107], v[48:51]\n"
    "v_mfma_f32_16x16x32_bf16 v[88:91], v[200:203], v[108:111], v[88:91]\n"
    "buffer_load_dwordx4 v[200:203], v0, s[40:43], s50 offen offset:2048\n"
    "v_mfma_f32_16x16x32_bf16 v[52:55], v[204:207], v[104:107], v[52:55]\n"
    "v_mfma_f32_16x16x32_bf16 v[92:95], v[204:207], v[108:111], v[92:95]\n"
    "buffer_load_dwordx4 v[204:207], v0, s[40:43], s50 offen offset:3072\n"
    "s_waitcnt lgkmcnt(5)\n"
    "v_mfma_f32_16x16x32_bf16 v[16:19], v[208:211], v[112:115], v[16:19]\n"
    "s_waitcnt lgkmcnt(4)\n"
    "v_mfma_f32_16x16x32_bf16 v[56:59], v[208:211], v[116:119], v[56:59]\n"
    "s_add_u32 s50, s50, 4096\n"
    "buffer_load_dwordx4 v[208:211], v0, s[40:43], s50 offen\n"
    "ds_read_b128 v[104:107], v9 offset:5120\n"
    "v_mfma_f32_16x16x32_bf16 v[20:23], v[212:215], v[112:115], v[20:23]\n"
    "v_mfma_f32_16x16x32_bf16 v[60:63], v[212:215], v[116:119], v[60:63]\n"
    "buffer_load_dwordx4 v[212:215], v0, s[40:43], s50 offen offset:1024\n"
    "v_mfma_f32_16x16x32_bf16 v[24:27], v[216:219], v[112:115], v[24:27]\n"
    "v_mfma_f32_16x16x32_bf16 v[64:67], v[216:219], v[116:119], v[64:67]\n"
    "buffer_load_dwordx4 v[216:219], v0, s[40:43], s50 offen offset:2048\n"
    "v_mfma_f32_16x16x32_bf16 v[28:31], v[220:223], v[112:115], v[28:31]\n"
    "v_mfma_f32_16x16x32_bf16 v[68:71], v[220:223], v[116:119], v[68:71]\n"
    "buffer_load_dwordx4 v[220:223], v0, s[40:43], s50 offen offset:3072\n"
    "v_mfma_f32_16x16x32_bf16 v[32:35], v[224:227], v[112:115], v[32:35]\n"
    "v_mfma_f32_16x16x32_bf16 v[72:75], v[224:227], v[116:119], v[72:75]\n"
    "s_add_u32 s50, s50, 4096\n"
    "buffer_load_dwordx4 v[224:227], v0, s[40:43], s50 offen\n"
    "v_mfma_f32_16x16x32_bf16 v[36:39], v[228:231], v[112:115], v[36:39]\n"
    "v_mfma_f32_16x16x32_bf16 v[76:79], v[228:231], v[116:119], v[76:79]\n"
    "buffer_load_dwordx4 v[228:231], v0, s[40:43], s50 offen offset:1024\n"
    "ds_read_b128 v[108:111], v9 offset:25600\n"
    "v_mfma_f32_16x16x32_bf16 v[40:43], v[232:235], v[112:115], v[40:43]\n"
    "v_mfma_f32_16x16x32_bf16 v[80:83], v[232:235], v[116:119], v[80:83]\n"
    "buffer_load_dwordx4 v[232:235], v0, s[40:43], s50 offen offset:2048\n"
    "v_mfma_f32_16x16x32_bf16 v[44:47], v[236:239], v[112:115], v[44:47]\n"
    "v_mfma_f32_16x16x32_bf16 v[84:87], v[236:239], v[116:119], v[84:87]\n"
    "buffer_load_dwordx4 v[236:239], v0, s[40:43], s50 offen offset:3072\n"
    "v_mfma_f32_16x16x32_bf16 v[48:51], v[240:243], v[112:115], v[48:51]\n"
    "v_mfma_f32_16x16x32_bf16 v[88:91], v[240:243], v[116:119], v[88:91]\n"
    "s_add_u32 s50, s50, 4096\n"
    "buffer_load_dwordx4 v[240:243], v0, s[40:43], s50 offen\n"
    "v_mfma_f32_16x16x32_bf16 v[52:55], v[244:247], v[112:115], v[52:55]\n"
    "v_mfma_f32_16x16x32_bf16 v[92:95], v[244:247], v[116:119], v[92:95]\n"
    "buffer_load_dwordx4 v[244:247], v0, s[40:43], s50 offen offset:1024\n"
    "s_waitcnt lgkmcnt(5)\n"
    "v_mfma_f32_16x16x32_bf16 v[16:19], v[248:251], v[120:123], v[16:19]\n"
    "s_waitcnt lgkmcnt(4)\n"
    "v_mfma_f32_16x16x32_bf16 v[56:59], v[248:251], v[124:127], v[56:59]\n"
    "buffer_load_dwordx4 v[248:251], v0, s[40:43], s50 offen offset:2048\n"
    "ds_read_b128 v[112:115], v9 offset:6144\n"
    "v_mfma_f32_16x16x32_bf16 v[20:23], v[252:255], v[120:123], v[20:23]\n"
    "v_mfma_f32_16x16x32_bf16 v[60:63], v[252:255], v[124:127], v[60:63]\n"
    "buffer_load_dwordx4 v[252:255], v0, s[40:43], s50 offen offset:3072\n"
    "s_waitcnt vmcnt(31)\n"
    "v_mfma_f32_16x16x32_bf16 v[24:27], v[128:131], v[120:123], v[24:27]\n"
    "v_mfma_f32_16x16x32_bf16 v[64:67], v[128:131], v[124:127], v[64:67]\n"
    "s_add_u32 s50, s50, 4096\n"
    "buffer_load_dwordx4 v[128:131], v0, s[40:43], s50 offen\n"
    "s_waitcnt vmcnt(31)\n"
    "v_mfma_f32_16x16x32_bf16 v[28:31], v[132:135], v[120:123], v[28:31]\n"
    "v_mfma_f32_16x16x32_bf16 v[68:71], v[132:135], v[124:127], v[68:71]\n"
    "buffer_load_dwordx4 v[132:135], v0, s[40:43], s50 offen offset:1024\n"
    "s_waitcnt vmcnt(31)\n"
    "v_mfma_f32_16x16x32_bf16 v[32:35], v[136:139], v[120:123], v[32:35]\n"
    "v_mfma_f32_16x16x32_bf16 v[72:75], v[136:139], v[124:127], v[72:75]\n"
    "buffer_load_dwordx4 v[136:139], v0, s[40:43], s50 offen offset:2048\n"
    "s_waitcnt vmcnt(31)\n"
    "v_mfma_f32_16x16x32_bf16 v[36:39], v[140:143], v[120:123], v[36:39]\n"
    "v_mfma_f32_16x16x32_bf16 v[76:79], v[140:143], v[124:127], v[76:79]\n"
    "buffer_load_dwordx4 v[140:143], v0, s[40:43], s50 offen offset:3072\n"
    "ds_read_b128 v[116:119], v9 offset:26624\n"
    "s_waitcnt vmcnt(31)\n"
    "v_mfma_f32_16x16x32_bf16 v[40:43], v[144:147], v[120:123], v[40:43]\n"
    "v_mfma_f32_16x16x32_bf16 v[80:83], v[144:147], v[124:127], v[80:83]\n"
    "s_add_u32 s50, s50, 4096\n"
    "buffer_load_dwordx4 v[144:147], v0, s[40:43], s50 offen\n"
    "s_waitcnt vmcnt(31)\n"
    "v_mfma_f32_16x16x32_bf16 v[44:47], v[148:151], v[120:123], v[44:47]\n"
    "v_mfma_f32_16x16x32_bf16 v[84:87], v[148:151], v[124:127], v[84:87]\n"
    "buffer_load_dwordx4 v[148:151], v0, s[40:43], s50 offen offset:1024\n"
    "s_waitcnt vmcnt(31)\n"
    "v_mfma_f32_16x16x32_bf16 v[48:51], v[152:155], v[120:123], v[48:51]\n"
    "v_mfma_f32_16x16x32_bf16 v[88:91], v[152:155], v[124:127], v[88:91]\n"
    "buffer_load_dwordx4 v[152:155], v0, s[40:43], s50 offen offset:2048\n"
    "s_waitcnt vmcnt(31)\n"
    "v_mfma_f32_16x16x32_bf16 v[52:55], v[156:159], v[120:123], v[52:55]\n"
    "v_mfma_f32_16x16x32_bf16 v[92:95], v[156:159], v[124:127], v[92:95]\n"
    "buffer_load_dwordx4 v[156:159], v0, s[40:43], s50 offen offset:3072\n"
    "s_waitcnt vmcnt(31) lgkmcnt(5)\n"
    "v_mfma_f32_16x16x32_bf16 v[16:19], v[160:163], v[96:99], v[16:19]\n"
    "s_waitcnt lgkmcnt(4)\n"
    "v_mfma_f32_16x16x32_bf16 v[56:59], v[160:163], v[100:103], v[56:59]\n"
    "s_add_u32 s50, s50, 4096\n"
    "buffer_load_dwordx4 v[160:163], v0, s[40:43], s50 offen\n"
    "ds_read_b128 v[120:123], v9 offset:7168\n"
    "s_waitcnt vmcnt(31)\n"
    "v_mfma_f32_16x16x32_bf16 v[20:23], v[164:167], v[96:99], v[20:23]\n"
    "v_mfma_f32_16x16x32_bf16 v[60:63], v[164:167], v[100:103], v[60:63]\n"
    "buffer_load_dwordx4 v[164:167], v0, s[40:43], s50 offen offset:1024\n"
    "s_waitcnt vmcnt(31)\n"
    "v_mfma_f32_16x16x32_bf16 v[24:27], v[168:171], v[96:99], v[24:27]\n"
    "v_mfma_f32_16x16x32_bf16 v[64:67], v[168:171], v[100:103], v[64:67]\n"
    "buffer_load_dwordx4 v[168:171], v0, s[40:43], s50 offen offset:2048\n"
    "s_waitcnt vmcnt(31)\n"
    "v_mfma_f32_16x16x32_bf16 v[28:31], v[172:175], v[96:99], v[28:31]\n"
    "v_mfma_f32_16x16x32_bf16 v[68:71], v[172:175], v[100:103], v[68:71]\n"
    "buffer_load_dwordx4 v[172:175], v0, s[40:43], s50 offen offset:3072\n"
    "s_waitcnt vmcnt(31)\n"
    "v_mfma_f32_16x16x32_bf16 v[32:35], v[176:179], v[96:99], v[32:35]\n"
    "v_mfma_f32_16x16x32_bf16 v[72:75], v[176:179], v[100:103], v[72:75]\n"
    "s_add_u32 s50, s50, 4096\n"
    "buffer_load_dwordx4 v[176:179], v0, s[40:43], s50 offen\n"
    "s_waitcnt vmcnt(31)\n"
    "v_mfma_f32_16x16x32_bf16 v[36:39], v[180:183], v[96:99], v[36:39]\n"
    "v_mfma_f32_16x16x32_bf16 v[76:79], v[180:183], v[100:103], v[76:79]\n"
    "buffer_load_dwordx4 v[180:183], v0, s[40:43], s50 offen offset:1024\n"
    "ds_read_b128 v[124:127], v9 offset:27648\n"
    "s_waitcnt vmcnt(31)\n"
    "v_mfma_f32_16x16x32_bf16 v[40:43], v[184:187], v[96:99], v[40:43]\n"
    "v_mfma_f32_16x16x32_bf16 v[80:83], v[184:187], v[100:103], v[80:83]\n"
    "buffer_load_dwordx4 v[184:187], v0, s[40:43], s50 offen offset:2048\n"
    "s_waitcnt vmcnt(31)\n"
    "v_mfma_f32_16x16x32_bf16 v[44:47], v[188:191], v[96:99], v[44:47]\n"
    "v_mfma_f32_16x16x32_bf16 v[84:87], v[188:191], v[100:103], v[84:87]\n"
    "buffer_load_dwordx4 v[188:191], v0, s[40:43], s50 offen offset:3072\n"
    "s_waitcnt vmcnt(31)\n"
    "v_mfma_f32_16x16x32_bf16 v[48:51], v[192:195], v[96:99], v[48:51]\n"
    "v_mfma_f32_16x16x32_bf16 v[88:91], v[192:195], v[100:103], v[88:91]\n"
    "s_add_u32 s50, s50, 4096\n"
    "buffer_load_dwordx4 v[192:195], v0, s[40:43], s50 offen\n"
    "s_waitcnt vmcnt(31)\n"
    "v_mfma_f32_16x16x32_bf16 v[52:55], v[196:199], v[96:99], v[52:55]\n"
    "v_mfma_f32_16x16x32_bf16 v[92:95], v[196:199], v[100:103], v[92:95]\n"
    "buffer_load_dwordx4 v[196:199], v0, s[40:43], s50 offen offset:1024\n"
    "s_waitcnt vmcnt(31) lgkmcnt(5)\n"
    "v_mfma_f32_16x16x32_bf16 v[16:19], v[200:203], v[104:107], v[16:19]\n"
    "s_waitcnt lgkmcnt(4)\n"
    "v_mfma_f32_16x16x32_bf16 v[56:59], v[200:203], v[108:111], v[56:59]\n"
    "buffer_load_dwordx4 v[200:203], v0, s[40:43], s50 offen offset:2048\n"
    "ds_read_b128 v[96:99], v9 offset:8192\n"
    "s_waitcnt vmcnt(31)\n"
    "v_mfma_f32_16x16x32_bf16 v[20:23], v[204:207], v[104:107], v[20:23]\n"
    "v_mfma_f32_16x16x32_bf16 v[60:63], v[204:207], v[108:111], v[60:63]\n"
    "buffer_load_dwordx4 v[204:207], v0, s[40:43], s50 offen offset:3072\n"
    "s_waitcnt vmcnt(31)\n"
    "v_mfma_f32_16x16x32_bf16 v[24:27], v[208:211], v[104:107], v[24:27]\n"
    "v_mfma_f32_16x16x32_bf16 v[64:67], v[208:211], v[108:111], v[64:67]\n"
    "s_add_u32 s50, s50, 4096\n"
    "buffer_load_dwordx4 v[208:211], v0, s[40:43], s50 offen\n"
    "s_waitcnt vmcnt(31)\n"
    "v_mfma_f32_16x16x32_bf16 v[28:31], v[212:215], v[104:107], v[28:31]\n"
    "v_mfma_f32_16x16x32_bf16 v[68:71], v[212:215], v[108:111], v[68:71]\n"
    "buffer_load_dwordx4 v[212:215], v0, s[40:43], s50 offen offset:1024\n"
    "s_waitcnt vmcnt(31)\n"
    "v_mfma_f32_16x16x32_bf16 v[32:35], v[216:219], v[104:107], v[32:35]\n"
    "v_mfma_f32_16x16x32_bf16 v[72:75], v[216:219], v[108:111], v[72:75]\n"
    "buffer_load_dwordx4 v[216:219], v0, s[40:43], s50 offen offset:2048\n"
    "s_waitcnt vmcnt(31)\n"
    "v_mfma_f32_16x16x32_bf16 v[36:39], v[220:223], v[104:107], v[36:39]\n"
    "v_mfma_f32_16x16x32_bf16 v[76:79], v[220:223], v[108:111], v[76:79]\n"
    "buffer_load_dwordx4 v[220:223], v0, s[40:43], s50 offen offset:3072\n"
    "ds_read_b128 v[100:103], v9 offset:28672\n"
    "s_waitcnt vmcnt(31)\n"
    "v_mfma_f32_16x16x32_bf16 v[40:43], v[224:227], v[104:107], v[40:43]\n"
    "v_mfma_f32_16x16x32_bf16 v[80:83], v[224:227], v[108:111], v[80:83]\n"
    "s_add_u32 s50, s50, 4096\n"
    "buffer_load_dwordx4 v[224:227], v0, s[40:43], s50 offen\n"
    "s_waitcnt vmcnt(31)\n"
    "v_mfma_f32_16x16x32_bf16 v[44:47], v[228:231], v[104:107], v[44:47]\n"
    "v_mfma_f32_16x16x32_bf16 v[84:87], v[228:231], v[108:111], v[84:87]\n"
    "buffer_load_dwordx4 v[228:231], v0, s[40:43], s50 offen offset:1024\n"
    "s_waitcnt vmcnt(31)\n"
    "v_mfma_f32_16x16x32_bf16 v[48:51], v[232:235], v[104:107], v[48:51]\n"
    "v_mfma_f32_16x16x32_bf16 v[88:91], v[232:235], v[108:111], v[88:91]\n"
    "buffer_load_dwordx4 v[232:235], v0, s[40:43], s50 offen offset:2048\n"
    "s_waitcnt vmcnt(31)\n"
    "v_mfma_f32_16x16x32_bf16 v[52:55], v[236:239], v[104:107], v[52:55]\n"
    "v_mfma_f32_16x16x32_bf16 v[92:95], v[236:239], v[108:111], v[92:95]\n"
    "buffer_load_dwordx4 v[236:239], v0, s[40:43], s50 offen offset:3072\n"
    "s_waitcnt vmcnt(31) lgkmcnt(5)\n"
    "v_mfma_f32_16x16x32_bf16 v[16:19], v[240:243], v[112:115], v[16:19]\n"
    "s_waitcnt lgkmcnt(4)\n"
    "v_mfma_f32_16x16x32_bf16 v[56:59], v[240:243], v[116:119], v[56:59]\n"
    "s_add_u32 s50, s50, 4096\n"
    "buffer_load_dwordx4 v[240:243], v0, s[40:43], s50 offen\n"
    "ds_read_b128 v[104:107], v9 offset:9216\n"
    "s_waitcnt vmcnt(31)\n"
    "v_mfma_f32_16x16x32_bf16 v[20:23], v[244:247], v[112:115], v[20:23]\n"
    "v_mfma_f32_16x16x32_bf16 v[60:63], v[244:247], v[116:119], v[60:63]\n"
    "buffer_load_dwordx4 v[244:247], v0, s[40:43], s50 offen offset:1024\n"
    "s_waitcnt vmcnt(31)\n"
    "v_mfma_f32_16x16x32_bf16 v[24:27], v[248:251], v[112:115], v[24:27]\n"
    "v_mfma_f32_16x16x32_bf16 v[64:67], v[248:251], v[116:119], v[64:67]\n"
    "buffer_load_dwordx4 v[248:251], v0, s[40:43], s50 offen offset:2048\n"
    "s_waitcnt vmcnt(31)\n"
    "v_mfma_f32_16x16x32_bf16 v[28:31], v[252:255], v[112:115], v[28:31]\n"
    "v_mfma_f32_16x16x32_bf16 v[68:71], v[252:255], v[116:119], v[68:71]\n"
    "buffer_load_dwordx4 v[252:255], v0, s[40:43], s50 offen offset:3072\n"
    "s_waitcnt vmcnt(31)\n"
    "v_mfma_f32_16x16x32_bf16 v[32:35], v[128:131], v[112:115], v[32:35]\n"
    "v_mfma_f32_16x16x32_bf16 v[72:75], v[128:131], v[116:119], v[72:75]\n"
    "s_add_u32 s50, s50, 4096\n"
    "buffer_load_dwordx4 v[128:131], v0, s[40:43], s50 offen\n"
    "s_waitcnt vmcnt(31)\n"
    "v_mfma_f32_16x16x32_bf16 v[36:39], v[132:135], v[112:115], v[36:39]\n"
    "v_mfma_f32_16x16x32_bf16 v[76:79], v[132:135], v[116:119], v[76:79]\n"
    "buffer_load_dwordx4 v[132:135], v0, s[40:43], s50 offen offset:1024\n"
    "ds_read_b128 v[108:111], v9 offset:29696\n"
    "s_waitcnt vmcnt(31)\n"
    "v_mfma_f32_16x16x32_bf16 v[40:43], v[136:139], v[112:115], v[40:43]\n"
    "v_mfma_f32_16x16x32_bf16 v[80:83], v[136:139], v[116:119], v[80:83]\n"
    "buffer_load_dwordx4 v[136:139], v0, s[40:43], s50 offen offset:2048\n"
    "s_waitcnt vmcnt(31)\n"
    "v_mfma_f32_16x16x32_bf16 v[44:47], v[140:143], v[112:115], v[44:47]\n"
    "v_mfma_f32_16x16x32_bf16 v[84:87], v[140:143], v[116:119], v[84:87]\n"
    "buffer_load_dwordx4 v[140:143], v0, s[40:43], s50 offen offset:3072\n"
    "s_waitcnt vmcnt(31)\n"
    "v_mfma_f32_16x16x32_bf16 v[48:51], v[144:147], v[112:115], v[48:51]\n"
    "v_mfma_f32_16x16x32_bf16 v[88:91], v[144:147], v[116:119], v[88:91]\n"
    "s_add_u32 s50, s50, 4096\n"
    "buffer_load_dwordx4 v[144:147], v0, s[40:43], s50 offen\n"
    "s_waitcnt vmcnt(31)\n"
    "v_mfma_f32_16x16x32_bf16 v[52:55], v[148:151], v[112:115], v[52:55]\n"
    "v_mfma_f32_16x16x32_bf16 v[92:95], v[148:151], v[116:119], v[92:95]\n"
    "buffer_load_dwordx4 v[148:151], v0, s[40:43], s50 offen offset:1024\n"
    "s_waitcnt vmcnt(31) lgkmcnt(5)\n"
    "v_mfma_f32_16x16x32_bf16 v[16:19], v[152:155], v[120:123], v[16:19]\n"
    "s_waitcnt lgkmcnt(4)\n"
    "v_mfma_f32_16x16x32_bf16 v[56:59], v[152:155], v[124:127], v[56:59]\n"
    "buffer_load_dwordx4 v[152:155], v0, s[40:43], s50 offen offset:2048\n"
    "ds_read_b128 v[112:115], v9 offset:10240\n"
    "s_waitcnt vmcnt(31)\n"
    "v_mfma_f32_16x16x32_bf16 v[20:23], v[156:159], v[120:123], v[20:23]\n"
    "v_mfma_f32_16x16x32_bf16 v[60:63], v[156:159], v[124:127], v[60:63]\n"
    "buffer_load_dwordx4 v[156:159], v0, s[40:43], s50 offen offset:3072\n"
    "s_waitcnt vmcnt(31)\n"
    "v_mfma_f32_16x16x32_bf16 v[24:27], v[160:163], v[120:123], v[24:27]\n"
    "v_mfma_f32_16x16x32_bf16 v[64:67], v[160:163], v[124:127], v[64:67]\n"
    "s_add_u32 s50, s50, 4096\n"
    "buffer_load_dwordx4 v[160:163], v0, s[40:43], s50 offen\n"
    "s_waitcnt vmcnt(31)\n"
    "v_mfma_f32_16x16x32_bf16 v[28:31], v[164:167], v[120:123], v[28:31]\n"
    "v_mfma_f32_16x16x32_bf16 v[68:71], v[164:167], v[124:127], v[68:71]\n"
    "buffer_load_dwordx4 v[164:167], v0, s[40:43], s50 offen offset:1024\n"
    "s_waitcnt vmcnt(31)\n"
    "v_mfma_f32_16x16x32_bf16 v[32:35], v[168:171], v[120:123], v[32:35]\n"
    "v_mfma_f32_16x16x32_bf16 v[72:75], v[168:171], v[124:127], v[72:75]\n"
    "buffer_load_dwordx4 v[168:171], v0, s[40:43], s50 offen offset:2048\n"
    "s_waitcnt vmcnt(31)\n"
    "v_mfma_f32_16x16x32_bf16 v[36:39], v[172:175], v[120:123], v[36:39]\n"
    "v_mfma_f32_16x16x32_bf16 v[76:79], v[172:175], v[124:127], v[76:79]\n"
    "buffer_load_dwordx4 v[172:175], v0, s[40:43], s50 offen offset:3072\n"
    "ds_read_b128 v[116:119], v9 offset:30720\n"
    "s_waitcnt vmcnt(31)\n"
    "v_mfma_f32_16x16x32_bf16 v[40:43], v[176:179], v[120:123], v[40:43]\n"
    "v_mfma_f32_16x16x32_bf16 v[80:83], v[176:179], v[124:127], v[80:83]\n"
    "s_add_u32 s50, s50, 4096\n"
    "buffer_load_dwordx4 v[176:179], v0, s[40:43], s50 offen\n"
    "s_waitcnt vmcnt(31)\n"
    "v_mfma_f32_16x16x32_bf16 v[44:47], v[180:183], v[120:123], v[44:47]\n"
    "v_mfma_f32_16x16x32_bf16 v[84:87], v[180:183], v[124:127], v[84:87]\n"
    "buffer_load_dwordx4 v[180:183], v0, s[40:43], s50 offen offset:1024\n"
    "s_waitcnt vmcnt(31)\n"
    "v_mfma_f32_16x16x32_bf16 v[48:51], v[184:187], v[120:123], v[48:51]\n"
    "v_mfma_f32_16x16x32_bf16 v[88:91], v[184:187], v[124:127], v[88:91]\n"
    "buffer_load_dwordx4 v[184:187], v0, s[40:43], s50 offen offset:2048\n"
    "s_waitcnt vmcnt(31)\n"
    "v_mfma_f32_16x16x32_bf16 v[52:55], v[188:191], v[120:123], v[52:55]\n"
    "v_mfma_f32_16x16x32_bf16 v[92:95], v[188:191], v[124:127], v[92:95]\n"
    "buffer_load_dwordx4 v[188:191], v0, s[40:43], s50 offen offset:3072\n"
    "s_waitcnt vmcnt(31) lgkmcnt(5)\n"
    "v_mfma_f32_16x16x32_bf16 v[16:19], v[192:195], v[96:99], v[16:19]\n"
    "s_waitcnt lgkmcnt(4)\n"
    "v_mfma_f32_16x16x32_bf16 v[56:59], v[192:195], v[100:103], v[56:59]\n"
    "s_add_u32 s50, s50, 4096\n"
    "buffer_load_dwordx4 v[192:195], v0, s[40:43], s50 offen\n"
    "ds_read_b128 v[120:123], v9 offset:11264\n"
    "s_waitcnt vmcnt(31)\n"
    "v_mfma_f32_16x16x32_bf16 v[20:23], v[196:199], v[96:99], v[20:23]\n"
    "v_mfma_f32_16x16x32_bf16 v[60:63], v[196:199], v[100:103], v[60:63]\n"
    "buffer_load_dwordx4 v[196:199], v0, s[40:43], s50 offen offset:1024\n"
    "s_waitcnt vmcnt(31)\n"
    "v_mfma_f32_16x16x32_bf16 v[24:27], v[200:203], v[96:99], v[24:27]\n"
    "v_mfma_f32_16x16x32_bf16 v[64:67], v[200:203], v[100:103], v[64:67]\n"
    "buffer_load_dwordx4 v[200:203], v0, s[40:43], s50 offen offset:2048\n"
    "s_waitcnt vmcnt(31)\n"
    "v_mfma_f32_16x16x32_bf16 v[28:31], v[204:207], v[96:99], v[28:31]\n"
    "v_mfma_f32_16x16x32_bf16 v[68:71], v[204:207], v[100:103], v[68:71]\n"
    "buffer_load_dwordx4 v[204:207], v0, s[40:43], s50 offen offset:3072\n"
    "s_waitcnt vmcnt(31)\n"
    "v_mfma_f32_16x16x32_bf16 v[32:35], v[208:211], v[96:99], v[32:35]\n"
    "v_mfma_f32_16x16x32_bf16 v[72:75], v[208:211], v[100:103], v[72:75]\n"
    "s_add_u32 s50, s50, 4096\n"
    "buffer_load_dwordx4 v[208:211], v0, s[40:43], s50 offen\n"
    "s_waitcnt vmcnt(31)\n"
    "v_mfma_f32_16x16x32_bf16 v[36:39], v[212:215], v[96:99], v[36:39]\n"
    "v_mfma_f32_16x16x32_bf16 v[76:79], v[212:215], v[100:103], v[76:79]\n"
    "buffer_load_dwordx4 v[212:215], v0, s[40:43], s50 offen offset:1024\n"
    "ds_read_b128 v[124:127], v9 offset:31744\n"
    "s_waitcnt vmcnt(31)\n"
    "v_mfma_f32_16x16x32_bf16 v[40:43], v[216:219], v[96:99], v[40:43]\n"
    "v_mfma_f32_16x16x32_bf16 v[80:83], v[216:219], v[100:103], v[80:83]\n"
    "buffer_load_dwordx4 v[216:219], v0, s[40:43], s50 offen offset:2048\n"
    "s_waitcnt vmcnt(31)\n"
    "v_mfma_f32_16x16x32_bf16 v[44:47], v[220:223], v[96:99], v[44:47]\n"
    "v_mfma_f32_16x16x32_bf16 v[84:87], v[220:223], v[100:103], v[84:87]\n"
    "buffer_load_dwordx4 v[220:223], v0, s[40:43], s50 offen offset:3072\n"
    "s_waitcnt vmcnt(31)\n"
    "v_mfma_f32_16x16x32_bf16 v[48:51], v[224:227], v[96:99], v[48:51]\n"
    "v_mfma_f32_16x16x32_bf16 v[88:91], v[224:227], v[100:103], v[88:91]\n"
    "s_add_u32 s50, s50, 4096\n"
    "buffer_load_dwordx4 v[224:227], v0, s[40:43], s50 offen\n"
    "s_waitcnt vmcnt(31)\n"
    "v_mfma_f32_16x16x32_bf16 v[52:55], v[228:231], v[96:99], v[52:55]\n"
    "v_mfma_f32_16x16x32_bf16 v[92:95], v[228:231], v[100:103], v[92:95]\n"
    "buffer_load_dwordx4 v[228:231], v0, s[40:43], s50 offen offset:1024\n"
    "s_waitcnt vmcnt(31) lgkmcnt(5)\n"
    "v_mfma_f32_16x16x32_bf16 v[16:19], v[232:235], v[104:107], v[16:19]\n"
    "s_waitcnt lgkmcnt(4)\n"
    "v_mfma_f32_16x16x32_bf16 v[56:59], v[232:235], v[108:111], v[56:59]\n"
    "buffer_load_dwordx4 v[232:235], v0, s[40:43], s50 offen offset:2048\n"
    "ds_read_b128 v[96:99], v9 offset:12288\n"
    "s_waitcnt vmcnt(31)\n"
    "v_mfma_f32_16x16x32_bf16 v[20:23], v[236:239], v[104:107], v[20:23]\n"
    "v_mfma_f32_16x16x32_bf16 v[60:63], v[236:239], v[108:111], v[60:63]\n"
    "buffer_load_dwordx4 v[236:239], v0, s[40:43], s50 offen offset:3072\n"
    "s_waitcnt vmcnt(31)\n"
    "v_mfma_f32_16x16x32_bf16 v[24:27], v[240:243], v[104:107], v[24:27]\n"
    "v_mfma_f32_16x16x32_bf16 v[64:67], v[240:243], v[108:111], v[64:67]\n"
    "s_add_u32 s50, s50, 4096\n"
    "buffer_load_dwordx4 v[240:243], v0, s[40:43], s50 offen\n"
    "s_waitcnt vmcnt(31)\n"
    "v_mfma_f32_16x16x32_bf16 v[28:31], v[244:247], v[104:107], v[28:31]\n"
    "v_mfma_f32_16x16x32_bf16 v[68:71], v[244:247], v[108:111], v[68:71]\n"
    "buffer_load_dwordx4 v[244:247], v0, s[40:43], s50 offen offset:1024\n"
    "s_waitcnt vmcnt(31)\n"
    "v_mfma_f32_16x16x32_bf16 v[32:35], v[248:251], v[104:107], v[32:35]\n"
    "v_mfma_f32_16x16x32_bf16 v[72:75], v[248:251], v[108:111], v[72:75]\n"
    "buffer_load_dwordx4 v[248:251], v0, s[40:43], s50 offen offset:2048\n"
    "s_waitcnt vmcnt(31)\n"
    "v_mfma_f32_16x16x32_bf16 v[36:39], v[252:255], v[104:107], v[36:39]\n"
    "v_mfma_f32_16x16x32_bf16 v[76:79], v[252:255], v[108:111], v[76:79]\n"
    "buffer_load_dwordx4 v[252:255], v0, s[40:43], s50 offen offset:3072\n"
    "ds_read_b128 v[100:103], v9 offset:32768\n"
    "s_waitcnt vmcnt(31)\n"
    "v_mfma_f32_16x16x32_bf16 v[40:43], v[128:131], v[104:107], v[40:43]\n"
    "v_mfma_f32_16x16x32_bf16 v[80:83], v[128:131], v[108:111], v[80:83]\n"
    "s_add_u32 s50, s50, 4096\n"
    "buffer_load_dwordx4 v[128:131], v0, s[40:43], s50 offen\n"
    "s_waitcnt vmcnt(31)\n"
    "v_mfma_f32_16x16x32_bf16 v[44:47], v[132:135], v[104:107], v[44:47]\n"
    "v_mfma_f32_16x16x32_bf16 v[84:87], v[132:135], v[108:111], v[84:87]\n"
    "buffer_load_dwordx4 v[132:135], v0, s[40:43], s50 offen offset:1024\n"
    "s_waitcnt vmcnt(31)\n"
    "v_mfma_f32_16x16x32_bf16 v[48:51], v[136:139], v[104:107], v[48:51]\n"
    "v_mfma_f32_16x16x32_bf16 v[88:91], v[136:139], v[108:111], v[88:91]\n"
    "buffer_load_dwordx4 v[136:139], v0, s[40:43], s50 offen offset:2048\n"
    "s_waitcnt vmcnt(31)\n"
    "v_mfma_f32_16x16x32_bf16 v[52:55], v[140:143], v[104:107], v[52:55]\n"
    "v_mfma_f32_16x16x32_bf16 v[92:95], v[140:143], v[108:111], v[92:95]\n"
    "buffer_load_dwordx4 v[140:143], v0, s[40:43], s50 offen offset:3072\n"
    "s_waitcnt vmcnt(31) lgkmcnt(5)\n"
    "v_mfma_f32_16x16x32_bf16 v[16:19], v[144:147], v[112:115], v[16:19]\n"
    "s_waitcnt lgkmcnt(4)\n"
    "v_mfma_f32_16x16x32_bf16 v[56:59], v[144:147], v[116:119], v[56:59]\n"
    "s_add_u32 s50, s50, 4096\n"
    "buffer_load_dwordx4 v[144:147], v0, s[40:43], s50 offen\n"
    "ds_read_b128 v[104:107], v9 offset:13312\n"
    "s_waitcnt vmcnt(31)\n"
    "v_mfma_f32_16x16x32_bf16 v[20:23], v[148:151], v[112:115], v[20:23]\n"
    "v_mfma_f32_16x16x32_bf16 v[60:63], v[148:151], v[116:119], v[60:63]\n"
    "buffer_load_dwordx4 v[148:151], v0, s[40:43], s50 offen offset:1024\n"
    "s_waitcnt vmcnt(31)\n"
    "v_mfma_f32_16x16x32_bf16 v[24:27], v[152:155], v[112:115], v[24:27]\n"
    "v_mfma_f32_16x16x32_bf16 v[64:67], v[152:155], v[116:119], v[64:67]\n"
    "buffer_load_dwordx4 v[152:155], v0, s[40:43], s50 offen offset:2048\n"
    "s_waitcnt vmcnt(31)\n"
    "v_mfma_f32_16x16x32_bf16 v[28:31], v[156:159], v[112:115], v[28:31]\n"
    "v_mfma_f32_16x16x32_bf16 v[68:71], v[156:159], v[116:119], v[68:71]\n"
    "buffer_load_dwordx4 v[156:159], v0, s[40:43], s50 offen offset:3072\n"
    "s_waitcnt vmcnt(31)\n"
    "v_mfma_f32_16x16x32_bf16 v[32:35], v[160:163], v[112:115], v[32:35]\n"
    "v_mfma_f32_16x16x32_bf16 v[72:75], v[160:163], v[116:119], v[72:75]\n"
    "s_add_u32 s50, s50, 4096\n"
    "buffer_load_dwordx4 v[160:163], v0, s[40:43], s50 offen\n"
    "s_waitcnt vmcnt(31)\n"
    "v_mfma_f32_16x16x32_bf16 v[36:39], v[164:167], v[112:115], v[36:39]\n"
    "v_mfma_f32_16x16x32_bf16 v[76:79], v[164:167], v[116:119], v[76:79]\n"
    "buffer_load_dwordx4 v[164:167], v0, s[40:43], s50 offen offset:1024\n"
    "ds_read_b128 v[108:111], v9 offset:33792\n"
    "s_waitcnt vmcnt(31)\n"
    "v_mfma_f32_16x16x32_bf16 v[40:43], v[168:171], v[112:115], v[40:43]\n"
    "v_mfma_f32_16x16x32_bf16 v[80:83], v[168:171], v[116:119], v[80:83]\n"
    "buffer_load_dwordx4 v[168:171], v0, s[40:43], s50 offen offset:2048\n"
    "s_waitcnt vmcnt(31)\n"
    "v_mfma_f32_16x16x32_bf16 v[44:47], v[172:175], v[112:115], v[44:47]\n"
    "v_mfma_f32_16x16x32_bf16 v[84:87], v[172:175], v[116:119], v[84:87]\n"
    "buffer_load_dwordx4 v[172:175], v0, s[40:43], s50 offen offset:3072\n"
    "s_waitcnt vmcnt(31)\n"
    "v_mfma_f32_16x16x32_bf16 v[48:51], v[176:179], v[112:115], v[48:51]\n"
    "v_mfma_f32_16x16x32_bf16 v[88:91], v[176:179], v[116:119], v[88:91]\n"
    "s_add_u32 s50, s50, 4096\n"
    "buffer_load_dwordx4 v[176:179], v0, s[40:43], s50 offen\n"
    "s_waitcnt vmcnt(31)\n"
    "v_mfma_f32_16x16x32_bf16 v[52:55], v[180:183], v[112:115], v[52:55]\n"
    "v_mfma_f32_16x16x32_bf16 v[92:95], v[180:183], v[116:119], v[92:95]\n"
    "buffer_load_dwordx4 v[180:183], v0, s[40:43], s50 offen offset:1024\n"
    "s_waitcnt vmcnt(31) lgkmcnt(5)\n"
    "v_mfma_f32_16x16x32_bf16 v[16:19], v[184:187], v[120:123], v[16:19]\n"
    "s_waitcnt lgkmcnt(4)\n"
    "v_mfma_f32_16x16x32_bf16 v[56:59], v[184:187], v[124:127], v[56:59]\n"
    "buffer_load_dwordx4 v[184:187], v0, s[40:43], s50 offen offset:2048\n"
    "ds_read_b128 v[112:115], v9 offset:14336\n"
    "s_waitcnt vmcnt(31)\n"
    "v_mfma_f32_16x16x32_bf16 v[20:23], v[188:191], v[120:123], v[20:23]\n"
    "v_mfma_f32_16x16x32_bf16 v[60:63], v[188:191], v[124:127], v[60:63]\n"
    "buffer_load_dwordx4 v[188:191], v0, s[40:43], s50 offen offset:3072\n"
    "s_waitcnt vmcnt(31)\n"
    "v_mfma_f32_16x16x32_bf16 v[24:27], v[192:195], v[120:123], v[24:27]\n"
    "v_mfma_f32_16x16x32_bf16 v[64:67], v[192:195], v[124:127], v[64:67]\n"
    "s_add_u32 s50, s50, 4096\n"
    "buffer_load_dwordx4 v[192:195], v0, s[40:43], s50 offen\n"
    "s_waitcnt vmcnt(31)\n"
    "v_mfma_f32_16x16x32_bf16 v[28:31], v[196:199], v[120:123], v[28:31]\n"
    "v_mfma_f32_16x16x32_bf16 v[68:71], v[196:199], v[124:127], v[68:71]\n"
    "buffer_load_dwordx4 v[196:199], v0, s[40:43], s50 offen offset:1024\n"
    "s_waitcnt vmcnt(31)\n"
    "v_mfma_f32_16x16x32_bf16 v[32:35], v[200:203], v[120:123], v[32:35]\n"
    "v_mfma_f32_16x16x32_bf16 v[72:75], v[200:203], v[124:127], v[72:75]\n"
    "buffer_load_dwordx4 v[200:203], v0, s[40:43], s50 offen offset:2048\n"
    "s_waitcnt vmcnt(31)\n"
    "v_mfma_f32_16x16x32_bf16 v[36:39], v[204:207], v[120:123], v[36:39]\n"
    "v_mfma_f32_16x16x32_bf16 v[76:79], v[204:207], v[124:127], v[76:79]\n"
    "buffer_load_dwordx4 v[204:207], v0, s[40:43], s50 offen offset:3072\n"
    "ds_read_b128 v[116:119], v9 offset:34816\n"
    "s_waitcnt vmcnt(31)\n"
    "v_mfma_f32_16x16x32_bf16 v[40:43], v[208:211], v[120:123], v[40:43]\n"
    "v_mfma_f32_16x16x32_bf16 v[80:83], v[208:211], v[124:127], v[80:83]\n"
    "s_add_u32 s50, s50, 4096\n"
    "buffer_load_dwordx4 v[208:211], v0, s[40:43], s50 offen\n"
    "s_waitcnt vmcnt(31)\n"
    "v_mfma_f32_16x16x32_bf16 v[44:47], v[212:215], v[120:123], v[44:47]\n"
    "v_mfma_f32_16x16x32_bf16 v[84:87], v[212:215], v[124:127], v[84:87]\n"
    "buffer_load_dwordx4 v[212:215], v0, s[40:43], s50 offen offset:1024\n"
    "s_waitcnt vmcnt(31)\n"
    "v_mfma_f32_16x16x32_bf16 v[48:51], v[216:219], v[120:123], v[48:51]\n"
    "v_mfma_f32_16x16x32_bf16 v[88:91], v[216:219], v[124:127], v[88:91]\n"
    "buffer_load_dwordx4 v[216:219], v0, s[40:43], s50 offen offset:2048\n"
    "s_waitcnt vmcnt(31)\n"
    "v_mfma_f32_16x16x32_bf16 v[52:55], v[220:223], v[120:123], v[52:55]\n"
    "v_mfma_f32_16x16x32_bf16 v[92:95], v[220:223], v[124:127], v[92:95]\n"
    "buffer_load_dwordx4 v[220:223], v0, s[40:43], s50 offen offset:3072\n"
    "s_waitcnt vmcnt(31) lgkmcnt(5)\n"
    "v_mfma_f32_16x16x32_bf16 v[16:19], v[224:227], v[96:99], v[16:19]\n"
    "s_waitcnt lgkmcnt(4)\n"
    "v_mfma_f32_16x16x32_bf16 v[56:59], v[224:227], v[100:103], v[56:59]\n"
    "s_add_u32 s50, s50, 4096\n"
    "buffer_load_dwordx4 v[224:227], v0, s[40:43], s50 offen\n"
    "ds_read_b128 v[120:123], v9 offset:15360\n"
    "s_waitcnt vmcnt(31)\n"
    "v_mfma_f32_16x16x32_bf16 v[20:23], v[228:231], v[96:99], v[20:23]\n"
    "v_mfma_f32_16x16x32_bf16 v[60:63], v[228:231], v[100:103], v[60:63]\n"
    "buffer_load_dwordx4 v[228:231], v0, s[40:43], s50 offen offset:1024\n"
    "s_waitcnt vmcnt(31)\n"
    "v_mfma_f32_16x16x32_bf16 v[24:27], v[232:235], v[96:99], v[24:27]\n"
    "v_mfma_f32_16x16x32_bf16 v[64:67], v[232:235], v[100:103], v[64:67]\n"
    "buffer_load_dwordx4 v[232:235], v0, s[40:43], s50 offen offset:2048\n"
    "s_waitcnt vmcnt(31)\n"
    "v_mfma_f32_16x16x32_bf16 v[28:31], v[236:239], v[96:99], v[28:31]\n"
    "v_mfma_f32_16x16x32_bf16 v[68:71], v[236:239], v[100:103], v[68:71]\n"
    "buffer_load_dwordx4 v[236:239], v0, s[40:43], s50 offen offset:3072\n"
    "s_waitcnt vmcnt(31)\n"
    "v_mfma_f32_16x16x32_bf16 v[32:35], v[240:243], v[96:99], v[32:35]\n"
    "v_mfma_f32_16x16x32_bf16 v[72:75], v[240:243], v[100:103], v[72:75]\n"
    "s_add_u32 s50, s50, 4096\n"
    "buffer_load_dwordx4 v[240:243], v0, s[40:43], s50 offen\n"
    "s_waitcnt vmcnt(31)\n"
    "v_mfma_f32_16x16x32_bf16 v[36:39], v[244:247], v[96:99], v[36:39]\n"
    "v_mfma_f32_16x16x32_bf16 v[76:79], v[244:247], v[100:103], v[76:79]\n"
    "buffer_load_dwordx4 v[244:247], v0, s[40:43], s50 offen offset:1024\n"
    "ds_read_b128 v[124:127], v9 offset:35840\n"
    "s_waitcnt vmcnt(31)\n"
    "v_mfma_f32_16x16x32_bf16 v[40:43], v[248:251], v[96:99], v[40:43]\n"
    "v_mfma_f32_16x16x32_bf16 v[80:83], v[248:251], v[100:103], v[80:83]\n"
    "buffer_load_dwordx4 v[248:251], v0, s[40:43], s50 offen offset:2048\n"
    "s_waitcnt vmcnt(31)\n"
    "v_mfma_f32_16x16x32_bf16 v[44:47], v[252:255], v[96:99], v[44:47]\n"
    "v_mfma_f32_16x16x32_bf16 v[84:87], v[252:255], v[100:103], v[84:87]\n"
    "buffer_load_dwordx4 v[252:255], v0, s[40:43], s50 offen offset:3072\n"
    "s_waitcnt vmcnt(31)\n"
    "v_mfma_f32_16x16x32_bf16 v[48:51], v[128:131], v[96:99], v[48:51]\n"
    "v_mfma_f32_16x16x32_bf16 v[88:91], v[128:131], v[100:103], v[88:91]\n"
    "s_add_u32 s50, s50, 4096\n"
    "buffer_load_dwordx4 v[128:131], v0, s[40:43], s50 offen\n"
    "s_waitcnt vmcnt(31)\n"
    "v_mfma_f32_16x16x32_bf16 v[52:55], v[132:135], v[96:99], v[52:55]\n"
    "v_mfma_f32_16x16x32_bf16 v[92:95], v[132:135], v[100:103], v[92:95]\n"
    "buffer_load_dwordx4 v[132:135], v0, s[40:43], s50 offen offset:1024\n"
    "s_waitcnt vmcnt(31) lgkmcnt(5)\n"
    "v_mfma_f32_16x16x32_bf16 v[16:19], v[136:139], v[104:107], v[16:19]\n"
    "s_waitcnt lgkmcnt(4)\n"
    "v_mfma_f32_16x16x32_bf16 v[56:59], v[136:139], v[108:111], v[56:59]\n"
    "buffer_load_dwordx4 v[136:139], v0, s[40:43], s50 offen offset:2048\n"
    "ds_read_b128 v[96:99], v9 offset:16384\n"
    "s_waitcnt vmcnt(31)\n"
    "v_mfma_f32_16x16x32_bf16 v[20:23], v[140:143], v[104:107], v[20:23]\n"
    "v_mfma_f32_16x16x32_bf16 v[60:63], v[140:143], v[108:111], v[60:63]\n"
    "buffer_load_dwordx4 v[140:143], v0, s[40:43], s50 offen offset:3072\n"
    "s_waitcnt vmcnt(31)\n"
    "v_mfma_f32_16x16x32_bf16 v[24:27], v[144:147], v[104:107], v[24:27]\n"
    "v_mfma_f32_16x16x32_bf16 v[64:67], v[144:147], v[108:111], v[64:67]\n"
    "s_add_u32 s50, s50, 4096\n"
    "buffer_load_dwordx4 v[144:147], v0, s[40:43], s50 offen\n"
    "s_waitcnt vmcnt(31)\n"
    "v_mfma_f32_16x16x32_bf16 v[28:31], v[148:151], v[104:107], v[28:31]\n"
    "v_mfma_f32_16x16x32_bf16 v[68:71], v[148:151], v[108:111], v[68:71]\n"
    "buffer_load_dwordx4 v[148:151], v0, s[40:43], s50 offen offset:1024\n"
    "s_waitcnt vmcnt(31)\n"
    "v_mfma_f32_16x16x32_bf16 v[32:35], v[152:155], v[104:107], v[32:35]\n"
    "v_mfma_f32_16x16x32_bf16 v[72:75], v[152:155], v[108:111], v[72:75]\n"
    "buffer_load_dwordx4 v[152:155], v0, s[40:43], s50 offen offset:2048\n"
    "s_waitcnt vmcnt(31)\n"
    "v_mfma_f32_16x16x32_bf16 v[36:39], v[156:159], v[104:107], v[36:39]\n"
    "v_mfma_f32_16x16x32_bf16 v[76:79], v[156:159], v[108:111], v[76:79]\n"
    "buffer_load_dwordx4 v[156:159], v0, s[40:43], s50 offen offset:3072\n"
    "ds_read_b128 v[100:103], v9 offset:36864\n"
    "s_waitcnt vmcnt(31)\n"
    "v_mfma_f32_16x16x32_bf16 v[40:43], v[160:163], v[104:107], v[40:43]\n"
    "v_mfma_f32_16x16x32_bf16 v[80:83], v[160:163], v[108:111], v[80:83]\n"
    "s_add_u32 s50, s50, 4096\n"
    "buffer_load_dwordx4 v[160:163], v0, s[40:43], s50 offen\n"
    "s_waitcnt vmcnt(31)\n"
    "v_mfma_f32_16x16x32_bf16 v[44:47], v[164:167], v[104:107], v[44:47]\n"
    "v_mfma_f32_16x16x32_bf16 v[84:87], v[164:167], v[108:111], v[84:87]\n"
    "buffer_load_dwordx4 v[164:167], v0, s[40:43], s50 offen offset:1024\n"
    "s_waitcnt vmcnt(31)\n"
    "v_mfma_f32_16x16x32_bf16 v[48:51], v[168:171], v[104:107], v[48:51]\n"
    "v_mfma_f32_16x16x32_bf16 v[88:91], v[168:171], v[108:111], v[88:91]\n"
    "buffer_load_dwordx4 v[168:171], v0, s[40:43], s50 offen offset:2048\n"
    "s_waitcnt vmcnt(31)\n"
    "v_mfma_f32_16x16x32_bf16 v[52:55], v[172:175], v[104:107], v[52:55]\n"
    "v_mfma_f32_16x16x32_bf16 v[92:95], v[172:175], v[108:111], v[92:95]\n"
    "buffer_load_dwordx4 v[172:175], v0, s[40:43], s50 offen offset:3072\n"
    "s_waitcnt vmcnt(31) lgkmcnt(5)\n"
    "v_mfma_f32_16x16x32_bf16 v[16:19], v[176:179], v[112:115], v[16:19]\n"
    "s_waitcnt lgkmcnt(4)\n"
    "v_mfma_f32_16x16x32_bf16 v[56:59], v[176:179], v[116:119], v[56:59]\n"
    "s_add_u32 s50, s50, 4096\n"
    "buffer_load_dwordx4 v[176:179], v0, s[40:43], s50 offen\n"
    "ds_read_b128 v[104:107], v9 offset:17408\n"
    "s_waitcnt vmcnt(31)\n"
    "v_mfma_f32_16x16x32_bf16 v[20:23], v[180:183], v[112:115], v[20:23]\n"
    "v_mfma_f32_16x16x32_bf16 v[60:63], v[180:183], v[116:119], v[60:63]\n"
    "buffer_load_dwordx4 v[180:183], v0, s[40:43], s50 offen offset:1024\n"
    "s_waitcnt vmcnt(31)\n"
    "v_mfma_f32_16x16x32_bf16 v[24:27], v[184:187], v[112:115], v[24:27]\n"
    "v_mfma_f32_16x16x32_bf16 v[64:67], v[184:187], v[116:119], v[64:67]\n"
    "buffer_load_dwordx4 v[184:187], v0, s[40:43], s50 offen offset:2048\n"
    "s_waitcnt vmcnt(31)\n"
    "v_mfma_f32_16x16x32_bf16 v[28:31], v[188:191], v[112:115], v[28:31]\n"
    "v_mfma_f32_16x16x32_bf16 v[68:71], v[188:191], v[116:119], v[68:71]\n"
    "buffer_load_dwordx4 v[188:191], v0, s[40:43], s50 offen offset:3072\n"
    "s_waitcnt vmcnt(31)\n"
    "v_mfma_f32_16x16x32_bf16 v[32:35], v[192:195], v[112:115], v[32:35]\n"
    "v_mfma_f32_16x16x32_bf16 v[72:75], v[192:195], v[116:119], v[72:75]\n"
    "s_add_u32 s50, s50, 4096\n"
    "buffer_load_dwordx4 v[192:195], v0, s[40:43], s50 offen\n"
    "s_waitcnt vmcnt(31)\n"
    "v_mfma_f32_16x16x32_bf16 v[36:39], v[196:199], v[112:115], v[36:39]\n"
    "v_mfma_f32_16x16x32_bf16 v[76:79], v[196:199], v[116:119], v[76:79]\n"
    "buffer_load_dwordx4 v[196:199], v0, s[40:43], s50 offen offset:1024\n"
    "ds_read_b128 v[108:111], v9 offset:37888\n"
    "s_waitcnt vmcnt(31)\n"
    "v_mfma_f32_16x16x32_bf16 v[40:43], v[200:203], v[112:115], v[40:43]\n"
    "v_mfma_f32_16x16x32_bf16 v[80:83], v[200:203], v[116:119], v[80:83]\n"
    "buffer_load_dwordx4 v[200:203], v0, s[40:43], s50 offen offset:2048\n"
    "s_waitcnt vmcnt(31)\n"
    "v_mfma_f32_16x16x32_bf16 v[44:47], v[204:207], v[112:115], v[44:47]\n"
    "v_mfma_f32_16x16x32_bf16 v[84:87], v[204:207], v[116:119], v[84:87]\n"
    "buffer_load_dwordx4 v[204:207], v0, s[40:43], s50 offen offset:3072\n"
    "s_waitcnt vmcnt(31)\n"
    "v_mfma_f32_16x16x32_bf16 v[48:51], v[208:211], v[112:115], v[48:51]\n"
    "v_mfma_f32_16x16x32_bf16 v[88:91], v[208:211], v[116:119], v[88:91]\n"
    "s_add_u32 s50, s50, 4096\n"
    "buffer_load_dwordx4 v[208:211], v0, s[40:43], s50 offen\n"
    "s_waitcnt vmcnt(31)\n"
    "v_mfma_f32_16x16x32_bf16 v[52:55], v[212:215], v[112:115], v[52:55]\n"
    "v_mfma_f32_16x16x32_bf16 v[92:95], v[212:215], v[116:119], v[92:95]\n"
    "buffer_load_dwordx4 v[212:215], v0, s[40:43], s50 offen offset:1024\n"
    "s_waitcnt vmcnt(31) lgkmcnt(5)\n"
    "v_mfma_f32_16x16x32_bf16 v[16:19], v[216:219], v[120:123], v[16:19]\n"
    "s_waitcnt lgkmcnt(4)\n"
    "v_mfma_f32_16x16x32_bf16 v[56:59], v[216:219], v[124:127], v[56:59]\n"
    "buffer_load_dwordx4 v[216:219], v0, s[40:43], s50 offen offset:2048\n"
    "ds_read_b128 v[112:115], v9 offset:18432\n"
    "s_waitcnt vmcnt(31)\n"
    "v_mfma_f32_16x16x32_bf16 v[20:23], v[220:223], v[120:123], v[20:23]\n"
    "v_mfma_f32_16x16x32_bf16 v[60:63], v[220:223], v[124:127], v[60:63]\n"
    "buffer_load_dwordx4 v[220:223], v0, s[40:43], s50 offen offset:3072\n"
    "s_waitcnt vmcnt(31)\n"
    "v_mfma_f32_16x16x32_bf16 v[24:27], v[224:227], v[120:123], v[24:27]\n"
    "v_mfma_f32_16x16x32_bf16 v[64:67], v[224:227], v[124:127], v[64:67]\n"
    "s_add_u32 s50, s50, 4096\n"
    "buffer_load_dwordx4 v[224:227], v0, s[40:43], s50 offen\n"
    "s_waitcnt vmcnt(31)\n"
    "v_mfma_f32_16x16x32_bf16 v[28:31], v[228:231], v[120:123], v[28:31]\n"
    "v_mfma_f32_16x16x32_bf16 v[68:71], v[228:231], v[124:127], v[68:71]\n"
    "buffer_load_dwordx4 v[228:231], v0, s[40:43], s50 offen offset:1024\n"
    "s_waitcnt vmcnt(31)\n"
    "v_mfma_f32_16x16x32_bf16 v[32:35], v[232:235], v[120:123], v[32:35]\n"
    "v_mfma_f32_16x16x32_bf16 v[72:75], v[232:235], v[124:127], v[72:75]\n"
    "buffer_load_dwordx4 v[232:235], v0, s[40:43], s50 offen offset:2048\n"
    "s_waitcnt vmcnt(31)\n"
    "v_mfma_f32_16x16x32_bf16 v[36:39], v[236:239], v[120:123], v[36:39]\n"
    "v_mfma_f32_16x16x32_bf16 v[76:79], v[236:239], v[124:127], v[76:79]\n"
    "buffer_load_dwordx4 v[236:239], v0, s[40:43], s50 offen offset:3072\n"
    "ds_read_b128 v[116:119], v9 offset:38912\n"
    "s_waitcnt vmcnt(31)\n"
    "v_mfma_f32_16x16x32_bf16 v[40:43], v[240:243], v[120:123], v[40:43]\n"
    "v_mfma_f32_16x16x32_bf16 v[80:83], v[240:243], v[124:127], v[80:83]\n"
    "s_add_u32 s50, s50, 4096\n"
    "buffer_load_dwordx4 v[240:243], v0, s[40:43], s50 offen\n"
    "s_waitcnt vmcnt(31)\n"
    "v_mfma_f32_16x16x32_bf16 v[44:47], v[244:247], v[120:123], v[44:47]\n"
    "v_mfma_f32_16x16x32_bf16 v[84:87], v[244:247], v[124:127], v[84:87]\n"
    "buffer_load_dwordx4 v[244:247], v0, s[40:43], s50 offen offset:1024\n"
    "s_waitcnt vmcnt(31)\n"
    "v_mfma_f32_16x16x32_bf16 v[48:51], v[248:251], v[120:123], v[48:51]\n"
    "v_mfma_f32_16x16x32_bf16 v[88:91], v[248:251], v[124:127], v[88:91]\n"
    "buffer_load_dwordx4 v[248:251], v0, s[40:43], s50 offen offset:2048\n"
    "s_waitcnt vmcnt(31)\n"
    "v_mfma_f32_16x16x32_bf16 v[52:55], v[252:255], v[120:123], v[52:55]\n"
    "v_mfma_f32_16x16x32_bf16 v[92:95], v[252:255], v[124:127], v[92:95]\n"
    "buffer_load_dwordx4 v[252:255], v0, s[40:43], s50 offen offset:3072\n"
    "s_waitcnt vmcnt(31) lgkmcnt(5)\n"
    "v_mfma_f32_16x16x32_bf16 v[16:19], v[128:131], v[96:99], v[16:19]\n"
    "s_waitcnt lgkmcnt(4)\n"
    "v_mfma_f32_16x16x32_bf16 v[56:59], v[128:131], v[100:103], v[56:59]\n"
    "s_add_u32 s50, s50, 4096\n"
    "buffer_load_dwordx4 v[128:131], v0, s[40:43], s50 offen\n"
    "ds_read_b128 v[120:123], v9 offset:19456\n"
    "s_waitcnt vmcnt(31)\n"
    "v_mfma_f32_16x16x32_bf16 v[20:23], v[132:135], v[96:99], v[20:23]\n"
    "v_mfma_f32_16x16x32_bf16 v[60:63], v[132:135], v[100:103], v[60:63]\n"
    "buffer_load_dwordx4 v[132:135], v0, s[40:43], s50 offen offset:1024\n"
    "s_waitcnt vmcnt(31)\n"
    "v_mfma_f32_16x16x32_bf16 v[24:27], v[136:139], v[96:99], v[24:27]\n"
    "v_mfma_f32_16x16x32_bf16 v[64:67], v[136:139], v[100:103], v[64:67]\n"
    "buffer_load_dwordx4 v[136:139], v0, s[40:43], s50 offen offset:2048\n"
    "s_waitcnt vmcnt(31)\n"
    "v_mfma_f32_16x16x32_bf16 v[28:31], v[140:143], v[96:99], v[28:31]\n"
    "v_mfma_f32_16x16x32_bf16 v[68:71], v[140:143], v[100:103], v[68:71]\n"
    "buffer_load_dwordx4 v[140:143], v0, s[40:43], s50 offen offset:3072\n"
    "s_waitcnt vmcnt(31)\n"
    "v_mfma_f32_16x16x32_bf16 v[32:35], v[144:147], v[96:99], v[32:35]\n"
    "v_mfma_f32_16x16x32_bf16 v[72:75], v[144:147], v[100:103], v[72:75]\n"
    "s_add_u32 s50, s50, 4096\n"
    "buffer_load_dwordx4 v[144:147], v0, s[40:43], s50 offen\n"
    "s_waitcnt vmcnt(31)\n"
    "v_mfma_f32_16x16x32_bf16 v[36:39], v[148:151], v[96:99], v[36:39]\n"
    "v_mfma_f32_16x16x32_bf16 v[76:79], v[148:151], v[100:103], v[76:79]\n"
    "buffer_load_dwordx4 v[148:151], v0, s[40:43], s50 offen offset:1024\n"
    "ds_read_b128 v[124:127], v9 offset:39936\n"
    "s_waitcnt vmcnt(31)\n"
    "v_mfma_f32_16x16x32_bf16 v[40:43], v[152:155], v[96:99], v[40:43]\n"
    "v_mfma_f32_16x16x32_bf16 v[80:83], v[152:155], v[100:103], v[80:83]\n"
    "buffer_load_dwordx4 v[152:155], v0, s[40:43], s50 offen offset:2048\n"
    "s_waitcnt vmcnt(31)\n"
    "v_mfma_f32_16x16x32_bf16 v[44:47], v[156:159], v[96:99], v[44:47]\n"
    "v_mfma_f32_16x16x32_bf16 v[84:87], v[156:159], v[100:103], v[84:87]\n"
    "buffer_load_dwordx4 v[156:159], v0, s[40:43], s50 offen offset:3072\n"
    "s_waitcnt vmcnt(31)\n"
    "v_mfma_f32_16x16x32_bf16 v[48:51], v[160:163], v[96:99], v[48:51]\n"
    "v_mfma_f32_16x16x32_bf16 v[88:91], v[160:163], v[100:103], v[88:91]\n"
    "s_add_u32 s48, s28, 138354688\n"
    "buffer_load_dwordx4 v[160:163], v8, s[40:43], s48 offen sc1\n"
    "s_waitcnt vmcnt(31)\n"
    "v_mfma_f32_16x16x32_bf16 v[52:55], v[164:167], v[96:99], v[52:55]\n"
    "v_mfma_f32_16x16x32_bf16 v[92:95], v[164:167], v[100:103], v[92:95]\n"
    "s_add_u32 s48, s28, 138354688\n"
    "buffer_load_dwordx4 v[164:167], v8, s[40:43], s48 offen offset:512 sc1\n"
    "s_waitcnt vmcnt(31) lgkmcnt(5)\n"
    "v_mfma_f32_16x16x32_bf16 v[16:19], v[168:171], v[104:107], v[16:19]\n"
    "s_waitcnt lgkmcnt(4)\n"
    "v_mfma_f32_16x16x32_bf16 v[56:59], v[168:171], v[108:111], v[56:59]\n"
    "s_add_u32 s48, s28, 138387456\n"
    "buffer_load_dwordx4 v[168:171], v8, s[40:43], s48 offen sc1\n"
    "s_waitcnt vmcnt(31)\n"
    "v_mfma_f32_16x16x32_bf16 v[20:23], v[172:175], v[104:107], v[20:23]\n"
    "v_mfma_f32_16x16x32_bf16 v[60:63], v[172:175], v[108:111], v[60:63]\n"
    "s_add_u32 s48, s28, 138387456\n"
    "buffer_load_dwordx4 v[172:175], v8, s[40:43], s48 offen offset:512 sc1\n"
    "s_waitcnt vmcnt(31)\n"
    "v_mfma_f32_16x16x32_bf16 v[24:27], v[176:179], v[104:107], v[24:27]\n"
    "v_mfma_f32_16x16x32_bf16 v[64:67], v[176:179], v[108:111], v[64:67]\n"
    "s_add_u32 s48, s28, 138389504\n"
    "buffer_load_dwordx4 v[176:179], v8, s[40:43], s48 offen sc1\n"
    "s_waitcnt vmcnt(31)\n"
    "v_mfma_f32_16x16x32_bf16 v[28:31], v[180:183], v[104:107], v[28:31]\n"
    "v_mfma_f32_16x16x32_bf16 v[68:71], v[180:183], v[108:111], v[68:71]\n"
    "s_add_u32 s48, s28, 138389504\n"
    "buffer_load_dwordx4 v[180:183], v8, s[40:43], s48 offen offset:512 sc1\n"
    "s_waitcnt vmcnt(31)\n"
    "v_mfma_f32_16x16x32_bf16 v[32:35], v[184:187], v[104:107], v[32:35]\n"
    "v_mfma_f32_16x16x32_bf16 v[72:75], v[184:187], v[108:111], v[72:75]\n"
    "s_add_u32 s48, s28, 138391552\n"
    "buffer_load_dwordx4 v[184:187], v8, s[40:43], s48 offen sc1\n"
    "s_waitcnt vmcnt(31)\n"
    "v_mfma_f32_16x16x32_bf16 v[36:39], v[188:191], v[104:107], v[36:39]\n"
    "v_mfma_f32_16x16x32_bf16 v[76:79], v[188:191], v[108:111], v[76:79]\n"
    "s_add_u32 s48, s28, 138391552\n"
    "buffer_load_dwordx4 v[188:191], v8, s[40:43], s48 offen offset:512 sc1\n"
    "s_waitcnt vmcnt(31)\n"
    "v_mfma_f32_16x16x32_bf16 v[40:43], v[192:195], v[104:107], v[40:43]\n"
    "v_mfma_f32_16x16x32_bf16 v[80:83], v[192:195], v[108:111], v[80:83]\n"
    "s_add_u32 s48, s28, 138393600\n"
    "buffer_load_dwordx4 v[192:195], v8, s[40:43], s48 offen sc1\n"
    "s_waitcnt vmcnt(31)\n"
    "v_mfma_f32_16x16x32_bf16 v[44:47], v[196:199], v[104:107], v[44:47]\n"
    "v_mfma_f32_16x16x32_bf16 v[84:87], v[196:199], v[108:111], v[84:87]\n"
    "s_add_u32 s48, s28, 138393600\n"
    "buffer_load_dwordx4 v[196:199], v8, s[40:43], s48 offen offset:512 sc1\n"
    "s_waitcnt vmcnt(31)\n"
    "v_mfma_f32_16x16x32_bf16 v[48:51], v[200:203], v[104:107], v[48:51]\n"
    "v_mfma_f32_16x16x32_bf16 v[88:91], v[200:203], v[108:111], v[88:91]\n"
    "s_add_u32 s48, s28, 138395648\n"
    "buffer_load_dwordx4 v[200:203], v8, s[40:43], s48 offen sc1\n"
    "s_waitcnt vmcnt(31)\n"
    "v_mfma_f32_16x16x32_bf16 v[52:55], v[204:207], v[104:107], v[52:55]\n"
    "v_mfma_f32_16x16x32_bf16 v[92:95], v[204:207], v[108:111], v[92:95]\n"
    "s_add_u32 s48, s28, 138395648\n"
    "buffer_load_dwordx4 v[204:207], v8, s[40:43], s48 offen offset:512 sc1\n"
    "s_waitcnt vmcnt(31) lgkmcnt(3)\n"
    "v_mfma_f32_16x16x32_bf16 v[16:19], v[208:211], v[112:115], v[16:19]\n"
    "s_waitcnt lgkmcnt(2)\n"
    "v_mfma_f32_16x16x32_bf16 v[56:59], v[208:211], v[116:119], v[56:59]\n"
    "s_waitcnt vmcnt(30)\n"
    "v_mfma_f32_16x16x32_bf16 v[20:23], v[212:215], v[112:115], v[20:23]\n"
    "v_mfma_f32_16x16x32_bf16 v[60:63], v[212:215], v[116:119], v[60:63]\n"
    "s_waitcnt vmcnt(29)\n"
    "v_mfma_f32_16x16x32_bf16 v[24:27], v[216:219], v[112:115], v[24:27]\n"
    "v_mfma_f32_16x16x32_bf16 v[64:67], v[216:219], v[116:119], v[64:67]\n"
    "s_waitcnt vmcnt(28)\n"
    "v_mfma_f32_16x16x32_bf16 v[28:31], v[220:223], v[112:115], v[28:31]\n"
    "v_mfma_f32_16x16x32_bf16 v[68:71], v[220:223], v[116:119], v[68:71]\n"
    "s_waitcnt vmcnt(27)\n"
    "v_mfma_f32_16x16x32_bf16 v[32:35], v[224:227], v[112:115], v[32:35]\n"
    "v_mfma_f32_16x16x32_bf16 v[72:75], v[224:227], v[116:119], v[72:75]\n"
    "s_waitcnt vmcnt(26)\n"
    "v_mfma_f32_16x16x32_bf16 v[36:39], v[228:231], v[112:115], v[36:39]\n"
    "v_mfma_f32_16x16x32_bf16 v[76:79], v[228:231], v[116:119], v[76:79]\n"
    "s_waitcnt vmcnt(25)\n"
    "v_mfma_f32_16x16x32_bf16 v[40:43], v[232:235], v[112:115], v[40:43]\n"
    "v_mfma_f32_16x16x32_bf16 v[80:83], v[232:235], v[116:119], v[80:83]\n"
    "s_waitcnt vmcnt(24)\n"
    "v_mfma_f32_16x16x32_bf16 v[44:47], v[236:239], v[112:115], v[44:47]\n"
    "v_mfma_f32_16x16x32_bf16 v[84:87], v[236:239], v[116:119], v[84:87]\n"
    "s_waitcnt vmcnt(23)\n"
    "v_mfma_f32_16x16x32_bf16 v[48:51], v[240:243], v[112:115], v[48:51]\n"
    "v_mfma_f32_16x16x32_bf16 v[88:91], v[240:243], v[116:119], v[88:91]\n"
    "s_waitcnt vmcnt(22)\n"
    "v_mfma_f32_16x16x32_bf16 v[52:55], v[244:247], v[112:115], v[52:55]\n"
    "v_mfma_f32_16x16x32_bf16 v[92:95], v[244:247], v[116:119], v[92:95]\n"
    "s_waitcnt vmcnt(21) lgkmcnt(1)\n"
    "v_mfma_f32_16x16x32_bf16 v[16:19], v[248:251], v[120:123], v[16:19]\n"
    "s_waitcnt lgkmcnt(0)\n"
    "v_mfma_f32_16x16x32_bf16 v[56:59], v[248:251], v[124:127], v[56:59]\n"
    "s_waitcnt vmcnt(20)\n"
    "v_mfma_f32_16x16x32_bf16 v[20:23], v[252:255], v[120:123], v[20:23]\n"
    "v_mfma_f32_16x16x32_bf16 v[60:63], v[252:255], v[124:127], v[60:63]\n"
    "s_waitcnt vmcnt(19)\n"
    "v_mfma_f32_16x16x32_bf16 v[24:27], v[128:131], v[120:123], v[24:27]\n"
    "v_mfma_f32_16x16x32_bf16 v[64:67], v[128:131], v[124:127], v[64:67]\n"
    "s_add_u32 s48, s28, 138346496\n"
    "buffer_load_dwordx4 v[128:131], v8, s[40:43], s48 offen sc1\n"
    "s_waitcnt vmcnt(19)\n"
    "v_mfma_f32_16x16x32_bf16 v[28:31], v[132:135], v[120:123], v[28:31]\n"
    "v_mfma_f32_16x16x32_bf16 v[68:71], v[132:135], v[124:127], v[68:71]\n"
    "s_add_u32 s48, s28, 138346496\n"
    "buffer_load_dwordx4 v[132:135], v8, s[40:43], s48 offen offset:512 sc1\n"
    "s_waitcnt vmcnt(19)\n"
    "v_mfma_f32_16x16x32_bf16 v[32:35], v[136:139], v[120:123], v[32:35]\n"
    "v_mfma_f32_16x16x32_bf16 v[72:75], v[136:139], v[124:127], v[72:75]\n"
    "s_add_u32 s48, s28, 138348544\n"
    "buffer_load_dwordx4 v[136:139], v8, s[40:43], s48 offen sc1\n"
    "s_waitcnt vmcnt(19)\n"
    "v_mfma_f32_16x16x32_bf16 v[36:39], v[140:143], v[120:123], v[36:39]\n"
    "v_mfma_f32_16x16x32_bf16 v[76:79], v[140:143], v[124:127], v[76:79]\n"
    "s_add_u32 s48, s28, 138348544\n"
    "buffer_load_dwordx4 v[140:143], v8, s[40:43], s48 offen offset:512 sc1\n"
    "s_waitcnt vmcnt(19)\n"
    "v_mfma_f32_16x16x32_bf16 v[40:43], v[144:147], v[120:123], v[40:43]\n"
    "v_mfma_f32_16x16x32_bf16 v[80:83], v[144:147], v[124:127], v[80:83]\n"
    "s_add_u32 s48, s28, 138350592\n"
    "buffer_load_dwordx4 v[144:147], v8, s[40:43], s48 offen sc1\n"
    "s_waitcnt vmcnt(19)\n"
    "v_mfma_f32_16x16x32_bf16 v[44:47], v[148:151], v[120:123], v[44:47]\n"
    "v_mfma_f32_16x16x32_bf16 v[84:87], v[148:151], v[124:127], v[84:87]\n"
    "s_add_u32 s48, s28, 138350592\n"
    "buffer_load_dwordx4 v[148:151], v8, s[40:43], s48 offen offset:512 sc1\n"
    "s_waitcnt vmcnt(19)\n"
    "v_mfma_f32_16x16x32_bf16 v[48:51], v[152:155], v[120:123], v[48:51]\n"
    "v_mfma_f32_16x16x32_bf16 v[88:91], v[152:155], v[124:127], v[88:91]\n"
    "s_add_u32 s48, s28, 138352640\n"
    "buffer_load_dwordx4 v[152:155], v8, s[40:43], s48 offen sc1\n"
    "s_waitcnt vmcnt(19)\n"
    "v_mfma_f32_16x16x32_bf16 v[52:55], v[156:159], v[120:123], v[52:55]\n"
    "v_mfma_f32_16x16x32_bf16 v[92:95], v[156:159], v[124:127], v[92:95]\n"
    "s_add_u32 s48, s28, 138352640\n"
    "buffer_load_dwordx4 v[156:159], v8, s[40:43], s48 offen offset:512 sc1\n"
    "s_waitcnt vmcnt(7)\n"
    "v_pk_add_f32 v[128:129], v[128:129], v[16:17]\n"
    "v_pk_add_f32 v[130:131], v[130:131], v[20:21]\n"
    "s_waitcnt vmcnt(6)\n"
    "v_pk_add_f32 v[132:133], v[132:133], v[18:19]\n"
    "v_pk_add_f32 v[134:135], v[134:135], v[22:23]\n"
    "s_waitcnt vmcnt(5)\n"
    "v_pk_add_f32 v[136:137], v[136:137], v[24:25]\n"
    "v_pk_add_f32 v[138:139], v[138:139], v[28:29]\n"
    "s_waitcnt vmcnt(4)\n"
    "v_pk_add_f32 v[140:141], v[140:141], v[26:27]\n"
    "v_pk_add_f32 v[142:143], v[142:143], v[30:31]\n"
    "s_waitcnt vmcnt(3)\n"
    "v_pk_add_f32 v[144:145], v[144:145], v[32:33]\n"
    "v_pk_add_f32 v[146:147], v[146:147], v[36:37]\n"
    "s_waitcnt vmcnt(2)\n"
    "v_pk_add_f32 v[148:149], v[148:149], v[34:35]\n"
    "v_pk_add_f32 v[150:151], v[150:151], v[38:39]\n"
    "s_waitcnt vmcnt(1)\n"
    "v_pk_add_f32 v[152:153], v[152:153], v[40:41]\n"
    "v_pk_add_f32 v[154:155], v[154:155], v[44:45]\n"
    "s_waitcnt vmcnt(0)\n"
    "v_pk_add_f32 v[156:157], v[156:157], v[42:43]\n"
    "v_pk_add_f32 v[158:159], v[158:159], v[46:47]\n"
    "v_pk_add_f32 v[160:161], v[160:161], v[48:49]\n"
    "v_pk_add_f32 v[162:163], v[162:163], v[52:53]\n"
    "v_pk_add_f32 v[164:165], v[164:165], v[50:51]\n"
    "v_pk_add_f32 v[166:167], v[166:167], v[54:55]\n"
    "v_pk_add_f32 v[168:169], v[168:169], v[56:57]\n"
    "v_pk_add_f32 v[170:171], v[170:171], v[60:61]\n"
    "v_pk_add_f32 v[172:173], v[172:173], v[58:59]\n"
    "v_pk_add_f32 v[174:175], v[174:175], v[62:63]\n"
    "v_pk_add_f32 v[176:177], v[176:177], v[64:65]\n"
    "v_pk_add_f32 v[178:179], v[178:179], v[68:69]\n"
    "v_pk_add_f32 v[180:181], v[180:181], v[66:67]\n"
    "v_pk_add_f32 v[182:183], v[182:183], v[70:71]\n"
    "v_pk_add_f32 v[184:185], v[184:185], v[72:73]\n"
    "v_pk_add_f32 v[186:187], v[186:187], v[76:77]\n"
    "v_pk_add_f32 v[188:189], v[188:189], v[74:75]\n"
    "v_pk_add_f32 v[190:191], v[190:191], v[78:79]\n"
    "v_pk_add_f32 v[192:193], v[192:193], v[80:81]\n"
    "v_pk_add_f32 v[194:195], v[194:195], v[84:85]\n"
    "v_pk_add_f32 v[196:197], v[196:197], v[82:83]\n"
    "v_pk_add_f32 v[198:199], v[198:199], v[86:87]\n"
    "v_pk_add_f32 v[200:201], v[200:201], v[88:89]\n"
    "v_pk_add_f32 v[202:203], v[202:203], v[92:93]\n"
    "v_pk_add_f32 v[204:205], v[204:205], v[90:91]\n"
    "v_pk_add_f32 v[206:207], v[206:207], v[94:95]\n"
    "s_cmp_eq_u32 s21, 0\n"
    "s_nop 0\n"
    "s_cbranch_scc1 hpb2_mid_%=\n"
    "s_mov_b32 s36, s66\n"
    "s_mov_b32 s37, s67\n"
    "s_mov_b32 s38, 2621440\n"
    "s_mov_b32 s39, 0x20000\n"
    "v_and_b32 v4, 15, v15\n"
    "v_mul_u32_u24 v4, 1280, v4\n"
    "v_and_b32 v5, 32, v15\n"
    "v_add_u32 v4, v4, v5\n"
    "v_bfe_u32 v5, v15, 4, 1\n"
    "v_lshl_add_u32 v4, v5, 3, v4\n"
    "s_mul_i32 s48, s26, 40960\n"
    "s_mul_i32 s49, s45, 320\n"
    "s_add_u32 s48, s48, s49\n"
    "s_add_u32 s49, s48, 20480\n"
    "v_cvt_pk_bf16_f32 v16, v128, v129\n"
    "v_cvt_pk_bf16_f32 v17, v130, v131\n"
    "v_cvt_pk_bf16_f32 v18, v132, v133\n"
    "v_cvt_pk_bf16_f32 v19, v134, v135\n"
    "v_cvt_pk_bf16_f32 v20, v136, v137\n"
    "v_cvt_pk_bf16_f32 v21, v138, v139\n"
    "v_cvt_pk_bf16_f32 v22, v140, v141\n"
    "v_cvt_pk_bf16_f32 v23, v142, v143\n"
    "v_cvt_pk_bf16_f32 v24, v144, v145\n"
    "v_cvt_pk_bf16_f32 v25, v146, v147\n"
    "v_cvt_pk_bf16_f32 v26, v148, v149\n"
    "v_cvt_pk_bf16_f32 v27, v150, v151\n"
    "v_cvt_pk_bf16_f32 v28, v152, v153\n"
    "v_cvt_pk_bf16_f32 v29, v154, v155\n"
    "v_cvt_pk_bf16_f32 v30, v156, v157\n"
    "v_cvt_pk_bf16_f32 v31, v158, v159\n"
    "v_cvt_pk_bf16_f32 v32, v160, v161\n"
    "v_cvt_pk_bf16_f32 v33, v162, v163\n"
    "v_cvt_pk_bf16_f32 v34, v164, v165\n"
    "v_cvt_pk_bf16_f32 v35, v166, v167\n"
    "v_cvt_pk_bf16_f32 v36, v168, v169\n"
    "v_cvt_pk_bf16_f32 v37, v170, v171\n"
    "v_cvt_pk_bf16_f32 v38, v172, v173\n"
    "v_cvt_pk_bf16_f32 v39, v174, v175\n"
    "v_cvt_pk_bf16_f32 v40, v176, v177\n"
    "v_cvt_pk_bf16_f32 v41, v178, v179\n"
    "v_cvt_pk_bf16_f32 v42, v180, v181\n"
    "v_cvt_pk_bf16_f32 v43, v182, v183\n"
    "v_cvt_pk_bf16_f32 v44, v184, v185\n"
    "v_cvt_pk_bf16_f32 v45, v186, v187\n"
    "v_cvt_pk_bf16_f32 v46, v188, v189\n"
    "v_cvt_pk_bf16_f32 v47, v190, v191\n"
    "v_cvt_pk_bf16_f32 v48, v192, v193\n"
    "v_cvt_pk_bf16_f32 v49, v194, v195\n"
    "v_cvt_pk_bf16_f32 v50, v196, v197\n"
    "v_cvt_pk_bf16_f32 v51, v198, v199\n"
    "v_cvt_pk_bf16_f32 v52, v200, v201\n"
    "v_cvt_pk_bf16_f32 v53, v202, v203\n"
    "v_cvt_pk_bf16_f32 v54, v204, v205\n"
    "v_cvt_pk_bf16_f32 v55, v206, v207\n"
    "buffer_store_dwordx2 v[16:17], v4, s[36:39], s48 offen offset:0\n"
    "buffer_store_dwordx2 v[18:19], v4, s[36:39], s48 offen offset:16\n"
    "buffer_store_dwordx2 v[20:21], v4, s[36:39], s48 offen offset:64\n"
    "buffer_store_dwordx2 v[22:23], v4, s[36:39], s48 offen offset:80\n"
    "buffer_store_dwordx2 v[24:25], v4, s[36:39], s48 offen offset:128\n"
    "buffer_store_dwordx2 v[26:27], v4, s[36:39], s48 offen offset:144\n"
    "buffer_store_dwordx2 v[28:29], v4, s[36:39], s48 offen offset:192\n"
    "buffer_store_dwordx2 v[30:31], v4, s[36:39], s48 offen offset:208\n"
    "buffer_store_dwordx2 v[32:33], v4, s[36:39], s48 offen offset:256\n"
    "buffer_store_dwordx2 v[34:35], v4, s[36:39], s48 offen offset:272\n"
    "buffer_store_dwordx2 v[36:37], v4, s[36:39], s49 offen offset:0\n"
    "buffer_store_dwordx2 v[38:39], v4, s[36:39], s49 offen offset:16\n"
    "buffer_store_dwordx2 v[40:41], v4, s[36:39], s49 offen offset:64\n"
    "buffer_store_dwordx2 v[42:43], v4, s[36:39], s49 offen offset:80\n"
    "buffer_store_dwordx2 v[44:45], v4, s[36:39], s49 offen offset:128\n"
    "buffer_store_dwordx2 v[46:47], v4, s[36:39], s49 offen offset:144\n"
    "buffer_store_dwordx2 v[48:49], v4, s[36:39], s49 offen offset:192\n"
    "buffer_store_dwordx2 v[50:51], v4, s[36:39], s49 offen offset:208\n"
    "buffer_store_dwordx2 v[52:53], v4, s[36:39], s49 offen offset:256\n"
    "buffer_store_dwordx2 v[54:55], v4, s[36:39], s49 offen offset:272\n"
    "s_mov_b32 s54, 0x40000000\n"
    "s_nop 0\n"
    "s_branch hp_done_%=\n"
    "hpb2_mid_%=:\n"
    "v_and_b32 v6, 0xf0, v0\n"
    "v_add_u32 v6, 117760, v6\n"
    "s_lshl_b32 s48, s45, 2\n"
    "v_add_u32 v7, s48, v6\n"
    "v_pk_add_f32 v[10:11], v[128:129], v[130:131]\n"
    "v_pk_add_f32 v[10:11], v[10:11], v[132:133]\n"
    "v_pk_add_f32 v[10:11], v[10:11], v[134:135]\n"
    "v_pk_add_f32 v[10:11], v[10:11], v[136:137]\n"
    "v_pk_add_f32 v[10:11], v[10:11], v[138:139]\n"
    "v_pk_add_f32 v[10:11], v[10:11], v[140:141]\n"
    "v_pk_add_f32 v[10:11], v[10:11], v[142:143]\n"
    "v_pk_add_f32 v[10:11], v[10:11], v[144:145]\n"
    "v_pk_add_f32 v[10:11], v[10:11], v[146:147]\n"
    "v_pk_add_f32 v[10:11], v[10:11], v[148:149]\n"
    "v_pk_add_f32 v[10:11], v[10:11], v[150:151]\n"
    "v_pk_add_f32 v[10:11], v[10:11], v[152:153]\n"
    "v_pk_add_f32 v[10:11], v[10:11], v[154:155]\n"
    "v_pk_add_f32 v[10:11], v[10:11], v[156:157]\n"
    "v_pk_add_f32 v[10:11], v[10:11], v[158:159]\n"
    "v_pk_add_f32 v[10:11], v[10:11], v[160:161]\n"
    "v_pk_add_f32 v[10:11], v[10:11], v[162:163]\n"
    "v_pk_add_f32 v[10:11], v[10:11], v[164:165]\n"
    "v_pk_add_f32 v[10:11], v[10:11], v[166:167]\n"
    "v_pk_add_f32 v[12:13], v[168:169], v[170:171]\n"
    "v_pk_add_f32 v[12:13], v[12:13], v[172:173]\n"
    "v_pk_add_f32 v[12:13], v[12:13], v[174:175]\n"
    "v_pk_add_f32 v[12:13], v[12:13], v[176:177]\n"
    "v_pk_add_f32 v[12:13], v[12:13], v[178:179]\n"
    "v_pk_add_f32 v[12:13], v[12:13], v[180:181]\n"
    "v_pk_add_f32 v[12:13], v[12:13], v[182:183]\n"
    "v_pk_add_f32 v[12:13], v[12:13], v[184:185]\n"
    "v_pk_add_f32 v[12:13], v[12:13], v[186:187]\n"
    "v_pk_add_f32 v[12:13], v[12:13], v[188:189]\n"
    "v_pk_add_f32 v[12:13], v[12:13], v[190:191]\n"
    "v_pk_add_f32 v[12:13], v[12:13], v[192:193]\n"
    "v_pk_add_f32 v[12:13], v[12:13], v[194:195]\n"
    "v_pk_add_f32 v[12:13], v[12:13], v[196:197]\n"
    "v_pk_add_f32 v[12:13], v[12:13], v[198:199]\n"
    "v_pk_add_f32 v[12:13], v[12:13], v[200:201]\n"
    "v_pk_add_f32 v[12:13], v[12:13], v[202:203]\n"
    "v_pk_add_f32 v[12:13], v[12:13], v[204:205]\n"
    "v_pk_add_f32 v[12:13], v[12:13], v[206:207]\n"
    "v_add_f32 v10, v10, v11\n"
    "v_mov_b32 v11, v10\n"
    "s_nop 1\n"
    "v_permlane16_swap_b32 v10, v11\n"
    "v_add_f32 v10, v10, v11\n"
    "v_mov_b32 v11, v10\n"
    "s_nop 1\n"
    "v_permlane32_swap_b32 v10, v11\n"
    "v_add_f32 v10, v10, v11\n"
    "ds_write_b32 v7, v10 offset:0\n"
    "v_add_f32 v12, v12, v13\n"
    "v_mov_b32 v13, v12\n"
    "s_nop 1\n"
    "v_permlane16_swap_b32 v12, v13\n"
    "v_add_f32 v12, v12, v13\n"
    "v_mov_b32 v13, v12\n"
    "s_nop 1\n"
    "v_permlane32_swap_b32 v12, v13\n"
    "v_add_f32 v12, v12, v13\n"
    "ds_write_b32 v7, v12 offset:256\n"
    "s_waitcnt lgkmcnt(0)\n"
    "s_barrier\n"
    "ds_read_b128 v[96:99], v6 offset:0\n"
    "ds_read_b128 v[100:103], v6 offset:256\n"
    "s_waitcnt lgkmcnt(1)\n"
    "v_pk_add_f32 v[96:97], v[96:97], v[98:99]\n"
    "v_add_f32 v10, v96, v97\n"
    "v_mul_f32 v10, 0xbacccccd, v10\n"
    "s_waitcnt lgkmcnt(0)\n"
    "v_pk_add_f32 v[100:101], v[100:101], v[102:103]\n"
    "v_add_f32 v12, v100, v101\n"
    "v_mul_f32 v12, 0xbacccccd, v12\n"
    "v_pk_add_f32 v[16:17], v[128:129], v[10:11] op_sel_hi:[1,0]\n"
    "v_pk_add_f32 v[18:19], v[130:131], v[10:11] op_sel_hi:[1,0]\n"
    "v_pk_add_f32 v[20:21], v[132:133], v[10:11] op_sel_hi:[1,0]\n"
    "v_pk_add_f32 v[22:23], v[134:135], v[10:11] op_sel_hi:[1,0]\n"
    "v_pk_add_f32 v[24:25], v[136:137], v[10:11] op_sel_hi:[1,0]\n"
    "v_pk_add_f32 v[26:27], v[138:139], v[10:11] op_sel_hi:[1,0]\n"
    "v_pk_add_f32 v[28:29], v[140:141], v[10:11] op_sel_hi:[1,0]\n"
    "v_pk_add_f32 v[30:31], v[142:143], v[10:11] op_sel_hi:[1,0]\n"
    "v_pk_add_f32 v[32:33], v[144:145], v[10:11] op_sel_hi:[1,0]\n"
    "v_pk_add_f32 v[34:35], v[146:147], v[10:11] op_sel_hi:[1,0]\n"
    "v_pk_add_f32 v[36:37], v[148:149], v[10:11] op_sel_hi:[1,0]\n"
    "v_pk_add_f32 v[38:39], v[150:151], v[10:11] op_sel_hi:[1,0]\n"
    "v_pk_add_f32 v[40:41], v[152:153], v[10:11] op_sel_hi:[1,0]\n"
    "v_pk_add_f32 v[42:43], v[154:155], v[10:11] op_sel_hi:[1,0]\n"
    "v_pk_add_f32 v[44:45], v[156:157], v[10:11] op_sel_hi:[1,0]\n"
    "v_pk_add_f32 v[46:47], v[158:159], v[10:11] op_sel_hi:[1,0]\n"
    "v_pk_add_f32 v[48:49], v[160:161], v[10:11] op_sel_hi:[1,0]\n"
    "v_pk_add_f32 v[50:51], v[162:163], v[10:11] op_sel_hi:[1,0]\n"
    "v_pk_add_f32 v[52:53], v[164:165], v[10:11] op_sel_hi:[1,0]\n"
    "v_pk_add_f32 v[54:55], v[166:167], v[10:11] op_sel_hi:[1,0]\n"
    "v_pk_add_f32 v[56:57], v[168:169], v[12:13] op_sel_hi:[1,0]\n"
    "v_pk_add_f32 v[58:59], v[170:171], v[12:13] op_sel_hi:[1,0]\n"
    "v_pk_add_f32 v[60:61], v[172:173], v[12:13] op_sel_hi:[1,0]\n"
    "v_pk_add_f32 v[62:63], v[174:175], v[12:13] op_sel_hi:[1,0]\n"
    "v_pk_add_f32 v[64:65], v[176:177], v[12:13] op_sel_hi:[1,0]\n"
    "v_pk_add_f32 v[66:67], v[178:179], v[12:13] op_sel_hi:[1,0]\n"
    "v_pk_add_f32 v[68:69], v[180:181], v[12:13] op_sel_hi:[1,0]\n"
    "v_pk_add_f32 v[70:71], v[182:183], v[12:13] op_sel_hi:[1,0]\n"
    "v_pk_add_f32 v[72:73], v[184:185], v[12:13] op_sel_hi:[1,0]\n"
    "v_pk_add_f32 v[74:75], v[186:187], v[12:13] op_sel_hi:[1,0]\n"
    "v_pk_add_f32 v[76:77], v[188:189], v[12:13] op_sel_hi:[1,0]\n"
    "v_pk_add_f32 v[78:79], v[190:191], v[12:13] op_sel_hi:[1,0]\n"
    "v_pk_add_f32 v[80:81], v[192:193], v[12:13] op_sel_hi:[1,0]\n"
    "v_pk_add_f32 v[82:83], v[194:195], v[12:13] op_sel_hi:[1,0]\n"
    "v_pk_add_f32 v[84:85], v[196:197], v[12:13] op_sel_hi:[1,0]\n"
    "v_pk_add_f32 v[86:87], v[198:199], v[12:13] op_sel_hi:[1,0]\n"
    "v_pk_add_f32 v[88:89], v[200:201], v[12:13] op_sel_hi:[1,0]\n"
    "v_pk_add_f32 v[90:91], v[202:203], v[12:13] op_sel_hi:[1,0]\n"
    "v_pk_add_f32 v[92:93], v[204:205], v[12:13] op_sel_hi:[1,0]\n"
    "v_pk_add_f32 v[94:95], v[206:207], v[12:13] op_sel_hi:[1,0]\n"
    "v_pk_mul_f32 v[10:11], v[16:17], v[16:17]\n"
    "v_pk_fma_f32 v[10:11], v[18:19], v[18:19], v[10:11]\n"
    "v_pk_fma_f32 v[10:11], v[20:21], v[20:21], v[10:11]\n"
    "v_pk_fma_f32 v[10:11], v[22:23], v[22:23], v[10:11]\n"
    "v_pk_fma_f32 v[10:11], v[24:25], v[24:25], v[10:11]\n"
    "v_pk_fma_f32 v[10:11], v[26:27], v[26:27], v[10:11]\n"
    "v_pk_fma_f32 v[10:11], v[28:29], v[28:29], v[10:11]\n"
    "v_pk_fma_f32 v[10:11], v[30:31], v[30:31], v[10:11]\n"
    "v_pk_fma_f32 v[10:11], v[32:33], v[32:33], v[10:11]\n"
    "v_pk_fma_f32 v[10:11], v[34:35], v[34:35], v[10:11]\n"
    "v_pk_fma_f32 v[10:11], v[36:37], v[36:37], v[10:11]\n"
    "v_pk_fma_f32 v[10:11], v[38:39], v[38:39], v[10:11]\n"
    "v_pk_fma_f32 v[10:11], v[40:41], v[40:41], v[10:11]\n"
    "v_pk_fma_f32 v[10:11], v[42:43], v[42:43], v[10:11]\n"
    "v_pk_fma_f32 v[10:11], v[44:45], v[44:45], v[10:11]\n"
    "v_pk_fma_f32 v[10:11], v[46:47], v[46:47], v[10:11]\n"
    "v_pk_fma_f32 v[10:11], v[48:49], v[48:49], v[10:11]\n"
    "v_pk_fma_f32 v[10:11], v[50:51], v[50:51], v[10:11]\n"
    "v_pk_fma_f32 v[10:11], v[52:53], v[52:53], v[10:11]\n"
    "v_pk_fma_f32 v[10:11], v[54:55], v[54:55], v[10:11]\n"
    "v_pk_mul_f32 v[12:13], v[56:57], v[56:57]\n"
    "v_pk_fma_f32 v[12:13], v[58:59], v[58:59], v[12:13]\n"
    "v_pk_fma_f32 v[12:13], v[60:61], v[60:61], v[12:13]\n"
    "v_pk_fma_f32 v[12:13], v[62:63], v[62:63], v[12:13]\n"
    "v_pk_fma_f32 v[12:13], v[64:65], v[64:65], v[12:13]\n"
    "v_pk_fma_f32 v[12:13], v[66:67], v[66:67], v[12:13]\n"
    "v_pk_fma_f32 v[12:13], v[68:69], v[68:69], v[12:13]\n"
    "v_pk_fma_f32 v[12:13], v[70:71], v[70:71], v[12:13]\n"
    "v_pk_fma_f32 v[12:13], v[72:73], v[72:73], v[12:13]\n"
    "v_pk_fma_f32 v[12:13], v[74:75], v[74:75], v[12:13]\n"
    "v_pk_fma_f32 v[12:13], v[76:77], v[76:77], v[12:13]\n"
    "v_pk_fma_f32 v[12:13], v[78:79], v[78:79], v[12:13]\n"
    "v_pk_fma_f32 v[12:13], v[80:81], v[80:81], v[12:13]\n"
    "v_pk_fma_f32 v[12:13], v[82:83], v[82:83], v[12:13]\n"
    "v_pk_fma_f32 v[12:13], v[84:85], v[84:85], v[12:13]\n"
    "v_pk_fma_f32 v[12:13], v[86:87], v[86:87], v[12:13]\n"
    "v_pk_fma_f32 v[12:13], v[88:89], v[88:89], v[12:13]\n"
    "v_pk_fma_f32 v[12:13], v[90:91], v[90:91], v[12:13]\n"
    "v_pk_fma_f32 v[12:13], v[92:93], v[92:93], v[12:13]\n"
    "v_pk_fma_f32 v[12:13], v[94:95], v[94:95], v[12:13]\n"
    "v_add_f32 v10, v10, v11\n"
    "v_mov_b32 v11, v10\n"
    "s_nop 1\n"
    "v_permlane16_swap_b32 v10, v11\n"
    "v_add_f32 v10, v10, v11\n"
    "v_mov_b32 v11, v10\n"
    "s_nop 1\n"
    "v_permlane32_swap_b32 v10, v11\n"
    "v_add_f32 v10, v10, v11\n"
    "ds_write_b32 v7, v10 offset:512\n"
    "v_add_f32 v12, v12, v13\n"
    "v_mov_b32 v13, v12\n"
    "s_nop 1\n"
    "v_permlane16_swap_b32 v12, v13\n"
    "v_add_f32 v12, v12, v13\n"
    "v_mov_b32 v13, v12\n"
    "s_nop 1\n"
    "v_permlane32_swap_b32 v12, v13\n"
    "v_add_f32 v12, v12, v13\n"
    "ds_write_b32 v7, v12 offset:768\n"
    "s_waitcnt lgkmcnt(0)\n"
    "s_barrier\n"
    "ds_read_b128 v[96:99], v6 offset:512\n"
    "ds_read_b128 v[100:103], v6 offset:768\n"
    "s_waitcnt lgkmcnt(1)\n"
    "v_pk_add_f32 v[96:97], v[96:97], v[98:99]\n"
    "v_add_f32 v10, v96, v97\n"
    "v_mul_f32 v10, 0x3acccccd, v10\n"
    "s_waitcnt lgkmcnt(0)\n"
    "v_pk_add_f32 v[100:101], v[100:101], v[102:103]\n"
    "v_add_f32 v12, v100, v101\n"
    "v_mul_f32 v12, 0x3acccccd, v12\n"
    "v_add_f32 v10, 0x3727c5ac, v10\n"
    "v_rsq_f32 v10, v10\n"
    "v_add_f32 v12, 0x3727c5ac, v12\n"
    "v_rsq_f32 v12, v12\n"
    "v_pk_mul_f32 v[16:17], v[16:17], v[10:11] op_sel_hi:[1,0]\n"
    "v_pk_mul_f32 v[18:19], v[18:19], v[10:11] op_sel_hi:[1,0]\n"
    "v_pk_mul_f32 v[20:21], v[20:21], v[10:11] op_sel_hi:[1,0]\n"
    "v_pk_mul_f32 v[22:23], v[22:23], v[10:11] op_sel_hi:[1,0]\n"
    "v_pk_mul_f32 v[24:25], v[24:25], v[10:11] op_sel_hi:[1,0]\n"
    "v_pk_mul_f32 v[26:27], v[26:27], v[10:11] op_sel_hi:[1,0]\n"
    "v_pk_mul_f32 v[28:29], v[28:29], v[10:11] op_sel_hi:[1,0]\n"
    "v_pk_mul_f32 v[30:31], v[30:31], v[10:11] op_sel_hi:[1,0]\n"
    "v_pk_mul_f32 v[32:33], v[32:33], v[10:11] op_sel_hi:[1,0]\n"
    "v_pk_mul_f32 v[34:35], v[34:35], v[10:11] op_sel_hi:[1,0]\n"
    "v_pk_mul_f32 v[36:37], v[36:37], v[10:11] op_sel_hi:[1,0]\n"
    "v_pk_mul_f32 v[38:39], v[38:39], v[10:11] op_sel_hi:[1,0]\n"
    "v_pk_mul_f32 v[40:41], v[40:41], v[10:11] op_sel_hi:[1,0]\n"
    "v_pk_mul_f32 v[42:43], v[42:43], v[10:11] op_sel_hi:[1,0]\n"
    "v_pk_mul_f32 v[44:45], v[44:45], v[10:11] op_sel_hi:[1,0]\n"
    "v_pk_mul_f32 v[46:47], v[46:47], v[10:11] op_sel_hi:[1,0]\n"
    "v_pk_mul_f32 v[48:49], v[48:49], v[10:11] op_sel_hi:[1,0]\n"
    "v_pk_mul_f32 v[50:51], v[50:51], v[10:11] op_sel_hi:[1,0]\n"
    "v_pk_mul_f32 v[52:53], v[52:53], v[10:11] op_sel_hi:[1,0]\n"
    "v_pk_mul_f32 v[54:55], v[54:55], v[10:11] op_sel_hi:[1,0]\n"
    "v_pk_mul_f32 v[56:57], v[56:57], v[12:13] op_sel_hi:[1,0]\n"
    "v_pk_mul_f32 v[58:59], v[58:59], v[12:13] op_sel_hi:[1,0]\n"
    "v_pk_mul_f32 v[60:61], v[60:61], v[12:13] op_sel_hi:[1,0]\n"
    "v_pk_mul_f32 v[62:63], v[62:63], v[12:13] op_sel_hi:[1,0]\n"
    "v_pk_mul_f32 v[64:65], v[64:65], v[12:13] op_sel_hi:[1,0]\n"
    "v_pk_mul_f32 v[66:67], v[66:67], v[12:13] op_sel_hi:[1,0]\n"
    "v_pk_mul_f32 v[68:69], v[68:69], v[12:13] op_sel_hi:[1,0]\n"
    "v_pk_mul_f32 v[70:71], v[70:71], v[12:13] op_sel_hi:[1,0]\n"
    "v_pk_mul_f32 v[72:73], v[72:73], v[12:13] op_sel_hi:[1,0]\n"
    "v_pk_mul_f32 v[74:75], v[74:75], v[12:13] op_sel_hi:[1,0]\n"
    "v_pk_mul_f32 v[76:77], v[76:77], v[12:13] op_sel_hi:[1,0]\n"
    "v_pk_mul_f32 v[78:79], v[78:79], v[12:13] op_sel_hi:[1,0]\n"
    "v_pk_mul_f32 v[80:81], v[80:81], v[12:13] op_sel_hi:[1,0]\n"
    "v_pk_mul_f32 v[82:83], v[82:83], v[12:13] op_sel_hi:[1,0]\n"
    "v_pk_mul_f32 v[84:85], v[84:85], v[12:13] op_sel_hi:[1,0]\n"
    "v_pk_mul_f32 v[86:87], v[86:87], v[12:13] op_sel_hi:[1,0]\n"
    "v_pk_mul_f32 v[88:89], v[88:89], v[12:13] op_sel_hi:[1,0]\n"
    "v_pk_mul_f32 v[90:91], v[90:91], v[12:13] op_sel_hi:[1,0]\n"
    "v_pk_mul_f32 v[92:93], v[92:93], v[12:13] op_sel_hi:[1,0]\n"
    "v_pk_mul_f32 v[94:95], v[94:95], v[12:13] op_sel_hi:[1,0]\n"
    "s_lshr_b32 s51, s28, 1\n"
    "v_cvt_pk_bf16_f32 v208, v16, v17\n"
    "v_cvt_pk_bf16_f32 v209, v20, v21\n"
    "v_cvt_pk_bf16_f32 v210, v18, v19\n"
    "v_cvt_pk_bf16_f32 v211, v22, v23\n"
    "s_add_u32 s48, s51, 5144576\n"
    "buffer_store_dwordx4 v[208:211], v0, s[40:43], s48 offen sc1\n"
    "v_cvt_pk_bf16_f32 v212, v24, v25\n"
    "v_cvt_pk_bf16_f32 v213, v28, v29\n"
    "v_cvt_pk_bf16_f32 v214, v26, v27\n"
    "v_cvt_pk_bf16_f32 v215, v30, v31\n"
    "s_add_u32 s48, s51, 5145600\n"
    "buffer_store_dwordx4 v[212:215], v0, s[40:43], s48 offen sc1\n"
    "v_cvt_pk_bf16_f32 v216, v32, v33\n"
    "v_cvt_pk_bf16_f32 v217, v36, v37\n"
    "v_cvt_pk_bf16_f32 v218, v34, v35\n"
    "v_cvt_pk_bf16_f32 v219, v38, v39\n"
    "s_add_u32 s48, s51, 5146624\n"
    "buffer_store_dwordx4 v[216:219], v0, s[40:43], s48 offen sc1\n"
    "v_cvt_pk_bf16_f32 v220, v40, v41\n"
    "v_cvt_pk_bf16_f32 v221, v44, v45\n"
    "v_cvt_pk_bf16_f32 v222, v42, v43\n"
    "v_cvt_pk_bf16_f32 v223, v46, v47\n"
    "s_add_u32 s48, s51, 5147648\n"
    "buffer_store_dwordx4 v[220:223], v0, s[40:43], s48 offen sc1\n"
    "v_cvt_pk_bf16_f32 v224, v48, v49\n"
    "v_cvt_pk_bf16_f32 v225, v52, v53\n"
    "v_cvt_pk_bf16_f32 v226, v50, v51\n"
    "v_cvt_pk_bf16_f32 v227, v54, v55\n"
    "s_add_u32 s48, s51, 5148672\n"
    "buffer_store_dwordx4 v[224:227], v0, s[40:43], s48 offen sc1\n"
    "v_cvt_pk_bf16_f32 v228, v56, v57\n"
    "v_cvt_pk_bf16_f32 v229, v60, v61\n"
    "v_cvt_pk_bf16_f32 v230, v58, v59\n"
    "v_cvt_pk_bf16_f32 v231, v62, v63\n"
    "s_add_u32 s48, s51, 5165056\n"
    "buffer_store_dwordx4 v[228:231], v0, s[40:43], s48 offen sc1\n"
    "v_cvt_pk_bf16_f32 v232, v64, v65\n"
    "v_cvt_pk_bf16_f32 v233, v68, v69\n"
    "v_cvt_pk_bf16_f32 v234, v66, v67\n"
    "v_cvt_pk_bf16_f32 v235, v70, v71\n"
    "s_add_u32 s48, s51, 5166080\n"
    "buffer_store_dwordx4 v[232:235], v0, s[40:43], s48 offen sc1\n"
    "v_cvt_pk_bf16_f32 v236, v72, v73\n"
    "v_cvt_pk_bf16_f32 v237, v76, v77\n"
    "v_cvt_pk_bf16_f32 v238, v74, v75\n"
    "v_cvt_pk_bf16_f32 v239, v78, v79\n"
    "s_add_u32 s48, s51, 5167104\n"
    "buffer_store_dwordx4 v[236:239], v0, s[40:43], s48 offen sc1\n"
    "v_cvt_pk_bf16_f32 v240, v80, v81\n"
    "v_cvt_pk_bf16_f32 v241, v84, v85\n"
    "v_cvt_pk_bf16_f32 v242, v82, v83\n"
    "v_cvt_pk_bf16_f32 v243, v86, v87\n"
    "s_add_u32 s48, s51, 5168128\n"
    "buffer_store_dwordx4 v[240:243], v0, s[40:43], s48 offen sc1\n"
    "v_cvt_pk_bf16_f32 v244, v88, v89\n"
    "v_cvt_pk_bf16_f32 v245, v92, v93\n"
    "v_cvt_pk_bf16_f32 v246, v90, v91\n"
    "v_cvt_pk_bf16_f32 v247, v94, v95\n"
    "s_add_u32 s48, s51, 5169152\n"
    "buffer_store_dwordx4 v[244:247], v0, s[40:43], s48 offen sc1\n"
    "s_add_u32 s48, s28, 133103616\n"
    "buffer_store_dwordx4 v[128:131], v8, s[40:43], s48 offen sc1\n"
    "s_add_u32 s48, s28, 133103616\n"
    "buffer_store_dwordx4 v[132:135], v8, s[40:43], s48 offen offset:512 sc1\n"
    "s_add_u32 s48, s28, 133105664\n"
    "buffer_store_dwordx4 v[136:139], v8, s[40:43], s48 offen sc1\n"
    "s_add_u32 s48, s28, 133105664\n"
    "buffer_store_dwordx4 v[140:143], v8, s[40:43], s48 offen offset:512 sc1\n"
    "s_add_u32 s48, s28, 133107712\n"
    "buffer_store_dwordx4 v[144:147], v8, s[40:43], s48 offen sc1\n"
    "s_add_u32 s48, s28, 133107712\n"
    "buffer_store_dwordx4 v[148:151], v8, s[40:43], s48 offen offset:512 sc1\n"
    "s_add_u32 s48, s28, 133109760\n"
    "buffer_store_dwordx4 v[152:155], v8, s[40:43], s48 offen sc1\n"
    "s_add_u32 s48, s28, 133109760\n"
    "buffer_store_dwordx4 v[156:159], v8, s[40:43], s48 offen offset:512 sc1\n"
    "s_add_u32 s48, s28, 133111808\n"
    "buffer_store_dwordx4 v[160:163], v8, s[40:43], s48 offen sc1\n"
    "s_add_u32 s48, s28, 133111808\n"
    "buffer_store_dwordx4 v[164:167], v8, s[40:43], s48 offen offset:512 sc1\n"
    "s_add_u32 s48, s28, 133144576\n"
    "buffer_store_dwordx4 v[168:171], v8, s[40:43], s48 offen sc1\n"
    "s_add_u32 s48, s28, 133144576\n"
    "buffer_store_dwordx4 v[172:175], v8, s[40:43], s48 offen offset:512 sc1\n"
    "s_add_u32 s48, s28, 133146624\n"
    "buffer_store_dwordx4 v[176:179], v8, s[40:43], s48 offen sc1\n"
    "s_add_u32 s48, s28, 133146624\n"
    "buffer_store_dwordx4 v[180:183], v8, s[40:43], s48 offen offset:512 sc1\n"
    "s_add_u32 s48, s28, 133148672\n"
    "buffer_store_dwordx4 v[184:187], v8, s[40:43], s48 offen sc1\n"
    "s_add_u32 s48, s28, 133148672\n"
    "buffer_store_dwordx4 v[188:191], v8, s[40:43], s48 offen offset:512 sc1\n"
    "s_add_u32 s48, s28, 133150720\n"
    "buffer_store_dwordx4 v[192:195], v8, s[40:43], s48 offen sc1\n"
    "s_add_u32 s48, s28, 133150720\n"
    "buffer_store_dwordx4 v[196:199], v8, s[40:43], s48 offen offset:512 sc1\n"
    "s_add_u32 s48, s28, 133152768\n"
    "buffer_store_dwordx4 v[200:203], v8, s[40:43], s48 offen sc1\n"
    "s_add_u32 s48, s28, 133152768\n"
    "buffer_store_dwordx4 v[204:207], v8, s[40:43], s48 offen offset:512 sc1\n"
    "s_add_u32 s54, s26, 64\n"
    "s_lshl_b32 s54, s54, 7\n"
    "s_add_u32 s54, s54, 2140672\n"
    "hp_done_%=:\n"
    "s_cmp_eq_u32 s45, 0\n"
    "s_cselect_b32 s59, 1, 0\n"
    "v_lshlrev_b32 v2, 7, v15\n"
    "v_cmp_gt_u32 vcc, s59, v15\n"
    "v_mov_b32 v4, 2147483632\n"
    "s_nop 0\n"
    "v_cndmask_b32 v2, v4, v2, vcc\n"
    "v_mov_b32 v1, 1\n"
    "s_waitcnt vmcnt(0)\n"
    "s_waitcnt lgkmcnt(0)\n"
    "s_barrier\n"
    "buffer_atomic_add v1, v2, s[40:43], s54 offen\n"
    "s_nop 1\n"
    "s_branch hp_next_%=\n"
    "hp_ret_%=:\n"
    "s_waitcnt vmcnt(0)\n"
    "s_waitcnt lgkmcnt(0)\n"
    "s_barrier\n"
    "hp_next_%=:\n"
    "v_mov_b32 v3, 155648\n"
    "s_cmp_lg_u32 s45, 0\n"
    "s_nop 0\n"
    "s_cbranch_scc1 hp_tk_%=\n"
    "s_cmp_eq_u32 s45, 0\n"
    "s_cselect_b32 s59, 1, 0\n"
    "v_lshlrev_b32 v2, 7, v15\n"
    "v_cmp_gt_u32 vcc, s59, v15\n"
    "v_mov_b32 v4, 2147483632\n"
    "s_nop 0\n"
    "v_cndmask_b32 v2, v4, v2, vcc\n"
    "v_mov_b32 v1, 1\n"
    "s_lshl_b32 s54, s21, 7\n"
    "s_add_u32 s54, s54, 2364672\n"
    "buffer_atomic_add v1, v2, s[40:43], s54 offen sc0\n"
    "s_waitcnt vmcnt(0)\n"
    "v_readfirstlane_b32 s48, v1\n"
    "s_nop 1\n"
    "v_mov_b32 v1, s48\n"
    "ds_write_b32 v3, v1\n"
    "s_nop 0\n"
    "hp_tk_%=:\n"
    "s_waitcnt lgkmcnt(0)\n"
    "s_barrier\n"
    "ds_read_b32 v1, v3\n"
    "s_waitcnt lgkmcnt(0)\n"
    "v_readfirstlane_b32 s20, v1\n"
    "s_nop 0\n"
    "s_cmp_lt_u32 s20, 256\n"
    "s_nop 2\n"
    "s_cbranch_scc1 hp_b1_%=\n"
    "s_cmp_lt_u32 s20, 288\n"
    "s_cbranch_scc1 hp_rd_%=\n"
    "s_cmp_lt_u32 s20, 352\n"
    "s_cbranch_scc1 hp_b2_%=\n"
    "s_add_u32 s21, s21, 1\n"
    "s_cmp_lt_u32 s21, 2\n"
    "s_cbranch_scc1 hp_layer_%=\n"
    "s_branch hp_end_%=\n"
    "hp_abort_%=:\n"
    "v_mov_b32 v4, 1\n"
    "v_mov_b32 v5, 1024\n"
    "buffer_store_dword v4, v5, s[40:43], s58 offen sc1\n"
    "hp_end_%=:\n"
    "s_endpgm\n"
    :: CHAIN_INPUTS : CHAIN_CLOBBERS);
    __builtin_unreachable();
}

__global__ void __launch_bounds__(256, 1) mega(char* ws, u16* out, const u16* Wq, const u16* Wk, const u16* Wv, const u16* Wo, Tune tn) {
    extern __shared__ __attribute__((aligned(16))) char smem[];
    LAS char* lds = (LAS char*)smem;
    rsrc_t r = __builtin_amdgcn_make_buffer_rsrc(ws, 0, WS_BYTES, 0x00020000);
    const int w = __builtin_amdgcn_readfirstlane(threadIdx.x >> 6);
    const unsigned xcc = xcc_id();
    if (threadIdx.x == 0) {
        const unsigned nearg = ld4(r, 0, OFF_FLAGS + FL_NEAR) & 1u;
        const unsigned pos = ((((xcc >> 2) ^ nearg) & 1u) << 2) | (xcc & 3u);
        unsigned role = pos * 32 + ((blockIdx.x >> 3) & 31);
        unsigned* claim = (unsigned*)(ws + OFF_FLAGS + FL_CLAIM);
#pragma unroll 1
        for (int n = 0; n < 256; ++n, role = (role + 1) & 255)
            if (__hip_atomic_exchange(claim + role, 1u, __ATOMIC_RELAXED, __HIP_MEMORY_SCOPE_AGENT) == 0u) break;
        *(LAS unsigned*)lds = role;
    }
    __syncthreads();
    const int role = __builtin_amdgcn_readfirstlane(*(LAS unsigned*)lds);
    __syncthreads();
    if ((role >> 2) == 7) {
        f4 p[2][10];
#pragma unroll
        for (int q = 0; q < 20; ++q) p[q / 10][q % 10] = __builtin_bit_cast(f4, ld16p(r, threadIdx.x * 16 + (q / 10) * 4096, OFF_CSP + (q % 10) * 8192));
#pragma unroll
        for (int y = 0; y < 2; ++y)
            st16(__builtin_bit_cast(u4, ((p[y][0] + p[y][1]) + (p[y][2] + p[y][3])) + ((p[y][4] + p[y][5]) + (p[y][6] + p[y][7])) + (p[y][8] + p[y][9])), r, threadIdx.x * 16 + y * 4096, OFF_CS);
        drain();
    }
    const int pos = role >> 5, idx = role & 31;
    const unsigned lo = (unsigned)(uintptr_t)ws, hi = (unsigned)((uintptr_t)ws >> 32) & 0xffffu;
    if (pos < 4) {
        if (idx < 8) role_head(lo, hi, pos * 8 + idx, w, xcc & 3, tn, Wq, Wk, 0);
        else if (idx < 28) role_col(lo, hi, pos * 20 + idx - 8, w, xcc & 3, tn, Wo, Wv, pos * 24 + idx - 8);
        else role_tok(lo, hi, pos * 4 + idx - 28, w, 0, tn, Wo, Wv, pos * 24 + idx - 8);
    } else {
        tn.p[0] = (int)(unsigned)(uintptr_t)out;
        tn.p[1] = (int)((unsigned)((uintptr_t)out >> 32) & 0xffffu);
        tn.p[2] = (int)plo(Wq);
        tn.p[3] = (int)phi(Wq);
        const int kind = idx >> 3;
        role_helper(lo, hi, (pos - 4) * 8 + (idx & 7), w, kind, tn, kind == 1 ? Wk : (kind == 2 ? Wv : Wo), Wv, 96 + (pos - 4) * 8 + (idx & 7));
    }
}

extern "C" void kernel_launch(void* const* d_in, const int* in_sizes, int n_in, void* d_out, int out_size, void* d_ws, size_t ws_size, hipStream_t stream) {
    const u16* x = (const u16*)d_in[0];
    const u16* mem0 = (const u16*)d_in[1];
    const u16 *Wq = (const u16*)d_in[2], *Wk = (const u16*)d_in[3], *Wv = (const u16*)d_in[4], *Wo = (const u16*)d_in[5];
    const u16 *wqkv = (const u16*)d_in[8], *wphi = (const u16*)d_in[9], *wo = (const u16*)d_in[10];
    char* ws = (char*)(((uintptr_t)d_ws + 0x1FFFFF) & ~(uintptr_t)0x1FFFFF);
    (void)hipFuncSetAttribute((const void*)mega, hipFuncAttributeMaxDynamicSharedMemorySize, LDS_BYTES);
    Tune tn = {{50,123,30,72,22,50,90,20}};
    prep<<<PB_END, 256, 0, stream>>>(x, mem0, wqkv, wphi, wo, ws);
    mega<<<256, 256, LDS_BYTES, stream>>>(ws, (u16*)d_out, Wq, Wk, Wv, Wo, tn);
}
```

```cpp
#include <hip/hip_runtime.h>
#include <hip/hip_bf16.h>
#include <stdint.h>

typedef __bf16 bf2 __attribute__((ext_vector_type(2)));
typedef __bf16 bf8 __attribute__((ext_vector_type(8)));
typedef __bf16 bf16v __attribute__((ext_vector_type(16)));
typedef float f4 __attribute__((ext_vector_type(4)));
typedef unsigned u2 __attribute__((ext_vector_type(2)));
typedef unsigned u4 __attribute__((ext_vector_type(4)));
typedef unsigned u8v __attribute__((ext_vector_type(8)));
typedef short s4 __attribute__((ext_vector_type(4)));
typedef unsigned short u16;
#define LAS __attribute__((address_space(3)))
#define DEV __device__ __forceinline__
typedef __amdgpu_buffer_rsrc_t rsrc_t;

constexpr unsigned SENT = 0xFFFFFFFFu;
constexpr int SC1 = 16;
constexpr unsigned SPIN_CAP = 1u << 16;

constexpr unsigned RING = 4u;
constexpr unsigned MCOPY = 4u;
constexpr unsigned PSLOT = 49152u;
constexpr unsigned MSLOT = 32768u;
constexpr unsigned OFF_PRING = 0u;
constexpr unsigned OFF_MRING = 786432u;
constexpr unsigned OFF_SCHED = 1310720u;
constexpr unsigned OFF_LNX = 1441792u;
constexpr unsigned RING_BYTES = 1572864u;
constexpr unsigned OFF_PROBE = 2031616u;
constexpr unsigned OFF_FLAGS = 2097152u;
constexpr unsigned FL_CLAIM = 0u;
constexpr unsigned FL_ABORT = 1024u;
constexpr unsigned FL_NEAR = 425856u;
constexpr unsigned FL_VX = 2048u;
constexpr unsigned FL_KM = 6144u;
constexpr unsigned FL_CK = 10240u;
constexpr unsigned FL_CU = 26880u;
constexpr unsigned FL_CX = 43520u;
constexpr unsigned FL_CT = 60416u;
constexpr unsigned FL_CD = 257024u;
constexpr unsigned FL_CB = 265216u;
constexpr unsigned FL_CL = 267264u;
constexpr unsigned FL_CE = 271360u;
constexpr unsigned FL_CR = 273408u;
constexpr unsigned FL_CF = 275456u;
constexpr unsigned FL_TK = 277504u;
constexpr unsigned FL_BG = 277760u;
constexpr unsigned FL_RP = 278784u;
constexpr unsigned FL_END = 411904u;
constexpr unsigned OFF_XF = 2523136u;
constexpr unsigned OFF_KX = 7766016u;
constexpr unsigned OFF_VX = 20348928u;
constexpr unsigned OFF_UX = 32964608u;
constexpr unsigned OFF_BT = 64421888u;
constexpr unsigned OFF_VO = 116850688u;
constexpr unsigned OFF_KM = 127336448u;
constexpr unsigned OFF_PTX = 130482176u;
constexpr unsigned OFF_PTM = 132579328u;
constexpr unsigned OFF_XA = 133103616u;
constexpr unsigned OFF_XB = 138346496u;
constexpr unsigned OFF_XR = 143589376u;
constexpr unsigned OFF_WB = 146210816u;
constexpr unsigned OFF_CS = 148668416u;
constexpr unsigned OFF_CSP = 148676608u;
constexpr unsigned OFF_WO2 = 148758528u;
constexpr unsigned OFF_PHI = 149577728u;
constexpr unsigned OFF_SLAB = 154820608u;
constexpr unsigned OFF_KVC = 162095104u;
constexpr unsigned OFF_PROF = 162160640u;
constexpr unsigned WS_BYTES = 164257792u;
constexpr unsigned OOB = 2147483632u;
constexpr unsigned NSEG = 64u;

DEV unsigned ringphys(unsigned o) { return ((o >> 13) << 14) | (o & 8191); }
DEV unsigned pk2(float a, float b) { bf2 v = {(__bf16)a, (__bf16)b}; return __builtin_bit_cast(unsigned, v); }
DEV float asf(unsigned v) { return __builtin_bit_cast(float, v); }
DEV float bf2f(u16 v) { return __builtin_bit_cast(float, (unsigned)v << 16); }
DEV f4 mfma(u4 a, u4 b, f4 c) { return __builtin_amdgcn_mfma_f32_16x16x32_bf16(__builtin_bit_cast(bf8, a), __builtin_bit_cast(bf8, b), c, 0, 0, 0); }
DEV f4 smfmac(u4 a, u8v b, f4 c, int idx) { return __builtin_amdgcn_smfmac_f32_16x16x64_bf16(__builtin_bit_cast(bf8, a), __builtin_bit_cast(bf16v, b), c, idx, 0, 0); }
DEV u4 ld16(rsrc_t r, unsigned v, unsigned s) { return __builtin_amdgcn_raw_buffer_load_b128(r, v, s, SC1); }
DEV u4 ld16p(rsrc_t r, unsigned v, unsigned s) { return __builtin_amdgcn_raw_buffer_load_b128(r, v, s, 0); }
DEV u2 ld8(rsrc_t r, unsigned v, unsigned s) { return __builtin_amdgcn_raw_buffer_load_b64(r, v, s, SC1); }
DEV unsigned ld4(rsrc_t r, unsigned v, unsigned s) { return __builtin_amdgcn_raw_buffer_load_b32(r, v, s, SC1); }
DEV void st16(u4 d, rsrc_t r, unsigned v, unsigned s) { __builtin_amdgcn_raw_buffer_store_b128(d, r, v, s, SC1); asm volatile("s_nop 1" :: "v"(d) : "memory"); }
DEV void st8(u2 d, rsrc_t r, unsigned v, unsigned s) { __builtin_amdgcn_raw_buffer_store_b64(d, r, v, s, SC1); }
DEV void st4(unsigned d, rsrc_t r, unsigned v, unsigned s) { __builtin_amdgcn_raw_buffer_store_b32(d, r, v, s, SC1); }
DEV u8v cat(u4 a, u4 b) { return __builtin_shufflevector(a, b, 0, 1, 2, 3, 4, 5, 6, 7); }
DEV unsigned umax3(unsigned a, unsigned b, unsigned c) { return max(max(a, b), c); }
DEV unsigned umax4(u4 v, unsigned m) { return umax3(umax3(v.x, v.y, v.z), v.w, m); }
DEV void memfence() { asm volatile("" ::: "memory"); __builtin_amdgcn_sched_barrier(0); }
DEV void drain() { asm volatile("s_waitcnt vmcnt(0)" ::: "memory"); }
DEV void bar() { asm volatile("s_waitcnt lgkmcnt(0)\n\ts_barrier" ::: "memory"); }
DEV unsigned xcc_id() { return (unsigned)__builtin_amdgcn_s_getreg(20 | (3 << 11)) & 0xfu; }
DEV float fold32(float x, float y) {
    auto r = __builtin_amdgcn_permlane32_swap(__builtin_bit_cast(unsigned, x), __builtin_bit_cast(unsigned, y), false, false);
    return asf(r[0]) + asf(r[1]);
}
DEV u2 trread(LAS char* p) { return __builtin_bit_cast(u2, __builtin_amdgcn_ds_read_tr16_b64_v4i16((LAS s4*)p)); }
template <int CTRL, int RM, int BM> DEV float dppf(float old, float v) {
    return __builtin_bit_cast(float, __builtin_amdgcn_update_dpp(__builtin_bit_cast(int, old), __builtin_bit_cast(int, v), CTRL, RM, BM, false));
}
DEV float xmax16(float v) { auto r = __builtin_amdgcn_permlane16_swap(__builtin_bit_cast(unsigned, v), __builtin_bit_cast(unsigned, v), false, false); return fmaxf(asf(r[0]), asf(r[1])); }
DEV float xmax32(float v) { auto r = __builtin_amdgcn_permlane32_swap(__builtin_bit_cast(unsigned, v), __builtin_bit_cast(unsigned, v), false, false); return fmaxf(asf(r[0]), asf(r[1])); }
DEV float xadd16(float v) { auto r = __builtin_amdgcn_permlane16_swap(__builtin_bit_cast(unsigned, v), __builtin_bit_cast(unsigned, v), false, false); return asf(r[0]) + asf(r[1]); }
DEV float xadd32(float v) { auto r = __builtin_amdgcn_permlane32_swap(__builtin_bit_cast(unsigned, v), __builtin_bit_cast(unsigned, v), false, false); return asf(r[0]) + asf(r[1]); }

DEV int dperm(int g, int j) { return 16 * (g >> 1) + 8 * ((j >> 1) & 1) + 4 * (g & 1) + 2 * (j >> 2) + (j & 1); }

DEV void prep_ln(const u16* __restrict__ x, char* ws, LAS char* lds, int b, int w, int lane) {
    const int m = w * 4 + (lane >> 4), k = lane & 15;
    float v[5][8], sum = 0.f, var = 0.f;
#pragma unroll
    for (int j = 0; j < 5; ++j) {
        const u4 q = *(const u4*)(x + (size_t)(b * 16 + m) * 640 + (j * 16 + k) * 8);
#pragma unroll
        for (int e = 0; e < 4; ++e) { const unsigned d = q[e]; v[j][2 * e] = asf(d << 16); v[j][2 * e + 1] = asf(d & 0xffff0000u); sum += v[j][2 * e] + v[j][2 * e + 1]; }
    }
    for (int o = 8; o; o >>= 1) sum += __shfl_xor(sum, o);
    const float mean = sum * (1.f / 640.f);
#pragma unroll
    for (int j = 0; j < 5; ++j)
#pragma unroll
        for (int e = 0; e < 8; ++e) { const float d = v[j][e] - mean; var += d * d; }
    for (int o = 8; o; o >>= 1) var += __shfl_xor(var, o);
    const float rs = rsqrtf(var * (1.f / 640.f) + 1e-5f);
#pragma unroll
    for (int j = 0; j < 5; ++j) {
        f4* xa = (f4*)(ws + OFF_XA + ((size_t)(b * 160 + (j * 16 + k) * 2) * 16 + m) * 16);
        xa[0] = (f4){v[j][0], v[j][1], v[j][2], v[j][3]}; xa[16] = (f4){v[j][4], v[j][5], v[j][6], v[j][7]};
        *(LAS u4*)(lds + m * 1296 + (j * 16 + k) * 16) = (u4){pk2((v[j][0] - mean) * rs, (v[j][1] - mean) * rs), pk2((v[j][2] - mean) * rs, (v[j][3] - mean) * rs),
                                                              pk2((v[j][4] - mean) * rs, (v[j][5] - mean) * rs), pk2((v[j][6] - mean) * rs, (v[j][7] - mean) * rs)};
    }
    bar();
    const int g = lane >> 4;
#pragma unroll
    for (int q = 0; q < 5; ++q) {
        const int kk = w * 5 + q;
        LAS char* row = lds + k * 1296 + (32 * kk + 16 * (g >> 1) + 4 * (g & 1)) * 2;
        const u2 a = *(LAS u2*)row, c = *(LAS u2*)(row + 16);
        *(u4*)(ws + OFF_XF + (size_t)(b * 20 + kk) * 1024 + lane * 16) = (u4){a.x, c.x, a.y, c.y};
    }
}
DEV void prep_wb(const u16* __restrict__ wqkv, const u16* __restrict__ wphi, char* ws, LAS char* lds, int b, int w, int lane) {
    const int q = b % 10, part = (b / 10) % 3, hg = b / 30, h = 4 * hg + w;
    LAS float* ph = (LAS float*)(lds + 16384) + w * 400;
    for (int e = lane; e < 400; e += 64) ph[e] = part < 2 ? bf2f(wphi[h * 400 + e]) : (e / 20 == e % 20 ? 1.f : 0.f);
    const u16* src = wqkv + (size_t)(64 * q + lane) * 1920 + part * 640 + h * 20;
    float in[20], o[20];
#pragma unroll
    for (int d = 0; d < 5; ++d) { const u2 t = *(const u2*)(src + 4 * d); in[4 * d] = asf(t.x << 16); in[4 * d + 1] = asf(t.x & 0xffff0000u); in[4 * d + 2] = asf(t.y << 16); in[4 * d + 3] = asf(t.y & 0xffff0000u); }
#pragma unroll
    for (int f = 0; f < 20; ++f) o[f] = 0.f;
#pragma unroll
    for (int d = 0; d < 20; ++d)
#pragma unroll
        for (int f = 0; f < 20; f += 4) { const f4 p = *(LAS f4*)(ph + d * 20 + f); o[f] += in[d] * p[0]; o[f + 1] += in[d] * p[1]; o[f + 2] += in[d] * p[2]; o[f + 3] += in[d] * p[3]; }
#pragma unroll
    for (int f = 0; f < 20; ++f) *(LAS u16*)(lds + (((f >> 2) * 16 + w * 4 + (f & 3)) * 72 + lane) * 2) = (u16)pk2(o[f], 0.f);
    bar();
    const int g = lane >> 4, m = lane & 15;
    for (int t = w; t < 5; t += 4) {
        float cs = 0.f;
#pragma unroll
        for (int u = 0; u < 2; ++u) {
            LAS char* row = lds + ((t * 16 + m) * 72 + 32 * u + 4 * g) * 2;
            const u2 a = *(LAS u2*)row, c = *(LAS u2*)(row + 32);
            cs += (asf(a.x << 16) + asf(a.x & 0xffff0000u)) + (asf(a.y << 16) + asf(a.y & 0xffff0000u)) + (asf(c.x << 16) + asf(c.x & 0xffff0000u)) + (asf(c.y << 16) + asf(c.y & 0xffff0000u));
            *(u4*)(ws + OFF_WB + (size_t)((hg * 20 + 2 * q + u) * 15 + part * 5 + t) * 1024 + lane * 16) = (u4){a.x, a.y, c.x, c.y};
        }
        cs = xadd32(xadd16(cs));
        if (lane < 16) *(float*)(ws + OFF_CSP + q * 8192 + ((hg * 15 + part * 5 + t) * 16 + lane) * 4) = cs;
    }
}
constexpr int PB_INIT = 1, PB_LN = PB_INIT + 71, PB_WB = PB_LN + 128, PB_WO = PB_WB + 240, PB_END = PB_WO + 200;
__global__ void __launch_bounds__(256, 4) prep(const u16* __restrict__ x, const u16* __restrict__ mem0, const u16* __restrict__ wqkv, const u16* __restrict__ wphi, const u16* __restrict__ wo, char* ws) {
    __shared__ __attribute__((aligned(16))) char smem[22784];
    LAS char* lds = (LAS char*)smem;
    const int tid = threadIdx.x, w = __builtin_amdgcn_readfirstlane(tid >> 6), lane = tid & 63, b = blockIdx.x;
    rsrc_t r = __builtin_amdgcn_make_buffer_rsrc(ws, 0, WS_BYTES, 0x00020000);
    if (b < PB_INIT) {
        if (w) return;
        unsigned best[2] = {1u << 30, 1u << 30};
        for (int rep = 0; rep < 6; ++rep) for (int c = 0; c < 2; ++c) {
            drain();
            const unsigned long long t0 = __builtin_amdgcn_s_memtime();
            asm volatile("s_waitcnt lgkmcnt(0)" ::: "memory");
            st16((u4){0, 0, 0, 0}, r, lane < 8 ? lane * 16 : OOB, OFF_PROBE + c * 8192 + (rep & 1) * 4096);
            drain();
            best[c] = min(best[c], (unsigned)(__builtin_amdgcn_s_memtime() - t0));
        }
        st4((xcc_id() >> 2) ^ (best[0] > best[1] ? 1u : 0u), r, lane == 0 ? 0 : OOB, OFF_FLAGS + FL_NEAR);
    } else if (b < PB_LN) {
        const int q = b - PB_INIT, n = PB_LN - PB_INIT;
        for (unsigned lo = (q * 256 + tid) * 16; lo < RING_BYTES / 2; lo += n * 4096) {
            const unsigned o = ringphys(lo), p = (lo - OFF_MRING / 2) % (MSLOT / 2);
            u4 v = {SENT, SENT, SENT, SENT};
            if (o >= OFF_MRING && o < OFF_MRING + MCOPY * MSLOT && p < 10240) v = *(const u4*)(mem0 + ((p >> 4) & 7) * 640 + (p >> 7) * 8);
            st16(v, r, o, 0);
        }
        for (unsigned o = (q * 256 + tid) * 16; o < FL_END; o += n * 4096) st16((u4){(o >= FL_CX && o < FL_CX + 64 * 128) ? 2u : 0u, 0, 0, 0}, r, o, OFF_FLAGS);
    } else if (b < PB_WB) prep_ln(x, ws, lds, b - PB_LN, w, lane);
    else if (b < PB_WO) prep_wb(wqkv, wphi, ws, lds, b - PB_WB, w, lane);
    else {
        const int f = (b - PB_WO) * 4 + w, ct = (f / 200) * 10 + f % 10, kq = (f % 200) / 10, g = lane >> 4, m = lane & 15;
        const int c = 32 * (ct >> 1) + dperm(m >> 2, 4 * (ct & 1) + (m & 3));
        unsigned v[8];
#pragma unroll
        for (int j = 0; j < 8; ++j) { const int idx = 8 * (kq % 5) + j; v[j] = wo[(size_t)((4 * (2 * (kq / 5) + idx / 20) + g) * 20 + idx % 20) * 640 + c]; }
        *(u4*)(ws + OFF_WO2 + (size_t)f * 1024 + lane * 16) = (u4){v[0] | v[1] << 16, v[2] | v[3] << 16, v[4] | v[5] << 16, v[6] | v[7] << 16};
    }
}

constexpr int LDS_BYTES = 152 * 1024 + 64;
struct Tune { int p[8]; };
DEV unsigned plo(const void* p) { return (unsigned)(uintptr_t)p; }
DEV unsigned phi(const void* p) { return (unsigned)((uintptr_t)p >> 32) & 0xffffu; }
#define CHAIN_CLOBBERS "v0", "v1", "v2", "v3", "v4", "v5", "v6", "v7", "v8", "v9", "v10", "v11", "v12", "v13", "v14", "v15", "v16", "v17", "v18", "v19", "v20", "v21", "v22", "v23", "v24", "v25", "v26", "v27", "v28", "v29", "v30", "v31", "v32", "v33", "v34", "v35", "v36", "v37", "v38", "v39", "v40", "v41", "v42", "v43", "v44", "v45", "v46", "v47", "v48", "v49", "v50", "v51", "v52", "v53", "v54", "v55", "v56", "v57", "v58", "v59", "v60", "v61", "v62", "v63", "v64", "v65", "v66", "v67", "v68", "v69", "v70", "v71", "v72", "v73", "v74", "v75", "v76", "v77", "v78", "v79", "v80", "v81", "v82", "v83", "v84", "v85", "v86", "v87", "v88", "v89", "v90", "v91", "v92", "v93", "v94", "v95", "v96", "v97", "v98", "v99", "v100", "v101", "v102", "v103", "v104", "v105", "v106", "v107", "v108", "v109", "v110", "v111", "v112", "v113", "v114", "v115", "v116", "v117", "v118", "v119", "v120", "v121", "v122", "v123", "v124", "v125", "v126", "v127", "v128", "v129", "v130", "v131", "v132", "v133", "v134", "v135", "v136", "v137", "v138", "v139", "v140", "v141", "v142", "v143", "v144", "v145", "v146", "v147", "v148", "v149", "v150", "v151", "v152", "v153", "v154", "v155", "v156", "v157", "v158", "v159", "v160", "v161", "v162", "v163", "v164", "v165", "v166", "v167", "v168", "v169", "v170", "v171", "v172", "v173", "v174", "v175", "v176", "v177", "v178", "v179", "v180", "v181", "v182", "v183", "v184", "v185", "v186", "v187", "v188", "v189", "v190", "v191", "v192", "v193", "v194", "v195", "v196", "v197", "v198", "v199", "v200", "v201", "v202", "v203", "v204", "v205", "v206", "v207", "v208", "v209", "v210", "v211", "v212", "v213", "v214", "v215", "v216", "v217", "v218", "v219", "v220", "v221", "v222", "v223", "v224", "v225", "v226", "v227", "v228", "v229", "v230", "v231", "v232", "v233", "v234", "v235", "v236", "v237", "v238", "v239", "v240", "v241", "v242", "v243", "v244", "v245", "v246", "v247", "v248", "v249", "v250", "v251", "v252", "v253", "v254", "v255", "a0", "a1", "a2", "a3", "a4", "a5", "a6", "a7", "a8", "a9", "a10", "a11", "a12", "a13", "a14", "a15", "a16", "a17", "a18", "a19", "a20", "a21", "a22", "a23", "a24", "a25", "a26", "a27", "a28", "a29", "a30", "a31", "a32", "a33", "a34", "a35", "a36", "a37", "a38", "a39", "a40", "a41", "a42", "a43", "a44", "a45", "a46", "a47", "a48", "a49", "a50", "a51", "a52", "a53", "a54", "a55", "a56", "a57", "a58", "a59", "a60", "a61", "a62", "a63", "a64", "a65", "a66", "a67", "a68", "a69", "a70", "a71", "a72", "a73", "a74", "a75", "a76", "a77", "a78", "a79", "a80", "a81", "a82", "a83", "a84", "a85", "a86", "a87", "a88", "a89", "a90", "a91", "a92", "a93", "a94", "a95", "a96", "a97", "a98", "a99", "a100", "a101", "a102", "a103", "a104", "a105", "a106", "a107", "a108", "a109", "a110", "a111", "a112", "a113", "a114", "a115", "a116", "a117", "a118", "a119", "a120", "a121", "a122", "a123", "a124", "a125", "a126", "a127", "a128", "a129", "a130", "a131", "a132", "a133", "a134", "a135", "a136", "a137", "a138", "a139", "a140", "a141", "a142", "a143", "a144", "a145", "a146", "a147", "a148", "a149", "a150", "a151", "a152", "a153", "a154", "a155", "a156", "a157", "a158", "a159", "a160", "a161", "a162", "a163", "a164", "a165", "a166", "a167", "a168", "a169", "a170", "a171", "a172", "a173", "a174", "a175", "a176", "a177", "a178", "a179", "a180", "a181", "a182", "a183", "a184", "a185", "a186", "a187", "a188", "a189", "a190", "a191", "a192", "a193", "a194", "a195", "a196", "a197", "a198", "a199", "a200", "a201", "a202", "a203", "a204", "a205", "a206", "a207", "a208", "a209", "a210", "a211", "a212", "a213", "a214", "a215", "a216", "a217", "a218", "a219", "a220", "a221", "a222", "a223", "a224", "a225", "a226", "a227", "a228", "a229", "a230", "a231", "a232", "a233", "a234", "a235", "a236", "a237", "a238", "a239", "a240", "a241", "a242", "a243", "a244", "a245", "a246", "a247", "a248", "a249", "a250", "a251", "a252", "a253", "a254", "a255", "s40", "s41", "s42", "s43", "s44", "s45", "s46", "s47", "s66", "s67", "s68", "s69", "s70", "s71", "s72", "s73", "s76", "s77", "s78", "s79", "s80", "s81", "s82", "s83", "s84", "s99", "vcc", "scc", "memory"
#define SRD_INPUTS "s"(lo), "s"(hi), "s"(WS_BYTES), "s"(0x00020000u), "s"(id), "s"(w), "s"(aux)
#define CHAIN_INPUTS SRD_INPUTS, "s"(tn.p[0]), "s"(tn.p[1]), "s"(tn.p[2]), "s"(tn.p[3]), "s"(tn.p[4]), "s"(tn.p[5]), "s"(tn.p[6]), "s"(tn.p[7]), "s"(plo(wa)), "s"(phi(wa)), "s"(plo(wb)), "s"(phi(wb)), "s"(bid)
DEV void role_head(unsigned lo, unsigned hi, int id, int w, int aux, const Tune& tn, const void* wa, const void* wb, int bid) {
    asm volatile(
R"(s_mov_b32 s40, %0
s_mov_b32 s41, %1
s_mov_b32 s42, %2
s_mov_b32 s43, %3
s_mov_b32 s44, %4
s_mov_b32 s45, %5
s_mov_b32 s46, %6
s_mov_b32 s66, %7
s_mov_b32 s67, %8
s_mov_b32 s68, %9
s_mov_b32 s69, %10
s_mov_b32 s70, %11
s_mov_b32 s71, %12
s_mov_b32 s72, %13
s_mov_b32 s73, %14
s_mov_b32 s76, %15
s_mov_b32 s77, %16
s_mov_b32 s78, 7864320
s_mov_b32 s79, 0x20000
s_mov_b32 s80, %17
s_mov_b32 s81, %18
s_mov_b32 s82, 7864320
s_mov_b32 s83, 0x20000
s_mov_b32 s84, %19
s_mov_b64 exec, -1
v_mbcnt_lo_u32_b32 v15, -1, 0
v_mbcnt_hi_u32_b32 v15, -1, v15
v_lshlrev_b32 v0, 4, v15
s_mov_b32 s58, 2097152
s_mov_b32 s92, 0
s_lshl_b32 s48, s45, 15
v_add_u32 v2, s48, v0
s_mov_b32 s20, 0
s_mov_b32 s21, 1024
s_mov_b32 s22, 2048
s_mov_b32 s23, 3072
s_mov_b32 s24, 4096
s_mov_b32 s25, 5120
s_mov_b32 s26, 6144
s_mov_b32 s27, 7168
s_mov_b32 s28, 16384
s_mov_b32 s29, 17408
v_add_u32 v150, 0, v15
v_mul_u32_u24 v144, 2731, v150
v_lshrrev_b32 v144, 16, v144
v_mul_u32_u24 v144, 11904, v144
v_lshl_add_u32 v144, v150, 4, v144
v_add_u32 v150, 64, v15
v_mul_u32_u24 v145, 2731, v150
v_lshrrev_b32 v145, 16, v145
v_mul_u32_u24 v145, 11904, v145
v_lshl_add_u32 v145, v150, 4, v145
v_add_u32 v150, 128, v15
v_mul_u32_u24 v146, 2731, v150
v_lshrrev_b32 v146, 16, v146
v_mul_u32_u24 v146, 11904, v146
v_lshl_add_u32 v146, v150, 4, v146
v_bfe_u32 v147, v15, 2, 1
v_and_b32 v148, 8, v15
v_add_u32 v147, v147, v148
v_bfe_u32 v148, v15, 4, 1
v_lshl_add_u32 v147, v148, 2, v147
v_lshrrev_b32 v148, 5, v15
v_lshl_add_u32 v147, v148, 4, v147
v_mul_u32_u24 v147, 384, v147
v_and_b32 v148, 3, v15
v_lshl_add_u32 v147, v148, 3, v147
s_mul_i32 s48, s45, 96
v_add_u32 v147, s48, v147
v_add_u32 v148, 49152, v147
v_add_u32 v149, 98304, v147
s_mul_i32 s50, s45, 196608
s_mul_i32 s48, s44, 384
s_add_u32 s50, s50, s48
s_mul_i32 s51, s45, 6144
s_add_u32 m0, s51, 0
s_add_u32 s49, s50, 0
buffer_load_dwordx4 v144, s[76:79], s49 offen lds
s_add_u32 m0, s51, 1024
s_add_u32 s49, s50, 0
buffer_load_dwordx4 v145, s[76:79], s49 offen lds
s_add_u32 m0, s51, 2048
s_add_u32 s49, s50, 0
buffer_load_dwordx4 v146, s[76:79], s49 offen lds
s_add_u32 m0, s51, 3072
s_add_u32 s49, s50, 98304
buffer_load_dwordx4 v144, s[76:79], s49 offen lds
s_add_u32 m0, s51, 4096
s_add_u32 s49, s50, 98304
buffer_load_dwordx4 v145, s[76:79], s49 offen lds
s_add_u32 m0, s51, 5120
s_add_u32 s49, s50, 98304
buffer_load_dwordx4 v146, s[76:79], s49 offen lds
s_add_u32 m0, s51, 24576
s_add_u32 s49, s50, 0
buffer_load_dwordx4 v144, s[80:83], s49 offen lds
s_add_u32 m0, s51, 25600
s_add_u32 s49, s50, 0
buffer_load_dwordx4 v145, s[80:83], s49 offen lds
s_add_u32 m0, s51, 26624
s_add_u32 s49, s50, 0
buffer_load_dwordx4 v146, s[80:83], s49 offen lds
s_add_u32 m0, s51, 27648
s_add_u32 s49, s50, 98304
buffer_load_dwordx4 v144, s[80:83], s49 offen lds
s_add_u32 m0, s51, 28672
s_add_u32 s49, s50, 98304
buffer_load_dwordx4 v145, s[80:83], s49 offen lds
s_add_u32 m0, s51, 29696
s_add_u32 s49, s50, 98304
buffer_load_dwordx4 v146, s[80:83], s49 offen lds
s_add_u32 m0, s51, 49152
s_add_u32 s49, s50, 786432
buffer_load_dwordx4 v144, s[76:79], s49 offen lds
s_add_u32 m0, s51, 50176
s_add_u32 s49, s50, 786432
buffer_load_dwordx4 v145, s[76:79], s49 offen lds
s_add_u32 m0, s51, 51200
s_add_u32 s49, s50, 786432
buffer_load_dwordx4 v146, s[76:79], s49 offen lds
s_add_u32 m0, s51, 52224
s_add_u32 s49, s50, 884736
buffer_load_dwordx4 v144, s[76:79], s49 offen lds
s_add_u32 m0, s51, 53248
s_add_u32 s49, s50, 884736
buffer_load_dwordx4 v145, s[76:79], s49 offen lds
s_add_u32 m0, s51, 54272
s_add_u32 s49, s50, 884736
buffer_load_dwordx4 v146, s[76:79], s49 offen lds
s_add_u32 m0, s51, 73728
s_add_u32 s49, s50, 786432
buffer_load_dwordx4 v144, s[80:83], s49 offen lds
s_add_u32 m0, s51, 74752
s_add_u32 s49, s50, 786432
buffer_load_dwordx4 v145, s[80:83], s49 offen lds
s_add_u32 m0, s51, 75776
s_add_u32 s49, s50, 786432
buffer_load_dwordx4 v146, s[80:83], s49 offen lds
s_add_u32 m0, s51, 76800
s_add_u32 s49, s50, 884736
buffer_load_dwordx4 v144, s[80:83], s49 offen lds
s_add_u32 m0, s51, 77824
s_add_u32 s49, s50, 884736
buffer_load_dwordx4 v145, s[80:83], s49 offen lds
s_add_u32 m0, s51, 78848
s_add_u32 s49, s50, 884736
buffer_load_dwordx4 v146, s[80:83], s49 offen lds
s_add_u32 m0, s51, 98304
s_add_u32 s49, s50, 1572864
buffer_load_dwordx4 v144, s[76:79], s49 offen lds
s_add_u32 m0, s51, 99328
s_add_u32 s49, s50, 1572864
buffer_load_dwordx4 v145, s[76:79], s49 offen lds
s_add_u32 m0, s51, 100352
s_add_u32 s49, s50, 1572864
buffer_load_dwordx4 v146, s[76:79], s49 offen lds
s_add_u32 m0, s51, 101376
s_add_u32 s49, s50, 1671168
buffer_load_dwordx4 v144, s[76:79], s49 offen lds
s_add_u32 m0, s51, 102400
s_add_u32 s49, s50, 1671168
buffer_load_dwordx4 v145, s[76:79], s49 offen lds
s_add_u32 m0, s51, 103424
s_add_u32 s49, s50, 1671168
buffer_load_dwordx4 v146, s[76:79], s49 offen lds
s_waitcnt vmcnt(24) lgkmcnt(0)
s_barrier
s_add_u32 m0, s51, 122880
s_add_u32 s49, s50, 1572864
buffer_load_dwordx4 v144, s[80:83], s49 offen lds
s_add_u32 m0, s51, 123904
s_add_u32 s49, s50, 1572864
buffer_load_dwordx4 v145, s[80:83], s49 offen lds
s_add_u32 m0, s51, 124928
s_add_u32 s49, s50, 1572864
buffer_load_dwordx4 v146, s[80:83], s49 offen lds
s_add_u32 m0, s51, 125952
s_add_u32 s49, s50, 1671168
buffer_load_dwordx4 v144, s[80:83], s49 offen lds
s_add_u32 m0, s51, 126976
s_add_u32 s49, s50, 1671168
buffer_load_dwordx4 v145, s[80:83], s49 offen lds
s_add_u32 m0, s51, 128000
s_add_u32 s49, s50, 1671168
buffer_load_dwordx4 v146, s[80:83], s49 offen lds
ds_read_b64_tr_b16 a[0:1], v147 offset:0
ds_read_b64_tr_b16 a[2:3], v147 offset:768
ds_read_b64_tr_b16 a[4:5], v147 offset:12288
ds_read_b64_tr_b16 a[6:7], v147 offset:13056
ds_read_b64_tr_b16 a[8:9], v147 offset:32
ds_read_b64_tr_b16 a[10:11], v147 offset:800
ds_read_b64_tr_b16 a[12:13], v147 offset:12320
ds_read_b64_tr_b16 a[14:15], v147 offset:13088
ds_read_b64_tr_b16 a[216:217], v147 offset:64
ds_read_b64_tr_b16 a[218:219], v147 offset:832
ds_read_b64_tr_b16 a[220:221], v147 offset:12352
ds_read_b64_tr_b16 a[222:223], v147 offset:13120
s_waitcnt vmcnt(24) lgkmcnt(0)
s_barrier
s_add_u32 m0, s51, 0
)"
R"(s_add_u32 s49, s50, 2359296
buffer_load_dwordx4 v144, s[76:79], s49 offen lds
s_add_u32 m0, s51, 1024
s_add_u32 s49, s50, 2359296
buffer_load_dwordx4 v145, s[76:79], s49 offen lds
s_add_u32 m0, s51, 2048
s_add_u32 s49, s50, 2359296
buffer_load_dwordx4 v146, s[76:79], s49 offen lds
s_add_u32 m0, s51, 3072
s_add_u32 s49, s50, 2457600
buffer_load_dwordx4 v144, s[76:79], s49 offen lds
s_add_u32 m0, s51, 4096
s_add_u32 s49, s50, 2457600
buffer_load_dwordx4 v145, s[76:79], s49 offen lds
s_add_u32 m0, s51, 5120
s_add_u32 s49, s50, 2457600
buffer_load_dwordx4 v146, s[76:79], s49 offen lds
ds_read_b64_tr_b16 a[16:17], v147 offset:24576
ds_read_b64_tr_b16 a[18:19], v147 offset:25344
ds_read_b64_tr_b16 a[20:21], v147 offset:36864
ds_read_b64_tr_b16 a[22:23], v147 offset:37632
ds_read_b64_tr_b16 v[16:17], v147 offset:24608
ds_read_b64_tr_b16 v[18:19], v147 offset:25376
ds_read_b64_tr_b16 v[20:21], v147 offset:36896
ds_read_b64_tr_b16 v[22:23], v147 offset:37664
ds_read_b64_tr_b16 v[120:121], v147 offset:24640
ds_read_b64_tr_b16 v[122:123], v147 offset:25408
ds_read_b64_tr_b16 v[124:125], v147 offset:36928
ds_read_b64_tr_b16 v[126:127], v147 offset:37696
s_waitcnt vmcnt(24) lgkmcnt(0)
s_barrier
s_add_u32 m0, s51, 24576
s_add_u32 s49, s50, 2359296
buffer_load_dwordx4 v144, s[80:83], s49 offen lds
s_add_u32 m0, s51, 25600
s_add_u32 s49, s50, 2359296
buffer_load_dwordx4 v145, s[80:83], s49 offen lds
s_add_u32 m0, s51, 26624
s_add_u32 s49, s50, 2359296
buffer_load_dwordx4 v146, s[80:83], s49 offen lds
s_add_u32 m0, s51, 27648
s_add_u32 s49, s50, 2457600
buffer_load_dwordx4 v144, s[80:83], s49 offen lds
s_add_u32 m0, s51, 28672
s_add_u32 s49, s50, 2457600
buffer_load_dwordx4 v145, s[80:83], s49 offen lds
s_add_u32 m0, s51, 29696
s_add_u32 s49, s50, 2457600
buffer_load_dwordx4 v146, s[80:83], s49 offen lds
ds_read_b64_tr_b16 a[24:25], v148 offset:0
ds_read_b64_tr_b16 a[26:27], v148 offset:768
ds_read_b64_tr_b16 a[28:29], v148 offset:12288
ds_read_b64_tr_b16 a[30:31], v148 offset:13056
ds_read_b64_tr_b16 v[32:33], v148 offset:32
ds_read_b64_tr_b16 v[34:35], v148 offset:800
ds_read_b64_tr_b16 v[36:37], v148 offset:12320
ds_read_b64_tr_b16 v[38:39], v148 offset:13088
ds_read_b64_tr_b16 a[224:225], v148 offset:64
ds_read_b64_tr_b16 a[226:227], v148 offset:832
ds_read_b64_tr_b16 a[228:229], v148 offset:12352
ds_read_b64_tr_b16 a[230:231], v148 offset:13120
s_waitcnt vmcnt(24) lgkmcnt(0)
s_barrier
s_add_u32 m0, s51, 49152
s_add_u32 s49, s50, 3145728
buffer_load_dwordx4 v144, s[76:79], s49 offen lds
s_add_u32 m0, s51, 50176
s_add_u32 s49, s50, 3145728
buffer_load_dwordx4 v145, s[76:79], s49 offen lds
s_add_u32 m0, s51, 51200
s_add_u32 s49, s50, 3145728
buffer_load_dwordx4 v146, s[76:79], s49 offen lds
s_add_u32 m0, s51, 52224
s_add_u32 s49, s50, 3244032
buffer_load_dwordx4 v144, s[76:79], s49 offen lds
s_add_u32 m0, s51, 53248
s_add_u32 s49, s50, 3244032
buffer_load_dwordx4 v145, s[76:79], s49 offen lds
s_add_u32 m0, s51, 54272
s_add_u32 s49, s50, 3244032
buffer_load_dwordx4 v146, s[76:79], s49 offen lds
ds_read_b64_tr_b16 a[32:33], v148 offset:24576
ds_read_b64_tr_b16 a[34:35], v148 offset:25344
ds_read_b64_tr_b16 a[36:37], v148 offset:36864
ds_read_b64_tr_b16 a[38:39], v148 offset:37632
ds_read_b64_tr_b16 v[24:25], v148 offset:24608
ds_read_b64_tr_b16 v[26:27], v148 offset:25376
ds_read_b64_tr_b16 v[28:29], v148 offset:36896
ds_read_b64_tr_b16 v[30:31], v148 offset:37664
ds_read_b64_tr_b16 v[128:129], v148 offset:24640
ds_read_b64_tr_b16 v[130:131], v148 offset:25408
ds_read_b64_tr_b16 v[132:133], v148 offset:36928
ds_read_b64_tr_b16 v[134:135], v148 offset:37696
s_waitcnt vmcnt(24) lgkmcnt(0)
s_barrier
s_add_u32 m0, s51, 73728
s_add_u32 s49, s50, 3145728
buffer_load_dwordx4 v144, s[80:83], s49 offen lds
s_add_u32 m0, s51, 74752
s_add_u32 s49, s50, 3145728
buffer_load_dwordx4 v145, s[80:83], s49 offen lds
s_add_u32 m0, s51, 75776
s_add_u32 s49, s50, 3145728
buffer_load_dwordx4 v146, s[80:83], s49 offen lds
s_add_u32 m0, s51, 76800
s_add_u32 s49, s50, 3244032
buffer_load_dwordx4 v144, s[80:83], s49 offen lds
s_add_u32 m0, s51, 77824
s_add_u32 s49, s50, 3244032
buffer_load_dwordx4 v145, s[80:83], s49 offen lds
s_add_u32 m0, s51, 78848
s_add_u32 s49, s50, 3244032
buffer_load_dwordx4 v146, s[80:83], s49 offen lds
ds_read_b64_tr_b16 a[40:41], v149 offset:0
ds_read_b64_tr_b16 a[42:43], v149 offset:768
ds_read_b64_tr_b16 a[44:45], v149 offset:12288
ds_read_b64_tr_b16 a[46:47], v149 offset:13056
ds_read_b64_tr_b16 a[48:49], v149 offset:32
ds_read_b64_tr_b16 a[50:51], v149 offset:800
ds_read_b64_tr_b16 a[52:53], v149 offset:12320
ds_read_b64_tr_b16 a[54:55], v149 offset:13088
ds_read_b64_tr_b16 a[232:233], v149 offset:64
ds_read_b64_tr_b16 a[234:235], v149 offset:832
ds_read_b64_tr_b16 a[236:237], v149 offset:12352
ds_read_b64_tr_b16 a[238:239], v149 offset:13120
s_waitcnt vmcnt(24) lgkmcnt(0)
s_barrier
s_add_u32 m0, s51, 98304
s_add_u32 s49, s50, 3932160
buffer_load_dwordx4 v144, s[76:79], s49 offen lds
s_add_u32 m0, s51, 99328
s_add_u32 s49, s50, 3932160
buffer_load_dwordx4 v145, s[76:79], s49 offen lds
s_add_u32 m0, s51, 100352
s_add_u32 s49, s50, 3932160
buffer_load_dwordx4 v146, s[76:79], s49 offen lds
s_add_u32 m0, s51, 101376
s_add_u32 s49, s50, 4030464
buffer_load_dwordx4 v144, s[76:79], s49 offen lds
s_add_u32 m0, s51, 102400
s_add_u32 s49, s50, 4030464
buffer_load_dwordx4 v145, s[76:79], s49 offen lds
s_add_u32 m0, s51, 103424
s_add_u32 s49, s50, 4030464
buffer_load_dwordx4 v146, s[76:79], s49 offen lds
ds_read_b64_tr_b16 a[56:57], v149 offset:24576
ds_read_b64_tr_b16 a[58:59], v149 offset:25344
ds_read_b64_tr_b16 a[60:61], v149 offset:36864
ds_read_b64_tr_b16 a[62:63], v149 offset:37632
ds_read_b64_tr_b16 v[40:41], v149 offset:24608
ds_read_b64_tr_b16 v[42:43], v149 offset:25376
ds_read_b64_tr_b16 v[44:45], v149 offset:36896
ds_read_b64_tr_b16 v[46:47], v149 offset:37664
ds_read_b64_tr_b16 v[136:137], v149 offset:24640
ds_read_b64_tr_b16 v[138:139], v149 offset:25408
ds_read_b64_tr_b16 v[140:141], v149 offset:36928
ds_read_b64_tr_b16 v[142:143], v149 offset:37696
s_waitcnt vmcnt(24) lgkmcnt(0)
s_barrier
s_add_u32 m0, s51, 122880
s_add_u32 s49, s50, 3932160
buffer_load_dwordx4 v144, s[80:83], s49 offen lds
s_add_u32 m0, s51, 123904
s_add_u32 s49, s50, 3932160
buffer_load_dwordx4 v145, s[80:83], s49 offen lds
s_add_u32 m0, s51, 124928
s_add_u32 s49, s50, 3932160
buffer_load_dwordx4 v146, s[80:83], s49 offen lds
s_add_u32 m0, s51, 125952
s_add_u32 s49, s50, 4030464
buffer_load_dwordx4 v144, s[80:83], s49 offen lds
s_add_u32 m0, s51, 126976
s_add_u32 s49, s50, 4030464
buffer_load_dwordx4 v145, s[80:83], s49 offen lds
s_add_u32 m0, s51, 128000
s_add_u32 s49, s50, 4030464
buffer_load_dwordx4 v146, s[80:83], s49 offen lds
ds_read_b64_tr_b16 a[64:65], v147 offset:0
ds_read_b64_tr_b16 a[66:67], v147 offset:768
ds_read_b64_tr_b16 a[68:69], v147 offset:12288
ds_read_b64_tr_b16 a[70:71], v147 offset:13056
ds_read_b64_tr_b16 a[72:73], v147 offset:32
ds_read_b64_tr_b16 a[74:75], v147 offset:800
ds_read_b64_tr_b16 a[76:77], v147 offset:12320
ds_read_b64_tr_b16 a[78:79], v147 offset:13088
ds_read_b64_tr_b16 a[240:241], v147 offset:64
ds_read_b64_tr_b16 a[242:243], v147 offset:832
ds_read_b64_tr_b16 a[244:245], v147 offset:12352
ds_read_b64_tr_b16 a[246:247], v147 offset:13120
s_waitcnt vmcnt(24) lgkmcnt(0)
s_barrier
s_add_u32 m0, s51, 0
s_add_u32 s49, s50, 4718592
buffer_load_dwordx4 v144, s[76:79], s49 offen lds
s_add_u32 m0, s51, 1024
s_add_u32 s49, s50, 4718592
buffer_load_dwordx4 v145, s[76:79], s49 offen lds
s_add_u32 m0, s51, 2048
s_add_u32 s49, s50, 4718592
buffer_load_dwordx4 v146, s[76:79], s49 offen lds
)"
R"(s_add_u32 m0, s51, 3072
s_add_u32 s49, s50, 4816896
buffer_load_dwordx4 v144, s[76:79], s49 offen lds
s_add_u32 m0, s51, 4096
s_add_u32 s49, s50, 4816896
buffer_load_dwordx4 v145, s[76:79], s49 offen lds
s_add_u32 m0, s51, 5120
s_add_u32 s49, s50, 4816896
buffer_load_dwordx4 v146, s[76:79], s49 offen lds
ds_read_b64_tr_b16 a[80:81], v147 offset:24576
ds_read_b64_tr_b16 a[82:83], v147 offset:25344
ds_read_b64_tr_b16 a[84:85], v147 offset:36864
ds_read_b64_tr_b16 a[86:87], v147 offset:37632
ds_read_b64_tr_b16 v[48:49], v147 offset:24608
ds_read_b64_tr_b16 v[50:51], v147 offset:25376
ds_read_b64_tr_b16 v[52:53], v147 offset:36896
ds_read_b64_tr_b16 v[54:55], v147 offset:37664
ds_read_b64_tr_b16 a[248:249], v147 offset:24640
ds_read_b64_tr_b16 a[250:251], v147 offset:25408
ds_read_b64_tr_b16 a[252:253], v147 offset:36928
ds_read_b64_tr_b16 a[254:255], v147 offset:37696
s_waitcnt vmcnt(24) lgkmcnt(0)
s_barrier
s_add_u32 m0, s51, 24576
s_add_u32 s49, s50, 4718592
buffer_load_dwordx4 v144, s[80:83], s49 offen lds
s_add_u32 m0, s51, 25600
s_add_u32 s49, s50, 4718592
buffer_load_dwordx4 v145, s[80:83], s49 offen lds
s_add_u32 m0, s51, 26624
s_add_u32 s49, s50, 4718592
buffer_load_dwordx4 v146, s[80:83], s49 offen lds
s_add_u32 m0, s51, 27648
s_add_u32 s49, s50, 4816896
buffer_load_dwordx4 v144, s[80:83], s49 offen lds
s_add_u32 m0, s51, 28672
s_add_u32 s49, s50, 4816896
buffer_load_dwordx4 v145, s[80:83], s49 offen lds
s_add_u32 m0, s51, 29696
s_add_u32 s49, s50, 4816896
buffer_load_dwordx4 v146, s[80:83], s49 offen lds
ds_read_b64_tr_b16 a[88:89], v148 offset:0
ds_read_b64_tr_b16 a[90:91], v148 offset:768
ds_read_b64_tr_b16 a[92:93], v148 offset:12288
ds_read_b64_tr_b16 a[94:95], v148 offset:13056
ds_read_b64_tr_b16 v[64:65], v148 offset:32
ds_read_b64_tr_b16 v[66:67], v148 offset:800
ds_read_b64_tr_b16 v[68:69], v148 offset:12320
ds_read_b64_tr_b16 v[70:71], v148 offset:13088
ds_read_b64_tr_b16 v[160:161], v148 offset:64
ds_read_b64_tr_b16 v[162:163], v148 offset:832
ds_read_b64_tr_b16 v[164:165], v148 offset:12352
ds_read_b64_tr_b16 v[166:167], v148 offset:13120
s_waitcnt vmcnt(24) lgkmcnt(0)
s_barrier
s_add_u32 m0, s51, 49152
s_add_u32 s49, s50, 5505024
buffer_load_dwordx4 v144, s[76:79], s49 offen lds
s_add_u32 m0, s51, 50176
s_add_u32 s49, s50, 5505024
buffer_load_dwordx4 v145, s[76:79], s49 offen lds
s_add_u32 m0, s51, 51200
s_add_u32 s49, s50, 5505024
buffer_load_dwordx4 v146, s[76:79], s49 offen lds
s_add_u32 m0, s51, 52224
s_add_u32 s49, s50, 5603328
buffer_load_dwordx4 v144, s[76:79], s49 offen lds
s_add_u32 m0, s51, 53248
s_add_u32 s49, s50, 5603328
buffer_load_dwordx4 v145, s[76:79], s49 offen lds
s_add_u32 m0, s51, 54272
s_add_u32 s49, s50, 5603328
buffer_load_dwordx4 v146, s[76:79], s49 offen lds
ds_read_b64_tr_b16 a[96:97], v148 offset:24576
ds_read_b64_tr_b16 a[98:99], v148 offset:25344
ds_read_b64_tr_b16 a[100:101], v148 offset:36864
ds_read_b64_tr_b16 a[102:103], v148 offset:37632
ds_read_b64_tr_b16 v[56:57], v148 offset:24608
ds_read_b64_tr_b16 v[58:59], v148 offset:25376
ds_read_b64_tr_b16 v[60:61], v148 offset:36896
ds_read_b64_tr_b16 v[62:63], v148 offset:37664
ds_read_b64_tr_b16 v[168:169], v148 offset:24640
ds_read_b64_tr_b16 v[170:171], v148 offset:25408
ds_read_b64_tr_b16 v[172:173], v148 offset:36928
ds_read_b64_tr_b16 v[174:175], v148 offset:37696
s_waitcnt vmcnt(24) lgkmcnt(0)
s_barrier
s_add_u32 m0, s51, 73728
s_add_u32 s49, s50, 5505024
buffer_load_dwordx4 v144, s[80:83], s49 offen lds
s_add_u32 m0, s51, 74752
s_add_u32 s49, s50, 5505024
buffer_load_dwordx4 v145, s[80:83], s49 offen lds
s_add_u32 m0, s51, 75776
s_add_u32 s49, s50, 5505024
buffer_load_dwordx4 v146, s[80:83], s49 offen lds
s_add_u32 m0, s51, 76800
s_add_u32 s49, s50, 5603328
buffer_load_dwordx4 v144, s[80:83], s49 offen lds
s_add_u32 m0, s51, 77824
s_add_u32 s49, s50, 5603328
buffer_load_dwordx4 v145, s[80:83], s49 offen lds
s_add_u32 m0, s51, 78848
s_add_u32 s49, s50, 5603328
buffer_load_dwordx4 v146, s[80:83], s49 offen lds
ds_read_b64_tr_b16 a[104:105], v149 offset:0
ds_read_b64_tr_b16 a[106:107], v149 offset:768
ds_read_b64_tr_b16 a[108:109], v149 offset:12288
ds_read_b64_tr_b16 a[110:111], v149 offset:13056
ds_read_b64_tr_b16 a[112:113], v149 offset:32
ds_read_b64_tr_b16 a[114:115], v149 offset:800
ds_read_b64_tr_b16 a[116:117], v149 offset:12320
ds_read_b64_tr_b16 a[118:119], v149 offset:13088
ds_read_b64_tr_b16 v[176:177], v149 offset:64
ds_read_b64_tr_b16 v[178:179], v149 offset:832
ds_read_b64_tr_b16 v[180:181], v149 offset:12352
ds_read_b64_tr_b16 v[182:183], v149 offset:13120
s_waitcnt vmcnt(24) lgkmcnt(0)
s_barrier
s_add_u32 m0, s51, 98304
s_add_u32 s49, s50, 6291456
buffer_load_dwordx4 v144, s[76:79], s49 offen lds
s_add_u32 m0, s51, 99328
s_add_u32 s49, s50, 6291456
buffer_load_dwordx4 v145, s[76:79], s49 offen lds
s_add_u32 m0, s51, 100352
s_add_u32 s49, s50, 6291456
buffer_load_dwordx4 v146, s[76:79], s49 offen lds
s_add_u32 m0, s51, 101376
s_add_u32 s49, s50, 6389760
buffer_load_dwordx4 v144, s[76:79], s49 offen lds
s_add_u32 m0, s51, 102400
s_add_u32 s49, s50, 6389760
buffer_load_dwordx4 v145, s[76:79], s49 offen lds
s_add_u32 m0, s51, 103424
s_add_u32 s49, s50, 6389760
buffer_load_dwordx4 v146, s[76:79], s49 offen lds
ds_read_b64_tr_b16 a[120:121], v149 offset:24576
ds_read_b64_tr_b16 a[122:123], v149 offset:25344
ds_read_b64_tr_b16 a[124:125], v149 offset:36864
ds_read_b64_tr_b16 a[126:127], v149 offset:37632
ds_read_b64_tr_b16 v[72:73], v149 offset:24608
ds_read_b64_tr_b16 v[74:75], v149 offset:25376
ds_read_b64_tr_b16 v[76:77], v149 offset:36896
ds_read_b64_tr_b16 v[78:79], v149 offset:37664
ds_read_b64_tr_b16 v[184:185], v149 offset:24640
ds_read_b64_tr_b16 v[186:187], v149 offset:25408
ds_read_b64_tr_b16 v[188:189], v149 offset:36928
ds_read_b64_tr_b16 v[190:191], v149 offset:37696
s_waitcnt vmcnt(24) lgkmcnt(0)
s_barrier
s_add_u32 m0, s51, 122880
s_add_u32 s49, s50, 6291456
buffer_load_dwordx4 v144, s[80:83], s49 offen lds
s_add_u32 m0, s51, 123904
s_add_u32 s49, s50, 6291456
buffer_load_dwordx4 v145, s[80:83], s49 offen lds
s_add_u32 m0, s51, 124928
s_add_u32 s49, s50, 6291456
buffer_load_dwordx4 v146, s[80:83], s49 offen lds
s_add_u32 m0, s51, 125952
s_add_u32 s49, s50, 6389760
buffer_load_dwordx4 v144, s[80:83], s49 offen lds
s_add_u32 m0, s51, 126976
s_add_u32 s49, s50, 6389760
buffer_load_dwordx4 v145, s[80:83], s49 offen lds
s_add_u32 m0, s51, 128000
s_add_u32 s49, s50, 6389760
buffer_load_dwordx4 v146, s[80:83], s49 offen lds
ds_read_b64_tr_b16 a[128:129], v147 offset:0
ds_read_b64_tr_b16 a[130:131], v147 offset:768
ds_read_b64_tr_b16 a[132:133], v147 offset:12288
ds_read_b64_tr_b16 a[134:135], v147 offset:13056
ds_read_b64_tr_b16 a[136:137], v147 offset:32
ds_read_b64_tr_b16 a[138:139], v147 offset:800
ds_read_b64_tr_b16 a[140:141], v147 offset:12320
ds_read_b64_tr_b16 a[142:143], v147 offset:13088
ds_read_b64_tr_b16 v[192:193], v147 offset:64
ds_read_b64_tr_b16 v[194:195], v147 offset:832
ds_read_b64_tr_b16 v[196:197], v147 offset:12352
ds_read_b64_tr_b16 v[198:199], v147 offset:13120
s_waitcnt vmcnt(24) lgkmcnt(0)
s_barrier
s_add_u32 m0, s51, 0
s_add_u32 s49, s50, 7077888
buffer_load_dwordx4 v144, s[76:79], s49 offen lds
s_add_u32 m0, s51, 1024
s_add_u32 s49, s50, 7077888
buffer_load_dwordx4 v145, s[76:79], s49 offen lds
s_add_u32 m0, s51, 2048
s_add_u32 s49, s50, 7077888
buffer_load_dwordx4 v146, s[76:79], s49 offen lds
s_add_u32 m0, s51, 3072
s_add_u32 s49, s50, 7176192
buffer_load_dwordx4 v144, s[76:79], s49 offen lds
s_add_u32 m0, s51, 4096
s_add_u32 s49, s50, 7176192
buffer_load_dwordx4 v145, s[76:79], s49 offen lds
s_add_u32 m0, s51, 5120
s_add_u32 s49, s50, 7176192
)"
R"(buffer_load_dwordx4 v146, s[76:79], s49 offen lds
ds_read_b64_tr_b16 a[144:145], v147 offset:24576
ds_read_b64_tr_b16 a[146:147], v147 offset:25344
ds_read_b64_tr_b16 a[148:149], v147 offset:36864
ds_read_b64_tr_b16 a[150:151], v147 offset:37632
ds_read_b64_tr_b16 v[80:81], v147 offset:24608
ds_read_b64_tr_b16 v[82:83], v147 offset:25376
ds_read_b64_tr_b16 v[84:85], v147 offset:36896
ds_read_b64_tr_b16 v[86:87], v147 offset:37664
ds_read_b64_tr_b16 v[200:201], v147 offset:24640
ds_read_b64_tr_b16 v[202:203], v147 offset:25408
ds_read_b64_tr_b16 v[204:205], v147 offset:36928
ds_read_b64_tr_b16 v[206:207], v147 offset:37696
s_waitcnt vmcnt(24) lgkmcnt(0)
s_barrier
s_add_u32 m0, s51, 24576
s_add_u32 s49, s50, 7077888
buffer_load_dwordx4 v144, s[80:83], s49 offen lds
s_add_u32 m0, s51, 25600
s_add_u32 s49, s50, 7077888
buffer_load_dwordx4 v145, s[80:83], s49 offen lds
s_add_u32 m0, s51, 26624
s_add_u32 s49, s50, 7077888
buffer_load_dwordx4 v146, s[80:83], s49 offen lds
s_add_u32 m0, s51, 27648
s_add_u32 s49, s50, 7176192
buffer_load_dwordx4 v144, s[80:83], s49 offen lds
s_add_u32 m0, s51, 28672
s_add_u32 s49, s50, 7176192
buffer_load_dwordx4 v145, s[80:83], s49 offen lds
s_add_u32 m0, s51, 29696
s_add_u32 s49, s50, 7176192
buffer_load_dwordx4 v146, s[80:83], s49 offen lds
ds_read_b64_tr_b16 a[152:153], v148 offset:0
ds_read_b64_tr_b16 a[154:155], v148 offset:768
ds_read_b64_tr_b16 a[156:157], v148 offset:12288
ds_read_b64_tr_b16 a[158:159], v148 offset:13056
ds_read_b64_tr_b16 v[96:97], v148 offset:32
ds_read_b64_tr_b16 v[98:99], v148 offset:800
ds_read_b64_tr_b16 v[100:101], v148 offset:12320
ds_read_b64_tr_b16 v[102:103], v148 offset:13088
ds_read_b64_tr_b16 v[208:209], v148 offset:64
ds_read_b64_tr_b16 v[210:211], v148 offset:832
ds_read_b64_tr_b16 v[212:213], v148 offset:12352
ds_read_b64_tr_b16 v[214:215], v148 offset:13120
s_waitcnt vmcnt(24) lgkmcnt(0)
s_barrier
ds_read_b64_tr_b16 a[160:161], v148 offset:24576
ds_read_b64_tr_b16 a[162:163], v148 offset:25344
ds_read_b64_tr_b16 a[164:165], v148 offset:36864
ds_read_b64_tr_b16 a[166:167], v148 offset:37632
ds_read_b64_tr_b16 v[88:89], v148 offset:24608
ds_read_b64_tr_b16 v[90:91], v148 offset:25376
ds_read_b64_tr_b16 v[92:93], v148 offset:36896
ds_read_b64_tr_b16 v[94:95], v148 offset:37664
ds_read_b64_tr_b16 v[216:217], v148 offset:24640
ds_read_b64_tr_b16 v[218:219], v148 offset:25408
ds_read_b64_tr_b16 v[220:221], v148 offset:36928
ds_read_b64_tr_b16 v[222:223], v148 offset:37696
s_waitcnt vmcnt(18) lgkmcnt(0)
s_barrier
ds_read_b64_tr_b16 a[168:169], v149 offset:0
ds_read_b64_tr_b16 a[170:171], v149 offset:768
ds_read_b64_tr_b16 a[172:173], v149 offset:12288
ds_read_b64_tr_b16 a[174:175], v149 offset:13056
ds_read_b64_tr_b16 a[176:177], v149 offset:32
ds_read_b64_tr_b16 a[178:179], v149 offset:800
ds_read_b64_tr_b16 a[180:181], v149 offset:12320
ds_read_b64_tr_b16 a[182:183], v149 offset:13088
ds_read_b64_tr_b16 v[224:225], v149 offset:64
ds_read_b64_tr_b16 v[226:227], v149 offset:832
ds_read_b64_tr_b16 v[228:229], v149 offset:12352
ds_read_b64_tr_b16 v[230:231], v149 offset:13120
s_waitcnt vmcnt(12) lgkmcnt(0)
s_barrier
ds_read_b64_tr_b16 a[184:185], v149 offset:24576
ds_read_b64_tr_b16 a[186:187], v149 offset:25344
ds_read_b64_tr_b16 a[188:189], v149 offset:36864
ds_read_b64_tr_b16 a[190:191], v149 offset:37632
ds_read_b64_tr_b16 v[104:105], v149 offset:24608
ds_read_b64_tr_b16 v[106:107], v149 offset:25376
ds_read_b64_tr_b16 v[108:109], v149 offset:36896
ds_read_b64_tr_b16 v[110:111], v149 offset:37664
ds_read_b64_tr_b16 v[232:233], v149 offset:24640
ds_read_b64_tr_b16 v[234:235], v149 offset:25408
ds_read_b64_tr_b16 v[236:237], v149 offset:36928
ds_read_b64_tr_b16 v[238:239], v149 offset:37696
s_waitcnt vmcnt(6) lgkmcnt(0)
s_barrier
ds_read_b64_tr_b16 a[192:193], v147 offset:0
ds_read_b64_tr_b16 a[194:195], v147 offset:768
ds_read_b64_tr_b16 a[196:197], v147 offset:12288
ds_read_b64_tr_b16 a[198:199], v147 offset:13056
ds_read_b64_tr_b16 a[200:201], v147 offset:32
ds_read_b64_tr_b16 a[202:203], v147 offset:800
ds_read_b64_tr_b16 a[204:205], v147 offset:12320
ds_read_b64_tr_b16 a[206:207], v147 offset:13088
ds_read_b64_tr_b16 v[240:241], v147 offset:64
ds_read_b64_tr_b16 v[242:243], v147 offset:832
ds_read_b64_tr_b16 v[244:245], v147 offset:12352
ds_read_b64_tr_b16 v[246:247], v147 offset:13120
s_waitcnt vmcnt(0) lgkmcnt(0)
s_barrier
ds_read_b64_tr_b16 a[208:209], v147 offset:24576
ds_read_b64_tr_b16 a[210:211], v147 offset:25344
ds_read_b64_tr_b16 a[212:213], v147 offset:36864
ds_read_b64_tr_b16 a[214:215], v147 offset:37632
ds_read_b64_tr_b16 v[112:113], v147 offset:24608
ds_read_b64_tr_b16 v[114:115], v147 offset:25376
ds_read_b64_tr_b16 v[116:117], v147 offset:36896
ds_read_b64_tr_b16 v[118:119], v147 offset:37664
ds_read_b64_tr_b16 v[248:249], v147 offset:24640
ds_read_b64_tr_b16 v[250:251], v147 offset:25408
ds_read_b64_tr_b16 v[252:253], v147 offset:36928
ds_read_b64_tr_b16 v[254:255], v147 offset:37696
s_waitcnt lgkmcnt(0)
s_barrier
ds_write_b128 v2, v[16:19] offset:0
ds_write_b128 v2, v[20:23] offset:1024
ds_write_b128 v2, v[24:27] offset:2048
ds_write_b128 v2, v[28:31] offset:3072
ds_write_b128 v2, v[32:35] offset:4096
ds_write_b128 v2, v[36:39] offset:5120
ds_write_b128 v2, v[40:43] offset:6144
ds_write_b128 v2, v[44:47] offset:7168
ds_write_b128 v2, v[48:51] offset:8192
ds_write_b128 v2, v[52:55] offset:9216
ds_write_b128 v2, v[56:59] offset:10240
ds_write_b128 v2, v[60:63] offset:11264
ds_write_b128 v2, v[64:67] offset:12288
ds_write_b128 v2, v[68:71] offset:13312
ds_write_b128 v2, v[72:75] offset:14336
ds_write_b128 v2, v[76:79] offset:15360
s_waitcnt lgkmcnt(14)
ds_write_b128 v2, v[80:83] offset:16384
ds_write_b128 v2, v[84:87] offset:17408
s_waitcnt lgkmcnt(14)
ds_write_b128 v2, v[88:91] offset:18432
ds_write_b128 v2, v[92:95] offset:19456
s_waitcnt lgkmcnt(14)
ds_write_b128 v2, v[96:99] offset:20480
ds_write_b128 v2, v[100:103] offset:21504
s_waitcnt lgkmcnt(14)
ds_write_b128 v2, v[104:107] offset:22528
ds_write_b128 v2, v[108:111] offset:23552
s_waitcnt lgkmcnt(14)
ds_write_b128 v2, v[112:115] offset:24576
ds_write_b128 v2, v[116:119] offset:25600
s_waitcnt lgkmcnt(14)
ds_write_b128 v2, v[120:123] offset:26624
ds_write_b128 v2, v[124:127] offset:27648
s_waitcnt lgkmcnt(14)
ds_write_b128 v2, v[128:131] offset:28672
ds_write_b128 v2, v[132:135] offset:29696
s_waitcnt lgkmcnt(14)
ds_write_b128 v2, v[136:139] offset:30720
ds_write_b128 v2, v[140:143] offset:31744
s_waitcnt lgkmcnt(14)
s_waitcnt vmcnt(0) lgkmcnt(0)
v_lshrrev_b32 v152, 4, v15
v_and_b32 v153, 7, v15
v_bfe_u32 v154, v15, 3, 1
v_cmp_eq_u32 vcc, 1, v154
v_mov_b32 v13, 0x4444
v_mov_b32 v1, 0xeeee
v_cndmask_b32 v1, v13, v1, vcc
v_mov_b32 v13, 0xff800000
v_cndmask_b32 v8, 0, v13, vcc
v_cndmask_b32 v9, 0, v13, vcc
v_cndmask_b32 v10, 0, v13, vcc
v_cndmask_b32 v11, 0, v13, vcc
v_bfe_u32 v13, v15, 2, 2
v_xor_b32 v13, v13, v152
v_lshlrev_b32 v13, 3, v13
v_and_b32 v3, 15, v15
v_lshl_add_u32 v3, v3, 5, v13
s_mul_i32 s48, s45, 1536
s_add_u32 s48, s48, 143360
v_add_u32 v3, s48, v3
v_xor_b32 v4, v15, v152
v_lshlrev_b32 v4, 3, v4
v_add_u32 v4, 143360, v4
v_add_u32 v5, 131072, v0
s_lshr_b32 s48, s44, 1
s_lshl_b32 s48, s48, 10
s_and_b32 s49, s44, 1
s_lshl_b32 s49, s49, 7
s_add_u32 s50, s48, s49
s_lshl_b32 s51, s44, 7
s_add_u32 s51, s51, 16384
s_mov_b32 s47, 0
s_mov_b32 s61, 6144
s_cmp_lg_u32 s45, 0
s_cbranch_scc1 hw_wx_%=
v_lshlrev_b32 v6, 8, v152
v_add_u32 v6, s50, v6
v_mov_b32 v7, s51
v_cmp_eq_u32 vcc, 0, v154
s_nop 1
v_cndmask_b32 v6, v7, v6, vcc
)"
R"(v_lshl_add_u32 v6, v153, 4, v6
v_and_b32 v13, 0xffffe000, v6
v_add_u32 v6, v6, v13
v_lshrrev_b32 v7, 3, v15
v_cmp_eq_u32 s[56:57], 5, v7
s_nop 0
s_or_b64 vcc, vcc, s[56:57]
v_mov_b32 v13, 2147483632
v_cndmask_b32 v6, v13, v6, vcc
v_mov_b32 v14, 0
v_cmp_eq_u32 vcc, 0, v15
v_mov_b32 v13, 2147483632
s_nop 0
v_cndmask_b32 v14, v13, v14, vcc
s_cmp_eq_u32 s44, 0
s_cselect_b32 s59, 4, 0
v_lshlrev_b32 v7, 15, v15
v_cmp_gt_u32 vcc, s59, v15
v_mov_b32 v13, 2147483632
s_nop 0
v_cndmask_b32 v7, v13, v7, vcc
s_lshl_b32 s59, s59, 1
v_lshlrev_b32 v155, 7, v15
v_cmp_gt_u32 vcc, s59, v15
v_mov_b32 v13, 2147483632
s_nop 0
v_cndmask_b32 v155, v13, v155, vcc
s_mov_b32 s64, 0x3dd53b95
s_mov_b32 s65, s64
s_lshr_b32 s48, s44, 3
s_lshl_b32 s48, s48, 7
v_add_u32 v159, s48, v14
s_mul_i32 s10, s67, 23
s_sub_u32 s10, s10, s71
s_mul_i32 s11, s66, 23
s_sub_u32 s11, s11, s72
hw_loopa_%=:
s_and_b32 s48, s47, 3
s_mul_i32 s53, s48, 196608
s_lshl_b32 s50, s48, 2
s_add_u32 s50, s50, s46
s_mul_i32 s50, s50, 32768
s_add_u32 s50, s50, 786432
v_add_u32 v158, s50, v0
s_add_u32 s50, s53, 49152
s_add_u32 s51, s53, 98304
s_add_u32 s52, s53, 147456
v_mov_b32 v56, 0
v_mov_b32 v57, 0
v_mov_b32 v58, 0
v_mov_b32 v59, 0
v_mov_b32 v60, 0
v_mov_b32 v61, 0
v_mov_b32 v62, 0
v_mov_b32 v63, 0
v_mov_b32 v64, 0
v_mov_b32 v65, 0
v_mov_b32 v66, 0
v_mov_b32 v67, 0
v_mov_b32 v68, 0
v_mov_b32 v69, 0
v_mov_b32 v70, 0
v_mov_b32 v71, 0
v_mov_b32 v72, 0
v_mov_b32 v73, 0
v_mov_b32 v74, 0
v_mov_b32 v75, 0
v_mov_b32 v76, 0
v_mov_b32 v77, 0
v_mov_b32 v78, 0
v_mov_b32 v79, 0
ds_read_b128 v[80:83], v2 offset:0
ds_read_b128 v[84:87], v2 offset:1024
ds_read_b128 v[88:91], v2 offset:2048
ds_read_b128 v[92:95], v2 offset:3072
ds_read_b128 v[96:99], v2 offset:4096
ds_read_b128 v[100:103], v2 offset:5120
s_and_b32 s59, s47, 64
s_add_u32 s63, s47, s59
s_lshl_b32 s63, s63, 7
s_add_u32 s63, s63, 1310720
s_lshl_b32 s59, s46, 15
s_add_u32 s59, s59, s63
s_add_u32 s14, s40, s59
s_addc_u32 s15, s41, 0
s_lshl_b32 s12, s47, 10
s_add_u32 s12, s12, 2375936
s_waitcnt lgkmcnt(0)
s_and_b32 s59, s47, 63
s_cbranch_scc1 hw_timed_%=
s_add_u32 s75, s47, 6
s_barrier
s_memrealtime s[56:57]
s_waitcnt lgkmcnt(0)
s_add_u32 s62, s56, s68
s_add_u32 s59, s62, s67
v_mov_b32 v152, s59
s_add_u32 s59, s59, s66
v_mov_b32 v153, s59
s_nop 1
s_branch hw_go_%=
hw_timed_%=:
s_mov_b32 s62, s17
s_cmp_lg_u32 s62, -1
s_cbranch_scc1 hw_rs_ok_%=
s_sub_u32 s55, s47, 1
s_and_b32 s59, s55, 64
s_add_u32 s54, s55, s59
s_lshl_b32 s54, s54, 7
s_add_u32 s54, s54, 1310720
s_mov_b32 s60, 0
hw_rs_%=:
buffer_load_dwordx2 v[152:153], v14, s[40:43], s54 offen sc1
s_waitcnt vmcnt(0)
v_readfirstlane_b32 s55, v152
v_readfirstlane_b32 s62, v153
s_nop 0
s_max_u32 s59, s55, s62
s_cmp_lg_u32 s59, -1
s_nop 1
s_cbranch_scc1 hw_rs_ok_%=
s_sleep 4
s_add_u32 s60, s60, 1
s_cmp_gt_u32 s60, 65536
s_cbranch_scc1 hw_abort_%=
s_and_b32 s59, s60, 63
s_cbranch_scc1 hw_rs_%=
v_mov_b32 v152, 1024
buffer_load_dword v152, v152, s[40:43], s58 offen sc1
s_waitcnt vmcnt(0)
v_readfirstlane_b32 s59, v152
s_nop 0
s_cmp_lg_u32 s59, 0
s_nop 2
s_cbranch_scc1 hw_end_%=
s_branch hw_rs_%=
hw_rs_ok_%=:
s_add_u32 s59, s62, s67
v_mov_b32 v152, s59
s_add_u32 s59, s59, s66
v_mov_b32 v153, s59
s_memrealtime s[56:57]
s_waitcnt lgkmcnt(0)
s_sub_u32 s59, s62, s56
s_cmp_gt_u32 s59, 1000
s_cbranch_scc1 hw_wu_go_%=
hw_wu_%=:
s_memrealtime s[56:57]
s_waitcnt lgkmcnt(0)
s_sub_u32 s59, s62, s56
s_cmp_gt_i32 s59, 15
s_cbranch_scc1 hw_wu_%=
s_memrealtime s[30:31]
s_nop 1
s_memrealtime s[18:19]
s_nop 1
s_memrealtime s[34:35]
s_nop 1
s_memrealtime s[36:37]
s_nop 1
s_memrealtime s[38:39]
s_nop 1
s_memrealtime s[94:95]
s_nop 1
s_memrealtime s[96:97]
s_nop 1
s_memrealtime s[98:99]
s_nop 1
s_getpc_b64 s[56:57]
hw_wu_pc_%=:
s_waitcnt lgkmcnt(0)
s_add_u32 s18, s18, s34
s_add_u32 s18, s18, s36
s_add_u32 s18, s18, s38
s_add_u32 s18, s18, s94
s_add_u32 s18, s18, s96
s_add_u32 s18, s18, s98
s_mul_i32 s34, s30, 7
s_sub_u32 s18, s18, s34
s_mul_i32 s18, s18, 3
s_sub_u32 s59, s62, s30
s_mul_i32 s59, s59, 23
s_sub_u32 s59, s59, s18
s_sub_u32 s59, s59, 170
s_max_i32 s59, s59, 0
s_min_i32 s59, s59, 252
s_andn2_b32 s59, s59, 3
s_sub_u32 s59, hw_wu_e_%=-hw_wu_pc_%=, s59
s_add_u32 s56, s56, s59
s_addc_u32 s57, s57, 0
s_setpc_b64 s[56:57]
s_nop 0
s_nop 0
s_nop 0
s_nop 0
s_nop 0
s_nop 0
s_nop 0
s_nop 0
s_nop 0
)"
R"(s_nop 0
s_nop 0
s_nop 0
s_nop 0
s_nop 0
s_nop 0
s_nop 0
s_nop 0
s_nop 0
s_nop 0
s_nop 0
s_nop 0
s_nop 0
s_nop 0
s_nop 0
s_nop 0
s_nop 0
s_nop 0
s_nop 0
s_nop 0
s_nop 0
s_nop 0
s_nop 0
s_nop 0
s_nop 0
s_nop 0
s_nop 0
s_nop 0
s_nop 0
s_nop 0
s_nop 0
s_nop 0
s_nop 0
s_nop 0
s_nop 0
s_nop 0
s_nop 0
s_nop 0
s_nop 0
s_nop 0
s_nop 0
s_nop 0
s_nop 0
s_nop 0
s_nop 0
s_nop 0
s_nop 0
s_nop 0
s_nop 0
s_nop 0
s_nop 0
s_nop 0
s_nop 0
s_nop 0
hw_wu_e_%=:
hw_wu_go_%=:
s_barrier
hw_go_%=:
s_mov_b32 s60, 0
hw_retrya_%=:
buffer_load_dwordx4 v[16:19], v158, s[40:43], s20 offen sc1
ds_read_b128 v[104:107], v5 offset:0
s_nop 6
buffer_load_dwordx4 v[20:23], v158, s[40:43], s21 offen sc1
ds_read_b128 v[108:111], v5 offset:1024
s_nop 6
buffer_load_dwordx4 v[24:27], v158, s[40:43], s22 offen sc1
ds_read_b128 v[112:115], v5 offset:2048
s_nop 6
buffer_load_dwordx4 v[28:31], v158, s[40:43], s23 offen sc1
ds_read_b128 v[116:119], v5 offset:3072
s_nop 6
buffer_load_dwordx4 v[32:35], v158, s[40:43], s24 offen sc1
ds_read_b128 v[120:123], v5 offset:4096
s_nop 6
buffer_load_dwordx4 v[36:39], v158, s[40:43], s25 offen sc1
ds_read_b128 v[124:127], v5 offset:5120
s_nop 6
buffer_load_dwordx4 v[40:43], v158, s[40:43], s26 offen sc1
ds_read_b128 v[128:131], v5 offset:6144
s_nop 6
buffer_load_dwordx4 v[44:47], v158, s[40:43], s27 offen sc1
ds_read_b128 v[132:135], v5 offset:7168
s_nop 6
buffer_load_dwordx4 v[48:51], v158, s[40:43], s28 offen sc1
s_nop 7
buffer_load_dwordx4 v[52:55], v158, s[40:43], s29 offen sc1
s_waitcnt lgkmcnt(0)
s_waitcnt vmcnt(9)
v_smfmac_f32_16x16x64_bf16 v[68:71], v[16:19], v[80:87], v1
ds_read_b128 v[80:83], v2 offset:6144
v_smfmac_f32_16x16x64_bf16 v[56:59], v[16:19], a[0:7], v1
ds_read_b128 v[84:87], v2 offset:7168
v_smfmac_f32_16x16x64_bf16 v[64:67], v[16:19], a[8:15], v1
v_smfmac_f32_16x16x64_bf16 v[60:63], v[16:19], a[16:23], v1
s_waitcnt vmcnt(8)
v_smfmac_f32_16x16x64_bf16 v[68:71], v[20:23], v[88:95], v1
ds_read_b128 v[88:91], v2 offset:8192
v_smfmac_f32_16x16x64_bf16 v[56:59], v[20:23], a[24:31], v1
ds_read_b128 v[92:95], v2 offset:9216
v_smfmac_f32_16x16x64_bf16 v[64:67], v[20:23], v[96:103], v1
ds_read_b128 v[96:99], v2 offset:10240
v_smfmac_f32_16x16x64_bf16 v[60:63], v[20:23], a[32:39], v1
ds_read_b128 v[100:103], v2 offset:11264
s_waitcnt vmcnt(7) lgkmcnt(4)
v_smfmac_f32_16x16x64_bf16 v[68:71], v[24:27], v[80:87], v1
ds_read_b128 v[80:83], v2 offset:12288
v_smfmac_f32_16x16x64_bf16 v[56:59], v[24:27], a[40:47], v1
ds_read_b128 v[84:87], v2 offset:13312
v_smfmac_f32_16x16x64_bf16 v[64:67], v[24:27], a[48:55], v1
v_smfmac_f32_16x16x64_bf16 v[60:63], v[24:27], a[56:63], v1
v_max3_u32 v12, v16, v20, v24
s_waitcnt vmcnt(6) lgkmcnt(4)
v_smfmac_f32_16x16x64_bf16 v[68:71], v[28:31], v[88:95], v1
ds_read_b128 v[88:91], v2 offset:14336
v_smfmac_f32_16x16x64_bf16 v[56:59], v[28:31], a[64:71], v1
ds_read_b128 v[92:95], v2 offset:15360
v_smfmac_f32_16x16x64_bf16 v[64:67], v[28:31], a[72:79], v1
v_cmp_eq_u32 vcc, -1, v12
v_smfmac_f32_16x16x64_bf16 v[60:63], v[28:31], a[80:87], v1
s_cbranch_vccnz hw_polla_%=
s_waitcnt vmcnt(5) lgkmcnt(4)
v_smfmac_f32_16x16x64_bf16 v[68:71], v[32:35], v[96:103], v1
ds_read_b128 v[96:99], v2 offset:16384
v_smfmac_f32_16x16x64_bf16 v[56:59], v[32:35], a[88:95], v1
ds_read_b128 v[100:103], v2 offset:17408
s_waitcnt lgkmcnt(4)
v_smfmac_f32_16x16x64_bf16 v[64:67], v[32:35], v[80:87], v1
ds_read_b128 v[80:83], v2 offset:18432
v_smfmac_f32_16x16x64_bf16 v[60:63], v[32:35], a[96:103], v1
ds_read_b128 v[84:87], v2 offset:19456
s_waitcnt vmcnt(4) lgkmcnt(4)
v_smfmac_f32_16x16x64_bf16 v[68:71], v[36:39], v[88:95], v1
ds_read_b128 v[88:91], v2 offset:20480
v_smfmac_f32_16x16x64_bf16 v[56:59], v[36:39], a[104:111], v1
ds_read_b128 v[92:95], v2 offset:21504
v_smfmac_f32_16x16x64_bf16 v[64:67], v[36:39], a[112:119], v1
v_smfmac_f32_16x16x64_bf16 v[60:63], v[36:39], a[120:127], v1
s_waitcnt vmcnt(3) lgkmcnt(4)
v_smfmac_f32_16x16x64_bf16 v[68:71], v[40:43], v[96:103], v1
ds_read_b128 v[96:99], v2 offset:22528
v_smfmac_f32_16x16x64_bf16 v[56:59], v[40:43], a[128:135], v1
ds_read_b128 v[100:103], v2 offset:23552
v_smfmac_f32_16x16x64_bf16 v[64:67], v[40:43], a[136:143], v1
v_smfmac_f32_16x16x64_bf16 v[60:63], v[40:43], a[144:151], v1
s_waitcnt vmcnt(2) lgkmcnt(4)
v_smfmac_f32_16x16x64_bf16 v[68:71], v[44:47], v[80:87], v1
ds_read_b128 v[80:83], v2 offset:24576
v_smfmac_f32_16x16x64_bf16 v[56:59], v[44:47], a[152:159], v1
ds_read_b128 v[84:87], v2 offset:25600
s_waitcnt lgkmcnt(4)
v_smfmac_f32_16x16x64_bf16 v[64:67], v[44:47], v[88:95], v1
ds_read_b128 v[88:91], v2 offset:26624
v_smfmac_f32_16x16x64_bf16 v[60:63], v[44:47], a[160:167], v1
ds_read_b128 v[92:95], v2 offset:27648
s_waitcnt vmcnt(1) lgkmcnt(4)
v_smfmac_f32_16x16x64_bf16 v[68:71], v[48:51], v[96:103], v1
ds_read_b128 v[96:99], v2 offset:28672
v_smfmac_f32_16x16x64_bf16 v[56:59], v[48:51], a[168:175], v1
ds_read_b128 v[100:103], v2 offset:29696
v_smfmac_f32_16x16x64_bf16 v[64:67], v[48:51], a[176:183], v1
v_smfmac_f32_16x16x64_bf16 v[60:63], v[48:51], a[184:191], v1
s_waitcnt vmcnt(0) lgkmcnt(4)
v_smfmac_f32_16x16x64_bf16 v[68:71], v[52:55], v[80:87], v1
ds_read_b128 v[80:83], v2 offset:30720
v_smfmac_f32_16x16x64_bf16 v[56:59], v[52:55], a[192:199], v1
ds_read_b128 v[84:87], v2 offset:31744
v_smfmac_f32_16x16x64_bf16 v[64:67], v[52:55], a[200:207], v1
v_smfmac_f32_16x16x64_bf16 v[60:63], v[52:55], a[208:215], v1
v_smfmac_f32_16x16x64_bf16 v[72:75], v[16:19], a[216:223], v1
v_mov_b32 v13, v13
v_mov_b32 v13, v13
v_mov_b32 v13, v13
v_mov_b32 v13, v13
v_mov_b32 v13, v13
v_mov_b32 v13, v13
v_mov_b32 v13, v13
v_permlane32_swap_b32 v56, v60
v_permlane32_swap_b32 v57, v61
s_waitcnt lgkmcnt(4)
v_smfmac_f32_16x16x64_bf16 v[76:79], v[16:19], v[88:95], v1
v_permlane32_swap_b32 v58, v62
v_permlane32_swap_b32 v59, v63
v_smfmac_f32_16x16x64_bf16 v[72:75], v[20:23], a[224:231], v1
v_add_f32 v56, v56, v60
v_add_f32 v57, v57, v61
s_waitcnt lgkmcnt(2)
v_smfmac_f32_16x16x64_bf16 v[76:79], v[20:23], v[96:103], v1
v_add_f32 v58, v58, v62
v_add_f32 v59, v59, v63
v_smfmac_f32_16x16x64_bf16 v[72:75], v[24:27], a[232:239], v1
v_cmp_u_f32 vcc, v56, v57
v_cmp_u_f32 s[56:57], v58, v59
s_waitcnt lgkmcnt(0)
v_smfmac_f32_16x16x64_bf16 v[76:79], v[24:27], v[80:87], v1
s_or_b64 vcc, vcc, s[56:57]
s_cbranch_vccnz hw_faila_%=
hw_resumea_%=:
buffer_store_dwordx2 v[152:153], v7, s[40:43], s63 offen sc1
v_cvt_pk_bf16_f32 v60, v56, v57
)"
R"(v_smfmac_f32_16x16x64_bf16 v[72:75], v[28:31], a[240:247], v1
v_cvt_pk_bf16_f32 v61, v58, v59
ds_write_b64 v3, v[60:61] offset:0
v_smfmac_f32_16x16x64_bf16 v[76:79], v[28:31], a[248:255], v1
v_permlane32_swap_b32 v64, v68
v_permlane32_swap_b32 v65, v69
v_smfmac_f32_16x16x64_bf16 v[72:75], v[32:35], v[160:167], v1
v_permlane32_swap_b32 v66, v70
v_permlane32_swap_b32 v67, v71
v_smfmac_f32_16x16x64_bf16 v[76:79], v[32:35], v[168:175], v1
v_add_f32 v64, v64, v68
v_smfmac_f32_16x16x64_bf16 v[72:75], v[36:39], v[176:183], v1
v_add_f32 v65, v65, v69
v_smfmac_f32_16x16x64_bf16 v[76:79], v[36:39], v[184:191], v1
v_add_f32 v66, v66, v70
v_smfmac_f32_16x16x64_bf16 v[72:75], v[40:43], v[192:199], v1
v_add_f32 v67, v67, v71
v_smfmac_f32_16x16x64_bf16 v[76:79], v[40:43], v[200:207], v1
v_cvt_pk_bf16_f32 v68, v64, v65
v_smfmac_f32_16x16x64_bf16 v[72:75], v[44:47], v[208:215], v1
v_cvt_pk_bf16_f32 v69, v66, v67
v_smfmac_f32_16x16x64_bf16 v[76:79], v[44:47], v[216:223], v1
ds_write_b64 v3, v[68:69] offset:512
buffer_load_dword v154, v155, s[40:43], s12 offen sc1
v_smfmac_f32_16x16x64_bf16 v[72:75], v[48:51], v[224:231], v1
ds_read_b128 v[16:19], v5 offset:8192
v_smfmac_f32_16x16x64_bf16 v[76:79], v[48:51], v[232:239], v1
ds_read_b128 v[20:23], v5 offset:9216
v_smfmac_f32_16x16x64_bf16 v[72:75], v[52:55], v[240:247], v1
ds_read_b128 v[24:27], v5 offset:10240
v_smfmac_f32_16x16x64_bf16 v[76:79], v[52:55], v[248:255], v1
ds_read_b128 v[28:31], v5 offset:11264
s_waitcnt lgkmcnt(4)
s_barrier
ds_read_b64_tr_b16 v[136:137], v4 offset:0
ds_read_b64_tr_b16 v[138:139], v4 offset:512
v_mov_b32 v13, v13
ds_read_b64_tr_b16 v[140:141], v4 offset:1536
ds_read_b64_tr_b16 v[142:143], v4 offset:2048
v_mov_b32 v13, v13
ds_read_b64_tr_b16 v[144:145], v4 offset:3072
ds_read_b64_tr_b16 v[146:147], v4 offset:3584
v_mov_b32 v13, v13
ds_read_b64_tr_b16 v[148:149], v4 offset:4608
ds_read_b64_tr_b16 v[150:151], v4 offset:5120
v_mov_b32 v13, v13
v_permlane32_swap_b32 v72, v76
v_permlane32_swap_b32 v73, v77
v_permlane32_swap_b32 v74, v78
v_permlane32_swap_b32 v75, v79
v_pk_add_f32 v[72:73], v[72:73], v[76:77]
v_pk_add_f32 v[74:75], v[74:75], v[78:79]
v_cvt_pk_bf16_f32 v76, v72, v73
v_cvt_pk_bf16_f32 v77, v74, v75
ds_write_b64 v3, v[76:77] offset:1024
s_waitcnt lgkmcnt(7)
v_mfma_f32_16x16x32_bf16 v[56:59], v[104:107], v[136:139], v[8:11]
v_mfma_f32_16x16x32_bf16 v[60:63], v[108:111], v[136:139], v[8:11]
v_mfma_f32_16x16x32_bf16 v[64:67], v[136:139], v[136:139], 0
s_waitcnt lgkmcnt(5)
v_mfma_f32_16x16x32_bf16 v[56:59], v[112:115], v[140:143], v[56:59]
s_waitcnt lgkmcnt(0)
s_barrier
ds_read_b64_tr_b16 v[32:33], v4 offset:1024
ds_read_b64_tr_b16 v[34:35], v4 offset:2560
ds_read_b64_tr_b16 v[36:37], v4 offset:4096
ds_read_b64_tr_b16 v[38:39], v4 offset:5632
v_mfma_f32_16x16x32_bf16 v[60:63], v[116:119], v[140:143], v[60:63]
v_mfma_f32_16x16x32_bf16 v[64:67], v[140:143], v[140:143], v[64:67]
v_mfma_f32_16x16x32_bf16 v[56:59], v[120:123], v[144:147], v[56:59]
v_mfma_f32_16x16x32_bf16 v[60:63], v[124:127], v[144:147], v[60:63]
v_mfma_f32_16x16x32_bf16 v[64:67], v[144:147], v[144:147], v[64:67]
v_mfma_f32_16x16x32_bf16 v[56:59], v[128:131], v[148:151], v[56:59]
v_mfma_f32_16x16x32_bf16 v[60:63], v[132:135], v[148:151], v[60:63]
v_mfma_f32_16x16x32_bf16 v[64:67], v[148:151], v[148:151], v[64:67]
s_waitcnt lgkmcnt(2)
v_mfma_f32_16x16x32_bf16 v[64:67], v[32:35], v[32:35], v[64:67]
s_waitcnt lgkmcnt(0)
v_mfma_f32_16x16x32_bf16 v[64:67], v[36:39], v[36:39], v[64:67]
v_mfma_f32_16x16x32_bf16 v[56:59], v[16:19], v[32:35], v[56:59]
v_mfma_f32_16x16x32_bf16 v[56:59], v[24:27], v[36:39], v[56:59]
v_mfma_f32_16x16x32_bf16 v[60:63], v[20:23], v[32:35], v[60:63]
v_mfma_f32_16x16x32_bf16 v[60:63], v[28:31], v[36:39], v[60:63]
s_load_dwordx2 s[16:17], s[14:15], 0x0
v_mov_b32 v13, v13
v_mov_b32 v13, v13
v_mov_b32 v13, v13
v_permlane16_swap_b32 v64, v72
v_permlane16_swap_b32 v65, v73
v_permlane16_swap_b32 v66, v74
v_permlane16_swap_b32 v67, v75
v_mov_b32_dpp v56, v64 row_shr:8 row_mask:0x4 bank_mask:0xc
v_mov_b32_dpp v57, v65 row_shr:8 row_mask:0x4 bank_mask:0xc
v_mov_b32_dpp v58, v66 row_shr:8 row_mask:0x4 bank_mask:0xc
v_mov_b32_dpp v59, v67 row_shr:8 row_mask:0x4 bank_mask:0xc
v_mov_b32_dpp v60, v72 row_shr:8 row_mask:0x4 bank_mask:0xc
v_mov_b32_dpp v61, v73 row_shr:8 row_mask:0x4 bank_mask:0xc
v_mov_b32_dpp v62, v74 row_shr:8 row_mask:0x4 bank_mask:0xc
v_mov_b32_dpp v63, v75 row_shr:8 row_mask:0x4 bank_mask:0xc
v_max3_f32 v68, v60, v61, v62
v_max3_f32 v69, v56, v57, v58
v_max3_f32 v68, v68, v63, v59
v_max_f32 v68, v68, v69
s_nop 1
v_max_f32_dpp v68, v68, v68 row_ror:8 row_mask:0xf bank_mask:0xf
v_mov_b32 v69, v68
s_nop 1
v_permlane16_swap_b32 v68, v69
v_max_f32 v68, v68, v69
v_mov_b32 v69, v68
s_nop 1
v_permlane32_swap_b32 v68, v69
v_max_f32 v68, v68, v69
v_mul_f32 v68, 0xbdd53b95, v68
v_pk_fma_f32 v[56:57], v[56:57], s[64:65], v[68:69] op_sel_hi:[1,1,0]
v_pk_fma_f32 v[58:59], v[58:59], s[64:65], v[68:69] op_sel_hi:[1,1,0]
v_pk_fma_f32 v[60:61], v[60:61], s[64:65], v[68:69] op_sel_hi:[1,1,0]
v_pk_fma_f32 v[62:63], v[62:63], s[64:65], v[68:69] op_sel_hi:[1,1,0]
v_exp_f32 v56, v56
v_exp_f32 v57, v57
v_exp_f32 v58, v58
v_exp_f32 v59, v59
v_exp_f32 v60, v60
v_exp_f32 v61, v61
v_exp_f32 v62, v62
v_exp_f32 v63, v63
v_pk_add_f32 v[64:65], v[56:57], v[58:59]
v_pk_add_f32 v[66:67], v[60:61], v[62:63]
v_pk_add_f32 v[64:65], v[64:65], v[66:67]
v_add_f32 v68, v64, v65
s_nop 1
v_add_f32_dpp v68, v68, v68 row_ror:8 row_mask:0xf bank_mask:0xf
v_mov_b32 v69, v68
s_nop 1
v_permlane16_swap_b32 v68, v69
v_add_f32 v68, v68, v69
v_mov_b32 v69, v68
s_nop 1
v_permlane32_swap_b32 v68, v69
v_add_f32 v68, v68, v69
v_rcp_f32 v68, v68
s_nop 0
v_pk_mul_f32 v[56:57], v[56:57], v[68:69] op_sel_hi:[1,0]
v_pk_mul_f32 v[58:59], v[58:59], v[68:69] op_sel_hi:[1,0]
v_pk_mul_f32 v[60:61], v[60:61], v[68:69] op_sel_hi:[1,0]
v_pk_mul_f32 v[62:63], v[62:63], v[68:69] op_sel_hi:[1,0]
v_cvt_pk_bf16_f32 v64, v56, v57
v_cvt_pk_bf16_f32 v65, v58, v59
v_cvt_pk_bf16_f32 v66, v60, v61
v_cvt_pk_bf16_f32 v67, v62, v63
buffer_store_dwordx4 v[64:67], v6, s[40:43], s53 offen sc1
buffer_store_dwordx4 v[64:67], v6, s[40:43], s50 offen sc1
buffer_store_dwordx4 v[64:67], v6, s[40:43], s51 offen sc1
buffer_store_dwordx4 v[64:67], v6, s[40:43], s52 offen sc1
s_memrealtime s[30:31]
s_nop 1
s_memrealtime s[18:19]
s_nop 1
s_memrealtime s[34:35]
s_nop 1
s_memrealtime s[36:37]
s_nop 1
s_memrealtime s[38:39]
s_nop 1
s_memrealtime s[94:95]
s_nop 1
s_memrealtime s[96:97]
s_nop 1
s_memrealtime s[98:99]
s_nop 1
s_waitcnt lgkmcnt(0)
s_add_u32 s18, s18, s34
s_add_u32 s18, s18, s36
s_add_u32 s18, s18, s38
s_add_u32 s18, s18, s94
s_add_u32 s18, s18, s96
s_add_u32 s18, s18, s98
s_mul_i32 s34, s30, 7
s_sub_u32 s18, s18, s34
s_mul_i32 s18, s18, 3
s_sub_u32 s59, s30, s62
s_mul_i32 s59, s59, 23
s_add_u32 s59, s59, s18
v_mov_b32 v13, s59
s_lshl_b32 s34, s47, 10
s_add_u32 s34, s34, 2377984
s_cmp_eq_u32 s60, 0
s_cselect_b32 s34, s34, 0x40000000
buffer_atomic_umax v13, v159, s[40:43], s34 offen
s_waitcnt vmcnt(5)
v_readlane_b32 s0, v154, 0
v_readlane_b32 s1, v154, 1
v_readlane_b32 s2, v154, 2
v_readlane_b32 s3, v154, 3
v_readlane_b32 s4, v154, 4
v_readlane_b32 s5, v154, 5
v_readlane_b32 s6, v154, 6
v_readlane_b32 s7, v154, 7
s_max_u32 s0, s0, s1
s_max_u32 s0, s0, s2
)"
R"(s_max_u32 s0, s0, s3
s_sub_u32 s59, s0, s10
s_add_u32 s57, s59, 500
s_cmp_lt_u32 s57, 900
s_cselect_b32 s59, s59, 0
s_ashr_i32 s57, s59, 4
s_max_i32 s59, s59, s57
s_min_i32 s59, s59, s73
s_add_u32 s10, s10, s59
s_add_u32 s59, s10, s71
s_mul_i32 s59, s59, 2850
s_lshr_b32 s67, s59, 16
s_max_u32 s4, s4, s5
s_max_u32 s4, s4, s6
s_max_u32 s4, s4, s7
s_sub_u32 s59, s4, s11
s_add_u32 s57, s59, 500
s_cmp_lt_u32 s57, 900
s_cselect_b32 s59, s59, 0
s_ashr_i32 s57, s59, 4
s_max_i32 s59, s59, s57
s_min_i32 s59, s59, s73
s_add_u32 s11, s11, s59
s_add_u32 s59, s11, s72
s_mul_i32 s59, s59, 2850
s_lshr_b32 s66, s59, 16
v_add_u32 v3, s61, v3
v_add_u32 v4, s61, v4
s_sub_u32 s61, 0, s61
s_add_u32 s47, s47, 1
s_cmp_lt_u32 s47, 128
s_cbranch_scc1 hw_loopa_%=
s_branch hw_end_%=
hw_faila_%=:
v_max_u32 v12, v16, v17
v_max3_u32 v12, v18, v19, v12
v_max3_u32 v12, v20, v21, v12
v_max3_u32 v12, v22, v23, v12
v_max3_u32 v12, v24, v25, v12
v_max3_u32 v12, v26, v27, v12
v_max3_u32 v12, v28, v29, v12
v_max3_u32 v12, v30, v31, v12
v_max3_u32 v12, v32, v33, v12
v_max3_u32 v12, v34, v35, v12
v_max3_u32 v12, v36, v37, v12
v_max3_u32 v12, v38, v39, v12
v_max3_u32 v12, v40, v41, v12
v_max3_u32 v12, v42, v43, v12
v_max3_u32 v12, v44, v45, v12
v_max3_u32 v12, v46, v47, v12
v_max3_u32 v12, v48, v49, v12
v_max3_u32 v12, v50, v51, v12
v_max3_u32 v12, v52, v53, v12
v_max3_u32 v12, v54, v55, v12
v_cmp_eq_u32 vcc, -1, v12
s_nop 4
s_cbranch_vccz hw_resumea_%=
hw_polla_%=:
s_nop 7
s_waitcnt vmcnt(0) lgkmcnt(0)
v_mov_b32 v56, 0
v_mov_b32 v57, 0
v_mov_b32 v58, 0
v_mov_b32 v59, 0
v_mov_b32 v60, 0
v_mov_b32 v61, 0
v_mov_b32 v62, 0
v_mov_b32 v63, 0
v_mov_b32 v64, 0
v_mov_b32 v65, 0
v_mov_b32 v66, 0
v_mov_b32 v67, 0
v_mov_b32 v68, 0
v_mov_b32 v69, 0
v_mov_b32 v70, 0
v_mov_b32 v71, 0
v_mov_b32 v72, 0
v_mov_b32 v73, 0
v_mov_b32 v74, 0
v_mov_b32 v75, 0
v_mov_b32 v76, 0
v_mov_b32 v77, 0
v_mov_b32 v78, 0
v_mov_b32 v79, 0
ds_read_b128 v[80:83], v2 offset:0
ds_read_b128 v[84:87], v2 offset:1024
ds_read_b128 v[88:91], v2 offset:2048
ds_read_b128 v[92:95], v2 offset:3072
ds_read_b128 v[96:99], v2 offset:4096
ds_read_b128 v[100:103], v2 offset:5120
v_sub_u32 v12, v158, v0
v_lshl_add_u32 v12, v15, 7, v12
v_and_b32 v18, 15, v15
v_sub_u32 v18, v18, v15
v_lshl_add_u32 v18, v18, 7, v12
s_movk_i32 s55, 0x4000
s_waitcnt vmcnt(0) lgkmcnt(0)
s_add_u32 s60, s60, 1
s_cmp_gt_u32 s60, 65536
s_cbranch_scc1 hw_abort_%=
s_and_b32 s59, s60, 63
s_cbranch_scc1 hw_againa_%=
v_mov_b32 v13, 1024
buffer_load_dword v13, v13, s[40:43], s58 offen sc1
s_waitcnt vmcnt(0)
v_readfirstlane_b32 s59, v13
s_nop 0
s_cmp_lg_u32 s59, 0
s_nop 2
s_cbranch_scc1 hw_end_%=
s_branch hw_againa_%=
hw_againa_%=:
buffer_load_dword v16, v12, s[40:43], 0 offen sc1
buffer_load_dword v17, v18, s[40:43], s55 offen sc1
s_waitcnt vmcnt(0)
v_max_u32 v16, v16, v17
v_cmp_eq_u32 vcc, -1, v16
s_nop 4
s_cbranch_vccz hw_relocka_%=
s_add_u32 s60, s60, 1
s_cmp_gt_u32 s60, 65536
s_cbranch_scc1 hw_abort_%=
s_and_b32 s59, s60, 63
s_cbranch_scc1 hw_againa_%=
v_mov_b32 v13, 1024
buffer_load_dword v13, v13, s[40:43], s58 offen sc1
s_waitcnt vmcnt(0)
v_readfirstlane_b32 s59, v13
s_nop 0
s_cmp_lg_u32 s59, 0
s_nop 2
s_cbranch_scc1 hw_end_%=
s_branch hw_againa_%=
hw_relocka_%=:
s_memrealtime s[56:57]
s_waitcnt lgkmcnt(0)
s_cmp_lt_u32 s47, s75
s_cbranch_scc1 hw_nobump_%=
s_add_u32 s71, s71, 6
s_add_u32 s72, s72, 6
hw_nobump_%=:
s_add_u32 s59, s47, 2
s_max_u32 s75, s75, s59
s_add_u32 s62, s56, s68
s_add_u32 s59, s62, s67
v_mov_b32 v152, s59
s_add_u32 s59, s59, s66
v_mov_b32 v153, s59
s_nop 1
s_branch hw_retrya_%=
hw_wx_%=:
v_lshrrev_b32 v6, 3, v15
v_lshlrev_b32 v6, 8, v6
v_add_u32 v6, s50, v6
v_mov_b32 v13, s51
v_cmp_gt_u32 vcc, 32, v15
s_nop 1
v_cndmask_b32 v6, v13, v6, vcc
v_lshl_add_u32 v6, v153, 4, v6
v_and_b32 v13, 0xffffe000, v6
v_add_u32 v6, v6, v13
v_cmp_gt_u32 vcc, 40, v15
v_mov_b32 v13, 2147483632
s_nop 0
v_cndmask_b32 v6, v13, v6, vcc
v_lshlrev_b32 v7, 7, v152
v_lshl_add_u32 v7, v153, 4, v7
v_cmp_eq_u32 vcc, 1, v154
v_mov_b32 v13, 2147483632
s_nop 0
v_cndmask_b32 v7, v13, v7, vcc
s_cmp_eq_u32 s45, 1
s_cselect_b64 vcc, -1, 0
v_cndmask_b32 v6, v6, v7, vcc
s_cmp_eq_u32 s45, 1
s_cselect_b32 s59, 1, 0
v_lshlrev_b32 v133, 7, v15
v_cmp_gt_u32 vcc, s59, v15
v_mov_b32 v13, 2147483632
s_nop 0
v_cndmask_b32 v133, v13, v133, vcc
s_lshl_b32 s64, s44, 7
s_add_u32 s64, s64, 2103296
s_lshl_b32 s49, s45, 10
s_sub_u32 s49, s49, 1024
s_cmp_eq_u32 s45, 3
s_cselect_b32 s49, 8192, s49
s_cselect_b32 s48, 8, 2048
s_cselect_b32 s30, 1024, 8
v_add_u32 v129, s49, v5
v_add_u32 v130, s48, v129
s_cselect_b32 s48, 2048, 0
s_sub_u32 s48, 4096, s48
v_add_u32 v131, s48, v129
v_add_u32 v132, s48, v130
v_mov_b32 v104, -1
v_mov_b32 v105, -1
v_mov_b32 v106, -1
v_mov_b32 v107, -1
v_mov_b32 v108, -1
)"
R"(v_mov_b32 v109, -1
v_mov_b32 v110, -1
v_mov_b32 v111, -1
v_mov_b32 v112, -1
v_mov_b32 v113, -1
v_mov_b32 v114, -1
v_mov_b32 v115, -1
v_mov_b32 v116, -1
v_mov_b32 v117, -1
v_mov_b32 v118, -1
v_mov_b32 v119, -1
v_mov_b32 v120, -1
v_mov_b32 v121, -1
v_mov_b32 v122, -1
v_mov_b32 v123, -1
v_mov_b32 v124, -1
v_mov_b32 v125, -1
v_mov_b32 v126, -1
v_mov_b32 v127, -1
v_mov_b32 v8, 0
v_add_u32 v10, 6144, v0
s_mov_b32 s65, 32
s_mov_b32 s63, 0x40000000
hw_first_%=:
s_mul_i32 s62, s44, 12288
s_lshl_b32 s49, s45, 10
s_add_u32 s62, s62, s49
s_add_u32 s62, s62, 7764992
s_mov_b32 s31, s62
s_lshl_b32 s49, s47, 7
s_add_u32 s49, s49, 10240
v_mov_b32 v128, s49
v_mov_b32 v14, 0
v_cmp_gt_u32 vcc, s65, v14
s_cbranch_vccz hw_kx0_ok_%=
s_mov_b32 s60, 0
s_nop 2
hw_kx0_poll_%=:
buffer_load_dword v14, v128, s[40:43], s58 offen sc1
s_waitcnt vmcnt(0)
v_cmp_gt_u32 vcc, s65, v14
s_nop 4
s_cbranch_vccz hw_kx0_ok_%=
s_sleep 8
s_add_u32 s60, s60, 1
s_cmp_gt_u32 s60, 65536
s_cbranch_scc1 hw_abort_%=
s_and_b32 s59, s60, 63
s_cbranch_scc1 hw_kx0_poll_%=
v_mov_b32 v13, 1024
buffer_load_dword v13, v13, s[40:43], s58 offen sc1
s_waitcnt vmcnt(0)
v_readfirstlane_b32 s59, v13
s_nop 0
s_cmp_lg_u32 s59, 0
s_nop 2
s_cbranch_scc1 hw_end_%=
s_branch hw_kx0_poll_%=
hw_kx0_ok_%=:
buffer_load_dwordx4 v[136:139], v0, s[40:43], s62 offen sc1
buffer_load_dwordx4 v[140:143], v0, s[40:43], s62 offen offset:3072 sc1
buffer_load_dwordx4 v[144:147], v10, s[40:43], s62 offen sc1
buffer_load_dwordx4 v[148:151], v10, s[40:43], s62 offen offset:3072 sc1
v_add_u32 v9, s30, v129
s_waitcnt vmcnt(3)
ds_write_b64 v129, v[136:137]
ds_write_b64 v9, v[138:139]
v_add_u32 v9, s30, v130
s_waitcnt vmcnt(2)
ds_write_b64 v130, v[140:141]
ds_write_b64 v9, v[142:143]
v_add_u32 v9, s30, v131
s_waitcnt vmcnt(1)
ds_write_b64 v131, v[144:145]
ds_write_b64 v9, v[146:147]
v_add_u32 v9, s30, v132
s_waitcnt vmcnt(0)
ds_write_b64 v132, v[148:149]
ds_write_b64 v9, v[150:151]
v_add_u32 v128, 128, v128
v_mov_b32 v14, 0
s_nop 1
hw_loopb_%=:
s_add_u32 s59, s47, 0
s_and_b32 s59, s59, 63
s_cmp_eq_u32 s59, 31
s_cselect_b32 s59, -31, 1
s_mul_i32 s57, s59, 393216
s_add_u32 s31, s31, s57
v_cmp_gt_u32 vcc, s65, v14
s_nop 0
s_cmp_eq_u64 vcc, 0
s_cselect_b32 s62, s31, 0x40000000
v_mov_b32 v134, v128
v_add_u32 v128, 128, v128
s_and_b32 s48, s47, 3
s_mul_i32 s53, s48, 196608
s_lshl_b32 s50, s48, 2
s_add_u32 s50, s50, s46
s_mul_i32 s50, s50, 32768
s_add_u32 s50, s50, 786432
v_add_u32 v158, s50, v0
s_add_u32 s50, s53, 49152
s_add_u32 s51, s53, 98304
s_add_u32 s52, s53, 147456
v_mov_b32 v56, 0
v_mov_b32 v57, 0
v_mov_b32 v58, 0
v_mov_b32 v59, 0
v_mov_b32 v60, 0
v_mov_b32 v61, 0
v_mov_b32 v62, 0
v_mov_b32 v63, 0
v_mov_b32 v64, 0
v_mov_b32 v65, 0
v_mov_b32 v66, 0
v_mov_b32 v67, 0
v_mov_b32 v68, 0
v_mov_b32 v69, 0
v_mov_b32 v70, 0
v_mov_b32 v71, 0
v_mov_b32 v72, 0
v_mov_b32 v73, 0
v_mov_b32 v74, 0
v_mov_b32 v75, 0
v_mov_b32 v76, 0
v_mov_b32 v77, 0
v_mov_b32 v78, 0
v_mov_b32 v79, 0
ds_read_b128 v[80:83], v2 offset:0
ds_read_b128 v[84:87], v2 offset:1024
ds_read_b128 v[88:91], v2 offset:2048
ds_read_b128 v[92:95], v2 offset:3072
ds_read_b128 v[96:99], v2 offset:4096
ds_read_b128 v[100:103], v2 offset:5120
s_xor_b32 s48, s48, 2
s_mul_i32 s54, s48, 196608
s_and_b32 s63, s47, 31
s_lshl_b32 s63, s63, 5
s_add_u32 s63, s63, s44
s_mul_i32 s63, s63, 3072
s_add_u32 s63, s63, 127336448
s_cmp_eq_u32 s45, 1
s_cselect_b32 s63, s63, 0x40000000
s_movk_i32 s49, 512
s_cmp_eq_u32 s45, 2
s_cselect_b32 s48, 49152, s49
s_cselect_b32 s34, s54, s63
s_cselect_b32 s49, 0x40000000, 0
s_add_u32 s35, s34, s48
s_add_u32 s36, s35, s48
s_add_u32 s37, s36, s48
s_add_u32 s38, s37, s48
s_add_u32 s39, s38, s48
s_or_b32 s38, s38, s49
s_or_b32 s39, s39, s49
s_waitcnt lgkmcnt(0)
s_barrier
s_mov_b32 s60, 0
hw_retryb_%=:
buffer_load_dwordx4 v[16:19], v158, s[40:43], s20 offen sc1
s_nop 7
buffer_load_dwordx4 v[20:23], v158, s[40:43], s21 offen sc1
s_nop 7
buffer_load_dwordx4 v[24:27], v158, s[40:43], s22 offen sc1
s_nop 7
buffer_load_dwordx4 v[28:31], v158, s[40:43], s23 offen sc1
s_nop 7
buffer_load_dwordx4 v[32:35], v158, s[40:43], s24 offen sc1
s_nop 7
buffer_load_dwordx4 v[36:39], v158, s[40:43], s25 offen sc1
s_nop 7
buffer_load_dwordx4 v[40:43], v158, s[40:43], s26 offen sc1
s_nop 7
buffer_load_dwordx4 v[44:47], v158, s[40:43], s27 offen sc1
s_nop 7
buffer_load_dwordx4 v[48:51], v158, s[40:43], s28 offen sc1
s_nop 7
buffer_load_dwordx4 v[52:55], v158, s[40:43], s29 offen sc1
s_waitcnt vmcnt(9)
v_smfmac_f32_16x16x64_bf16 v[68:71], v[16:19], v[80:87], v1
ds_read_b128 v[80:83], v2 offset:6144
v_smfmac_f32_16x16x64_bf16 v[56:59], v[16:19], a[0:7], v1
ds_read_b128 v[84:87], v2 offset:7168
v_smfmac_f32_16x16x64_bf16 v[64:67], v[16:19], a[8:15], v1
v_smfmac_f32_16x16x64_bf16 v[60:63], v[16:19], a[16:23], v1
buffer_store_dword v8, v133, s[40:43], s64 offen sc1
s_waitcnt vmcnt(9)
v_smfmac_f32_16x16x64_bf16 v[68:71], v[20:23], v[88:95], v1
ds_read_b128 v[88:91], v2 offset:8192
v_smfmac_f32_16x16x64_bf16 v[56:59], v[20:23], a[24:31], v1
ds_read_b128 v[92:95], v2 offset:9216
v_smfmac_f32_16x16x64_bf16 v[64:67], v[20:23], v[96:103], v1
ds_read_b128 v[96:99], v2 offset:10240
v_smfmac_f32_16x16x64_bf16 v[60:63], v[20:23], a[32:39], v1
ds_read_b128 v[100:103], v2 offset:11264
s_waitcnt vmcnt(8) lgkmcnt(4)
v_smfmac_f32_16x16x64_bf16 v[68:71], v[24:27], v[80:87], v1
ds_read_b128 v[80:83], v2 offset:12288
v_smfmac_f32_16x16x64_bf16 v[56:59], v[24:27], a[40:47], v1
)"
R"(ds_read_b128 v[84:87], v2 offset:13312
v_smfmac_f32_16x16x64_bf16 v[64:67], v[24:27], a[48:55], v1
v_smfmac_f32_16x16x64_bf16 v[60:63], v[24:27], a[56:63], v1
v_max3_u32 v12, v16, v20, v24
s_waitcnt vmcnt(7) lgkmcnt(4)
v_smfmac_f32_16x16x64_bf16 v[68:71], v[28:31], v[88:95], v1
ds_read_b128 v[88:91], v2 offset:14336
v_smfmac_f32_16x16x64_bf16 v[56:59], v[28:31], a[64:71], v1
ds_read_b128 v[92:95], v2 offset:15360
v_smfmac_f32_16x16x64_bf16 v[64:67], v[28:31], a[72:79], v1
v_cmp_eq_u32 vcc, -1, v12
v_smfmac_f32_16x16x64_bf16 v[60:63], v[28:31], a[80:87], v1
s_cbranch_vccnz hw_pollb_%=
s_waitcnt vmcnt(6) lgkmcnt(4)
v_smfmac_f32_16x16x64_bf16 v[68:71], v[32:35], v[96:103], v1
ds_read_b128 v[96:99], v2 offset:16384
v_smfmac_f32_16x16x64_bf16 v[56:59], v[32:35], a[88:95], v1
ds_read_b128 v[100:103], v2 offset:17408
s_waitcnt lgkmcnt(4)
v_smfmac_f32_16x16x64_bf16 v[64:67], v[32:35], v[80:87], v1
ds_read_b128 v[80:83], v2 offset:18432
v_smfmac_f32_16x16x64_bf16 v[60:63], v[32:35], a[96:103], v1
ds_read_b128 v[84:87], v2 offset:19456
s_waitcnt vmcnt(5) lgkmcnt(4)
v_smfmac_f32_16x16x64_bf16 v[68:71], v[36:39], v[88:95], v1
ds_read_b128 v[88:91], v2 offset:20480
v_smfmac_f32_16x16x64_bf16 v[56:59], v[36:39], a[104:111], v1
ds_read_b128 v[92:95], v2 offset:21504
v_smfmac_f32_16x16x64_bf16 v[64:67], v[36:39], a[112:119], v1
buffer_load_dwordx4 v[136:139], v0, s[40:43], s62 offen offset:0 sc1
v_smfmac_f32_16x16x64_bf16 v[60:63], v[36:39], a[120:127], v1
buffer_load_dwordx4 v[140:143], v0, s[40:43], s62 offen offset:3072 sc1
s_waitcnt vmcnt(6) lgkmcnt(4)
v_smfmac_f32_16x16x64_bf16 v[68:71], v[40:43], v[96:103], v1
ds_read_b128 v[96:99], v2 offset:22528
v_smfmac_f32_16x16x64_bf16 v[56:59], v[40:43], a[128:135], v1
ds_read_b128 v[100:103], v2 offset:23552
v_smfmac_f32_16x16x64_bf16 v[64:67], v[40:43], a[136:143], v1
buffer_load_dwordx4 v[144:147], v10, s[40:43], s62 offen offset:0 sc1
v_smfmac_f32_16x16x64_bf16 v[60:63], v[40:43], a[144:151], v1
buffer_load_dwordx4 v[148:151], v10, s[40:43], s62 offen offset:3072 sc1
s_waitcnt vmcnt(7) lgkmcnt(4)
v_smfmac_f32_16x16x64_bf16 v[68:71], v[44:47], v[80:87], v1
ds_read_b128 v[80:83], v2 offset:24576
v_smfmac_f32_16x16x64_bf16 v[56:59], v[44:47], a[152:159], v1
ds_read_b128 v[84:87], v2 offset:25600
s_waitcnt lgkmcnt(4)
v_smfmac_f32_16x16x64_bf16 v[64:67], v[44:47], v[88:95], v1
ds_read_b128 v[88:91], v2 offset:26624
v_smfmac_f32_16x16x64_bf16 v[60:63], v[44:47], a[160:167], v1
ds_read_b128 v[92:95], v2 offset:27648
s_waitcnt vmcnt(6) lgkmcnt(4)
v_smfmac_f32_16x16x64_bf16 v[68:71], v[48:51], v[96:103], v1
ds_read_b128 v[96:99], v2 offset:28672
v_smfmac_f32_16x16x64_bf16 v[56:59], v[48:51], a[168:175], v1
ds_read_b128 v[100:103], v2 offset:29696
v_smfmac_f32_16x16x64_bf16 v[64:67], v[48:51], a[176:183], v1
buffer_load_dword v14, v128, s[40:43], s58 offen sc1
v_smfmac_f32_16x16x64_bf16 v[60:63], v[48:51], a[184:191], v1
s_waitcnt vmcnt(6) lgkmcnt(4)
v_smfmac_f32_16x16x64_bf16 v[68:71], v[52:55], v[80:87], v1
ds_read_b128 v[80:83], v2 offset:30720
v_smfmac_f32_16x16x64_bf16 v[56:59], v[52:55], a[192:199], v1
ds_read_b128 v[84:87], v2 offset:31744
v_smfmac_f32_16x16x64_bf16 v[64:67], v[52:55], a[200:207], v1
v_smfmac_f32_16x16x64_bf16 v[60:63], v[52:55], a[208:215], v1
v_smfmac_f32_16x16x64_bf16 v[72:75], v[16:19], a[216:223], v1
v_mov_b32 v13, v13
v_mov_b32 v13, v13
v_mov_b32 v13, v13
v_mov_b32 v13, v13
v_mov_b32 v13, v13
v_mov_b32 v13, v13
v_mov_b32 v13, v13
v_permlane32_swap_b32 v56, v60
v_permlane32_swap_b32 v57, v61
s_waitcnt lgkmcnt(4)
v_smfmac_f32_16x16x64_bf16 v[76:79], v[16:19], v[88:95], v1
v_permlane32_swap_b32 v58, v62
v_permlane32_swap_b32 v59, v63
v_smfmac_f32_16x16x64_bf16 v[72:75], v[20:23], a[224:231], v1
v_add_f32 v56, v56, v60
v_add_f32 v57, v57, v61
s_waitcnt lgkmcnt(2)
v_smfmac_f32_16x16x64_bf16 v[76:79], v[20:23], v[96:103], v1
v_add_f32 v58, v58, v62
v_add_f32 v59, v59, v63
v_smfmac_f32_16x16x64_bf16 v[72:75], v[24:27], a[232:239], v1
v_cmp_u_f32 vcc, v56, v57
v_cmp_u_f32 s[56:57], v58, v59
s_waitcnt lgkmcnt(0)
v_smfmac_f32_16x16x64_bf16 v[76:79], v[24:27], v[80:87], v1
s_or_b64 vcc, vcc, s[56:57]
s_cbranch_vccnz hw_failb_%=
hw_resumeb_%=:
v_cvt_pk_bf16_f32 v60, v56, v57
v_smfmac_f32_16x16x64_bf16 v[72:75], v[28:31], a[240:247], v1
v_cvt_pk_bf16_f32 v61, v58, v59
ds_write_b64 v3, v[60:61] offset:0
v_smfmac_f32_16x16x64_bf16 v[76:79], v[28:31], a[248:255], v1
v_permlane32_swap_b32 v64, v68
v_permlane32_swap_b32 v65, v69
v_smfmac_f32_16x16x64_bf16 v[72:75], v[32:35], v[160:167], v1
v_permlane32_swap_b32 v66, v70
v_permlane32_swap_b32 v67, v71
v_smfmac_f32_16x16x64_bf16 v[76:79], v[32:35], v[168:175], v1
v_add_f32 v64, v64, v68
v_smfmac_f32_16x16x64_bf16 v[72:75], v[36:39], v[176:183], v1
v_add_f32 v65, v65, v69
v_smfmac_f32_16x16x64_bf16 v[76:79], v[36:39], v[184:191], v1
v_add_f32 v66, v66, v70
v_smfmac_f32_16x16x64_bf16 v[72:75], v[40:43], v[192:199], v1
v_add_f32 v67, v67, v71
v_smfmac_f32_16x16x64_bf16 v[76:79], v[40:43], v[200:207], v1
v_cvt_pk_bf16_f32 v68, v64, v65
v_smfmac_f32_16x16x64_bf16 v[72:75], v[44:47], v[208:215], v1
v_cvt_pk_bf16_f32 v69, v66, v67
v_smfmac_f32_16x16x64_bf16 v[76:79], v[44:47], v[216:223], v1
ds_write_b64 v3, v[68:69] offset:512
v_smfmac_f32_16x16x64_bf16 v[72:75], v[48:51], v[224:231], v1
v_smfmac_f32_16x16x64_bf16 v[76:79], v[48:51], v[232:239], v1
v_smfmac_f32_16x16x64_bf16 v[72:75], v[52:55], v[240:247], v1
v_smfmac_f32_16x16x64_bf16 v[76:79], v[52:55], v[248:255], v1
s_waitcnt lgkmcnt(0)
s_barrier
v_mov_b32 v13, v13
v_mov_b32 v13, v13
v_mov_b32 v13, v13
v_mov_b32 v13, v13
v_mov_b32 v13, v13
v_mov_b32 v13, v13
v_permlane32_swap_b32 v72, v76
v_permlane32_swap_b32 v73, v77
v_permlane32_swap_b32 v74, v78
v_permlane32_swap_b32 v75, v79
v_pk_add_f32 v[72:73], v[72:73], v[76:77]
v_pk_add_f32 v[74:75], v[74:75], v[78:79]
v_cvt_pk_bf16_f32 v76, v72, v73
v_cvt_pk_bf16_f32 v77, v74, v75
ds_write_b64 v3, v[76:77] offset:1024
s_waitcnt lgkmcnt(0)
s_barrier
s_cmp_lg_u32 s45, 1
s_cbranch_scc1 hw_n1_%=
ds_read_b64_tr_b16 v[104:105], v4 offset:0
ds_read_b64_tr_b16 v[106:107], v4 offset:512
ds_read_b64_tr_b16 v[108:109], v4 offset:1536
ds_read_b64_tr_b16 v[110:111], v4 offset:2048
ds_read_b64_tr_b16 v[112:113], v4 offset:3072
ds_read_b64_tr_b16 v[114:115], v4 offset:3584
ds_read_b64_tr_b16 v[116:117], v4 offset:4608
ds_read_b64_tr_b16 v[118:119], v4 offset:5120
ds_read_b64_tr_b16 v[120:121], v4 offset:1024
ds_read_b64_tr_b16 v[122:123], v4 offset:2560
ds_read_b64_tr_b16 v[124:125], v4 offset:4096
ds_read_b64_tr_b16 v[126:127], v4 offset:5632
s_waitcnt lgkmcnt(0)
hw_n1_%=:
buffer_store_dwordx4 v[104:107], v6, s[40:43], s34 offen sc1
buffer_store_dwordx4 v[108:111], v6, s[40:43], s35 offen sc1
buffer_store_dwordx4 v[112:115], v6, s[40:43], s36 offen sc1
buffer_store_dwordx4 v[116:119], v6, s[40:43], s37 offen sc1
buffer_store_dwordx4 v[120:123], v6, s[40:43], s38 offen sc1
buffer_store_dwordx4 v[124:127], v6, s[40:43], s39 offen sc1
v_add_u32 v9, s30, v129
s_waitcnt vmcnt(10)
ds_write_b64 v129, v[136:137]
ds_write_b64 v9, v[138:139]
v_add_u32 v9, s30, v130
s_waitcnt vmcnt(9)
ds_write_b64 v130, v[140:141]
ds_write_b64 v9, v[142:143]
v_add_u32 v9, s30, v131
s_waitcnt vmcnt(8)
ds_write_b64 v131, v[144:145]
ds_write_b64 v9, v[146:147]
v_add_u32 v9, s30, v132
s_waitcnt vmcnt(7)
ds_write_b64 v132, v[148:149]
ds_write_b64 v9, v[150:151]
s_add_u32 s49, s47, 1
v_mov_b32 v8, s49
s_waitcnt vmcnt(6)
v_add_u32 v3, s61, v3
v_add_u32 v4, s61, v4
s_sub_u32 s61, 0, s61
s_add_u32 s47, s47, 1
s_and_b32 s59, s47, 63
s_cbranch_scc0 hw_flush_%=
s_cmp_eq_u32 s62, s31
s_cbranch_scc1 hw_loopb_%=
v_mov_b32 v135, 0
v_cmp_gt_u32 vcc, s65, v135
s_cbranch_vccz hw_kx1_ok_%=
s_mov_b32 s60, 0
s_nop 2
hw_kx1_poll_%=:
buffer_load_dword v135, v134, s[40:43], s58 offen sc1
s_waitcnt vmcnt(0)
v_cmp_gt_u32 vcc, s65, v135
)"
R"(s_nop 4
s_cbranch_vccz hw_kx1_ok_%=
s_sleep 4
s_add_u32 s60, s60, 1
s_cmp_gt_u32 s60, 65536
s_cbranch_scc1 hw_abort_%=
s_and_b32 s59, s60, 63
s_cbranch_scc1 hw_kx1_poll_%=
v_mov_b32 v13, 1024
buffer_load_dword v13, v13, s[40:43], s58 offen sc1
s_waitcnt vmcnt(0)
v_readfirstlane_b32 s59, v13
s_nop 0
s_cmp_lg_u32 s59, 0
s_nop 2
s_cbranch_scc1 hw_end_%=
s_branch hw_kx1_poll_%=
hw_kx1_ok_%=:
buffer_load_dwordx4 v[136:139], v0, s[40:43], s31 offen sc1
buffer_load_dwordx4 v[140:143], v0, s[40:43], s31 offen offset:3072 sc1
buffer_load_dwordx4 v[144:147], v10, s[40:43], s31 offen sc1
buffer_load_dwordx4 v[148:151], v10, s[40:43], s31 offen offset:3072 sc1
v_add_u32 v9, s30, v129
s_waitcnt vmcnt(3)
ds_write_b64 v129, v[136:137]
ds_write_b64 v9, v[138:139]
v_add_u32 v9, s30, v130
s_waitcnt vmcnt(2)
ds_write_b64 v130, v[140:141]
ds_write_b64 v9, v[142:143]
v_add_u32 v9, s30, v131
s_waitcnt vmcnt(1)
ds_write_b64 v131, v[144:145]
ds_write_b64 v9, v[146:147]
v_add_u32 v9, s30, v132
s_waitcnt vmcnt(0)
ds_write_b64 v132, v[148:149]
ds_write_b64 v9, v[150:151]
s_branch hw_loopb_%=
hw_flush_%=:
s_waitcnt vmcnt(0)
buffer_store_dword v8, v133, s[40:43], s64 offen sc1
s_cmp_lt_u32 s47, 128
s_nop 0
s_cbranch_scc1 hw_first_%=
s_branch hw_end_%=
hw_failb_%=:
v_max_u32 v12, v16, v17
v_max3_u32 v12, v18, v19, v12
v_max3_u32 v12, v20, v21, v12
v_max3_u32 v12, v22, v23, v12
v_max3_u32 v12, v24, v25, v12
v_max3_u32 v12, v26, v27, v12
v_max3_u32 v12, v28, v29, v12
v_max3_u32 v12, v30, v31, v12
v_max3_u32 v12, v32, v33, v12
v_max3_u32 v12, v34, v35, v12
v_max3_u32 v12, v36, v37, v12
v_max3_u32 v12, v38, v39, v12
v_max3_u32 v12, v40, v41, v12
v_max3_u32 v12, v42, v43, v12
v_max3_u32 v12, v44, v45, v12
v_max3_u32 v12, v46, v47, v12
v_max3_u32 v12, v48, v49, v12
v_max3_u32 v12, v50, v51, v12
v_max3_u32 v12, v52, v53, v12
v_max3_u32 v12, v54, v55, v12
v_cmp_eq_u32 vcc, -1, v12
s_nop 4
s_cbranch_vccz hw_resumeb_%=
hw_pollb_%=:
s_nop 7
s_waitcnt vmcnt(0) lgkmcnt(0)
v_mov_b32 v56, 0
v_mov_b32 v57, 0
v_mov_b32 v58, 0
v_mov_b32 v59, 0
v_mov_b32 v60, 0
v_mov_b32 v61, 0
v_mov_b32 v62, 0
v_mov_b32 v63, 0
v_mov_b32 v64, 0
v_mov_b32 v65, 0
v_mov_b32 v66, 0
v_mov_b32 v67, 0
v_mov_b32 v68, 0
v_mov_b32 v69, 0
v_mov_b32 v70, 0
v_mov_b32 v71, 0
v_mov_b32 v72, 0
v_mov_b32 v73, 0
v_mov_b32 v74, 0
v_mov_b32 v75, 0
v_mov_b32 v76, 0
v_mov_b32 v77, 0
v_mov_b32 v78, 0
v_mov_b32 v79, 0
ds_read_b128 v[80:83], v2 offset:0
ds_read_b128 v[84:87], v2 offset:1024
ds_read_b128 v[88:91], v2 offset:2048
ds_read_b128 v[92:95], v2 offset:3072
ds_read_b128 v[96:99], v2 offset:4096
ds_read_b128 v[100:103], v2 offset:5120
v_sub_u32 v12, v158, v0
v_lshl_add_u32 v12, v15, 7, v12
v_and_b32 v18, 15, v15
v_sub_u32 v18, v18, v15
v_lshl_add_u32 v18, v18, 7, v12
s_movk_i32 s55, 0x4000
s_waitcnt vmcnt(0) lgkmcnt(0)
s_add_u32 s60, s60, 1
s_cmp_gt_u32 s60, 65536
s_cbranch_scc1 hw_abort_%=
s_and_b32 s59, s60, 63
s_cbranch_scc1 hw_againb_%=
v_mov_b32 v13, 1024
buffer_load_dword v13, v13, s[40:43], s58 offen sc1
s_waitcnt vmcnt(0)
v_readfirstlane_b32 s59, v13
s_nop 0
s_cmp_lg_u32 s59, 0
s_nop 2
s_cbranch_scc1 hw_end_%=
s_branch hw_againb_%=
hw_againb_%=:
buffer_load_dword v16, v12, s[40:43], 0 offen sc1
buffer_load_dword v17, v18, s[40:43], s55 offen sc1
s_waitcnt vmcnt(0)
v_max_u32 v16, v16, v17
v_cmp_eq_u32 vcc, -1, v16
s_nop 4
s_cbranch_vccz hw_retryb_%=
s_add_u32 s60, s60, 1
s_cmp_gt_u32 s60, 65536
s_cbranch_scc1 hw_abort_%=
s_and_b32 s59, s60, 63
s_cbranch_scc1 hw_againb_%=
v_mov_b32 v13, 1024
buffer_load_dword v13, v13, s[40:43], s58 offen sc1
s_waitcnt vmcnt(0)
v_readfirstlane_b32 s59, v13
s_nop 0
s_cmp_lg_u32 s59, 0
s_nop 2
s_cbranch_scc1 hw_end_%=
s_branch hw_againb_%=
s_branch hw_end_%=
hw_abort_%=:
v_mov_b32 v13, 1
v_mov_b32 v14, 1024
buffer_store_dword v13, v14, s[40:43], s58 offen sc1
hw_end_%=:
s_endpgm
)"
    :: CHAIN_INPUTS : CHAIN_CLOBBERS);
    __builtin_unreachable();
}
DEV void role_col(unsigned lo, unsigned hi, int id, int w, int aux, const Tune& tn, const void* wa, const void* wb, int bid) {
    asm volatile(
R"(s_mov_b32 s40, %0
s_mov_b32 s41, %1
s_mov_b32 s42, %2
s_mov_b32 s43, %3
s_mov_b32 s44, %4
s_mov_b32 s45, %5
s_mov_b32 s46, %6
s_mov_b32 s66, %7
s_mov_b32 s67, %8
s_mov_b32 s68, %9
s_mov_b32 s69, %10
s_mov_b32 s70, %11
s_mov_b32 s71, %12
s_mov_b32 s72, %13
s_mov_b32 s73, %14
s_mov_b32 s76, %15
s_mov_b32 s77, %16
s_mov_b32 s78, 7864320
s_mov_b32 s79, 0x20000
s_mov_b32 s80, %17
s_mov_b32 s81, %18
s_mov_b32 s82, 7864320
s_mov_b32 s83, 0x20000
s_mov_b32 s84, %19
s_mov_b64 exec, -1
v_mbcnt_lo_u32_b32 v15, -1, 0
v_mbcnt_hi_u32_b32 v15, -1, v15
v_lshlrev_b32 v0, 4, v15
s_mov_b32 s58, 2097152
s_mov_b32 s92, 0
s_lshr_b32 s85, s84, 2
s_and_b32 s86, s84, 3
v_add_u32 v26, 0, v15
v_mul_u32_u24 v27, 2731, v26
v_lshrrev_b32 v27, 16, v27
v_mul_u32_u24 v16, 11904, v27
v_lshl_add_u32 v16, v26, 4, v16
v_add_u32 v26, v26, v27
s_mul_i32 s48, s45, 3200
v_lshl_add_u32 v19, v26, 4, s48
v_add_u32 v26, 64, v15
v_mul_u32_u24 v27, 2731, v26
v_lshrrev_b32 v27, 16, v27
v_mul_u32_u24 v17, 11904, v27
v_lshl_add_u32 v17, v26, 4, v17
v_add_u32 v26, v26, v27
s_mul_i32 s48, s45, 3200
v_lshl_add_u32 v20, v26, 4, s48
v_add_u32 v26, 128, v15
v_mul_u32_u24 v27, 2731, v26
v_lshrrev_b32 v27, 16, v27
v_mul_u32_u24 v18, 11904, v27
v_lshl_add_u32 v18, v26, 4, v18
v_add_u32 v26, v26, v27
s_mul_i32 s48, s45, 3200
v_lshl_add_u32 v21, v26, 4, s48
s_mul_i32 s48, s86, 160
s_lshl3_add_u32 s48, s45, s48
s_mul_i32 s48, s48, 12288
s_mul_i32 s49, s85, 384
s_add_u32 s48, s48, s49
buffer_load_dwordx4 v[32:35], v16, s[80:83], s48 offen
buffer_load_dwordx4 v[36:39], v17, s[80:83], s48 offen
buffer_load_dwordx4 v[40:43], v18, s[80:83], s48 offen
s_add_u32 s48, s48, 393216
buffer_load_dwordx4 v[44:47], v16, s[80:83], s48 offen
buffer_load_dwordx4 v[48:51], v17, s[80:83], s48 offen
buffer_load_dwordx4 v[52:55], v18, s[80:83], s48 offen
s_add_u32 s48, s48, 393216
buffer_load_dwordx4 v[56:59], v16, s[80:83], s48 offen
buffer_load_dwordx4 v[60:63], v17, s[80:83], s48 offen
buffer_load_dwordx4 v[64:67], v18, s[80:83], s48 offen
s_add_u32 s48, s48, 393216
buffer_load_dwordx4 v[68:71], v16, s[80:83], s48 offen
buffer_load_dwordx4 v[72:75], v17, s[80:83], s48 offen
buffer_load_dwordx4 v[76:79], v18, s[80:83], s48 offen
s_add_u32 s48, s48, 393216
buffer_load_dwordx4 v[80:83], v16, s[80:83], s48 offen
buffer_load_dwordx4 v[84:87], v17, s[80:83], s48 offen
buffer_load_dwordx4 v[88:91], v18, s[80:83], s48 offen
s_add_u32 s48, s48, 393216
v_lshrrev_b32 v28, 2, v15
v_mul_u32_u24 v28, 1280, v28
v_and_b32 v29, 3, v15
v_lshl_add_u32 v28, v29, 3, v28
s_mul_i32 s48, s45, 320
v_add_u32 v28, s48, v28
v_add_u32 v29, 40960, v28
v_add_u32 v30, 81920, v28
s_mul_i32 s51, s45, 10240
s_mul_i32 s50, s85, 245760
s_add_u32 s50, s50, s51
s_lshr_b32 s52, s45, 1
s_mul_i32 s52, s52, 40960
s_add_u32 s52, s52, s50
s_add_u32 m0, s51, 0
s_add_u32 s49, s50, 0
buffer_load_dwordx4 v0, s[76:79], s49 offen offset:0 lds
buffer_load_dwordx4 v0, s[76:79], s49 offen offset:1024 lds
buffer_load_dwordx4 v0, s[76:79], s49 offen offset:2048 lds
buffer_load_dwordx4 v0, s[76:79], s49 offen offset:3072 lds
s_add_u32 m0, s51, 4096
s_add_u32 s49, s50, 4096
buffer_load_dwordx4 v0, s[76:79], s49 offen offset:0 lds
buffer_load_dwordx4 v0, s[76:79], s49 offen offset:1024 lds
buffer_load_dwordx4 v0, s[76:79], s49 offen offset:2048 lds
buffer_load_dwordx4 v0, s[76:79], s49 offen offset:3072 lds
s_add_u32 m0, s51, 8192
s_add_u32 s49, s50, 8192
buffer_load_dwordx4 v0, s[76:79], s49 offen offset:0 lds
buffer_load_dwordx4 v0, s[76:79], s49 offen offset:1024 lds
s_add_u32 m0, s51, 40960
s_add_u32 s49, s50, 61440
buffer_load_dwordx4 v0, s[76:79], s49 offen offset:0 lds
buffer_load_dwordx4 v0, s[76:79], s49 offen offset:1024 lds
buffer_load_dwordx4 v0, s[76:79], s49 offen offset:2048 lds
buffer_load_dwordx4 v0, s[76:79], s49 offen offset:3072 lds
s_add_u32 m0, s51, 45056
s_add_u32 s49, s50, 65536
buffer_load_dwordx4 v0, s[76:79], s49 offen offset:0 lds
buffer_load_dwordx4 v0, s[76:79], s49 offen offset:1024 lds
buffer_load_dwordx4 v0, s[76:79], s49 offen offset:2048 lds
buffer_load_dwordx4 v0, s[76:79], s49 offen offset:3072 lds
s_add_u32 m0, s51, 49152
s_add_u32 s49, s50, 69632
buffer_load_dwordx4 v0, s[76:79], s49 offen offset:0 lds
buffer_load_dwordx4 v0, s[76:79], s49 offen offset:1024 lds
s_waitcnt vmcnt(10) lgkmcnt(0)
s_barrier
s_add_u32 m0, s51, 81920
s_add_u32 s49, s50, 122880
buffer_load_dwordx4 v0, s[76:79], s49 offen offset:0 lds
buffer_load_dwordx4 v0, s[76:79], s49 offen offset:1024 lds
buffer_load_dwordx4 v0, s[76:79], s49 offen offset:2048 lds
buffer_load_dwordx4 v0, s[76:79], s49 offen offset:3072 lds
s_add_u32 m0, s51, 86016
s_add_u32 s49, s50, 126976
buffer_load_dwordx4 v0, s[76:79], s49 offen offset:0 lds
buffer_load_dwordx4 v0, s[76:79], s49 offen offset:1024 lds
buffer_load_dwordx4 v0, s[76:79], s49 offen offset:2048 lds
buffer_load_dwordx4 v0, s[76:79], s49 offen offset:3072 lds
s_add_u32 m0, s51, 90112
s_add_u32 s49, s50, 131072
buffer_load_dwordx4 v0, s[76:79], s49 offen offset:0 lds
buffer_load_dwordx4 v0, s[76:79], s49 offen offset:1024 lds
ds_read_b64_tr_b16 a[0:1], v28 offset:0
ds_read_b64_tr_b16 a[2:3], v28 offset:20480
ds_read_b64_tr_b16 a[24:25], v28 offset:32
ds_read_b64_tr_b16 a[26:27], v28 offset:20512
ds_read_b64_tr_b16 a[48:49], v28 offset:64
ds_read_b64_tr_b16 a[50:51], v28 offset:20544
ds_read_b64_tr_b16 a[72:73], v28 offset:96
ds_read_b64_tr_b16 a[74:75], v28 offset:20576
ds_read_b64_tr_b16 a[96:97], v28 offset:128
ds_read_b64_tr_b16 a[98:99], v28 offset:20608
ds_read_b64_tr_b16 a[120:121], v28 offset:160
ds_read_b64_tr_b16 a[122:123], v28 offset:20640
ds_read_b64_tr_b16 a[144:145], v28 offset:192
ds_read_b64_tr_b16 a[146:147], v28 offset:20672
ds_read_b64_tr_b16 a[168:169], v28 offset:224
ds_read_b64_tr_b16 a[170:171], v28 offset:20704
s_waitcnt lgkmcnt(14)
ds_read_b64_tr_b16 a[192:193], v28 offset:256
ds_read_b64_tr_b16 a[194:195], v28 offset:20736
s_waitcnt lgkmcnt(14)
ds_read_b64_tr_b16 a[216:217], v28 offset:288
ds_read_b64_tr_b16 a[218:219], v28 offset:20768
s_waitcnt lgkmcnt(14)
s_waitcnt vmcnt(10) lgkmcnt(0)
s_barrier
s_add_u32 m0, s51, 0
s_add_u32 s49, s50, 184320
buffer_load_dwordx4 v0, s[76:79], s49 offen offset:0 lds
buffer_load_dwordx4 v0, s[76:79], s49 offen offset:1024 lds
buffer_load_dwordx4 v0, s[76:79], s49 offen offset:2048 lds
buffer_load_dwordx4 v0, s[76:79], s49 offen offset:3072 lds
s_add_u32 m0, s51, 4096
s_add_u32 s49, s50, 188416
buffer_load_dwordx4 v0, s[76:79], s49 offen offset:0 lds
buffer_load_dwordx4 v0, s[76:79], s49 offen offset:1024 lds
buffer_load_dwordx4 v0, s[76:79], s49 offen offset:2048 lds
buffer_load_dwordx4 v0, s[76:79], s49 offen offset:3072 lds
s_add_u32 m0, s51, 8192
s_add_u32 s49, s50, 192512
buffer_load_dwordx4 v0, s[76:79], s49 offen offset:0 lds
buffer_load_dwordx4 v0, s[76:79], s49 offen offset:1024 lds
ds_read_b64_tr_b16 a[4:5], v29 offset:0
ds_read_b64_tr_b16 a[6:7], v29 offset:20480
ds_read_b64_tr_b16 a[28:29], v29 offset:32
ds_read_b64_tr_b16 a[30:31], v29 offset:20512
ds_read_b64_tr_b16 a[52:53], v29 offset:64
ds_read_b64_tr_b16 a[54:55], v29 offset:20544
ds_read_b64_tr_b16 a[76:77], v29 offset:96
ds_read_b64_tr_b16 a[78:79], v29 offset:20576
ds_read_b64_tr_b16 a[100:101], v29 offset:128
ds_read_b64_tr_b16 a[102:103], v29 offset:20608
ds_read_b64_tr_b16 a[124:125], v29 offset:160
ds_read_b64_tr_b16 a[126:127], v29 offset:20640
ds_read_b64_tr_b16 a[148:149], v29 offset:192
ds_read_b64_tr_b16 a[150:151], v29 offset:20672
)"
R"(ds_read_b64_tr_b16 a[172:173], v29 offset:224
ds_read_b64_tr_b16 a[174:175], v29 offset:20704
s_waitcnt lgkmcnt(14)
ds_read_b64_tr_b16 a[196:197], v29 offset:256
ds_read_b64_tr_b16 a[198:199], v29 offset:20736
s_waitcnt lgkmcnt(14)
ds_read_b64_tr_b16 a[220:221], v29 offset:288
ds_read_b64_tr_b16 a[222:223], v29 offset:20768
s_waitcnt lgkmcnt(14)
s_waitcnt vmcnt(10) lgkmcnt(0)
s_barrier
s_add_u32 m0, s51, 40960
s_add_u32 s49, s52, 40960
buffer_load_dwordx4 v0, s[76:79], s49 offen offset:0 lds
buffer_load_dwordx4 v0, s[76:79], s49 offen offset:1024 lds
buffer_load_dwordx4 v0, s[76:79], s49 offen offset:2048 lds
buffer_load_dwordx4 v0, s[76:79], s49 offen offset:3072 lds
s_add_u32 m0, s51, 45056
s_add_u32 s49, s52, 45056
buffer_load_dwordx4 v0, s[76:79], s49 offen offset:0 lds
buffer_load_dwordx4 v0, s[76:79], s49 offen offset:1024 lds
buffer_load_dwordx4 v0, s[76:79], s49 offen offset:2048 lds
buffer_load_dwordx4 v0, s[76:79], s49 offen offset:3072 lds
s_add_u32 m0, s51, 49152
s_add_u32 s49, s52, 49152
buffer_load_dwordx4 v0, s[76:79], s49 offen offset:0 lds
buffer_load_dwordx4 v0, s[76:79], s49 offen offset:1024 lds
ds_read_b64_tr_b16 a[8:9], v30 offset:0
ds_read_b64_tr_b16 a[10:11], v30 offset:20480
ds_read_b64_tr_b16 a[32:33], v30 offset:32
ds_read_b64_tr_b16 a[34:35], v30 offset:20512
ds_read_b64_tr_b16 a[56:57], v30 offset:64
ds_read_b64_tr_b16 a[58:59], v30 offset:20544
ds_read_b64_tr_b16 a[80:81], v30 offset:96
ds_read_b64_tr_b16 a[82:83], v30 offset:20576
ds_read_b64_tr_b16 a[104:105], v30 offset:128
ds_read_b64_tr_b16 a[106:107], v30 offset:20608
ds_read_b64_tr_b16 a[128:129], v30 offset:160
ds_read_b64_tr_b16 a[130:131], v30 offset:20640
ds_read_b64_tr_b16 a[152:153], v30 offset:192
ds_read_b64_tr_b16 a[154:155], v30 offset:20672
ds_read_b64_tr_b16 a[176:177], v30 offset:224
ds_read_b64_tr_b16 a[178:179], v30 offset:20704
s_waitcnt lgkmcnt(14)
ds_read_b64_tr_b16 a[200:201], v30 offset:256
ds_read_b64_tr_b16 a[202:203], v30 offset:20736
s_waitcnt lgkmcnt(14)
ds_read_b64_tr_b16 a[224:225], v30 offset:288
ds_read_b64_tr_b16 a[226:227], v30 offset:20768
s_waitcnt lgkmcnt(14)
s_waitcnt vmcnt(10) lgkmcnt(0)
s_barrier
s_add_u32 m0, s51, 81920
s_add_u32 s49, s52, 163840
buffer_load_dwordx4 v0, s[76:79], s49 offen offset:0 lds
buffer_load_dwordx4 v0, s[76:79], s49 offen offset:1024 lds
buffer_load_dwordx4 v0, s[76:79], s49 offen offset:2048 lds
buffer_load_dwordx4 v0, s[76:79], s49 offen offset:3072 lds
s_add_u32 m0, s51, 86016
s_add_u32 s49, s52, 167936
buffer_load_dwordx4 v0, s[76:79], s49 offen offset:0 lds
buffer_load_dwordx4 v0, s[76:79], s49 offen offset:1024 lds
buffer_load_dwordx4 v0, s[76:79], s49 offen offset:2048 lds
buffer_load_dwordx4 v0, s[76:79], s49 offen offset:3072 lds
s_add_u32 m0, s51, 90112
s_add_u32 s49, s52, 172032
buffer_load_dwordx4 v0, s[76:79], s49 offen offset:0 lds
buffer_load_dwordx4 v0, s[76:79], s49 offen offset:1024 lds
ds_read_b64_tr_b16 a[12:13], v28 offset:0
ds_read_b64_tr_b16 a[14:15], v28 offset:20480
ds_read_b64_tr_b16 a[36:37], v28 offset:32
ds_read_b64_tr_b16 a[38:39], v28 offset:20512
ds_read_b64_tr_b16 a[60:61], v28 offset:64
ds_read_b64_tr_b16 a[62:63], v28 offset:20544
ds_read_b64_tr_b16 a[84:85], v28 offset:96
ds_read_b64_tr_b16 a[86:87], v28 offset:20576
ds_read_b64_tr_b16 a[108:109], v28 offset:128
ds_read_b64_tr_b16 a[110:111], v28 offset:20608
ds_read_b64_tr_b16 a[132:133], v28 offset:160
ds_read_b64_tr_b16 a[134:135], v28 offset:20640
ds_read_b64_tr_b16 a[156:157], v28 offset:192
ds_read_b64_tr_b16 a[158:159], v28 offset:20672
ds_read_b64_tr_b16 a[180:181], v28 offset:224
ds_read_b64_tr_b16 a[182:183], v28 offset:20704
s_waitcnt lgkmcnt(14)
ds_read_b64_tr_b16 a[204:205], v28 offset:256
ds_read_b64_tr_b16 a[206:207], v28 offset:20736
s_waitcnt lgkmcnt(14)
ds_read_b64_tr_b16 a[228:229], v28 offset:288
ds_read_b64_tr_b16 a[230:231], v28 offset:20768
s_waitcnt lgkmcnt(14)
s_waitcnt vmcnt(10) lgkmcnt(0)
s_barrier
ds_read_b64_tr_b16 a[16:17], v29 offset:0
ds_read_b64_tr_b16 a[18:19], v29 offset:20480
ds_read_b64_tr_b16 a[40:41], v29 offset:32
ds_read_b64_tr_b16 a[42:43], v29 offset:20512
ds_read_b64_tr_b16 a[64:65], v29 offset:64
ds_read_b64_tr_b16 a[66:67], v29 offset:20544
ds_read_b64_tr_b16 a[88:89], v29 offset:96
ds_read_b64_tr_b16 a[90:91], v29 offset:20576
ds_read_b64_tr_b16 a[112:113], v29 offset:128
ds_read_b64_tr_b16 a[114:115], v29 offset:20608
ds_read_b64_tr_b16 a[136:137], v29 offset:160
ds_read_b64_tr_b16 a[138:139], v29 offset:20640
ds_read_b64_tr_b16 a[160:161], v29 offset:192
ds_read_b64_tr_b16 a[162:163], v29 offset:20672
ds_read_b64_tr_b16 a[184:185], v29 offset:224
ds_read_b64_tr_b16 a[186:187], v29 offset:20704
s_waitcnt lgkmcnt(14)
ds_read_b64_tr_b16 a[208:209], v29 offset:256
ds_read_b64_tr_b16 a[210:211], v29 offset:20736
s_waitcnt lgkmcnt(14)
ds_read_b64_tr_b16 a[232:233], v29 offset:288
ds_read_b64_tr_b16 a[234:235], v29 offset:20768
s_waitcnt lgkmcnt(14)
s_waitcnt vmcnt(0) lgkmcnt(0)
s_barrier
ds_read_b64_tr_b16 a[20:21], v30 offset:0
ds_read_b64_tr_b16 a[22:23], v30 offset:20480
ds_read_b64_tr_b16 a[44:45], v30 offset:32
ds_read_b64_tr_b16 a[46:47], v30 offset:20512
ds_read_b64_tr_b16 a[68:69], v30 offset:64
ds_read_b64_tr_b16 a[70:71], v30 offset:20544
ds_read_b64_tr_b16 a[92:93], v30 offset:96
ds_read_b64_tr_b16 a[94:95], v30 offset:20576
ds_read_b64_tr_b16 a[116:117], v30 offset:128
ds_read_b64_tr_b16 a[118:119], v30 offset:20608
ds_read_b64_tr_b16 a[140:141], v30 offset:160
ds_read_b64_tr_b16 a[142:143], v30 offset:20640
ds_read_b64_tr_b16 a[164:165], v30 offset:192
ds_read_b64_tr_b16 a[166:167], v30 offset:20672
ds_read_b64_tr_b16 a[188:189], v30 offset:224
ds_read_b64_tr_b16 a[190:191], v30 offset:20704
s_waitcnt lgkmcnt(14)
ds_read_b64_tr_b16 a[212:213], v30 offset:256
ds_read_b64_tr_b16 a[214:215], v30 offset:20736
s_waitcnt lgkmcnt(14)
ds_read_b64_tr_b16 a[236:237], v30 offset:288
ds_read_b64_tr_b16 a[238:239], v30 offset:20768
s_waitcnt lgkmcnt(14)
s_waitcnt lgkmcnt(0)
s_barrier
ds_write_b128 v19, v[32:35] offset:0
ds_write_b128 v20, v[36:39] offset:0
ds_write_b128 v21, v[40:43] offset:0
ds_write_b128 v19, v[44:47] offset:12800
ds_write_b128 v20, v[48:51] offset:12800
ds_write_b128 v21, v[52:55] offset:12800
ds_write_b128 v19, v[56:59] offset:25600
ds_write_b128 v20, v[60:63] offset:25600
ds_write_b128 v21, v[64:67] offset:25600
ds_write_b128 v19, v[68:71] offset:38400
ds_write_b128 v20, v[72:75] offset:38400
ds_write_b128 v21, v[76:79] offset:38400
ds_write_b128 v19, v[80:83] offset:51200
ds_write_b128 v20, v[84:87] offset:51200
ds_write_b128 v21, v[88:91] offset:51200
v_and_b32 v22, 1, v15
v_and_b32 v26, 2, v15
v_lshl_add_u32 v22, v26, 2, v22
v_and_b32 v26, 4, v15
v_add_u32 v22, v22, v26
v_and_b32 v26, 8, v15
v_lshl_add_u32 v22, v26, 1, v22
v_mul_u32_u24 v22, 400, v22
v_lshrrev_b32 v26, 4, v15
v_lshl_add_u32 v22, v26, 3, v22
v_bfe_u32 v26, v15, 3, 1
v_mul_u32_u24 v26, 655360, v26
v_and_b32 v24, 0x370, v0
v_add_u32 v24, v24, v26
s_mul_i32 s48, s45, 13107200
s_add_u32 s48, s48, 64421888
v_add_u32 v24, s48, v24
s_lshr_b32 s50, s85, 1
s_mul_i32 s50, s50, 20480
s_and_b32 s48, s85, 1
s_lshl_b32 s48, s48, 7
s_add_u32 s50, s50, s48
s_mul_i32 s48, s86, 5120
s_add_u32 s50, s50, s48
s_movk_i32 s52, 0x2000
s_mov_b32 s51, 5
s_waitcnt lgkmcnt(0)
s_barrier
cw_bg_%=:
ds_read2_b64 v[96:99], v22 offset0:0 offset1:4
ds_read2_b64 v[100:103], v22 offset0:100 offset1:104
ds_read2_b64 v[104:107], v22 offset0:12 offset1:16
ds_read2_b64 v[108:111], v22 offset0:112 offset1:116
ds_read2_b64 v[112:115], v22 offset0:24 offset1:28
ds_read2_b64 v[116:119], v22 offset0:124 offset1:128
ds_read2_b64 v[120:123], v22 offset0:36 offset1:40
ds_read2_b64 v[124:127], v22 offset0:136 offset1:140
ds_read2_b64 v[128:131], v22 offset0:8 offset1:20
ds_read2_b64 v[132:135], v22 offset0:108 offset1:120
ds_read2_b64 v[136:139], v22 offset0:32 offset1:44
ds_read2_b64 v[140:143], v22 offset0:132 offset1:144
v_add_u32 v22, 12800, v22
)"
R"(s_and_b32 s53, s50, 0xffffe000
s_add_u32 s53, s53, s50
s_waitcnt lgkmcnt(11)
v_mfma_f32_16x16x32_bf16 v[144:147], v[96:99], a[0:3], 0
s_waitcnt lgkmcnt(10)
v_mfma_f32_16x16x32_bf16 v[148:151], v[100:103], a[0:3], 0
s_waitcnt lgkmcnt(9)
v_mfma_f32_16x16x32_bf16 v[144:147], v[104:107], a[4:7], v[144:147]
s_waitcnt lgkmcnt(8)
v_mfma_f32_16x16x32_bf16 v[148:151], v[108:111], a[4:7], v[148:151]
s_waitcnt lgkmcnt(7)
v_mfma_f32_16x16x32_bf16 v[144:147], v[112:115], a[8:11], v[144:147]
s_waitcnt lgkmcnt(6)
v_mfma_f32_16x16x32_bf16 v[148:151], v[116:119], a[8:11], v[148:151]
s_waitcnt lgkmcnt(5)
v_mfma_f32_16x16x32_bf16 v[144:147], v[120:123], a[12:15], v[144:147]
s_waitcnt lgkmcnt(4)
v_mfma_f32_16x16x32_bf16 v[148:151], v[124:127], a[12:15], v[148:151]
s_waitcnt lgkmcnt(3)
v_mfma_f32_16x16x32_bf16 v[144:147], v[128:131], a[16:19], v[144:147]
s_waitcnt lgkmcnt(2)
v_mfma_f32_16x16x32_bf16 v[148:151], v[132:135], a[16:19], v[148:151]
s_waitcnt lgkmcnt(1)
v_mfma_f32_16x16x32_bf16 v[144:147], v[136:139], a[20:23], v[144:147]
s_waitcnt lgkmcnt(0)
v_mfma_f32_16x16x32_bf16 v[148:151], v[140:143], a[20:23], v[148:151]
v_mfma_f32_16x16x32_bf16 v[152:155], v[96:99], a[24:27], 0
v_add_u32 v25, s53, v24
v_mfma_f32_16x16x32_bf16 v[156:159], v[100:103], a[24:27], 0
v_lshrrev_b32 v26, 8, v25
v_mfma_f32_16x16x32_bf16 v[152:155], v[104:107], a[28:31], v[152:155]
v_and_or_b32 v25, v26, s52, v25
v_mfma_f32_16x16x32_bf16 v[156:159], v[108:111], a[28:31], v[156:159]
s_add_u32 s53, s53, 1310720
v_mfma_f32_16x16x32_bf16 v[152:155], v[112:115], a[32:35], v[152:155]
v_mfma_f32_16x16x32_bf16 v[156:159], v[116:119], a[32:35], v[156:159]
v_mfma_f32_16x16x32_bf16 v[152:155], v[120:123], a[36:39], v[152:155]
v_cvt_pk_bf16_f32 v160, v144, v145
v_mfma_f32_16x16x32_bf16 v[156:159], v[124:127], a[36:39], v[156:159]
v_cvt_pk_bf16_f32 v161, v146, v147
v_mfma_f32_16x16x32_bf16 v[152:155], v[128:131], a[40:43], v[152:155]
v_cvt_pk_bf16_f32 v162, v148, v149
v_mfma_f32_16x16x32_bf16 v[156:159], v[132:135], a[40:43], v[156:159]
v_cvt_pk_bf16_f32 v163, v150, v151
v_mfma_f32_16x16x32_bf16 v[152:155], v[136:139], a[44:47], v[152:155]
buffer_store_dwordx4 v[160:163], v25, s[40:43], 0 offen sc1
v_mfma_f32_16x16x32_bf16 v[156:159], v[140:143], a[44:47], v[156:159]
v_mfma_f32_16x16x32_bf16 v[144:147], v[96:99], a[48:51], 0
v_add_u32 v25, s53, v24
v_mfma_f32_16x16x32_bf16 v[148:151], v[100:103], a[48:51], 0
v_lshrrev_b32 v26, 8, v25
v_mfma_f32_16x16x32_bf16 v[144:147], v[104:107], a[52:55], v[144:147]
v_and_or_b32 v25, v26, s52, v25
v_mfma_f32_16x16x32_bf16 v[148:151], v[108:111], a[52:55], v[148:151]
s_add_u32 s53, s53, 1310720
v_mfma_f32_16x16x32_bf16 v[144:147], v[112:115], a[56:59], v[144:147]
v_mfma_f32_16x16x32_bf16 v[148:151], v[116:119], a[56:59], v[148:151]
v_mfma_f32_16x16x32_bf16 v[144:147], v[120:123], a[60:63], v[144:147]
v_cvt_pk_bf16_f32 v164, v152, v153
v_mfma_f32_16x16x32_bf16 v[148:151], v[124:127], a[60:63], v[148:151]
v_cvt_pk_bf16_f32 v165, v154, v155
v_mfma_f32_16x16x32_bf16 v[144:147], v[128:131], a[64:67], v[144:147]
v_cvt_pk_bf16_f32 v166, v156, v157
v_mfma_f32_16x16x32_bf16 v[148:151], v[132:135], a[64:67], v[148:151]
v_cvt_pk_bf16_f32 v167, v158, v159
v_mfma_f32_16x16x32_bf16 v[144:147], v[136:139], a[68:71], v[144:147]
buffer_store_dwordx4 v[164:167], v25, s[40:43], 0 offen sc1
v_mfma_f32_16x16x32_bf16 v[148:151], v[140:143], a[68:71], v[148:151]
v_mfma_f32_16x16x32_bf16 v[152:155], v[96:99], a[72:75], 0
v_add_u32 v25, s53, v24
v_mfma_f32_16x16x32_bf16 v[156:159], v[100:103], a[72:75], 0
v_lshrrev_b32 v26, 8, v25
v_mfma_f32_16x16x32_bf16 v[152:155], v[104:107], a[76:79], v[152:155]
v_and_or_b32 v25, v26, s52, v25
v_mfma_f32_16x16x32_bf16 v[156:159], v[108:111], a[76:79], v[156:159]
s_add_u32 s53, s53, 1310720
v_mfma_f32_16x16x32_bf16 v[152:155], v[112:115], a[80:83], v[152:155]
v_mfma_f32_16x16x32_bf16 v[156:159], v[116:119], a[80:83], v[156:159]
v_mfma_f32_16x16x32_bf16 v[152:155], v[120:123], a[84:87], v[152:155]
v_cvt_pk_bf16_f32 v160, v144, v145
v_mfma_f32_16x16x32_bf16 v[156:159], v[124:127], a[84:87], v[156:159]
v_cvt_pk_bf16_f32 v161, v146, v147
v_mfma_f32_16x16x32_bf16 v[152:155], v[128:131], a[88:91], v[152:155]
v_cvt_pk_bf16_f32 v162, v148, v149
v_mfma_f32_16x16x32_bf16 v[156:159], v[132:135], a[88:91], v[156:159]
v_cvt_pk_bf16_f32 v163, v150, v151
v_mfma_f32_16x16x32_bf16 v[152:155], v[136:139], a[92:95], v[152:155]
buffer_store_dwordx4 v[160:163], v25, s[40:43], 0 offen sc1
v_mfma_f32_16x16x32_bf16 v[156:159], v[140:143], a[92:95], v[156:159]
v_mfma_f32_16x16x32_bf16 v[144:147], v[96:99], a[96:99], 0
v_add_u32 v25, s53, v24
v_mfma_f32_16x16x32_bf16 v[148:151], v[100:103], a[96:99], 0
v_lshrrev_b32 v26, 8, v25
v_mfma_f32_16x16x32_bf16 v[144:147], v[104:107], a[100:103], v[144:147]
v_and_or_b32 v25, v26, s52, v25
v_mfma_f32_16x16x32_bf16 v[148:151], v[108:111], a[100:103], v[148:151]
s_add_u32 s53, s53, 1310720
v_mfma_f32_16x16x32_bf16 v[144:147], v[112:115], a[104:107], v[144:147]
v_mfma_f32_16x16x32_bf16 v[148:151], v[116:119], a[104:107], v[148:151]
v_mfma_f32_16x16x32_bf16 v[144:147], v[120:123], a[108:111], v[144:147]
v_cvt_pk_bf16_f32 v164, v152, v153
v_mfma_f32_16x16x32_bf16 v[148:151], v[124:127], a[108:111], v[148:151]
v_cvt_pk_bf16_f32 v165, v154, v155
v_mfma_f32_16x16x32_bf16 v[144:147], v[128:131], a[112:115], v[144:147]
v_cvt_pk_bf16_f32 v166, v156, v157
v_mfma_f32_16x16x32_bf16 v[148:151], v[132:135], a[112:115], v[148:151]
v_cvt_pk_bf16_f32 v167, v158, v159
v_mfma_f32_16x16x32_bf16 v[144:147], v[136:139], a[116:119], v[144:147]
buffer_store_dwordx4 v[164:167], v25, s[40:43], 0 offen sc1
v_mfma_f32_16x16x32_bf16 v[148:151], v[140:143], a[116:119], v[148:151]
v_mfma_f32_16x16x32_bf16 v[152:155], v[96:99], a[120:123], 0
v_add_u32 v25, s53, v24
v_mfma_f32_16x16x32_bf16 v[156:159], v[100:103], a[120:123], 0
v_lshrrev_b32 v26, 8, v25
v_mfma_f32_16x16x32_bf16 v[152:155], v[104:107], a[124:127], v[152:155]
v_and_or_b32 v25, v26, s52, v25
v_mfma_f32_16x16x32_bf16 v[156:159], v[108:111], a[124:127], v[156:159]
s_add_u32 s53, s53, 1310720
v_mfma_f32_16x16x32_bf16 v[152:155], v[112:115], a[128:131], v[152:155]
v_mfma_f32_16x16x32_bf16 v[156:159], v[116:119], a[128:131], v[156:159]
v_mfma_f32_16x16x32_bf16 v[152:155], v[120:123], a[132:135], v[152:155]
v_cvt_pk_bf16_f32 v160, v144, v145
v_mfma_f32_16x16x32_bf16 v[156:159], v[124:127], a[132:135], v[156:159]
v_cvt_pk_bf16_f32 v161, v146, v147
v_mfma_f32_16x16x32_bf16 v[152:155], v[128:131], a[136:139], v[152:155]
v_cvt_pk_bf16_f32 v162, v148, v149
v_mfma_f32_16x16x32_bf16 v[156:159], v[132:135], a[136:139], v[156:159]
v_cvt_pk_bf16_f32 v163, v150, v151
v_mfma_f32_16x16x32_bf16 v[152:155], v[136:139], a[140:143], v[152:155]
buffer_store_dwordx4 v[160:163], v25, s[40:43], 0 offen sc1
v_mfma_f32_16x16x32_bf16 v[156:159], v[140:143], a[140:143], v[156:159]
v_mfma_f32_16x16x32_bf16 v[144:147], v[96:99], a[144:147], 0
v_add_u32 v25, s53, v24
v_mfma_f32_16x16x32_bf16 v[148:151], v[100:103], a[144:147], 0
v_lshrrev_b32 v26, 8, v25
v_mfma_f32_16x16x32_bf16 v[144:147], v[104:107], a[148:151], v[144:147]
v_and_or_b32 v25, v26, s52, v25
v_mfma_f32_16x16x32_bf16 v[148:151], v[108:111], a[148:151], v[148:151]
s_add_u32 s53, s53, 1310720
v_mfma_f32_16x16x32_bf16 v[144:147], v[112:115], a[152:155], v[144:147]
v_mfma_f32_16x16x32_bf16 v[148:151], v[116:119], a[152:155], v[148:151]
v_mfma_f32_16x16x32_bf16 v[144:147], v[120:123], a[156:159], v[144:147]
v_cvt_pk_bf16_f32 v164, v152, v153
v_mfma_f32_16x16x32_bf16 v[148:151], v[124:127], a[156:159], v[148:151]
v_cvt_pk_bf16_f32 v165, v154, v155
v_mfma_f32_16x16x32_bf16 v[144:147], v[128:131], a[160:163], v[144:147]
v_cvt_pk_bf16_f32 v166, v156, v157
v_mfma_f32_16x16x32_bf16 v[148:151], v[132:135], a[160:163], v[148:151]
v_cvt_pk_bf16_f32 v167, v158, v159
v_mfma_f32_16x16x32_bf16 v[144:147], v[136:139], a[164:167], v[144:147]
buffer_store_dwordx4 v[164:167], v25, s[40:43], 0 offen sc1
v_mfma_f32_16x16x32_bf16 v[148:151], v[140:143], a[164:167], v[148:151]
v_mfma_f32_16x16x32_bf16 v[152:155], v[96:99], a[168:171], 0
v_add_u32 v25, s53, v24
v_mfma_f32_16x16x32_bf16 v[156:159], v[100:103], a[168:171], 0
v_lshrrev_b32 v26, 8, v25
v_mfma_f32_16x16x32_bf16 v[152:155], v[104:107], a[172:175], v[152:155]
v_and_or_b32 v25, v26, s52, v25
v_mfma_f32_16x16x32_bf16 v[156:159], v[108:111], a[172:175], v[156:159]
s_add_u32 s53, s53, 1310720
v_mfma_f32_16x16x32_bf16 v[152:155], v[112:115], a[176:179], v[152:155]
v_mfma_f32_16x16x32_bf16 v[156:159], v[116:119], a[176:179], v[156:159]
v_mfma_f32_16x16x32_bf16 v[152:155], v[120:123], a[180:183], v[152:155]
v_cvt_pk_bf16_f32 v160, v144, v145
v_mfma_f32_16x16x32_bf16 v[156:159], v[124:127], a[180:183], v[156:159]
v_cvt_pk_bf16_f32 v161, v146, v147
v_mfma_f32_16x16x32_bf16 v[152:155], v[128:131], a[184:187], v[152:155]
v_cvt_pk_bf16_f32 v162, v148, v149
v_mfma_f32_16x16x32_bf16 v[156:159], v[132:135], a[184:187], v[156:159]
v_cvt_pk_bf16_f32 v163, v150, v151
v_mfma_f32_16x16x32_bf16 v[152:155], v[136:139], a[188:191], v[152:155]
buffer_store_dwordx4 v[160:163], v25, s[40:43], 0 offen sc1
v_mfma_f32_16x16x32_bf16 v[156:159], v[140:143], a[188:191], v[156:159]
v_mfma_f32_16x16x32_bf16 v[144:147], v[96:99], a[192:195], 0
v_add_u32 v25, s53, v24
v_mfma_f32_16x16x32_bf16 v[148:151], v[100:103], a[192:195], 0
v_lshrrev_b32 v26, 8, v25
v_mfma_f32_16x16x32_bf16 v[144:147], v[104:107], a[196:199], v[144:147]
v_and_or_b32 v25, v26, s52, v25
v_mfma_f32_16x16x32_bf16 v[148:151], v[108:111], a[196:199], v[148:151]
s_add_u32 s53, s53, 1310720
v_mfma_f32_16x16x32_bf16 v[144:147], v[112:115], a[200:203], v[144:147]
v_mfma_f32_16x16x32_bf16 v[148:151], v[116:119], a[200:203], v[148:151]
v_mfma_f32_16x16x32_bf16 v[144:147], v[120:123], a[204:207], v[144:147]
v_cvt_pk_bf16_f32 v164, v152, v153
v_mfma_f32_16x16x32_bf16 v[148:151], v[124:127], a[204:207], v[148:151]
v_cvt_pk_bf16_f32 v165, v154, v155
v_mfma_f32_16x16x32_bf16 v[144:147], v[128:131], a[208:211], v[144:147]
v_cvt_pk_bf16_f32 v166, v156, v157
v_mfma_f32_16x16x32_bf16 v[148:151], v[132:135], a[208:211], v[148:151]
v_cvt_pk_bf16_f32 v167, v158, v159
v_mfma_f32_16x16x32_bf16 v[144:147], v[136:139], a[212:215], v[144:147]
buffer_store_dwordx4 v[164:167], v25, s[40:43], 0 offen sc1
v_mfma_f32_16x16x32_bf16 v[148:151], v[140:143], a[212:215], v[148:151]
v_mfma_f32_16x16x32_bf16 v[152:155], v[96:99], a[216:219], 0
v_add_u32 v25, s53, v24
v_mfma_f32_16x16x32_bf16 v[156:159], v[100:103], a[216:219], 0
v_lshrrev_b32 v26, 8, v25
v_mfma_f32_16x16x32_bf16 v[152:155], v[104:107], a[220:223], v[152:155]
v_and_or_b32 v25, v26, s52, v25
)"
R"(v_mfma_f32_16x16x32_bf16 v[156:159], v[108:111], a[220:223], v[156:159]
s_add_u32 s53, s53, 1310720
v_mfma_f32_16x16x32_bf16 v[152:155], v[112:115], a[224:227], v[152:155]
v_mfma_f32_16x16x32_bf16 v[156:159], v[116:119], a[224:227], v[156:159]
v_mfma_f32_16x16x32_bf16 v[152:155], v[120:123], a[228:231], v[152:155]
v_cvt_pk_bf16_f32 v160, v144, v145
v_mfma_f32_16x16x32_bf16 v[156:159], v[124:127], a[228:231], v[156:159]
v_cvt_pk_bf16_f32 v161, v146, v147
v_mfma_f32_16x16x32_bf16 v[152:155], v[128:131], a[232:235], v[152:155]
v_cvt_pk_bf16_f32 v162, v148, v149
v_mfma_f32_16x16x32_bf16 v[156:159], v[132:135], a[232:235], v[156:159]
v_cvt_pk_bf16_f32 v163, v150, v151
v_mfma_f32_16x16x32_bf16 v[152:155], v[136:139], a[236:239], v[152:155]
buffer_store_dwordx4 v[160:163], v25, s[40:43], 0 offen sc1
v_mfma_f32_16x16x32_bf16 v[156:159], v[140:143], a[236:239], v[156:159]
v_add_u32 v25, s53, v24
v_lshrrev_b32 v26, 8, v25
v_and_or_b32 v25, v26, s52, v25
s_add_u32 s53, s53, 1310720
s_nop 1
v_cvt_pk_bf16_f32 v164, v152, v153
v_cvt_pk_bf16_f32 v165, v154, v155
v_cvt_pk_bf16_f32 v166, v156, v157
v_cvt_pk_bf16_f32 v167, v158, v159
buffer_store_dwordx4 v[164:167], v25, s[40:43], 0 offen sc1
s_add_u32 s50, s50, 1024
s_sub_u32 s51, s51, 1
s_cmp_lg_u32 s51, 0
s_cbranch_scc1 cw_bg_%=
s_cmp_eq_u32 s45, 0
s_cselect_b32 s59, 1, 0
v_lshlrev_b32 v26, 7, v15
v_cmp_gt_u32 vcc, s59, v15
v_mov_b32 v27, 2147483632
s_nop 0
v_cndmask_b32 v26, v27, v26, vcc
v_mov_b32 v27, 1
s_and_b32 s54, s84, 7
s_lshl_b32 s54, s54, 7
s_add_u32 s54, s54, 2374912
s_waitcnt vmcnt(0)
s_waitcnt lgkmcnt(0)
s_barrier
buffer_atomic_add v27, v26, s[40:43], s54 offen
v_lshrrev_b32 v12, 4, v15
v_and_b32 v13, 7, v15
v_and_b32 v10, 8, v15
v_cmp_eq_u32 vcc, 0, v10
v_mov_b32 v10, 0xeeee
v_mov_b32 v1, 0x4444
v_cndmask_b32 v1, v10, v1, vcc
s_lshl_b32 s48, s44, 7
v_lshlrev_b32 v3, 4, v13
v_add_u32 v3, s48, v3
v_and_b32 v10, 0xffffe000, v3
v_add_u32 v3, v3, v10
v_lshl_add_u32 v3, v12, 15, v3
v_mov_b32 v10, 2147483632
v_cndmask_b32 v3, v10, v3, vcc
s_mov_b32 s47, 0
s_cmp_lg_u32 s45, 0
s_cbranch_scc1 cw_wx_%=
v_mov_b32 v5, 0
v_cmp_eq_u32 vcc, 0, v15
v_mov_b32 v10, 2147483632
s_nop 0
v_cndmask_b32 v5, v10, v5, vcc
s_mov_b32 s65, 0
v_and_b32 v2, 31, v15
v_lshlrev_b32 v2, 7, v2
v_mov_b32 v7, 0
v_mov_b32 v8, 26880
s_mov_b32 s55, 32
s_mul_i32 s48, s44, 3277
s_lshr_b32 s48, s48, 16
s_lshl_b32 s48, s48, 7
s_add_u32 s48, s48, 512
v_add_u32 v4, s48, v5
s_mov_b32 s48, 786432
buffer_load_dwordx4 v[192:195], v3, s[40:43], s48 offen sc1
s_waitcnt vmcnt(0)
s_nop 0
cw_loopa_%=:
s_and_b32 s48, s47, 3
s_lshl_b32 s50, s48, 2
s_add_u32 s50, s50, s46
s_mul_i32 s50, s50, 32768
s_add_u32 s50, s50, 786432
s_add_u32 s51, s50, 4096
s_add_u32 s52, s50, 16384
s_add_u32 s64, s65, s70
s_and_b32 s61, s47, 31
s_mul_i32 s61, s61, 80
s_add_u32 s61, s61, s44
s_lshl_b32 s61, s61, 15
s_add_u32 s61, s61, 32964608
s_lshr_b32 s59, s61, 8
s_and_b32 s59, s59, 0x2000
s_add_u32 s61, s61, s59
v_cmp_gt_u32 vcc, s55, v7
s_cbranch_vccz cw_ux_ok_%=
s_mov_b32 s60, 0
s_nop 2
cw_ux_poll_%=:
buffer_load_dword v7, v8, s[40:43], s58 offen sc1
s_waitcnt vmcnt(0)
v_cmp_gt_u32 vcc, s55, v7
s_nop 4
s_cbranch_vccz cw_ux_ok_%=
s_sleep 6
s_add_u32 s60, s60, 1
s_cmp_gt_u32 s60, 65536
s_cbranch_scc1 cw_abort_%=
s_and_b32 s59, s60, 63
s_cbranch_scc1 cw_ux_poll_%=
v_mov_b32 v10, 1024
buffer_load_dword v10, v10, s[40:43], s58 offen sc1
s_waitcnt vmcnt(0)
v_readfirstlane_b32 s59, v10
s_nop 0
s_cmp_lg_u32 s59, 0
s_nop 2
s_cbranch_scc1 cw_end_%=
s_branch cw_ux_poll_%=
cw_ux_ok_%=:
v_add_u32 v8, 128, v8
s_memrealtime s[56:57]
s_waitcnt lgkmcnt(0)
s_sub_u32 s59, s64, s56
s_cmp_gt_u32 s59, 1000
s_cbranch_scc1 cw_wm_go_%=
cw_wm_%=:
s_memrealtime s[56:57]
s_waitcnt lgkmcnt(0)
s_sub_u32 s59, s64, s56
s_cmp_gt_i32 s59, 3
s_cbranch_scc1 cw_wm_%=
cw_wm_f_%=:
s_sub_u32 s59, s59, 1
s_cmp_ge_i32 s59, 0
s_cbranch_scc1 cw_wm_f_%=
cw_wm_go_%=:
s_mov_b32 s60, 0
buffer_load_dwordx4 v[32:35], v0, s[40:43], s50 offen sc1
buffer_load_dwordx4 v[36:39], v0, s[40:43], s50 offen offset:1024 sc1
buffer_load_dwordx4 v[40:43], v0, s[40:43], s50 offen offset:2048 sc1
buffer_load_dwordx4 v[44:47], v0, s[40:43], s50 offen offset:3072 sc1
buffer_load_dwordx4 v[48:51], v0, s[40:43], s51 offen sc1
buffer_load_dwordx4 v[52:55], v0, s[40:43], s51 offen offset:1024 sc1
buffer_load_dwordx4 v[56:59], v0, s[40:43], s51 offen offset:2048 sc1
buffer_load_dwordx4 v[60:63], v0, s[40:43], s51 offen offset:3072 sc1
buffer_load_dwordx4 v[64:67], v0, s[40:43], s52 offen sc1
buffer_load_dwordx4 v[68:71], v0, s[40:43], s52 offen offset:1024 sc1
buffer_load_dwordx4 v[112:115], v0, s[40:43], s61 offen sc1
buffer_load_dwordx4 v[116:119], v0, s[40:43], s61 offen offset:1024 sc1
buffer_load_dwordx4 v[120:123], v0, s[40:43], s61 offen offset:2048 sc1
buffer_load_dwordx4 v[124:127], v0, s[40:43], s61 offen offset:3072 sc1
s_add_u32 s61, s61, 4096
buffer_load_dwordx4 v[128:131], v0, s[40:43], s61 offen sc1
buffer_load_dwordx4 v[132:135], v0, s[40:43], s61 offen offset:1024 sc1
buffer_load_dwordx4 v[136:139], v0, s[40:43], s61 offen offset:2048 sc1
buffer_load_dwordx4 v[140:143], v0, s[40:43], s61 offen offset:3072 sc1
s_add_u32 s61, s61, 12288
buffer_load_dwordx4 v[144:147], v0, s[40:43], s61 offen sc1
buffer_load_dwordx4 v[148:151], v0, s[40:43], s61 offen offset:1024 sc1
buffer_load_dwordx4 v[152:155], v0, s[40:43], s61 offen offset:2048 sc1
buffer_load_dwordx4 v[156:159], v0, s[40:43], s61 offen offset:3072 sc1
s_add_u32 s61, s61, 4096
buffer_load_dwordx4 v[160:163], v0, s[40:43], s61 offen sc1
buffer_load_dwordx4 v[164:167], v0, s[40:43], s61 offen offset:1024 sc1
buffer_load_dwordx4 v[168:171], v0, s[40:43], s61 offen offset:2048 sc1
buffer_load_dwordx4 v[172:175], v0, s[40:43], s61 offen offset:3072 sc1
s_add_u32 s61, s61, 4096
buffer_load_dword v7, v8, s[40:43], s58 offen sc1
cw_mcomp_%=:
s_waitcnt vmcnt(26)
ds_write_b128 v0, v[32:35] offset:4096
s_waitcnt vmcnt(25)
ds_write_b128 v0, v[36:39] offset:5120
s_waitcnt vmcnt(24)
ds_write_b128 v0, v[40:43] offset:6144
s_waitcnt vmcnt(23)
ds_write_b128 v0, v[44:47] offset:7168
s_waitcnt vmcnt(22)
ds_write_b128 v0, v[48:51] offset:8192
s_waitcnt vmcnt(21)
ds_write_b128 v0, v[52:55] offset:9216
s_waitcnt vmcnt(20)
ds_write_b128 v0, v[56:59] offset:10240
s_waitcnt vmcnt(19)
ds_write_b128 v0, v[60:63] offset:11264
s_waitcnt vmcnt(18)
ds_write_b128 v0, v[64:67] offset:12288
s_waitcnt vmcnt(17)
ds_write_b128 v0, v[68:71] offset:13312
v_max_u32 v6, v32, v33
v_max3_u32 v6, v34, v35, v6
v_max3_u32 v6, v36, v37, v6
v_max3_u32 v6, v38, v39, v6
v_max3_u32 v6, v40, v41, v6
)"
R"(v_max3_u32 v6, v42, v43, v6
v_max3_u32 v6, v44, v45, v6
v_max3_u32 v6, v46, v47, v6
v_max3_u32 v6, v48, v49, v6
v_max3_u32 v6, v50, v51, v6
v_max3_u32 v6, v52, v53, v6
v_max3_u32 v6, v54, v55, v6
v_max3_u32 v6, v56, v57, v6
v_max3_u32 v6, v58, v59, v6
v_max3_u32 v6, v60, v61, v6
v_max3_u32 v6, v62, v63, v6
v_max3_u32 v6, v64, v65, v6
v_max3_u32 v6, v66, v67, v6
v_max3_u32 v6, v68, v69, v6
v_max3_u32 v6, v70, v71, v6
v_cmp_eq_u32 vcc, -1, v6
s_nop 4
s_cbranch_vccnz cw_mfail_%=
s_waitcnt lgkmcnt(0)
s_barrier
s_mul_i32 s50, s48, 4
s_and_b32 s51, s46, 3
s_add_u32 s50, s50, s51
s_mul_i32 s50, s50, 49152
s_add_u32 s51, s50, 4096
s_add_u32 s52, s50, 16384
s_add_u32 s53, s50, 20480
s_add_u32 s62, s50, 32768
s_add_u32 s49, s47, 1
s_and_b32 s49, s49, 3
s_mul_i32 s63, s49, 131072
s_add_u32 s63, s63, 786432
s_and_b32 s59, s47, 63
s_cmp_eq_u32 s59, 63
s_cbranch_scc1 cw_last_%=
s_add_u32 s64, s65, s69
s_memrealtime s[56:57]
s_waitcnt lgkmcnt(0)
s_sub_u32 s59, s64, s56
s_cmp_gt_u32 s59, 1000
s_cbranch_scc1 cw_ws_go_%=
cw_ws_%=:
s_memrealtime s[56:57]
s_waitcnt lgkmcnt(0)
s_sub_u32 s59, s64, s56
s_cmp_gt_i32 s59, 3
s_cbranch_scc1 cw_ws_%=
cw_ws_f_%=:
s_sub_u32 s59, s59, 1
s_cmp_ge_i32 s59, 0
s_cbranch_scc1 cw_ws_f_%=
cw_ws_go_%=:
s_and_b32 s59, s47, 64
s_add_u32 s54, s47, s59
s_lshl_b32 s54, s54, 7
s_add_u32 s54, s54, 1310720
s_lshl_b32 s59, s46, 15
s_add_u32 s59, s59, s54
s_add_u32 s56, s40, s59
s_addc_u32 s57, s41, 0
s_load_dwordx2 s[64:65], s[56:57], 0x0
s_waitcnt lgkmcnt(0)
s_max_u32 s59, s64, s65
s_cmp_lg_u32 s59, -1
s_cbranch_scc1 cw_rs_ok_%=
s_and_b32 s59, s47, 64
s_add_u32 s54, s47, s59
s_lshl_b32 s54, s54, 7
s_add_u32 s54, s54, 1310720
s_mov_b32 s60, 0
cw_rs_%=:
buffer_load_dwordx2 v[10:11], v5, s[40:43], s54 offen sc1
s_waitcnt vmcnt(0)
v_readfirstlane_b32 s64, v10
v_readfirstlane_b32 s65, v11
s_nop 0
s_max_u32 s59, s64, s65
s_cmp_lg_u32 s59, -1
s_nop 1
s_cbranch_scc1 cw_rs_ok_%=
s_sleep 4
s_add_u32 s60, s60, 1
s_cmp_gt_u32 s60, 65536
s_cbranch_scc1 cw_abort_%=
s_and_b32 s59, s60, 63
s_cbranch_scc1 cw_rs_%=
v_mov_b32 v10, 1024
buffer_load_dword v10, v10, s[40:43], s58 offen sc1
s_waitcnt vmcnt(0)
v_readfirstlane_b32 s59, v10
s_nop 0
s_cmp_lg_u32 s59, 0
s_nop 2
s_cbranch_scc1 cw_end_%=
s_branch cw_rs_%=
cw_rs_ok_%=:
s_mov_b32 s60, 0
s_memrealtime s[56:57]
s_waitcnt lgkmcnt(0)
s_sub_u32 s59, s64, s56
s_cmp_gt_u32 s59, 1000
s_cbranch_scc1 cw_wu_go_%=
cw_wu_%=:
s_memrealtime s[56:57]
s_waitcnt lgkmcnt(0)
s_sub_u32 s59, s64, s56
s_cmp_gt_i32 s59, 15
s_cbranch_scc1 cw_wu_%=
s_memrealtime s[30:31]
s_nop 1
s_memrealtime s[18:19]
s_nop 1
s_memrealtime s[34:35]
s_nop 1
s_memrealtime s[36:37]
s_nop 1
s_memrealtime s[38:39]
s_nop 1
s_memrealtime s[94:95]
s_nop 1
s_memrealtime s[96:97]
s_nop 1
s_memrealtime s[98:99]
s_nop 1
s_getpc_b64 s[56:57]
cw_wu_pc_%=:
s_waitcnt lgkmcnt(0)
s_add_u32 s18, s18, s34
s_add_u32 s18, s18, s36
s_add_u32 s18, s18, s38
s_add_u32 s18, s18, s94
s_add_u32 s18, s18, s96
s_add_u32 s18, s18, s98
s_mul_i32 s34, s30, 7
s_sub_u32 s18, s18, s34
s_mul_i32 s18, s18, 3
s_sub_u32 s59, s64, s30
s_mul_i32 s59, s59, 23
s_sub_u32 s59, s59, s18
s_sub_u32 s59, s59, 170
s_max_i32 s59, s59, 0
s_min_i32 s59, s59, 252
s_andn2_b32 s59, s59, 3
s_sub_u32 s59, cw_wu_e_%=-cw_wu_pc_%=, s59
s_add_u32 s56, s56, s59
s_addc_u32 s57, s57, 0
s_setpc_b64 s[56:57]
s_nop 0
s_nop 0
s_nop 0
s_nop 0
s_nop 0
s_nop 0
s_nop 0
s_nop 0
s_nop 0
s_nop 0
s_nop 0
s_nop 0
s_nop 0
s_nop 0
s_nop 0
s_nop 0
s_nop 0
s_nop 0
s_nop 0
s_nop 0
s_nop 0
s_nop 0
s_nop 0
s_nop 0
s_nop 0
s_nop 0
s_nop 0
s_nop 0
s_nop 0
s_nop 0
s_nop 0
s_nop 0
s_nop 0
s_nop 0
s_nop 0
s_nop 0
s_nop 0
s_nop 0
s_nop 0
s_nop 0
s_nop 0
s_nop 0
s_nop 0
s_nop 0
s_nop 0
s_nop 0
s_nop 0
s_nop 0
s_nop 0
s_nop 0
s_nop 0
s_nop 0
s_nop 0
)"
R"(s_nop 0
s_nop 0
s_nop 0
s_nop 0
s_nop 0
s_nop 0
s_nop 0
s_nop 0
s_nop 0
s_nop 0
cw_wu_e_%=:
cw_wu_go_%=:
buffer_load_dwordx4 v[32:35], v0, s[40:43], s50 offen sc1
buffer_load_dwordx4 v[36:39], v0, s[40:43], s50 offen offset:1024 sc1
buffer_load_dwordx4 v[40:43], v0, s[40:43], s50 offen offset:2048 sc1
buffer_load_dwordx4 v[44:47], v0, s[40:43], s50 offen offset:3072 sc1
buffer_load_dwordx4 v[48:51], v0, s[40:43], s51 offen sc1
buffer_load_dwordx4 v[52:55], v0, s[40:43], s51 offen offset:1024 sc1
buffer_load_dwordx4 v[56:59], v0, s[40:43], s51 offen offset:2048 sc1
buffer_load_dwordx4 v[60:63], v0, s[40:43], s51 offen offset:3072 sc1
buffer_load_dwordx4 v[64:67], v0, s[40:43], s52 offen sc1
buffer_load_dwordx4 v[68:71], v0, s[40:43], s52 offen offset:1024 sc1
buffer_load_dwordx4 v[72:75], v0, s[40:43], s52 offen offset:2048 sc1
buffer_load_dwordx4 v[76:79], v0, s[40:43], s52 offen offset:3072 sc1
buffer_load_dwordx4 v[80:83], v0, s[40:43], s53 offen sc1
buffer_load_dwordx4 v[84:87], v0, s[40:43], s53 offen offset:1024 sc1
buffer_load_dwordx4 v[88:91], v0, s[40:43], s53 offen offset:2048 sc1
buffer_load_dwordx4 v[92:95], v0, s[40:43], s53 offen offset:3072 sc1
buffer_load_dwordx4 v[96:99], v0, s[40:43], s62 offen sc1
buffer_load_dwordx4 v[100:103], v0, s[40:43], s62 offen offset:1024 sc1
buffer_load_dwordx4 v[104:107], v0, s[40:43], s62 offen offset:2048 sc1
buffer_load_dwordx4 v[108:111], v0, s[40:43], s62 offen offset:3072 sc1
s_barrier
ds_read_b128 v[176:179], v0 offset:0
ds_read_b128 v[180:183], v0 offset:1024
ds_read_b128 v[184:187], v0 offset:2048
ds_read_b128 v[188:191], v0 offset:3072
cw_pcomp_%=:
s_waitcnt vmcnt(19)
v_mfma_f32_16x16x32_bf16 v[24:27], v[112:115], v[32:35], 0
s_waitcnt vmcnt(18)
v_mfma_f32_16x16x32_bf16 v[24:27], v[116:119], v[36:39], v[24:27]
s_waitcnt vmcnt(17)
v_mfma_f32_16x16x32_bf16 v[24:27], v[120:123], v[40:43], v[24:27]
s_waitcnt vmcnt(16)
v_mfma_f32_16x16x32_bf16 v[24:27], v[124:127], v[44:47], v[24:27]
s_waitcnt vmcnt(15)
v_mfma_f32_16x16x32_bf16 v[24:27], v[128:131], v[48:51], v[24:27]
s_waitcnt vmcnt(14)
v_mfma_f32_16x16x32_bf16 v[24:27], v[132:135], v[52:55], v[24:27]
s_waitcnt vmcnt(13)
v_mfma_f32_16x16x32_bf16 v[24:27], v[136:139], v[56:59], v[24:27]
s_waitcnt vmcnt(12)
v_mfma_f32_16x16x32_bf16 v[24:27], v[140:143], v[60:63], v[24:27]
s_waitcnt vmcnt(11)
v_mfma_f32_16x16x32_bf16 v[24:27], v[144:147], v[64:67], v[24:27]
s_waitcnt vmcnt(10)
v_mfma_f32_16x16x32_bf16 v[24:27], v[148:151], v[68:71], v[24:27]
s_waitcnt vmcnt(9)
v_mfma_f32_16x16x32_bf16 v[24:27], v[152:155], v[72:75], v[24:27]
s_waitcnt vmcnt(8)
v_mfma_f32_16x16x32_bf16 v[24:27], v[156:159], v[76:79], v[24:27]
s_waitcnt vmcnt(7)
v_mfma_f32_16x16x32_bf16 v[24:27], v[160:163], v[80:83], v[24:27]
s_waitcnt vmcnt(6)
v_mfma_f32_16x16x32_bf16 v[24:27], v[164:167], v[84:87], v[24:27]
s_waitcnt vmcnt(5)
v_mfma_f32_16x16x32_bf16 v[24:27], v[168:171], v[88:91], v[24:27]
s_waitcnt vmcnt(4)
v_mfma_f32_16x16x32_bf16 v[24:27], v[172:175], v[92:95], v[24:27]
s_waitcnt vmcnt(3) lgkmcnt(3)
v_mfma_f32_16x16x32_bf16 v[24:27], v[176:179], v[96:99], v[24:27]
s_waitcnt vmcnt(2) lgkmcnt(2)
v_mfma_f32_16x16x32_bf16 v[24:27], v[180:183], v[100:103], v[24:27]
s_waitcnt vmcnt(1) lgkmcnt(1)
v_mfma_f32_16x16x32_bf16 v[24:27], v[184:187], v[104:107], v[24:27]
s_waitcnt vmcnt(0) lgkmcnt(0)
v_mfma_f32_16x16x32_bf16 v[24:27], v[188:191], v[108:111], v[24:27]
v_mov_b32 v11, v11
v_mov_b32 v11, v11
v_mov_b32 v11, v11
v_mov_b32 v11, v11
v_mov_b32 v11, v11
v_mov_b32 v11, v11
v_mov_b32 v11, v11
v_mov_b32 v11, v11
v_cmp_u_f32 vcc, v24, v24
v_permlane32_swap_b32 v24, v26
v_permlane32_swap_b32 v25, v27
s_nop 0
v_add_f32_dpp v24, v26, v24 row_ror:8 row_mask:0xf bank_mask:0xf
v_add_f32_dpp v25, v27, v25 row_ror:8 row_mask:0xf bank_mask:0xf
v_cvt_pk_bf16_f32 v28, v24, v25
v_cvt_pk_bf16_f32 v30, v24, v25
s_nop 1
v_permlane16_swap_b32 v28, v30
v_mov_b32 v29, v28
v_mov_b32 v31, v30
s_nop 0
v_permlane32_swap_b32 v28, v29
v_permlane32_swap_b32 v30, v31
s_cbranch_vccnz cw_pfail_%=
cw_presume_%=:
buffer_store_dwordx4 v[28:31], v3, s[40:43], s63 offen sc1
s_memrealtime s[30:31]
s_nop 1
s_memrealtime s[18:19]
s_nop 1
s_memrealtime s[34:35]
s_nop 1
s_memrealtime s[36:37]
s_nop 1
s_memrealtime s[38:39]
s_nop 1
s_memrealtime s[94:95]
s_nop 1
s_memrealtime s[96:97]
s_nop 1
s_memrealtime s[98:99]
s_nop 1
s_waitcnt lgkmcnt(0)
s_add_u32 s18, s18, s34
s_add_u32 s18, s18, s36
s_add_u32 s18, s18, s38
s_add_u32 s18, s18, s94
s_add_u32 s18, s18, s96
s_add_u32 s18, s18, s98
s_mul_i32 s34, s30, 7
s_sub_u32 s18, s18, s34
s_mul_i32 s18, s18, 3
s_sub_u32 s59, s30, s64
s_mul_i32 s59, s59, 23
s_add_u32 s59, s59, s18
v_mov_b32 v10, s59
s_lshl_b32 s34, s47, 10
s_add_u32 s34, s34, 2377984
s_cmp_eq_u32 s60, 0
s_cselect_b32 s34, s34, 0x40000000
buffer_atomic_umax v10, v4, s[40:43], s34 offen
s_add_u32 s47, s47, 1
s_nop 0
s_branch cw_loopa_%=
cw_last_%=:
s_cmp_eq_u32 s47, 127
s_cbranch_scc1 cw_end_%=
buffer_store_dwordx4 v[192:195], v3, s[40:43], s63 offen sc1
s_waitcnt vmcnt(0)
s_barrier
s_add_u32 s47, s47, 1
s_branch cw_loopa_%=
cw_pagain_%=:
buffer_load_dwordx4 v[32:35], v0, s[40:43], s50 offen sc1
buffer_load_dwordx4 v[36:39], v0, s[40:43], s50 offen offset:1024 sc1
buffer_load_dwordx4 v[40:43], v0, s[40:43], s50 offen offset:2048 sc1
buffer_load_dwordx4 v[44:47], v0, s[40:43], s50 offen offset:3072 sc1
buffer_load_dwordx4 v[48:51], v0, s[40:43], s51 offen sc1
buffer_load_dwordx4 v[52:55], v0, s[40:43], s51 offen offset:1024 sc1
buffer_load_dwordx4 v[56:59], v0, s[40:43], s51 offen offset:2048 sc1
buffer_load_dwordx4 v[60:63], v0, s[40:43], s51 offen offset:3072 sc1
buffer_load_dwordx4 v[64:67], v0, s[40:43], s52 offen sc1
buffer_load_dwordx4 v[68:71], v0, s[40:43], s52 offen offset:1024 sc1
buffer_load_dwordx4 v[72:75], v0, s[40:43], s52 offen offset:2048 sc1
buffer_load_dwordx4 v[76:79], v0, s[40:43], s52 offen offset:3072 sc1
buffer_load_dwordx4 v[80:83], v0, s[40:43], s53 offen sc1
buffer_load_dwordx4 v[84:87], v0, s[40:43], s53 offen offset:1024 sc1
buffer_load_dwordx4 v[88:91], v0, s[40:43], s53 offen offset:2048 sc1
buffer_load_dwordx4 v[92:95], v0, s[40:43], s53 offen offset:3072 sc1
buffer_load_dwordx4 v[96:99], v0, s[40:43], s62 offen sc1
buffer_load_dwordx4 v[100:103], v0, s[40:43], s62 offen offset:1024 sc1
buffer_load_dwordx4 v[104:107], v0, s[40:43], s62 offen offset:2048 sc1
buffer_load_dwordx4 v[108:111], v0, s[40:43], s62 offen offset:3072 sc1
s_nop 1
s_branch cw_pcomp_%=
cw_mfail_%=:
s_sleep 6
s_add_u32 s60, s60, 1
s_cmp_gt_u32 s60, 65536
s_cbranch_scc1 cw_abort_%=
s_and_b32 s59, s60, 63
s_cbranch_scc1 cw_mretry_%=
v_mov_b32 v10, 1024
buffer_load_dword v10, v10, s[40:43], s58 offen sc1
s_waitcnt vmcnt(0)
v_readfirstlane_b32 s59, v10
s_nop 0
s_cmp_lg_u32 s59, 0
s_nop 2
s_cbranch_scc1 cw_end_%=
s_branch cw_mretry_%=
cw_mretry_%=:
buffer_load_dwordx4 v[32:35], v0, s[40:43], s50 offen sc1
buffer_load_dwordx4 v[36:39], v0, s[40:43], s50 offen offset:1024 sc1
buffer_load_dwordx4 v[40:43], v0, s[40:43], s50 offen offset:2048 sc1
buffer_load_dwordx4 v[44:47], v0, s[40:43], s50 offen offset:3072 sc1
buffer_load_dwordx4 v[48:51], v0, s[40:43], s51 offen sc1
buffer_load_dwordx4 v[52:55], v0, s[40:43], s51 offen offset:1024 sc1
buffer_load_dwordx4 v[56:59], v0, s[40:43], s51 offen offset:2048 sc1
buffer_load_dwordx4 v[60:63], v0, s[40:43], s51 offen offset:3072 sc1
buffer_load_dwordx4 v[64:67], v0, s[40:43], s52 offen sc1
buffer_load_dwordx4 v[68:71], v0, s[40:43], s52 offen offset:1024 sc1
)"
R"(s_waitcnt vmcnt(0)
s_nop 0
s_branch cw_mcomp_%=
cw_pfail_%=:
v_max_u32 v6, v32, v33
v_max3_u32 v6, v34, v35, v6
v_max3_u32 v6, v36, v37, v6
v_max3_u32 v6, v38, v39, v6
v_max3_u32 v6, v40, v41, v6
v_max3_u32 v6, v42, v43, v6
v_max3_u32 v6, v44, v45, v6
v_max3_u32 v6, v46, v47, v6
v_max3_u32 v6, v48, v49, v6
v_max3_u32 v6, v50, v51, v6
v_max3_u32 v6, v52, v53, v6
v_max3_u32 v6, v54, v55, v6
v_max3_u32 v6, v56, v57, v6
v_max3_u32 v6, v58, v59, v6
v_max3_u32 v6, v60, v61, v6
v_max3_u32 v6, v62, v63, v6
v_max3_u32 v6, v64, v65, v6
v_max3_u32 v6, v66, v67, v6
v_max3_u32 v6, v68, v69, v6
v_max3_u32 v6, v70, v71, v6
v_max3_u32 v6, v72, v73, v6
v_max3_u32 v6, v74, v75, v6
v_max3_u32 v6, v76, v77, v6
v_max3_u32 v6, v78, v79, v6
v_max3_u32 v6, v80, v81, v6
v_max3_u32 v6, v82, v83, v6
v_max3_u32 v6, v84, v85, v6
v_max3_u32 v6, v86, v87, v6
v_max3_u32 v6, v88, v89, v6
v_max3_u32 v6, v90, v91, v6
v_max3_u32 v6, v92, v93, v6
v_max3_u32 v6, v94, v95, v6
v_max3_u32 v6, v96, v97, v6
v_max3_u32 v6, v98, v99, v6
v_max3_u32 v6, v100, v101, v6
v_max3_u32 v6, v102, v103, v6
v_max3_u32 v6, v104, v105, v6
v_max3_u32 v6, v106, v107, v6
v_max3_u32 v6, v108, v109, v6
v_max3_u32 v6, v110, v111, v6
v_cmp_eq_u32 vcc, -1, v6
s_nop 4
s_cbranch_vccz cw_presume_%=
s_cmp_lg_u32 s60, 0
s_cbranch_scc1 cw_pspin_%=
s_mov_b32 s60, 1
s_branch cw_pagain_%=
cw_ppoll_%=:
buffer_load_dword v10, v2, s[40:43], s62 offen sc1
s_waitcnt vmcnt(0)
v_cmp_eq_u32 vcc, -1, v10
s_nop 4
s_cbranch_vccz cw_pagain_%=
cw_pspin_%=:
s_add_u32 s60, s60, 1
s_cmp_gt_u32 s60, 65536
s_cbranch_scc1 cw_abort_%=
s_and_b32 s59, s60, 63
s_cbranch_scc1 cw_ppoll_%=
v_mov_b32 v10, 1024
buffer_load_dword v10, v10, s[40:43], s58 offen sc1
s_waitcnt vmcnt(0)
v_readfirstlane_b32 s59, v10
s_nop 0
s_cmp_lg_u32 s59, 0
s_nop 2
s_cbranch_scc1 cw_end_%=
s_branch cw_ppoll_%=
cw_wx_%=:
s_mul_i32 s61, s45, 6
s_sub_u32 s61, s61, 6
s_min_u32 s61, s61, 10
s_lshl_b32 s48, s44, 4
s_add_u32 s48, s48, s61
s_mul_i32 s50, s48, 20480
s_lshl_b32 s49, s45, 15
s_add_u32 s49, s49, -18432
v_add_u32 v14, s49, v0
v_and_b32 v8, 7, v15
v_lshlrev_b32 v8, 7, v8
v_add_u32 v8, 277760, v8
v_mov_b32 v7, 0
s_mov_b32 s55, 16
v_cmp_gt_u32 vcc, s55, v7
s_cbranch_vccz cw_bgw_ok_%=
s_mov_b32 s60, 0
s_nop 2
cw_bgw_poll_%=:
buffer_load_dword v7, v8, s[40:43], s58 offen sc1
s_waitcnt vmcnt(0)
v_cmp_gt_u32 vcc, s55, v7
s_nop 4
s_cbranch_vccz cw_bgw_ok_%=
s_sleep 6
s_add_u32 s60, s60, 1
s_cmp_gt_u32 s60, 65536
s_cbranch_scc1 cw_abort_%=
s_and_b32 s59, s60, 63
s_cbranch_scc1 cw_bgw_poll_%=
v_mov_b32 v10, 1024
buffer_load_dword v10, v10, s[40:43], s58 offen sc1
s_waitcnt vmcnt(0)
v_readfirstlane_b32 s59, v10
s_nop 0
s_cmp_lg_u32 s59, 0
s_nop 2
s_cbranch_scc1 cw_end_%=
s_branch cw_bgw_poll_%=
cw_bgw_ok_%=:
v_mov_b32 v13, v14
v_mov_b32 v14, v13
s_and_b32 s48, s50, 0xffffe000
s_add_u32 s48, s48, s50
s_add_u32 s48, s48, 64421888
s_lshr_b32 s59, s48, 8
s_and_b32 s59, s59, 0x2000
s_add_u32 s48, s48, s59
s_add_u32 s50, s50, 4096
buffer_load_dwordx4 v[16:19], v0, s[40:43], s48 offen sc1
buffer_load_dwordx4 v[20:23], v0, s[40:43], s48 offen offset:1024 sc1
buffer_load_dwordx4 v[32:35], v0, s[40:43], s48 offen offset:2048 sc1
buffer_load_dwordx4 v[36:39], v0, s[40:43], s48 offen offset:3072 sc1
s_and_b32 s48, s50, 0xffffe000
s_add_u32 s48, s48, s50
s_add_u32 s48, s48, 64421888
s_lshr_b32 s59, s48, 8
s_and_b32 s59, s59, 0x2000
s_add_u32 s48, s48, s59
s_add_u32 s50, s50, 4096
buffer_load_dwordx4 v[48:51], v0, s[40:43], s48 offen sc1
buffer_load_dwordx4 v[52:55], v0, s[40:43], s48 offen offset:1024 sc1
buffer_load_dwordx4 v[64:67], v0, s[40:43], s48 offen offset:2048 sc1
buffer_load_dwordx4 v[68:71], v0, s[40:43], s48 offen offset:3072 sc1
s_and_b32 s48, s50, 0xffffe000
s_add_u32 s48, s48, s50
s_add_u32 s48, s48, 64421888
s_lshr_b32 s59, s48, 8
s_and_b32 s59, s59, 0x2000
s_add_u32 s48, s48, s59
s_add_u32 s50, s50, 4096
buffer_load_dwordx4 v[80:83], v0, s[40:43], s48 offen sc1
buffer_load_dwordx4 v[84:87], v0, s[40:43], s48 offen offset:1024 sc1
buffer_load_dwordx4 v[96:99], v0, s[40:43], s48 offen offset:2048 sc1
buffer_load_dwordx4 v[100:103], v0, s[40:43], s48 offen offset:3072 sc1
s_and_b32 s48, s50, 0xffffe000
s_add_u32 s48, s48, s50
s_add_u32 s48, s48, 64421888
s_lshr_b32 s59, s48, 8
s_and_b32 s59, s59, 0x2000
s_add_u32 s48, s48, s59
s_add_u32 s50, s50, 4096
buffer_load_dwordx4 v[112:115], v0, s[40:43], s48 offen sc1
buffer_load_dwordx4 v[116:119], v0, s[40:43], s48 offen offset:1024 sc1
buffer_load_dwordx4 v[120:123], v0, s[40:43], s48 offen offset:2048 sc1
buffer_load_dwordx4 v[124:127], v0, s[40:43], s48 offen offset:3072 sc1
s_and_b32 s48, s50, 0xffffe000
s_add_u32 s48, s48, s50
s_add_u32 s48, s48, 64421888
s_lshr_b32 s59, s48, 8
s_and_b32 s59, s59, 0x2000
s_add_u32 s48, s48, s59
s_add_u32 s50, s50, 4096
buffer_load_dwordx4 v[128:131], v0, s[40:43], s48 offen sc1
buffer_load_dwordx4 v[132:135], v0, s[40:43], s48 offen offset:1024 sc1
buffer_load_dwordx4 v[136:139], v0, s[40:43], s48 offen offset:2048 sc1
buffer_load_dwordx4 v[140:143], v0, s[40:43], s48 offen offset:3072 sc1
s_and_b32 s48, s50, 0xffffe000
s_add_u32 s48, s48, s50
s_add_u32 s48, s48, 64421888
s_lshr_b32 s59, s48, 8
s_and_b32 s59, s59, 0x2000
s_add_u32 s48, s48, s59
s_add_u32 s50, s50, 4096
buffer_load_dwordx4 a[0:3], v0, s[40:43], s48 offen sc1
buffer_load_dwordx4 a[4:7], v0, s[40:43], s48 offen offset:1024 sc1
buffer_load_dwordx4 a[32:35], v0, s[40:43], s48 offen offset:2048 sc1
buffer_load_dwordx4 a[36:39], v0, s[40:43], s48 offen offset:3072 sc1
s_and_b32 s48, s50, 0xffffe000
s_add_u32 s48, s48, s50
s_add_u32 s48, s48, 64421888
s_lshr_b32 s59, s48, 8
s_and_b32 s59, s59, 0x2000
s_add_u32 s48, s48, s59
s_add_u32 s50, s50, 4096
buffer_load_dwordx4 a[64:67], v0, s[40:43], s48 offen sc1
buffer_load_dwordx4 a[68:71], v0, s[40:43], s48 offen offset:1024 sc1
buffer_load_dwordx4 a[96:99], v0, s[40:43], s48 offen offset:2048 sc1
buffer_load_dwordx4 a[100:103], v0, s[40:43], s48 offen offset:3072 sc1
s_and_b32 s48, s50, 0xffffe000
s_add_u32 s48, s48, s50
s_add_u32 s48, s48, 64421888
s_lshr_b32 s59, s48, 8
s_and_b32 s59, s59, 0x2000
s_add_u32 s48, s48, s59
s_add_u32 s50, s50, 4096
buffer_load_dwordx4 a[128:131], v0, s[40:43], s48 offen sc1
)"
R"(buffer_load_dwordx4 a[132:135], v0, s[40:43], s48 offen offset:1024 sc1
buffer_load_dwordx4 a[160:163], v0, s[40:43], s48 offen offset:2048 sc1
buffer_load_dwordx4 a[164:167], v0, s[40:43], s48 offen offset:3072 sc1
s_and_b32 s48, s50, 0xffffe000
s_add_u32 s48, s48, s50
s_add_u32 s48, s48, 64421888
s_lshr_b32 s59, s48, 8
s_and_b32 s59, s59, 0x2000
s_add_u32 s48, s48, s59
s_add_u32 s50, s50, 4096
buffer_load_dwordx4 a[192:195], v0, s[40:43], s48 offen sc1
buffer_load_dwordx4 a[196:199], v0, s[40:43], s48 offen offset:1024 sc1
buffer_load_dwordx4 a[232:235], v0, s[40:43], s48 offen offset:2048 sc1
buffer_load_dwordx4 a[236:239], v0, s[40:43], s48 offen offset:3072 sc1
s_and_b32 s48, s50, 0xffffe000
s_add_u32 s48, s48, s50
s_add_u32 s48, s48, 64421888
s_lshr_b32 s59, s48, 8
s_and_b32 s59, s59, 0x2000
s_add_u32 s48, s48, s59
s_add_u32 s50, s50, 4096
buffer_load_dwordx4 v[176:179], v0, s[40:43], s48 offen sc1
buffer_load_dwordx4 v[180:183], v0, s[40:43], s48 offen offset:1024 sc1
buffer_load_dwordx4 v[216:219], v0, s[40:43], s48 offen offset:2048 sc1
buffer_load_dwordx4 v[220:223], v0, s[40:43], s48 offen offset:3072 sc1
s_and_b32 s48, s50, 0xffffe000
s_add_u32 s48, s48, s50
s_add_u32 s48, s48, 64421888
s_lshr_b32 s59, s48, 8
s_and_b32 s59, s59, 0x2000
s_add_u32 s48, s48, s59
s_add_u32 s50, s50, 4096
buffer_load_dwordx4 a[8:11], v0, s[40:43], s48 offen sc1
buffer_load_dwordx4 a[12:15], v0, s[40:43], s48 offen offset:1024 sc1
buffer_load_dwordx4 a[40:43], v0, s[40:43], s48 offen offset:2048 sc1
buffer_load_dwordx4 a[44:47], v0, s[40:43], s48 offen offset:3072 sc1
s_and_b32 s48, s50, 0xffffe000
s_add_u32 s48, s48, s50
s_add_u32 s48, s48, 64421888
s_lshr_b32 s59, s48, 8
s_and_b32 s59, s59, 0x2000
s_add_u32 s48, s48, s59
s_add_u32 s50, s50, 4096
buffer_load_dwordx4 a[72:75], v0, s[40:43], s48 offen sc1
buffer_load_dwordx4 a[76:79], v0, s[40:43], s48 offen offset:1024 sc1
buffer_load_dwordx4 a[104:107], v0, s[40:43], s48 offen offset:2048 sc1
buffer_load_dwordx4 a[108:111], v0, s[40:43], s48 offen offset:3072 sc1
s_and_b32 s48, s50, 0xffffe000
s_add_u32 s48, s48, s50
s_add_u32 s48, s48, 64421888
s_lshr_b32 s59, s48, 8
s_and_b32 s59, s59, 0x2000
s_add_u32 s48, s48, s59
s_add_u32 s50, s50, 4096
buffer_load_dwordx4 a[136:139], v0, s[40:43], s48 offen sc1
buffer_load_dwordx4 a[140:143], v0, s[40:43], s48 offen offset:1024 sc1
buffer_load_dwordx4 a[168:171], v0, s[40:43], s48 offen offset:2048 sc1
buffer_load_dwordx4 a[172:175], v0, s[40:43], s48 offen offset:3072 sc1
s_and_b32 s48, s50, 0xffffe000
s_add_u32 s48, s48, s50
s_add_u32 s48, s48, 64421888
s_lshr_b32 s59, s48, 8
s_and_b32 s59, s59, 0x2000
s_add_u32 s48, s48, s59
s_add_u32 s50, s50, 4096
buffer_load_dwordx4 a[200:203], v0, s[40:43], s48 offen sc1
buffer_load_dwordx4 a[204:207], v0, s[40:43], s48 offen offset:1024 sc1
buffer_load_dwordx4 a[240:243], v0, s[40:43], s48 offen offset:2048 sc1
buffer_load_dwordx4 a[244:247], v0, s[40:43], s48 offen offset:3072 sc1
s_and_b32 s48, s50, 0xffffe000
s_add_u32 s48, s48, s50
s_add_u32 s48, s48, 64421888
s_lshr_b32 s59, s48, 8
s_and_b32 s59, s59, 0x2000
s_add_u32 s48, s48, s59
s_add_u32 s50, s50, 4096
buffer_load_dwordx4 v[184:187], v0, s[40:43], s48 offen sc1
buffer_load_dwordx4 v[188:191], v0, s[40:43], s48 offen offset:1024 sc1
buffer_load_dwordx4 v[224:227], v0, s[40:43], s48 offen offset:2048 sc1
buffer_load_dwordx4 v[228:231], v0, s[40:43], s48 offen offset:3072 sc1
s_and_b32 s48, s50, 0xffffe000
s_add_u32 s48, s48, s50
s_add_u32 s48, s48, 64421888
s_lshr_b32 s59, s48, 8
s_and_b32 s59, s59, 0x2000
s_add_u32 s48, s48, s59
s_add_u32 s50, s50, 4096
buffer_load_dwordx4 v[24:27], v0, s[40:43], s48 offen sc1
buffer_load_dwordx4 v[28:31], v0, s[40:43], s48 offen offset:1024 sc1
buffer_load_dwordx4 v[40:43], v0, s[40:43], s48 offen offset:2048 sc1
s_waitcnt vmcnt(61)
buffer_load_dwordx4 v[44:47], v0, s[40:43], s48 offen offset:3072 sc1
s_and_b32 s48, s50, 0xffffe000
s_add_u32 s48, s48, s50
s_add_u32 s48, s48, 64421888
s_lshr_b32 s59, s48, 8
s_and_b32 s59, s59, 0x2000
s_add_u32 s48, s48, s59
s_add_u32 s50, s50, 4096
buffer_load_dwordx4 v[56:59], v0, s[40:43], s48 offen sc1
s_waitcnt vmcnt(61)
buffer_load_dwordx4 v[60:63], v0, s[40:43], s48 offen offset:1024 sc1
buffer_load_dwordx4 v[72:75], v0, s[40:43], s48 offen offset:2048 sc1
s_waitcnt vmcnt(61)
buffer_load_dwordx4 v[76:79], v0, s[40:43], s48 offen offset:3072 sc1
s_and_b32 s48, s50, 0xffffe000
s_add_u32 s48, s48, s50
s_add_u32 s48, s48, 64421888
s_lshr_b32 s59, s48, 8
s_and_b32 s59, s59, 0x2000
s_add_u32 s48, s48, s59
s_add_u32 s50, s50, 4096
buffer_load_dwordx4 v[88:91], v0, s[40:43], s48 offen sc1
s_waitcnt vmcnt(61)
buffer_load_dwordx4 v[92:95], v0, s[40:43], s48 offen offset:1024 sc1
buffer_load_dwordx4 v[104:107], v0, s[40:43], s48 offen offset:2048 sc1
s_waitcnt vmcnt(61)
buffer_load_dwordx4 v[108:111], v0, s[40:43], s48 offen offset:3072 sc1
s_and_b32 s48, s50, 0xffffe000
s_add_u32 s48, s48, s50
s_add_u32 s48, s48, 64421888
s_lshr_b32 s59, s48, 8
s_and_b32 s59, s59, 0x2000
s_add_u32 s48, s48, s59
s_add_u32 s50, s50, 4096
buffer_load_dwordx4 a[208:211], v0, s[40:43], s48 offen sc1
s_waitcnt vmcnt(61)
buffer_load_dwordx4 a[212:215], v0, s[40:43], s48 offen offset:1024 sc1
buffer_load_dwordx4 a[248:251], v0, s[40:43], s48 offen offset:2048 sc1
s_waitcnt vmcnt(61)
buffer_load_dwordx4 a[252:255], v0, s[40:43], s48 offen offset:3072 sc1
s_and_b32 s48, s50, 0xffffe000
s_add_u32 s48, s48, s50
s_add_u32 s48, s48, 64421888
s_lshr_b32 s59, s48, 8
s_and_b32 s59, s59, 0x2000
s_add_u32 s48, s48, s59
s_add_u32 s50, s50, 4096
buffer_load_dwordx4 v[192:195], v0, s[40:43], s48 offen sc1
s_waitcnt vmcnt(61)
buffer_load_dwordx4 v[196:199], v0, s[40:43], s48 offen offset:1024 sc1
buffer_load_dwordx4 v[232:235], v0, s[40:43], s48 offen offset:2048 sc1
s_waitcnt vmcnt(61)
buffer_load_dwordx4 v[236:239], v0, s[40:43], s48 offen offset:3072 sc1
s_and_b32 s48, s50, 0xffffe000
s_add_u32 s48, s48, s50
s_add_u32 s48, s48, 64421888
s_lshr_b32 s59, s48, 8
s_and_b32 s59, s59, 0x2000
s_add_u32 s48, s48, s59
s_add_u32 s50, s50, 4096
buffer_load_dwordx4 a[16:19], v0, s[40:43], s48 offen sc1
s_waitcnt vmcnt(61)
buffer_load_dwordx4 a[20:23], v0, s[40:43], s48 offen offset:1024 sc1
buffer_load_dwordx4 a[48:51], v0, s[40:43], s48 offen offset:2048 sc1
s_waitcnt vmcnt(61)
buffer_load_dwordx4 a[52:55], v0, s[40:43], s48 offen offset:3072 sc1
s_and_b32 s48, s50, 0xffffe000
s_add_u32 s48, s48, s50
s_add_u32 s48, s48, 64421888
s_lshr_b32 s59, s48, 8
s_and_b32 s59, s59, 0x2000
s_add_u32 s48, s48, s59
s_add_u32 s50, s50, 4096
buffer_load_dwordx4 a[80:83], v0, s[40:43], s48 offen sc1
s_waitcnt vmcnt(61)
buffer_load_dwordx4 a[84:87], v0, s[40:43], s48 offen offset:1024 sc1
buffer_load_dwordx4 a[112:115], v0, s[40:43], s48 offen offset:2048 sc1
s_waitcnt vmcnt(61)
buffer_load_dwordx4 a[116:119], v0, s[40:43], s48 offen offset:3072 sc1
s_and_b32 s48, s50, 0xffffe000
s_add_u32 s48, s48, s50
s_add_u32 s48, s48, 64421888
s_lshr_b32 s59, s48, 8
s_and_b32 s59, s59, 0x2000
s_add_u32 s48, s48, s59
s_add_u32 s50, s50, 4096
buffer_load_dwordx4 a[144:147], v0, s[40:43], s48 offen sc1
s_waitcnt vmcnt(61)
buffer_load_dwordx4 a[148:151], v0, s[40:43], s48 offen offset:1024 sc1
buffer_load_dwordx4 a[176:179], v0, s[40:43], s48 offen offset:2048 sc1
s_waitcnt vmcnt(61)
buffer_load_dwordx4 a[180:183], v0, s[40:43], s48 offen offset:3072 sc1
s_and_b32 s48, s50, 0xffffe000
s_add_u32 s48, s48, s50
s_add_u32 s48, s48, 64421888
s_lshr_b32 s59, s48, 8
s_and_b32 s59, s59, 0x2000
s_add_u32 s48, s48, s59
s_add_u32 s50, s50, 4096
buffer_load_dwordx4 a[216:219], v0, s[40:43], s48 offen sc1
s_waitcnt vmcnt(61)
buffer_load_dwordx4 a[220:223], v0, s[40:43], s48 offen offset:1024 sc1
buffer_load_dwordx4 v[160:163], v0, s[40:43], s48 offen offset:2048 sc1
s_waitcnt vmcnt(61)
buffer_load_dwordx4 v[164:167], v0, s[40:43], s48 offen offset:3072 sc1
s_and_b32 s48, s50, 0xffffe000
s_add_u32 s48, s48, s50
s_add_u32 s48, s48, 64421888
s_lshr_b32 s59, s48, 8
)"
R"(s_and_b32 s59, s59, 0x2000
s_add_u32 s48, s48, s59
s_add_u32 s50, s50, 4096
buffer_load_dwordx4 v[200:203], v0, s[40:43], s48 offen sc1
s_waitcnt vmcnt(61)
buffer_load_dwordx4 v[204:207], v0, s[40:43], s48 offen offset:1024 sc1
buffer_load_dwordx4 v[240:243], v0, s[40:43], s48 offen offset:2048 sc1
s_waitcnt vmcnt(61)
buffer_load_dwordx4 v[244:247], v0, s[40:43], s48 offen offset:3072 sc1
s_and_b32 s48, s50, 0xffffe000
s_add_u32 s48, s48, s50
s_add_u32 s48, s48, 64421888
s_lshr_b32 s59, s48, 8
s_and_b32 s59, s59, 0x2000
s_add_u32 s48, s48, s59
s_add_u32 s50, s50, 4096
buffer_load_dwordx4 a[24:27], v0, s[40:43], s48 offen sc1
s_waitcnt vmcnt(61)
buffer_load_dwordx4 a[28:31], v0, s[40:43], s48 offen offset:1024 sc1
buffer_load_dwordx4 a[56:59], v0, s[40:43], s48 offen offset:2048 sc1
s_waitcnt vmcnt(61)
buffer_load_dwordx4 a[60:63], v0, s[40:43], s48 offen offset:3072 sc1
s_and_b32 s48, s50, 0xffffe000
s_add_u32 s48, s48, s50
s_add_u32 s48, s48, 64421888
s_lshr_b32 s59, s48, 8
s_and_b32 s59, s59, 0x2000
s_add_u32 s48, s48, s59
s_add_u32 s50, s50, 4096
buffer_load_dwordx4 a[88:91], v0, s[40:43], s48 offen sc1
s_waitcnt vmcnt(61)
buffer_load_dwordx4 a[92:95], v0, s[40:43], s48 offen offset:1024 sc1
buffer_load_dwordx4 a[120:123], v0, s[40:43], s48 offen offset:2048 sc1
s_waitcnt vmcnt(61)
buffer_load_dwordx4 a[124:127], v0, s[40:43], s48 offen offset:3072 sc1
s_and_b32 s48, s50, 0xffffe000
s_add_u32 s48, s48, s50
s_add_u32 s48, s48, 64421888
s_lshr_b32 s59, s48, 8
s_and_b32 s59, s59, 0x2000
s_add_u32 s48, s48, s59
s_add_u32 s50, s50, 4096
buffer_load_dwordx4 a[152:155], v0, s[40:43], s48 offen sc1
s_waitcnt vmcnt(61)
buffer_load_dwordx4 a[156:159], v0, s[40:43], s48 offen offset:1024 sc1
buffer_load_dwordx4 a[184:187], v0, s[40:43], s48 offen offset:2048 sc1
s_waitcnt vmcnt(61)
buffer_load_dwordx4 a[188:191], v0, s[40:43], s48 offen offset:3072 sc1
s_and_b32 s48, s50, 0xffffe000
s_add_u32 s48, s48, s50
s_add_u32 s48, s48, 64421888
s_lshr_b32 s59, s48, 8
s_and_b32 s59, s59, 0x2000
s_add_u32 s48, s48, s59
s_add_u32 s50, s50, 4096
buffer_load_dwordx4 a[224:227], v0, s[40:43], s48 offen sc1
s_waitcnt vmcnt(61)
buffer_load_dwordx4 a[228:231], v0, s[40:43], s48 offen offset:1024 sc1
buffer_load_dwordx4 v[168:171], v0, s[40:43], s48 offen offset:2048 sc1
s_waitcnt vmcnt(61)
buffer_load_dwordx4 v[172:175], v0, s[40:43], s48 offen offset:3072 sc1
s_and_b32 s48, s50, 0xffffe000
s_add_u32 s48, s48, s50
s_add_u32 s48, s48, 64421888
s_lshr_b32 s59, s48, 8
s_and_b32 s59, s59, 0x2000
s_add_u32 s48, s48, s59
s_add_u32 s50, s50, 4096
buffer_load_dwordx4 v[208:211], v0, s[40:43], s48 offen sc1
s_waitcnt vmcnt(61)
buffer_load_dwordx4 v[212:215], v0, s[40:43], s48 offen offset:1024 sc1
buffer_load_dwordx4 v[248:251], v0, s[40:43], s48 offen offset:2048 sc1
s_waitcnt vmcnt(61)
buffer_load_dwordx4 v[252:255], v0, s[40:43], s48 offen offset:3072 sc1
ds_write_b128 v14, v[16:19] offset:0
ds_write_b128 v14, v[20:23] offset:1024
s_waitcnt vmcnt(59)
ds_write_b128 v14, v[24:27] offset:2048
s_waitcnt vmcnt(58)
ds_write_b128 v14, v[28:31] offset:3072
ds_write_b128 v14, v[32:35] offset:4096
ds_write_b128 v14, v[36:39] offset:5120
s_waitcnt vmcnt(57)
ds_write_b128 v14, v[40:43] offset:6144
s_waitcnt vmcnt(56)
ds_write_b128 v14, v[44:47] offset:7168
ds_write_b128 v14, v[48:51] offset:8192
ds_write_b128 v14, v[52:55] offset:9216
s_waitcnt vmcnt(55)
ds_write_b128 v14, v[56:59] offset:10240
s_waitcnt vmcnt(54)
ds_write_b128 v14, v[60:63] offset:11264
ds_write_b128 v14, v[64:67] offset:12288
ds_write_b128 v14, v[68:71] offset:13312
s_waitcnt vmcnt(53)
ds_write_b128 v14, v[72:75] offset:14336
s_waitcnt vmcnt(52)
ds_write_b128 v14, v[76:79] offset:15360
s_waitcnt lgkmcnt(14)
ds_write_b128 v14, v[80:83] offset:16384
ds_write_b128 v14, v[84:87] offset:17408
s_waitcnt lgkmcnt(14)
s_waitcnt vmcnt(51)
ds_write_b128 v14, v[88:91] offset:18432
s_waitcnt vmcnt(50)
ds_write_b128 v14, v[92:95] offset:19456
s_waitcnt lgkmcnt(14)
ds_write_b128 v14, v[96:99] offset:20480
ds_write_b128 v14, v[100:103] offset:21504
s_waitcnt lgkmcnt(14)
s_waitcnt vmcnt(49)
ds_write_b128 v14, v[104:107] offset:22528
s_waitcnt vmcnt(48)
ds_write_b128 v14, v[108:111] offset:23552
s_waitcnt lgkmcnt(14)
ds_write_b128 v14, v[112:115] offset:24576
ds_write_b128 v14, v[116:119] offset:25600
s_waitcnt lgkmcnt(14)
ds_write_b128 v14, v[120:123] offset:26624
ds_write_b128 v14, v[124:127] offset:27648
s_waitcnt lgkmcnt(14)
ds_write_b128 v14, v[128:131] offset:28672
ds_write_b128 v14, v[132:135] offset:29696
s_waitcnt lgkmcnt(14)
ds_write_b128 v14, v[136:139] offset:30720
ds_write_b128 v14, v[140:143] offset:31744
s_waitcnt lgkmcnt(14)
s_waitcnt vmcnt(0) lgkmcnt(0)
v_mov_b32 v134, v14
v_mov_b32 v128, -1
v_mov_b32 v129, -1
v_mov_b32 v130, -1
v_mov_b32 v131, -1
s_cmp_eq_u32 s45, 2
s_cselect_b32 s59, 1, 0
v_lshlrev_b32 v132, 7, v15
v_cmp_gt_u32 vcc, s59, v15
v_mov_b32 v10, 2147483632
s_nop 0
v_cndmask_b32 v132, v10, v132, vcc
v_mov_b32 v133, 1
s_mul_i32 s48, s44, 3277
s_lshr_b32 s48, s48, 16
s_lshl_b32 s48, s48, 7
s_add_u32 s65, s48, 2158592
s_mov_b32 s62, 0x40000000
s_mov_b32 s63, 0x40000000
s_mov_b32 s64, 0x40000000
v_and_b32 v9, 0xf0, v0
v_lshrrev_b32 v10, 1, v12
v_lshl_add_u32 v9, v10, 8, v9
v_and_b32 v10, 1, v12
v_lshl_add_u32 v9, v10, 3, v9
s_lshl_b32 s48, s61, 8
v_add_u32 v9, s48, v9
cw_loopb_%=:
v_mov_b32 v56, 0
v_mov_b32 v57, 0
v_mov_b32 v58, 0
v_mov_b32 v59, 0
v_mov_b32 v60, 0
v_mov_b32 v61, 0
v_mov_b32 v62, 0
v_mov_b32 v63, 0
v_mov_b32 v64, 0
v_mov_b32 v65, 0
v_mov_b32 v66, 0
v_mov_b32 v67, 0
v_mov_b32 v68, 0
v_mov_b32 v69, 0
v_mov_b32 v70, 0
v_mov_b32 v71, 0
v_mov_b32 v72, 0
v_mov_b32 v73, 0
v_mov_b32 v74, 0
v_mov_b32 v75, 0
v_mov_b32 v76, 0
v_mov_b32 v77, 0
v_mov_b32 v78, 0
v_mov_b32 v79, 0
ds_read_b128 v[80:83], v134 offset:0
ds_read_b128 v[84:87], v134 offset:1024
ds_read_b128 v[88:91], v134 offset:2048
ds_read_b128 v[92:95], v134 offset:3072
ds_read_b128 v[96:99], v134 offset:4096
ds_read_b128 v[100:103], v134 offset:5120
s_add_u32 s49, s47, 3
s_and_b32 s49, s49, 3
s_mul_i32 s54, s49, 131072
s_add_u32 s54, s54, 786432
s_cmp_eq_u32 s45, 1
s_cselect_b32 s54, s54, 0x40000000
s_waitcnt vmcnt(0)
s_waitcnt lgkmcnt(0)
s_barrier
ds_read_b128 v[16:19], v0 offset:4096
ds_read_b128 v[20:23], v0 offset:5120
ds_read_b128 v[24:27], v0 offset:6144
ds_read_b128 v[28:31], v0 offset:7168
ds_read_b128 v[32:35], v0 offset:8192
)"
R"(ds_read_b128 v[36:39], v0 offset:9216
ds_read_b128 v[40:43], v0 offset:10240
ds_read_b128 v[44:47], v0 offset:11264
ds_read_b128 v[48:51], v0 offset:12288
ds_read_b128 v[52:55], v0 offset:13312
s_waitcnt lgkmcnt(9)
v_smfmac_f32_16x16x64_bf16 v[56:59], v[16:19], v[80:87], v1
ds_read_b128 v[80:83], v134 offset:6144
v_smfmac_f32_16x16x64_bf16 v[60:63], v[16:19], a[0:7], v1
ds_read_b128 v[84:87], v134 offset:7168
v_smfmac_f32_16x16x64_bf16 v[64:67], v[16:19], a[8:15], v1
buffer_store_dwordx4 v[128:131], v3, s[40:43], s54 offen sc1
v_smfmac_f32_16x16x64_bf16 v[68:71], v[16:19], v[88:95], v1
ds_read_b128 v[88:91], v134 offset:8192
v_smfmac_f32_16x16x64_bf16 v[72:75], v[16:19], a[16:23], v1
ds_read_b128 v[92:95], v134 offset:9216
v_smfmac_f32_16x16x64_bf16 v[76:79], v[16:19], a[24:31], v1
buffer_store_dwordx4 v[104:107], v0, s[40:43], s62 offen offset:0 sc1
s_waitcnt lgkmcnt(12)
v_smfmac_f32_16x16x64_bf16 v[56:59], v[20:23], v[96:103], v1
ds_read_b128 v[96:99], v134 offset:10240
v_smfmac_f32_16x16x64_bf16 v[60:63], v[20:23], a[32:39], v1
ds_read_b128 v[100:103], v134 offset:11264
v_smfmac_f32_16x16x64_bf16 v[64:67], v[20:23], a[40:47], v1
buffer_store_dwordx4 v[108:111], v0, s[40:43], s62 offen offset:1024 sc1
s_waitcnt lgkmcnt(4)
v_smfmac_f32_16x16x64_bf16 v[68:71], v[20:23], v[80:87], v1
ds_read_b128 v[80:83], v134 offset:12288
v_smfmac_f32_16x16x64_bf16 v[72:75], v[20:23], a[48:55], v1
ds_read_b128 v[84:87], v134 offset:13312
v_smfmac_f32_16x16x64_bf16 v[76:79], v[20:23], a[56:63], v1
buffer_store_dwordx4 v[112:115], v0, s[40:43], s62 offen offset:2048 sc1
s_waitcnt lgkmcnt(4)
v_smfmac_f32_16x16x64_bf16 v[56:59], v[24:27], v[88:95], v1
ds_read_b128 v[88:91], v134 offset:14336
v_smfmac_f32_16x16x64_bf16 v[60:63], v[24:27], a[64:71], v1
ds_read_b128 v[92:95], v134 offset:15360
v_smfmac_f32_16x16x64_bf16 v[64:67], v[24:27], a[72:79], v1
buffer_store_dwordx4 v[116:119], v0, s[40:43], s62 offen offset:3072 sc1
s_waitcnt lgkmcnt(4)
v_smfmac_f32_16x16x64_bf16 v[68:71], v[24:27], v[96:103], v1
ds_read_b128 v[96:99], v134 offset:16384
v_smfmac_f32_16x16x64_bf16 v[72:75], v[24:27], a[80:87], v1
ds_read_b128 v[100:103], v134 offset:17408
v_smfmac_f32_16x16x64_bf16 v[76:79], v[24:27], a[88:95], v1
s_waitcnt lgkmcnt(4)
v_smfmac_f32_16x16x64_bf16 v[56:59], v[28:31], v[80:87], v1
ds_read_b128 v[80:83], v134 offset:18432
v_smfmac_f32_16x16x64_bf16 v[60:63], v[28:31], a[96:103], v1
ds_read_b128 v[84:87], v134 offset:19456
v_smfmac_f32_16x16x64_bf16 v[64:67], v[28:31], a[104:111], v1
s_waitcnt lgkmcnt(4)
v_smfmac_f32_16x16x64_bf16 v[68:71], v[28:31], v[88:95], v1
ds_read_b128 v[88:91], v134 offset:20480
v_smfmac_f32_16x16x64_bf16 v[72:75], v[28:31], a[112:119], v1
ds_read_b128 v[92:95], v134 offset:21504
v_smfmac_f32_16x16x64_bf16 v[76:79], v[28:31], a[120:127], v1
s_waitcnt lgkmcnt(4)
v_smfmac_f32_16x16x64_bf16 v[56:59], v[32:35], v[96:103], v1
ds_read_b128 v[96:99], v134 offset:22528
v_smfmac_f32_16x16x64_bf16 v[60:63], v[32:35], a[128:135], v1
ds_read_b128 v[100:103], v134 offset:23552
v_smfmac_f32_16x16x64_bf16 v[64:67], v[32:35], a[136:143], v1
s_waitcnt lgkmcnt(4)
v_smfmac_f32_16x16x64_bf16 v[68:71], v[32:35], v[80:87], v1
ds_read_b128 v[80:83], v134 offset:24576
v_smfmac_f32_16x16x64_bf16 v[72:75], v[32:35], a[144:151], v1
ds_read_b128 v[84:87], v134 offset:25600
v_smfmac_f32_16x16x64_bf16 v[76:79], v[32:35], a[152:159], v1
s_waitcnt lgkmcnt(4)
v_smfmac_f32_16x16x64_bf16 v[56:59], v[36:39], v[88:95], v1
ds_read_b128 v[88:91], v134 offset:26624
v_smfmac_f32_16x16x64_bf16 v[60:63], v[36:39], a[160:167], v1
ds_read_b128 v[92:95], v134 offset:27648
v_smfmac_f32_16x16x64_bf16 v[64:67], v[36:39], a[168:175], v1
s_waitcnt lgkmcnt(4)
v_smfmac_f32_16x16x64_bf16 v[68:71], v[36:39], v[96:103], v1
ds_read_b128 v[96:99], v134 offset:28672
v_smfmac_f32_16x16x64_bf16 v[72:75], v[36:39], a[176:183], v1
ds_read_b128 v[100:103], v134 offset:29696
v_smfmac_f32_16x16x64_bf16 v[76:79], v[36:39], a[184:191], v1
s_waitcnt lgkmcnt(4)
v_smfmac_f32_16x16x64_bf16 v[56:59], v[40:43], v[80:87], v1
ds_read_b128 v[80:83], v134 offset:30720
v_smfmac_f32_16x16x64_bf16 v[60:63], v[40:43], a[192:199], v1
ds_read_b128 v[84:87], v134 offset:31744
v_smfmac_f32_16x16x64_bf16 v[64:67], v[40:43], a[200:207], v1
v_smfmac_f32_16x16x64_bf16 v[68:71], v[40:43], a[208:215], v1
v_smfmac_f32_16x16x64_bf16 v[72:75], v[40:43], a[216:223], v1
v_smfmac_f32_16x16x64_bf16 v[76:79], v[40:43], a[224:231], v1
s_waitcnt lgkmcnt(4)
v_smfmac_f32_16x16x64_bf16 v[56:59], v[44:47], v[88:95], v1
v_smfmac_f32_16x16x64_bf16 v[60:63], v[44:47], a[232:239], v1
v_smfmac_f32_16x16x64_bf16 v[64:67], v[44:47], a[240:247], v1
v_smfmac_f32_16x16x64_bf16 v[68:71], v[44:47], a[248:255], v1
v_smfmac_f32_16x16x64_bf16 v[72:75], v[44:47], v[160:167], v1
v_smfmac_f32_16x16x64_bf16 v[76:79], v[44:47], v[168:175], v1
s_waitcnt lgkmcnt(2)
v_smfmac_f32_16x16x64_bf16 v[56:59], v[48:51], v[96:103], v1
v_smfmac_f32_16x16x64_bf16 v[60:63], v[48:51], v[176:183], v1
v_smfmac_f32_16x16x64_bf16 v[64:67], v[48:51], v[184:191], v1
v_smfmac_f32_16x16x64_bf16 v[68:71], v[48:51], v[192:199], v1
v_smfmac_f32_16x16x64_bf16 v[72:75], v[48:51], v[200:207], v1
v_smfmac_f32_16x16x64_bf16 v[76:79], v[48:51], v[208:215], v1
s_waitcnt lgkmcnt(0)
v_smfmac_f32_16x16x64_bf16 v[56:59], v[52:55], v[80:87], v1
v_smfmac_f32_16x16x64_bf16 v[60:63], v[52:55], v[216:223], v1
v_smfmac_f32_16x16x64_bf16 v[64:67], v[52:55], v[224:231], v1
v_smfmac_f32_16x16x64_bf16 v[68:71], v[52:55], v[232:239], v1
v_smfmac_f32_16x16x64_bf16 v[72:75], v[52:55], v[240:247], v1
v_smfmac_f32_16x16x64_bf16 v[76:79], v[52:55], v[248:255], v1
v_mov_b32 v11, v11
v_mov_b32 v11, v11
v_mov_b32 v11, v11
v_mov_b32 v11, v11
v_permlane32_swap_b32 v56, v60
v_permlane32_swap_b32 v57, v61
v_permlane32_swap_b32 v58, v62
v_permlane32_swap_b32 v59, v63
v_pk_add_f32 v[56:57], v[56:57], v[60:61]
v_pk_add_f32 v[58:59], v[58:59], v[62:63]
v_cvt_pk_bf16_f32 v60, v56, v57
v_cvt_pk_bf16_f32 v61, v58, v59
ds_write_b64 v9, v[60:61] offset:0
v_permlane32_swap_b32 v64, v68
v_permlane32_swap_b32 v65, v69
v_permlane32_swap_b32 v66, v70
v_permlane32_swap_b32 v67, v71
v_pk_add_f32 v[64:65], v[64:65], v[68:69]
v_pk_add_f32 v[66:67], v[66:67], v[70:71]
v_cvt_pk_bf16_f32 v68, v64, v65
v_cvt_pk_bf16_f32 v69, v66, v67
ds_write_b64 v9, v[68:69] offset:512
v_permlane32_swap_b32 v72, v76
v_permlane32_swap_b32 v73, v77
v_permlane32_swap_b32 v74, v78
v_permlane32_swap_b32 v75, v79
v_pk_add_f32 v[72:73], v[72:73], v[76:77]
v_pk_add_f32 v[74:75], v[74:75], v[78:79]
v_cvt_pk_bf16_f32 v76, v72, v73
v_cvt_pk_bf16_f32 v77, v74, v75
ds_write_b64 v9, v[76:77] offset:1024
s_waitcnt lgkmcnt(0)
s_barrier
s_waitcnt vmcnt(0)
buffer_atomic_add v133, v132, s[40:43], s63 offen
ds_read_b128 v[104:107], v0 offset:0
ds_read_b128 v[108:111], v0 offset:1024
ds_read_b128 v[112:115], v0 offset:2048
ds_read_b128 v[116:119], v0 offset:3072
s_and_b32 s62, s47, 31
s_mul_i32 s62, s62, 80
s_add_u32 s62, s62, s44
s_lshl_b32 s62, s62, 12
s_add_u32 s62, s62, 116850688
s_cmp_eq_u32 s45, 2
s_cselect_b32 s62, s62, 0x40000000
s_mul_i32 s63, s47, 1536
s_add_u32 s63, s63, s65
s_waitcnt lgkmcnt(0)
s_add_u32 s47, s47, 1
s_and_b32 s59, s47, 63
s_cbranch_scc1 cw_loopb_%=
buffer_store_dwordx4 v[104:107], v0, s[40:43], s62 offen offset:0 sc1
buffer_store_dwordx4 v[108:111], v0, s[40:43], s62 offen offset:1024 sc1
buffer_store_dwordx4 v[112:115], v0, s[40:43], s62 offen offset:2048 sc1
buffer_store_dwordx4 v[116:119], v0, s[40:43], s62 offen offset:3072 sc1
s_waitcnt vmcnt(0)
buffer_atomic_add v133, v132, s[40:43], s63 offen
s_mov_b32 s62, 0x40000000
s_mov_b32 s63, 0x40000000
s_mov_b32 s64, 0x40000000
s_cmp_lt_u32 s47, 128
s_cbranch_scc1 cw_loopb_%=
s_branch cw_end_%=
cw_abort_%=:
v_mov_b32 v10, 1
v_mov_b32 v11, 1024
buffer_store_dword v10, v11, s[40:43], s58 offen sc1
cw_end_%=:
s_endpgm
)"
    :: CHAIN_INPUTS : CHAIN_CLOBBERS);
    __builtin_unreachable();
}
DEV void role_helper(unsigned lo, unsigned hi, int id, int w, int aux, const Tune& tn, const void* wa, const void* wb, int bid) {
    asm volatile(
R"(s_mov_b32 s40, %0
s_mov_b32 s41, %1
s_mov_b32 s42, %2
s_mov_b32 s43, %3
s_mov_b32 s44, %4
s_mov_b32 s45, %5
s_mov_b32 s46, %6
s_mov_b32 s66, %7
s_mov_b32 s67, %8
s_mov_b32 s68, %9
s_mov_b32 s69, %10
s_mov_b32 s70, %11
s_mov_b32 s71, %12
s_mov_b32 s72, %13
s_mov_b32 s73, %14
s_mov_b32 s76, %15
s_mov_b32 s77, %16
s_mov_b32 s78, 7864320
s_mov_b32 s79, 0x20000
s_mov_b32 s80, %17
s_mov_b32 s81, %18
s_mov_b32 s82, 7864320
s_mov_b32 s83, 0x20000
s_mov_b32 s84, %19
s_mov_b64 exec, -1
v_mbcnt_lo_u32_b32 v15, -1, 0
v_mbcnt_hi_u32_b32 v15, -1, v15
v_lshlrev_b32 v0, 4, v15
s_mov_b32 s58, 2097152
s_mov_b32 s92, 0
s_mov_b32 s21, 0
s_mov_b32 s22, 0x3fb8aa3b
s_mov_b32 s23, s22
s_mov_b32 s24, 0xbfb8aa3b
s_mov_b32 s25, s24
s_cmp_eq_u32 s46, 3
s_cbranch_scc1 hp_ldo_%=
s_and_b32 s48, s46, 3
s_cbranch_scc1 hp_ldw_%=
s_lshr_b32 s85, s84, 2
s_and_b32 s86, s84, 3
v_add_u32 v26, 0, v15
v_mul_u32_u24 v27, 2731, v26
v_lshrrev_b32 v27, 16, v27
v_mul_u32_u24 v16, 11904, v27
v_lshl_add_u32 v16, v26, 4, v16
v_add_u32 v26, v26, v27
s_mul_i32 s48, s45, 3200
v_lshl_add_u32 v19, v26, 4, s48
v_add_u32 v26, 64, v15
v_mul_u32_u24 v27, 2731, v26
v_lshrrev_b32 v27, 16, v27
v_mul_u32_u24 v17, 11904, v27
v_lshl_add_u32 v17, v26, 4, v17
v_add_u32 v26, v26, v27
s_mul_i32 s48, s45, 3200
v_lshl_add_u32 v20, v26, 4, s48
v_add_u32 v26, 128, v15
v_mul_u32_u24 v27, 2731, v26
v_lshrrev_b32 v27, 16, v27
v_mul_u32_u24 v18, 11904, v27
v_lshl_add_u32 v18, v26, 4, v18
v_add_u32 v26, v26, v27
s_mul_i32 s48, s45, 3200
v_lshl_add_u32 v21, v26, 4, s48
s_mul_i32 s48, s86, 160
s_lshl3_add_u32 s48, s45, s48
s_mul_i32 s48, s48, 12288
s_mul_i32 s49, s85, 384
s_add_u32 s48, s48, s49
buffer_load_dwordx4 v[32:35], v16, s[80:83], s48 offen
buffer_load_dwordx4 v[36:39], v17, s[80:83], s48 offen
buffer_load_dwordx4 v[40:43], v18, s[80:83], s48 offen
s_add_u32 s48, s48, 393216
buffer_load_dwordx4 v[44:47], v16, s[80:83], s48 offen
buffer_load_dwordx4 v[48:51], v17, s[80:83], s48 offen
buffer_load_dwordx4 v[52:55], v18, s[80:83], s48 offen
s_add_u32 s48, s48, 393216
buffer_load_dwordx4 v[56:59], v16, s[80:83], s48 offen
buffer_load_dwordx4 v[60:63], v17, s[80:83], s48 offen
buffer_load_dwordx4 v[64:67], v18, s[80:83], s48 offen
s_add_u32 s48, s48, 393216
buffer_load_dwordx4 v[68:71], v16, s[80:83], s48 offen
buffer_load_dwordx4 v[72:75], v17, s[80:83], s48 offen
buffer_load_dwordx4 v[76:79], v18, s[80:83], s48 offen
s_add_u32 s48, s48, 393216
buffer_load_dwordx4 v[80:83], v16, s[80:83], s48 offen
buffer_load_dwordx4 v[84:87], v17, s[80:83], s48 offen
buffer_load_dwordx4 v[88:91], v18, s[80:83], s48 offen
s_add_u32 s48, s48, 393216
v_lshrrev_b32 v28, 2, v15
v_mul_u32_u24 v28, 1280, v28
v_and_b32 v29, 3, v15
v_lshl_add_u32 v28, v29, 3, v28
s_mul_i32 s48, s45, 320
v_add_u32 v28, s48, v28
v_add_u32 v29, 40960, v28
v_add_u32 v30, 81920, v28
s_mul_i32 s51, s45, 10240
s_mul_i32 s50, s85, 245760
s_add_u32 s50, s50, s51
s_lshr_b32 s52, s45, 1
s_mul_i32 s52, s52, 40960
s_add_u32 s52, s52, s50
s_add_u32 m0, s51, 0
s_add_u32 s49, s50, 0
buffer_load_dwordx4 v0, s[76:79], s49 offen offset:0 lds
buffer_load_dwordx4 v0, s[76:79], s49 offen offset:1024 lds
buffer_load_dwordx4 v0, s[76:79], s49 offen offset:2048 lds
buffer_load_dwordx4 v0, s[76:79], s49 offen offset:3072 lds
s_add_u32 m0, s51, 4096
s_add_u32 s49, s50, 4096
buffer_load_dwordx4 v0, s[76:79], s49 offen offset:0 lds
buffer_load_dwordx4 v0, s[76:79], s49 offen offset:1024 lds
buffer_load_dwordx4 v0, s[76:79], s49 offen offset:2048 lds
buffer_load_dwordx4 v0, s[76:79], s49 offen offset:3072 lds
s_add_u32 m0, s51, 8192
s_add_u32 s49, s50, 8192
buffer_load_dwordx4 v0, s[76:79], s49 offen offset:0 lds
buffer_load_dwordx4 v0, s[76:79], s49 offen offset:1024 lds
s_add_u32 m0, s51, 40960
s_add_u32 s49, s50, 61440
buffer_load_dwordx4 v0, s[76:79], s49 offen offset:0 lds
buffer_load_dwordx4 v0, s[76:79], s49 offen offset:1024 lds
buffer_load_dwordx4 v0, s[76:79], s49 offen offset:2048 lds
buffer_load_dwordx4 v0, s[76:79], s49 offen offset:3072 lds
s_add_u32 m0, s51, 45056
s_add_u32 s49, s50, 65536
buffer_load_dwordx4 v0, s[76:79], s49 offen offset:0 lds
buffer_load_dwordx4 v0, s[76:79], s49 offen offset:1024 lds
buffer_load_dwordx4 v0, s[76:79], s49 offen offset:2048 lds
buffer_load_dwordx4 v0, s[76:79], s49 offen offset:3072 lds
s_add_u32 m0, s51, 49152
s_add_u32 s49, s50, 69632
buffer_load_dwordx4 v0, s[76:79], s49 offen offset:0 lds
buffer_load_dwordx4 v0, s[76:79], s49 offen offset:1024 lds
s_waitcnt vmcnt(10) lgkmcnt(0)
s_barrier
s_add_u32 m0, s51, 81920
s_add_u32 s49, s50, 122880
buffer_load_dwordx4 v0, s[76:79], s49 offen offset:0 lds
buffer_load_dwordx4 v0, s[76:79], s49 offen offset:1024 lds
buffer_load_dwordx4 v0, s[76:79], s49 offen offset:2048 lds
buffer_load_dwordx4 v0, s[76:79], s49 offen offset:3072 lds
s_add_u32 m0, s51, 86016
s_add_u32 s49, s50, 126976
buffer_load_dwordx4 v0, s[76:79], s49 offen offset:0 lds
buffer_load_dwordx4 v0, s[76:79], s49 offen offset:1024 lds
buffer_load_dwordx4 v0, s[76:79], s49 offen offset:2048 lds
buffer_load_dwordx4 v0, s[76:79], s49 offen offset:3072 lds
s_add_u32 m0, s51, 90112
s_add_u32 s49, s50, 131072
buffer_load_dwordx4 v0, s[76:79], s49 offen offset:0 lds
buffer_load_dwordx4 v0, s[76:79], s49 offen offset:1024 lds
ds_read_b64_tr_b16 a[0:1], v28 offset:0
ds_read_b64_tr_b16 a[2:3], v28 offset:20480
ds_read_b64_tr_b16 a[24:25], v28 offset:32
ds_read_b64_tr_b16 a[26:27], v28 offset:20512
ds_read_b64_tr_b16 a[48:49], v28 offset:64
ds_read_b64_tr_b16 a[50:51], v28 offset:20544
ds_read_b64_tr_b16 a[72:73], v28 offset:96
ds_read_b64_tr_b16 a[74:75], v28 offset:20576
ds_read_b64_tr_b16 a[96:97], v28 offset:128
ds_read_b64_tr_b16 a[98:99], v28 offset:20608
ds_read_b64_tr_b16 a[120:121], v28 offset:160
ds_read_b64_tr_b16 a[122:123], v28 offset:20640
ds_read_b64_tr_b16 a[144:145], v28 offset:192
ds_read_b64_tr_b16 a[146:147], v28 offset:20672
ds_read_b64_tr_b16 a[168:169], v28 offset:224
ds_read_b64_tr_b16 a[170:171], v28 offset:20704
s_waitcnt lgkmcnt(14)
ds_read_b64_tr_b16 a[192:193], v28 offset:256
ds_read_b64_tr_b16 a[194:195], v28 offset:20736
s_waitcnt lgkmcnt(14)
ds_read_b64_tr_b16 a[216:217], v28 offset:288
ds_read_b64_tr_b16 a[218:219], v28 offset:20768
s_waitcnt lgkmcnt(14)
s_waitcnt vmcnt(10) lgkmcnt(0)
s_barrier
s_add_u32 m0, s51, 0
s_add_u32 s49, s50, 184320
buffer_load_dwordx4 v0, s[76:79], s49 offen offset:0 lds
buffer_load_dwordx4 v0, s[76:79], s49 offen offset:1024 lds
buffer_load_dwordx4 v0, s[76:79], s49 offen offset:2048 lds
buffer_load_dwordx4 v0, s[76:79], s49 offen offset:3072 lds
s_add_u32 m0, s51, 4096
s_add_u32 s49, s50, 188416
buffer_load_dwordx4 v0, s[76:79], s49 offen offset:0 lds
buffer_load_dwordx4 v0, s[76:79], s49 offen offset:1024 lds
buffer_load_dwordx4 v0, s[76:79], s49 offen offset:2048 lds
buffer_load_dwordx4 v0, s[76:79], s49 offen offset:3072 lds
s_add_u32 m0, s51, 8192
s_add_u32 s49, s50, 192512
buffer_load_dwordx4 v0, s[76:79], s49 offen offset:0 lds
buffer_load_dwordx4 v0, s[76:79], s49 offen offset:1024 lds
ds_read_b64_tr_b16 a[4:5], v29 offset:0
ds_read_b64_tr_b16 a[6:7], v29 offset:20480
ds_read_b64_tr_b16 a[28:29], v29 offset:32
ds_read_b64_tr_b16 a[30:31], v29 offset:20512
ds_read_b64_tr_b16 a[52:53], v29 offset:64
)"
R"(ds_read_b64_tr_b16 a[54:55], v29 offset:20544
ds_read_b64_tr_b16 a[76:77], v29 offset:96
ds_read_b64_tr_b16 a[78:79], v29 offset:20576
ds_read_b64_tr_b16 a[100:101], v29 offset:128
ds_read_b64_tr_b16 a[102:103], v29 offset:20608
ds_read_b64_tr_b16 a[124:125], v29 offset:160
ds_read_b64_tr_b16 a[126:127], v29 offset:20640
ds_read_b64_tr_b16 a[148:149], v29 offset:192
ds_read_b64_tr_b16 a[150:151], v29 offset:20672
ds_read_b64_tr_b16 a[172:173], v29 offset:224
ds_read_b64_tr_b16 a[174:175], v29 offset:20704
s_waitcnt lgkmcnt(14)
ds_read_b64_tr_b16 a[196:197], v29 offset:256
ds_read_b64_tr_b16 a[198:199], v29 offset:20736
s_waitcnt lgkmcnt(14)
ds_read_b64_tr_b16 a[220:221], v29 offset:288
ds_read_b64_tr_b16 a[222:223], v29 offset:20768
s_waitcnt lgkmcnt(14)
s_waitcnt vmcnt(10) lgkmcnt(0)
s_barrier
s_add_u32 m0, s51, 40960
s_add_u32 s49, s52, 40960
buffer_load_dwordx4 v0, s[76:79], s49 offen offset:0 lds
buffer_load_dwordx4 v0, s[76:79], s49 offen offset:1024 lds
buffer_load_dwordx4 v0, s[76:79], s49 offen offset:2048 lds
buffer_load_dwordx4 v0, s[76:79], s49 offen offset:3072 lds
s_add_u32 m0, s51, 45056
s_add_u32 s49, s52, 45056
buffer_load_dwordx4 v0, s[76:79], s49 offen offset:0 lds
buffer_load_dwordx4 v0, s[76:79], s49 offen offset:1024 lds
buffer_load_dwordx4 v0, s[76:79], s49 offen offset:2048 lds
buffer_load_dwordx4 v0, s[76:79], s49 offen offset:3072 lds
s_add_u32 m0, s51, 49152
s_add_u32 s49, s52, 49152
buffer_load_dwordx4 v0, s[76:79], s49 offen offset:0 lds
buffer_load_dwordx4 v0, s[76:79], s49 offen offset:1024 lds
ds_read_b64_tr_b16 a[8:9], v30 offset:0
ds_read_b64_tr_b16 a[10:11], v30 offset:20480
ds_read_b64_tr_b16 a[32:33], v30 offset:32
ds_read_b64_tr_b16 a[34:35], v30 offset:20512
ds_read_b64_tr_b16 a[56:57], v30 offset:64
ds_read_b64_tr_b16 a[58:59], v30 offset:20544
ds_read_b64_tr_b16 a[80:81], v30 offset:96
ds_read_b64_tr_b16 a[82:83], v30 offset:20576
ds_read_b64_tr_b16 a[104:105], v30 offset:128
ds_read_b64_tr_b16 a[106:107], v30 offset:20608
ds_read_b64_tr_b16 a[128:129], v30 offset:160
ds_read_b64_tr_b16 a[130:131], v30 offset:20640
ds_read_b64_tr_b16 a[152:153], v30 offset:192
ds_read_b64_tr_b16 a[154:155], v30 offset:20672
ds_read_b64_tr_b16 a[176:177], v30 offset:224
ds_read_b64_tr_b16 a[178:179], v30 offset:20704
s_waitcnt lgkmcnt(14)
ds_read_b64_tr_b16 a[200:201], v30 offset:256
ds_read_b64_tr_b16 a[202:203], v30 offset:20736
s_waitcnt lgkmcnt(14)
ds_read_b64_tr_b16 a[224:225], v30 offset:288
ds_read_b64_tr_b16 a[226:227], v30 offset:20768
s_waitcnt lgkmcnt(14)
s_waitcnt vmcnt(10) lgkmcnt(0)
s_barrier
s_add_u32 m0, s51, 81920
s_add_u32 s49, s52, 163840
buffer_load_dwordx4 v0, s[76:79], s49 offen offset:0 lds
buffer_load_dwordx4 v0, s[76:79], s49 offen offset:1024 lds
buffer_load_dwordx4 v0, s[76:79], s49 offen offset:2048 lds
buffer_load_dwordx4 v0, s[76:79], s49 offen offset:3072 lds
s_add_u32 m0, s51, 86016
s_add_u32 s49, s52, 167936
buffer_load_dwordx4 v0, s[76:79], s49 offen offset:0 lds
buffer_load_dwordx4 v0, s[76:79], s49 offen offset:1024 lds
buffer_load_dwordx4 v0, s[76:79], s49 offen offset:2048 lds
buffer_load_dwordx4 v0, s[76:79], s49 offen offset:3072 lds
s_add_u32 m0, s51, 90112
s_add_u32 s49, s52, 172032
buffer_load_dwordx4 v0, s[76:79], s49 offen offset:0 lds
buffer_load_dwordx4 v0, s[76:79], s49 offen offset:1024 lds
ds_read_b64_tr_b16 a[12:13], v28 offset:0
ds_read_b64_tr_b16 a[14:15], v28 offset:20480
ds_read_b64_tr_b16 a[36:37], v28 offset:32
ds_read_b64_tr_b16 a[38:39], v28 offset:20512
ds_read_b64_tr_b16 a[60:61], v28 offset:64
ds_read_b64_tr_b16 a[62:63], v28 offset:20544
ds_read_b64_tr_b16 a[84:85], v28 offset:96
ds_read_b64_tr_b16 a[86:87], v28 offset:20576
ds_read_b64_tr_b16 a[108:109], v28 offset:128
ds_read_b64_tr_b16 a[110:111], v28 offset:20608
ds_read_b64_tr_b16 a[132:133], v28 offset:160
ds_read_b64_tr_b16 a[134:135], v28 offset:20640
ds_read_b64_tr_b16 a[156:157], v28 offset:192
ds_read_b64_tr_b16 a[158:159], v28 offset:20672
ds_read_b64_tr_b16 a[180:181], v28 offset:224
ds_read_b64_tr_b16 a[182:183], v28 offset:20704
s_waitcnt lgkmcnt(14)
ds_read_b64_tr_b16 a[204:205], v28 offset:256
ds_read_b64_tr_b16 a[206:207], v28 offset:20736
s_waitcnt lgkmcnt(14)
ds_read_b64_tr_b16 a[228:229], v28 offset:288
ds_read_b64_tr_b16 a[230:231], v28 offset:20768
s_waitcnt lgkmcnt(14)
s_waitcnt vmcnt(10) lgkmcnt(0)
s_barrier
ds_read_b64_tr_b16 a[16:17], v29 offset:0
ds_read_b64_tr_b16 a[18:19], v29 offset:20480
ds_read_b64_tr_b16 a[40:41], v29 offset:32
ds_read_b64_tr_b16 a[42:43], v29 offset:20512
ds_read_b64_tr_b16 a[64:65], v29 offset:64
ds_read_b64_tr_b16 a[66:67], v29 offset:20544
ds_read_b64_tr_b16 a[88:89], v29 offset:96
ds_read_b64_tr_b16 a[90:91], v29 offset:20576
ds_read_b64_tr_b16 a[112:113], v29 offset:128
ds_read_b64_tr_b16 a[114:115], v29 offset:20608
ds_read_b64_tr_b16 a[136:137], v29 offset:160
ds_read_b64_tr_b16 a[138:139], v29 offset:20640
ds_read_b64_tr_b16 a[160:161], v29 offset:192
ds_read_b64_tr_b16 a[162:163], v29 offset:20672
ds_read_b64_tr_b16 a[184:185], v29 offset:224
ds_read_b64_tr_b16 a[186:187], v29 offset:20704
s_waitcnt lgkmcnt(14)
ds_read_b64_tr_b16 a[208:209], v29 offset:256
ds_read_b64_tr_b16 a[210:211], v29 offset:20736
s_waitcnt lgkmcnt(14)
ds_read_b64_tr_b16 a[232:233], v29 offset:288
ds_read_b64_tr_b16 a[234:235], v29 offset:20768
s_waitcnt lgkmcnt(14)
s_waitcnt vmcnt(0) lgkmcnt(0)
s_barrier
ds_read_b64_tr_b16 a[20:21], v30 offset:0
ds_read_b64_tr_b16 a[22:23], v30 offset:20480
ds_read_b64_tr_b16 a[44:45], v30 offset:32
ds_read_b64_tr_b16 a[46:47], v30 offset:20512
ds_read_b64_tr_b16 a[68:69], v30 offset:64
ds_read_b64_tr_b16 a[70:71], v30 offset:20544
ds_read_b64_tr_b16 a[92:93], v30 offset:96
ds_read_b64_tr_b16 a[94:95], v30 offset:20576
ds_read_b64_tr_b16 a[116:117], v30 offset:128
ds_read_b64_tr_b16 a[118:119], v30 offset:20608
ds_read_b64_tr_b16 a[140:141], v30 offset:160
ds_read_b64_tr_b16 a[142:143], v30 offset:20640
ds_read_b64_tr_b16 a[164:165], v30 offset:192
ds_read_b64_tr_b16 a[166:167], v30 offset:20672
ds_read_b64_tr_b16 a[188:189], v30 offset:224
ds_read_b64_tr_b16 a[190:191], v30 offset:20704
s_waitcnt lgkmcnt(14)
ds_read_b64_tr_b16 a[212:213], v30 offset:256
ds_read_b64_tr_b16 a[214:215], v30 offset:20736
s_waitcnt lgkmcnt(14)
ds_read_b64_tr_b16 a[236:237], v30 offset:288
ds_read_b64_tr_b16 a[238:239], v30 offset:20768
s_waitcnt lgkmcnt(14)
s_waitcnt lgkmcnt(0)
s_barrier
ds_write_b128 v19, v[32:35] offset:0
ds_write_b128 v20, v[36:39] offset:0
ds_write_b128 v21, v[40:43] offset:0
ds_write_b128 v19, v[44:47] offset:12800
ds_write_b128 v20, v[48:51] offset:12800
ds_write_b128 v21, v[52:55] offset:12800
ds_write_b128 v19, v[56:59] offset:25600
ds_write_b128 v20, v[60:63] offset:25600
ds_write_b128 v21, v[64:67] offset:25600
ds_write_b128 v19, v[68:71] offset:38400
ds_write_b128 v20, v[72:75] offset:38400
ds_write_b128 v21, v[76:79] offset:38400
ds_write_b128 v19, v[80:83] offset:51200
ds_write_b128 v20, v[84:87] offset:51200
ds_write_b128 v21, v[88:91] offset:51200
v_and_b32 v22, 1, v15
v_and_b32 v26, 2, v15
v_lshl_add_u32 v22, v26, 2, v22
v_and_b32 v26, 4, v15
v_add_u32 v22, v22, v26
v_and_b32 v26, 8, v15
v_lshl_add_u32 v22, v26, 1, v22
v_mul_u32_u24 v22, 400, v22
v_lshrrev_b32 v26, 4, v15
v_lshl_add_u32 v22, v26, 3, v22
v_bfe_u32 v26, v15, 3, 1
v_mul_u32_u24 v26, 655360, v26
v_and_b32 v24, 0x370, v0
v_add_u32 v24, v24, v26
s_mul_i32 s48, s45, 13107200
s_add_u32 s48, s48, 64421888
v_add_u32 v24, s48, v24
s_lshr_b32 s50, s85, 1
s_mul_i32 s50, s50, 20480
s_and_b32 s48, s85, 1
s_lshl_b32 s48, s48, 7
s_add_u32 s50, s50, s48
s_mul_i32 s48, s86, 5120
s_add_u32 s50, s50, s48
s_movk_i32 s52, 0x2000
s_mov_b32 s51, 5
s_waitcnt lgkmcnt(0)
s_barrier
hp_bg_%=:
ds_read2_b64 v[96:99], v22 offset0:0 offset1:4
ds_read2_b64 v[100:103], v22 offset0:100 offset1:104
ds_read2_b64 v[104:107], v22 offset0:12 offset1:16
ds_read2_b64 v[108:111], v22 offset0:112 offset1:116
)"
R"(ds_read2_b64 v[112:115], v22 offset0:24 offset1:28
ds_read2_b64 v[116:119], v22 offset0:124 offset1:128
ds_read2_b64 v[120:123], v22 offset0:36 offset1:40
ds_read2_b64 v[124:127], v22 offset0:136 offset1:140
ds_read2_b64 v[128:131], v22 offset0:8 offset1:20
ds_read2_b64 v[132:135], v22 offset0:108 offset1:120
ds_read2_b64 v[136:139], v22 offset0:32 offset1:44
ds_read2_b64 v[140:143], v22 offset0:132 offset1:144
v_add_u32 v22, 12800, v22
s_and_b32 s53, s50, 0xffffe000
s_add_u32 s53, s53, s50
s_waitcnt lgkmcnt(11)
v_mfma_f32_16x16x32_bf16 v[144:147], v[96:99], a[0:3], 0
s_waitcnt lgkmcnt(10)
v_mfma_f32_16x16x32_bf16 v[148:151], v[100:103], a[0:3], 0
s_waitcnt lgkmcnt(9)
v_mfma_f32_16x16x32_bf16 v[144:147], v[104:107], a[4:7], v[144:147]
s_waitcnt lgkmcnt(8)
v_mfma_f32_16x16x32_bf16 v[148:151], v[108:111], a[4:7], v[148:151]
s_waitcnt lgkmcnt(7)
v_mfma_f32_16x16x32_bf16 v[144:147], v[112:115], a[8:11], v[144:147]
s_waitcnt lgkmcnt(6)
v_mfma_f32_16x16x32_bf16 v[148:151], v[116:119], a[8:11], v[148:151]
s_waitcnt lgkmcnt(5)
v_mfma_f32_16x16x32_bf16 v[144:147], v[120:123], a[12:15], v[144:147]
s_waitcnt lgkmcnt(4)
v_mfma_f32_16x16x32_bf16 v[148:151], v[124:127], a[12:15], v[148:151]
s_waitcnt lgkmcnt(3)
v_mfma_f32_16x16x32_bf16 v[144:147], v[128:131], a[16:19], v[144:147]
s_waitcnt lgkmcnt(2)
v_mfma_f32_16x16x32_bf16 v[148:151], v[132:135], a[16:19], v[148:151]
s_waitcnt lgkmcnt(1)
v_mfma_f32_16x16x32_bf16 v[144:147], v[136:139], a[20:23], v[144:147]
s_waitcnt lgkmcnt(0)
v_mfma_f32_16x16x32_bf16 v[148:151], v[140:143], a[20:23], v[148:151]
v_mfma_f32_16x16x32_bf16 v[152:155], v[96:99], a[24:27], 0
v_add_u32 v25, s53, v24
v_mfma_f32_16x16x32_bf16 v[156:159], v[100:103], a[24:27], 0
v_lshrrev_b32 v26, 8, v25
v_mfma_f32_16x16x32_bf16 v[152:155], v[104:107], a[28:31], v[152:155]
v_and_or_b32 v25, v26, s52, v25
v_mfma_f32_16x16x32_bf16 v[156:159], v[108:111], a[28:31], v[156:159]
s_add_u32 s53, s53, 1310720
v_mfma_f32_16x16x32_bf16 v[152:155], v[112:115], a[32:35], v[152:155]
v_mfma_f32_16x16x32_bf16 v[156:159], v[116:119], a[32:35], v[156:159]
v_mfma_f32_16x16x32_bf16 v[152:155], v[120:123], a[36:39], v[152:155]
v_cvt_pk_bf16_f32 v160, v144, v145
v_mfma_f32_16x16x32_bf16 v[156:159], v[124:127], a[36:39], v[156:159]
v_cvt_pk_bf16_f32 v161, v146, v147
v_mfma_f32_16x16x32_bf16 v[152:155], v[128:131], a[40:43], v[152:155]
v_cvt_pk_bf16_f32 v162, v148, v149
v_mfma_f32_16x16x32_bf16 v[156:159], v[132:135], a[40:43], v[156:159]
v_cvt_pk_bf16_f32 v163, v150, v151
v_mfma_f32_16x16x32_bf16 v[152:155], v[136:139], a[44:47], v[152:155]
buffer_store_dwordx4 v[160:163], v25, s[40:43], 0 offen sc1
v_mfma_f32_16x16x32_bf16 v[156:159], v[140:143], a[44:47], v[156:159]
v_mfma_f32_16x16x32_bf16 v[144:147], v[96:99], a[48:51], 0
v_add_u32 v25, s53, v24
v_mfma_f32_16x16x32_bf16 v[148:151], v[100:103], a[48:51], 0
v_lshrrev_b32 v26, 8, v25
v_mfma_f32_16x16x32_bf16 v[144:147], v[104:107], a[52:55], v[144:147]
v_and_or_b32 v25, v26, s52, v25
v_mfma_f32_16x16x32_bf16 v[148:151], v[108:111], a[52:55], v[148:151]
s_add_u32 s53, s53, 1310720
v_mfma_f32_16x16x32_bf16 v[144:147], v[112:115], a[56:59], v[144:147]
v_mfma_f32_16x16x32_bf16 v[148:151], v[116:119], a[56:59], v[148:151]
v_mfma_f32_16x16x32_bf16 v[144:147], v[120:123], a[60:63], v[144:147]
v_cvt_pk_bf16_f32 v164, v152, v153
v_mfma_f32_16x16x32_bf16 v[148:151], v[124:127], a[60:63], v[148:151]
v_cvt_pk_bf16_f32 v165, v154, v155
v_mfma_f32_16x16x32_bf16 v[144:147], v[128:131], a[64:67], v[144:147]
v_cvt_pk_bf16_f32 v166, v156, v157
v_mfma_f32_16x16x32_bf16 v[148:151], v[132:135], a[64:67], v[148:151]
v_cvt_pk_bf16_f32 v167, v158, v159
v_mfma_f32_16x16x32_bf16 v[144:147], v[136:139], a[68:71], v[144:147]
buffer_store_dwordx4 v[164:167], v25, s[40:43], 0 offen sc1
v_mfma_f32_16x16x32_bf16 v[148:151], v[140:143], a[68:71], v[148:151]
v_mfma_f32_16x16x32_bf16 v[152:155], v[96:99], a[72:75], 0
v_add_u32 v25, s53, v24
v_mfma_f32_16x16x32_bf16 v[156:159], v[100:103], a[72:75], 0
v_lshrrev_b32 v26, 8, v25
v_mfma_f32_16x16x32_bf16 v[152:155], v[104:107], a[76:79], v[152:155]
v_and_or_b32 v25, v26, s52, v25
v_mfma_f32_16x16x32_bf16 v[156:159], v[108:111], a[76:79], v[156:159]
s_add_u32 s53, s53, 1310720
v_mfma_f32_16x16x32_bf16 v[152:155], v[112:115], a[80:83], v[152:155]
v_mfma_f32_16x16x32_bf16 v[156:159], v[116:119], a[80:83], v[156:159]
v_mfma_f32_16x16x32_bf16 v[152:155], v[120:123], a[84:87], v[152:155]
v_cvt_pk_bf16_f32 v160, v144, v145
v_mfma_f32_16x16x32_bf16 v[156:159], v[124:127], a[84:87], v[156:159]
v_cvt_pk_bf16_f32 v161, v146, v147
v_mfma_f32_16x16x32_bf16 v[152:155], v[128:131], a[88:91], v[152:155]
v_cvt_pk_bf16_f32 v162, v148, v149
v_mfma_f32_16x16x32_bf16 v[156:159], v[132:135], a[88:91], v[156:159]
v_cvt_pk_bf16_f32 v163, v150, v151
v_mfma_f32_16x16x32_bf16 v[152:155], v[136:139], a[92:95], v[152:155]
buffer_store_dwordx4 v[160:163], v25, s[40:43], 0 offen sc1
v_mfma_f32_16x16x32_bf16 v[156:159], v[140:143], a[92:95], v[156:159]
v_mfma_f32_16x16x32_bf16 v[144:147], v[96:99], a[96:99], 0
v_add_u32 v25, s53, v24
v_mfma_f32_16x16x32_bf16 v[148:151], v[100:103], a[96:99], 0
v_lshrrev_b32 v26, 8, v25
v_mfma_f32_16x16x32_bf16 v[144:147], v[104:107], a[100:103], v[144:147]
v_and_or_b32 v25, v26, s52, v25
v_mfma_f32_16x16x32_bf16 v[148:151], v[108:111], a[100:103], v[148:151]
s_add_u32 s53, s53, 1310720
v_mfma_f32_16x16x32_bf16 v[144:147], v[112:115], a[104:107], v[144:147]
v_mfma_f32_16x16x32_bf16 v[148:151], v[116:119], a[104:107], v[148:151]
v_mfma_f32_16x16x32_bf16 v[144:147], v[120:123], a[108:111], v[144:147]
v_cvt_pk_bf16_f32 v164, v152, v153
v_mfma_f32_16x16x32_bf16 v[148:151], v[124:127], a[108:111], v[148:151]
v_cvt_pk_bf16_f32 v165, v154, v155
v_mfma_f32_16x16x32_bf16 v[144:147], v[128:131], a[112:115], v[144:147]
v_cvt_pk_bf16_f32 v166, v156, v157
v_mfma_f32_16x16x32_bf16 v[148:151], v[132:135], a[112:115], v[148:151]
v_cvt_pk_bf16_f32 v167, v158, v159
v_mfma_f32_16x16x32_bf16 v[144:147], v[136:139], a[116:119], v[144:147]
buffer_store_dwordx4 v[164:167], v25, s[40:43], 0 offen sc1
v_mfma_f32_16x16x32_bf16 v[148:151], v[140:143], a[116:119], v[148:151]
v_mfma_f32_16x16x32_bf16 v[152:155], v[96:99], a[120:123], 0
v_add_u32 v25, s53, v24
v_mfma_f32_16x16x32_bf16 v[156:159], v[100:103], a[120:123], 0
v_lshrrev_b32 v26, 8, v25
v_mfma_f32_16x16x32_bf16 v[152:155], v[104:107], a[124:127], v[152:155]
v_and_or_b32 v25, v26, s52, v25
v_mfma_f32_16x16x32_bf16 v[156:159], v[108:111], a[124:127], v[156:159]
s_add_u32 s53, s53, 1310720
v_mfma_f32_16x16x32_bf16 v[152:155], v[112:115], a[128:131], v[152:155]
v_mfma_f32_16x16x32_bf16 v[156:159], v[116:119], a[128:131], v[156:159]
v_mfma_f32_16x16x32_bf16 v[152:155], v[120:123], a[132:135], v[152:155]
v_cvt_pk_bf16_f32 v160, v144, v145
v_mfma_f32_16x16x32_bf16 v[156:159], v[124:127], a[132:135], v[156:159]
v_cvt_pk_bf16_f32 v161, v146, v147
v_mfma_f32_16x16x32_bf16 v[152:155], v[128:131], a[136:139], v[152:155]
v_cvt_pk_bf16_f32 v162, v148, v149
v_mfma_f32_16x16x32_bf16 v[156:159], v[132:135], a[136:139], v[156:159]
v_cvt_pk_bf16_f32 v163, v150, v151
v_mfma_f32_16x16x32_bf16 v[152:155], v[136:139], a[140:143], v[152:155]
buffer_store_dwordx4 v[160:163], v25, s[40:43], 0 offen sc1
v_mfma_f32_16x16x32_bf16 v[156:159], v[140:143], a[140:143], v[156:159]
v_mfma_f32_16x16x32_bf16 v[144:147], v[96:99], a[144:147], 0
v_add_u32 v25, s53, v24
v_mfma_f32_16x16x32_bf16 v[148:151], v[100:103], a[144:147], 0
v_lshrrev_b32 v26, 8, v25
v_mfma_f32_16x16x32_bf16 v[144:147], v[104:107], a[148:151], v[144:147]
v_and_or_b32 v25, v26, s52, v25
v_mfma_f32_16x16x32_bf16 v[148:151], v[108:111], a[148:151], v[148:151]
s_add_u32 s53, s53, 1310720
v_mfma_f32_16x16x32_bf16 v[144:147], v[112:115], a[152:155], v[144:147]
v_mfma_f32_16x16x32_bf16 v[148:151], v[116:119], a[152:155], v[148:151]
v_mfma_f32_16x16x32_bf16 v[144:147], v[120:123], a[156:159], v[144:147]
v_cvt_pk_bf16_f32 v164, v152, v153
v_mfma_f32_16x16x32_bf16 v[148:151], v[124:127], a[156:159], v[148:151]
v_cvt_pk_bf16_f32 v165, v154, v155
v_mfma_f32_16x16x32_bf16 v[144:147], v[128:131], a[160:163], v[144:147]
v_cvt_pk_bf16_f32 v166, v156, v157
v_mfma_f32_16x16x32_bf16 v[148:151], v[132:135], a[160:163], v[148:151]
v_cvt_pk_bf16_f32 v167, v158, v159
v_mfma_f32_16x16x32_bf16 v[144:147], v[136:139], a[164:167], v[144:147]
buffer_store_dwordx4 v[164:167], v25, s[40:43], 0 offen sc1
v_mfma_f32_16x16x32_bf16 v[148:151], v[140:143], a[164:167], v[148:151]
v_mfma_f32_16x16x32_bf16 v[152:155], v[96:99], a[168:171], 0
v_add_u32 v25, s53, v24
v_mfma_f32_16x16x32_bf16 v[156:159], v[100:103], a[168:171], 0
v_lshrrev_b32 v26, 8, v25
v_mfma_f32_16x16x32_bf16 v[152:155], v[104:107], a[172:175], v[152:155]
v_and_or_b32 v25, v26, s52, v25
v_mfma_f32_16x16x32_bf16 v[156:159], v[108:111], a[172:175], v[156:159]
s_add_u32 s53, s53, 1310720
v_mfma_f32_16x16x32_bf16 v[152:155], v[112:115], a[176:179], v[152:155]
v_mfma_f32_16x16x32_bf16 v[156:159], v[116:119], a[176:179], v[156:159]
v_mfma_f32_16x16x32_bf16 v[152:155], v[120:123], a[180:183], v[152:155]
v_cvt_pk_bf16_f32 v160, v144, v145
v_mfma_f32_16x16x32_bf16 v[156:159], v[124:127], a[180:183], v[156:159]
v_cvt_pk_bf16_f32 v161, v146, v147
v_mfma_f32_16x16x32_bf16 v[152:155], v[128:131], a[184:187], v[152:155]
v_cvt_pk_bf16_f32 v162, v148, v149
v_mfma_f32_16x16x32_bf16 v[156:159], v[132:135], a[184:187], v[156:159]
v_cvt_pk_bf16_f32 v163, v150, v151
v_mfma_f32_16x16x32_bf16 v[152:155], v[136:139], a[188:191], v[152:155]
buffer_store_dwordx4 v[160:163], v25, s[40:43], 0 offen sc1
v_mfma_f32_16x16x32_bf16 v[156:159], v[140:143], a[188:191], v[156:159]
v_mfma_f32_16x16x32_bf16 v[144:147], v[96:99], a[192:195], 0
v_add_u32 v25, s53, v24
v_mfma_f32_16x16x32_bf16 v[148:151], v[100:103], a[192:195], 0
v_lshrrev_b32 v26, 8, v25
v_mfma_f32_16x16x32_bf16 v[144:147], v[104:107], a[196:199], v[144:147]
v_and_or_b32 v25, v26, s52, v25
v_mfma_f32_16x16x32_bf16 v[148:151], v[108:111], a[196:199], v[148:151]
s_add_u32 s53, s53, 1310720
v_mfma_f32_16x16x32_bf16 v[144:147], v[112:115], a[200:203], v[144:147]
v_mfma_f32_16x16x32_bf16 v[148:151], v[116:119], a[200:203], v[148:151]
v_mfma_f32_16x16x32_bf16 v[144:147], v[120:123], a[204:207], v[144:147]
v_cvt_pk_bf16_f32 v164, v152, v153
v_mfma_f32_16x16x32_bf16 v[148:151], v[124:127], a[204:207], v[148:151]
v_cvt_pk_bf16_f32 v165, v154, v155
v_mfma_f32_16x16x32_bf16 v[144:147], v[128:131], a[208:211], v[144:147]
v_cvt_pk_bf16_f32 v166, v156, v157
v_mfma_f32_16x16x32_bf16 v[148:151], v[132:135], a[208:211], v[148:151]
v_cvt_pk_bf16_f32 v167, v158, v159
)"
R"(v_mfma_f32_16x16x32_bf16 v[144:147], v[136:139], a[212:215], v[144:147]
buffer_store_dwordx4 v[164:167], v25, s[40:43], 0 offen sc1
v_mfma_f32_16x16x32_bf16 v[148:151], v[140:143], a[212:215], v[148:151]
v_mfma_f32_16x16x32_bf16 v[152:155], v[96:99], a[216:219], 0
v_add_u32 v25, s53, v24
v_mfma_f32_16x16x32_bf16 v[156:159], v[100:103], a[216:219], 0
v_lshrrev_b32 v26, 8, v25
v_mfma_f32_16x16x32_bf16 v[152:155], v[104:107], a[220:223], v[152:155]
v_and_or_b32 v25, v26, s52, v25
v_mfma_f32_16x16x32_bf16 v[156:159], v[108:111], a[220:223], v[156:159]
s_add_u32 s53, s53, 1310720
v_mfma_f32_16x16x32_bf16 v[152:155], v[112:115], a[224:227], v[152:155]
v_mfma_f32_16x16x32_bf16 v[156:159], v[116:119], a[224:227], v[156:159]
v_mfma_f32_16x16x32_bf16 v[152:155], v[120:123], a[228:231], v[152:155]
v_cvt_pk_bf16_f32 v160, v144, v145
v_mfma_f32_16x16x32_bf16 v[156:159], v[124:127], a[228:231], v[156:159]
v_cvt_pk_bf16_f32 v161, v146, v147
v_mfma_f32_16x16x32_bf16 v[152:155], v[128:131], a[232:235], v[152:155]
v_cvt_pk_bf16_f32 v162, v148, v149
v_mfma_f32_16x16x32_bf16 v[156:159], v[132:135], a[232:235], v[156:159]
v_cvt_pk_bf16_f32 v163, v150, v151
v_mfma_f32_16x16x32_bf16 v[152:155], v[136:139], a[236:239], v[152:155]
buffer_store_dwordx4 v[160:163], v25, s[40:43], 0 offen sc1
v_mfma_f32_16x16x32_bf16 v[156:159], v[140:143], a[236:239], v[156:159]
v_add_u32 v25, s53, v24
v_lshrrev_b32 v26, 8, v25
v_and_or_b32 v25, v26, s52, v25
s_add_u32 s53, s53, 1310720
s_nop 1
v_cvt_pk_bf16_f32 v164, v152, v153
v_cvt_pk_bf16_f32 v165, v154, v155
v_cvt_pk_bf16_f32 v166, v156, v157
v_cvt_pk_bf16_f32 v167, v158, v159
buffer_store_dwordx4 v[164:167], v25, s[40:43], 0 offen sc1
s_add_u32 s50, s50, 1024
s_sub_u32 s51, s51, 1
s_cmp_lg_u32 s51, 0
s_cbranch_scc1 hp_bg_%=
s_cmp_eq_u32 s45, 0
s_cselect_b32 s59, 1, 0
v_lshlrev_b32 v26, 7, v15
v_cmp_gt_u32 vcc, s59, v15
v_mov_b32 v27, 2147483632
s_nop 0
v_cndmask_b32 v26, v27, v26, vcc
v_mov_b32 v27, 1
s_and_b32 s54, s84, 7
s_lshl_b32 s54, s54, 7
s_add_u32 s54, s54, 2374912
s_waitcnt vmcnt(0)
s_waitcnt lgkmcnt(0)
s_barrier
buffer_atomic_add v27, v26, s[40:43], s54 offen
s_cmp_eq_u32 s46, 4
s_nop 0
s_cbranch_scc1 hp_layer_%=
s_mov_b32 s76, s68
s_mov_b32 s77, s69
hp_ldw_%=:
v_add_u32 v22, 0, v15
v_mul_u32_u24 v16, 2731, v22
v_lshrrev_b32 v16, 16, v16
v_mul_u32_u24 v16, 11904, v16
v_lshl_add_u32 v16, v22, 4, v16
v_add_u32 v22, 64, v15
v_mul_u32_u24 v17, 2731, v22
v_lshrrev_b32 v17, 16, v17
v_mul_u32_u24 v17, 11904, v17
v_lshl_add_u32 v17, v22, 4, v17
v_add_u32 v22, 128, v15
v_mul_u32_u24 v18, 2731, v22
v_lshrrev_b32 v18, 16, v18
v_mul_u32_u24 v18, 11904, v18
v_lshl_add_u32 v18, v22, 4, v18
v_bfe_u32 v19, v15, 2, 1
v_and_b32 v20, 8, v15
v_add_u32 v19, v19, v20
v_bfe_u32 v20, v15, 4, 1
v_lshl_add_u32 v19, v20, 2, v19
v_lshrrev_b32 v20, 5, v15
v_lshl_add_u32 v19, v20, 4, v19
v_mul_u32_u24 v19, 384, v19
v_and_b32 v20, 3, v15
v_lshl_add_u32 v19, v20, 3, v19
s_mul_i32 s48, s45, 96
v_add_u32 v19, s48, v19
v_add_u32 v20, 49152, v19
v_add_u32 v21, 98304, v19
s_mul_i32 s50, s45, 196608
s_mul_i32 s48, s44, 384
s_add_u32 s50, s50, s48
s_mul_i32 s51, s45, 6144
s_add_u32 m0, s51, 0
s_add_u32 s49, s50, 0
buffer_load_dwordx4 v16, s[76:79], s49 offen lds
s_add_u32 m0, s51, 1024
s_add_u32 s49, s50, 0
buffer_load_dwordx4 v17, s[76:79], s49 offen lds
s_add_u32 m0, s51, 2048
s_add_u32 s49, s50, 0
buffer_load_dwordx4 v18, s[76:79], s49 offen lds
s_add_u32 m0, s51, 3072
s_add_u32 s49, s50, 98304
buffer_load_dwordx4 v16, s[76:79], s49 offen lds
s_add_u32 m0, s51, 4096
s_add_u32 s49, s50, 98304
buffer_load_dwordx4 v17, s[76:79], s49 offen lds
s_add_u32 m0, s51, 5120
s_add_u32 s49, s50, 98304
buffer_load_dwordx4 v18, s[76:79], s49 offen lds
s_add_u32 m0, s51, 24576
s_add_u32 s49, s50, 786432
buffer_load_dwordx4 v16, s[76:79], s49 offen lds
s_add_u32 m0, s51, 25600
s_add_u32 s49, s50, 786432
buffer_load_dwordx4 v17, s[76:79], s49 offen lds
s_add_u32 m0, s51, 26624
s_add_u32 s49, s50, 786432
buffer_load_dwordx4 v18, s[76:79], s49 offen lds
s_add_u32 m0, s51, 27648
s_add_u32 s49, s50, 884736
buffer_load_dwordx4 v16, s[76:79], s49 offen lds
s_add_u32 m0, s51, 28672
s_add_u32 s49, s50, 884736
buffer_load_dwordx4 v17, s[76:79], s49 offen lds
s_add_u32 m0, s51, 29696
s_add_u32 s49, s50, 884736
buffer_load_dwordx4 v18, s[76:79], s49 offen lds
s_add_u32 m0, s51, 49152
s_add_u32 s49, s50, 1572864
buffer_load_dwordx4 v16, s[76:79], s49 offen lds
s_add_u32 m0, s51, 50176
s_add_u32 s49, s50, 1572864
buffer_load_dwordx4 v17, s[76:79], s49 offen lds
s_add_u32 m0, s51, 51200
s_add_u32 s49, s50, 1572864
buffer_load_dwordx4 v18, s[76:79], s49 offen lds
s_add_u32 m0, s51, 52224
s_add_u32 s49, s50, 1671168
buffer_load_dwordx4 v16, s[76:79], s49 offen lds
s_add_u32 m0, s51, 53248
s_add_u32 s49, s50, 1671168
buffer_load_dwordx4 v17, s[76:79], s49 offen lds
s_add_u32 m0, s51, 54272
s_add_u32 s49, s50, 1671168
buffer_load_dwordx4 v18, s[76:79], s49 offen lds
s_add_u32 m0, s51, 73728
s_add_u32 s49, s50, 2359296
buffer_load_dwordx4 v16, s[76:79], s49 offen lds
s_add_u32 m0, s51, 74752
s_add_u32 s49, s50, 2359296
buffer_load_dwordx4 v17, s[76:79], s49 offen lds
s_add_u32 m0, s51, 75776
s_add_u32 s49, s50, 2359296
buffer_load_dwordx4 v18, s[76:79], s49 offen lds
s_add_u32 m0, s51, 76800
s_add_u32 s49, s50, 2457600
buffer_load_dwordx4 v16, s[76:79], s49 offen lds
s_add_u32 m0, s51, 77824
s_add_u32 s49, s50, 2457600
buffer_load_dwordx4 v17, s[76:79], s49 offen lds
s_add_u32 m0, s51, 78848
s_add_u32 s49, s50, 2457600
buffer_load_dwordx4 v18, s[76:79], s49 offen lds
s_add_u32 m0, s51, 98304
s_add_u32 s49, s50, 3145728
buffer_load_dwordx4 v16, s[76:79], s49 offen lds
s_add_u32 m0, s51, 99328
s_add_u32 s49, s50, 3145728
buffer_load_dwordx4 v17, s[76:79], s49 offen lds
s_add_u32 m0, s51, 100352
s_add_u32 s49, s50, 3145728
buffer_load_dwordx4 v18, s[76:79], s49 offen lds
s_add_u32 m0, s51, 101376
s_add_u32 s49, s50, 3244032
buffer_load_dwordx4 v16, s[76:79], s49 offen lds
s_add_u32 m0, s51, 102400
s_add_u32 s49, s50, 3244032
buffer_load_dwordx4 v17, s[76:79], s49 offen lds
s_add_u32 m0, s51, 103424
s_add_u32 s49, s50, 3244032
buffer_load_dwordx4 v18, s[76:79], s49 offen lds
s_waitcnt vmcnt(24) lgkmcnt(0)
s_barrier
s_add_u32 m0, s51, 122880
s_add_u32 s49, s50, 3932160
buffer_load_dwordx4 v16, s[76:79], s49 offen lds
s_add_u32 m0, s51, 123904
s_add_u32 s49, s50, 3932160
buffer_load_dwordx4 v17, s[76:79], s49 offen lds
s_add_u32 m0, s51, 124928
s_add_u32 s49, s50, 3932160
buffer_load_dwordx4 v18, s[76:79], s49 offen lds
s_add_u32 m0, s51, 125952
s_add_u32 s49, s50, 4030464
buffer_load_dwordx4 v16, s[76:79], s49 offen lds
s_add_u32 m0, s51, 126976
s_add_u32 s49, s50, 4030464
buffer_load_dwordx4 v17, s[76:79], s49 offen lds
s_add_u32 m0, s51, 128000
)"
R"(s_add_u32 s49, s50, 4030464
buffer_load_dwordx4 v18, s[76:79], s49 offen lds
ds_read_b64_tr_b16 a[0:1], v19 offset:0
ds_read_b64_tr_b16 a[2:3], v19 offset:768
ds_read_b64_tr_b16 a[4:5], v19 offset:12288
ds_read_b64_tr_b16 a[6:7], v19 offset:13056
ds_read_b64_tr_b16 a[80:81], v19 offset:32
ds_read_b64_tr_b16 a[82:83], v19 offset:800
ds_read_b64_tr_b16 a[84:85], v19 offset:12320
ds_read_b64_tr_b16 a[86:87], v19 offset:13088
ds_read_b64_tr_b16 a[160:161], v19 offset:64
ds_read_b64_tr_b16 a[162:163], v19 offset:832
ds_read_b64_tr_b16 a[164:165], v19 offset:12352
ds_read_b64_tr_b16 a[166:167], v19 offset:13120
s_waitcnt vmcnt(24) lgkmcnt(0)
s_barrier
s_add_u32 m0, s51, 0
s_add_u32 s49, s50, 4718592
buffer_load_dwordx4 v16, s[76:79], s49 offen lds
s_add_u32 m0, s51, 1024
s_add_u32 s49, s50, 4718592
buffer_load_dwordx4 v17, s[76:79], s49 offen lds
s_add_u32 m0, s51, 2048
s_add_u32 s49, s50, 4718592
buffer_load_dwordx4 v18, s[76:79], s49 offen lds
s_add_u32 m0, s51, 3072
s_add_u32 s49, s50, 4816896
buffer_load_dwordx4 v16, s[76:79], s49 offen lds
s_add_u32 m0, s51, 4096
s_add_u32 s49, s50, 4816896
buffer_load_dwordx4 v17, s[76:79], s49 offen lds
s_add_u32 m0, s51, 5120
s_add_u32 s49, s50, 4816896
buffer_load_dwordx4 v18, s[76:79], s49 offen lds
ds_read_b64_tr_b16 a[8:9], v19 offset:24576
ds_read_b64_tr_b16 a[10:11], v19 offset:25344
ds_read_b64_tr_b16 a[12:13], v19 offset:36864
ds_read_b64_tr_b16 a[14:15], v19 offset:37632
ds_read_b64_tr_b16 a[88:89], v19 offset:24608
ds_read_b64_tr_b16 a[90:91], v19 offset:25376
ds_read_b64_tr_b16 a[92:93], v19 offset:36896
ds_read_b64_tr_b16 a[94:95], v19 offset:37664
ds_read_b64_tr_b16 a[168:169], v19 offset:24640
ds_read_b64_tr_b16 a[170:171], v19 offset:25408
ds_read_b64_tr_b16 a[172:173], v19 offset:36928
ds_read_b64_tr_b16 a[174:175], v19 offset:37696
s_waitcnt vmcnt(24) lgkmcnt(0)
s_barrier
s_add_u32 m0, s51, 24576
s_add_u32 s49, s50, 5505024
buffer_load_dwordx4 v16, s[76:79], s49 offen lds
s_add_u32 m0, s51, 25600
s_add_u32 s49, s50, 5505024
buffer_load_dwordx4 v17, s[76:79], s49 offen lds
s_add_u32 m0, s51, 26624
s_add_u32 s49, s50, 5505024
buffer_load_dwordx4 v18, s[76:79], s49 offen lds
s_add_u32 m0, s51, 27648
s_add_u32 s49, s50, 5603328
buffer_load_dwordx4 v16, s[76:79], s49 offen lds
s_add_u32 m0, s51, 28672
s_add_u32 s49, s50, 5603328
buffer_load_dwordx4 v17, s[76:79], s49 offen lds
s_add_u32 m0, s51, 29696
s_add_u32 s49, s50, 5603328
buffer_load_dwordx4 v18, s[76:79], s49 offen lds
ds_read_b64_tr_b16 a[16:17], v20 offset:0
ds_read_b64_tr_b16 a[18:19], v20 offset:768
ds_read_b64_tr_b16 a[20:21], v20 offset:12288
ds_read_b64_tr_b16 a[22:23], v20 offset:13056
ds_read_b64_tr_b16 a[96:97], v20 offset:32
ds_read_b64_tr_b16 a[98:99], v20 offset:800
ds_read_b64_tr_b16 a[100:101], v20 offset:12320
ds_read_b64_tr_b16 a[102:103], v20 offset:13088
ds_read_b64_tr_b16 a[176:177], v20 offset:64
ds_read_b64_tr_b16 a[178:179], v20 offset:832
ds_read_b64_tr_b16 a[180:181], v20 offset:12352
ds_read_b64_tr_b16 a[182:183], v20 offset:13120
s_waitcnt vmcnt(24) lgkmcnt(0)
s_barrier
s_add_u32 m0, s51, 49152
s_add_u32 s49, s50, 6291456
buffer_load_dwordx4 v16, s[76:79], s49 offen lds
s_add_u32 m0, s51, 50176
s_add_u32 s49, s50, 6291456
buffer_load_dwordx4 v17, s[76:79], s49 offen lds
s_add_u32 m0, s51, 51200
s_add_u32 s49, s50, 6291456
buffer_load_dwordx4 v18, s[76:79], s49 offen lds
s_add_u32 m0, s51, 52224
s_add_u32 s49, s50, 6389760
buffer_load_dwordx4 v16, s[76:79], s49 offen lds
s_add_u32 m0, s51, 53248
s_add_u32 s49, s50, 6389760
buffer_load_dwordx4 v17, s[76:79], s49 offen lds
s_add_u32 m0, s51, 54272
s_add_u32 s49, s50, 6389760
buffer_load_dwordx4 v18, s[76:79], s49 offen lds
ds_read_b64_tr_b16 a[24:25], v20 offset:24576
ds_read_b64_tr_b16 a[26:27], v20 offset:25344
ds_read_b64_tr_b16 a[28:29], v20 offset:36864
ds_read_b64_tr_b16 a[30:31], v20 offset:37632
ds_read_b64_tr_b16 a[104:105], v20 offset:24608
ds_read_b64_tr_b16 a[106:107], v20 offset:25376
ds_read_b64_tr_b16 a[108:109], v20 offset:36896
ds_read_b64_tr_b16 a[110:111], v20 offset:37664
ds_read_b64_tr_b16 a[184:185], v20 offset:24640
ds_read_b64_tr_b16 a[186:187], v20 offset:25408
ds_read_b64_tr_b16 a[188:189], v20 offset:36928
ds_read_b64_tr_b16 a[190:191], v20 offset:37696
s_waitcnt vmcnt(24) lgkmcnt(0)
s_barrier
s_add_u32 m0, s51, 73728
s_add_u32 s49, s50, 7077888
buffer_load_dwordx4 v16, s[76:79], s49 offen lds
s_add_u32 m0, s51, 74752
s_add_u32 s49, s50, 7077888
buffer_load_dwordx4 v17, s[76:79], s49 offen lds
s_add_u32 m0, s51, 75776
s_add_u32 s49, s50, 7077888
buffer_load_dwordx4 v18, s[76:79], s49 offen lds
s_add_u32 m0, s51, 76800
s_add_u32 s49, s50, 7176192
buffer_load_dwordx4 v16, s[76:79], s49 offen lds
s_add_u32 m0, s51, 77824
s_add_u32 s49, s50, 7176192
buffer_load_dwordx4 v17, s[76:79], s49 offen lds
s_add_u32 m0, s51, 78848
s_add_u32 s49, s50, 7176192
buffer_load_dwordx4 v18, s[76:79], s49 offen lds
ds_read_b64_tr_b16 a[32:33], v21 offset:0
ds_read_b64_tr_b16 a[34:35], v21 offset:768
ds_read_b64_tr_b16 a[36:37], v21 offset:12288
ds_read_b64_tr_b16 a[38:39], v21 offset:13056
ds_read_b64_tr_b16 a[112:113], v21 offset:32
ds_read_b64_tr_b16 a[114:115], v21 offset:800
ds_read_b64_tr_b16 a[116:117], v21 offset:12320
ds_read_b64_tr_b16 a[118:119], v21 offset:13088
ds_read_b64_tr_b16 a[192:193], v21 offset:64
ds_read_b64_tr_b16 a[194:195], v21 offset:832
ds_read_b64_tr_b16 a[196:197], v21 offset:12352
ds_read_b64_tr_b16 a[198:199], v21 offset:13120
s_waitcnt vmcnt(24) lgkmcnt(0)
s_barrier
ds_read_b64_tr_b16 a[40:41], v21 offset:24576
ds_read_b64_tr_b16 a[42:43], v21 offset:25344
ds_read_b64_tr_b16 a[44:45], v21 offset:36864
ds_read_b64_tr_b16 a[46:47], v21 offset:37632
ds_read_b64_tr_b16 a[120:121], v21 offset:24608
ds_read_b64_tr_b16 a[122:123], v21 offset:25376
ds_read_b64_tr_b16 a[124:125], v21 offset:36896
ds_read_b64_tr_b16 a[126:127], v21 offset:37664
ds_read_b64_tr_b16 a[200:201], v21 offset:24640
ds_read_b64_tr_b16 a[202:203], v21 offset:25408
ds_read_b64_tr_b16 a[204:205], v21 offset:36928
ds_read_b64_tr_b16 a[206:207], v21 offset:37696
s_waitcnt vmcnt(18) lgkmcnt(0)
s_barrier
ds_read_b64_tr_b16 a[48:49], v19 offset:0
ds_read_b64_tr_b16 a[50:51], v19 offset:768
ds_read_b64_tr_b16 a[52:53], v19 offset:12288
ds_read_b64_tr_b16 a[54:55], v19 offset:13056
ds_read_b64_tr_b16 a[128:129], v19 offset:32
ds_read_b64_tr_b16 a[130:131], v19 offset:800
ds_read_b64_tr_b16 a[132:133], v19 offset:12320
ds_read_b64_tr_b16 a[134:135], v19 offset:13088
ds_read_b64_tr_b16 a[208:209], v19 offset:64
ds_read_b64_tr_b16 a[210:211], v19 offset:832
ds_read_b64_tr_b16 a[212:213], v19 offset:12352
ds_read_b64_tr_b16 a[214:215], v19 offset:13120
s_waitcnt vmcnt(12) lgkmcnt(0)
s_barrier
ds_read_b64_tr_b16 a[56:57], v19 offset:24576
ds_read_b64_tr_b16 a[58:59], v19 offset:25344
ds_read_b64_tr_b16 a[60:61], v19 offset:36864
ds_read_b64_tr_b16 a[62:63], v19 offset:37632
ds_read_b64_tr_b16 a[136:137], v19 offset:24608
ds_read_b64_tr_b16 a[138:139], v19 offset:25376
ds_read_b64_tr_b16 a[140:141], v19 offset:36896
ds_read_b64_tr_b16 a[142:143], v19 offset:37664
ds_read_b64_tr_b16 a[216:217], v19 offset:24640
ds_read_b64_tr_b16 a[218:219], v19 offset:25408
ds_read_b64_tr_b16 a[220:221], v19 offset:36928
ds_read_b64_tr_b16 a[222:223], v19 offset:37696
s_waitcnt vmcnt(6) lgkmcnt(0)
s_barrier
ds_read_b64_tr_b16 a[64:65], v20 offset:0
ds_read_b64_tr_b16 a[66:67], v20 offset:768
ds_read_b64_tr_b16 a[68:69], v20 offset:12288
ds_read_b64_tr_b16 a[70:71], v20 offset:13056
ds_read_b64_tr_b16 a[144:145], v20 offset:32
ds_read_b64_tr_b16 a[146:147], v20 offset:800
ds_read_b64_tr_b16 a[148:149], v20 offset:12320
ds_read_b64_tr_b16 a[150:151], v20 offset:13088
ds_read_b64_tr_b16 a[224:225], v20 offset:64
ds_read_b64_tr_b16 a[226:227], v20 offset:832
ds_read_b64_tr_b16 a[228:229], v20 offset:12352
ds_read_b64_tr_b16 a[230:231], v20 offset:13120
s_waitcnt vmcnt(0) lgkmcnt(0)
s_barrier
)"
R"(ds_read_b64_tr_b16 a[72:73], v20 offset:24576
ds_read_b64_tr_b16 a[74:75], v20 offset:25344
ds_read_b64_tr_b16 a[76:77], v20 offset:36864
ds_read_b64_tr_b16 a[78:79], v20 offset:37632
ds_read_b64_tr_b16 a[152:153], v20 offset:24608
ds_read_b64_tr_b16 a[154:155], v20 offset:25376
ds_read_b64_tr_b16 a[156:157], v20 offset:36896
ds_read_b64_tr_b16 a[158:159], v20 offset:37664
ds_read_b64_tr_b16 a[232:233], v20 offset:24640
ds_read_b64_tr_b16 a[234:235], v20 offset:25408
ds_read_b64_tr_b16 a[236:237], v20 offset:36928
ds_read_b64_tr_b16 a[238:239], v20 offset:37696
s_waitcnt lgkmcnt(0)
s_barrier
s_cmp_eq_u32 s46, 0
s_cbranch_scc1 hp_layer_%=
s_branch hp_layer_%=
hp_ldo_%=:
v_lshrrev_b32 v16, 2, v15
v_mul_u32_u24 v16, 1280, v16
v_and_b32 v17, 3, v15
v_lshl_add_u32 v16, v17, 3, v16
s_mul_i32 s48, s45, 320
v_add_u32 v16, s48, v16
v_add_u32 v17, 40960, v16
v_add_u32 v18, 81920, v16
s_mul_i32 s51, s45, 10240
s_mul_i32 s50, s44, 245760
s_add_u32 s50, s50, s51
s_lshr_b32 s52, s45, 1
s_mul_i32 s52, s52, 40960
s_add_u32 s52, s52, s50
s_add_u32 m0, s51, 0
s_add_u32 s49, s50, 0
buffer_load_dwordx4 v0, s[76:79], s49 offen offset:0 lds
buffer_load_dwordx4 v0, s[76:79], s49 offen offset:1024 lds
buffer_load_dwordx4 v0, s[76:79], s49 offen offset:2048 lds
buffer_load_dwordx4 v0, s[76:79], s49 offen offset:3072 lds
s_add_u32 m0, s51, 4096
s_add_u32 s49, s50, 4096
buffer_load_dwordx4 v0, s[76:79], s49 offen offset:0 lds
buffer_load_dwordx4 v0, s[76:79], s49 offen offset:1024 lds
buffer_load_dwordx4 v0, s[76:79], s49 offen offset:2048 lds
buffer_load_dwordx4 v0, s[76:79], s49 offen offset:3072 lds
s_add_u32 m0, s51, 8192
s_add_u32 s49, s50, 8192
buffer_load_dwordx4 v0, s[76:79], s49 offen offset:0 lds
buffer_load_dwordx4 v0, s[76:79], s49 offen offset:1024 lds
s_add_u32 m0, s51, 40960
s_add_u32 s49, s50, 61440
buffer_load_dwordx4 v0, s[76:79], s49 offen offset:0 lds
buffer_load_dwordx4 v0, s[76:79], s49 offen offset:1024 lds
buffer_load_dwordx4 v0, s[76:79], s49 offen offset:2048 lds
buffer_load_dwordx4 v0, s[76:79], s49 offen offset:3072 lds
s_add_u32 m0, s51, 45056
s_add_u32 s49, s50, 65536
buffer_load_dwordx4 v0, s[76:79], s49 offen offset:0 lds
buffer_load_dwordx4 v0, s[76:79], s49 offen offset:1024 lds
buffer_load_dwordx4 v0, s[76:79], s49 offen offset:2048 lds
buffer_load_dwordx4 v0, s[76:79], s49 offen offset:3072 lds
s_add_u32 m0, s51, 49152
s_add_u32 s49, s50, 69632
buffer_load_dwordx4 v0, s[76:79], s49 offen offset:0 lds
buffer_load_dwordx4 v0, s[76:79], s49 offen offset:1024 lds
s_waitcnt vmcnt(10) lgkmcnt(0)
s_barrier
s_add_u32 m0, s51, 81920
s_add_u32 s49, s50, 122880
buffer_load_dwordx4 v0, s[76:79], s49 offen offset:0 lds
buffer_load_dwordx4 v0, s[76:79], s49 offen offset:1024 lds
buffer_load_dwordx4 v0, s[76:79], s49 offen offset:2048 lds
buffer_load_dwordx4 v0, s[76:79], s49 offen offset:3072 lds
s_add_u32 m0, s51, 86016
s_add_u32 s49, s50, 126976
buffer_load_dwordx4 v0, s[76:79], s49 offen offset:0 lds
buffer_load_dwordx4 v0, s[76:79], s49 offen offset:1024 lds
buffer_load_dwordx4 v0, s[76:79], s49 offen offset:2048 lds
buffer_load_dwordx4 v0, s[76:79], s49 offen offset:3072 lds
s_add_u32 m0, s51, 90112
s_add_u32 s49, s50, 131072
buffer_load_dwordx4 v0, s[76:79], s49 offen offset:0 lds
buffer_load_dwordx4 v0, s[76:79], s49 offen offset:1024 lds
ds_read_b64_tr_b16 a[0:1], v16 offset:0
ds_read_b64_tr_b16 a[2:3], v16 offset:20480
ds_read_b64_tr_b16 a[24:25], v16 offset:32
ds_read_b64_tr_b16 a[26:27], v16 offset:20512
ds_read_b64_tr_b16 a[48:49], v16 offset:64
ds_read_b64_tr_b16 a[50:51], v16 offset:20544
ds_read_b64_tr_b16 a[72:73], v16 offset:96
ds_read_b64_tr_b16 a[74:75], v16 offset:20576
ds_read_b64_tr_b16 a[96:97], v16 offset:128
ds_read_b64_tr_b16 a[98:99], v16 offset:20608
ds_read_b64_tr_b16 a[120:121], v16 offset:160
ds_read_b64_tr_b16 a[122:123], v16 offset:20640
ds_read_b64_tr_b16 a[144:145], v16 offset:192
ds_read_b64_tr_b16 a[146:147], v16 offset:20672
ds_read_b64_tr_b16 a[168:169], v16 offset:224
ds_read_b64_tr_b16 a[170:171], v16 offset:20704
s_waitcnt lgkmcnt(14)
ds_read_b64_tr_b16 a[192:193], v16 offset:256
ds_read_b64_tr_b16 a[194:195], v16 offset:20736
s_waitcnt lgkmcnt(14)
ds_read_b64_tr_b16 a[216:217], v16 offset:288
ds_read_b64_tr_b16 a[218:219], v16 offset:20768
s_waitcnt lgkmcnt(14)
s_waitcnt vmcnt(10) lgkmcnt(0)
s_barrier
s_add_u32 m0, s51, 0
s_add_u32 s49, s50, 184320
buffer_load_dwordx4 v0, s[76:79], s49 offen offset:0 lds
buffer_load_dwordx4 v0, s[76:79], s49 offen offset:1024 lds
buffer_load_dwordx4 v0, s[76:79], s49 offen offset:2048 lds
buffer_load_dwordx4 v0, s[76:79], s49 offen offset:3072 lds
s_add_u32 m0, s51, 4096
s_add_u32 s49, s50, 188416
buffer_load_dwordx4 v0, s[76:79], s49 offen offset:0 lds
buffer_load_dwordx4 v0, s[76:79], s49 offen offset:1024 lds
buffer_load_dwordx4 v0, s[76:79], s49 offen offset:2048 lds
buffer_load_dwordx4 v0, s[76:79], s49 offen offset:3072 lds
s_add_u32 m0, s51, 8192
s_add_u32 s49, s50, 192512
buffer_load_dwordx4 v0, s[76:79], s49 offen offset:0 lds
buffer_load_dwordx4 v0, s[76:79], s49 offen offset:1024 lds
ds_read_b64_tr_b16 a[4:5], v17 offset:0
ds_read_b64_tr_b16 a[6:7], v17 offset:20480
ds_read_b64_tr_b16 a[28:29], v17 offset:32
ds_read_b64_tr_b16 a[30:31], v17 offset:20512
ds_read_b64_tr_b16 a[52:53], v17 offset:64
ds_read_b64_tr_b16 a[54:55], v17 offset:20544
ds_read_b64_tr_b16 a[76:77], v17 offset:96
ds_read_b64_tr_b16 a[78:79], v17 offset:20576
ds_read_b64_tr_b16 a[100:101], v17 offset:128
ds_read_b64_tr_b16 a[102:103], v17 offset:20608
ds_read_b64_tr_b16 a[124:125], v17 offset:160
ds_read_b64_tr_b16 a[126:127], v17 offset:20640
ds_read_b64_tr_b16 a[148:149], v17 offset:192
ds_read_b64_tr_b16 a[150:151], v17 offset:20672
ds_read_b64_tr_b16 a[172:173], v17 offset:224
ds_read_b64_tr_b16 a[174:175], v17 offset:20704
s_waitcnt lgkmcnt(14)
ds_read_b64_tr_b16 a[196:197], v17 offset:256
ds_read_b64_tr_b16 a[198:199], v17 offset:20736
s_waitcnt lgkmcnt(14)
ds_read_b64_tr_b16 a[220:221], v17 offset:288
ds_read_b64_tr_b16 a[222:223], v17 offset:20768
s_waitcnt lgkmcnt(14)
s_waitcnt vmcnt(10) lgkmcnt(0)
s_barrier
s_add_u32 m0, s51, 40960
s_add_u32 s49, s52, 40960
buffer_load_dwordx4 v0, s[76:79], s49 offen offset:0 lds
buffer_load_dwordx4 v0, s[76:79], s49 offen offset:1024 lds
buffer_load_dwordx4 v0, s[76:79], s49 offen offset:2048 lds
buffer_load_dwordx4 v0, s[76:79], s49 offen offset:3072 lds
s_add_u32 m0, s51, 45056
s_add_u32 s49, s52, 45056
buffer_load_dwordx4 v0, s[76:79], s49 offen offset:0 lds
buffer_load_dwordx4 v0, s[76:79], s49 offen offset:1024 lds
buffer_load_dwordx4 v0, s[76:79], s49 offen offset:2048 lds
buffer_load_dwordx4 v0, s[76:79], s49 offen offset:3072 lds
s_add_u32 m0, s51, 49152
s_add_u32 s49, s52, 49152
buffer_load_dwordx4 v0, s[76:79], s49 offen offset:0 lds
buffer_load_dwordx4 v0, s[76:79], s49 offen offset:1024 lds
ds_read_b64_tr_b16 a[8:9], v18 offset:0
ds_read_b64_tr_b16 a[10:11], v18 offset:20480
ds_read_b64_tr_b16 a[32:33], v18 offset:32
ds_read_b64_tr_b16 a[34:35], v18 offset:20512
ds_read_b64_tr_b16 a[56:57], v18 offset:64
ds_read_b64_tr_b16 a[58:59], v18 offset:20544
ds_read_b64_tr_b16 a[80:81], v18 offset:96
ds_read_b64_tr_b16 a[82:83], v18 offset:20576
ds_read_b64_tr_b16 a[104:105], v18 offset:128
ds_read_b64_tr_b16 a[106:107], v18 offset:20608
ds_read_b64_tr_b16 a[128:129], v18 offset:160
ds_read_b64_tr_b16 a[130:131], v18 offset:20640
ds_read_b64_tr_b16 a[152:153], v18 offset:192
ds_read_b64_tr_b16 a[154:155], v18 offset:20672
ds_read_b64_tr_b16 a[176:177], v18 offset:224
ds_read_b64_tr_b16 a[178:179], v18 offset:20704
s_waitcnt lgkmcnt(14)
ds_read_b64_tr_b16 a[200:201], v18 offset:256
ds_read_b64_tr_b16 a[202:203], v18 offset:20736
s_waitcnt lgkmcnt(14)
ds_read_b64_tr_b16 a[224:225], v18 offset:288
ds_read_b64_tr_b16 a[226:227], v18 offset:20768
s_waitcnt lgkmcnt(14)
s_waitcnt vmcnt(10) lgkmcnt(0)
s_barrier
s_add_u32 m0, s51, 81920
s_add_u32 s49, s52, 163840
buffer_load_dwordx4 v0, s[76:79], s49 offen offset:0 lds
buffer_load_dwordx4 v0, s[76:79], s49 offen offset:1024 lds
buffer_load_dwordx4 v0, s[76:79], s49 offen offset:2048 lds
buffer_load_dwordx4 v0, s[76:79], s49 offen offset:3072 lds
s_add_u32 m0, s51, 86016
s_add_u32 s49, s52, 167936
buffer_load_dwordx4 v0, s[76:79], s49 offen offset:0 lds
buffer_load_dwordx4 v0, s[76:79], s49 offen offset:1024 lds
buffer_load_dwordx4 v0, s[76:79], s49 offen offset:2048 lds
)"
R"(buffer_load_dwordx4 v0, s[76:79], s49 offen offset:3072 lds
s_add_u32 m0, s51, 90112
s_add_u32 s49, s52, 172032
buffer_load_dwordx4 v0, s[76:79], s49 offen offset:0 lds
buffer_load_dwordx4 v0, s[76:79], s49 offen offset:1024 lds
ds_read_b64_tr_b16 a[12:13], v16 offset:0
ds_read_b64_tr_b16 a[14:15], v16 offset:20480
ds_read_b64_tr_b16 a[36:37], v16 offset:32
ds_read_b64_tr_b16 a[38:39], v16 offset:20512
ds_read_b64_tr_b16 a[60:61], v16 offset:64
ds_read_b64_tr_b16 a[62:63], v16 offset:20544
ds_read_b64_tr_b16 a[84:85], v16 offset:96
ds_read_b64_tr_b16 a[86:87], v16 offset:20576
ds_read_b64_tr_b16 a[108:109], v16 offset:128
ds_read_b64_tr_b16 a[110:111], v16 offset:20608
ds_read_b64_tr_b16 a[132:133], v16 offset:160
ds_read_b64_tr_b16 a[134:135], v16 offset:20640
ds_read_b64_tr_b16 a[156:157], v16 offset:192
ds_read_b64_tr_b16 a[158:159], v16 offset:20672
ds_read_b64_tr_b16 a[180:181], v16 offset:224
ds_read_b64_tr_b16 a[182:183], v16 offset:20704
s_waitcnt lgkmcnt(14)
ds_read_b64_tr_b16 a[204:205], v16 offset:256
ds_read_b64_tr_b16 a[206:207], v16 offset:20736
s_waitcnt lgkmcnt(14)
ds_read_b64_tr_b16 a[228:229], v16 offset:288
ds_read_b64_tr_b16 a[230:231], v16 offset:20768
s_waitcnt lgkmcnt(14)
s_waitcnt vmcnt(10) lgkmcnt(0)
s_barrier
ds_read_b64_tr_b16 a[16:17], v17 offset:0
ds_read_b64_tr_b16 a[18:19], v17 offset:20480
ds_read_b64_tr_b16 a[40:41], v17 offset:32
ds_read_b64_tr_b16 a[42:43], v17 offset:20512
ds_read_b64_tr_b16 a[64:65], v17 offset:64
ds_read_b64_tr_b16 a[66:67], v17 offset:20544
ds_read_b64_tr_b16 a[88:89], v17 offset:96
ds_read_b64_tr_b16 a[90:91], v17 offset:20576
ds_read_b64_tr_b16 a[112:113], v17 offset:128
ds_read_b64_tr_b16 a[114:115], v17 offset:20608
ds_read_b64_tr_b16 a[136:137], v17 offset:160
ds_read_b64_tr_b16 a[138:139], v17 offset:20640
ds_read_b64_tr_b16 a[160:161], v17 offset:192
ds_read_b64_tr_b16 a[162:163], v17 offset:20672
ds_read_b64_tr_b16 a[184:185], v17 offset:224
ds_read_b64_tr_b16 a[186:187], v17 offset:20704
s_waitcnt lgkmcnt(14)
ds_read_b64_tr_b16 a[208:209], v17 offset:256
ds_read_b64_tr_b16 a[210:211], v17 offset:20736
s_waitcnt lgkmcnt(14)
ds_read_b64_tr_b16 a[232:233], v17 offset:288
ds_read_b64_tr_b16 a[234:235], v17 offset:20768
s_waitcnt lgkmcnt(14)
s_waitcnt vmcnt(0) lgkmcnt(0)
s_barrier
ds_read_b64_tr_b16 a[20:21], v18 offset:0
ds_read_b64_tr_b16 a[22:23], v18 offset:20480
ds_read_b64_tr_b16 a[44:45], v18 offset:32
ds_read_b64_tr_b16 a[46:47], v18 offset:20512
ds_read_b64_tr_b16 a[68:69], v18 offset:64
ds_read_b64_tr_b16 a[70:71], v18 offset:20544
ds_read_b64_tr_b16 a[92:93], v18 offset:96
ds_read_b64_tr_b16 a[94:95], v18 offset:20576
ds_read_b64_tr_b16 a[116:117], v18 offset:128
ds_read_b64_tr_b16 a[118:119], v18 offset:20608
ds_read_b64_tr_b16 a[140:141], v18 offset:160
ds_read_b64_tr_b16 a[142:143], v18 offset:20640
ds_read_b64_tr_b16 a[164:165], v18 offset:192
ds_read_b64_tr_b16 a[166:167], v18 offset:20672
ds_read_b64_tr_b16 a[188:189], v18 offset:224
ds_read_b64_tr_b16 a[190:191], v18 offset:20704
s_waitcnt lgkmcnt(14)
ds_read_b64_tr_b16 a[212:213], v18 offset:256
ds_read_b64_tr_b16 a[214:215], v18 offset:20736
s_waitcnt lgkmcnt(14)
ds_read_b64_tr_b16 a[236:237], v18 offset:288
ds_read_b64_tr_b16 a[238:239], v18 offset:20768
s_waitcnt lgkmcnt(14)
s_waitcnt lgkmcnt(0)
s_barrier
hp_layer_%=:
s_lshl_b32 s47, s21, 6
s_cmp_eq_u32 s46, 0
s_cbranch_scc1 hp_roleq_%=
s_cmp_eq_u32 s46, 3
s_cbranch_scc1 hp_roleo_%=
s_cmp_eq_u32 s46, 4
s_cbranch_scc1 hp_rolet_%=
s_mul_i32 s62, s45, 10240
s_mul_i32 s50, s47, 40960
s_add_u32 s50, s50, s62
s_add_u32 s50, s50, 2523136
s_mov_b32 s61, s62
s_lshl_b32 s48, s47, 7
s_add_u32 s48, s48, 43520
v_mov_b32 v225, s48
s_mov_b32 s48, 2
v_and_b32 v224, 1, v15
v_lshl_add_u32 v226, v224, 7, v225
v_mov_b32 v224, 0
v_cmp_gt_u32 vcc, s48, v224
s_cbranch_vccz hpk_xfp_ok_%=
s_mov_b32 s60, 0
s_nop 2
hpk_xfp_poll_%=:
buffer_load_dword v224, v226, s[40:43], s58 offen sc1
s_waitcnt vmcnt(0)
v_cmp_gt_u32 vcc, s48, v224
s_nop 4
s_cbranch_vccz hpk_xfp_ok_%=
s_sleep 1
s_add_u32 s60, s60, 1
s_cmp_gt_u32 s60, 65536
s_cbranch_scc1 hp_abort_%=
s_and_b32 s59, s60, 63
s_cbranch_scc1 hpk_xfp_poll_%=
v_mov_b32 v4, 1024
buffer_load_dword v4, v4, s[40:43], s58 offen sc1
s_waitcnt vmcnt(0)
v_readfirstlane_b32 s59, v4
s_nop 0
s_cmp_lg_u32 s59, 0
s_nop 2
s_cbranch_scc1 hp_end_%=
s_branch hpk_xfp_poll_%=
hpk_xfp_ok_%=:
v_add_u32 v225, 256, v225
s_add_u32 m0, s61, 0
s_add_u32 s51, s50, 0
buffer_load_dwordx4 v0, s[40:43], s51 offen offset:0 sc1 lds
buffer_load_dwordx4 v0, s[40:43], s51 offen offset:1024 sc1 lds
buffer_load_dwordx4 v0, s[40:43], s51 offen offset:2048 sc1 lds
buffer_load_dwordx4 v0, s[40:43], s51 offen offset:3072 sc1 lds
s_add_u32 m0, s61, 4096
s_add_u32 s51, s50, 4096
buffer_load_dwordx4 v0, s[40:43], s51 offen offset:0 sc1 lds
buffer_load_dwordx4 v0, s[40:43], s51 offen offset:1024 sc1 lds
buffer_load_dwordx4 v0, s[40:43], s51 offen offset:2048 sc1 lds
buffer_load_dwordx4 v0, s[40:43], s51 offen offset:3072 sc1 lds
s_add_u32 m0, s61, 8192
s_add_u32 s51, s50, 8192
buffer_load_dwordx4 v0, s[40:43], s51 offen offset:0 sc1 lds
buffer_load_dwordx4 v0, s[40:43], s51 offen offset:1024 sc1 lds
s_add_u32 s50, s50, 40960
s_add_u32 s61, s61, 40960
s_cmp_ge_u32 s61, 122880
s_cselect_b32 s51, 122880, 0
s_sub_u32 s61, s61, s51
s_add_u32 m0, s61, 0
s_add_u32 s51, s50, 0
buffer_load_dwordx4 v0, s[40:43], s51 offen offset:0 sc1 lds
buffer_load_dwordx4 v0, s[40:43], s51 offen offset:1024 sc1 lds
buffer_load_dwordx4 v0, s[40:43], s51 offen offset:2048 sc1 lds
buffer_load_dwordx4 v0, s[40:43], s51 offen offset:3072 sc1 lds
s_add_u32 m0, s61, 4096
s_add_u32 s51, s50, 4096
buffer_load_dwordx4 v0, s[40:43], s51 offen offset:0 sc1 lds
buffer_load_dwordx4 v0, s[40:43], s51 offen offset:1024 sc1 lds
buffer_load_dwordx4 v0, s[40:43], s51 offen offset:2048 sc1 lds
buffer_load_dwordx4 v0, s[40:43], s51 offen offset:3072 sc1 lds
s_add_u32 m0, s61, 8192
s_add_u32 s51, s50, 8192
buffer_load_dwordx4 v0, s[40:43], s51 offen offset:0 sc1 lds
buffer_load_dwordx4 v0, s[40:43], s51 offen offset:1024 sc1 lds
s_add_u32 s50, s50, 40960
s_add_u32 s61, s61, 40960
s_cmp_ge_u32 s61, 122880
s_cselect_b32 s51, 122880, 0
s_sub_u32 s61, s61, s51
v_mov_b32 v224, 0
v_mov_b32 v226, 1
v_mov_b32 v1, v0
s_mov_b32 s64, 0
s_lshl_b32 s48, s44, 7
s_cmp_eq_u32 s45, 0
s_cselect_b32 s59, 1, 0
v_lshlrev_b32 v3, 7, v15
v_cmp_gt_u32 vcc, s59, v15
v_mov_b32 v4, 2147483632
s_nop 0
v_cndmask_b32 v3, v4, v3, vcc
s_lshr_b32 s49, s47, 1
s_lshl_b32 s49, s49, 7
s_add_u32 s49, s49, 254976
v_mov_b32 v10, s49
v_mov_b32 v9, 0
s_add_u32 s53, s48, 2099200
s_lshl_b32 s54, s47, 7
s_add_u32 s54, s54, 2107392
s_mov_b32 s63, 20348928
s_cmp_eq_u32 s46, 1
s_cselect_b32 s53, s54, s53
s_cselect_b32 s54, 128, 0
s_cselect_b32 s63, 7766016, s63
s_mul_i32 s49, s44, 12288
s_add_u32 s63, s63, s49
s_mul_i32 s49, s45, 3072
s_add_u32 s63, s63, s49
s_mov_b32 s52, 0x40000000
s_waitcnt vmcnt(10)
)"
R"(s_waitcnt lgkmcnt(0)
s_barrier
s_mov_b32 s65, 0x40000000
ds_read_b128 v[80:83], v1 offset:0
ds_read_b128 v[84:87], v1 offset:20480
ds_read_b128 v[88:91], v1 offset:1024
ds_read_b128 v[92:95], v1 offset:21504
ds_read_b128 v[96:99], v1 offset:2048
hpk_loop_%=:
s_bitcmp1_b32 s47, 5
s_cselect_b32 s48, 8, 0
v_cmp_gt_u32 vcc, s48, v9
s_cbranch_vccz hpk_free_ok_%=
s_mov_b32 s60, 0
s_nop 2
hpk_free_poll_%=:
buffer_load_dword v9, v10, s[40:43], s58 offen sc1
s_waitcnt vmcnt(0)
v_cmp_gt_u32 vcc, s48, v9
s_nop 4
s_cbranch_vccz hpk_free_ok_%=
s_sleep 8
s_add_u32 s60, s60, 1
s_cmp_gt_u32 s60, 65536
s_cbranch_scc1 hp_abort_%=
s_and_b32 s59, s60, 63
s_cbranch_scc1 hpk_free_poll_%=
v_mov_b32 v4, 1024
buffer_load_dword v4, v4, s[40:43], s58 offen sc1
s_waitcnt vmcnt(0)
v_readfirstlane_b32 s59, v4
s_nop 0
s_cmp_lg_u32 s59, 0
s_nop 2
s_cbranch_scc1 hp_end_%=
s_branch hpk_free_poll_%=
hpk_free_ok_%=:
v_add_u32 v10, 128, v10
s_waitcnt lgkmcnt(4)
v_mfma_f32_16x16x32_bf16 v[16:19], a[0:3], v[80:83], 0
v_mfma_f32_16x16x32_bf16 v[24:27], a[80:83], v[80:83], 0
v_mfma_f32_16x16x32_bf16 v[32:35], a[160:163], v[80:83], 0
ds_read_b128 v[80:83], v1 offset:22528
s_waitcnt lgkmcnt(4)
v_mfma_f32_16x16x32_bf16 v[20:23], a[0:3], v[84:87], 0
v_mfma_f32_16x16x32_bf16 v[28:31], a[80:83], v[84:87], 0
v_mfma_f32_16x16x32_bf16 v[36:39], a[160:163], v[84:87], 0
ds_read_b128 v[84:87], v1 offset:3072
s_waitcnt lgkmcnt(4)
v_mfma_f32_16x16x32_bf16 v[16:19], a[4:7], v[88:91], v[16:19]
v_mfma_f32_16x16x32_bf16 v[24:27], a[84:87], v[88:91], v[24:27]
v_mfma_f32_16x16x32_bf16 v[32:35], a[164:167], v[88:91], v[32:35]
ds_read_b128 v[88:91], v1 offset:23552
s_waitcnt lgkmcnt(4)
v_mfma_f32_16x16x32_bf16 v[20:23], a[4:7], v[92:95], v[20:23]
v_cvt_pk_bf16_f32 v104, v40, v41
v_cvt_pk_bf16_f32 v105, v42, v43
v_mfma_f32_16x16x32_bf16 v[28:31], a[84:87], v[92:95], v[28:31]
v_cvt_pk_bf16_f32 v106, v48, v49
v_cvt_pk_bf16_f32 v107, v50, v51
v_mfma_f32_16x16x32_bf16 v[36:39], a[164:167], v[92:95], v[36:39]
ds_read_b128 v[92:95], v1 offset:4096
s_waitcnt lgkmcnt(4)
v_mfma_f32_16x16x32_bf16 v[16:19], a[8:11], v[96:99], v[16:19]
buffer_store_dwordx4 v[104:107], v0, s[40:43], s65 offen offset:0 sc1
v_mfma_f32_16x16x32_bf16 v[24:27], a[88:91], v[96:99], v[24:27]
v_cvt_pk_bf16_f32 v108, v44, v45
v_cvt_pk_bf16_f32 v109, v46, v47
v_mfma_f32_16x16x32_bf16 v[32:35], a[168:171], v[96:99], v[32:35]
v_cvt_pk_bf16_f32 v110, v52, v53
ds_read_b128 v[96:99], v1 offset:24576
s_waitcnt lgkmcnt(4)
v_mfma_f32_16x16x32_bf16 v[20:23], a[8:11], v[80:83], v[20:23]
v_cvt_pk_bf16_f32 v111, v54, v55
v_mfma_f32_16x16x32_bf16 v[28:31], a[88:91], v[80:83], v[28:31]
buffer_store_dwordx4 v[108:111], v0, s[40:43], s65 offen offset:1024 sc1
v_mfma_f32_16x16x32_bf16 v[36:39], a[168:171], v[80:83], v[36:39]
v_cvt_pk_bf16_f32 v112, v56, v57
ds_read_b128 v[80:83], v1 offset:5120
s_waitcnt lgkmcnt(4)
v_mfma_f32_16x16x32_bf16 v[16:19], a[12:15], v[84:87], v[16:19]
v_cvt_pk_bf16_f32 v113, v58, v59
v_cvt_pk_bf16_f32 v114, v60, v61
v_mfma_f32_16x16x32_bf16 v[24:27], a[92:95], v[84:87], v[24:27]
v_cvt_pk_bf16_f32 v115, v62, v63
v_mfma_f32_16x16x32_bf16 v[32:35], a[172:175], v[84:87], v[32:35]
ds_read_b128 v[84:87], v1 offset:25600
s_waitcnt lgkmcnt(4)
v_mfma_f32_16x16x32_bf16 v[20:23], a[12:15], v[88:91], v[20:23]
buffer_store_dwordx4 v[112:115], v0, s[40:43], s65 offen offset:2048 sc1
v_mfma_f32_16x16x32_bf16 v[28:31], a[92:95], v[88:91], v[28:31]
v_mfma_f32_16x16x32_bf16 v[36:39], a[172:175], v[88:91], v[36:39]
ds_read_b128 v[88:91], v1 offset:6144
s_waitcnt lgkmcnt(4)
v_mfma_f32_16x16x32_bf16 v[16:19], a[16:19], v[92:95], v[16:19]
buffer_load_dword v224, v225, s[40:43], s58 offen sc1
v_mfma_f32_16x16x32_bf16 v[24:27], a[96:99], v[92:95], v[24:27]
v_mfma_f32_16x16x32_bf16 v[32:35], a[176:179], v[92:95], v[32:35]
ds_read_b128 v[92:95], v1 offset:26624
s_waitcnt lgkmcnt(4)
v_mfma_f32_16x16x32_bf16 v[20:23], a[16:19], v[96:99], v[20:23]
v_mfma_f32_16x16x32_bf16 v[28:31], a[96:99], v[96:99], v[28:31]
v_mfma_f32_16x16x32_bf16 v[36:39], a[176:179], v[96:99], v[36:39]
ds_read_b128 v[96:99], v1 offset:7168
s_waitcnt lgkmcnt(4)
v_mfma_f32_16x16x32_bf16 v[16:19], a[20:23], v[80:83], v[16:19]
v_mfma_f32_16x16x32_bf16 v[24:27], a[100:103], v[80:83], v[24:27]
v_mfma_f32_16x16x32_bf16 v[32:35], a[180:183], v[80:83], v[32:35]
ds_read_b128 v[80:83], v1 offset:27648
s_waitcnt lgkmcnt(4)
v_mfma_f32_16x16x32_bf16 v[20:23], a[20:23], v[84:87], v[20:23]
v_mfma_f32_16x16x32_bf16 v[28:31], a[100:103], v[84:87], v[28:31]
v_mfma_f32_16x16x32_bf16 v[36:39], a[180:183], v[84:87], v[36:39]
ds_read_b128 v[84:87], v1 offset:8192
s_waitcnt lgkmcnt(4)
v_mfma_f32_16x16x32_bf16 v[16:19], a[24:27], v[88:91], v[16:19]
v_mfma_f32_16x16x32_bf16 v[24:27], a[104:107], v[88:91], v[24:27]
v_mfma_f32_16x16x32_bf16 v[32:35], a[184:187], v[88:91], v[32:35]
ds_read_b128 v[88:91], v1 offset:28672
s_waitcnt lgkmcnt(4)
v_mfma_f32_16x16x32_bf16 v[20:23], a[24:27], v[92:95], v[20:23]
v_mfma_f32_16x16x32_bf16 v[28:31], a[104:107], v[92:95], v[28:31]
v_mfma_f32_16x16x32_bf16 v[36:39], a[184:187], v[92:95], v[36:39]
ds_read_b128 v[92:95], v1 offset:9216
s_waitcnt lgkmcnt(4)
v_mfma_f32_16x16x32_bf16 v[16:19], a[28:31], v[96:99], v[16:19]
v_mfma_f32_16x16x32_bf16 v[24:27], a[108:111], v[96:99], v[24:27]
v_mfma_f32_16x16x32_bf16 v[32:35], a[188:191], v[96:99], v[32:35]
ds_read_b128 v[96:99], v1 offset:29696
s_waitcnt lgkmcnt(4)
v_mfma_f32_16x16x32_bf16 v[20:23], a[28:31], v[80:83], v[20:23]
v_mfma_f32_16x16x32_bf16 v[28:31], a[108:111], v[80:83], v[28:31]
v_mfma_f32_16x16x32_bf16 v[36:39], a[188:191], v[80:83], v[36:39]
ds_read_b128 v[80:83], v1 offset:10240
s_waitcnt lgkmcnt(4)
v_mfma_f32_16x16x32_bf16 v[16:19], a[32:35], v[84:87], v[16:19]
v_mfma_f32_16x16x32_bf16 v[24:27], a[112:115], v[84:87], v[24:27]
v_mfma_f32_16x16x32_bf16 v[32:35], a[192:195], v[84:87], v[32:35]
ds_read_b128 v[84:87], v1 offset:30720
s_waitcnt lgkmcnt(4)
v_mfma_f32_16x16x32_bf16 v[20:23], a[32:35], v[88:91], v[20:23]
v_mfma_f32_16x16x32_bf16 v[28:31], a[112:115], v[88:91], v[28:31]
v_mfma_f32_16x16x32_bf16 v[36:39], a[192:195], v[88:91], v[36:39]
ds_read_b128 v[88:91], v1 offset:11264
s_waitcnt lgkmcnt(4)
v_mfma_f32_16x16x32_bf16 v[16:19], a[36:39], v[92:95], v[16:19]
v_mfma_f32_16x16x32_bf16 v[24:27], a[116:119], v[92:95], v[24:27]
v_mfma_f32_16x16x32_bf16 v[32:35], a[196:199], v[92:95], v[32:35]
ds_read_b128 v[92:95], v1 offset:31744
s_waitcnt lgkmcnt(4)
v_mfma_f32_16x16x32_bf16 v[20:23], a[36:39], v[96:99], v[20:23]
v_mfma_f32_16x16x32_bf16 v[28:31], a[116:119], v[96:99], v[28:31]
v_mfma_f32_16x16x32_bf16 v[36:39], a[196:199], v[96:99], v[36:39]
ds_read_b128 v[96:99], v1 offset:12288
s_waitcnt lgkmcnt(4)
v_mfma_f32_16x16x32_bf16 v[16:19], a[40:43], v[80:83], v[16:19]
v_mfma_f32_16x16x32_bf16 v[24:27], a[120:123], v[80:83], v[24:27]
v_mfma_f32_16x16x32_bf16 v[32:35], a[200:203], v[80:83], v[32:35]
ds_read_b128 v[80:83], v1 offset:32768
s_waitcnt lgkmcnt(4)
v_mfma_f32_16x16x32_bf16 v[20:23], a[40:43], v[84:87], v[20:23]
v_mfma_f32_16x16x32_bf16 v[28:31], a[120:123], v[84:87], v[28:31]
v_mfma_f32_16x16x32_bf16 v[36:39], a[200:203], v[84:87], v[36:39]
ds_read_b128 v[84:87], v1 offset:13312
s_waitcnt lgkmcnt(4)
v_mfma_f32_16x16x32_bf16 v[16:19], a[44:47], v[88:91], v[16:19]
v_mfma_f32_16x16x32_bf16 v[24:27], a[124:127], v[88:91], v[24:27]
v_mfma_f32_16x16x32_bf16 v[32:35], a[204:207], v[88:91], v[32:35]
ds_read_b128 v[88:91], v1 offset:33792
s_waitcnt lgkmcnt(4)
v_mfma_f32_16x16x32_bf16 v[20:23], a[44:47], v[92:95], v[20:23]
v_mfma_f32_16x16x32_bf16 v[28:31], a[124:127], v[92:95], v[28:31]
v_mfma_f32_16x16x32_bf16 v[36:39], a[204:207], v[92:95], v[36:39]
ds_read_b128 v[92:95], v1 offset:14336
s_waitcnt lgkmcnt(4)
v_mfma_f32_16x16x32_bf16 v[16:19], a[48:51], v[96:99], v[16:19]
v_mfma_f32_16x16x32_bf16 v[24:27], a[128:131], v[96:99], v[24:27]
v_mfma_f32_16x16x32_bf16 v[32:35], a[208:211], v[96:99], v[32:35]
ds_read_b128 v[96:99], v1 offset:34816
s_waitcnt lgkmcnt(4)
v_mfma_f32_16x16x32_bf16 v[20:23], a[48:51], v[80:83], v[20:23]
v_mfma_f32_16x16x32_bf16 v[28:31], a[128:131], v[80:83], v[28:31]
v_mfma_f32_16x16x32_bf16 v[36:39], a[208:211], v[80:83], v[36:39]
ds_read_b128 v[80:83], v1 offset:15360
s_waitcnt lgkmcnt(4)
v_mfma_f32_16x16x32_bf16 v[16:19], a[52:55], v[84:87], v[16:19]
v_mfma_f32_16x16x32_bf16 v[24:27], a[132:135], v[84:87], v[24:27]
v_mfma_f32_16x16x32_bf16 v[32:35], a[212:215], v[84:87], v[32:35]
ds_read_b128 v[84:87], v1 offset:35840
s_waitcnt lgkmcnt(4)
v_mfma_f32_16x16x32_bf16 v[20:23], a[52:55], v[88:91], v[20:23]
v_mfma_f32_16x16x32_bf16 v[28:31], a[132:135], v[88:91], v[28:31]
v_mfma_f32_16x16x32_bf16 v[36:39], a[212:215], v[88:91], v[36:39]
ds_read_b128 v[88:91], v1 offset:16384
s_waitcnt lgkmcnt(4)
v_mfma_f32_16x16x32_bf16 v[16:19], a[56:59], v[92:95], v[16:19]
v_mfma_f32_16x16x32_bf16 v[24:27], a[136:139], v[92:95], v[24:27]
v_mfma_f32_16x16x32_bf16 v[32:35], a[216:219], v[92:95], v[32:35]
ds_read_b128 v[92:95], v1 offset:36864
s_waitcnt lgkmcnt(4)
)"
R"(v_mfma_f32_16x16x32_bf16 v[20:23], a[56:59], v[96:99], v[20:23]
v_mfma_f32_16x16x32_bf16 v[28:31], a[136:139], v[96:99], v[28:31]
v_mfma_f32_16x16x32_bf16 v[36:39], a[216:219], v[96:99], v[36:39]
s_waitcnt vmcnt(0)
s_barrier
buffer_atomic_add v226, v3, s[40:43], s52 offen
s_and_b32 s48, s47, 63
s_cmp_lt_u32 s48, 62
s_cselect_b32 s48, 2, 0
v_cmp_gt_u32 vcc, s48, v224
s_cbranch_vccz hpk_xf0_ok_%=
s_mov_b32 s60, 0
s_nop 2
hpk_xf0_poll_%=:
buffer_load_dword v224, v225, s[40:43], s58 offen sc1
s_waitcnt vmcnt(0)
v_cmp_gt_u32 vcc, s48, v224
s_nop 4
s_cbranch_vccz hpk_xf0_ok_%=
s_sleep 2
s_add_u32 s60, s60, 1
s_cmp_gt_u32 s60, 65536
s_cbranch_scc1 hp_abort_%=
s_and_b32 s59, s60, 63
s_cbranch_scc1 hpk_xf0_poll_%=
v_mov_b32 v4, 1024
buffer_load_dword v4, v4, s[40:43], s58 offen sc1
s_waitcnt vmcnt(0)
v_readfirstlane_b32 s59, v4
s_nop 0
s_cmp_lg_u32 s59, 0
s_nop 2
s_cbranch_scc1 hp_end_%=
s_branch hpk_xf0_poll_%=
hpk_xf0_ok_%=:
v_add_u32 v225, 128, v225
ds_read_b128 v[96:99], v1 offset:17408
s_waitcnt lgkmcnt(4)
v_mfma_f32_16x16x32_bf16 v[16:19], a[60:63], v[80:83], v[16:19]
s_add_u32 m0, s61, 0
s_add_u32 s51, s50, 0
buffer_load_dwordx4 v0, s[40:43], s51 offen offset:0 sc1 lds
v_mfma_f32_16x16x32_bf16 v[24:27], a[140:143], v[80:83], v[24:27]
v_mfma_f32_16x16x32_bf16 v[32:35], a[220:223], v[80:83], v[32:35]
ds_read_b128 v[80:83], v1 offset:37888
s_waitcnt lgkmcnt(4)
v_mfma_f32_16x16x32_bf16 v[20:23], a[60:63], v[84:87], v[20:23]
buffer_load_dwordx4 v0, s[40:43], s51 offen offset:1024 sc1 lds
v_mfma_f32_16x16x32_bf16 v[28:31], a[140:143], v[84:87], v[28:31]
v_mfma_f32_16x16x32_bf16 v[36:39], a[220:223], v[84:87], v[36:39]
ds_read_b128 v[84:87], v1 offset:18432
s_waitcnt lgkmcnt(4)
v_mfma_f32_16x16x32_bf16 v[16:19], a[64:67], v[88:91], v[16:19]
buffer_load_dwordx4 v0, s[40:43], s51 offen offset:2048 sc1 lds
v_mfma_f32_16x16x32_bf16 v[24:27], a[144:147], v[88:91], v[24:27]
v_mfma_f32_16x16x32_bf16 v[32:35], a[224:227], v[88:91], v[32:35]
ds_read_b128 v[88:91], v1 offset:38912
s_waitcnt lgkmcnt(4)
v_mfma_f32_16x16x32_bf16 v[20:23], a[64:67], v[92:95], v[20:23]
buffer_load_dwordx4 v0, s[40:43], s51 offen offset:3072 sc1 lds
v_mfma_f32_16x16x32_bf16 v[28:31], a[144:147], v[92:95], v[28:31]
v_mfma_f32_16x16x32_bf16 v[36:39], a[224:227], v[92:95], v[36:39]
ds_read_b128 v[92:95], v1 offset:19456
s_waitcnt lgkmcnt(4)
v_mfma_f32_16x16x32_bf16 v[16:19], a[68:71], v[96:99], v[16:19]
s_add_u32 m0, s61, 4096
s_add_u32 s51, s50, 4096
buffer_load_dwordx4 v0, s[40:43], s51 offen offset:0 sc1 lds
v_mfma_f32_16x16x32_bf16 v[24:27], a[148:151], v[96:99], v[24:27]
v_mfma_f32_16x16x32_bf16 v[32:35], a[228:231], v[96:99], v[32:35]
ds_read_b128 v[96:99], v1 offset:39936
s_waitcnt lgkmcnt(4)
v_mfma_f32_16x16x32_bf16 v[20:23], a[68:71], v[80:83], v[20:23]
buffer_load_dwordx4 v0, s[40:43], s51 offen offset:1024 sc1 lds
v_mfma_f32_16x16x32_bf16 v[28:31], a[148:151], v[80:83], v[28:31]
v_mfma_f32_16x16x32_bf16 v[36:39], a[228:231], v[80:83], v[36:39]
s_add_u32 s64, s64, 40960
s_cmp_ge_u32 s64, 122880
s_cselect_b32 s64, 0, s64
v_add_u32 v1, s64, v0
ds_read_b128 v[80:83], v1 offset:0
s_waitcnt lgkmcnt(4)
v_mfma_f32_16x16x32_bf16 v[16:19], a[72:75], v[84:87], v[16:19]
buffer_load_dwordx4 v0, s[40:43], s51 offen offset:2048 sc1 lds
v_mfma_f32_16x16x32_bf16 v[24:27], a[152:155], v[84:87], v[24:27]
v_mfma_f32_16x16x32_bf16 v[32:35], a[232:235], v[84:87], v[32:35]
ds_read_b128 v[84:87], v1 offset:20480
s_waitcnt lgkmcnt(4)
v_mfma_f32_16x16x32_bf16 v[20:23], a[72:75], v[88:91], v[20:23]
buffer_load_dwordx4 v0, s[40:43], s51 offen offset:3072 sc1 lds
v_mfma_f32_16x16x32_bf16 v[28:31], a[152:155], v[88:91], v[28:31]
v_mfma_f32_16x16x32_bf16 v[36:39], a[232:235], v[88:91], v[36:39]
ds_read_b128 v[88:91], v1 offset:1024
s_waitcnt lgkmcnt(4)
v_mfma_f32_16x16x32_bf16 v[16:19], a[76:79], v[92:95], v[16:19]
s_add_u32 m0, s61, 8192
s_add_u32 s51, s50, 8192
buffer_load_dwordx4 v0, s[40:43], s51 offen offset:0 sc1 lds
v_mfma_f32_16x16x32_bf16 v[24:27], a[156:159], v[92:95], v[24:27]
v_mfma_f32_16x16x32_bf16 v[32:35], a[236:239], v[92:95], v[32:35]
ds_read_b128 v[92:95], v1 offset:21504
s_waitcnt lgkmcnt(4)
v_mfma_f32_16x16x32_bf16 v[20:23], a[76:79], v[96:99], v[20:23]
buffer_load_dwordx4 v0, s[40:43], s51 offen offset:1024 sc1 lds
v_mfma_f32_16x16x32_bf16 v[28:31], a[156:159], v[96:99], v[28:31]
s_add_u32 s50, s50, 40960
s_add_u32 s61, s61, 40960
s_cmp_ge_u32 s61, 122880
s_cselect_b32 s51, 122880, 0
s_sub_u32 s61, s61, s51
v_mfma_f32_16x16x32_bf16 v[36:39], a[236:239], v[96:99], v[36:39]
ds_read_b128 v[96:99], v1 offset:2048
s_mov_b32 s65, s63
s_add_u32 s59, s47, 0
s_and_b32 s59, s59, 63
s_cmp_eq_u32 s59, 31
s_cselect_b32 s59, -31, 1
s_mul_i32 s57, s59, 393216
s_add_u32 s63, s63, s57
s_mov_b32 s52, s53
s_add_u32 s53, s53, s54
s_and_b32 s59, s47, 62
s_cbranch_scc1 hpk_late0_%=
v_cvt_pk_bf16_f32 v104, v16, v17
v_cvt_pk_bf16_f32 v105, v18, v19
v_cvt_pk_bf16_f32 v106, v24, v25
v_cvt_pk_bf16_f32 v107, v26, v27
buffer_store_dwordx4 v[104:107], v0, s[40:43], s65 offen offset:0 sc1
v_cvt_pk_bf16_f32 v108, v20, v21
v_cvt_pk_bf16_f32 v109, v22, v23
v_cvt_pk_bf16_f32 v110, v28, v29
v_cvt_pk_bf16_f32 v111, v30, v31
buffer_store_dwordx4 v[108:111], v0, s[40:43], s65 offen offset:1024 sc1
v_cvt_pk_bf16_f32 v112, v32, v33
v_cvt_pk_bf16_f32 v113, v34, v35
v_cvt_pk_bf16_f32 v114, v36, v37
v_cvt_pk_bf16_f32 v115, v38, v39
buffer_store_dwordx4 v[112:115], v0, s[40:43], s65 offen offset:2048 sc1
s_waitcnt vmcnt(0)
s_barrier
buffer_atomic_add v226, v3, s[40:43], s52 offen
s_mov_b32 s52, 0x40000000
s_nop 0
hpk_late0_%=:
s_add_u32 s47, s47, 1
s_waitcnt lgkmcnt(4)
v_mfma_f32_16x16x32_bf16 v[40:43], a[0:3], v[80:83], 0
v_mfma_f32_16x16x32_bf16 v[48:51], a[80:83], v[80:83], 0
v_mfma_f32_16x16x32_bf16 v[56:59], a[160:163], v[80:83], 0
ds_read_b128 v[80:83], v1 offset:22528
s_waitcnt lgkmcnt(4)
v_mfma_f32_16x16x32_bf16 v[44:47], a[0:3], v[84:87], 0
v_mfma_f32_16x16x32_bf16 v[52:55], a[80:83], v[84:87], 0
v_mfma_f32_16x16x32_bf16 v[60:63], a[160:163], v[84:87], 0
ds_read_b128 v[84:87], v1 offset:3072
s_waitcnt lgkmcnt(4)
v_mfma_f32_16x16x32_bf16 v[40:43], a[4:7], v[88:91], v[40:43]
v_mfma_f32_16x16x32_bf16 v[48:51], a[84:87], v[88:91], v[48:51]
v_mfma_f32_16x16x32_bf16 v[56:59], a[164:167], v[88:91], v[56:59]
ds_read_b128 v[88:91], v1 offset:23552
s_waitcnt lgkmcnt(4)
v_mfma_f32_16x16x32_bf16 v[44:47], a[4:7], v[92:95], v[44:47]
v_cvt_pk_bf16_f32 v104, v16, v17
v_cvt_pk_bf16_f32 v105, v18, v19
v_mfma_f32_16x16x32_bf16 v[52:55], a[84:87], v[92:95], v[52:55]
v_cvt_pk_bf16_f32 v106, v24, v25
v_cvt_pk_bf16_f32 v107, v26, v27
v_mfma_f32_16x16x32_bf16 v[60:63], a[164:167], v[92:95], v[60:63]
ds_read_b128 v[92:95], v1 offset:4096
s_waitcnt lgkmcnt(4)
v_mfma_f32_16x16x32_bf16 v[40:43], a[8:11], v[96:99], v[40:43]
buffer_store_dwordx4 v[104:107], v0, s[40:43], s65 offen offset:0 sc1
v_mfma_f32_16x16x32_bf16 v[48:51], a[88:91], v[96:99], v[48:51]
v_cvt_pk_bf16_f32 v108, v20, v21
v_cvt_pk_bf16_f32 v109, v22, v23
v_mfma_f32_16x16x32_bf16 v[56:59], a[168:171], v[96:99], v[56:59]
v_cvt_pk_bf16_f32 v110, v28, v29
ds_read_b128 v[96:99], v1 offset:24576
s_waitcnt lgkmcnt(4)
v_mfma_f32_16x16x32_bf16 v[44:47], a[8:11], v[80:83], v[44:47]
v_cvt_pk_bf16_f32 v111, v30, v31
v_mfma_f32_16x16x32_bf16 v[52:55], a[88:91], v[80:83], v[52:55]
buffer_store_dwordx4 v[108:111], v0, s[40:43], s65 offen offset:1024 sc1
v_mfma_f32_16x16x32_bf16 v[60:63], a[168:171], v[80:83], v[60:63]
v_cvt_pk_bf16_f32 v112, v32, v33
ds_read_b128 v[80:83], v1 offset:5120
s_waitcnt lgkmcnt(4)
v_mfma_f32_16x16x32_bf16 v[40:43], a[12:15], v[84:87], v[40:43]
v_cvt_pk_bf16_f32 v113, v34, v35
v_cvt_pk_bf16_f32 v114, v36, v37
v_mfma_f32_16x16x32_bf16 v[48:51], a[92:95], v[84:87], v[48:51]
v_cvt_pk_bf16_f32 v115, v38, v39
v_mfma_f32_16x16x32_bf16 v[56:59], a[172:175], v[84:87], v[56:59]
ds_read_b128 v[84:87], v1 offset:25600
s_waitcnt lgkmcnt(4)
v_mfma_f32_16x16x32_bf16 v[44:47], a[12:15], v[88:91], v[44:47]
buffer_store_dwordx4 v[112:115], v0, s[40:43], s65 offen offset:2048 sc1
v_mfma_f32_16x16x32_bf16 v[52:55], a[92:95], v[88:91], v[52:55]
v_mfma_f32_16x16x32_bf16 v[60:63], a[172:175], v[88:91], v[60:63]
ds_read_b128 v[88:91], v1 offset:6144
)"
R"(s_waitcnt lgkmcnt(4)
v_mfma_f32_16x16x32_bf16 v[40:43], a[16:19], v[92:95], v[40:43]
buffer_load_dword v224, v225, s[40:43], s58 offen sc1
v_mfma_f32_16x16x32_bf16 v[48:51], a[96:99], v[92:95], v[48:51]
v_mfma_f32_16x16x32_bf16 v[56:59], a[176:179], v[92:95], v[56:59]
ds_read_b128 v[92:95], v1 offset:26624
s_waitcnt lgkmcnt(4)
v_mfma_f32_16x16x32_bf16 v[44:47], a[16:19], v[96:99], v[44:47]
buffer_load_dword v9, v10, s[40:43], s58 offen sc1
v_mfma_f32_16x16x32_bf16 v[52:55], a[96:99], v[96:99], v[52:55]
v_mfma_f32_16x16x32_bf16 v[60:63], a[176:179], v[96:99], v[60:63]
ds_read_b128 v[96:99], v1 offset:7168
s_waitcnt lgkmcnt(4)
v_mfma_f32_16x16x32_bf16 v[40:43], a[20:23], v[80:83], v[40:43]
v_mfma_f32_16x16x32_bf16 v[48:51], a[100:103], v[80:83], v[48:51]
v_mfma_f32_16x16x32_bf16 v[56:59], a[180:183], v[80:83], v[56:59]
ds_read_b128 v[80:83], v1 offset:27648
s_waitcnt lgkmcnt(4)
v_mfma_f32_16x16x32_bf16 v[44:47], a[20:23], v[84:87], v[44:47]
v_mfma_f32_16x16x32_bf16 v[52:55], a[100:103], v[84:87], v[52:55]
v_mfma_f32_16x16x32_bf16 v[60:63], a[180:183], v[84:87], v[60:63]
ds_read_b128 v[84:87], v1 offset:8192
s_waitcnt lgkmcnt(4)
v_mfma_f32_16x16x32_bf16 v[40:43], a[24:27], v[88:91], v[40:43]
v_mfma_f32_16x16x32_bf16 v[48:51], a[104:107], v[88:91], v[48:51]
v_mfma_f32_16x16x32_bf16 v[56:59], a[184:187], v[88:91], v[56:59]
ds_read_b128 v[88:91], v1 offset:28672
s_waitcnt lgkmcnt(4)
v_mfma_f32_16x16x32_bf16 v[44:47], a[24:27], v[92:95], v[44:47]
v_mfma_f32_16x16x32_bf16 v[52:55], a[104:107], v[92:95], v[52:55]
v_mfma_f32_16x16x32_bf16 v[60:63], a[184:187], v[92:95], v[60:63]
ds_read_b128 v[92:95], v1 offset:9216
s_waitcnt lgkmcnt(4)
v_mfma_f32_16x16x32_bf16 v[40:43], a[28:31], v[96:99], v[40:43]
v_mfma_f32_16x16x32_bf16 v[48:51], a[108:111], v[96:99], v[48:51]
v_mfma_f32_16x16x32_bf16 v[56:59], a[188:191], v[96:99], v[56:59]
ds_read_b128 v[96:99], v1 offset:29696
s_waitcnt lgkmcnt(4)
v_mfma_f32_16x16x32_bf16 v[44:47], a[28:31], v[80:83], v[44:47]
v_mfma_f32_16x16x32_bf16 v[52:55], a[108:111], v[80:83], v[52:55]
v_mfma_f32_16x16x32_bf16 v[60:63], a[188:191], v[80:83], v[60:63]
ds_read_b128 v[80:83], v1 offset:10240
s_waitcnt lgkmcnt(4)
v_mfma_f32_16x16x32_bf16 v[40:43], a[32:35], v[84:87], v[40:43]
v_mfma_f32_16x16x32_bf16 v[48:51], a[112:115], v[84:87], v[48:51]
v_mfma_f32_16x16x32_bf16 v[56:59], a[192:195], v[84:87], v[56:59]
ds_read_b128 v[84:87], v1 offset:30720
s_waitcnt lgkmcnt(4)
v_mfma_f32_16x16x32_bf16 v[44:47], a[32:35], v[88:91], v[44:47]
v_mfma_f32_16x16x32_bf16 v[52:55], a[112:115], v[88:91], v[52:55]
v_mfma_f32_16x16x32_bf16 v[60:63], a[192:195], v[88:91], v[60:63]
ds_read_b128 v[88:91], v1 offset:11264
s_waitcnt lgkmcnt(4)
v_mfma_f32_16x16x32_bf16 v[40:43], a[36:39], v[92:95], v[40:43]
v_mfma_f32_16x16x32_bf16 v[48:51], a[116:119], v[92:95], v[48:51]
v_mfma_f32_16x16x32_bf16 v[56:59], a[196:199], v[92:95], v[56:59]
ds_read_b128 v[92:95], v1 offset:31744
s_waitcnt lgkmcnt(4)
v_mfma_f32_16x16x32_bf16 v[44:47], a[36:39], v[96:99], v[44:47]
v_mfma_f32_16x16x32_bf16 v[52:55], a[116:119], v[96:99], v[52:55]
v_mfma_f32_16x16x32_bf16 v[60:63], a[196:199], v[96:99], v[60:63]
ds_read_b128 v[96:99], v1 offset:12288
s_waitcnt lgkmcnt(4)
v_mfma_f32_16x16x32_bf16 v[40:43], a[40:43], v[80:83], v[40:43]
v_mfma_f32_16x16x32_bf16 v[48:51], a[120:123], v[80:83], v[48:51]
v_mfma_f32_16x16x32_bf16 v[56:59], a[200:203], v[80:83], v[56:59]
ds_read_b128 v[80:83], v1 offset:32768
s_waitcnt lgkmcnt(4)
v_mfma_f32_16x16x32_bf16 v[44:47], a[40:43], v[84:87], v[44:47]
v_mfma_f32_16x16x32_bf16 v[52:55], a[120:123], v[84:87], v[52:55]
v_mfma_f32_16x16x32_bf16 v[60:63], a[200:203], v[84:87], v[60:63]
ds_read_b128 v[84:87], v1 offset:13312
s_waitcnt lgkmcnt(4)
v_mfma_f32_16x16x32_bf16 v[40:43], a[44:47], v[88:91], v[40:43]
v_mfma_f32_16x16x32_bf16 v[48:51], a[124:127], v[88:91], v[48:51]
v_mfma_f32_16x16x32_bf16 v[56:59], a[204:207], v[88:91], v[56:59]
ds_read_b128 v[88:91], v1 offset:33792
s_waitcnt lgkmcnt(4)
v_mfma_f32_16x16x32_bf16 v[44:47], a[44:47], v[92:95], v[44:47]
v_mfma_f32_16x16x32_bf16 v[52:55], a[124:127], v[92:95], v[52:55]
v_mfma_f32_16x16x32_bf16 v[60:63], a[204:207], v[92:95], v[60:63]
ds_read_b128 v[92:95], v1 offset:14336
s_waitcnt lgkmcnt(4)
v_mfma_f32_16x16x32_bf16 v[40:43], a[48:51], v[96:99], v[40:43]
v_mfma_f32_16x16x32_bf16 v[48:51], a[128:131], v[96:99], v[48:51]
v_mfma_f32_16x16x32_bf16 v[56:59], a[208:211], v[96:99], v[56:59]
ds_read_b128 v[96:99], v1 offset:34816
s_waitcnt lgkmcnt(4)
v_mfma_f32_16x16x32_bf16 v[44:47], a[48:51], v[80:83], v[44:47]
v_mfma_f32_16x16x32_bf16 v[52:55], a[128:131], v[80:83], v[52:55]
v_mfma_f32_16x16x32_bf16 v[60:63], a[208:211], v[80:83], v[60:63]
ds_read_b128 v[80:83], v1 offset:15360
s_waitcnt lgkmcnt(4)
v_mfma_f32_16x16x32_bf16 v[40:43], a[52:55], v[84:87], v[40:43]
v_mfma_f32_16x16x32_bf16 v[48:51], a[132:135], v[84:87], v[48:51]
v_mfma_f32_16x16x32_bf16 v[56:59], a[212:215], v[84:87], v[56:59]
ds_read_b128 v[84:87], v1 offset:35840
s_waitcnt lgkmcnt(4)
v_mfma_f32_16x16x32_bf16 v[44:47], a[52:55], v[88:91], v[44:47]
v_mfma_f32_16x16x32_bf16 v[52:55], a[132:135], v[88:91], v[52:55]
v_mfma_f32_16x16x32_bf16 v[60:63], a[212:215], v[88:91], v[60:63]
ds_read_b128 v[88:91], v1 offset:16384
s_waitcnt lgkmcnt(4)
v_mfma_f32_16x16x32_bf16 v[40:43], a[56:59], v[92:95], v[40:43]
v_mfma_f32_16x16x32_bf16 v[48:51], a[136:139], v[92:95], v[48:51]
v_mfma_f32_16x16x32_bf16 v[56:59], a[216:219], v[92:95], v[56:59]
ds_read_b128 v[92:95], v1 offset:36864
s_waitcnt lgkmcnt(4)
v_mfma_f32_16x16x32_bf16 v[44:47], a[56:59], v[96:99], v[44:47]
v_mfma_f32_16x16x32_bf16 v[52:55], a[136:139], v[96:99], v[52:55]
v_mfma_f32_16x16x32_bf16 v[60:63], a[216:219], v[96:99], v[60:63]
s_waitcnt vmcnt(0)
s_barrier
buffer_atomic_add v226, v3, s[40:43], s52 offen
s_and_b32 s48, s47, 63
s_cmp_lt_u32 s48, 62
s_cselect_b32 s48, 2, 0
v_cmp_gt_u32 vcc, s48, v224
s_cbranch_vccz hpk_xf1_ok_%=
s_mov_b32 s60, 0
s_nop 2
hpk_xf1_poll_%=:
buffer_load_dword v224, v225, s[40:43], s58 offen sc1
s_waitcnt vmcnt(0)
v_cmp_gt_u32 vcc, s48, v224
s_nop 4
s_cbranch_vccz hpk_xf1_ok_%=
s_sleep 2
s_add_u32 s60, s60, 1
s_cmp_gt_u32 s60, 65536
s_cbranch_scc1 hp_abort_%=
s_and_b32 s59, s60, 63
s_cbranch_scc1 hpk_xf1_poll_%=
v_mov_b32 v4, 1024
buffer_load_dword v4, v4, s[40:43], s58 offen sc1
s_waitcnt vmcnt(0)
v_readfirstlane_b32 s59, v4
s_nop 0
s_cmp_lg_u32 s59, 0
s_nop 2
s_cbranch_scc1 hp_end_%=
s_branch hpk_xf1_poll_%=
hpk_xf1_ok_%=:
v_add_u32 v225, 128, v225
ds_read_b128 v[96:99], v1 offset:17408
s_waitcnt lgkmcnt(4)
v_mfma_f32_16x16x32_bf16 v[40:43], a[60:63], v[80:83], v[40:43]
s_add_u32 m0, s61, 0
s_add_u32 s51, s50, 0
buffer_load_dwordx4 v0, s[40:43], s51 offen offset:0 sc1 lds
v_mfma_f32_16x16x32_bf16 v[48:51], a[140:143], v[80:83], v[48:51]
v_mfma_f32_16x16x32_bf16 v[56:59], a[220:223], v[80:83], v[56:59]
ds_read_b128 v[80:83], v1 offset:37888
s_waitcnt lgkmcnt(4)
v_mfma_f32_16x16x32_bf16 v[44:47], a[60:63], v[84:87], v[44:47]
buffer_load_dwordx4 v0, s[40:43], s51 offen offset:1024 sc1 lds
v_mfma_f32_16x16x32_bf16 v[52:55], a[140:143], v[84:87], v[52:55]
v_mfma_f32_16x16x32_bf16 v[60:63], a[220:223], v[84:87], v[60:63]
ds_read_b128 v[84:87], v1 offset:18432
s_waitcnt lgkmcnt(4)
v_mfma_f32_16x16x32_bf16 v[40:43], a[64:67], v[88:91], v[40:43]
buffer_load_dwordx4 v0, s[40:43], s51 offen offset:2048 sc1 lds
v_mfma_f32_16x16x32_bf16 v[48:51], a[144:147], v[88:91], v[48:51]
v_mfma_f32_16x16x32_bf16 v[56:59], a[224:227], v[88:91], v[56:59]
ds_read_b128 v[88:91], v1 offset:38912
s_waitcnt lgkmcnt(4)
v_mfma_f32_16x16x32_bf16 v[44:47], a[64:67], v[92:95], v[44:47]
buffer_load_dwordx4 v0, s[40:43], s51 offen offset:3072 sc1 lds
v_mfma_f32_16x16x32_bf16 v[52:55], a[144:147], v[92:95], v[52:55]
v_mfma_f32_16x16x32_bf16 v[60:63], a[224:227], v[92:95], v[60:63]
ds_read_b128 v[92:95], v1 offset:19456
s_waitcnt lgkmcnt(4)
v_mfma_f32_16x16x32_bf16 v[40:43], a[68:71], v[96:99], v[40:43]
s_add_u32 m0, s61, 4096
s_add_u32 s51, s50, 4096
buffer_load_dwordx4 v0, s[40:43], s51 offen offset:0 sc1 lds
v_mfma_f32_16x16x32_bf16 v[48:51], a[148:151], v[96:99], v[48:51]
v_mfma_f32_16x16x32_bf16 v[56:59], a[228:231], v[96:99], v[56:59]
ds_read_b128 v[96:99], v1 offset:39936
s_waitcnt lgkmcnt(4)
v_mfma_f32_16x16x32_bf16 v[44:47], a[68:71], v[80:83], v[44:47]
buffer_load_dwordx4 v0, s[40:43], s51 offen offset:1024 sc1 lds
v_mfma_f32_16x16x32_bf16 v[52:55], a[148:151], v[80:83], v[52:55]
v_mfma_f32_16x16x32_bf16 v[60:63], a[228:231], v[80:83], v[60:63]
s_add_u32 s64, s64, 40960
s_cmp_ge_u32 s64, 122880
s_cselect_b32 s64, 0, s64
v_add_u32 v1, s64, v0
ds_read_b128 v[80:83], v1 offset:0
s_waitcnt lgkmcnt(4)
v_mfma_f32_16x16x32_bf16 v[40:43], a[72:75], v[84:87], v[40:43]
buffer_load_dwordx4 v0, s[40:43], s51 offen offset:2048 sc1 lds
v_mfma_f32_16x16x32_bf16 v[48:51], a[152:155], v[84:87], v[48:51]
v_mfma_f32_16x16x32_bf16 v[56:59], a[232:235], v[84:87], v[56:59]
ds_read_b128 v[84:87], v1 offset:20480
s_waitcnt lgkmcnt(4)
v_mfma_f32_16x16x32_bf16 v[44:47], a[72:75], v[88:91], v[44:47]
buffer_load_dwordx4 v0, s[40:43], s51 offen offset:3072 sc1 lds
v_mfma_f32_16x16x32_bf16 v[52:55], a[152:155], v[88:91], v[52:55]
v_mfma_f32_16x16x32_bf16 v[60:63], a[232:235], v[88:91], v[60:63]
)"
R"(ds_read_b128 v[88:91], v1 offset:1024
s_waitcnt lgkmcnt(4)
v_mfma_f32_16x16x32_bf16 v[40:43], a[76:79], v[92:95], v[40:43]
s_add_u32 m0, s61, 8192
s_add_u32 s51, s50, 8192
buffer_load_dwordx4 v0, s[40:43], s51 offen offset:0 sc1 lds
v_mfma_f32_16x16x32_bf16 v[48:51], a[156:159], v[92:95], v[48:51]
v_mfma_f32_16x16x32_bf16 v[56:59], a[236:239], v[92:95], v[56:59]
ds_read_b128 v[92:95], v1 offset:21504
s_waitcnt lgkmcnt(4)
v_mfma_f32_16x16x32_bf16 v[44:47], a[76:79], v[96:99], v[44:47]
buffer_load_dwordx4 v0, s[40:43], s51 offen offset:1024 sc1 lds
v_mfma_f32_16x16x32_bf16 v[52:55], a[156:159], v[96:99], v[52:55]
s_add_u32 s50, s50, 40960
s_add_u32 s61, s61, 40960
s_cmp_ge_u32 s61, 122880
s_cselect_b32 s51, 122880, 0
s_sub_u32 s61, s61, s51
v_mfma_f32_16x16x32_bf16 v[60:63], a[236:239], v[96:99], v[60:63]
ds_read_b128 v[96:99], v1 offset:2048
s_mov_b32 s65, s63
s_add_u32 s59, s47, 0
s_and_b32 s59, s59, 63
s_cmp_eq_u32 s59, 31
s_cselect_b32 s59, -31, 1
s_mul_i32 s57, s59, 393216
s_add_u32 s63, s63, s57
s_mov_b32 s52, s53
s_add_u32 s53, s53, s54
s_and_b32 s59, s47, 62
s_cbranch_scc1 hpk_late1_%=
v_cvt_pk_bf16_f32 v104, v40, v41
v_cvt_pk_bf16_f32 v105, v42, v43
v_cvt_pk_bf16_f32 v106, v48, v49
v_cvt_pk_bf16_f32 v107, v50, v51
buffer_store_dwordx4 v[104:107], v0, s[40:43], s65 offen offset:0 sc1
v_cvt_pk_bf16_f32 v108, v44, v45
v_cvt_pk_bf16_f32 v109, v46, v47
v_cvt_pk_bf16_f32 v110, v52, v53
v_cvt_pk_bf16_f32 v111, v54, v55
buffer_store_dwordx4 v[108:111], v0, s[40:43], s65 offen offset:1024 sc1
v_cvt_pk_bf16_f32 v112, v56, v57
v_cvt_pk_bf16_f32 v113, v58, v59
v_cvt_pk_bf16_f32 v114, v60, v61
v_cvt_pk_bf16_f32 v115, v62, v63
buffer_store_dwordx4 v[112:115], v0, s[40:43], s65 offen offset:2048 sc1
s_waitcnt vmcnt(0)
s_barrier
buffer_atomic_add v226, v3, s[40:43], s52 offen
s_mov_b32 s52, 0x40000000
s_nop 0
hpk_late1_%=:
s_add_u32 s47, s47, 1
s_and_b32 s59, s47, 63
s_cbranch_scc1 hpk_loop_%=
s_waitcnt lgkmcnt(0)
v_cvt_pk_bf16_f32 v104, v40, v41
v_cvt_pk_bf16_f32 v105, v42, v43
v_cvt_pk_bf16_f32 v106, v48, v49
v_cvt_pk_bf16_f32 v107, v50, v51
buffer_store_dwordx4 v[104:107], v0, s[40:43], s65 offen offset:0 sc1
v_cvt_pk_bf16_f32 v108, v44, v45
v_cvt_pk_bf16_f32 v109, v46, v47
v_cvt_pk_bf16_f32 v110, v52, v53
v_cvt_pk_bf16_f32 v111, v54, v55
buffer_store_dwordx4 v[108:111], v0, s[40:43], s65 offen offset:1024 sc1
v_cvt_pk_bf16_f32 v112, v56, v57
v_cvt_pk_bf16_f32 v113, v58, v59
v_cvt_pk_bf16_f32 v114, v60, v61
v_cvt_pk_bf16_f32 v115, v62, v63
buffer_store_dwordx4 v[112:115], v0, s[40:43], s65 offen offset:2048 sc1
s_waitcnt vmcnt(0)
s_waitcnt lgkmcnt(0)
s_barrier
buffer_atomic_add v226, v3, s[40:43], s52 offen
s_nop 1
s_branch hp_ret_%=
hp_roleq_%=:
s_mul_i32 s62, s45, 10240
s_mul_i32 s50, s47, 40960
s_add_u32 s50, s50, s62
s_add_u32 s50, s50, 2523136
s_mov_b32 s61, s62
s_lshl_b32 s48, s47, 7
s_add_u32 s48, s48, 43520
v_mov_b32 v225, s48
s_mov_b32 s48, 2
v_and_b32 v224, 1, v15
v_lshl_add_u32 v226, v224, 7, v225
v_mov_b32 v224, 0
v_cmp_gt_u32 vcc, s48, v224
s_cbranch_vccz hpq_xfp_ok_%=
s_mov_b32 s60, 0
s_nop 2
hpq_xfp_poll_%=:
buffer_load_dword v224, v226, s[40:43], s58 offen sc1
s_waitcnt vmcnt(0)
v_cmp_gt_u32 vcc, s48, v224
s_nop 4
s_cbranch_vccz hpq_xfp_ok_%=
s_sleep 1
s_add_u32 s60, s60, 1
s_cmp_gt_u32 s60, 65536
s_cbranch_scc1 hp_abort_%=
s_and_b32 s59, s60, 63
s_cbranch_scc1 hpq_xfp_poll_%=
v_mov_b32 v4, 1024
buffer_load_dword v4, v4, s[40:43], s58 offen sc1
s_waitcnt vmcnt(0)
v_readfirstlane_b32 s59, v4
s_nop 0
s_cmp_lg_u32 s59, 0
s_nop 2
s_cbranch_scc1 hp_end_%=
s_branch hpq_xfp_poll_%=
hpq_xfp_ok_%=:
v_add_u32 v225, 256, v225
s_add_u32 m0, s61, 0
s_add_u32 s51, s50, 0
buffer_load_dwordx4 v0, s[40:43], s51 offen offset:0 sc1 lds
buffer_load_dwordx4 v0, s[40:43], s51 offen offset:1024 sc1 lds
buffer_load_dwordx4 v0, s[40:43], s51 offen offset:2048 sc1 lds
buffer_load_dwordx4 v0, s[40:43], s51 offen offset:3072 sc1 lds
s_add_u32 m0, s61, 4096
s_add_u32 s51, s50, 4096
buffer_load_dwordx4 v0, s[40:43], s51 offen offset:0 sc1 lds
buffer_load_dwordx4 v0, s[40:43], s51 offen offset:1024 sc1 lds
buffer_load_dwordx4 v0, s[40:43], s51 offen offset:2048 sc1 lds
buffer_load_dwordx4 v0, s[40:43], s51 offen offset:3072 sc1 lds
s_add_u32 m0, s61, 8192
s_add_u32 s51, s50, 8192
buffer_load_dwordx4 v0, s[40:43], s51 offen offset:0 sc1 lds
buffer_load_dwordx4 v0, s[40:43], s51 offen offset:1024 sc1 lds
s_add_u32 s50, s50, 40960
s_add_u32 s61, s61, 40960
s_cmp_ge_u32 s61, 122880
s_cselect_b32 s51, 122880, 0
s_sub_u32 s61, s61, s51
s_add_u32 m0, s61, 0
s_add_u32 s51, s50, 0
buffer_load_dwordx4 v0, s[40:43], s51 offen offset:0 sc1 lds
buffer_load_dwordx4 v0, s[40:43], s51 offen offset:1024 sc1 lds
buffer_load_dwordx4 v0, s[40:43], s51 offen offset:2048 sc1 lds
buffer_load_dwordx4 v0, s[40:43], s51 offen offset:3072 sc1 lds
s_add_u32 m0, s61, 4096
s_add_u32 s51, s50, 4096
buffer_load_dwordx4 v0, s[40:43], s51 offen offset:0 sc1 lds
buffer_load_dwordx4 v0, s[40:43], s51 offen offset:1024 sc1 lds
buffer_load_dwordx4 v0, s[40:43], s51 offen offset:2048 sc1 lds
buffer_load_dwordx4 v0, s[40:43], s51 offen offset:3072 sc1 lds
s_add_u32 m0, s61, 8192
s_add_u32 s51, s50, 8192
buffer_load_dwordx4 v0, s[40:43], s51 offen offset:0 sc1 lds
buffer_load_dwordx4 v0, s[40:43], s51 offen offset:1024 sc1 lds
s_add_u32 s50, s50, 40960
s_add_u32 s61, s61, 40960
s_cmp_ge_u32 s61, 122880
s_cselect_b32 s51, 122880, 0
s_sub_u32 s61, s61, s51
v_mov_b32 v224, 0
v_mov_b32 v226, 1
v_mov_b32 v1, v0
s_mov_b32 s64, 0
s_lshl_b32 s48, s44, 7
s_lshr_b32 s49, s44, 2
s_lshl_b32 s49, s49, 7
v_mov_b32 v3, s49
v_cmp_eq_u32 vcc, 0, v15
v_mov_b32 v4, 2147483632
s_nop 0
v_cndmask_b32 v3, v4, v3, vcc
s_mov_b32 s52, 0x40000000
v_lshrrev_b32 v13, 4, v15
v_and_b32 v14, 15, v15
s_lshl_b32 s49, s45, 11
s_add_u32 s49, s49, 122880
v_add_u32 v6, s49, v0
s_lshr_b32 s49, s45, 1
s_lshl_b32 s49, s49, 11
s_and_b32 s53, s45, 1
s_lshl_b32 s53, s53, 3
s_add_u32 s49, s49, s53
s_add_u32 s49, s49, 131072
v_add_u32 v7, s49, v0
s_lshl_b32 s49, s45, 10
s_add_u32 s49, s49, 122880
v_add_u32 v8, s49, v0
v_mov_b32 v9, 0
s_add_u32 s49, s48, 6144
v_mov_b32 v10, s49
v_and_b32 v11, 7, v15
v_lshlrev_b32 v11, 4, v11
v_lshl_add_u32 v11, v13, 7, v11
s_bfe_u32 s49, s44, 0x20001
s_lshl_b32 s49, s49, 8
v_add_u32 v12, s49, v0
v_cmp_gt_u32 vcc, 16, v15
v_mov_b32 v4, 2147483632
s_nop 0
v_cndmask_b32 v12, v4, v12, vcc
)"
R"(s_lshl_b32 s49, s45, 4
v_add_u32 v14, s49, v14
v_lshlrev_b32 v13, 2, v13
v_sub_u32 v14, v14, v13
v_mov_b32 v4, 0xff800000
v_cmp_gt_i32 vcc, 0, v14
s_nop 1
v_cndmask_b32 v212, 0, v4, vcc
v_cmp_gt_i32 vcc, 1, v14
s_nop 1
v_cndmask_b32 v213, 0, v4, vcc
v_cmp_gt_i32 vcc, 2, v14
s_nop 1
v_cndmask_b32 v214, 0, v4, vcc
v_cmp_gt_i32 vcc, 3, v14
s_nop 1
v_cndmask_b32 v215, 0, v4, vcc
v_cmp_gt_i32 vcc, 16, v14
s_nop 1
v_cndmask_b32 v216, 0, v4, vcc
v_cmp_gt_i32 vcc, 17, v14
s_nop 1
v_cndmask_b32 v217, 0, v4, vcc
v_cmp_gt_i32 vcc, 18, v14
s_nop 1
v_cndmask_b32 v218, 0, v4, vcc
v_cmp_gt_i32 vcc, 19, v14
s_nop 1
v_cndmask_b32 v219, 0, v4, vcc
v_cmp_lt_u32 vcc, 31, v15
s_nop 1
v_cndmask_b32 v220, 0, v4, vcc
v_cndmask_b32 v221, 0, v4, vcc
v_cndmask_b32 v222, 0, v4, vcc
v_cndmask_b32 v223, 0, v4, vcc
s_waitcnt vmcnt(10)
s_waitcnt lgkmcnt(0)
s_barrier
s_mov_b32 s65, s47
s_sub_u32 s63, s47, 2
s_cmp_gt_u32 s45, 1
s_cselect_b32 s30, 0x40000000, 0
s_mul_i32 s34, s44, 3072
s_add_u32 s34, s34, 127139840
s_mul_i32 s35, s63, 1536
s_add_u32 s35, s35, 2157568
s_lshl1_add_u32 s36, s44, s45
s_lshl_b32 s36, s36, 10
s_add_u32 s36, s36, 130351104
s_lshr_b32 s37, s44, 3
s_lshl_b32 s37, s37, 1
s_and_b32 s48, s44, 1
s_add_u32 s37, s37, s48
s_lshl3_add_u32 s37, s45, s37
s_lshl_b32 s37, s37, 10
s_add_u32 s37, s37, 132546560
s_mul_i32 s54, s44, 12288
s_add_u32 s54, s54, 7372800
s_or_b32 s54, s54, s30
s_add_u32 s55, s54, 4096
s_add_u32 s56, s54, 8192
s_lshl_b32 s48, s47, 7
s_add_u32 s48, s48, 10240
v_mov_b32 v227, s48
v_mov_b32 v236, 0
s_mov_b32 s48, 32
v_add_u32 v227, -128, v227
v_mov_b32 v236, 32
s_cmp_gt_u32 s45, 1
s_nop 0
s_cbranch_scc1 hpq_light_%=
ds_read_b128 v[80:83], v1 offset:0
ds_read_b128 v[84:87], v1 offset:20480
ds_read_b128 v[88:91], v1 offset:1024
ds_read_b128 v[92:95], v1 offset:21504
ds_read_b128 v[96:99], v1 offset:2048
hpq_loop_%=:
s_mov_b32 s29, s52
s_waitcnt vmcnt(25)
s_add_u32 s48, s63, 1
s_cmp_ge_i32 s63, s65
s_cselect_b32 s48, s48, 0
s_cselect_b32 s49, s30, 0x40000000
v_cmp_gt_u32 vcc, s48, v9
s_cbranch_vccz hpq_kma0_ok_%=
s_waitcnt vmcnt(0)
buffer_atomic_add v226, v3, s[40:43], s29 offen
s_mov_b32 s29, 0x40000000
v_cmp_gt_u32 vcc, s48, v9
s_cbranch_vccz hpq_kma0_ok_%=
s_mov_b32 s60, 0
s_nop 2
hpq_kma0_poll_%=:
buffer_load_dword v9, v10, s[40:43], s58 offen sc1
s_waitcnt vmcnt(0)
v_cmp_gt_u32 vcc, s48, v9
s_nop 4
s_cbranch_vccz hpq_kma0_ok_%=
s_sleep 2
s_add_u32 s60, s60, 1
s_cmp_gt_u32 s60, 65536
s_cbranch_scc1 hp_abort_%=
s_and_b32 s59, s60, 63
s_cbranch_scc1 hpq_kma0_poll_%=
v_mov_b32 v4, 1024
buffer_load_dword v4, v4, s[40:43], s58 offen sc1
s_waitcnt vmcnt(0)
v_readfirstlane_b32 s59, v4
s_nop 0
s_cmp_lg_u32 s59, 0
s_nop 2
s_cbranch_scc1 hp_end_%=
s_branch hpq_kma0_poll_%=
hpq_kma0_ok_%=:
s_movk_i32 s48, 32
v_cmp_gt_u32 vcc, s48, v236
s_cbranch_vccz hpq_kxa0_ok_%=
s_mov_b32 s60, 0
s_nop 2
hpq_kxa0_poll_%=:
buffer_load_dword v236, v227, s[40:43], s58 offen sc1
s_waitcnt vmcnt(0)
v_cmp_gt_u32 vcc, s48, v236
s_nop 4
s_cbranch_vccz hpq_kxa0_ok_%=
s_sleep 2
s_add_u32 s60, s60, 1
s_cmp_gt_u32 s60, 65536
s_cbranch_scc1 hp_abort_%=
s_and_b32 s59, s60, 63
s_cbranch_scc1 hpq_kxa0_poll_%=
v_mov_b32 v4, 1024
buffer_load_dword v4, v4, s[40:43], s58 offen sc1
s_waitcnt vmcnt(0)
v_readfirstlane_b32 s59, v4
s_nop 0
s_cmp_lg_u32 s59, 0
s_nop 2
s_cbranch_scc1 hp_end_%=
s_branch hpq_kxa0_poll_%=
hpq_kxa0_ok_%=:
v_add_u32 v227, 128, v227
s_or_b32 s57, s34, s49
s_waitcnt lgkmcnt(4)
v_mfma_f32_16x16x32_bf16 v[16:19], a[0:3], v[80:83], 0
s_or_b32 s52, s35, s49
buffer_load_dwordx4 v[164:167], v11, s[40:43], s57 offen sc1
v_mfma_f32_16x16x32_bf16 v[24:27], a[80:83], v[80:83], 0
s_or_b32 s26, s36, s49
buffer_load_dwordx4 v[168:171], v11, s[40:43], s57 offen offset:512 sc1
v_mfma_f32_16x16x32_bf16 v[32:35], a[160:163], v[80:83], 0
s_or_b32 s27, s37, s49
ds_read_b128 v[80:83], v1 offset:22528
s_waitcnt lgkmcnt(4)
v_mfma_f32_16x16x32_bf16 v[20:23], a[0:3], v[84:87], 0
s_add_u32 s38, s63, 0
buffer_load_dwordx4 v[172:175], v11, s[40:43], s57 offen offset:1024 sc1
v_mfma_f32_16x16x32_bf16 v[28:31], a[80:83], v[84:87], 0
s_and_b32 s38, s38, 63
buffer_load_dwordx4 v[176:179], v11, s[40:43], s57 offen offset:1536 sc1
v_mfma_f32_16x16x32_bf16 v[36:39], a[160:163], v[84:87], 0
s_cmp_eq_u32 s38, 31
ds_read_b128 v[84:87], v1 offset:3072
s_waitcnt lgkmcnt(4)
v_mfma_f32_16x16x32_bf16 v[16:19], a[4:7], v[88:91], v[16:19]
s_cselect_b32 s38, -31, 1
buffer_load_dwordx4 v[180:183], v11, s[40:43], s57 offen offset:2048 sc1
v_mfma_f32_16x16x32_bf16 v[24:27], a[84:87], v[88:91], v[24:27]
s_mul_i32 s39, s38, 98304
buffer_load_dwordx4 v[184:187], v11, s[40:43], s57 offen offset:2560 sc1
v_mfma_f32_16x16x32_bf16 v[32:35], a[164:167], v[88:91], v[32:35]
s_add_u32 s34, s34, s39
ds_read_b128 v[88:91], v1 offset:23552
s_waitcnt lgkmcnt(4)
v_mfma_f32_16x16x32_bf16 v[20:23], a[4:7], v[92:95], v[20:23]
s_mul_i32 s39, s38, 65536
buffer_load_dword v236, v227, s[40:43], s58 offen sc1
v_cvt_pk_bf16_f32 v104, v40, v41
v_mfma_f32_16x16x32_bf16 v[28:31], a[84:87], v[92:95], v[28:31]
s_add_u32 s36, s36, s39
s_waitcnt vmcnt(20)
v_swap_b32 v150, v152
v_mfma_f32_16x16x32_bf16 v[36:39], a[164:167], v[92:95], v[36:39]
s_mul_i32 s39, s38, 16384
v_cvt_pk_bf16_f32 v105, v42, v43
ds_read_b128 v[92:95], v1 offset:4096
s_waitcnt lgkmcnt(4)
v_mfma_f32_16x16x32_bf16 v[16:19], a[8:11], v[96:99], v[16:19]
s_add_u32 s37, s37, s39
v_swap_b32 v151, v153
v_mfma_f32_16x16x32_bf16 v[24:27], a[88:91], v[96:99], v[24:27]
s_add_u32 s35, s35, 1536
v_swap_b32 v158, v160
v_mfma_f32_16x16x32_bf16 v[32:35], a[168:171], v[96:99], v[32:35]
s_add_u32 s63, s63, 1
v_cvt_pk_bf16_f32 v106, v48, v49
ds_read_b128 v[96:99], v1 offset:24576
s_waitcnt lgkmcnt(4)
v_mfma_f32_16x16x32_bf16 v[20:23], a[8:11], v[80:83], v[20:23]
v_swap_b32 v159, v161
)"
R"(v_mfma_f32_16x16x32_bf16 v[28:31], a[88:91], v[80:83], v[28:31]
v_cvt_pk_bf16_f32 v107, v50, v51
v_mfma_f32_16x16x32_bf16 v[36:39], a[168:171], v[80:83], v[36:39]
ds_read_b128 v[80:83], v1 offset:5120
s_waitcnt lgkmcnt(4)
v_mfma_f32_16x16x32_bf16 v[16:19], a[12:15], v[84:87], v[16:19]
ds_write_b128 v6, v[104:107] offset:12288
v_mfma_f32_16x16x32_bf16 v[64:67], v[116:119], v[188:191], v[212:215]
v_cvt_pk_bf16_f32 v108, v44, v45
v_cvt_pk_bf16_f32 v109, v46, v47
v_mfma_f32_16x16x32_bf16 v[24:27], a[92:95], v[84:87], v[24:27]
v_cvt_pk_bf16_f32 v110, v52, v53
v_cvt_pk_bf16_f32 v111, v54, v55
v_mfma_f32_16x16x32_bf16 v[68:71], v[120:123], v[188:191], v[216:219]
ds_write_b128 v6, v[108:111] offset:13312
v_mfma_f32_16x16x32_bf16 v[32:35], a[172:175], v[84:87], v[32:35]
v_cvt_pk_bf16_f32 v112, v56, v57
ds_read_b128 v[84:87], v1 offset:25600
s_waitcnt lgkmcnt(6)
v_mfma_f32_16x16x32_bf16 v[20:23], a[12:15], v[88:91], v[20:23]
v_cvt_pk_bf16_f32 v113, v58, v59
v_cvt_pk_bf16_f32 v114, v60, v61
v_mfma_f32_16x16x32_bf16 v[64:67], v[124:127], v[192:195], v[64:67]
v_cvt_pk_bf16_f32 v115, v62, v63
v_mfma_f32_16x16x32_bf16 v[28:31], a[92:95], v[88:91], v[28:31]
ds_write2st64_b64 v7, v[112:113], v[114:115] offset0:24 offset1:26
v_mfma_f32_16x16x32_bf16 v[68:71], v[128:131], v[192:195], v[68:71]
buffer_load_dword v224, v225, s[40:43], s58 offen sc1
v_mfma_f32_16x16x32_bf16 v[36:39], a[172:175], v[88:91], v[36:39]
ds_read_b128 v[88:91], v1 offset:6144
s_waitcnt lgkmcnt(7)
v_mfma_f32_16x16x32_bf16 v[16:19], a[16:19], v[92:95], v[16:19]
v_mfma_f32_16x16x32_bf16 v[64:67], v[132:135], v[196:199], v[64:67]
v_mfma_f32_16x16x32_bf16 v[24:27], a[96:99], v[92:95], v[24:27]
v_mfma_f32_16x16x32_bf16 v[68:71], v[136:139], v[196:199], v[68:71]
v_mfma_f32_16x16x32_bf16 v[32:35], a[176:179], v[92:95], v[32:35]
ds_read_b128 v[92:95], v1 offset:26624
s_waitcnt lgkmcnt(7)
v_mfma_f32_16x16x32_bf16 v[20:23], a[16:19], v[96:99], v[20:23]
v_mfma_f32_16x16x32_bf16 v[64:67], v[140:143], v[200:203], v[64:67]
v_mfma_f32_16x16x32_bf16 v[28:31], a[96:99], v[96:99], v[28:31]
v_mfma_f32_16x16x32_bf16 v[68:71], v[144:147], v[200:203], v[68:71]
v_mfma_f32_16x16x32_bf16 v[36:39], a[176:179], v[96:99], v[36:39]
ds_read_b128 v[96:99], v1 offset:7168
s_waitcnt lgkmcnt(7)
v_mfma_f32_16x16x32_bf16 v[16:19], a[20:23], v[80:83], v[16:19]
v_mfma_f32_16x16x32_bf16 v[64:67], v[148:151], v[204:207], v[64:67]
v_mfma_f32_16x16x32_bf16 v[24:27], a[100:103], v[80:83], v[24:27]
v_mfma_f32_16x16x32_bf16 v[68:71], v[152:155], v[204:207], v[68:71]
v_mfma_f32_16x16x32_bf16 v[32:35], a[180:183], v[80:83], v[32:35]
ds_read_b128 v[80:83], v1 offset:27648
s_waitcnt lgkmcnt(5)
v_mfma_f32_16x16x32_bf16 v[20:23], a[20:23], v[84:87], v[20:23]
v_mfma_f32_16x16x32_bf16 v[64:67], v[156:159], v[208:211], v[64:67]
v_mfma_f32_16x16x32_bf16 v[28:31], a[100:103], v[84:87], v[28:31]
v_mfma_f32_16x16x32_bf16 v[68:71], v[160:163], v[208:211], v[68:71]
v_mfma_f32_16x16x32_bf16 v[36:39], a[180:183], v[84:87], v[36:39]
ds_read_b128 v[84:87], v1 offset:8192
s_waitcnt lgkmcnt(4)
v_mfma_f32_16x16x32_bf16 v[16:19], a[24:27], v[88:91], v[16:19]
v_mfma_f32_16x16x32_bf16 v[24:27], a[104:107], v[88:91], v[24:27]
v_mfma_f32_16x16x32_bf16 v[32:35], a[184:187], v[88:91], v[32:35]
ds_read_b128 v[88:91], v1 offset:28672
s_waitcnt lgkmcnt(4)
v_mfma_f32_16x16x32_bf16 v[20:23], a[24:27], v[92:95], v[20:23]
buffer_load_dword v9, v10, s[40:43], s58 offen sc1
s_waitcnt vmcnt(8)
v_mfma_f32_16x16x32_bf16 v[72:75], v[164:167], v[188:191], v[220:223]
v_mfma_f32_16x16x32_bf16 v[28:31], a[104:107], v[92:95], v[28:31]
s_waitcnt vmcnt(7)
v_mfma_f32_16x16x32_bf16 v[72:75], v[168:171], v[192:195], v[72:75]
v_mfma_f32_16x16x32_bf16 v[36:39], a[184:187], v[92:95], v[36:39]
ds_read_b128 v[92:95], v1 offset:9216
s_waitcnt lgkmcnt(4)
v_mfma_f32_16x16x32_bf16 v[16:19], a[28:31], v[96:99], v[16:19]
buffer_load_dwordx4 v[116:119], v0, s[40:43], s54 offen sc1
s_waitcnt vmcnt(7)
v_mfma_f32_16x16x32_bf16 v[72:75], v[172:175], v[196:199], v[72:75]
v_mfma_f32_16x16x32_bf16 v[24:27], a[108:111], v[96:99], v[24:27]
s_waitcnt vmcnt(6)
v_mfma_f32_16x16x32_bf16 v[72:75], v[176:179], v[200:203], v[72:75]
v_mfma_f32_16x16x32_bf16 v[32:35], a[188:191], v[96:99], v[32:35]
ds_read_b128 v[96:99], v1 offset:29696
s_waitcnt lgkmcnt(4)
v_mfma_f32_16x16x32_bf16 v[20:23], a[28:31], v[80:83], v[20:23]
buffer_load_dwordx4 v[120:123], v0, s[40:43], s54 offen offset:1024 sc1
s_waitcnt vmcnt(6)
v_mfma_f32_16x16x32_bf16 v[72:75], v[180:183], v[204:207], v[72:75]
v_mfma_f32_16x16x32_bf16 v[28:31], a[108:111], v[80:83], v[28:31]
s_waitcnt vmcnt(5)
v_mfma_f32_16x16x32_bf16 v[72:75], v[184:187], v[208:211], v[72:75]
v_mfma_f32_16x16x32_bf16 v[36:39], a[188:191], v[80:83], v[36:39]
ds_read_b128 v[80:83], v1 offset:10240
s_waitcnt lgkmcnt(4)
v_mfma_f32_16x16x32_bf16 v[16:19], a[32:35], v[84:87], v[16:19]
buffer_load_dwordx4 v[124:127], v0, s[40:43], s54 offen offset:3072 sc1
v_mfma_f32_16x16x32_bf16 v[24:27], a[112:115], v[84:87], v[24:27]
v_mfma_f32_16x16x32_bf16 v[32:35], a[192:195], v[84:87], v[32:35]
ds_read_b128 v[84:87], v1 offset:30720
s_waitcnt lgkmcnt(4)
v_mfma_f32_16x16x32_bf16 v[20:23], a[32:35], v[88:91], v[20:23]
v_max3_f32 v76, v64, v65, v66
v_max3_f32 v76, v76, v67, v68
v_mfma_f32_16x16x32_bf16 v[28:31], a[112:115], v[88:91], v[28:31]
v_max3_f32 v76, v76, v69, v70
v_max3_f32 v76, v76, v71, v72
v_mfma_f32_16x16x32_bf16 v[36:39], a[192:195], v[88:91], v[36:39]
v_max3_f32 v76, v76, v73, v74
ds_read_b128 v[88:91], v1 offset:11264
s_waitcnt lgkmcnt(4)
v_mfma_f32_16x16x32_bf16 v[16:19], a[36:39], v[92:95], v[16:19]
buffer_load_dwordx4 v[128:131], v0, s[40:43], s55 offen sc1
v_max_f32 v76, v76, v75
v_mfma_f32_16x16x32_bf16 v[24:27], a[116:119], v[92:95], v[24:27]
v_mov_b32 v77, v76
v_mfma_f32_16x16x32_bf16 v[32:35], a[196:199], v[92:95], v[32:35]
ds_read_b128 v[92:95], v1 offset:31744
s_waitcnt lgkmcnt(4)
v_mfma_f32_16x16x32_bf16 v[20:23], a[36:39], v[96:99], v[20:23]
v_permlane16_swap_b32 v76, v77
v_mfma_f32_16x16x32_bf16 v[28:31], a[116:119], v[96:99], v[28:31]
v_max_f32 v76, v76, v77
v_mov_b32 v77, v76
v_mfma_f32_16x16x32_bf16 v[36:39], a[196:199], v[96:99], v[36:39]
buffer_load_dwordx4 v[132:135], v0, s[40:43], s55 offen offset:2048 sc1
ds_read_b128 v[96:99], v1 offset:12288
s_waitcnt lgkmcnt(4)
v_mfma_f32_16x16x32_bf16 v[16:19], a[40:43], v[80:83], v[16:19]
buffer_load_dwordx4 v[136:139], v0, s[40:43], s55 offen offset:3072 sc1
v_mfma_f32_16x16x32_bf16 v[24:27], a[120:123], v[80:83], v[24:27]
v_permlane32_swap_b32 v76, v77
v_mfma_f32_16x16x32_bf16 v[32:35], a[200:203], v[80:83], v[32:35]
v_max_f32 v76, v76, v77
ds_read_b128 v[80:83], v1 offset:32768
s_waitcnt lgkmcnt(4)
v_mfma_f32_16x16x32_bf16 v[20:23], a[40:43], v[84:87], v[20:23]
v_mul_f32 v76, 0xbdd53b95, v76
v_fmamk_f32 v64, v64, 0x3dd53b95, v76
v_mfma_f32_16x16x32_bf16 v[28:31], a[120:123], v[84:87], v[28:31]
v_fmamk_f32 v65, v65, 0x3dd53b95, v76
v_fmamk_f32 v66, v66, 0x3dd53b95, v76
v_mfma_f32_16x16x32_bf16 v[36:39], a[200:203], v[84:87], v[36:39]
v_fmamk_f32 v67, v67, 0x3dd53b95, v76
ds_read_b128 v[84:87], v1 offset:13312
s_waitcnt lgkmcnt(4)
v_mfma_f32_16x16x32_bf16 v[16:19], a[44:47], v[88:91], v[16:19]
buffer_load_dwordx4 v[140:143], v0, s[40:43], s56 offen offset:1024 sc1
v_fmamk_f32 v68, v68, 0x3dd53b95, v76
v_mfma_f32_16x16x32_bf16 v[24:27], a[124:127], v[88:91], v[24:27]
v_fmamk_f32 v69, v69, 0x3dd53b95, v76
v_fmamk_f32 v70, v70, 0x3dd53b95, v76
v_mfma_f32_16x16x32_bf16 v[32:35], a[204:207], v[88:91], v[32:35]
v_fmamk_f32 v71, v71, 0x3dd53b95, v76
ds_read_b128 v[88:91], v1 offset:33792
s_waitcnt lgkmcnt(4)
v_mfma_f32_16x16x32_bf16 v[20:23], a[44:47], v[92:95], v[20:23]
v_fmamk_f32 v72, v72, 0x3dd53b95, v76
v_fmamk_f32 v73, v73, 0x3dd53b95, v76
v_mfma_f32_16x16x32_bf16 v[28:31], a[124:127], v[92:95], v[28:31]
v_fmamk_f32 v74, v74, 0x3dd53b95, v76
v_fmamk_f32 v75, v75, 0x3dd53b95, v76
v_mfma_f32_16x16x32_bf16 v[36:39], a[204:207], v[92:95], v[36:39]
buffer_load_dwordx4 v[144:147], v0, s[40:43], s56 offen offset:2048 sc1
ds_read_b128 v[92:95], v1 offset:14336
s_waitcnt lgkmcnt(4)
v_mfma_f32_16x16x32_bf16 v[16:19], a[48:51], v[96:99], v[16:19]
buffer_load_dwordx4 v[148:151], v0, s[40:43], s54 offen offset:2048 sc1
v_mfma_f32_16x16x32_bf16 v[24:27], a[128:131], v[96:99], v[24:27]
v_exp_f32 v64, v64
v_mfma_f32_16x16x32_bf16 v[32:35], a[208:211], v[96:99], v[32:35]
ds_read_b128 v[96:99], v1 offset:34816
s_waitcnt lgkmcnt(4)
v_mfma_f32_16x16x32_bf16 v[20:23], a[48:51], v[80:83], v[20:23]
v_exp_f32 v65, v65
v_mfma_f32_16x16x32_bf16 v[28:31], a[128:131], v[80:83], v[28:31]
v_exp_f32 v66, v66
v_mfma_f32_16x16x32_bf16 v[36:39], a[208:211], v[80:83], v[36:39]
buffer_load_dwordx4 v[152:155], v0, s[40:43], s55 offen offset:1024 sc1
ds_read_b128 v[80:83], v1 offset:15360
s_waitcnt lgkmcnt(4)
v_mfma_f32_16x16x32_bf16 v[16:19], a[52:55], v[84:87], v[16:19]
buffer_load_dwordx4 v[156:159], v0, s[40:43], s56 offen sc1
v_mfma_f32_16x16x32_bf16 v[24:27], a[132:135], v[84:87], v[24:27]
v_exp_f32 v67, v67
v_mfma_f32_16x16x32_bf16 v[32:35], a[212:215], v[84:87], v[32:35]
s_add_u32 s38, s63, 0
ds_read_b128 v[84:87], v1 offset:35840
s_waitcnt lgkmcnt(4)
v_mfma_f32_16x16x32_bf16 v[20:23], a[52:55], v[88:91], v[20:23]
s_and_b32 s38, s38, 63
v_exp_f32 v68, v68
v_mfma_f32_16x16x32_bf16 v[28:31], a[132:135], v[88:91], v[28:31]
s_cmp_eq_u32 s38, 31
v_exp_f32 v69, v69
v_mfma_f32_16x16x32_bf16 v[36:39], a[212:215], v[88:91], v[36:39]
s_cselect_b32 s38, -31, 1
buffer_load_dwordx4 v[160:163], v0, s[40:43], s56 offen offset:3072 sc1
ds_read_b128 v[88:91], v1 offset:16384
s_waitcnt lgkmcnt(4)
v_mfma_f32_16x16x32_bf16 v[16:19], a[56:59], v[92:95], v[16:19]
)"
R"(s_mul_i32 s39, s38, 393216
v_exp_f32 v70, v70
v_mfma_f32_16x16x32_bf16 v[24:27], a[136:139], v[92:95], v[24:27]
s_add_u32 s54, s54, s39
v_exp_f32 v71, v71
v_mfma_f32_16x16x32_bf16 v[32:35], a[216:219], v[92:95], v[32:35]
s_add_u32 s55, s55, s39
ds_read_b128 v[92:95], v1 offset:36864
s_waitcnt lgkmcnt(4)
v_mfma_f32_16x16x32_bf16 v[20:23], a[56:59], v[96:99], v[20:23]
s_add_u32 s56, s56, s39
v_exp_f32 v72, v72
v_mfma_f32_16x16x32_bf16 v[28:31], a[136:139], v[96:99], v[28:31]
v_exp_f32 v73, v73
v_mfma_f32_16x16x32_bf16 v[36:39], a[216:219], v[96:99], v[36:39]
s_waitcnt vmcnt(13) lgkmcnt(4)
s_barrier
buffer_atomic_add v226, v3, s[40:43], s29 offen
s_and_b32 s48, s47, 63
s_cmp_lt_u32 s48, 62
s_cselect_b32 s48, 2, 0
v_cmp_gt_u32 vcc, s48, v224
s_cbranch_vccz hpq_xf0_ok_%=
s_mov_b32 s60, 0
s_nop 2
hpq_xf0_poll_%=:
buffer_load_dword v224, v225, s[40:43], s58 offen sc1
s_waitcnt vmcnt(0)
v_cmp_gt_u32 vcc, s48, v224
s_nop 4
s_cbranch_vccz hpq_xf0_ok_%=
s_sleep 2
s_add_u32 s60, s60, 1
s_cmp_gt_u32 s60, 65536
s_cbranch_scc1 hp_abort_%=
s_and_b32 s59, s60, 63
s_cbranch_scc1 hpq_xf0_poll_%=
v_mov_b32 v4, 1024
buffer_load_dword v4, v4, s[40:43], s58 offen sc1
s_waitcnt vmcnt(0)
v_readfirstlane_b32 s59, v4
s_nop 0
s_cmp_lg_u32 s59, 0
s_nop 2
s_cbranch_scc1 hp_end_%=
s_branch hpq_xf0_poll_%=
hpq_xf0_ok_%=:
v_add_u32 v225, 128, v225
ds_read_b128 v[96:99], v1 offset:17408
s_waitcnt lgkmcnt(4)
v_mfma_f32_16x16x32_bf16 v[16:19], a[60:63], v[80:83], v[16:19]
s_add_u32 m0, s61, 0
s_add_u32 s51, s50, 0
buffer_load_dwordx4 v0, s[40:43], s51 offen offset:0 sc1 lds
v_mfma_f32_16x16x32_bf16 v[24:27], a[140:143], v[80:83], v[24:27]
v_exp_f32 v74, v74
v_mfma_f32_16x16x32_bf16 v[32:35], a[220:223], v[80:83], v[32:35]
ds_read_b128 v[188:191], v8 offset:12288
ds_read_b128 v[80:83], v1 offset:37888
s_waitcnt lgkmcnt(5)
v_mfma_f32_16x16x32_bf16 v[20:23], a[60:63], v[84:87], v[20:23]
buffer_load_dwordx4 v0, s[40:43], s51 offen offset:1024 sc1 lds
v_mfma_f32_16x16x32_bf16 v[28:31], a[140:143], v[84:87], v[28:31]
v_exp_f32 v75, v75
v_mfma_f32_16x16x32_bf16 v[36:39], a[220:223], v[84:87], v[36:39]
v_add_f32 v76, v64, v65
ds_read_b128 v[84:87], v1 offset:18432
s_waitcnt lgkmcnt(5)
v_mfma_f32_16x16x32_bf16 v[16:19], a[64:67], v[88:91], v[16:19]
buffer_load_dwordx4 v0, s[40:43], s51 offen offset:2048 sc1 lds
v_mfma_f32_16x16x32_bf16 v[24:27], a[144:147], v[88:91], v[24:27]
v_add_f32 v76, v76, v66
v_add_f32 v76, v76, v67
v_mfma_f32_16x16x32_bf16 v[32:35], a[224:227], v[88:91], v[32:35]
v_add_f32 v76, v76, v68
ds_read_b128 v[88:91], v1 offset:38912
s_waitcnt lgkmcnt(5)
v_mfma_f32_16x16x32_bf16 v[20:23], a[64:67], v[92:95], v[20:23]
buffer_load_dwordx4 v0, s[40:43], s51 offen offset:3072 sc1 lds
v_mfma_f32_16x16x32_bf16 v[28:31], a[144:147], v[92:95], v[28:31]
v_add_f32 v76, v76, v69
v_add_f32 v76, v76, v70
v_mfma_f32_16x16x32_bf16 v[36:39], a[224:227], v[92:95], v[36:39]
v_add_f32 v76, v76, v71
ds_read_b128 v[92:95], v1 offset:19456
s_waitcnt lgkmcnt(5)
v_mfma_f32_16x16x32_bf16 v[16:19], a[68:71], v[96:99], v[16:19]
s_add_u32 m0, s61, 4096
s_add_u32 s51, s50, 4096
buffer_load_dwordx4 v0, s[40:43], s51 offen offset:0 sc1 lds
v_mfma_f32_16x16x32_bf16 v[24:27], a[148:151], v[96:99], v[24:27]
v_add_f32 v76, v76, v72
v_add_f32 v76, v76, v73
v_mfma_f32_16x16x32_bf16 v[32:35], a[228:231], v[96:99], v[32:35]
v_add_f32 v76, v76, v74
ds_read_b128 v[192:195], v8 offset:14336
ds_read_b128 v[196:199], v8 offset:16384
ds_read_b128 v[200:203], v8 offset:18432
ds_read_b128 v[204:207], v8 offset:20480
ds_read_b128 v[208:211], v8 offset:22528
ds_read_b128 v[96:99], v1 offset:39936
s_waitcnt lgkmcnt(9)
v_mfma_f32_16x16x32_bf16 v[20:23], a[68:71], v[80:83], v[20:23]
buffer_load_dwordx4 v0, s[40:43], s51 offen offset:1024 sc1 lds
v_mfma_f32_16x16x32_bf16 v[28:31], a[148:151], v[80:83], v[28:31]
v_add_f32 v76, v76, v75
v_mov_b32 v77, v76
v_mfma_f32_16x16x32_bf16 v[36:39], a[228:231], v[80:83], v[36:39]
s_add_u32 s64, s64, 40960
s_cmp_ge_u32 s64, 122880
s_cselect_b32 s64, 0, s64
v_add_u32 v1, s64, v0
ds_read_b128 v[80:83], v1 offset:0
s_waitcnt lgkmcnt(9)
v_mfma_f32_16x16x32_bf16 v[16:19], a[72:75], v[84:87], v[16:19]
buffer_load_dwordx4 v0, s[40:43], s51 offen offset:2048 sc1 lds
v_mfma_f32_16x16x32_bf16 v[24:27], a[152:155], v[84:87], v[24:27]
v_permlane16_swap_b32 v76, v77
v_mfma_f32_16x16x32_bf16 v[32:35], a[232:235], v[84:87], v[32:35]
v_add_f32 v76, v76, v77
ds_read_b128 v[84:87], v1 offset:20480
s_waitcnt lgkmcnt(9)
v_mfma_f32_16x16x32_bf16 v[20:23], a[72:75], v[88:91], v[20:23]
buffer_load_dwordx4 v0, s[40:43], s51 offen offset:3072 sc1 lds
v_mfma_f32_16x16x32_bf16 v[28:31], a[152:155], v[88:91], v[28:31]
v_mov_b32 v77, v76
v_mfma_f32_16x16x32_bf16 v[36:39], a[232:235], v[88:91], v[36:39]
ds_read_b128 v[88:91], v1 offset:1024
s_waitcnt lgkmcnt(9)
v_mfma_f32_16x16x32_bf16 v[16:19], a[76:79], v[92:95], v[16:19]
s_add_u32 m0, s61, 8192
s_add_u32 s51, s50, 8192
buffer_load_dwordx4 v0, s[40:43], s51 offen offset:0 sc1 lds
v_mfma_f32_16x16x32_bf16 v[24:27], a[156:159], v[92:95], v[24:27]
v_permlane32_swap_b32 v76, v77
v_mfma_f32_16x16x32_bf16 v[32:35], a[236:239], v[92:95], v[32:35]
v_add_f32 v76, v76, v77
ds_read_b128 v[92:95], v1 offset:21504
s_waitcnt lgkmcnt(4)
v_mfma_f32_16x16x32_bf16 v[20:23], a[76:79], v[96:99], v[20:23]
buffer_load_dwordx4 v0, s[40:43], s51 offen offset:1024 sc1 lds
v_mfma_f32_16x16x32_bf16 v[28:31], a[156:159], v[96:99], v[28:31]
s_add_u32 s50, s50, 40960
s_add_u32 s61, s61, 40960
s_cmp_ge_u32 s61, 122880
s_cselect_b32 s51, 122880, 0
s_sub_u32 s61, s61, s51
v_rcp_f32 v76, v76
v_mfma_f32_16x16x32_bf16 v[36:39], a[236:239], v[96:99], v[36:39]
v_mul_f32 v64, v64, v76
ds_read_b128 v[96:99], v1 offset:2048
v_mul_f32 v65, v65, v76
v_mul_f32 v66, v66, v76
v_mul_f32 v67, v67, v76
v_mul_f32 v68, v68, v76
v_mul_f32 v69, v69, v76
v_mul_f32 v70, v70, v76
v_mul_f32 v71, v71, v76
v_mul_f32 v72, v72, v76
v_mul_f32 v73, v73, v76
v_mul_f32 v74, v74, v76
v_mul_f32 v75, v75, v76
v_cvt_pk_bf16_f32 v228, v64, v65
v_cvt_pk_bf16_f32 v229, v66, v67
v_cvt_pk_bf16_f32 v230, v68, v69
v_cvt_pk_bf16_f32 v231, v70, v71
v_cvt_pk_bf16_f32 v232, v72, v73
v_cvt_pk_bf16_f32 v233, v74, v75
v_mov_b32 v234, v232
v_mov_b32 v235, v233
buffer_store_dwordx4 v[228:231], v0, s[40:43], s26 offen sc1
v_permlane16_swap_b32 v232, v234
v_permlane16_swap_b32 v233, v235
buffer_store_dwordx4 v[232:235], v12, s[40:43], s27 offen sc1
s_add_u32 s47, s47, 1
s_mov_b32 s29, s52
s_waitcnt vmcnt(25)
s_add_u32 s48, s63, 1
s_cmp_ge_i32 s63, s65
s_cselect_b32 s48, s48, 0
s_cselect_b32 s49, s30, 0x40000000
v_cmp_gt_u32 vcc, s48, v9
s_cbranch_vccz hpq_kma1_ok_%=
s_waitcnt vmcnt(0)
buffer_atomic_add v226, v3, s[40:43], s29 offen
s_mov_b32 s29, 0x40000000
v_cmp_gt_u32 vcc, s48, v9
s_cbranch_vccz hpq_kma1_ok_%=
s_mov_b32 s60, 0
s_nop 2
hpq_kma1_poll_%=:
buffer_load_dword v9, v10, s[40:43], s58 offen sc1
s_waitcnt vmcnt(0)
v_cmp_gt_u32 vcc, s48, v9
s_nop 4
s_cbranch_vccz hpq_kma1_ok_%=
s_sleep 2
s_add_u32 s60, s60, 1
s_cmp_gt_u32 s60, 65536
s_cbranch_scc1 hp_abort_%=
)"
R"(s_and_b32 s59, s60, 63
s_cbranch_scc1 hpq_kma1_poll_%=
v_mov_b32 v4, 1024
buffer_load_dword v4, v4, s[40:43], s58 offen sc1
s_waitcnt vmcnt(0)
v_readfirstlane_b32 s59, v4
s_nop 0
s_cmp_lg_u32 s59, 0
s_nop 2
s_cbranch_scc1 hp_end_%=
s_branch hpq_kma1_poll_%=
hpq_kma1_ok_%=:
s_movk_i32 s48, 32
v_cmp_gt_u32 vcc, s48, v236
s_cbranch_vccz hpq_kxa1_ok_%=
s_mov_b32 s60, 0
s_nop 2
hpq_kxa1_poll_%=:
buffer_load_dword v236, v227, s[40:43], s58 offen sc1
s_waitcnt vmcnt(0)
v_cmp_gt_u32 vcc, s48, v236
s_nop 4
s_cbranch_vccz hpq_kxa1_ok_%=
s_sleep 2
s_add_u32 s60, s60, 1
s_cmp_gt_u32 s60, 65536
s_cbranch_scc1 hp_abort_%=
s_and_b32 s59, s60, 63
s_cbranch_scc1 hpq_kxa1_poll_%=
v_mov_b32 v4, 1024
buffer_load_dword v4, v4, s[40:43], s58 offen sc1
s_waitcnt vmcnt(0)
v_readfirstlane_b32 s59, v4
s_nop 0
s_cmp_lg_u32 s59, 0
s_nop 2
s_cbranch_scc1 hp_end_%=
s_branch hpq_kxa1_poll_%=
hpq_kxa1_ok_%=:
v_add_u32 v227, 128, v227
s_or_b32 s57, s34, s49
s_waitcnt lgkmcnt(4)
v_mfma_f32_16x16x32_bf16 v[40:43], a[0:3], v[80:83], 0
s_or_b32 s52, s35, s49
buffer_load_dwordx4 v[164:167], v11, s[40:43], s57 offen sc1
v_mfma_f32_16x16x32_bf16 v[48:51], a[80:83], v[80:83], 0
s_or_b32 s26, s36, s49
buffer_load_dwordx4 v[168:171], v11, s[40:43], s57 offen offset:512 sc1
v_mfma_f32_16x16x32_bf16 v[56:59], a[160:163], v[80:83], 0
s_or_b32 s27, s37, s49
ds_read_b128 v[80:83], v1 offset:22528
s_waitcnt lgkmcnt(4)
v_mfma_f32_16x16x32_bf16 v[44:47], a[0:3], v[84:87], 0
s_add_u32 s38, s63, 0
buffer_load_dwordx4 v[172:175], v11, s[40:43], s57 offen offset:1024 sc1
v_mfma_f32_16x16x32_bf16 v[52:55], a[80:83], v[84:87], 0
s_and_b32 s38, s38, 63
buffer_load_dwordx4 v[176:179], v11, s[40:43], s57 offen offset:1536 sc1
v_mfma_f32_16x16x32_bf16 v[60:63], a[160:163], v[84:87], 0
s_cmp_eq_u32 s38, 31
ds_read_b128 v[84:87], v1 offset:3072
s_waitcnt lgkmcnt(4)
v_mfma_f32_16x16x32_bf16 v[40:43], a[4:7], v[88:91], v[40:43]
s_cselect_b32 s38, -31, 1
buffer_load_dwordx4 v[180:183], v11, s[40:43], s57 offen offset:2048 sc1
v_mfma_f32_16x16x32_bf16 v[48:51], a[84:87], v[88:91], v[48:51]
s_mul_i32 s39, s38, 98304
buffer_load_dwordx4 v[184:187], v11, s[40:43], s57 offen offset:2560 sc1
v_mfma_f32_16x16x32_bf16 v[56:59], a[164:167], v[88:91], v[56:59]
s_add_u32 s34, s34, s39
ds_read_b128 v[88:91], v1 offset:23552
s_waitcnt lgkmcnt(4)
v_mfma_f32_16x16x32_bf16 v[44:47], a[4:7], v[92:95], v[44:47]
s_mul_i32 s39, s38, 65536
buffer_load_dword v236, v227, s[40:43], s58 offen sc1
v_cvt_pk_bf16_f32 v104, v16, v17
v_mfma_f32_16x16x32_bf16 v[52:55], a[84:87], v[92:95], v[52:55]
s_add_u32 s36, s36, s39
s_waitcnt vmcnt(20)
v_swap_b32 v150, v152
v_mfma_f32_16x16x32_bf16 v[60:63], a[164:167], v[92:95], v[60:63]
s_mul_i32 s39, s38, 16384
v_cvt_pk_bf16_f32 v105, v18, v19
ds_read_b128 v[92:95], v1 offset:4096
s_waitcnt lgkmcnt(4)
v_mfma_f32_16x16x32_bf16 v[40:43], a[8:11], v[96:99], v[40:43]
s_add_u32 s37, s37, s39
v_swap_b32 v151, v153
v_mfma_f32_16x16x32_bf16 v[48:51], a[88:91], v[96:99], v[48:51]
s_add_u32 s35, s35, 1536
v_swap_b32 v158, v160
v_mfma_f32_16x16x32_bf16 v[56:59], a[168:171], v[96:99], v[56:59]
s_add_u32 s63, s63, 1
v_cvt_pk_bf16_f32 v106, v24, v25
ds_read_b128 v[96:99], v1 offset:24576
s_waitcnt lgkmcnt(4)
v_mfma_f32_16x16x32_bf16 v[44:47], a[8:11], v[80:83], v[44:47]
v_swap_b32 v159, v161
v_mfma_f32_16x16x32_bf16 v[52:55], a[88:91], v[80:83], v[52:55]
v_cvt_pk_bf16_f32 v107, v26, v27
v_mfma_f32_16x16x32_bf16 v[60:63], a[168:171], v[80:83], v[60:63]
ds_read_b128 v[80:83], v1 offset:5120
s_waitcnt lgkmcnt(4)
v_mfma_f32_16x16x32_bf16 v[40:43], a[12:15], v[84:87], v[40:43]
ds_write_b128 v6, v[104:107] offset:0
v_mfma_f32_16x16x32_bf16 v[64:67], v[116:119], v[188:191], v[212:215]
v_cvt_pk_bf16_f32 v108, v20, v21
v_cvt_pk_bf16_f32 v109, v22, v23
v_mfma_f32_16x16x32_bf16 v[48:51], a[92:95], v[84:87], v[48:51]
v_cvt_pk_bf16_f32 v110, v28, v29
v_cvt_pk_bf16_f32 v111, v30, v31
v_mfma_f32_16x16x32_bf16 v[68:71], v[120:123], v[188:191], v[216:219]
ds_write_b128 v6, v[108:111] offset:1024
v_mfma_f32_16x16x32_bf16 v[56:59], a[172:175], v[84:87], v[56:59]
v_cvt_pk_bf16_f32 v112, v32, v33
ds_read_b128 v[84:87], v1 offset:25600
s_waitcnt lgkmcnt(6)
v_mfma_f32_16x16x32_bf16 v[44:47], a[12:15], v[88:91], v[44:47]
v_cvt_pk_bf16_f32 v113, v34, v35
v_cvt_pk_bf16_f32 v114, v36, v37
v_mfma_f32_16x16x32_bf16 v[64:67], v[124:127], v[192:195], v[64:67]
v_cvt_pk_bf16_f32 v115, v38, v39
v_mfma_f32_16x16x32_bf16 v[52:55], a[92:95], v[88:91], v[52:55]
ds_write2st64_b64 v7, v[112:113], v[114:115] offset0:0 offset1:2
v_mfma_f32_16x16x32_bf16 v[68:71], v[128:131], v[192:195], v[68:71]
buffer_load_dword v224, v225, s[40:43], s58 offen sc1
v_mfma_f32_16x16x32_bf16 v[60:63], a[172:175], v[88:91], v[60:63]
ds_read_b128 v[88:91], v1 offset:6144
s_waitcnt lgkmcnt(7)
v_mfma_f32_16x16x32_bf16 v[40:43], a[16:19], v[92:95], v[40:43]
v_mfma_f32_16x16x32_bf16 v[64:67], v[132:135], v[196:199], v[64:67]
v_mfma_f32_16x16x32_bf16 v[48:51], a[96:99], v[92:95], v[48:51]
v_mfma_f32_16x16x32_bf16 v[68:71], v[136:139], v[196:199], v[68:71]
v_mfma_f32_16x16x32_bf16 v[56:59], a[176:179], v[92:95], v[56:59]
ds_read_b128 v[92:95], v1 offset:26624
s_waitcnt lgkmcnt(7)
v_mfma_f32_16x16x32_bf16 v[44:47], a[16:19], v[96:99], v[44:47]
v_mfma_f32_16x16x32_bf16 v[64:67], v[140:143], v[200:203], v[64:67]
v_mfma_f32_16x16x32_bf16 v[52:55], a[96:99], v[96:99], v[52:55]
v_mfma_f32_16x16x32_bf16 v[68:71], v[144:147], v[200:203], v[68:71]
v_mfma_f32_16x16x32_bf16 v[60:63], a[176:179], v[96:99], v[60:63]
ds_read_b128 v[96:99], v1 offset:7168
s_waitcnt lgkmcnt(7)
v_mfma_f32_16x16x32_bf16 v[40:43], a[20:23], v[80:83], v[40:43]
v_mfma_f32_16x16x32_bf16 v[64:67], v[148:151], v[204:207], v[64:67]
v_mfma_f32_16x16x32_bf16 v[48:51], a[100:103], v[80:83], v[48:51]
v_mfma_f32_16x16x32_bf16 v[68:71], v[152:155], v[204:207], v[68:71]
v_mfma_f32_16x16x32_bf16 v[56:59], a[180:183], v[80:83], v[56:59]
ds_read_b128 v[80:83], v1 offset:27648
s_waitcnt lgkmcnt(5)
v_mfma_f32_16x16x32_bf16 v[44:47], a[20:23], v[84:87], v[44:47]
v_mfma_f32_16x16x32_bf16 v[64:67], v[156:159], v[208:211], v[64:67]
v_mfma_f32_16x16x32_bf16 v[52:55], a[100:103], v[84:87], v[52:55]
v_mfma_f32_16x16x32_bf16 v[68:71], v[160:163], v[208:211], v[68:71]
v_mfma_f32_16x16x32_bf16 v[60:63], a[180:183], v[84:87], v[60:63]
ds_read_b128 v[84:87], v1 offset:8192
s_waitcnt lgkmcnt(4)
v_mfma_f32_16x16x32_bf16 v[40:43], a[24:27], v[88:91], v[40:43]
v_mfma_f32_16x16x32_bf16 v[48:51], a[104:107], v[88:91], v[48:51]
v_mfma_f32_16x16x32_bf16 v[56:59], a[184:187], v[88:91], v[56:59]
ds_read_b128 v[88:91], v1 offset:28672
s_waitcnt lgkmcnt(4)
v_mfma_f32_16x16x32_bf16 v[44:47], a[24:27], v[92:95], v[44:47]
buffer_load_dword v9, v10, s[40:43], s58 offen sc1
s_waitcnt vmcnt(8)
v_mfma_f32_16x16x32_bf16 v[72:75], v[164:167], v[188:191], v[220:223]
v_mfma_f32_16x16x32_bf16 v[52:55], a[104:107], v[92:95], v[52:55]
s_waitcnt vmcnt(7)
v_mfma_f32_16x16x32_bf16 v[72:75], v[168:171], v[192:195], v[72:75]
v_mfma_f32_16x16x32_bf16 v[60:63], a[184:187], v[92:95], v[60:63]
ds_read_b128 v[92:95], v1 offset:9216
s_waitcnt lgkmcnt(4)
v_mfma_f32_16x16x32_bf16 v[40:43], a[28:31], v[96:99], v[40:43]
buffer_load_dwordx4 v[116:119], v0, s[40:43], s54 offen sc1
s_waitcnt vmcnt(7)
v_mfma_f32_16x16x32_bf16 v[72:75], v[172:175], v[196:199], v[72:75]
v_mfma_f32_16x16x32_bf16 v[48:51], a[108:111], v[96:99], v[48:51]
s_waitcnt vmcnt(6)
v_mfma_f32_16x16x32_bf16 v[72:75], v[176:179], v[200:203], v[72:75]
v_mfma_f32_16x16x32_bf16 v[56:59], a[188:191], v[96:99], v[56:59]
ds_read_b128 v[96:99], v1 offset:29696
s_waitcnt lgkmcnt(4)
v_mfma_f32_16x16x32_bf16 v[44:47], a[28:31], v[80:83], v[44:47]
buffer_load_dwordx4 v[120:123], v0, s[40:43], s54 offen offset:1024 sc1
s_waitcnt vmcnt(6)
v_mfma_f32_16x16x32_bf16 v[72:75], v[180:183], v[204:207], v[72:75]
v_mfma_f32_16x16x32_bf16 v[52:55], a[108:111], v[80:83], v[52:55]
s_waitcnt vmcnt(5)
v_mfma_f32_16x16x32_bf16 v[72:75], v[184:187], v[208:211], v[72:75]
v_mfma_f32_16x16x32_bf16 v[60:63], a[188:191], v[80:83], v[60:63]
ds_read_b128 v[80:83], v1 offset:10240
s_waitcnt lgkmcnt(4)
v_mfma_f32_16x16x32_bf16 v[40:43], a[32:35], v[84:87], v[40:43]
buffer_load_dwordx4 v[124:127], v0, s[40:43], s54 offen offset:3072 sc1
v_mfma_f32_16x16x32_bf16 v[48:51], a[112:115], v[84:87], v[48:51]
v_mfma_f32_16x16x32_bf16 v[56:59], a[192:195], v[84:87], v[56:59]
ds_read_b128 v[84:87], v1 offset:30720
s_waitcnt lgkmcnt(4)
v_mfma_f32_16x16x32_bf16 v[44:47], a[32:35], v[88:91], v[44:47]
v_max3_f32 v76, v64, v65, v66
)"
R"(v_max3_f32 v76, v76, v67, v68
v_mfma_f32_16x16x32_bf16 v[52:55], a[112:115], v[88:91], v[52:55]
v_max3_f32 v76, v76, v69, v70
v_max3_f32 v76, v76, v71, v72
v_mfma_f32_16x16x32_bf16 v[60:63], a[192:195], v[88:91], v[60:63]
v_max3_f32 v76, v76, v73, v74
ds_read_b128 v[88:91], v1 offset:11264
s_waitcnt lgkmcnt(4)
v_mfma_f32_16x16x32_bf16 v[40:43], a[36:39], v[92:95], v[40:43]
buffer_load_dwordx4 v[128:131], v0, s[40:43], s55 offen sc1
v_max_f32 v76, v76, v75
v_mfma_f32_16x16x32_bf16 v[48:51], a[116:119], v[92:95], v[48:51]
v_mov_b32 v77, v76
v_mfma_f32_16x16x32_bf16 v[56:59], a[196:199], v[92:95], v[56:59]
ds_read_b128 v[92:95], v1 offset:31744
s_waitcnt lgkmcnt(4)
v_mfma_f32_16x16x32_bf16 v[44:47], a[36:39], v[96:99], v[44:47]
v_permlane16_swap_b32 v76, v77
v_mfma_f32_16x16x32_bf16 v[52:55], a[116:119], v[96:99], v[52:55]
v_max_f32 v76, v76, v77
v_mov_b32 v77, v76
v_mfma_f32_16x16x32_bf16 v[60:63], a[196:199], v[96:99], v[60:63]
buffer_load_dwordx4 v[132:135], v0, s[40:43], s55 offen offset:2048 sc1
ds_read_b128 v[96:99], v1 offset:12288
s_waitcnt lgkmcnt(4)
v_mfma_f32_16x16x32_bf16 v[40:43], a[40:43], v[80:83], v[40:43]
buffer_load_dwordx4 v[136:139], v0, s[40:43], s55 offen offset:3072 sc1
v_mfma_f32_16x16x32_bf16 v[48:51], a[120:123], v[80:83], v[48:51]
v_permlane32_swap_b32 v76, v77
v_mfma_f32_16x16x32_bf16 v[56:59], a[200:203], v[80:83], v[56:59]
v_max_f32 v76, v76, v77
ds_read_b128 v[80:83], v1 offset:32768
s_waitcnt lgkmcnt(4)
v_mfma_f32_16x16x32_bf16 v[44:47], a[40:43], v[84:87], v[44:47]
v_mul_f32 v76, 0xbdd53b95, v76
v_fmamk_f32 v64, v64, 0x3dd53b95, v76
v_mfma_f32_16x16x32_bf16 v[52:55], a[120:123], v[84:87], v[52:55]
v_fmamk_f32 v65, v65, 0x3dd53b95, v76
v_fmamk_f32 v66, v66, 0x3dd53b95, v76
v_mfma_f32_16x16x32_bf16 v[60:63], a[200:203], v[84:87], v[60:63]
v_fmamk_f32 v67, v67, 0x3dd53b95, v76
ds_read_b128 v[84:87], v1 offset:13312
s_waitcnt lgkmcnt(4)
v_mfma_f32_16x16x32_bf16 v[40:43], a[44:47], v[88:91], v[40:43]
buffer_load_dwordx4 v[140:143], v0, s[40:43], s56 offen offset:1024 sc1
v_fmamk_f32 v68, v68, 0x3dd53b95, v76
v_mfma_f32_16x16x32_bf16 v[48:51], a[124:127], v[88:91], v[48:51]
v_fmamk_f32 v69, v69, 0x3dd53b95, v76
v_fmamk_f32 v70, v70, 0x3dd53b95, v76
v_mfma_f32_16x16x32_bf16 v[56:59], a[204:207], v[88:91], v[56:59]
v_fmamk_f32 v71, v71, 0x3dd53b95, v76
ds_read_b128 v[88:91], v1 offset:33792
s_waitcnt lgkmcnt(4)
v_mfma_f32_16x16x32_bf16 v[44:47], a[44:47], v[92:95], v[44:47]
v_fmamk_f32 v72, v72, 0x3dd53b95, v76
v_fmamk_f32 v73, v73, 0x3dd53b95, v76
v_mfma_f32_16x16x32_bf16 v[52:55], a[124:127], v[92:95], v[52:55]
v_fmamk_f32 v74, v74, 0x3dd53b95, v76
v_fmamk_f32 v75, v75, 0x3dd53b95, v76
v_mfma_f32_16x16x32_bf16 v[60:63], a[204:207], v[92:95], v[60:63]
buffer_load_dwordx4 v[144:147], v0, s[40:43], s56 offen offset:2048 sc1
ds_read_b128 v[92:95], v1 offset:14336
s_waitcnt lgkmcnt(4)
v_mfma_f32_16x16x32_bf16 v[40:43], a[48:51], v[96:99], v[40:43]
buffer_load_dwordx4 v[148:151], v0, s[40:43], s54 offen offset:2048 sc1
v_mfma_f32_16x16x32_bf16 v[48:51], a[128:131], v[96:99], v[48:51]
v_exp_f32 v64, v64
v_mfma_f32_16x16x32_bf16 v[56:59], a[208:211], v[96:99], v[56:59]
ds_read_b128 v[96:99], v1 offset:34816
s_waitcnt lgkmcnt(4)
v_mfma_f32_16x16x32_bf16 v[44:47], a[48:51], v[80:83], v[44:47]
v_exp_f32 v65, v65
v_mfma_f32_16x16x32_bf16 v[52:55], a[128:131], v[80:83], v[52:55]
v_exp_f32 v66, v66
v_mfma_f32_16x16x32_bf16 v[60:63], a[208:211], v[80:83], v[60:63]
buffer_load_dwordx4 v[152:155], v0, s[40:43], s55 offen offset:1024 sc1
ds_read_b128 v[80:83], v1 offset:15360
s_waitcnt lgkmcnt(4)
v_mfma_f32_16x16x32_bf16 v[40:43], a[52:55], v[84:87], v[40:43]
buffer_load_dwordx4 v[156:159], v0, s[40:43], s56 offen sc1
v_mfma_f32_16x16x32_bf16 v[48:51], a[132:135], v[84:87], v[48:51]
v_exp_f32 v67, v67
v_mfma_f32_16x16x32_bf16 v[56:59], a[212:215], v[84:87], v[56:59]
s_add_u32 s38, s63, 0
ds_read_b128 v[84:87], v1 offset:35840
s_waitcnt lgkmcnt(4)
v_mfma_f32_16x16x32_bf16 v[44:47], a[52:55], v[88:91], v[44:47]
s_and_b32 s38, s38, 63
v_exp_f32 v68, v68
v_mfma_f32_16x16x32_bf16 v[52:55], a[132:135], v[88:91], v[52:55]
s_cmp_eq_u32 s38, 31
v_exp_f32 v69, v69
v_mfma_f32_16x16x32_bf16 v[60:63], a[212:215], v[88:91], v[60:63]
s_cselect_b32 s38, -31, 1
buffer_load_dwordx4 v[160:163], v0, s[40:43], s56 offen offset:3072 sc1
ds_read_b128 v[88:91], v1 offset:16384
s_waitcnt lgkmcnt(4)
v_mfma_f32_16x16x32_bf16 v[40:43], a[56:59], v[92:95], v[40:43]
s_mul_i32 s39, s38, 393216
v_exp_f32 v70, v70
v_mfma_f32_16x16x32_bf16 v[48:51], a[136:139], v[92:95], v[48:51]
s_add_u32 s54, s54, s39
v_exp_f32 v71, v71
v_mfma_f32_16x16x32_bf16 v[56:59], a[216:219], v[92:95], v[56:59]
s_add_u32 s55, s55, s39
ds_read_b128 v[92:95], v1 offset:36864
s_waitcnt lgkmcnt(4)
v_mfma_f32_16x16x32_bf16 v[44:47], a[56:59], v[96:99], v[44:47]
s_add_u32 s56, s56, s39
v_exp_f32 v72, v72
v_mfma_f32_16x16x32_bf16 v[52:55], a[136:139], v[96:99], v[52:55]
v_exp_f32 v73, v73
v_mfma_f32_16x16x32_bf16 v[60:63], a[216:219], v[96:99], v[60:63]
s_waitcnt vmcnt(13) lgkmcnt(4)
s_barrier
buffer_atomic_add v226, v3, s[40:43], s29 offen
s_and_b32 s48, s47, 63
s_cmp_lt_u32 s48, 62
s_cselect_b32 s48, 2, 0
v_cmp_gt_u32 vcc, s48, v224
s_cbranch_vccz hpq_xf1_ok_%=
s_mov_b32 s60, 0
s_nop 2
hpq_xf1_poll_%=:
buffer_load_dword v224, v225, s[40:43], s58 offen sc1
s_waitcnt vmcnt(0)
v_cmp_gt_u32 vcc, s48, v224
s_nop 4
s_cbranch_vccz hpq_xf1_ok_%=
s_sleep 2
s_add_u32 s60, s60, 1
s_cmp_gt_u32 s60, 65536
s_cbranch_scc1 hp_abort_%=
s_and_b32 s59, s60, 63
s_cbranch_scc1 hpq_xf1_poll_%=
v_mov_b32 v4, 1024
buffer_load_dword v4, v4, s[40:43], s58 offen sc1
s_waitcnt vmcnt(0)
v_readfirstlane_b32 s59, v4
s_nop 0
s_cmp_lg_u32 s59, 0
s_nop 2
s_cbranch_scc1 hp_end_%=
s_branch hpq_xf1_poll_%=
hpq_xf1_ok_%=:
v_add_u32 v225, 128, v225
ds_read_b128 v[96:99], v1 offset:17408
s_waitcnt lgkmcnt(4)
v_mfma_f32_16x16x32_bf16 v[40:43], a[60:63], v[80:83], v[40:43]
s_add_u32 m0, s61, 0
s_add_u32 s51, s50, 0
buffer_load_dwordx4 v0, s[40:43], s51 offen offset:0 sc1 lds
v_mfma_f32_16x16x32_bf16 v[48:51], a[140:143], v[80:83], v[48:51]
v_exp_f32 v74, v74
v_mfma_f32_16x16x32_bf16 v[56:59], a[220:223], v[80:83], v[56:59]
ds_read_b128 v[188:191], v8 offset:0
ds_read_b128 v[80:83], v1 offset:37888
s_waitcnt lgkmcnt(5)
v_mfma_f32_16x16x32_bf16 v[44:47], a[60:63], v[84:87], v[44:47]
buffer_load_dwordx4 v0, s[40:43], s51 offen offset:1024 sc1 lds
v_mfma_f32_16x16x32_bf16 v[52:55], a[140:143], v[84:87], v[52:55]
v_exp_f32 v75, v75
v_mfma_f32_16x16x32_bf16 v[60:63], a[220:223], v[84:87], v[60:63]
v_add_f32 v76, v64, v65
ds_read_b128 v[84:87], v1 offset:18432
s_waitcnt lgkmcnt(5)
v_mfma_f32_16x16x32_bf16 v[40:43], a[64:67], v[88:91], v[40:43]
buffer_load_dwordx4 v0, s[40:43], s51 offen offset:2048 sc1 lds
v_mfma_f32_16x16x32_bf16 v[48:51], a[144:147], v[88:91], v[48:51]
v_add_f32 v76, v76, v66
v_add_f32 v76, v76, v67
v_mfma_f32_16x16x32_bf16 v[56:59], a[224:227], v[88:91], v[56:59]
v_add_f32 v76, v76, v68
ds_read_b128 v[88:91], v1 offset:38912
s_waitcnt lgkmcnt(5)
v_mfma_f32_16x16x32_bf16 v[44:47], a[64:67], v[92:95], v[44:47]
buffer_load_dwordx4 v0, s[40:43], s51 offen offset:3072 sc1 lds
v_mfma_f32_16x16x32_bf16 v[52:55], a[144:147], v[92:95], v[52:55]
v_add_f32 v76, v76, v69
v_add_f32 v76, v76, v70
v_mfma_f32_16x16x32_bf16 v[60:63], a[224:227], v[92:95], v[60:63]
v_add_f32 v76, v76, v71
ds_read_b128 v[92:95], v1 offset:19456
s_waitcnt lgkmcnt(5)
v_mfma_f32_16x16x32_bf16 v[40:43], a[68:71], v[96:99], v[40:43]
s_add_u32 m0, s61, 4096
s_add_u32 s51, s50, 4096
buffer_load_dwordx4 v0, s[40:43], s51 offen offset:0 sc1 lds
v_mfma_f32_16x16x32_bf16 v[48:51], a[148:151], v[96:99], v[48:51]
v_add_f32 v76, v76, v72
v_add_f32 v76, v76, v73
v_mfma_f32_16x16x32_bf16 v[56:59], a[228:231], v[96:99], v[56:59]
v_add_f32 v76, v76, v74
ds_read_b128 v[192:195], v8 offset:2048
ds_read_b128 v[196:199], v8 offset:4096
ds_read_b128 v[200:203], v8 offset:6144
ds_read_b128 v[204:207], v8 offset:8192
ds_read_b128 v[208:211], v8 offset:10240
ds_read_b128 v[96:99], v1 offset:39936
s_waitcnt lgkmcnt(9)
)"
R"(v_mfma_f32_16x16x32_bf16 v[44:47], a[68:71], v[80:83], v[44:47]
buffer_load_dwordx4 v0, s[40:43], s51 offen offset:1024 sc1 lds
v_mfma_f32_16x16x32_bf16 v[52:55], a[148:151], v[80:83], v[52:55]
v_add_f32 v76, v76, v75
v_mov_b32 v77, v76
v_mfma_f32_16x16x32_bf16 v[60:63], a[228:231], v[80:83], v[60:63]
s_add_u32 s64, s64, 40960
s_cmp_ge_u32 s64, 122880
s_cselect_b32 s64, 0, s64
v_add_u32 v1, s64, v0
ds_read_b128 v[80:83], v1 offset:0
s_waitcnt lgkmcnt(9)
v_mfma_f32_16x16x32_bf16 v[40:43], a[72:75], v[84:87], v[40:43]
buffer_load_dwordx4 v0, s[40:43], s51 offen offset:2048 sc1 lds
v_mfma_f32_16x16x32_bf16 v[48:51], a[152:155], v[84:87], v[48:51]
v_permlane16_swap_b32 v76, v77
v_mfma_f32_16x16x32_bf16 v[56:59], a[232:235], v[84:87], v[56:59]
v_add_f32 v76, v76, v77
ds_read_b128 v[84:87], v1 offset:20480
s_waitcnt lgkmcnt(9)
v_mfma_f32_16x16x32_bf16 v[44:47], a[72:75], v[88:91], v[44:47]
buffer_load_dwordx4 v0, s[40:43], s51 offen offset:3072 sc1 lds
v_mfma_f32_16x16x32_bf16 v[52:55], a[152:155], v[88:91], v[52:55]
v_mov_b32 v77, v76
v_mfma_f32_16x16x32_bf16 v[60:63], a[232:235], v[88:91], v[60:63]
ds_read_b128 v[88:91], v1 offset:1024
s_waitcnt lgkmcnt(9)
v_mfma_f32_16x16x32_bf16 v[40:43], a[76:79], v[92:95], v[40:43]
s_add_u32 m0, s61, 8192
s_add_u32 s51, s50, 8192
buffer_load_dwordx4 v0, s[40:43], s51 offen offset:0 sc1 lds
v_mfma_f32_16x16x32_bf16 v[48:51], a[156:159], v[92:95], v[48:51]
v_permlane32_swap_b32 v76, v77
v_mfma_f32_16x16x32_bf16 v[56:59], a[236:239], v[92:95], v[56:59]
v_add_f32 v76, v76, v77
ds_read_b128 v[92:95], v1 offset:21504
s_waitcnt lgkmcnt(4)
v_mfma_f32_16x16x32_bf16 v[44:47], a[76:79], v[96:99], v[44:47]
buffer_load_dwordx4 v0, s[40:43], s51 offen offset:1024 sc1 lds
v_mfma_f32_16x16x32_bf16 v[52:55], a[156:159], v[96:99], v[52:55]
s_add_u32 s50, s50, 40960
s_add_u32 s61, s61, 40960
s_cmp_ge_u32 s61, 122880
s_cselect_b32 s51, 122880, 0
s_sub_u32 s61, s61, s51
v_rcp_f32 v76, v76
v_mfma_f32_16x16x32_bf16 v[60:63], a[236:239], v[96:99], v[60:63]
v_mul_f32 v64, v64, v76
ds_read_b128 v[96:99], v1 offset:2048
v_mul_f32 v65, v65, v76
v_mul_f32 v66, v66, v76
v_mul_f32 v67, v67, v76
v_mul_f32 v68, v68, v76
v_mul_f32 v69, v69, v76
v_mul_f32 v70, v70, v76
v_mul_f32 v71, v71, v76
v_mul_f32 v72, v72, v76
v_mul_f32 v73, v73, v76
v_mul_f32 v74, v74, v76
v_mul_f32 v75, v75, v76
v_cvt_pk_bf16_f32 v228, v64, v65
v_cvt_pk_bf16_f32 v229, v66, v67
v_cvt_pk_bf16_f32 v230, v68, v69
v_cvt_pk_bf16_f32 v231, v70, v71
v_cvt_pk_bf16_f32 v232, v72, v73
v_cvt_pk_bf16_f32 v233, v74, v75
v_mov_b32 v234, v232
v_mov_b32 v235, v233
buffer_store_dwordx4 v[228:231], v0, s[40:43], s26 offen sc1
v_permlane16_swap_b32 v232, v234
v_permlane16_swap_b32 v233, v235
buffer_store_dwordx4 v[232:235], v12, s[40:43], s27 offen sc1
s_add_u32 s47, s47, 1
s_and_b32 s59, s47, 63
s_cbranch_scc1 hpq_loop_%=
s_waitcnt lgkmcnt(0)
v_cvt_pk_bf16_f32 v104, v40, v41
v_cvt_pk_bf16_f32 v105, v42, v43
v_cvt_pk_bf16_f32 v106, v48, v49
v_cvt_pk_bf16_f32 v107, v50, v51
ds_write_b128 v6, v[104:107] offset:12288
v_cvt_pk_bf16_f32 v108, v44, v45
v_cvt_pk_bf16_f32 v109, v46, v47
v_cvt_pk_bf16_f32 v110, v52, v53
v_cvt_pk_bf16_f32 v111, v54, v55
ds_write_b128 v6, v[108:111] offset:13312
v_cvt_pk_bf16_f32 v112, v56, v57
v_cvt_pk_bf16_f32 v113, v58, v59
v_cvt_pk_bf16_f32 v114, v60, v61
v_cvt_pk_bf16_f32 v115, v62, v63
ds_write2st64_b64 v7, v[112:113], v[114:115] offset0:24 offset1:26
s_waitcnt lgkmcnt(0)
s_barrier
s_waitcnt vmcnt(13)
s_mov_b32 s29, s52
s_waitcnt vmcnt(25)
s_add_u32 s48, s63, 1
s_cmp_ge_i32 s63, s65
s_cselect_b32 s48, s48, 0
s_cselect_b32 s49, s30, 0x40000000
v_cmp_gt_u32 vcc, s48, v9
s_cbranch_vccz hpq_kme0_ok_%=
s_waitcnt vmcnt(0)
buffer_atomic_add v226, v3, s[40:43], s29 offen
s_mov_b32 s29, 0x40000000
v_cmp_gt_u32 vcc, s48, v9
s_cbranch_vccz hpq_kme0_ok_%=
s_mov_b32 s60, 0
s_nop 2
hpq_kme0_poll_%=:
buffer_load_dword v9, v10, s[40:43], s58 offen sc1
s_waitcnt vmcnt(0)
v_cmp_gt_u32 vcc, s48, v9
s_nop 4
s_cbranch_vccz hpq_kme0_ok_%=
s_sleep 2
s_add_u32 s60, s60, 1
s_cmp_gt_u32 s60, 65536
s_cbranch_scc1 hp_abort_%=
s_and_b32 s59, s60, 63
s_cbranch_scc1 hpq_kme0_poll_%=
v_mov_b32 v4, 1024
buffer_load_dword v4, v4, s[40:43], s58 offen sc1
s_waitcnt vmcnt(0)
v_readfirstlane_b32 s59, v4
s_nop 0
s_cmp_lg_u32 s59, 0
s_nop 2
s_cbranch_scc1 hp_end_%=
s_branch hpq_kme0_poll_%=
hpq_kme0_ok_%=:
s_movk_i32 s48, 32
v_cmp_gt_u32 vcc, s48, v236
s_cbranch_vccz hpq_kxe0_ok_%=
s_mov_b32 s60, 0
s_nop 2
hpq_kxe0_poll_%=:
buffer_load_dword v236, v227, s[40:43], s58 offen sc1
s_waitcnt vmcnt(0)
v_cmp_gt_u32 vcc, s48, v236
s_nop 4
s_cbranch_vccz hpq_kxe0_ok_%=
s_sleep 2
s_add_u32 s60, s60, 1
s_cmp_gt_u32 s60, 65536
s_cbranch_scc1 hp_abort_%=
s_and_b32 s59, s60, 63
s_cbranch_scc1 hpq_kxe0_poll_%=
v_mov_b32 v4, 1024
buffer_load_dword v4, v4, s[40:43], s58 offen sc1
s_waitcnt vmcnt(0)
v_readfirstlane_b32 s59, v4
s_nop 0
s_cmp_lg_u32 s59, 0
s_nop 2
s_cbranch_scc1 hp_end_%=
s_branch hpq_kxe0_poll_%=
hpq_kxe0_ok_%=:
v_add_u32 v227, 128, v227
s_or_b32 s57, s34, s49
buffer_load_dwordx4 v[164:167], v11, s[40:43], s57 offen sc1
buffer_load_dwordx4 v[168:171], v11, s[40:43], s57 offen offset:512 sc1
buffer_load_dwordx4 v[172:175], v11, s[40:43], s57 offen offset:1024 sc1
buffer_load_dwordx4 v[176:179], v11, s[40:43], s57 offen offset:1536 sc1
buffer_load_dwordx4 v[180:183], v11, s[40:43], s57 offen offset:2048 sc1
buffer_load_dwordx4 v[184:187], v11, s[40:43], s57 offen offset:2560 sc1
s_or_b32 s52, s35, s49
s_or_b32 s26, s36, s49
s_or_b32 s27, s37, s49
s_add_u32 s38, s63, 0
s_and_b32 s38, s38, 63
s_cmp_eq_u32 s38, 31
s_cselect_b32 s38, -31, 1
s_mul_i32 s39, s38, 98304
s_add_u32 s34, s34, s39
s_mul_i32 s39, s38, 65536
s_add_u32 s36, s36, s39
s_mul_i32 s39, s38, 16384
s_add_u32 s37, s37, s39
s_add_u32 s35, s35, 1536
s_add_u32 s63, s63, 1
v_swap_b32 v150, v152
v_swap_b32 v151, v153
v_swap_b32 v158, v160
v_swap_b32 v159, v161
v_mfma_f32_16x16x32_bf16 v[64:67], v[116:119], v[188:191], v[212:215]
v_mfma_f32_16x16x32_bf16 v[68:71], v[120:123], v[188:191], v[216:219]
v_mfma_f32_16x16x32_bf16 v[64:67], v[124:127], v[192:195], v[64:67]
v_mfma_f32_16x16x32_bf16 v[68:71], v[128:131], v[192:195], v[68:71]
v_mfma_f32_16x16x32_bf16 v[64:67], v[132:135], v[196:199], v[64:67]
v_mfma_f32_16x16x32_bf16 v[68:71], v[136:139], v[196:199], v[68:71]
v_mfma_f32_16x16x32_bf16 v[64:67], v[140:143], v[200:203], v[64:67]
v_mfma_f32_16x16x32_bf16 v[68:71], v[144:147], v[200:203], v[68:71]
v_mfma_f32_16x16x32_bf16 v[64:67], v[148:151], v[204:207], v[64:67]
v_mfma_f32_16x16x32_bf16 v[68:71], v[152:155], v[204:207], v[68:71]
v_mfma_f32_16x16x32_bf16 v[64:67], v[156:159], v[208:211], v[64:67]
v_mfma_f32_16x16x32_bf16 v[68:71], v[160:163], v[208:211], v[68:71]
s_waitcnt vmcnt(5)
v_mfma_f32_16x16x32_bf16 v[72:75], v[164:167], v[188:191], v[220:223]
s_waitcnt vmcnt(4)
)"
R"(v_mfma_f32_16x16x32_bf16 v[72:75], v[168:171], v[192:195], v[72:75]
s_waitcnt vmcnt(3)
v_mfma_f32_16x16x32_bf16 v[72:75], v[172:175], v[196:199], v[72:75]
s_waitcnt vmcnt(2)
v_mfma_f32_16x16x32_bf16 v[72:75], v[176:179], v[200:203], v[72:75]
s_waitcnt vmcnt(1)
v_mfma_f32_16x16x32_bf16 v[72:75], v[180:183], v[204:207], v[72:75]
s_waitcnt vmcnt(0)
v_mfma_f32_16x16x32_bf16 v[72:75], v[184:187], v[208:211], v[72:75]
buffer_load_dwordx4 v[116:119], v0, s[40:43], s54 offen sc1
buffer_load_dwordx4 v[120:123], v0, s[40:43], s54 offen offset:1024 sc1
buffer_load_dwordx4 v[124:127], v0, s[40:43], s54 offen offset:3072 sc1
buffer_load_dwordx4 v[128:131], v0, s[40:43], s55 offen sc1
buffer_load_dwordx4 v[132:135], v0, s[40:43], s55 offen offset:2048 sc1
buffer_load_dwordx4 v[136:139], v0, s[40:43], s55 offen offset:3072 sc1
buffer_load_dwordx4 v[140:143], v0, s[40:43], s56 offen offset:1024 sc1
buffer_load_dwordx4 v[144:147], v0, s[40:43], s56 offen offset:2048 sc1
buffer_load_dwordx4 v[148:151], v0, s[40:43], s54 offen offset:2048 sc1
buffer_load_dwordx4 v[152:155], v0, s[40:43], s55 offen offset:1024 sc1
buffer_load_dwordx4 v[156:159], v0, s[40:43], s56 offen sc1
buffer_load_dwordx4 v[160:163], v0, s[40:43], s56 offen offset:3072 sc1
s_add_u32 s38, s63, 0
s_and_b32 s38, s38, 63
s_cmp_eq_u32 s38, 31
s_cselect_b32 s38, -31, 1
s_mul_i32 s39, s38, 393216
s_add_u32 s54, s54, s39
s_add_u32 s55, s55, s39
s_add_u32 s56, s56, s39
v_max3_f32 v76, v64, v65, v66
v_max3_f32 v76, v76, v67, v68
v_max3_f32 v76, v76, v69, v70
v_max3_f32 v76, v76, v71, v72
v_max3_f32 v76, v76, v73, v74
v_max_f32 v76, v76, v75
v_mov_b32 v77, v76
s_nop 1
v_permlane16_swap_b32 v76, v77
v_max_f32 v76, v76, v77
v_mov_b32 v77, v76
s_nop 1
v_permlane32_swap_b32 v76, v77
v_max_f32 v76, v76, v77
v_mul_f32 v76, 0xbdd53b95, v76
v_fmamk_f32 v64, v64, 0x3dd53b95, v76
v_fmamk_f32 v65, v65, 0x3dd53b95, v76
v_fmamk_f32 v66, v66, 0x3dd53b95, v76
v_fmamk_f32 v67, v67, 0x3dd53b95, v76
v_fmamk_f32 v68, v68, 0x3dd53b95, v76
v_fmamk_f32 v69, v69, 0x3dd53b95, v76
v_fmamk_f32 v70, v70, 0x3dd53b95, v76
v_fmamk_f32 v71, v71, 0x3dd53b95, v76
v_fmamk_f32 v72, v72, 0x3dd53b95, v76
v_fmamk_f32 v73, v73, 0x3dd53b95, v76
v_fmamk_f32 v74, v74, 0x3dd53b95, v76
v_fmamk_f32 v75, v75, 0x3dd53b95, v76
v_exp_f32 v64, v64
v_exp_f32 v65, v65
v_exp_f32 v66, v66
v_exp_f32 v67, v67
v_exp_f32 v68, v68
v_exp_f32 v69, v69
v_exp_f32 v70, v70
v_exp_f32 v71, v71
v_exp_f32 v72, v72
v_exp_f32 v73, v73
v_exp_f32 v74, v74
v_exp_f32 v75, v75
v_add_f32 v76, v64, v65
v_add_f32 v76, v76, v66
v_add_f32 v76, v76, v67
v_add_f32 v76, v76, v68
v_add_f32 v76, v76, v69
v_add_f32 v76, v76, v70
v_add_f32 v76, v76, v71
v_add_f32 v76, v76, v72
v_add_f32 v76, v76, v73
v_add_f32 v76, v76, v74
v_add_f32 v76, v76, v75
v_mov_b32 v77, v76
s_nop 1
v_permlane16_swap_b32 v76, v77
v_add_f32 v76, v76, v77
v_mov_b32 v77, v76
s_nop 1
v_permlane32_swap_b32 v76, v77
v_add_f32 v76, v76, v77
v_rcp_f32 v76, v76
s_nop 0
v_mul_f32 v64, v64, v76
v_mul_f32 v65, v65, v76
v_mul_f32 v66, v66, v76
v_mul_f32 v67, v67, v76
v_mul_f32 v68, v68, v76
v_mul_f32 v69, v69, v76
v_mul_f32 v70, v70, v76
v_mul_f32 v71, v71, v76
v_mul_f32 v72, v72, v76
v_mul_f32 v73, v73, v76
v_mul_f32 v74, v74, v76
v_mul_f32 v75, v75, v76
v_cvt_pk_bf16_f32 v228, v64, v65
v_cvt_pk_bf16_f32 v229, v66, v67
v_cvt_pk_bf16_f32 v230, v68, v69
v_cvt_pk_bf16_f32 v231, v70, v71
v_cvt_pk_bf16_f32 v232, v72, v73
v_cvt_pk_bf16_f32 v233, v74, v75
v_mov_b32 v234, v232
v_mov_b32 v235, v233
buffer_store_dwordx4 v[228:231], v0, s[40:43], s26 offen sc1
v_permlane16_swap_b32 v232, v234
v_permlane16_swap_b32 v233, v235
buffer_store_dwordx4 v[232:235], v12, s[40:43], s27 offen sc1
s_waitcnt vmcnt(2)
buffer_atomic_add v226, v3, s[40:43], s29 offen
s_mov_b32 s29, s52
s_waitcnt vmcnt(25)
s_add_u32 s48, s63, 1
s_cmp_ge_i32 s63, s65
s_cselect_b32 s48, s48, 0
s_cselect_b32 s49, s30, 0x40000000
v_cmp_gt_u32 vcc, s48, v9
s_cbranch_vccz hpq_kme1_ok_%=
s_waitcnt vmcnt(0)
buffer_atomic_add v226, v3, s[40:43], s29 offen
s_mov_b32 s29, 0x40000000
v_cmp_gt_u32 vcc, s48, v9
s_cbranch_vccz hpq_kme1_ok_%=
s_mov_b32 s60, 0
s_nop 2
hpq_kme1_poll_%=:
buffer_load_dword v9, v10, s[40:43], s58 offen sc1
s_waitcnt vmcnt(0)
v_cmp_gt_u32 vcc, s48, v9
s_nop 4
s_cbranch_vccz hpq_kme1_ok_%=
s_sleep 2
s_add_u32 s60, s60, 1
s_cmp_gt_u32 s60, 65536
s_cbranch_scc1 hp_abort_%=
s_and_b32 s59, s60, 63
s_cbranch_scc1 hpq_kme1_poll_%=
v_mov_b32 v4, 1024
buffer_load_dword v4, v4, s[40:43], s58 offen sc1
s_waitcnt vmcnt(0)
v_readfirstlane_b32 s59, v4
s_nop 0
s_cmp_lg_u32 s59, 0
s_nop 2
s_cbranch_scc1 hp_end_%=
s_branch hpq_kme1_poll_%=
hpq_kme1_ok_%=:
s_or_b32 s57, s34, s49
buffer_load_dwordx4 v[164:167], v11, s[40:43], s57 offen sc1
buffer_load_dwordx4 v[168:171], v11, s[40:43], s57 offen offset:512 sc1
buffer_load_dwordx4 v[172:175], v11, s[40:43], s57 offen offset:1024 sc1
buffer_load_dwordx4 v[176:179], v11, s[40:43], s57 offen offset:1536 sc1
buffer_load_dwordx4 v[180:183], v11, s[40:43], s57 offen offset:2048 sc1
buffer_load_dwordx4 v[184:187], v11, s[40:43], s57 offen offset:2560 sc1
s_or_b32 s52, s35, s49
s_or_b32 s26, s36, s49
s_or_b32 s27, s37, s49
s_add_u32 s38, s63, 0
s_and_b32 s38, s38, 63
s_cmp_eq_u32 s38, 31
s_cselect_b32 s38, -31, 1
s_mul_i32 s39, s38, 98304
s_add_u32 s34, s34, s39
s_mul_i32 s39, s38, 65536
s_add_u32 s36, s36, s39
s_mul_i32 s39, s38, 16384
s_add_u32 s37, s37, s39
s_add_u32 s35, s35, 1536
s_add_u32 s63, s63, 1
ds_read_b128 v[188:191], v8 offset:12288
ds_read_b128 v[192:195], v8 offset:14336
ds_read_b128 v[196:199], v8 offset:16384
ds_read_b128 v[200:203], v8 offset:18432
ds_read_b128 v[204:207], v8 offset:20480
ds_read_b128 v[208:211], v8 offset:22528
v_swap_b32 v150, v152
v_swap_b32 v151, v153
v_swap_b32 v158, v160
v_swap_b32 v159, v161
s_waitcnt lgkmcnt(5)
v_mfma_f32_16x16x32_bf16 v[64:67], v[116:119], v[188:191], v[212:215]
v_mfma_f32_16x16x32_bf16 v[68:71], v[120:123], v[188:191], v[216:219]
s_waitcnt lgkmcnt(4)
v_mfma_f32_16x16x32_bf16 v[64:67], v[124:127], v[192:195], v[64:67]
v_mfma_f32_16x16x32_bf16 v[68:71], v[128:131], v[192:195], v[68:71]
s_waitcnt lgkmcnt(3)
v_mfma_f32_16x16x32_bf16 v[64:67], v[132:135], v[196:199], v[64:67]
v_mfma_f32_16x16x32_bf16 v[68:71], v[136:139], v[196:199], v[68:71]
s_waitcnt lgkmcnt(2)
v_mfma_f32_16x16x32_bf16 v[64:67], v[140:143], v[200:203], v[64:67]
v_mfma_f32_16x16x32_bf16 v[68:71], v[144:147], v[200:203], v[68:71]
s_waitcnt lgkmcnt(1)
v_mfma_f32_16x16x32_bf16 v[64:67], v[148:151], v[204:207], v[64:67]
v_mfma_f32_16x16x32_bf16 v[68:71], v[152:155], v[204:207], v[68:71]
s_waitcnt lgkmcnt(0)
)"
R"(v_mfma_f32_16x16x32_bf16 v[64:67], v[156:159], v[208:211], v[64:67]
v_mfma_f32_16x16x32_bf16 v[68:71], v[160:163], v[208:211], v[68:71]
s_waitcnt vmcnt(5)
v_mfma_f32_16x16x32_bf16 v[72:75], v[164:167], v[188:191], v[220:223]
s_waitcnt vmcnt(4)
v_mfma_f32_16x16x32_bf16 v[72:75], v[168:171], v[192:195], v[72:75]
s_waitcnt vmcnt(3)
v_mfma_f32_16x16x32_bf16 v[72:75], v[172:175], v[196:199], v[72:75]
s_waitcnt vmcnt(2)
v_mfma_f32_16x16x32_bf16 v[72:75], v[176:179], v[200:203], v[72:75]
s_waitcnt vmcnt(1)
v_mfma_f32_16x16x32_bf16 v[72:75], v[180:183], v[204:207], v[72:75]
s_waitcnt vmcnt(0)
v_mfma_f32_16x16x32_bf16 v[72:75], v[184:187], v[208:211], v[72:75]
v_max3_f32 v76, v64, v65, v66
v_max3_f32 v76, v76, v67, v68
v_max3_f32 v76, v76, v69, v70
s_nop 4
v_max3_f32 v76, v76, v71, v72
v_max3_f32 v76, v76, v73, v74
v_max_f32 v76, v76, v75
v_mov_b32 v77, v76
s_nop 1
v_permlane16_swap_b32 v76, v77
v_max_f32 v76, v76, v77
v_mov_b32 v77, v76
s_nop 1
v_permlane32_swap_b32 v76, v77
v_max_f32 v76, v76, v77
v_mul_f32 v76, 0xbdd53b95, v76
v_fmamk_f32 v64, v64, 0x3dd53b95, v76
v_fmamk_f32 v65, v65, 0x3dd53b95, v76
v_fmamk_f32 v66, v66, 0x3dd53b95, v76
v_fmamk_f32 v67, v67, 0x3dd53b95, v76
v_fmamk_f32 v68, v68, 0x3dd53b95, v76
v_fmamk_f32 v69, v69, 0x3dd53b95, v76
v_fmamk_f32 v70, v70, 0x3dd53b95, v76
v_fmamk_f32 v71, v71, 0x3dd53b95, v76
v_fmamk_f32 v72, v72, 0x3dd53b95, v76
v_fmamk_f32 v73, v73, 0x3dd53b95, v76
v_fmamk_f32 v74, v74, 0x3dd53b95, v76
v_fmamk_f32 v75, v75, 0x3dd53b95, v76
v_exp_f32 v64, v64
v_exp_f32 v65, v65
v_exp_f32 v66, v66
v_exp_f32 v67, v67
v_exp_f32 v68, v68
v_exp_f32 v69, v69
v_exp_f32 v70, v70
v_exp_f32 v71, v71
v_exp_f32 v72, v72
v_exp_f32 v73, v73
v_exp_f32 v74, v74
v_exp_f32 v75, v75
v_add_f32 v76, v64, v65
v_add_f32 v76, v76, v66
v_add_f32 v76, v76, v67
v_add_f32 v76, v76, v68
v_add_f32 v76, v76, v69
v_add_f32 v76, v76, v70
v_add_f32 v76, v76, v71
v_add_f32 v76, v76, v72
v_add_f32 v76, v76, v73
v_add_f32 v76, v76, v74
v_add_f32 v76, v76, v75
v_mov_b32 v77, v76
s_nop 1
v_permlane16_swap_b32 v76, v77
v_add_f32 v76, v76, v77
v_mov_b32 v77, v76
s_nop 1
v_permlane32_swap_b32 v76, v77
v_add_f32 v76, v76, v77
v_rcp_f32 v76, v76
s_nop 0
v_mul_f32 v64, v64, v76
v_mul_f32 v65, v65, v76
v_mul_f32 v66, v66, v76
v_mul_f32 v67, v67, v76
v_mul_f32 v68, v68, v76
v_mul_f32 v69, v69, v76
v_mul_f32 v70, v70, v76
v_mul_f32 v71, v71, v76
v_mul_f32 v72, v72, v76
v_mul_f32 v73, v73, v76
v_mul_f32 v74, v74, v76
v_mul_f32 v75, v75, v76
v_cvt_pk_bf16_f32 v228, v64, v65
v_cvt_pk_bf16_f32 v229, v66, v67
v_cvt_pk_bf16_f32 v230, v68, v69
v_cvt_pk_bf16_f32 v231, v70, v71
v_cvt_pk_bf16_f32 v232, v72, v73
v_cvt_pk_bf16_f32 v233, v74, v75
v_mov_b32 v234, v232
v_mov_b32 v235, v233
buffer_store_dwordx4 v[228:231], v0, s[40:43], s26 offen sc1
v_permlane16_swap_b32 v232, v234
v_permlane16_swap_b32 v233, v235
buffer_store_dwordx4 v[232:235], v12, s[40:43], s27 offen sc1
s_waitcnt vmcnt(2)
buffer_atomic_add v226, v3, s[40:43], s29 offen
s_waitcnt vmcnt(0)
buffer_atomic_add v226, v3, s[40:43], s52 offen
s_nop 1
s_branch hp_ret_%=
hpq_light_%=:
ds_read_b128 v[80:83], v1 offset:0
ds_read_b128 v[84:87], v1 offset:20480
ds_read_b128 v[88:91], v1 offset:1024
ds_read_b128 v[92:95], v1 offset:21504
ds_read_b128 v[96:99], v1 offset:2048
hpl_loop_%=:
s_waitcnt lgkmcnt(4)
v_mfma_f32_16x16x32_bf16 v[16:19], a[0:3], v[80:83], 0
v_mfma_f32_16x16x32_bf16 v[24:27], a[80:83], v[80:83], 0
v_mfma_f32_16x16x32_bf16 v[32:35], a[160:163], v[80:83], 0
ds_read_b128 v[80:83], v1 offset:22528
s_waitcnt lgkmcnt(4)
v_mfma_f32_16x16x32_bf16 v[20:23], a[0:3], v[84:87], 0
v_mfma_f32_16x16x32_bf16 v[28:31], a[80:83], v[84:87], 0
v_mfma_f32_16x16x32_bf16 v[36:39], a[160:163], v[84:87], 0
ds_read_b128 v[84:87], v1 offset:3072
s_waitcnt lgkmcnt(4)
v_mfma_f32_16x16x32_bf16 v[16:19], a[4:7], v[88:91], v[16:19]
v_mfma_f32_16x16x32_bf16 v[24:27], a[84:87], v[88:91], v[24:27]
v_mfma_f32_16x16x32_bf16 v[32:35], a[164:167], v[88:91], v[32:35]
ds_read_b128 v[88:91], v1 offset:23552
s_waitcnt lgkmcnt(4)
v_mfma_f32_16x16x32_bf16 v[20:23], a[4:7], v[92:95], v[20:23]
v_cvt_pk_bf16_f32 v104, v40, v41
v_cvt_pk_bf16_f32 v105, v42, v43
v_mfma_f32_16x16x32_bf16 v[28:31], a[84:87], v[92:95], v[28:31]
v_cvt_pk_bf16_f32 v106, v48, v49
v_cvt_pk_bf16_f32 v107, v50, v51
v_mfma_f32_16x16x32_bf16 v[36:39], a[164:167], v[92:95], v[36:39]
ds_read_b128 v[92:95], v1 offset:4096
s_waitcnt lgkmcnt(4)
v_mfma_f32_16x16x32_bf16 v[16:19], a[8:11], v[96:99], v[16:19]
ds_write_b128 v6, v[104:107] offset:12288
v_mfma_f32_16x16x32_bf16 v[24:27], a[88:91], v[96:99], v[24:27]
v_cvt_pk_bf16_f32 v108, v44, v45
v_cvt_pk_bf16_f32 v109, v46, v47
v_mfma_f32_16x16x32_bf16 v[32:35], a[168:171], v[96:99], v[32:35]
v_cvt_pk_bf16_f32 v110, v52, v53
ds_read_b128 v[96:99], v1 offset:24576
s_waitcnt lgkmcnt(5)
v_mfma_f32_16x16x32_bf16 v[20:23], a[8:11], v[80:83], v[20:23]
v_cvt_pk_bf16_f32 v111, v54, v55
v_mfma_f32_16x16x32_bf16 v[28:31], a[88:91], v[80:83], v[28:31]
ds_write_b128 v6, v[108:111] offset:13312
v_mfma_f32_16x16x32_bf16 v[36:39], a[168:171], v[80:83], v[36:39]
v_cvt_pk_bf16_f32 v112, v56, v57
ds_read_b128 v[80:83], v1 offset:5120
s_waitcnt lgkmcnt(6)
v_mfma_f32_16x16x32_bf16 v[16:19], a[12:15], v[84:87], v[16:19]
v_cvt_pk_bf16_f32 v113, v58, v59
v_cvt_pk_bf16_f32 v114, v60, v61
v_mfma_f32_16x16x32_bf16 v[24:27], a[92:95], v[84:87], v[24:27]
v_cvt_pk_bf16_f32 v115, v62, v63
v_mfma_f32_16x16x32_bf16 v[32:35], a[172:175], v[84:87], v[32:35]
ds_read_b128 v[84:87], v1 offset:25600
s_waitcnt lgkmcnt(6)
v_mfma_f32_16x16x32_bf16 v[20:23], a[12:15], v[88:91], v[20:23]
ds_write2st64_b64 v7, v[112:113], v[114:115] offset0:24 offset1:26
v_mfma_f32_16x16x32_bf16 v[28:31], a[92:95], v[88:91], v[28:31]
buffer_load_dword v224, v225, s[40:43], s58 offen sc1
v_mfma_f32_16x16x32_bf16 v[36:39], a[172:175], v[88:91], v[36:39]
ds_read_b128 v[88:91], v1 offset:6144
s_waitcnt lgkmcnt(7)
v_mfma_f32_16x16x32_bf16 v[16:19], a[16:19], v[92:95], v[16:19]
v_mfma_f32_16x16x32_bf16 v[24:27], a[96:99], v[92:95], v[24:27]
v_mfma_f32_16x16x32_bf16 v[32:35], a[176:179], v[92:95], v[32:35]
ds_read_b128 v[92:95], v1 offset:26624
s_waitcnt lgkmcnt(6)
v_mfma_f32_16x16x32_bf16 v[20:23], a[16:19], v[96:99], v[20:23]
v_mfma_f32_16x16x32_bf16 v[28:31], a[96:99], v[96:99], v[28:31]
v_mfma_f32_16x16x32_bf16 v[36:39], a[176:179], v[96:99], v[36:39]
ds_read_b128 v[96:99], v1 offset:7168
s_waitcnt lgkmcnt(5)
v_mfma_f32_16x16x32_bf16 v[16:19], a[20:23], v[80:83], v[16:19]
v_mfma_f32_16x16x32_bf16 v[24:27], a[100:103], v[80:83], v[24:27]
v_mfma_f32_16x16x32_bf16 v[32:35], a[180:183], v[80:83], v[32:35]
ds_read_b128 v[80:83], v1 offset:27648
s_waitcnt lgkmcnt(5)
v_mfma_f32_16x16x32_bf16 v[20:23], a[20:23], v[84:87], v[20:23]
v_mfma_f32_16x16x32_bf16 v[28:31], a[100:103], v[84:87], v[28:31]
v_mfma_f32_16x16x32_bf16 v[36:39], a[180:183], v[84:87], v[36:39]
ds_read_b128 v[84:87], v1 offset:8192
s_waitcnt lgkmcnt(4)
v_mfma_f32_16x16x32_bf16 v[16:19], a[24:27], v[88:91], v[16:19]
v_mfma_f32_16x16x32_bf16 v[24:27], a[104:107], v[88:91], v[24:27]
v_mfma_f32_16x16x32_bf16 v[32:35], a[184:187], v[88:91], v[32:35]
ds_read_b128 v[88:91], v1 offset:28672
s_waitcnt lgkmcnt(4)
v_mfma_f32_16x16x32_bf16 v[20:23], a[24:27], v[92:95], v[20:23]
v_mfma_f32_16x16x32_bf16 v[28:31], a[104:107], v[92:95], v[28:31]
v_mfma_f32_16x16x32_bf16 v[36:39], a[184:187], v[92:95], v[36:39]
ds_read_b128 v[92:95], v1 offset:9216
s_waitcnt lgkmcnt(4)
v_mfma_f32_16x16x32_bf16 v[16:19], a[28:31], v[96:99], v[16:19]
)"
R"(v_mfma_f32_16x16x32_bf16 v[24:27], a[108:111], v[96:99], v[24:27]
v_mfma_f32_16x16x32_bf16 v[32:35], a[188:191], v[96:99], v[32:35]
ds_read_b128 v[96:99], v1 offset:29696
s_waitcnt lgkmcnt(4)
v_mfma_f32_16x16x32_bf16 v[20:23], a[28:31], v[80:83], v[20:23]
v_mfma_f32_16x16x32_bf16 v[28:31], a[108:111], v[80:83], v[28:31]
v_mfma_f32_16x16x32_bf16 v[36:39], a[188:191], v[80:83], v[36:39]
ds_read_b128 v[80:83], v1 offset:10240
s_waitcnt lgkmcnt(4)
v_mfma_f32_16x16x32_bf16 v[16:19], a[32:35], v[84:87], v[16:19]
v_mfma_f32_16x16x32_bf16 v[24:27], a[112:115], v[84:87], v[24:27]
v_mfma_f32_16x16x32_bf16 v[32:35], a[192:195], v[84:87], v[32:35]
ds_read_b128 v[84:87], v1 offset:30720
s_waitcnt lgkmcnt(4)
v_mfma_f32_16x16x32_bf16 v[20:23], a[32:35], v[88:91], v[20:23]
v_mfma_f32_16x16x32_bf16 v[28:31], a[112:115], v[88:91], v[28:31]
v_mfma_f32_16x16x32_bf16 v[36:39], a[192:195], v[88:91], v[36:39]
ds_read_b128 v[88:91], v1 offset:11264
s_waitcnt lgkmcnt(4)
v_mfma_f32_16x16x32_bf16 v[16:19], a[36:39], v[92:95], v[16:19]
v_mfma_f32_16x16x32_bf16 v[24:27], a[116:119], v[92:95], v[24:27]
v_mfma_f32_16x16x32_bf16 v[32:35], a[196:199], v[92:95], v[32:35]
ds_read_b128 v[92:95], v1 offset:31744
s_waitcnt lgkmcnt(4)
v_mfma_f32_16x16x32_bf16 v[20:23], a[36:39], v[96:99], v[20:23]
v_mfma_f32_16x16x32_bf16 v[28:31], a[116:119], v[96:99], v[28:31]
v_mfma_f32_16x16x32_bf16 v[36:39], a[196:199], v[96:99], v[36:39]
ds_read_b128 v[96:99], v1 offset:12288
s_waitcnt lgkmcnt(4)
v_mfma_f32_16x16x32_bf16 v[16:19], a[40:43], v[80:83], v[16:19]
v_mfma_f32_16x16x32_bf16 v[24:27], a[120:123], v[80:83], v[24:27]
v_mfma_f32_16x16x32_bf16 v[32:35], a[200:203], v[80:83], v[32:35]
ds_read_b128 v[80:83], v1 offset:32768
s_waitcnt lgkmcnt(4)
v_mfma_f32_16x16x32_bf16 v[20:23], a[40:43], v[84:87], v[20:23]
v_mfma_f32_16x16x32_bf16 v[28:31], a[120:123], v[84:87], v[28:31]
v_mfma_f32_16x16x32_bf16 v[36:39], a[200:203], v[84:87], v[36:39]
ds_read_b128 v[84:87], v1 offset:13312
s_waitcnt lgkmcnt(4)
v_mfma_f32_16x16x32_bf16 v[16:19], a[44:47], v[88:91], v[16:19]
v_mfma_f32_16x16x32_bf16 v[24:27], a[124:127], v[88:91], v[24:27]
v_mfma_f32_16x16x32_bf16 v[32:35], a[204:207], v[88:91], v[32:35]
ds_read_b128 v[88:91], v1 offset:33792
s_waitcnt lgkmcnt(4)
v_mfma_f32_16x16x32_bf16 v[20:23], a[44:47], v[92:95], v[20:23]
v_mfma_f32_16x16x32_bf16 v[28:31], a[124:127], v[92:95], v[28:31]
v_mfma_f32_16x16x32_bf16 v[36:39], a[204:207], v[92:95], v[36:39]
ds_read_b128 v[92:95], v1 offset:14336
s_waitcnt lgkmcnt(4)
v_mfma_f32_16x16x32_bf16 v[16:19], a[48:51], v[96:99], v[16:19]
v_mfma_f32_16x16x32_bf16 v[24:27], a[128:131], v[96:99], v[24:27]
v_mfma_f32_16x16x32_bf16 v[32:35], a[208:211], v[96:99], v[32:35]
ds_read_b128 v[96:99], v1 offset:34816
s_waitcnt lgkmcnt(4)
v_mfma_f32_16x16x32_bf16 v[20:23], a[48:51], v[80:83], v[20:23]
v_mfma_f32_16x16x32_bf16 v[28:31], a[128:131], v[80:83], v[28:31]
v_mfma_f32_16x16x32_bf16 v[36:39], a[208:211], v[80:83], v[36:39]
ds_read_b128 v[80:83], v1 offset:15360
s_waitcnt lgkmcnt(4)
v_mfma_f32_16x16x32_bf16 v[16:19], a[52:55], v[84:87], v[16:19]
v_mfma_f32_16x16x32_bf16 v[24:27], a[132:135], v[84:87], v[24:27]
v_mfma_f32_16x16x32_bf16 v[32:35], a[212:215], v[84:87], v[32:35]
ds_read_b128 v[84:87], v1 offset:35840
s_waitcnt lgkmcnt(4)
v_mfma_f32_16x16x32_bf16 v[20:23], a[52:55], v[88:91], v[20:23]
v_mfma_f32_16x16x32_bf16 v[28:31], a[132:135], v[88:91], v[28:31]
v_mfma_f32_16x16x32_bf16 v[36:39], a[212:215], v[88:91], v[36:39]
ds_read_b128 v[88:91], v1 offset:16384
s_waitcnt lgkmcnt(4)
v_mfma_f32_16x16x32_bf16 v[16:19], a[56:59], v[92:95], v[16:19]
v_mfma_f32_16x16x32_bf16 v[24:27], a[136:139], v[92:95], v[24:27]
v_mfma_f32_16x16x32_bf16 v[32:35], a[216:219], v[92:95], v[32:35]
ds_read_b128 v[92:95], v1 offset:36864
s_waitcnt lgkmcnt(4)
v_mfma_f32_16x16x32_bf16 v[20:23], a[56:59], v[96:99], v[20:23]
v_mfma_f32_16x16x32_bf16 v[28:31], a[136:139], v[96:99], v[28:31]
v_mfma_f32_16x16x32_bf16 v[36:39], a[216:219], v[96:99], v[36:39]
s_waitcnt vmcnt(0) lgkmcnt(4)
s_barrier
s_and_b32 s48, s47, 63
s_cmp_lt_u32 s48, 62
s_cselect_b32 s48, 2, 0
v_cmp_gt_u32 vcc, s48, v224
s_cbranch_vccz hpl_xf0_ok_%=
s_mov_b32 s60, 0
s_nop 2
hpl_xf0_poll_%=:
buffer_load_dword v224, v225, s[40:43], s58 offen sc1
s_waitcnt vmcnt(0)
v_cmp_gt_u32 vcc, s48, v224
s_nop 4
s_cbranch_vccz hpl_xf0_ok_%=
s_sleep 2
s_add_u32 s60, s60, 1
s_cmp_gt_u32 s60, 65536
s_cbranch_scc1 hp_abort_%=
s_and_b32 s59, s60, 63
s_cbranch_scc1 hpl_xf0_poll_%=
v_mov_b32 v4, 1024
buffer_load_dword v4, v4, s[40:43], s58 offen sc1
s_waitcnt vmcnt(0)
v_readfirstlane_b32 s59, v4
s_nop 0
s_cmp_lg_u32 s59, 0
s_nop 2
s_cbranch_scc1 hp_end_%=
s_branch hpl_xf0_poll_%=
hpl_xf0_ok_%=:
v_add_u32 v225, 128, v225
ds_read_b128 v[96:99], v1 offset:17408
s_waitcnt lgkmcnt(4)
v_mfma_f32_16x16x32_bf16 v[16:19], a[60:63], v[80:83], v[16:19]
s_add_u32 m0, s61, 0
s_add_u32 s51, s50, 0
buffer_load_dwordx4 v0, s[40:43], s51 offen offset:0 sc1 lds
v_mfma_f32_16x16x32_bf16 v[24:27], a[140:143], v[80:83], v[24:27]
v_mfma_f32_16x16x32_bf16 v[32:35], a[220:223], v[80:83], v[32:35]
ds_read_b128 v[80:83], v1 offset:37888
s_waitcnt lgkmcnt(4)
v_mfma_f32_16x16x32_bf16 v[20:23], a[60:63], v[84:87], v[20:23]
buffer_load_dwordx4 v0, s[40:43], s51 offen offset:1024 sc1 lds
v_mfma_f32_16x16x32_bf16 v[28:31], a[140:143], v[84:87], v[28:31]
v_mfma_f32_16x16x32_bf16 v[36:39], a[220:223], v[84:87], v[36:39]
ds_read_b128 v[84:87], v1 offset:18432
s_waitcnt lgkmcnt(4)
v_mfma_f32_16x16x32_bf16 v[16:19], a[64:67], v[88:91], v[16:19]
buffer_load_dwordx4 v0, s[40:43], s51 offen offset:2048 sc1 lds
v_mfma_f32_16x16x32_bf16 v[24:27], a[144:147], v[88:91], v[24:27]
v_mfma_f32_16x16x32_bf16 v[32:35], a[224:227], v[88:91], v[32:35]
ds_read_b128 v[88:91], v1 offset:38912
s_waitcnt lgkmcnt(4)
v_mfma_f32_16x16x32_bf16 v[20:23], a[64:67], v[92:95], v[20:23]
buffer_load_dwordx4 v0, s[40:43], s51 offen offset:3072 sc1 lds
v_mfma_f32_16x16x32_bf16 v[28:31], a[144:147], v[92:95], v[28:31]
v_mfma_f32_16x16x32_bf16 v[36:39], a[224:227], v[92:95], v[36:39]
ds_read_b128 v[92:95], v1 offset:19456
s_waitcnt lgkmcnt(4)
v_mfma_f32_16x16x32_bf16 v[16:19], a[68:71], v[96:99], v[16:19]
s_add_u32 m0, s61, 4096
s_add_u32 s51, s50, 4096
buffer_load_dwordx4 v0, s[40:43], s51 offen offset:0 sc1 lds
v_mfma_f32_16x16x32_bf16 v[24:27], a[148:151], v[96:99], v[24:27]
v_mfma_f32_16x16x32_bf16 v[32:35], a[228:231], v[96:99], v[32:35]
ds_read_b128 v[96:99], v1 offset:39936
s_waitcnt lgkmcnt(4)
v_mfma_f32_16x16x32_bf16 v[20:23], a[68:71], v[80:83], v[20:23]
buffer_load_dwordx4 v0, s[40:43], s51 offen offset:1024 sc1 lds
v_mfma_f32_16x16x32_bf16 v[28:31], a[148:151], v[80:83], v[28:31]
v_mfma_f32_16x16x32_bf16 v[36:39], a[228:231], v[80:83], v[36:39]
s_add_u32 s64, s64, 40960
s_cmp_ge_u32 s64, 122880
s_cselect_b32 s64, 0, s64
v_add_u32 v1, s64, v0
ds_read_b128 v[80:83], v1 offset:0
s_waitcnt lgkmcnt(4)
v_mfma_f32_16x16x32_bf16 v[16:19], a[72:75], v[84:87], v[16:19]
buffer_load_dwordx4 v0, s[40:43], s51 offen offset:2048 sc1 lds
v_mfma_f32_16x16x32_bf16 v[24:27], a[152:155], v[84:87], v[24:27]
v_mfma_f32_16x16x32_bf16 v[32:35], a[232:235], v[84:87], v[32:35]
ds_read_b128 v[84:87], v1 offset:20480
s_waitcnt lgkmcnt(4)
v_mfma_f32_16x16x32_bf16 v[20:23], a[72:75], v[88:91], v[20:23]
buffer_load_dwordx4 v0, s[40:43], s51 offen offset:3072 sc1 lds
v_mfma_f32_16x16x32_bf16 v[28:31], a[152:155], v[88:91], v[28:31]
v_mfma_f32_16x16x32_bf16 v[36:39], a[232:235], v[88:91], v[36:39]
ds_read_b128 v[88:91], v1 offset:1024
s_waitcnt lgkmcnt(4)
v_mfma_f32_16x16x32_bf16 v[16:19], a[76:79], v[92:95], v[16:19]
s_add_u32 m0, s61, 8192
s_add_u32 s51, s50, 8192
buffer_load_dwordx4 v0, s[40:43], s51 offen offset:0 sc1 lds
v_mfma_f32_16x16x32_bf16 v[24:27], a[156:159], v[92:95], v[24:27]
v_mfma_f32_16x16x32_bf16 v[32:35], a[236:239], v[92:95], v[32:35]
ds_read_b128 v[92:95], v1 offset:21504
s_waitcnt lgkmcnt(4)
v_mfma_f32_16x16x32_bf16 v[20:23], a[76:79], v[96:99], v[20:23]
buffer_load_dwordx4 v0, s[40:43], s51 offen offset:1024 sc1 lds
v_mfma_f32_16x16x32_bf16 v[28:31], a[156:159], v[96:99], v[28:31]
s_add_u32 s50, s50, 40960
s_add_u32 s61, s61, 40960
s_cmp_ge_u32 s61, 122880
s_cselect_b32 s51, 122880, 0
s_sub_u32 s61, s61, s51
v_mfma_f32_16x16x32_bf16 v[36:39], a[236:239], v[96:99], v[36:39]
ds_read_b128 v[96:99], v1 offset:2048
s_add_u32 s47, s47, 1
s_waitcnt lgkmcnt(4)
v_mfma_f32_16x16x32_bf16 v[40:43], a[0:3], v[80:83], 0
v_mfma_f32_16x16x32_bf16 v[48:51], a[80:83], v[80:83], 0
v_mfma_f32_16x16x32_bf16 v[56:59], a[160:163], v[80:83], 0
ds_read_b128 v[80:83], v1 offset:22528
s_waitcnt lgkmcnt(4)
v_mfma_f32_16x16x32_bf16 v[44:47], a[0:3], v[84:87], 0
v_mfma_f32_16x16x32_bf16 v[52:55], a[80:83], v[84:87], 0
v_mfma_f32_16x16x32_bf16 v[60:63], a[160:163], v[84:87], 0
ds_read_b128 v[84:87], v1 offset:3072
s_waitcnt lgkmcnt(4)
v_mfma_f32_16x16x32_bf16 v[40:43], a[4:7], v[88:91], v[40:43]
v_mfma_f32_16x16x32_bf16 v[48:51], a[84:87], v[88:91], v[48:51]
v_mfma_f32_16x16x32_bf16 v[56:59], a[164:167], v[88:91], v[56:59]
)"
R"(ds_read_b128 v[88:91], v1 offset:23552
s_waitcnt lgkmcnt(4)
v_mfma_f32_16x16x32_bf16 v[44:47], a[4:7], v[92:95], v[44:47]
v_cvt_pk_bf16_f32 v104, v16, v17
v_cvt_pk_bf16_f32 v105, v18, v19
v_mfma_f32_16x16x32_bf16 v[52:55], a[84:87], v[92:95], v[52:55]
v_cvt_pk_bf16_f32 v106, v24, v25
v_cvt_pk_bf16_f32 v107, v26, v27
v_mfma_f32_16x16x32_bf16 v[60:63], a[164:167], v[92:95], v[60:63]
ds_read_b128 v[92:95], v1 offset:4096
s_waitcnt lgkmcnt(4)
v_mfma_f32_16x16x32_bf16 v[40:43], a[8:11], v[96:99], v[40:43]
ds_write_b128 v6, v[104:107] offset:0
v_mfma_f32_16x16x32_bf16 v[48:51], a[88:91], v[96:99], v[48:51]
v_cvt_pk_bf16_f32 v108, v20, v21
v_cvt_pk_bf16_f32 v109, v22, v23
v_mfma_f32_16x16x32_bf16 v[56:59], a[168:171], v[96:99], v[56:59]
v_cvt_pk_bf16_f32 v110, v28, v29
ds_read_b128 v[96:99], v1 offset:24576
s_waitcnt lgkmcnt(5)
v_mfma_f32_16x16x32_bf16 v[44:47], a[8:11], v[80:83], v[44:47]
v_cvt_pk_bf16_f32 v111, v30, v31
v_mfma_f32_16x16x32_bf16 v[52:55], a[88:91], v[80:83], v[52:55]
ds_write_b128 v6, v[108:111] offset:1024
v_mfma_f32_16x16x32_bf16 v[60:63], a[168:171], v[80:83], v[60:63]
v_cvt_pk_bf16_f32 v112, v32, v33
ds_read_b128 v[80:83], v1 offset:5120
s_waitcnt lgkmcnt(6)
v_mfma_f32_16x16x32_bf16 v[40:43], a[12:15], v[84:87], v[40:43]
v_cvt_pk_bf16_f32 v113, v34, v35
v_cvt_pk_bf16_f32 v114, v36, v37
v_mfma_f32_16x16x32_bf16 v[48:51], a[92:95], v[84:87], v[48:51]
v_cvt_pk_bf16_f32 v115, v38, v39
v_mfma_f32_16x16x32_bf16 v[56:59], a[172:175], v[84:87], v[56:59]
ds_read_b128 v[84:87], v1 offset:25600
s_waitcnt lgkmcnt(6)
v_mfma_f32_16x16x32_bf16 v[44:47], a[12:15], v[88:91], v[44:47]
ds_write2st64_b64 v7, v[112:113], v[114:115] offset0:0 offset1:2
v_mfma_f32_16x16x32_bf16 v[52:55], a[92:95], v[88:91], v[52:55]
buffer_load_dword v224, v225, s[40:43], s58 offen sc1
v_mfma_f32_16x16x32_bf16 v[60:63], a[172:175], v[88:91], v[60:63]
ds_read_b128 v[88:91], v1 offset:6144
s_waitcnt lgkmcnt(7)
v_mfma_f32_16x16x32_bf16 v[40:43], a[16:19], v[92:95], v[40:43]
v_mfma_f32_16x16x32_bf16 v[48:51], a[96:99], v[92:95], v[48:51]
v_mfma_f32_16x16x32_bf16 v[56:59], a[176:179], v[92:95], v[56:59]
ds_read_b128 v[92:95], v1 offset:26624
s_waitcnt lgkmcnt(6)
v_mfma_f32_16x16x32_bf16 v[44:47], a[16:19], v[96:99], v[44:47]
v_mfma_f32_16x16x32_bf16 v[52:55], a[96:99], v[96:99], v[52:55]
v_mfma_f32_16x16x32_bf16 v[60:63], a[176:179], v[96:99], v[60:63]
ds_read_b128 v[96:99], v1 offset:7168
s_waitcnt lgkmcnt(5)
v_mfma_f32_16x16x32_bf16 v[40:43], a[20:23], v[80:83], v[40:43]
v_mfma_f32_16x16x32_bf16 v[48:51], a[100:103], v[80:83], v[48:51]
v_mfma_f32_16x16x32_bf16 v[56:59], a[180:183], v[80:83], v[56:59]
ds_read_b128 v[80:83], v1 offset:27648
s_waitcnt lgkmcnt(5)
v_mfma_f32_16x16x32_bf16 v[44:47], a[20:23], v[84:87], v[44:47]
v_mfma_f32_16x16x32_bf16 v[52:55], a[100:103], v[84:87], v[52:55]
v_mfma_f32_16x16x32_bf16 v[60:63], a[180:183], v[84:87], v[60:63]
ds_read_b128 v[84:87], v1 offset:8192
s_waitcnt lgkmcnt(4)
v_mfma_f32_16x16x32_bf16 v[40:43], a[24:27], v[88:91], v[40:43]
v_mfma_f32_16x16x32_bf16 v[48:51], a[104:107], v[88:91], v[48:51]
v_mfma_f32_16x16x32_bf16 v[56:59], a[184:187], v[88:91], v[56:59]
ds_read_b128 v[88:91], v1 offset:28672
s_waitcnt lgkmcnt(4)
v_mfma_f32_16x16x32_bf16 v[44:47], a[24:27], v[92:95], v[44:47]
v_mfma_f32_16x16x32_bf16 v[52:55], a[104:107], v[92:95], v[52:55]
v_mfma_f32_16x16x32_bf16 v[60:63], a[184:187], v[92:95], v[60:63]
ds_read_b128 v[92:95], v1 offset:9216
s_waitcnt lgkmcnt(4)
v_mfma_f32_16x16x32_bf16 v[40:43], a[28:31], v[96:99], v[40:43]
v_mfma_f32_16x16x32_bf16 v[48:51], a[108:111], v[96:99], v[48:51]
v_mfma_f32_16x16x32_bf16 v[56:59], a[188:191], v[96:99], v[56:59]
ds_read_b128 v[96:99], v1 offset:29696
s_waitcnt lgkmcnt(4)
v_mfma_f32_16x16x32_bf16 v[44:47], a[28:31], v[80:83], v[44:47]
v_mfma_f32_16x16x32_bf16 v[52:55], a[108:111], v[80:83], v[52:55]
v_mfma_f32_16x16x32_bf16 v[60:63], a[188:191], v[80:83], v[60:63]
ds_read_b128 v[80:83], v1 offset:10240
s_waitcnt lgkmcnt(4)
v_mfma_f32_16x16x32_bf16 v[40:43], a[32:35], v[84:87], v[40:43]
v_mfma_f32_16x16x32_bf16 v[48:51], a[112:115], v[84:87], v[48:51]
v_mfma_f32_16x16x32_bf16 v[56:59], a[192:195], v[84:87], v[56:59]
ds_read_b128 v[84:87], v1 offset:30720
s_waitcnt lgkmcnt(4)
v_mfma_f32_16x16x32_bf16 v[44:47], a[32:35], v[88:91], v[44:47]
v_mfma_f32_16x16x32_bf16 v[52:55], a[112:115], v[88:91], v[52:55]
v_mfma_f32_16x16x32_bf16 v[60:63], a[192:195], v[88:91], v[60:63]
ds_read_b128 v[88:91], v1 offset:11264
s_waitcnt lgkmcnt(4)
v_mfma_f32_16x16x32_bf16 v[40:43], a[36:39], v[92:95], v[40:43]
v_mfma_f32_16x16x32_bf16 v[48:51], a[116:119], v[92:95], v[48:51]
v_mfma_f32_16x16x32_bf16 v[56:59], a[196:199], v[92:95], v[56:59]
ds_read_b128 v[92:95], v1 offset:31744
s_waitcnt lgkmcnt(4)
v_mfma_f32_16x16x32_bf16 v[44:47], a[36:39], v[96:99], v[44:47]
v_mfma_f32_16x16x32_bf16 v[52:55], a[116:119], v[96:99], v[52:55]
v_mfma_f32_16x16x32_bf16 v[60:63], a[196:199], v[96:99], v[60:63]
ds_read_b128 v[96:99], v1 offset:12288
s_waitcnt lgkmcnt(4)
v_mfma_f32_16x16x32_bf16 v[40:43], a[40:43], v[80:83], v[40:43]
v_mfma_f32_16x16x32_bf16 v[48:51], a[120:123], v[80:83], v[48:51]
v_mfma_f32_16x16x32_bf16 v[56:59], a[200:203], v[80:83], v[56:59]
ds_read_b128 v[80:83], v1 offset:32768
s_waitcnt lgkmcnt(4)
v_mfma_f32_16x16x32_bf16 v[44:47], a[40:43], v[84:87], v[44:47]
v_mfma_f32_16x16x32_bf16 v[52:55], a[120:123], v[84:87], v[52:55]
v_mfma_f32_16x16x32_bf16 v[60:63], a[200:203], v[84:87], v[60:63]
ds_read_b128 v[84:87], v1 offset:13312
s_waitcnt lgkmcnt(4)
v_mfma_f32_16x16x32_bf16 v[40:43], a[44:47], v[88:91], v[40:43]
v_mfma_f32_16x16x32_bf16 v[48:51], a[124:127], v[88:91], v[48:51]
v_mfma_f32_16x16x32_bf16 v[56:59], a[204:207], v[88:91], v[56:59]
ds_read_b128 v[88:91], v1 offset:33792
s_waitcnt lgkmcnt(4)
v_mfma_f32_16x16x32_bf16 v[44:47], a[44:47], v[92:95], v[44:47]
v_mfma_f32_16x16x32_bf16 v[52:55], a[124:127], v[92:95], v[52:55]
v_mfma_f32_16x16x32_bf16 v[60:63], a[204:207], v[92:95], v[60:63]
ds_read_b128 v[92:95], v1 offset:14336
s_waitcnt lgkmcnt(4)
v_mfma_f32_16x16x32_bf16 v[40:43], a[48:51], v[96:99], v[40:43]
v_mfma_f32_16x16x32_bf16 v[48:51], a[128:131], v[96:99], v[48:51]
v_mfma_f32_16x16x32_bf16 v[56:59], a[208:211], v[96:99], v[56:59]
ds_read_b128 v[96:99], v1 offset:34816
s_waitcnt lgkmcnt(4)
v_mfma_f32_16x16x32_bf16 v[44:47], a[48:51], v[80:83], v[44:47]
v_mfma_f32_16x16x32_bf16 v[52:55], a[128:131], v[80:83], v[52:55]
v_mfma_f32_16x16x32_bf16 v[60:63], a[208:211], v[80:83], v[60:63]
ds_read_b128 v[80:83], v1 offset:15360
s_waitcnt lgkmcnt(4)
v_mfma_f32_16x16x32_bf16 v[40:43], a[52:55], v[84:87], v[40:43]
v_mfma_f32_16x16x32_bf16 v[48:51], a[132:135], v[84:87], v[48:51]
v_mfma_f32_16x16x32_bf16 v[56:59], a[212:215], v[84:87], v[56:59]
ds_read_b128 v[84:87], v1 offset:35840
s_waitcnt lgkmcnt(4)
v_mfma_f32_16x16x32_bf16 v[44:47], a[52:55], v[88:91], v[44:47]
v_mfma_f32_16x16x32_bf16 v[52:55], a[132:135], v[88:91], v[52:55]
v_mfma_f32_16x16x32_bf16 v[60:63], a[212:215], v[88:91], v[60:63]
ds_read_b128 v[88:91], v1 offset:16384
s_waitcnt lgkmcnt(4)
v_mfma_f32_16x16x32_bf16 v[40:43], a[56:59], v[92:95], v[40:43]
v_mfma_f32_16x16x32_bf16 v[48:51], a[136:139], v[92:95], v[48:51]
v_mfma_f32_16x16x32_bf16 v[56:59], a[216:219], v[92:95], v[56:59]
ds_read_b128 v[92:95], v1 offset:36864
s_waitcnt lgkmcnt(4)
v_mfma_f32_16x16x32_bf16 v[44:47], a[56:59], v[96:99], v[44:47]
v_mfma_f32_16x16x32_bf16 v[52:55], a[136:139], v[96:99], v[52:55]
v_mfma_f32_16x16x32_bf16 v[60:63], a[216:219], v[96:99], v[60:63]
s_waitcnt vmcnt(0) lgkmcnt(4)
s_barrier
s_and_b32 s48, s47, 63
s_cmp_lt_u32 s48, 62
s_cselect_b32 s48, 2, 0
v_cmp_gt_u32 vcc, s48, v224
s_cbranch_vccz hpl_xf1_ok_%=
s_mov_b32 s60, 0
s_nop 2
hpl_xf1_poll_%=:
buffer_load_dword v224, v225, s[40:43], s58 offen sc1
s_waitcnt vmcnt(0)
v_cmp_gt_u32 vcc, s48, v224
s_nop 4
s_cbranch_vccz hpl_xf1_ok_%=
s_sleep 2
s_add_u32 s60, s60, 1
s_cmp_gt_u32 s60, 65536
s_cbranch_scc1 hp_abort_%=
s_and_b32 s59, s60, 63
s_cbranch_scc1 hpl_xf1_poll_%=
v_mov_b32 v4, 1024
buffer_load_dword v4, v4, s[40:43], s58 offen sc1
s_waitcnt vmcnt(0)
v_readfirstlane_b32 s59, v4
s_nop 0
s_cmp_lg_u32 s59, 0
s_nop 2
s_cbranch_scc1 hp_end_%=
s_branch hpl_xf1_poll_%=
hpl_xf1_ok_%=:
v_add_u32 v225, 128, v225
ds_read_b128 v[96:99], v1 offset:17408
s_waitcnt lgkmcnt(4)
v_mfma_f32_16x16x32_bf16 v[40:43], a[60:63], v[80:83], v[40:43]
s_add_u32 m0, s61, 0
s_add_u32 s51, s50, 0
buffer_load_dwordx4 v0, s[40:43], s51 offen offset:0 sc1 lds
v_mfma_f32_16x16x32_bf16 v[48:51], a[140:143], v[80:83], v[48:51]
v_mfma_f32_16x16x32_bf16 v[56:59], a[220:223], v[80:83], v[56:59]
ds_read_b128 v[80:83], v1 offset:37888
s_waitcnt lgkmcnt(4)
v_mfma_f32_16x16x32_bf16 v[44:47], a[60:63], v[84:87], v[44:47]
buffer_load_dwordx4 v0, s[40:43], s51 offen offset:1024 sc1 lds
v_mfma_f32_16x16x32_bf16 v[52:55], a[140:143], v[84:87], v[52:55]
v_mfma_f32_16x16x32_bf16 v[60:63], a[220:223], v[84:87], v[60:63]
ds_read_b128 v[84:87], v1 offset:18432
s_waitcnt lgkmcnt(4)
v_mfma_f32_16x16x32_bf16 v[40:43], a[64:67], v[88:91], v[40:43]
)"
R"(buffer_load_dwordx4 v0, s[40:43], s51 offen offset:2048 sc1 lds
v_mfma_f32_16x16x32_bf16 v[48:51], a[144:147], v[88:91], v[48:51]
v_mfma_f32_16x16x32_bf16 v[56:59], a[224:227], v[88:91], v[56:59]
ds_read_b128 v[88:91], v1 offset:38912
s_waitcnt lgkmcnt(4)
v_mfma_f32_16x16x32_bf16 v[44:47], a[64:67], v[92:95], v[44:47]
buffer_load_dwordx4 v0, s[40:43], s51 offen offset:3072 sc1 lds
v_mfma_f32_16x16x32_bf16 v[52:55], a[144:147], v[92:95], v[52:55]
v_mfma_f32_16x16x32_bf16 v[60:63], a[224:227], v[92:95], v[60:63]
ds_read_b128 v[92:95], v1 offset:19456
s_waitcnt lgkmcnt(4)
v_mfma_f32_16x16x32_bf16 v[40:43], a[68:71], v[96:99], v[40:43]
s_add_u32 m0, s61, 4096
s_add_u32 s51, s50, 4096
buffer_load_dwordx4 v0, s[40:43], s51 offen offset:0 sc1 lds
v_mfma_f32_16x16x32_bf16 v[48:51], a[148:151], v[96:99], v[48:51]
v_mfma_f32_16x16x32_bf16 v[56:59], a[228:231], v[96:99], v[56:59]
ds_read_b128 v[96:99], v1 offset:39936
s_waitcnt lgkmcnt(4)
v_mfma_f32_16x16x32_bf16 v[44:47], a[68:71], v[80:83], v[44:47]
buffer_load_dwordx4 v0, s[40:43], s51 offen offset:1024 sc1 lds
v_mfma_f32_16x16x32_bf16 v[52:55], a[148:151], v[80:83], v[52:55]
v_mfma_f32_16x16x32_bf16 v[60:63], a[228:231], v[80:83], v[60:63]
s_add_u32 s64, s64, 40960
s_cmp_ge_u32 s64, 122880
s_cselect_b32 s64, 0, s64
v_add_u32 v1, s64, v0
ds_read_b128 v[80:83], v1 offset:0
s_waitcnt lgkmcnt(4)
v_mfma_f32_16x16x32_bf16 v[40:43], a[72:75], v[84:87], v[40:43]
buffer_load_dwordx4 v0, s[40:43], s51 offen offset:2048 sc1 lds
v_mfma_f32_16x16x32_bf16 v[48:51], a[152:155], v[84:87], v[48:51]
v_mfma_f32_16x16x32_bf16 v[56:59], a[232:235], v[84:87], v[56:59]
ds_read_b128 v[84:87], v1 offset:20480
s_waitcnt lgkmcnt(4)
v_mfma_f32_16x16x32_bf16 v[44:47], a[72:75], v[88:91], v[44:47]
buffer_load_dwordx4 v0, s[40:43], s51 offen offset:3072 sc1 lds
v_mfma_f32_16x16x32_bf16 v[52:55], a[152:155], v[88:91], v[52:55]
v_mfma_f32_16x16x32_bf16 v[60:63], a[232:235], v[88:91], v[60:63]
ds_read_b128 v[88:91], v1 offset:1024
s_waitcnt lgkmcnt(4)
v_mfma_f32_16x16x32_bf16 v[40:43], a[76:79], v[92:95], v[40:43]
s_add_u32 m0, s61, 8192
s_add_u32 s51, s50, 8192
buffer_load_dwordx4 v0, s[40:43], s51 offen offset:0 sc1 lds
v_mfma_f32_16x16x32_bf16 v[48:51], a[156:159], v[92:95], v[48:51]
v_mfma_f32_16x16x32_bf16 v[56:59], a[236:239], v[92:95], v[56:59]
ds_read_b128 v[92:95], v1 offset:21504
s_waitcnt lgkmcnt(4)
v_mfma_f32_16x16x32_bf16 v[44:47], a[76:79], v[96:99], v[44:47]
buffer_load_dwordx4 v0, s[40:43], s51 offen offset:1024 sc1 lds
v_mfma_f32_16x16x32_bf16 v[52:55], a[156:159], v[96:99], v[52:55]
s_add_u32 s50, s50, 40960
s_add_u32 s61, s61, 40960
s_cmp_ge_u32 s61, 122880
s_cselect_b32 s51, 122880, 0
s_sub_u32 s61, s61, s51
v_mfma_f32_16x16x32_bf16 v[60:63], a[236:239], v[96:99], v[60:63]
ds_read_b128 v[96:99], v1 offset:2048
s_add_u32 s47, s47, 1
s_and_b32 s59, s47, 63
s_cbranch_scc1 hpl_loop_%=
s_waitcnt lgkmcnt(0)
v_cvt_pk_bf16_f32 v104, v40, v41
v_cvt_pk_bf16_f32 v105, v42, v43
v_cvt_pk_bf16_f32 v106, v48, v49
v_cvt_pk_bf16_f32 v107, v50, v51
ds_write_b128 v6, v[104:107] offset:12288
v_cvt_pk_bf16_f32 v108, v44, v45
v_cvt_pk_bf16_f32 v109, v46, v47
v_cvt_pk_bf16_f32 v110, v52, v53
v_cvt_pk_bf16_f32 v111, v54, v55
ds_write_b128 v6, v[108:111] offset:13312
v_cvt_pk_bf16_f32 v112, v56, v57
v_cvt_pk_bf16_f32 v113, v58, v59
v_cvt_pk_bf16_f32 v114, v60, v61
v_cvt_pk_bf16_f32 v115, v62, v63
ds_write2st64_b64 v7, v[112:113], v[114:115] offset0:24 offset1:26
s_waitcnt lgkmcnt(0)
s_barrier
s_branch hp_ret_%=
hp_roleo_%=:
v_and_b32 v1, 0x370, v0
v_bfe_u32 v4, v15, 3, 1
v_lshl_add_u32 v1, v4, 15, v1
s_lshr_b32 s48, s44, 1
s_lshl_b32 s48, s48, 10
s_and_b32 s49, s48, 0x2000
s_add_u32 s48, s48, s49
s_and_b32 s49, s44, 1
s_lshl_b32 s49, s49, 7
s_add_u32 s48, s48, s49
v_add_u32 v1, s48, v1
s_mul_i32 s62, s45, 655360
s_add_u32 s62, s62, 32964608
s_mov_b32 s63, s62
s_lshr_b32 s48, s47, 1
s_lshl_b32 s48, s48, 7
s_add_u32 s48, s48, 254976
v_mov_b32 v13, s48
v_mov_b32 v14, 0
s_lshl_b32 s48, s45, 11
v_add_u32 v8, s48, v0
s_lshr_b32 s48, s45, 1
s_lshl_b32 s48, s48, 11
s_and_b32 s49, s45, 1
s_lshl3_add_u32 s48, s49, s48
s_add_u32 s48, s48, 8192
v_add_u32 v9, s48, v0
s_mul_i32 s49, s45, 3072
s_mul_i32 s50, s44, 12288
s_add_u32 s50, s50, 20348928
s_add_u32 s50, s50, s49
s_lshl_b32 s48, s44, 7
s_add_u32 s49, s48, 2048
v_mov_b32 v7, s49
v_mov_b32 v6, 0
s_cmp_eq_u32 s45, 0
s_cselect_b32 s59, 1, 0
v_lshlrev_b32 v2, 7, v15
v_cmp_gt_u32 vcc, s59, v15
v_mov_b32 v4, 2147483632
s_nop 0
v_cndmask_b32 v2, v4, v2, vcc
v_mov_b32 v12, 1
s_add_u32 s61, s47, 64
s_waitcnt vmcnt(0)
s_add_u32 s55, s47, 1
s_mov_b32 s64, 0x40000000
v_cmp_gt_u32 vcc, s55, v6
s_cbranch_vccz hpo_v0_ok_%=
s_mov_b32 s60, 0
s_nop 2
hpo_v0_poll_%=:
buffer_load_dword v6, v7, s[40:43], s58 offen sc1
s_waitcnt vmcnt(0)
v_cmp_gt_u32 vcc, s55, v6
s_nop 4
s_cbranch_vccz hpo_v0_ok_%=
s_sleep 1
s_add_u32 s60, s60, 1
s_cmp_gt_u32 s60, 65536
s_cbranch_scc1 hp_abort_%=
s_and_b32 s59, s60, 63
s_cbranch_scc1 hpo_v0_poll_%=
v_mov_b32 v4, 1024
buffer_load_dword v4, v4, s[40:43], s58 offen sc1
s_waitcnt vmcnt(0)
v_readfirstlane_b32 s59, v4
s_nop 0
s_cmp_lg_u32 s59, 0
s_nop 2
s_cbranch_scc1 hp_end_%=
s_branch hpo_v0_poll_%=
hpo_v0_ok_%=:
buffer_load_dwordx4 v[144:147], v0, s[40:43], s50 offen offset:0 sc1
buffer_load_dwordx4 v[148:151], v0, s[40:43], s50 offen offset:1024 sc1
buffer_load_dwordx4 v[152:155], v0, s[40:43], s50 offen offset:2048 sc1
s_waitcnt vmcnt(2)
ds_write_b128 v8, v[144:147] offset:0
s_waitcnt vmcnt(1)
ds_write_b128 v8, v[148:151] offset:1024
s_waitcnt vmcnt(0)
ds_write2st64_b64 v9, v[152:153], v[154:155] offset0:0 offset1:2
s_waitcnt lgkmcnt(0)
s_barrier
ds_read_b128 v[16:19], v0 offset:0
ds_read_b128 v[20:23], v0 offset:1024
ds_read_b128 v[24:27], v0 offset:2048
ds_read_b128 v[28:31], v0 offset:3072
ds_read_b128 v[32:35], v0 offset:4096
ds_read_b128 v[36:39], v0 offset:5120
ds_read_b128 v[40:43], v0 offset:6144
ds_read_b128 v[44:47], v0 offset:7168
ds_read_b128 v[48:51], v0 offset:8192
ds_read_b128 v[52:55], v0 offset:9216
ds_read_b128 v[56:59], v0 offset:10240
ds_read_b128 v[60:63], v0 offset:11264
s_waitcnt lgkmcnt(0)
s_mov_b32 s53, 0x40000000
hpo_loop_%=:
s_bitcmp1_b32 s47, 5
s_cselect_b32 s48, 8, 0
v_cmp_gt_u32 vcc, s48, v14
s_cbranch_vccz hpo_free_ok_%=
s_mov_b32 s60, 0
s_nop 2
hpo_free_poll_%=:
buffer_load_dword v14, v13, s[40:43], s58 offen sc1
s_waitcnt vmcnt(0)
v_cmp_gt_u32 vcc, s48, v14
s_nop 4
s_cbranch_vccz hpo_free_ok_%=
s_sleep 8
s_add_u32 s60, s60, 1
s_cmp_gt_u32 s60, 65536
s_cbranch_scc1 hp_abort_%=
s_and_b32 s59, s60, 63
s_cbranch_scc1 hpo_free_poll_%=
v_mov_b32 v4, 1024
)"
R"(buffer_load_dword v4, v4, s[40:43], s58 offen sc1
s_waitcnt vmcnt(0)
v_readfirstlane_b32 s59, v4
s_nop 0
s_cmp_lg_u32 s59, 0
s_nop 2
s_cbranch_scc1 hp_end_%=
s_branch hpo_free_poll_%=
hpo_free_ok_%=:
v_add_u32 v13, 128, v13
s_cmp_lg_u32 s47, 12
s_cbranch_scc1 hpo_gate_ok_%=
v_add_u32 v3, 4096, v7
v_mov_b32 v11, 0
s_mov_b32 s48, 2
v_cmp_gt_u32 vcc, s48, v11
s_cbranch_vccz hpo_gate_ok_%=
s_mov_b32 s60, 0
s_nop 2
hpo_gate_poll_%=:
buffer_load_dword v11, v3, s[40:43], s58 offen sc1
s_waitcnt vmcnt(0)
v_cmp_gt_u32 vcc, s48, v11
s_nop 4
s_cbranch_vccz hpo_gate_ok_%=
s_sleep 8
s_add_u32 s60, s60, 1
s_cmp_gt_u32 s60, 65536
s_cbranch_scc1 hp_abort_%=
s_and_b32 s59, s60, 63
s_cbranch_scc1 hpo_gate_poll_%=
v_mov_b32 v4, 1024
buffer_load_dword v4, v4, s[40:43], s58 offen sc1
s_waitcnt vmcnt(0)
v_readfirstlane_b32 s59, v4
s_nop 0
s_cmp_lg_u32 s59, 0
s_nop 2
s_cbranch_scc1 hp_end_%=
s_branch hpo_gate_poll_%=
hpo_gate_ok_%=:
s_add_u32 s55, s47, 2
s_min_u32 s55, s55, s61
s_add_u32 s59, s47, 0
s_and_b32 s59, s59, 63
s_cmp_eq_u32 s59, 31
s_cselect_b32 s59, -31, 1
s_mul_i32 s57, s59, 393216
s_add_u32 s50, s50, s57
v_cmp_gt_u32 vcc, s55, v6
s_nop 0
s_cmp_eq_u64 vcc, 0
s_cselect_b32 s56, s50, 0x40000000
v_mfma_f32_16x16x32_bf16 v[112:115], v[16:19], a[0:3], 0
buffer_load_dwordx4 v[144:147], v0, s[40:43], s56 offen offset:0 sc1
v_mfma_f32_16x16x32_bf16 v[116:119], v[20:23], a[0:3], 0
buffer_load_dwordx4 v[148:151], v0, s[40:43], s56 offen offset:1024 sc1
v_mfma_f32_16x16x32_bf16 v[112:115], v[24:27], a[4:7], v[112:115]
buffer_load_dwordx4 v[152:155], v0, s[40:43], s56 offen offset:2048 sc1
v_mfma_f32_16x16x32_bf16 v[116:119], v[28:31], a[4:7], v[116:119]
buffer_load_dword v6, v7, s[40:43], s58 offen sc1
v_mfma_f32_16x16x32_bf16 v[112:115], v[32:35], a[8:11], v[112:115]
v_mfma_f32_16x16x32_bf16 v[116:119], v[36:39], a[8:11], v[116:119]
v_mfma_f32_16x16x32_bf16 v[112:115], v[40:43], a[12:15], v[112:115]
v_cvt_pk_bf16_f32 v140, v120, v121
v_mfma_f32_16x16x32_bf16 v[116:119], v[44:47], a[12:15], v[116:119]
v_cvt_pk_bf16_f32 v141, v122, v123
v_mfma_f32_16x16x32_bf16 v[112:115], v[48:51], a[16:19], v[112:115]
v_cvt_pk_bf16_f32 v142, v124, v125
v_mfma_f32_16x16x32_bf16 v[116:119], v[52:55], a[16:19], v[116:119]
v_cvt_pk_bf16_f32 v143, v126, v127
v_mfma_f32_16x16x32_bf16 v[112:115], v[56:59], a[20:23], v[112:115]
buffer_store_dwordx4 v[140:143], v1, s[40:43], s53 offen sc1
v_mfma_f32_16x16x32_bf16 v[116:119], v[60:63], a[20:23], v[116:119]
s_mov_b32 s52, s63
s_lshr_b32 s59, s52, 8
s_and_b32 s59, s59, 0x2000
s_add_u32 s53, s52, s59
v_mfma_f32_16x16x32_bf16 v[120:123], v[16:19], a[24:27], 0
v_mfma_f32_16x16x32_bf16 v[124:127], v[20:23], a[24:27], 0
v_mfma_f32_16x16x32_bf16 v[120:123], v[24:27], a[28:31], v[120:123]
v_mfma_f32_16x16x32_bf16 v[124:127], v[28:31], a[28:31], v[124:127]
v_mfma_f32_16x16x32_bf16 v[120:123], v[32:35], a[32:35], v[120:123]
v_mfma_f32_16x16x32_bf16 v[124:127], v[36:39], a[32:35], v[124:127]
v_mfma_f32_16x16x32_bf16 v[120:123], v[40:43], a[36:39], v[120:123]
v_cvt_pk_bf16_f32 v136, v112, v113
v_mfma_f32_16x16x32_bf16 v[124:127], v[44:47], a[36:39], v[124:127]
v_cvt_pk_bf16_f32 v137, v114, v115
v_mfma_f32_16x16x32_bf16 v[120:123], v[48:51], a[40:43], v[120:123]
v_cvt_pk_bf16_f32 v138, v116, v117
v_mfma_f32_16x16x32_bf16 v[124:127], v[52:55], a[40:43], v[124:127]
v_cvt_pk_bf16_f32 v139, v118, v119
v_mfma_f32_16x16x32_bf16 v[120:123], v[56:59], a[44:47], v[120:123]
buffer_store_dwordx4 v[136:139], v1, s[40:43], s53 offen sc1
v_mfma_f32_16x16x32_bf16 v[124:127], v[60:63], a[44:47], v[124:127]
s_add_u32 s52, s52, 65536
s_lshr_b32 s59, s52, 8
s_and_b32 s59, s59, 0x2000
s_add_u32 s53, s52, s59
v_mfma_f32_16x16x32_bf16 v[112:115], v[16:19], a[48:51], 0
v_mfma_f32_16x16x32_bf16 v[116:119], v[20:23], a[48:51], 0
v_mfma_f32_16x16x32_bf16 v[112:115], v[24:27], a[52:55], v[112:115]
v_mfma_f32_16x16x32_bf16 v[116:119], v[28:31], a[52:55], v[116:119]
v_mfma_f32_16x16x32_bf16 v[112:115], v[32:35], a[56:59], v[112:115]
v_mfma_f32_16x16x32_bf16 v[116:119], v[36:39], a[56:59], v[116:119]
v_mfma_f32_16x16x32_bf16 v[112:115], v[40:43], a[60:63], v[112:115]
v_cvt_pk_bf16_f32 v140, v120, v121
v_mfma_f32_16x16x32_bf16 v[116:119], v[44:47], a[60:63], v[116:119]
v_cvt_pk_bf16_f32 v141, v122, v123
v_mfma_f32_16x16x32_bf16 v[112:115], v[48:51], a[64:67], v[112:115]
v_cvt_pk_bf16_f32 v142, v124, v125
v_mfma_f32_16x16x32_bf16 v[116:119], v[52:55], a[64:67], v[116:119]
v_cvt_pk_bf16_f32 v143, v126, v127
v_mfma_f32_16x16x32_bf16 v[112:115], v[56:59], a[68:71], v[112:115]
buffer_store_dwordx4 v[140:143], v1, s[40:43], s53 offen sc1
v_mfma_f32_16x16x32_bf16 v[116:119], v[60:63], a[68:71], v[116:119]
s_add_u32 s52, s52, 65536
s_lshr_b32 s59, s52, 8
s_and_b32 s59, s59, 0x2000
s_add_u32 s53, s52, s59
v_mfma_f32_16x16x32_bf16 v[120:123], v[16:19], a[72:75], 0
v_mfma_f32_16x16x32_bf16 v[124:127], v[20:23], a[72:75], 0
v_mfma_f32_16x16x32_bf16 v[120:123], v[24:27], a[76:79], v[120:123]
v_mfma_f32_16x16x32_bf16 v[124:127], v[28:31], a[76:79], v[124:127]
v_mfma_f32_16x16x32_bf16 v[120:123], v[32:35], a[80:83], v[120:123]
v_mfma_f32_16x16x32_bf16 v[124:127], v[36:39], a[80:83], v[124:127]
v_mfma_f32_16x16x32_bf16 v[120:123], v[40:43], a[84:87], v[120:123]
v_cvt_pk_bf16_f32 v136, v112, v113
v_mfma_f32_16x16x32_bf16 v[124:127], v[44:47], a[84:87], v[124:127]
v_cvt_pk_bf16_f32 v137, v114, v115
v_mfma_f32_16x16x32_bf16 v[120:123], v[48:51], a[88:91], v[120:123]
v_cvt_pk_bf16_f32 v138, v116, v117
v_mfma_f32_16x16x32_bf16 v[124:127], v[52:55], a[88:91], v[124:127]
v_cvt_pk_bf16_f32 v139, v118, v119
v_mfma_f32_16x16x32_bf16 v[120:123], v[56:59], a[92:95], v[120:123]
buffer_store_dwordx4 v[136:139], v1, s[40:43], s53 offen sc1
v_mfma_f32_16x16x32_bf16 v[124:127], v[60:63], a[92:95], v[124:127]
s_add_u32 s52, s52, 65536
s_lshr_b32 s59, s52, 8
s_and_b32 s59, s59, 0x2000
s_add_u32 s53, s52, s59
v_mfma_f32_16x16x32_bf16 v[112:115], v[16:19], a[96:99], 0
v_mfma_f32_16x16x32_bf16 v[116:119], v[20:23], a[96:99], 0
v_mfma_f32_16x16x32_bf16 v[112:115], v[24:27], a[100:103], v[112:115]
v_mfma_f32_16x16x32_bf16 v[116:119], v[28:31], a[100:103], v[116:119]
v_mfma_f32_16x16x32_bf16 v[112:115], v[32:35], a[104:107], v[112:115]
v_mfma_f32_16x16x32_bf16 v[116:119], v[36:39], a[104:107], v[116:119]
v_mfma_f32_16x16x32_bf16 v[112:115], v[40:43], a[108:111], v[112:115]
v_cvt_pk_bf16_f32 v140, v120, v121
v_mfma_f32_16x16x32_bf16 v[116:119], v[44:47], a[108:111], v[116:119]
v_cvt_pk_bf16_f32 v141, v122, v123
v_mfma_f32_16x16x32_bf16 v[112:115], v[48:51], a[112:115], v[112:115]
v_cvt_pk_bf16_f32 v142, v124, v125
v_mfma_f32_16x16x32_bf16 v[116:119], v[52:55], a[112:115], v[116:119]
v_cvt_pk_bf16_f32 v143, v126, v127
v_mfma_f32_16x16x32_bf16 v[112:115], v[56:59], a[116:119], v[112:115]
buffer_store_dwordx4 v[140:143], v1, s[40:43], s53 offen sc1
v_mfma_f32_16x16x32_bf16 v[116:119], v[60:63], a[116:119], v[116:119]
s_add_u32 s52, s52, 65536
s_lshr_b32 s59, s52, 8
s_and_b32 s59, s59, 0x2000
s_add_u32 s53, s52, s59
v_mfma_f32_16x16x32_bf16 v[120:123], v[16:19], a[120:123], 0
v_mfma_f32_16x16x32_bf16 v[124:127], v[20:23], a[120:123], 0
v_mfma_f32_16x16x32_bf16 v[120:123], v[24:27], a[124:127], v[120:123]
v_mfma_f32_16x16x32_bf16 v[124:127], v[28:31], a[124:127], v[124:127]
v_mfma_f32_16x16x32_bf16 v[120:123], v[32:35], a[128:131], v[120:123]
v_mfma_f32_16x16x32_bf16 v[124:127], v[36:39], a[128:131], v[124:127]
v_mfma_f32_16x16x32_bf16 v[120:123], v[40:43], a[132:135], v[120:123]
v_cvt_pk_bf16_f32 v136, v112, v113
v_mfma_f32_16x16x32_bf16 v[124:127], v[44:47], a[132:135], v[124:127]
v_cvt_pk_bf16_f32 v137, v114, v115
v_mfma_f32_16x16x32_bf16 v[120:123], v[48:51], a[136:139], v[120:123]
v_cvt_pk_bf16_f32 v138, v116, v117
v_mfma_f32_16x16x32_bf16 v[124:127], v[52:55], a[136:139], v[124:127]
v_cvt_pk_bf16_f32 v139, v118, v119
v_mfma_f32_16x16x32_bf16 v[120:123], v[56:59], a[140:143], v[120:123]
buffer_store_dwordx4 v[136:139], v1, s[40:43], s53 offen sc1
v_mfma_f32_16x16x32_bf16 v[124:127], v[60:63], a[140:143], v[124:127]
s_add_u32 s52, s52, 65536
s_lshr_b32 s59, s52, 8
s_and_b32 s59, s59, 0x2000
s_add_u32 s53, s52, s59
v_mfma_f32_16x16x32_bf16 v[112:115], v[16:19], a[144:147], 0
v_mfma_f32_16x16x32_bf16 v[116:119], v[20:23], a[144:147], 0
v_mfma_f32_16x16x32_bf16 v[112:115], v[24:27], a[148:151], v[112:115]
v_mfma_f32_16x16x32_bf16 v[116:119], v[28:31], a[148:151], v[116:119]
v_mfma_f32_16x16x32_bf16 v[112:115], v[32:35], a[152:155], v[112:115]
v_mfma_f32_16x16x32_bf16 v[116:119], v[36:39], a[152:155], v[116:119]
v_mfma_f32_16x16x32_bf16 v[112:115], v[40:43], a[156:159], v[112:115]
v_cvt_pk_bf16_f32 v140, v120, v121
v_mfma_f32_16x16x32_bf16 v[116:119], v[44:47], a[156:159], v[116:119]
v_cvt_pk_bf16_f32 v141, v122, v123
v_mfma_f32_16x16x32_bf16 v[112:115], v[48:51], a[160:163], v[112:115]
v_cvt_pk_bf16_f32 v142, v124, v125
v_mfma_f32_16x16x32_bf16 v[116:119], v[52:55], a[160:163], v[116:119]
v_cvt_pk_bf16_f32 v143, v126, v127
v_mfma_f32_16x16x32_bf16 v[112:115], v[56:59], a[164:167], v[112:115]
buffer_store_dwordx4 v[140:143], v1, s[40:43], s53 offen sc1
v_mfma_f32_16x16x32_bf16 v[116:119], v[60:63], a[164:167], v[116:119]
)"
R"(s_add_u32 s52, s52, 65536
s_lshr_b32 s59, s52, 8
s_and_b32 s59, s59, 0x2000
s_add_u32 s53, s52, s59
s_waitcnt vmcnt(6)
s_cmp_eq_u32 s56, s50
s_cbranch_scc1 hpo_have0_%=
v_cmp_gt_u32 vcc, s55, v6
s_cbranch_vccz hpo_v10_ok_%=
s_mov_b32 s60, 0
s_nop 2
hpo_v10_poll_%=:
buffer_load_dword v6, v7, s[40:43], s58 offen sc1
s_waitcnt vmcnt(0)
v_cmp_gt_u32 vcc, s55, v6
s_nop 4
s_cbranch_vccz hpo_v10_ok_%=
s_sleep 2
s_add_u32 s60, s60, 1
s_cmp_gt_u32 s60, 65536
s_cbranch_scc1 hp_abort_%=
s_and_b32 s59, s60, 63
s_cbranch_scc1 hpo_v10_poll_%=
v_mov_b32 v4, 1024
buffer_load_dword v4, v4, s[40:43], s58 offen sc1
s_waitcnt vmcnt(0)
v_readfirstlane_b32 s59, v4
s_nop 0
s_cmp_lg_u32 s59, 0
s_nop 2
s_cbranch_scc1 hp_end_%=
s_branch hpo_v10_poll_%=
hpo_v10_ok_%=:
buffer_load_dwordx4 v[144:147], v0, s[40:43], s50 offen offset:0 sc1
buffer_load_dwordx4 v[148:151], v0, s[40:43], s50 offen offset:1024 sc1
buffer_load_dwordx4 v[152:155], v0, s[40:43], s50 offen offset:2048 sc1
s_waitcnt vmcnt(0)
hpo_have0_%=:
v_mfma_f32_16x16x32_bf16 v[120:123], v[16:19], a[168:171], 0
ds_write_b128 v8, v[144:147] offset:12288
v_mfma_f32_16x16x32_bf16 v[124:127], v[20:23], a[168:171], 0
ds_write_b128 v8, v[148:151] offset:13312
v_mfma_f32_16x16x32_bf16 v[120:123], v[24:27], a[172:175], v[120:123]
ds_write2st64_b64 v9, v[152:153], v[154:155] offset0:24 offset1:26
v_mfma_f32_16x16x32_bf16 v[124:127], v[28:31], a[172:175], v[124:127]
v_mfma_f32_16x16x32_bf16 v[120:123], v[32:35], a[176:179], v[120:123]
v_mfma_f32_16x16x32_bf16 v[124:127], v[36:39], a[176:179], v[124:127]
v_mfma_f32_16x16x32_bf16 v[120:123], v[40:43], a[180:183], v[120:123]
v_cvt_pk_bf16_f32 v136, v112, v113
v_mfma_f32_16x16x32_bf16 v[124:127], v[44:47], a[180:183], v[124:127]
v_cvt_pk_bf16_f32 v137, v114, v115
v_mfma_f32_16x16x32_bf16 v[120:123], v[48:51], a[184:187], v[120:123]
v_cvt_pk_bf16_f32 v138, v116, v117
v_mfma_f32_16x16x32_bf16 v[124:127], v[52:55], a[184:187], v[124:127]
v_cvt_pk_bf16_f32 v139, v118, v119
v_mfma_f32_16x16x32_bf16 v[120:123], v[56:59], a[188:191], v[120:123]
buffer_store_dwordx4 v[136:139], v1, s[40:43], s53 offen sc1
v_mfma_f32_16x16x32_bf16 v[124:127], v[60:63], a[188:191], v[124:127]
s_add_u32 s52, s52, 65536
s_lshr_b32 s59, s52, 8
s_and_b32 s59, s59, 0x2000
s_add_u32 s53, s52, s59
s_waitcnt lgkmcnt(0)
s_barrier
buffer_atomic_add v12, v2, s[40:43], s64 offen
v_mfma_f32_16x16x32_bf16 v[112:115], v[16:19], a[192:195], 0
ds_read_b128 v[64:67], v0 offset:12288
v_mfma_f32_16x16x32_bf16 v[116:119], v[20:23], a[192:195], 0
ds_read_b128 v[68:71], v0 offset:13312
v_mfma_f32_16x16x32_bf16 v[112:115], v[24:27], a[196:199], v[112:115]
ds_read_b128 v[72:75], v0 offset:14336
v_mfma_f32_16x16x32_bf16 v[116:119], v[28:31], a[196:199], v[116:119]
ds_read_b128 v[76:79], v0 offset:15360
v_mfma_f32_16x16x32_bf16 v[112:115], v[32:35], a[200:203], v[112:115]
ds_read_b128 v[80:83], v0 offset:16384
v_mfma_f32_16x16x32_bf16 v[116:119], v[36:39], a[200:203], v[116:119]
ds_read_b128 v[84:87], v0 offset:17408
v_mfma_f32_16x16x32_bf16 v[112:115], v[40:43], a[204:207], v[112:115]
v_cvt_pk_bf16_f32 v140, v120, v121
v_mfma_f32_16x16x32_bf16 v[116:119], v[44:47], a[204:207], v[116:119]
v_cvt_pk_bf16_f32 v141, v122, v123
v_mfma_f32_16x16x32_bf16 v[112:115], v[48:51], a[208:211], v[112:115]
v_cvt_pk_bf16_f32 v142, v124, v125
v_mfma_f32_16x16x32_bf16 v[116:119], v[52:55], a[208:211], v[116:119]
v_cvt_pk_bf16_f32 v143, v126, v127
v_mfma_f32_16x16x32_bf16 v[112:115], v[56:59], a[212:215], v[112:115]
buffer_store_dwordx4 v[140:143], v1, s[40:43], s53 offen sc1
v_mfma_f32_16x16x32_bf16 v[116:119], v[60:63], a[212:215], v[116:119]
s_add_u32 s52, s52, 65536
s_lshr_b32 s59, s52, 8
s_and_b32 s59, s59, 0x2000
s_add_u32 s53, s52, s59
v_mfma_f32_16x16x32_bf16 v[120:123], v[16:19], a[216:219], 0
ds_read_b128 v[88:91], v0 offset:18432
v_mfma_f32_16x16x32_bf16 v[124:127], v[20:23], a[216:219], 0
ds_read_b128 v[92:95], v0 offset:19456
v_mfma_f32_16x16x32_bf16 v[120:123], v[24:27], a[220:223], v[120:123]
ds_read_b128 v[96:99], v0 offset:20480
v_mfma_f32_16x16x32_bf16 v[124:127], v[28:31], a[220:223], v[124:127]
ds_read_b128 v[100:103], v0 offset:21504
v_mfma_f32_16x16x32_bf16 v[120:123], v[32:35], a[224:227], v[120:123]
ds_read_b128 v[104:107], v0 offset:22528
v_mfma_f32_16x16x32_bf16 v[124:127], v[36:39], a[224:227], v[124:127]
ds_read_b128 v[108:111], v0 offset:23552
v_mfma_f32_16x16x32_bf16 v[120:123], v[40:43], a[228:231], v[120:123]
v_cvt_pk_bf16_f32 v136, v112, v113
v_mfma_f32_16x16x32_bf16 v[124:127], v[44:47], a[228:231], v[124:127]
v_cvt_pk_bf16_f32 v137, v114, v115
v_mfma_f32_16x16x32_bf16 v[120:123], v[48:51], a[232:235], v[120:123]
v_cvt_pk_bf16_f32 v138, v116, v117
v_mfma_f32_16x16x32_bf16 v[124:127], v[52:55], a[232:235], v[124:127]
v_cvt_pk_bf16_f32 v139, v118, v119
v_mfma_f32_16x16x32_bf16 v[120:123], v[56:59], a[236:239], v[120:123]
buffer_store_dwordx4 v[136:139], v1, s[40:43], s53 offen sc1
v_mfma_f32_16x16x32_bf16 v[124:127], v[60:63], a[236:239], v[124:127]
s_add_u32 s52, s52, 65536
s_lshr_b32 s59, s52, 8
s_and_b32 s59, s59, 0x2000
s_add_u32 s53, s52, s59
s_waitcnt lgkmcnt(0)
s_add_u32 s63, s63, 2621440
s_and_b32 s59, s47, 31
s_cmp_eq_u32 s59, 31
s_cselect_b32 s63, s62, s63
s_lshl_b32 s64, s47, 7
s_add_u32 s64, s64, 2124032
s_and_b32 s57, s47, 63
s_sub_u32 s59, s57, 2
s_cmp_lt_u32 s59, 61
s_cbranch_scc1 hpo_late0_%=
v_cvt_pk_bf16_f32 v140, v120, v121
v_cvt_pk_bf16_f32 v141, v122, v123
v_cvt_pk_bf16_f32 v142, v124, v125
v_cvt_pk_bf16_f32 v143, v126, v127
buffer_store_dwordx4 v[140:143], v1, s[40:43], s53 offen sc1
s_waitcnt vmcnt(0)
s_barrier
buffer_atomic_add v12, v2, s[40:43], s64 offen
s_mov_b32 s64, 0x40000000
s_cmp_eq_u32 s57, 63
s_cbranch_scc1 hp_ret_%=
hpo_late0_%=:
s_add_u32 s47, s47, 1
s_add_u32 s55, s47, 2
s_min_u32 s55, s55, s61
s_add_u32 s59, s47, 0
s_and_b32 s59, s59, 63
s_cmp_eq_u32 s59, 31
s_cselect_b32 s59, -31, 1
s_mul_i32 s57, s59, 393216
s_add_u32 s50, s50, s57
v_cmp_gt_u32 vcc, s55, v6
s_nop 0
s_cmp_eq_u64 vcc, 0
s_cselect_b32 s56, s50, 0x40000000
v_mfma_f32_16x16x32_bf16 v[112:115], v[64:67], a[0:3], 0
buffer_load_dwordx4 v[144:147], v0, s[40:43], s56 offen offset:0 sc1
v_mfma_f32_16x16x32_bf16 v[116:119], v[68:71], a[0:3], 0
buffer_load_dwordx4 v[148:151], v0, s[40:43], s56 offen offset:1024 sc1
v_mfma_f32_16x16x32_bf16 v[112:115], v[72:75], a[4:7], v[112:115]
buffer_load_dwordx4 v[152:155], v0, s[40:43], s56 offen offset:2048 sc1
v_mfma_f32_16x16x32_bf16 v[116:119], v[76:79], a[4:7], v[116:119]
buffer_load_dword v6, v7, s[40:43], s58 offen sc1
v_mfma_f32_16x16x32_bf16 v[112:115], v[80:83], a[8:11], v[112:115]
buffer_load_dword v14, v13, s[40:43], s58 offen sc1
v_mfma_f32_16x16x32_bf16 v[116:119], v[84:87], a[8:11], v[116:119]
v_mfma_f32_16x16x32_bf16 v[112:115], v[88:91], a[12:15], v[112:115]
v_cvt_pk_bf16_f32 v140, v120, v121
v_mfma_f32_16x16x32_bf16 v[116:119], v[92:95], a[12:15], v[116:119]
v_cvt_pk_bf16_f32 v141, v122, v123
v_mfma_f32_16x16x32_bf16 v[112:115], v[96:99], a[16:19], v[112:115]
v_cvt_pk_bf16_f32 v142, v124, v125
v_mfma_f32_16x16x32_bf16 v[116:119], v[100:103], a[16:19], v[116:119]
v_cvt_pk_bf16_f32 v143, v126, v127
v_mfma_f32_16x16x32_bf16 v[112:115], v[104:107], a[20:23], v[112:115]
buffer_store_dwordx4 v[140:143], v1, s[40:43], s53 offen sc1
v_mfma_f32_16x16x32_bf16 v[116:119], v[108:111], a[20:23], v[116:119]
s_mov_b32 s52, s63
s_lshr_b32 s59, s52, 8
s_and_b32 s59, s59, 0x2000
s_add_u32 s53, s52, s59
v_mfma_f32_16x16x32_bf16 v[120:123], v[64:67], a[24:27], 0
v_mfma_f32_16x16x32_bf16 v[124:127], v[68:71], a[24:27], 0
v_mfma_f32_16x16x32_bf16 v[120:123], v[72:75], a[28:31], v[120:123]
v_mfma_f32_16x16x32_bf16 v[124:127], v[76:79], a[28:31], v[124:127]
v_mfma_f32_16x16x32_bf16 v[120:123], v[80:83], a[32:35], v[120:123]
v_mfma_f32_16x16x32_bf16 v[124:127], v[84:87], a[32:35], v[124:127]
v_mfma_f32_16x16x32_bf16 v[120:123], v[88:91], a[36:39], v[120:123]
v_cvt_pk_bf16_f32 v136, v112, v113
v_mfma_f32_16x16x32_bf16 v[124:127], v[92:95], a[36:39], v[124:127]
v_cvt_pk_bf16_f32 v137, v114, v115
v_mfma_f32_16x16x32_bf16 v[120:123], v[96:99], a[40:43], v[120:123]
v_cvt_pk_bf16_f32 v138, v116, v117
v_mfma_f32_16x16x32_bf16 v[124:127], v[100:103], a[40:43], v[124:127]
v_cvt_pk_bf16_f32 v139, v118, v119
v_mfma_f32_16x16x32_bf16 v[120:123], v[104:107], a[44:47], v[120:123]
buffer_store_dwordx4 v[136:139], v1, s[40:43], s53 offen sc1
v_mfma_f32_16x16x32_bf16 v[124:127], v[108:111], a[44:47], v[124:127]
s_add_u32 s52, s52, 65536
s_lshr_b32 s59, s52, 8
)"
R"(s_and_b32 s59, s59, 0x2000
s_add_u32 s53, s52, s59
v_mfma_f32_16x16x32_bf16 v[112:115], v[64:67], a[48:51], 0
v_mfma_f32_16x16x32_bf16 v[116:119], v[68:71], a[48:51], 0
v_mfma_f32_16x16x32_bf16 v[112:115], v[72:75], a[52:55], v[112:115]
v_mfma_f32_16x16x32_bf16 v[116:119], v[76:79], a[52:55], v[116:119]
v_mfma_f32_16x16x32_bf16 v[112:115], v[80:83], a[56:59], v[112:115]
v_mfma_f32_16x16x32_bf16 v[116:119], v[84:87], a[56:59], v[116:119]
v_mfma_f32_16x16x32_bf16 v[112:115], v[88:91], a[60:63], v[112:115]
v_cvt_pk_bf16_f32 v140, v120, v121
v_mfma_f32_16x16x32_bf16 v[116:119], v[92:95], a[60:63], v[116:119]
v_cvt_pk_bf16_f32 v141, v122, v123
v_mfma_f32_16x16x32_bf16 v[112:115], v[96:99], a[64:67], v[112:115]
v_cvt_pk_bf16_f32 v142, v124, v125
v_mfma_f32_16x16x32_bf16 v[116:119], v[100:103], a[64:67], v[116:119]
v_cvt_pk_bf16_f32 v143, v126, v127
v_mfma_f32_16x16x32_bf16 v[112:115], v[104:107], a[68:71], v[112:115]
buffer_store_dwordx4 v[140:143], v1, s[40:43], s53 offen sc1
v_mfma_f32_16x16x32_bf16 v[116:119], v[108:111], a[68:71], v[116:119]
s_add_u32 s52, s52, 65536
s_lshr_b32 s59, s52, 8
s_and_b32 s59, s59, 0x2000
s_add_u32 s53, s52, s59
v_mfma_f32_16x16x32_bf16 v[120:123], v[64:67], a[72:75], 0
v_mfma_f32_16x16x32_bf16 v[124:127], v[68:71], a[72:75], 0
v_mfma_f32_16x16x32_bf16 v[120:123], v[72:75], a[76:79], v[120:123]
v_mfma_f32_16x16x32_bf16 v[124:127], v[76:79], a[76:79], v[124:127]
v_mfma_f32_16x16x32_bf16 v[120:123], v[80:83], a[80:83], v[120:123]
v_mfma_f32_16x16x32_bf16 v[124:127], v[84:87], a[80:83], v[124:127]
v_mfma_f32_16x16x32_bf16 v[120:123], v[88:91], a[84:87], v[120:123]
v_cvt_pk_bf16_f32 v136, v112, v113
v_mfma_f32_16x16x32_bf16 v[124:127], v[92:95], a[84:87], v[124:127]
v_cvt_pk_bf16_f32 v137, v114, v115
v_mfma_f32_16x16x32_bf16 v[120:123], v[96:99], a[88:91], v[120:123]
v_cvt_pk_bf16_f32 v138, v116, v117
v_mfma_f32_16x16x32_bf16 v[124:127], v[100:103], a[88:91], v[124:127]
v_cvt_pk_bf16_f32 v139, v118, v119
v_mfma_f32_16x16x32_bf16 v[120:123], v[104:107], a[92:95], v[120:123]
buffer_store_dwordx4 v[136:139], v1, s[40:43], s53 offen sc1
v_mfma_f32_16x16x32_bf16 v[124:127], v[108:111], a[92:95], v[124:127]
s_add_u32 s52, s52, 65536
s_lshr_b32 s59, s52, 8
s_and_b32 s59, s59, 0x2000
s_add_u32 s53, s52, s59
v_mfma_f32_16x16x32_bf16 v[112:115], v[64:67], a[96:99], 0
v_mfma_f32_16x16x32_bf16 v[116:119], v[68:71], a[96:99], 0
v_mfma_f32_16x16x32_bf16 v[112:115], v[72:75], a[100:103], v[112:115]
v_mfma_f32_16x16x32_bf16 v[116:119], v[76:79], a[100:103], v[116:119]
v_mfma_f32_16x16x32_bf16 v[112:115], v[80:83], a[104:107], v[112:115]
v_mfma_f32_16x16x32_bf16 v[116:119], v[84:87], a[104:107], v[116:119]
v_mfma_f32_16x16x32_bf16 v[112:115], v[88:91], a[108:111], v[112:115]
v_cvt_pk_bf16_f32 v140, v120, v121
v_mfma_f32_16x16x32_bf16 v[116:119], v[92:95], a[108:111], v[116:119]
v_cvt_pk_bf16_f32 v141, v122, v123
v_mfma_f32_16x16x32_bf16 v[112:115], v[96:99], a[112:115], v[112:115]
v_cvt_pk_bf16_f32 v142, v124, v125
v_mfma_f32_16x16x32_bf16 v[116:119], v[100:103], a[112:115], v[116:119]
v_cvt_pk_bf16_f32 v143, v126, v127
v_mfma_f32_16x16x32_bf16 v[112:115], v[104:107], a[116:119], v[112:115]
buffer_store_dwordx4 v[140:143], v1, s[40:43], s53 offen sc1
v_mfma_f32_16x16x32_bf16 v[116:119], v[108:111], a[116:119], v[116:119]
s_add_u32 s52, s52, 65536
s_lshr_b32 s59, s52, 8
s_and_b32 s59, s59, 0x2000
s_add_u32 s53, s52, s59
v_mfma_f32_16x16x32_bf16 v[120:123], v[64:67], a[120:123], 0
v_mfma_f32_16x16x32_bf16 v[124:127], v[68:71], a[120:123], 0
v_mfma_f32_16x16x32_bf16 v[120:123], v[72:75], a[124:127], v[120:123]
v_mfma_f32_16x16x32_bf16 v[124:127], v[76:79], a[124:127], v[124:127]
v_mfma_f32_16x16x32_bf16 v[120:123], v[80:83], a[128:131], v[120:123]
v_mfma_f32_16x16x32_bf16 v[124:127], v[84:87], a[128:131], v[124:127]
v_mfma_f32_16x16x32_bf16 v[120:123], v[88:91], a[132:135], v[120:123]
v_cvt_pk_bf16_f32 v136, v112, v113
v_mfma_f32_16x16x32_bf16 v[124:127], v[92:95], a[132:135], v[124:127]
v_cvt_pk_bf16_f32 v137, v114, v115
v_mfma_f32_16x16x32_bf16 v[120:123], v[96:99], a[136:139], v[120:123]
v_cvt_pk_bf16_f32 v138, v116, v117
v_mfma_f32_16x16x32_bf16 v[124:127], v[100:103], a[136:139], v[124:127]
v_cvt_pk_bf16_f32 v139, v118, v119
v_mfma_f32_16x16x32_bf16 v[120:123], v[104:107], a[140:143], v[120:123]
buffer_store_dwordx4 v[136:139], v1, s[40:43], s53 offen sc1
v_mfma_f32_16x16x32_bf16 v[124:127], v[108:111], a[140:143], v[124:127]
s_add_u32 s52, s52, 65536
s_lshr_b32 s59, s52, 8
s_and_b32 s59, s59, 0x2000
s_add_u32 s53, s52, s59
v_mfma_f32_16x16x32_bf16 v[112:115], v[64:67], a[144:147], 0
v_mfma_f32_16x16x32_bf16 v[116:119], v[68:71], a[144:147], 0
v_mfma_f32_16x16x32_bf16 v[112:115], v[72:75], a[148:151], v[112:115]
v_mfma_f32_16x16x32_bf16 v[116:119], v[76:79], a[148:151], v[116:119]
v_mfma_f32_16x16x32_bf16 v[112:115], v[80:83], a[152:155], v[112:115]
v_mfma_f32_16x16x32_bf16 v[116:119], v[84:87], a[152:155], v[116:119]
v_mfma_f32_16x16x32_bf16 v[112:115], v[88:91], a[156:159], v[112:115]
v_cvt_pk_bf16_f32 v140, v120, v121
v_mfma_f32_16x16x32_bf16 v[116:119], v[92:95], a[156:159], v[116:119]
v_cvt_pk_bf16_f32 v141, v122, v123
v_mfma_f32_16x16x32_bf16 v[112:115], v[96:99], a[160:163], v[112:115]
v_cvt_pk_bf16_f32 v142, v124, v125
v_mfma_f32_16x16x32_bf16 v[116:119], v[100:103], a[160:163], v[116:119]
v_cvt_pk_bf16_f32 v143, v126, v127
v_mfma_f32_16x16x32_bf16 v[112:115], v[104:107], a[164:167], v[112:115]
buffer_store_dwordx4 v[140:143], v1, s[40:43], s53 offen sc1
v_mfma_f32_16x16x32_bf16 v[116:119], v[108:111], a[164:167], v[116:119]
s_add_u32 s52, s52, 65536
s_lshr_b32 s59, s52, 8
s_and_b32 s59, s59, 0x2000
s_add_u32 s53, s52, s59
s_waitcnt vmcnt(6)
s_cmp_eq_u32 s56, s50
s_cbranch_scc1 hpo_have1_%=
v_cmp_gt_u32 vcc, s55, v6
s_cbranch_vccz hpo_v11_ok_%=
s_mov_b32 s60, 0
s_nop 2
hpo_v11_poll_%=:
buffer_load_dword v6, v7, s[40:43], s58 offen sc1
s_waitcnt vmcnt(0)
v_cmp_gt_u32 vcc, s55, v6
s_nop 4
s_cbranch_vccz hpo_v11_ok_%=
s_sleep 2
s_add_u32 s60, s60, 1
s_cmp_gt_u32 s60, 65536
s_cbranch_scc1 hp_abort_%=
s_and_b32 s59, s60, 63
s_cbranch_scc1 hpo_v11_poll_%=
v_mov_b32 v4, 1024
buffer_load_dword v4, v4, s[40:43], s58 offen sc1
s_waitcnt vmcnt(0)
v_readfirstlane_b32 s59, v4
s_nop 0
s_cmp_lg_u32 s59, 0
s_nop 2
s_cbranch_scc1 hp_end_%=
s_branch hpo_v11_poll_%=
hpo_v11_ok_%=:
buffer_load_dwordx4 v[144:147], v0, s[40:43], s50 offen offset:0 sc1
buffer_load_dwordx4 v[148:151], v0, s[40:43], s50 offen offset:1024 sc1
buffer_load_dwordx4 v[152:155], v0, s[40:43], s50 offen offset:2048 sc1
s_waitcnt vmcnt(0)
hpo_have1_%=:
v_mfma_f32_16x16x32_bf16 v[120:123], v[64:67], a[168:171], 0
ds_write_b128 v8, v[144:147] offset:0
v_mfma_f32_16x16x32_bf16 v[124:127], v[68:71], a[168:171], 0
ds_write_b128 v8, v[148:151] offset:1024
v_mfma_f32_16x16x32_bf16 v[120:123], v[72:75], a[172:175], v[120:123]
ds_write2st64_b64 v9, v[152:153], v[154:155] offset0:0 offset1:2
v_mfma_f32_16x16x32_bf16 v[124:127], v[76:79], a[172:175], v[124:127]
v_mfma_f32_16x16x32_bf16 v[120:123], v[80:83], a[176:179], v[120:123]
v_mfma_f32_16x16x32_bf16 v[124:127], v[84:87], a[176:179], v[124:127]
v_mfma_f32_16x16x32_bf16 v[120:123], v[88:91], a[180:183], v[120:123]
v_cvt_pk_bf16_f32 v136, v112, v113
v_mfma_f32_16x16x32_bf16 v[124:127], v[92:95], a[180:183], v[124:127]
v_cvt_pk_bf16_f32 v137, v114, v115
v_mfma_f32_16x16x32_bf16 v[120:123], v[96:99], a[184:187], v[120:123]
v_cvt_pk_bf16_f32 v138, v116, v117
v_mfma_f32_16x16x32_bf16 v[124:127], v[100:103], a[184:187], v[124:127]
v_cvt_pk_bf16_f32 v139, v118, v119
v_mfma_f32_16x16x32_bf16 v[120:123], v[104:107], a[188:191], v[120:123]
buffer_store_dwordx4 v[136:139], v1, s[40:43], s53 offen sc1
v_mfma_f32_16x16x32_bf16 v[124:127], v[108:111], a[188:191], v[124:127]
s_add_u32 s52, s52, 65536
s_lshr_b32 s59, s52, 8
s_and_b32 s59, s59, 0x2000
s_add_u32 s53, s52, s59
s_waitcnt lgkmcnt(0)
s_barrier
buffer_atomic_add v12, v2, s[40:43], s64 offen
v_mfma_f32_16x16x32_bf16 v[112:115], v[64:67], a[192:195], 0
ds_read_b128 v[16:19], v0 offset:0
v_mfma_f32_16x16x32_bf16 v[116:119], v[68:71], a[192:195], 0
ds_read_b128 v[20:23], v0 offset:1024
v_mfma_f32_16x16x32_bf16 v[112:115], v[72:75], a[196:199], v[112:115]
ds_read_b128 v[24:27], v0 offset:2048
v_mfma_f32_16x16x32_bf16 v[116:119], v[76:79], a[196:199], v[116:119]
ds_read_b128 v[28:31], v0 offset:3072
v_mfma_f32_16x16x32_bf16 v[112:115], v[80:83], a[200:203], v[112:115]
ds_read_b128 v[32:35], v0 offset:4096
v_mfma_f32_16x16x32_bf16 v[116:119], v[84:87], a[200:203], v[116:119]
ds_read_b128 v[36:39], v0 offset:5120
v_mfma_f32_16x16x32_bf16 v[112:115], v[88:91], a[204:207], v[112:115]
v_cvt_pk_bf16_f32 v140, v120, v121
v_mfma_f32_16x16x32_bf16 v[116:119], v[92:95], a[204:207], v[116:119]
v_cvt_pk_bf16_f32 v141, v122, v123
v_mfma_f32_16x16x32_bf16 v[112:115], v[96:99], a[208:211], v[112:115]
v_cvt_pk_bf16_f32 v142, v124, v125
v_mfma_f32_16x16x32_bf16 v[116:119], v[100:103], a[208:211], v[116:119]
v_cvt_pk_bf16_f32 v143, v126, v127
v_mfma_f32_16x16x32_bf16 v[112:115], v[104:107], a[212:215], v[112:115]
buffer_store_dwordx4 v[140:143], v1, s[40:43], s53 offen sc1
v_mfma_f32_16x16x32_bf16 v[116:119], v[108:111], a[212:215], v[116:119]
s_add_u32 s52, s52, 65536
s_lshr_b32 s59, s52, 8
s_and_b32 s59, s59, 0x2000
s_add_u32 s53, s52, s59
v_mfma_f32_16x16x32_bf16 v[120:123], v[64:67], a[216:219], 0
ds_read_b128 v[40:43], v0 offset:6144
v_mfma_f32_16x16x32_bf16 v[124:127], v[68:71], a[216:219], 0
ds_read_b128 v[44:47], v0 offset:7168
v_mfma_f32_16x16x32_bf16 v[120:123], v[72:75], a[220:223], v[120:123]
)"
R"(ds_read_b128 v[48:51], v0 offset:8192
v_mfma_f32_16x16x32_bf16 v[124:127], v[76:79], a[220:223], v[124:127]
ds_read_b128 v[52:55], v0 offset:9216
v_mfma_f32_16x16x32_bf16 v[120:123], v[80:83], a[224:227], v[120:123]
ds_read_b128 v[56:59], v0 offset:10240
v_mfma_f32_16x16x32_bf16 v[124:127], v[84:87], a[224:227], v[124:127]
ds_read_b128 v[60:63], v0 offset:11264
v_mfma_f32_16x16x32_bf16 v[120:123], v[88:91], a[228:231], v[120:123]
v_cvt_pk_bf16_f32 v136, v112, v113
v_mfma_f32_16x16x32_bf16 v[124:127], v[92:95], a[228:231], v[124:127]
v_cvt_pk_bf16_f32 v137, v114, v115
v_mfma_f32_16x16x32_bf16 v[120:123], v[96:99], a[232:235], v[120:123]
v_cvt_pk_bf16_f32 v138, v116, v117
v_mfma_f32_16x16x32_bf16 v[124:127], v[100:103], a[232:235], v[124:127]
v_cvt_pk_bf16_f32 v139, v118, v119
v_mfma_f32_16x16x32_bf16 v[120:123], v[104:107], a[236:239], v[120:123]
buffer_store_dwordx4 v[136:139], v1, s[40:43], s53 offen sc1
v_mfma_f32_16x16x32_bf16 v[124:127], v[108:111], a[236:239], v[124:127]
s_add_u32 s52, s52, 65536
s_lshr_b32 s59, s52, 8
s_and_b32 s59, s59, 0x2000
s_add_u32 s53, s52, s59
s_waitcnt lgkmcnt(0)
s_add_u32 s63, s63, 2621440
s_and_b32 s59, s47, 31
s_cmp_eq_u32 s59, 31
s_cselect_b32 s63, s62, s63
s_lshl_b32 s64, s47, 7
s_add_u32 s64, s64, 2124032
s_and_b32 s57, s47, 63
s_sub_u32 s59, s57, 2
s_cmp_lt_u32 s59, 61
s_cbranch_scc1 hpo_late1_%=
v_cvt_pk_bf16_f32 v140, v120, v121
v_cvt_pk_bf16_f32 v141, v122, v123
v_cvt_pk_bf16_f32 v142, v124, v125
v_cvt_pk_bf16_f32 v143, v126, v127
buffer_store_dwordx4 v[140:143], v1, s[40:43], s53 offen sc1
s_waitcnt vmcnt(0)
s_barrier
buffer_atomic_add v12, v2, s[40:43], s64 offen
s_mov_b32 s64, 0x40000000
s_cmp_eq_u32 s57, 63
s_cbranch_scc1 hp_ret_%=
hpo_late1_%=:
s_add_u32 s47, s47, 1
s_branch hpo_loop_%=
s_branch hp_ret_%=
hp_rolet_%=:
s_lshr_b32 s61, s44, 2
s_and_b32 s48, s44, 3
s_add_u32 s47, s47, s48
v_and_b32 v4, 0x370, v0
v_bfe_u32 v5, v15, 3, 1
v_lshl_add_u32 v1, v5, 15, v4
v_lshl_add_u32 v2, v5, 12, v4
s_cmp_eq_u32 s45, 0
s_cselect_b32 s59, 1, 0
v_lshlrev_b32 v3, 7, v15
v_cmp_gt_u32 vcc, s59, v15
v_mov_b32 v4, 2147483632
s_nop 0
v_cndmask_b32 v3, v4, v3, vcc
v_mov_b32 v6, 1
s_mul_i32 s48, s45, 20480
v_add_u32 v10, s48, v0
s_add_u32 s88, s45, 0
s_min_u32 s88, s88, 9
s_lshl_b32 s48, s88, 11
v_add_u32 v11, s48, v0
s_cmp_ge_u32 s88, 5
s_cselect_b32 s48, 15, 0
s_add_u32 s88, s88, s48
s_mul_i32 s48, s61, 5
s_add_u32 s88, s88, s48
s_add_u32 s89, s45, 4
s_min_u32 s89, s89, 9
s_lshl_b32 s48, s89, 11
v_add_u32 v12, s48, v0
s_cmp_ge_u32 s89, 5
s_cselect_b32 s48, 15, 0
s_add_u32 s89, s89, s48
s_mul_i32 s48, s61, 5
s_add_u32 s89, s89, s48
s_add_u32 s90, s45, 8
s_min_u32 s90, s90, 9
s_lshl_b32 s48, s90, 11
v_add_u32 v13, s48, v0
s_cmp_ge_u32 s90, 5
s_cselect_b32 s48, 15, 0
s_add_u32 s90, s90, s48
s_mul_i32 s48, s61, 5
s_add_u32 s90, s90, s48
v_lshlrev_b32 v7, 7, v15
s_lshl_b32 s48, s61, 7
v_add_u32 v8, s48, v7
v_cmp_eq_u32 vcc, 8, v15
s_nop 1
v_cndmask_b32 v7, v7, v8, vcc
v_mov_b32 v8, 8
v_mov_b32 v9, 20
v_cndmask_b32 v8, v8, v9, vcc
v_cmp_gt_u32 vcc, 9, v15
s_nop 1
v_cndmask_b32 v8, 0, v8, vcc
v_mov_b32 v4, 2147483632
v_cndmask_b32 v7, v4, v7, vcc
v_mov_b32 v9, 32
v_cmp_eq_u32 vcc, 0, v15
s_nop 1
v_cndmask_b32 v9, 0, v9, vcc
s_mov_b32 s63, s47
s_and_b32 s47, s63, 63
s_lshl_b32 s54, s63, 7
s_add_u32 s54, s54, 2124032
s_cmp_lg_u32 s45, 0
s_cbranch_scc1 hpt_u0_ok_%=
s_mov_b32 s60, 0
hpt_u0_%=:
v_mov_b32 v4, 0
buffer_atomic_add v4, v3, s[40:43], s54 offen sc0
s_waitcnt vmcnt(0)
v_cmp_gt_u32 vcc, v9, v4
s_nop 4
s_cbranch_vccz hpt_u0_ok_%=
s_sleep 4
s_add_u32 s60, s60, 1
s_cmp_gt_u32 s60, 65536
s_cbranch_scc1 hp_abort_%=
s_and_b32 s59, s60, 63
s_cbranch_scc1 hpt_u0_%=
v_mov_b32 v4, 1024
buffer_load_dword v4, v4, s[40:43], s58 offen sc1
s_waitcnt vmcnt(0)
v_readfirstlane_b32 s59, v4
s_nop 0
s_cmp_lg_u32 s59, 0
s_nop 2
s_cbranch_scc1 hp_end_%=
s_branch hpt_u0_%=
hpt_u0_ok_%=:
s_barrier
s_mov_b32 s64, 0
s_add_u32 s50, s47, 0
s_and_b32 s50, s50, 31
s_mul_i32 s50, s50, 80
s_mul_i32 s48, s61, 20
s_add_u32 s50, s50, s48
s_lshl_b32 s50, s50, 15
s_lshl_b32 s48, s45, 12
s_and_b32 s49, s48, 0x2000
s_add_u32 s48, s48, s49
s_add_u32 s50, s50, s48
s_add_u32 s50, s50, 32964608
s_or_b32 s50, s50, s64
s_lshr_b32 s59, s50, 8
s_and_b32 s59, s59, 0x2000
s_add_u32 s30, s50, s59
s_add_u32 s50, s50, 65536
s_lshr_b32 s59, s50, 8
s_and_b32 s59, s59, 0x2000
s_add_u32 s31, s50, s59
s_add_u32 s50, s50, 65536
s_lshr_b32 s59, s50, 8
s_and_b32 s59, s59, 0x2000
s_add_u32 s34, s50, s59
s_add_u32 s50, s50, 65536
s_lshr_b32 s59, s50, 8
s_and_b32 s59, s59, 0x2000
s_add_u32 s35, s50, s59
s_add_u32 s50, s50, 65536
s_lshr_b32 s59, s50, 8
s_and_b32 s59, s59, 0x2000
s_add_u32 s36, s50, s59
s_add_u32 s50, s50, 65536
s_lshr_b32 s59, s50, 8
s_and_b32 s59, s59, 0x2000
s_add_u32 s37, s50, s59
s_add_u32 s50, s50, 65536
s_lshr_b32 s59, s50, 8
s_and_b32 s59, s59, 0x2000
s_add_u32 s38, s50, s59
s_add_u32 s50, s50, 65536
s_lshr_b32 s59, s50, 8
s_and_b32 s59, s59, 0x2000
s_add_u32 s39, s50, s59
s_add_u32 s50, s50, 65536
s_lshr_b32 s59, s50, 8
s_and_b32 s59, s59, 0x2000
s_add_u32 s96, s50, s59
s_add_u32 s50, s50, 65536
s_lshr_b32 s59, s50, 8
s_and_b32 s59, s59, 0x2000
s_add_u32 s97, s50, s59
s_add_u32 s50, s50, 65536
buffer_load_dwordx4 a[0:3], v1, s[40:43], s30 offen sc1
buffer_load_dwordx4 a[4:7], v1, s[40:43], s31 offen sc1
buffer_load_dwordx4 a[8:11], v1, s[40:43], s34 offen sc1
buffer_load_dwordx4 a[12:15], v1, s[40:43], s35 offen sc1
buffer_load_dwordx4 a[16:19], v1, s[40:43], s36 offen sc1
)"
R"(buffer_load_dwordx4 a[20:23], v1, s[40:43], s37 offen sc1
buffer_load_dwordx4 a[24:27], v1, s[40:43], s38 offen sc1
buffer_load_dwordx4 a[28:31], v1, s[40:43], s39 offen sc1
buffer_load_dwordx4 a[32:35], v1, s[40:43], s96 offen sc1
buffer_load_dwordx4 a[36:39], v1, s[40:43], s97 offen sc1
buffer_load_dwordx4 a[40:43], v1, s[40:43], s30 offen offset:128 sc1
buffer_load_dwordx4 a[44:47], v1, s[40:43], s31 offen offset:128 sc1
buffer_load_dwordx4 a[48:51], v1, s[40:43], s34 offen offset:128 sc1
buffer_load_dwordx4 a[52:55], v1, s[40:43], s35 offen offset:128 sc1
buffer_load_dwordx4 a[56:59], v1, s[40:43], s36 offen offset:128 sc1
buffer_load_dwordx4 a[60:63], v1, s[40:43], s37 offen offset:128 sc1
buffer_load_dwordx4 a[64:67], v1, s[40:43], s38 offen offset:128 sc1
buffer_load_dwordx4 a[68:71], v1, s[40:43], s39 offen offset:128 sc1
buffer_load_dwordx4 a[72:75], v1, s[40:43], s96 offen offset:128 sc1
buffer_load_dwordx4 a[76:79], v1, s[40:43], s97 offen offset:128 sc1
buffer_load_dwordx4 a[80:83], v1, s[40:43], s30 offen offset:1024 sc1
buffer_load_dwordx4 a[84:87], v1, s[40:43], s31 offen offset:1024 sc1
buffer_load_dwordx4 a[88:91], v1, s[40:43], s34 offen offset:1024 sc1
buffer_load_dwordx4 a[92:95], v1, s[40:43], s35 offen offset:1024 sc1
buffer_load_dwordx4 a[96:99], v1, s[40:43], s36 offen offset:1024 sc1
buffer_load_dwordx4 a[100:103], v1, s[40:43], s37 offen offset:1024 sc1
buffer_load_dwordx4 a[104:107], v1, s[40:43], s38 offen offset:1024 sc1
buffer_load_dwordx4 a[108:111], v1, s[40:43], s39 offen offset:1024 sc1
buffer_load_dwordx4 a[112:115], v1, s[40:43], s96 offen offset:1024 sc1
buffer_load_dwordx4 a[116:119], v1, s[40:43], s97 offen offset:1024 sc1
buffer_load_dwordx4 a[120:123], v1, s[40:43], s30 offen offset:1152 sc1
buffer_load_dwordx4 a[124:127], v1, s[40:43], s31 offen offset:1152 sc1
buffer_load_dwordx4 a[128:131], v1, s[40:43], s34 offen offset:1152 sc1
buffer_load_dwordx4 a[132:135], v1, s[40:43], s35 offen offset:1152 sc1
buffer_load_dwordx4 a[136:139], v1, s[40:43], s36 offen offset:1152 sc1
buffer_load_dwordx4 a[140:143], v1, s[40:43], s37 offen offset:1152 sc1
buffer_load_dwordx4 a[144:147], v1, s[40:43], s38 offen offset:1152 sc1
buffer_load_dwordx4 a[148:151], v1, s[40:43], s39 offen offset:1152 sc1
buffer_load_dwordx4 a[152:155], v1, s[40:43], s96 offen offset:1152 sc1
buffer_load_dwordx4 a[156:159], v1, s[40:43], s97 offen offset:1152 sc1
buffer_load_dwordx4 a[160:163], v1, s[40:43], s30 offen offset:2048 sc1
buffer_load_dwordx4 a[164:167], v1, s[40:43], s31 offen offset:2048 sc1
buffer_load_dwordx4 a[168:171], v1, s[40:43], s34 offen offset:2048 sc1
buffer_load_dwordx4 a[172:175], v1, s[40:43], s35 offen offset:2048 sc1
buffer_load_dwordx4 a[176:179], v1, s[40:43], s36 offen offset:2048 sc1
buffer_load_dwordx4 a[180:183], v1, s[40:43], s37 offen offset:2048 sc1
buffer_load_dwordx4 a[184:187], v1, s[40:43], s38 offen offset:2048 sc1
buffer_load_dwordx4 a[188:191], v1, s[40:43], s39 offen offset:2048 sc1
buffer_load_dwordx4 a[192:195], v1, s[40:43], s96 offen offset:2048 sc1
buffer_load_dwordx4 a[196:199], v1, s[40:43], s97 offen offset:2048 sc1
buffer_load_dwordx4 a[200:203], v1, s[40:43], s30 offen offset:2176 sc1
buffer_load_dwordx4 a[204:207], v1, s[40:43], s31 offen offset:2176 sc1
buffer_load_dwordx4 a[208:211], v1, s[40:43], s34 offen offset:2176 sc1
buffer_load_dwordx4 a[212:215], v1, s[40:43], s35 offen offset:2176 sc1
buffer_load_dwordx4 a[216:219], v1, s[40:43], s36 offen offset:2176 sc1
buffer_load_dwordx4 a[220:223], v1, s[40:43], s37 offen offset:2176 sc1
buffer_load_dwordx4 a[224:227], v1, s[40:43], s38 offen offset:2176 sc1
buffer_load_dwordx4 a[228:231], v1, s[40:43], s39 offen offset:2176 sc1
buffer_load_dwordx4 a[232:235], v1, s[40:43], s96 offen offset:2176 sc1
buffer_load_dwordx4 a[236:239], v1, s[40:43], s97 offen offset:2176 sc1
buffer_load_dwordx4 a[240:243], v1, s[40:43], s30 offen offset:3072 sc1
buffer_load_dwordx4 a[244:247], v1, s[40:43], s31 offen offset:3072 sc1
buffer_load_dwordx4 a[248:251], v1, s[40:43], s34 offen offset:3072 sc1
s_waitcnt vmcnt(61)
buffer_load_dwordx4 a[252:255], v1, s[40:43], s35 offen offset:3072 sc1
buffer_load_dwordx4 v[192:195], v1, s[40:43], s36 offen offset:3072 sc1
s_waitcnt vmcnt(61)
buffer_load_dwordx4 v[196:199], v1, s[40:43], s37 offen offset:3072 sc1
buffer_load_dwordx4 v[200:203], v1, s[40:43], s38 offen offset:3072 sc1
s_waitcnt vmcnt(61)
buffer_load_dwordx4 v[204:207], v1, s[40:43], s39 offen offset:3072 sc1
buffer_load_dwordx4 v[208:211], v1, s[40:43], s96 offen offset:3072 sc1
s_waitcnt vmcnt(61)
buffer_load_dwordx4 v[212:215], v1, s[40:43], s97 offen offset:3072 sc1
buffer_load_dwordx4 v[216:219], v1, s[40:43], s30 offen offset:3200 sc1
s_waitcnt vmcnt(61)
buffer_load_dwordx4 v[220:223], v1, s[40:43], s31 offen offset:3200 sc1
buffer_load_dwordx4 v[224:227], v1, s[40:43], s34 offen offset:3200 sc1
s_waitcnt vmcnt(61)
buffer_load_dwordx4 v[228:231], v1, s[40:43], s35 offen offset:3200 sc1
buffer_load_dwordx4 v[232:235], v1, s[40:43], s36 offen offset:3200 sc1
s_waitcnt vmcnt(61)
buffer_load_dwordx4 v[236:239], v1, s[40:43], s37 offen offset:3200 sc1
buffer_load_dwordx4 v[240:243], v1, s[40:43], s38 offen offset:3200 sc1
s_waitcnt vmcnt(61)
buffer_load_dwordx4 v[244:247], v1, s[40:43], s39 offen offset:3200 sc1
buffer_load_dwordx4 v[248:251], v1, s[40:43], s96 offen offset:3200 sc1
s_waitcnt vmcnt(61)
buffer_load_dwordx4 v[252:255], v1, s[40:43], s97 offen offset:3200 sc1
hpt_loop_%=:
s_and_b32 s47, s63, 63
s_cmp_lt_u32 s47, 52
s_cselect_b32 s48, 4, 0
s_cselect_b32 s64, 0, 0x40000000
s_add_u32 s54, s63, s48
s_lshl_b32 s54, s54, 7
s_add_u32 s54, s54, 2124032
s_cmp_lg_u32 s45, 0
s_cbranch_scc1 hpt_ux_ok_%=
s_mov_b32 s60, 0
hpt_ux_%=:
v_mov_b32 v4, 0
buffer_atomic_add v4, v3, s[40:43], s54 offen sc0
s_waitcnt vmcnt(0)
v_cmp_gt_u32 vcc, v9, v4
s_nop 4
s_cbranch_vccz hpt_ux_ok_%=
s_sleep 4
s_add_u32 s60, s60, 1
s_cmp_gt_u32 s60, 65536
s_cbranch_scc1 hp_abort_%=
s_and_b32 s59, s60, 63
s_cbranch_scc1 hpt_ux_%=
v_mov_b32 v4, 1024
buffer_load_dword v4, v4, s[40:43], s58 offen sc1
s_waitcnt vmcnt(0)
v_readfirstlane_b32 s59, v4
s_nop 0
s_cmp_lg_u32 s59, 0
s_nop 2
s_cbranch_scc1 hp_end_%=
s_branch hpt_ux_%=
hpt_ux_ok_%=:
s_barrier
s_add_u32 s50, s47, 4
s_and_b32 s50, s50, 31
s_mul_i32 s50, s50, 80
s_mul_i32 s48, s61, 20
s_add_u32 s50, s50, s48
s_lshl_b32 s50, s50, 15
s_lshl_b32 s48, s45, 12
s_and_b32 s49, s48, 0x2000
s_add_u32 s48, s48, s49
s_add_u32 s50, s50, s48
s_add_u32 s50, s50, 32964608
s_or_b32 s50, s50, s64
s_lshr_b32 s59, s50, 8
s_and_b32 s59, s59, 0x2000
s_add_u32 s30, s50, s59
s_add_u32 s50, s50, 65536
s_lshr_b32 s59, s50, 8
s_and_b32 s59, s59, 0x2000
s_add_u32 s31, s50, s59
s_add_u32 s50, s50, 65536
s_lshr_b32 s59, s50, 8
s_and_b32 s59, s59, 0x2000
s_add_u32 s34, s50, s59
s_add_u32 s50, s50, 65536
s_lshr_b32 s59, s50, 8
s_and_b32 s59, s59, 0x2000
s_add_u32 s35, s50, s59
s_add_u32 s50, s50, 65536
s_lshr_b32 s59, s50, 8
s_and_b32 s59, s59, 0x2000
s_add_u32 s36, s50, s59
s_add_u32 s50, s50, 65536
s_lshr_b32 s59, s50, 8
s_and_b32 s59, s59, 0x2000
s_add_u32 s37, s50, s59
s_add_u32 s50, s50, 65536
s_lshr_b32 s59, s50, 8
s_and_b32 s59, s59, 0x2000
s_add_u32 s38, s50, s59
s_add_u32 s50, s50, 65536
s_lshr_b32 s59, s50, 8
s_and_b32 s59, s59, 0x2000
s_add_u32 s39, s50, s59
s_add_u32 s50, s50, 65536
s_lshr_b32 s59, s50, 8
s_and_b32 s59, s59, 0x2000
s_add_u32 s96, s50, s59
s_add_u32 s50, s50, 65536
s_lshr_b32 s59, s50, 8
s_and_b32 s59, s59, 0x2000
s_add_u32 s97, s50, s59
s_add_u32 s50, s50, 65536
s_and_b32 s65, s47, 31
s_mul_i32 s62, s65, 80
s_mul_i32 s48, s61, 20
s_add_u32 s62, s62, s48
s_mul_i32 s54, s63, 1536
s_add_u32 s54, s54, 2157568
s_cmp_lg_u32 s45, 0
s_cbranch_scc1 hpt_pt_ok_%=
s_mov_b32 s60, 0
hpt_pt_%=:
v_mov_b32 v4, 0
buffer_atomic_add v4, v7, s[40:43], s54 offen sc0
s_waitcnt vmcnt(0)
v_cmp_gt_u32 vcc, v8, v4
s_nop 4
s_cbranch_vccz hpt_pt_ok_%=
s_sleep 4
s_add_u32 s60, s60, 1
s_cmp_gt_u32 s60, 65536
s_cbranch_scc1 hp_abort_%=
s_and_b32 s59, s60, 63
s_cbranch_scc1 hpt_pt_%=
v_mov_b32 v4, 1024
buffer_load_dword v4, v4, s[40:43], s58 offen sc1
s_waitcnt vmcnt(0)
v_readfirstlane_b32 s59, v4
s_nop 0
s_cmp_lg_u32 s59, 0
s_nop 2
)"
R"(s_cbranch_scc1 hp_end_%=
s_branch hpt_pt_%=
hpt_pt_ok_%=:
s_barrier
s_lshl_b32 s50, s65, 5
s_lshl_b32 s48, s45, 3
s_add_u32 s50, s50, s48
s_lshl_b32 s50, s50, 11
s_add_u32 s50, s50, 130482176
buffer_load_dwordx4 v[32:35], v0, s[40:43], s50 offen sc1
buffer_load_dwordx4 v[36:39], v0, s[40:43], s50 offen offset:1024 sc1
buffer_load_dwordx4 v[40:43], v0, s[40:43], s50 offen offset:2048 sc1
buffer_load_dwordx4 v[44:47], v0, s[40:43], s50 offen offset:3072 sc1
s_add_u32 s50, s50, 4096
buffer_load_dwordx4 v[48:51], v0, s[40:43], s50 offen sc1
buffer_load_dwordx4 v[52:55], v0, s[40:43], s50 offen offset:1024 sc1
buffer_load_dwordx4 v[56:59], v0, s[40:43], s50 offen offset:2048 sc1
buffer_load_dwordx4 v[60:63], v0, s[40:43], s50 offen offset:3072 sc1
s_add_u32 s50, s50, 4096
buffer_load_dwordx4 v[64:67], v0, s[40:43], s50 offen sc1
buffer_load_dwordx4 v[68:71], v0, s[40:43], s50 offen offset:1024 sc1
buffer_load_dwordx4 v[72:75], v0, s[40:43], s50 offen offset:2048 sc1
buffer_load_dwordx4 v[76:79], v0, s[40:43], s50 offen offset:3072 sc1
s_add_u32 s50, s50, 4096
buffer_load_dwordx4 v[80:83], v0, s[40:43], s50 offen sc1
buffer_load_dwordx4 v[84:87], v0, s[40:43], s50 offen offset:1024 sc1
buffer_load_dwordx4 v[88:91], v0, s[40:43], s50 offen offset:2048 sc1
buffer_load_dwordx4 v[92:95], v0, s[40:43], s50 offen offset:3072 sc1
s_lshl_b32 s50, s65, 14
s_lshl_b32 s48, s45, 11
s_add_u32 s50, s50, s48
s_add_u32 s50, s50, 132579328
s_add_u32 s51, s50, 8192
buffer_load_dwordx4 v[96:99], v0, s[40:43], s50 offen sc1
buffer_load_dwordx4 v[100:103], v0, s[40:43], s51 offen sc1
buffer_load_dwordx4 v[104:107], v0, s[40:43], s50 offen offset:1024 sc1
buffer_load_dwordx4 v[108:111], v0, s[40:43], s51 offen offset:1024 sc1
s_lshl_b32 s50, s62, 12
s_lshl_b32 s48, s45, 10
s_add_u32 s50, s50, s48
s_add_u32 s50, s50, 116850688
s_add_u32 s76, s50, 0
s_add_u32 s77, s50, 8192
s_add_u32 s78, s50, 16384
s_add_u32 s79, s50, 24576
s_add_u32 s80, s50, 32768
s_add_u32 s81, s50, 40960
s_add_u32 s82, s50, 49152
s_add_u32 s83, s50, 57344
s_add_u32 s84, s50, 65536
s_add_u32 s85, s50, 73728
s_mul_i32 s48, s47, 40
s_add_u32 s48, s48, s88
s_lshl_b32 s12, s48, 11
s_add_u32 s15, s12, 133103616
s_mul_i32 s48, s47, 40
s_add_u32 s48, s48, s89
s_lshl_b32 s13, s48, 11
s_add_u32 s16, s13, 133103616
s_mul_i32 s48, s47, 40
s_add_u32 s48, s48, s90
s_lshl_b32 s14, s48, 11
s_add_u32 s17, s14, 133103616
s_waitcnt vmcnt(19)
v_mfma_f32_16x16x32_bf16 v[112:115], a[0:3], v[32:35], 0
s_waitcnt vmcnt(18)
v_mfma_f32_16x16x32_bf16 v[152:155], a[0:3], v[36:39], 0
buffer_load_dwordx4 a[0:3], v2, s[40:43], s76 offen sc1
v_mfma_f32_16x16x32_bf16 v[116:119], a[4:7], v[32:35], 0
v_mfma_f32_16x16x32_bf16 v[156:159], a[4:7], v[36:39], 0
buffer_load_dwordx4 a[4:7], v2, s[40:43], s77 offen sc1
v_mfma_f32_16x16x32_bf16 v[120:123], a[8:11], v[32:35], 0
v_mfma_f32_16x16x32_bf16 v[160:163], a[8:11], v[36:39], 0
buffer_load_dwordx4 a[8:11], v2, s[40:43], s78 offen sc1
v_mfma_f32_16x16x32_bf16 v[124:127], a[12:15], v[32:35], 0
v_mfma_f32_16x16x32_bf16 v[164:167], a[12:15], v[36:39], 0
buffer_load_dwordx4 a[12:15], v2, s[40:43], s79 offen sc1
v_mfma_f32_16x16x32_bf16 v[128:131], a[16:19], v[32:35], 0
v_mfma_f32_16x16x32_bf16 v[168:171], a[16:19], v[36:39], 0
buffer_load_dwordx4 a[16:19], v2, s[40:43], s80 offen sc1
v_mfma_f32_16x16x32_bf16 v[132:135], a[20:23], v[32:35], 0
v_mfma_f32_16x16x32_bf16 v[172:175], a[20:23], v[36:39], 0
buffer_load_dwordx4 a[20:23], v2, s[40:43], s81 offen sc1
v_mfma_f32_16x16x32_bf16 v[136:139], a[24:27], v[32:35], 0
v_mfma_f32_16x16x32_bf16 v[176:179], a[24:27], v[36:39], 0
buffer_load_dwordx4 a[24:27], v2, s[40:43], s82 offen sc1
v_mfma_f32_16x16x32_bf16 v[140:143], a[28:31], v[32:35], 0
v_mfma_f32_16x16x32_bf16 v[180:183], a[28:31], v[36:39], 0
buffer_load_dwordx4 a[28:31], v2, s[40:43], s83 offen sc1
v_mfma_f32_16x16x32_bf16 v[144:147], a[32:35], v[32:35], 0
v_mfma_f32_16x16x32_bf16 v[184:187], a[32:35], v[36:39], 0
buffer_load_dwordx4 a[32:35], v2, s[40:43], s84 offen sc1
v_mfma_f32_16x16x32_bf16 v[148:151], a[36:39], v[32:35], 0
v_mfma_f32_16x16x32_bf16 v[188:191], a[36:39], v[36:39], 0
buffer_load_dwordx4 a[36:39], v2, s[40:43], s85 offen sc1
s_waitcnt vmcnt(27)
v_mfma_f32_16x16x32_bf16 v[112:115], a[40:43], v[40:43], v[112:115]
s_waitcnt vmcnt(26)
v_mfma_f32_16x16x32_bf16 v[152:155], a[40:43], v[44:47], v[152:155]
buffer_load_dwordx4 a[40:43], v2, s[40:43], s76 offen offset:128 sc1
v_mfma_f32_16x16x32_bf16 v[116:119], a[44:47], v[40:43], v[116:119]
v_mfma_f32_16x16x32_bf16 v[156:159], a[44:47], v[44:47], v[156:159]
buffer_load_dwordx4 a[44:47], v2, s[40:43], s77 offen offset:128 sc1
v_mfma_f32_16x16x32_bf16 v[120:123], a[48:51], v[40:43], v[120:123]
v_mfma_f32_16x16x32_bf16 v[160:163], a[48:51], v[44:47], v[160:163]
buffer_load_dwordx4 a[48:51], v2, s[40:43], s78 offen offset:128 sc1
v_mfma_f32_16x16x32_bf16 v[124:127], a[52:55], v[40:43], v[124:127]
v_mfma_f32_16x16x32_bf16 v[164:167], a[52:55], v[44:47], v[164:167]
buffer_load_dwordx4 a[52:55], v2, s[40:43], s79 offen offset:128 sc1
v_mfma_f32_16x16x32_bf16 v[128:131], a[56:59], v[40:43], v[128:131]
v_mfma_f32_16x16x32_bf16 v[168:171], a[56:59], v[44:47], v[168:171]
buffer_load_dwordx4 a[56:59], v2, s[40:43], s80 offen offset:128 sc1
v_mfma_f32_16x16x32_bf16 v[132:135], a[60:63], v[40:43], v[132:135]
v_mfma_f32_16x16x32_bf16 v[172:175], a[60:63], v[44:47], v[172:175]
buffer_load_dwordx4 a[60:63], v2, s[40:43], s81 offen offset:128 sc1
v_mfma_f32_16x16x32_bf16 v[136:139], a[64:67], v[40:43], v[136:139]
v_mfma_f32_16x16x32_bf16 v[176:179], a[64:67], v[44:47], v[176:179]
buffer_load_dwordx4 a[64:67], v2, s[40:43], s82 offen offset:128 sc1
v_mfma_f32_16x16x32_bf16 v[140:143], a[68:71], v[40:43], v[140:143]
v_mfma_f32_16x16x32_bf16 v[180:183], a[68:71], v[44:47], v[180:183]
buffer_load_dwordx4 a[68:71], v2, s[40:43], s83 offen offset:128 sc1
v_mfma_f32_16x16x32_bf16 v[144:147], a[72:75], v[40:43], v[144:147]
v_mfma_f32_16x16x32_bf16 v[184:187], a[72:75], v[44:47], v[184:187]
buffer_load_dwordx4 a[72:75], v2, s[40:43], s84 offen offset:128 sc1
v_mfma_f32_16x16x32_bf16 v[148:151], a[76:79], v[40:43], v[148:151]
v_mfma_f32_16x16x32_bf16 v[188:191], a[76:79], v[44:47], v[188:191]
buffer_load_dwordx4 a[76:79], v2, s[40:43], s85 offen offset:128 sc1
s_waitcnt vmcnt(35)
v_mfma_f32_16x16x32_bf16 v[112:115], a[80:83], v[48:51], v[112:115]
s_waitcnt vmcnt(34)
v_mfma_f32_16x16x32_bf16 v[152:155], a[80:83], v[52:55], v[152:155]
v_mfma_f32_16x16x32_bf16 v[116:119], a[84:87], v[48:51], v[116:119]
v_mfma_f32_16x16x32_bf16 v[156:159], a[84:87], v[52:55], v[156:159]
v_mfma_f32_16x16x32_bf16 v[120:123], a[88:91], v[48:51], v[120:123]
v_mfma_f32_16x16x32_bf16 v[160:163], a[88:91], v[52:55], v[160:163]
v_mfma_f32_16x16x32_bf16 v[124:127], a[92:95], v[48:51], v[124:127]
v_mfma_f32_16x16x32_bf16 v[164:167], a[92:95], v[52:55], v[164:167]
v_mfma_f32_16x16x32_bf16 v[128:131], a[96:99], v[48:51], v[128:131]
v_mfma_f32_16x16x32_bf16 v[168:171], a[96:99], v[52:55], v[168:171]
v_mfma_f32_16x16x32_bf16 v[132:135], a[100:103], v[48:51], v[132:135]
v_mfma_f32_16x16x32_bf16 v[172:175], a[100:103], v[52:55], v[172:175]
v_mfma_f32_16x16x32_bf16 v[136:139], a[104:107], v[48:51], v[136:139]
v_mfma_f32_16x16x32_bf16 v[176:179], a[104:107], v[52:55], v[176:179]
v_mfma_f32_16x16x32_bf16 v[140:143], a[108:111], v[48:51], v[140:143]
v_mfma_f32_16x16x32_bf16 v[180:183], a[108:111], v[52:55], v[180:183]
v_mfma_f32_16x16x32_bf16 v[144:147], a[112:115], v[48:51], v[144:147]
v_mfma_f32_16x16x32_bf16 v[184:187], a[112:115], v[52:55], v[184:187]
v_mfma_f32_16x16x32_bf16 v[148:151], a[116:119], v[48:51], v[148:151]
v_mfma_f32_16x16x32_bf16 v[188:191], a[116:119], v[52:55], v[188:191]
s_waitcnt vmcnt(33)
v_mfma_f32_16x16x32_bf16 v[112:115], a[120:123], v[56:59], v[112:115]
s_waitcnt vmcnt(32)
v_mfma_f32_16x16x32_bf16 v[152:155], a[120:123], v[60:63], v[152:155]
buffer_load_dwordx4 v[32:35], v0, s[40:43], s15 offen sc1
v_mfma_f32_16x16x32_bf16 v[116:119], a[124:127], v[56:59], v[116:119]
v_mfma_f32_16x16x32_bf16 v[156:159], a[124:127], v[60:63], v[156:159]
buffer_load_dwordx4 v[36:39], v0, s[40:43], s15 offen offset:1024 sc1
v_mfma_f32_16x16x32_bf16 v[120:123], a[128:131], v[56:59], v[120:123]
v_mfma_f32_16x16x32_bf16 v[160:163], a[128:131], v[60:63], v[160:163]
buffer_load_dwordx4 v[40:43], v0, s[40:43], s16 offen sc1
v_mfma_f32_16x16x32_bf16 v[124:127], a[132:135], v[56:59], v[124:127]
v_mfma_f32_16x16x32_bf16 v[164:167], a[132:135], v[60:63], v[164:167]
buffer_load_dwordx4 v[44:47], v0, s[40:43], s16 offen offset:1024 sc1
v_mfma_f32_16x16x32_bf16 v[128:131], a[136:139], v[56:59], v[128:131]
v_mfma_f32_16x16x32_bf16 v[168:171], a[136:139], v[60:63], v[168:171]
buffer_load_dwordx4 v[48:51], v0, s[40:43], s17 offen sc1
v_mfma_f32_16x16x32_bf16 v[132:135], a[140:143], v[56:59], v[132:135]
v_mfma_f32_16x16x32_bf16 v[172:175], a[140:143], v[60:63], v[172:175]
buffer_load_dwordx4 v[52:55], v0, s[40:43], s17 offen offset:1024 sc1
v_mfma_f32_16x16x32_bf16 v[136:139], a[144:147], v[56:59], v[136:139]
v_mfma_f32_16x16x32_bf16 v[176:179], a[144:147], v[60:63], v[176:179]
v_mfma_f32_16x16x32_bf16 v[140:143], a[148:151], v[56:59], v[140:143]
v_mfma_f32_16x16x32_bf16 v[180:183], a[148:151], v[60:63], v[180:183]
v_mfma_f32_16x16x32_bf16 v[144:147], a[152:155], v[56:59], v[144:147]
v_mfma_f32_16x16x32_bf16 v[184:187], a[152:155], v[60:63], v[184:187]
v_mfma_f32_16x16x32_bf16 v[148:151], a[156:159], v[56:59], v[148:151]
v_mfma_f32_16x16x32_bf16 v[188:191], a[156:159], v[60:63], v[188:191]
s_waitcnt vmcnt(37)
v_mfma_f32_16x16x32_bf16 v[112:115], a[160:163], v[64:67], v[112:115]
s_waitcnt vmcnt(36)
v_mfma_f32_16x16x32_bf16 v[152:155], a[160:163], v[68:71], v[152:155]
buffer_load_dwordx4 a[80:83], v1, s[40:43], s30 offen offset:1024 sc1
v_mfma_f32_16x16x32_bf16 v[116:119], a[164:167], v[64:67], v[116:119]
v_mfma_f32_16x16x32_bf16 v[156:159], a[164:167], v[68:71], v[156:159]
buffer_load_dwordx4 a[84:87], v1, s[40:43], s31 offen offset:1024 sc1
v_mfma_f32_16x16x32_bf16 v[120:123], a[168:171], v[64:67], v[120:123]
v_mfma_f32_16x16x32_bf16 v[160:163], a[168:171], v[68:71], v[160:163]
buffer_load_dwordx4 a[88:91], v1, s[40:43], s34 offen offset:1024 sc1
v_mfma_f32_16x16x32_bf16 v[124:127], a[172:175], v[64:67], v[124:127]
v_mfma_f32_16x16x32_bf16 v[164:167], a[172:175], v[68:71], v[164:167]
buffer_load_dwordx4 a[92:95], v1, s[40:43], s35 offen offset:1024 sc1
v_mfma_f32_16x16x32_bf16 v[128:131], a[176:179], v[64:67], v[128:131]
v_mfma_f32_16x16x32_bf16 v[168:171], a[176:179], v[68:71], v[168:171]
buffer_load_dwordx4 a[96:99], v1, s[40:43], s36 offen offset:1024 sc1
v_mfma_f32_16x16x32_bf16 v[132:135], a[180:183], v[64:67], v[132:135]
v_mfma_f32_16x16x32_bf16 v[172:175], a[180:183], v[68:71], v[172:175]
buffer_load_dwordx4 a[100:103], v1, s[40:43], s37 offen offset:1024 sc1
v_mfma_f32_16x16x32_bf16 v[136:139], a[184:187], v[64:67], v[136:139]
v_mfma_f32_16x16x32_bf16 v[176:179], a[184:187], v[68:71], v[176:179]
buffer_load_dwordx4 a[104:107], v1, s[40:43], s38 offen offset:1024 sc1
)"
R"(v_mfma_f32_16x16x32_bf16 v[140:143], a[188:191], v[64:67], v[140:143]
v_mfma_f32_16x16x32_bf16 v[180:183], a[188:191], v[68:71], v[180:183]
buffer_load_dwordx4 a[108:111], v1, s[40:43], s39 offen offset:1024 sc1
v_mfma_f32_16x16x32_bf16 v[144:147], a[192:195], v[64:67], v[144:147]
v_mfma_f32_16x16x32_bf16 v[184:187], a[192:195], v[68:71], v[184:187]
buffer_load_dwordx4 a[112:115], v1, s[40:43], s96 offen offset:1024 sc1
v_mfma_f32_16x16x32_bf16 v[148:151], a[196:199], v[64:67], v[148:151]
v_mfma_f32_16x16x32_bf16 v[188:191], a[196:199], v[68:71], v[188:191]
buffer_load_dwordx4 a[116:119], v1, s[40:43], s97 offen offset:1024 sc1
s_waitcnt vmcnt(45)
v_mfma_f32_16x16x32_bf16 v[112:115], a[200:203], v[72:75], v[112:115]
s_waitcnt vmcnt(44)
v_mfma_f32_16x16x32_bf16 v[152:155], a[200:203], v[76:79], v[152:155]
buffer_load_dwordx4 a[120:123], v1, s[40:43], s30 offen offset:1152 sc1
v_mfma_f32_16x16x32_bf16 v[116:119], a[204:207], v[72:75], v[116:119]
v_mfma_f32_16x16x32_bf16 v[156:159], a[204:207], v[76:79], v[156:159]
buffer_load_dwordx4 a[124:127], v1, s[40:43], s31 offen offset:1152 sc1
v_mfma_f32_16x16x32_bf16 v[120:123], a[208:211], v[72:75], v[120:123]
v_mfma_f32_16x16x32_bf16 v[160:163], a[208:211], v[76:79], v[160:163]
buffer_load_dwordx4 a[128:131], v1, s[40:43], s34 offen offset:1152 sc1
v_mfma_f32_16x16x32_bf16 v[124:127], a[212:215], v[72:75], v[124:127]
v_mfma_f32_16x16x32_bf16 v[164:167], a[212:215], v[76:79], v[164:167]
buffer_load_dwordx4 a[132:135], v1, s[40:43], s35 offen offset:1152 sc1
v_mfma_f32_16x16x32_bf16 v[128:131], a[216:219], v[72:75], v[128:131]
v_mfma_f32_16x16x32_bf16 v[168:171], a[216:219], v[76:79], v[168:171]
buffer_load_dwordx4 a[136:139], v1, s[40:43], s36 offen offset:1152 sc1
v_mfma_f32_16x16x32_bf16 v[132:135], a[220:223], v[72:75], v[132:135]
v_mfma_f32_16x16x32_bf16 v[172:175], a[220:223], v[76:79], v[172:175]
buffer_load_dwordx4 a[140:143], v1, s[40:43], s37 offen offset:1152 sc1
v_mfma_f32_16x16x32_bf16 v[136:139], a[224:227], v[72:75], v[136:139]
v_mfma_f32_16x16x32_bf16 v[176:179], a[224:227], v[76:79], v[176:179]
buffer_load_dwordx4 a[144:147], v1, s[40:43], s38 offen offset:1152 sc1
v_mfma_f32_16x16x32_bf16 v[140:143], a[228:231], v[72:75], v[140:143]
v_mfma_f32_16x16x32_bf16 v[180:183], a[228:231], v[76:79], v[180:183]
buffer_load_dwordx4 a[148:151], v1, s[40:43], s39 offen offset:1152 sc1
v_mfma_f32_16x16x32_bf16 v[144:147], a[232:235], v[72:75], v[144:147]
v_mfma_f32_16x16x32_bf16 v[184:187], a[232:235], v[76:79], v[184:187]
buffer_load_dwordx4 a[152:155], v1, s[40:43], s96 offen offset:1152 sc1
v_mfma_f32_16x16x32_bf16 v[148:151], a[236:239], v[72:75], v[148:151]
v_mfma_f32_16x16x32_bf16 v[188:191], a[236:239], v[76:79], v[188:191]
buffer_load_dwordx4 a[156:159], v1, s[40:43], s97 offen offset:1152 sc1
s_waitcnt vmcnt(53)
v_mfma_f32_16x16x32_bf16 v[112:115], a[240:243], v[80:83], v[112:115]
s_waitcnt vmcnt(52)
v_mfma_f32_16x16x32_bf16 v[152:155], a[240:243], v[84:87], v[152:155]
buffer_load_dwordx4 a[160:163], v1, s[40:43], s30 offen offset:2048 sc1
v_mfma_f32_16x16x32_bf16 v[116:119], a[244:247], v[80:83], v[116:119]
v_mfma_f32_16x16x32_bf16 v[156:159], a[244:247], v[84:87], v[156:159]
buffer_load_dwordx4 a[164:167], v1, s[40:43], s31 offen offset:2048 sc1
v_mfma_f32_16x16x32_bf16 v[120:123], a[248:251], v[80:83], v[120:123]
v_mfma_f32_16x16x32_bf16 v[160:163], a[248:251], v[84:87], v[160:163]
buffer_load_dwordx4 a[168:171], v1, s[40:43], s34 offen offset:2048 sc1
v_mfma_f32_16x16x32_bf16 v[124:127], a[252:255], v[80:83], v[124:127]
v_mfma_f32_16x16x32_bf16 v[164:167], a[252:255], v[84:87], v[164:167]
buffer_load_dwordx4 a[172:175], v1, s[40:43], s35 offen offset:2048 sc1
v_mfma_f32_16x16x32_bf16 v[128:131], v[192:195], v[80:83], v[128:131]
v_mfma_f32_16x16x32_bf16 v[168:171], v[192:195], v[84:87], v[168:171]
buffer_load_dwordx4 a[176:179], v1, s[40:43], s36 offen offset:2048 sc1
v_mfma_f32_16x16x32_bf16 v[132:135], v[196:199], v[80:83], v[132:135]
v_mfma_f32_16x16x32_bf16 v[172:175], v[196:199], v[84:87], v[172:175]
buffer_load_dwordx4 a[180:183], v1, s[40:43], s37 offen offset:2048 sc1
v_mfma_f32_16x16x32_bf16 v[136:139], v[200:203], v[80:83], v[136:139]
v_mfma_f32_16x16x32_bf16 v[176:179], v[200:203], v[84:87], v[176:179]
buffer_load_dwordx4 a[184:187], v1, s[40:43], s38 offen offset:2048 sc1
v_mfma_f32_16x16x32_bf16 v[140:143], v[204:207], v[80:83], v[140:143]
v_mfma_f32_16x16x32_bf16 v[180:183], v[204:207], v[84:87], v[180:183]
buffer_load_dwordx4 a[188:191], v1, s[40:43], s39 offen offset:2048 sc1
v_mfma_f32_16x16x32_bf16 v[144:147], v[208:211], v[80:83], v[144:147]
v_mfma_f32_16x16x32_bf16 v[184:187], v[208:211], v[84:87], v[184:187]
buffer_load_dwordx4 a[192:195], v1, s[40:43], s96 offen offset:2048 sc1
v_mfma_f32_16x16x32_bf16 v[148:151], v[212:215], v[80:83], v[148:151]
v_mfma_f32_16x16x32_bf16 v[188:191], v[212:215], v[84:87], v[188:191]
buffer_load_dwordx4 a[196:199], v1, s[40:43], s97 offen offset:2048 sc1
s_waitcnt vmcnt(61)
v_mfma_f32_16x16x32_bf16 v[112:115], v[216:219], v[88:91], v[112:115]
s_waitcnt vmcnt(60)
v_mfma_f32_16x16x32_bf16 v[152:155], v[216:219], v[92:95], v[152:155]
buffer_load_dwordx4 a[200:203], v1, s[40:43], s30 offen offset:2176 sc1
v_mfma_f32_16x16x32_bf16 v[116:119], v[220:223], v[88:91], v[116:119]
v_mfma_f32_16x16x32_bf16 v[156:159], v[220:223], v[92:95], v[156:159]
buffer_load_dwordx4 a[204:207], v1, s[40:43], s31 offen offset:2176 sc1
v_mfma_f32_16x16x32_bf16 v[120:123], v[224:227], v[88:91], v[120:123]
v_mfma_f32_16x16x32_bf16 v[160:163], v[224:227], v[92:95], v[160:163]
buffer_load_dwordx4 a[208:211], v1, s[40:43], s34 offen offset:2176 sc1
s_waitcnt vmcnt(61)
v_mfma_f32_16x16x32_bf16 v[124:127], v[228:231], v[88:91], v[124:127]
v_mfma_f32_16x16x32_bf16 v[164:167], v[228:231], v[92:95], v[164:167]
buffer_load_dwordx4 a[212:215], v1, s[40:43], s35 offen offset:2176 sc1
v_mfma_f32_16x16x32_bf16 v[128:131], v[232:235], v[88:91], v[128:131]
v_mfma_f32_16x16x32_bf16 v[168:171], v[232:235], v[92:95], v[168:171]
buffer_load_dwordx4 a[216:219], v1, s[40:43], s36 offen offset:2176 sc1
s_waitcnt vmcnt(61)
v_mfma_f32_16x16x32_bf16 v[132:135], v[236:239], v[88:91], v[132:135]
v_mfma_f32_16x16x32_bf16 v[172:175], v[236:239], v[92:95], v[172:175]
buffer_load_dwordx4 a[220:223], v1, s[40:43], s37 offen offset:2176 sc1
v_mfma_f32_16x16x32_bf16 v[136:139], v[240:243], v[88:91], v[136:139]
v_mfma_f32_16x16x32_bf16 v[176:179], v[240:243], v[92:95], v[176:179]
buffer_load_dwordx4 a[224:227], v1, s[40:43], s38 offen offset:2176 sc1
s_waitcnt vmcnt(61)
v_mfma_f32_16x16x32_bf16 v[140:143], v[244:247], v[88:91], v[140:143]
v_mfma_f32_16x16x32_bf16 v[180:183], v[244:247], v[92:95], v[180:183]
buffer_load_dwordx4 a[228:231], v1, s[40:43], s39 offen offset:2176 sc1
v_mfma_f32_16x16x32_bf16 v[144:147], v[248:251], v[88:91], v[144:147]
v_mfma_f32_16x16x32_bf16 v[184:187], v[248:251], v[92:95], v[184:187]
buffer_load_dwordx4 a[232:235], v1, s[40:43], s96 offen offset:2176 sc1
s_waitcnt vmcnt(61)
v_mfma_f32_16x16x32_bf16 v[148:151], v[252:255], v[88:91], v[148:151]
v_mfma_f32_16x16x32_bf16 v[188:191], v[252:255], v[92:95], v[188:191]
buffer_load_dwordx4 a[236:239], v1, s[40:43], s97 offen offset:2176 sc1
v_mfma_f32_16x16x32_bf16 v[112:115], a[0:3], v[96:99], v[112:115]
v_mfma_f32_16x16x32_bf16 v[152:155], a[0:3], v[100:103], v[152:155]
v_mfma_f32_16x16x32_bf16 v[116:119], a[4:7], v[96:99], v[116:119]
v_mfma_f32_16x16x32_bf16 v[156:159], a[4:7], v[100:103], v[156:159]
v_mfma_f32_16x16x32_bf16 v[120:123], a[8:11], v[96:99], v[120:123]
v_mfma_f32_16x16x32_bf16 v[160:163], a[8:11], v[100:103], v[160:163]
v_mfma_f32_16x16x32_bf16 v[124:127], a[12:15], v[96:99], v[124:127]
v_mfma_f32_16x16x32_bf16 v[164:167], a[12:15], v[100:103], v[164:167]
s_waitcnt vmcnt(61)
v_mfma_f32_16x16x32_bf16 v[128:131], a[16:19], v[96:99], v[128:131]
v_mfma_f32_16x16x32_bf16 v[168:171], a[16:19], v[100:103], v[168:171]
s_waitcnt vmcnt(60)
v_mfma_f32_16x16x32_bf16 v[132:135], a[20:23], v[96:99], v[132:135]
v_mfma_f32_16x16x32_bf16 v[172:175], a[20:23], v[100:103], v[172:175]
s_waitcnt vmcnt(59)
v_mfma_f32_16x16x32_bf16 v[136:139], a[24:27], v[96:99], v[136:139]
v_mfma_f32_16x16x32_bf16 v[176:179], a[24:27], v[100:103], v[176:179]
s_waitcnt vmcnt(58)
v_mfma_f32_16x16x32_bf16 v[140:143], a[28:31], v[96:99], v[140:143]
v_mfma_f32_16x16x32_bf16 v[180:183], a[28:31], v[100:103], v[180:183]
s_waitcnt vmcnt(57)
v_mfma_f32_16x16x32_bf16 v[144:147], a[32:35], v[96:99], v[144:147]
v_mfma_f32_16x16x32_bf16 v[184:187], a[32:35], v[100:103], v[184:187]
s_waitcnt vmcnt(56)
v_mfma_f32_16x16x32_bf16 v[148:151], a[36:39], v[96:99], v[148:151]
v_mfma_f32_16x16x32_bf16 v[188:191], a[36:39], v[100:103], v[188:191]
s_waitcnt vmcnt(55)
v_mfma_f32_16x16x32_bf16 v[112:115], a[40:43], v[104:107], v[112:115]
v_mfma_f32_16x16x32_bf16 v[152:155], a[40:43], v[108:111], v[152:155]
s_waitcnt vmcnt(54)
v_mfma_f32_16x16x32_bf16 v[116:119], a[44:47], v[104:107], v[116:119]
v_mfma_f32_16x16x32_bf16 v[156:159], a[44:47], v[108:111], v[156:159]
s_waitcnt vmcnt(53)
v_mfma_f32_16x16x32_bf16 v[120:123], a[48:51], v[104:107], v[120:123]
v_mfma_f32_16x16x32_bf16 v[160:163], a[48:51], v[108:111], v[160:163]
s_waitcnt vmcnt(52)
v_mfma_f32_16x16x32_bf16 v[124:127], a[52:55], v[104:107], v[124:127]
v_mfma_f32_16x16x32_bf16 v[164:167], a[52:55], v[108:111], v[164:167]
s_waitcnt vmcnt(51)
v_mfma_f32_16x16x32_bf16 v[128:131], a[56:59], v[104:107], v[128:131]
v_mfma_f32_16x16x32_bf16 v[168:171], a[56:59], v[108:111], v[168:171]
s_waitcnt vmcnt(50)
v_mfma_f32_16x16x32_bf16 v[132:135], a[60:63], v[104:107], v[132:135]
v_mfma_f32_16x16x32_bf16 v[172:175], a[60:63], v[108:111], v[172:175]
s_waitcnt vmcnt(49)
v_mfma_f32_16x16x32_bf16 v[136:139], a[64:67], v[104:107], v[136:139]
v_mfma_f32_16x16x32_bf16 v[176:179], a[64:67], v[108:111], v[176:179]
s_waitcnt vmcnt(48)
v_mfma_f32_16x16x32_bf16 v[140:143], a[68:71], v[104:107], v[140:143]
v_mfma_f32_16x16x32_bf16 v[180:183], a[68:71], v[108:111], v[180:183]
s_waitcnt vmcnt(47)
v_mfma_f32_16x16x32_bf16 v[144:147], a[72:75], v[104:107], v[144:147]
v_mfma_f32_16x16x32_bf16 v[184:187], a[72:75], v[108:111], v[184:187]
s_waitcnt vmcnt(46)
v_mfma_f32_16x16x32_bf16 v[148:151], a[76:79], v[104:107], v[148:151]
v_mfma_f32_16x16x32_bf16 v[188:191], a[76:79], v[108:111], v[188:191]
ds_write_b128 v10, v[112:115] offset:0
ds_write_b128 v10, v[116:119] offset:1024
ds_write_b128 v10, v[120:123] offset:2048
ds_write_b128 v10, v[124:127] offset:3072
ds_write_b128 v10, v[128:131] offset:4096
ds_write_b128 v10, v[132:135] offset:5120
ds_write_b128 v10, v[136:139] offset:6144
ds_write_b128 v10, v[140:143] offset:7168
ds_write_b128 v10, v[144:147] offset:8192
ds_write_b128 v10, v[148:151] offset:9216
ds_write_b128 v10, v[152:155] offset:10240
ds_write_b128 v10, v[156:159] offset:11264
ds_write_b128 v10, v[160:163] offset:12288
ds_write_b128 v10, v[164:167] offset:13312
ds_write_b128 v10, v[168:171] offset:14336
ds_write_b128 v10, v[172:175] offset:15360
s_waitcnt lgkmcnt(14)
ds_write_b128 v10, v[176:179] offset:16384
ds_write_b128 v10, v[180:183] offset:17408
s_waitcnt lgkmcnt(14)
ds_write_b128 v10, v[184:187] offset:18432
ds_write_b128 v10, v[188:191] offset:19456
s_waitcnt lgkmcnt(14)
s_waitcnt lgkmcnt(0)
s_barrier
ds_read_b128 v[112:115], v11 offset:0
ds_read_b128 v[116:119], v11 offset:20480
ds_read_b128 v[120:123], v11 offset:40960
ds_read_b128 v[124:127], v11 offset:61440
ds_read_b128 v[128:131], v11 offset:1024
ds_read_b128 v[132:135], v11 offset:21504
ds_read_b128 v[136:139], v11 offset:41984
ds_read_b128 v[140:143], v11 offset:62464
ds_read_b128 v[144:147], v12 offset:0
ds_read_b128 v[148:151], v12 offset:20480
)"
R"(ds_read_b128 v[152:155], v12 offset:40960
ds_read_b128 v[156:159], v12 offset:61440
ds_read_b128 v[160:163], v12 offset:1024
ds_read_b128 v[164:167], v12 offset:21504
ds_read_b128 v[168:171], v12 offset:41984
ds_read_b128 v[172:175], v12 offset:62464
s_waitcnt lgkmcnt(14)
ds_read_b128 v[176:179], v13 offset:0
ds_read_b128 v[180:183], v13 offset:20480
s_waitcnt lgkmcnt(14)
ds_read_b128 v[184:187], v13 offset:40960
ds_read_b128 v[188:191], v13 offset:61440
s_waitcnt lgkmcnt(14)
ds_read_b128 v[192:195], v13 offset:1024
ds_read_b128 v[196:199], v13 offset:21504
s_waitcnt lgkmcnt(14)
ds_read_b128 v[200:203], v13 offset:41984
ds_read_b128 v[204:207], v13 offset:62464
s_waitcnt lgkmcnt(14)
v_pk_add_f32 v[112:113], v[112:113], v[116:117]
v_pk_add_f32 v[120:121], v[120:121], v[124:125]
s_waitcnt vmcnt(45)
v_pk_add_f32 v[32:33], v[32:33], v[112:113]
v_pk_add_f32 v[32:33], v[32:33], v[120:121]
v_pk_add_f32 v[114:115], v[114:115], v[118:119]
v_pk_add_f32 v[122:123], v[122:123], v[126:127]
v_pk_add_f32 v[34:35], v[34:35], v[114:115]
v_pk_add_f32 v[34:35], v[34:35], v[122:123]
v_pk_add_f32 v[128:129], v[128:129], v[132:133]
v_pk_add_f32 v[136:137], v[136:137], v[140:141]
s_waitcnt vmcnt(44)
v_pk_add_f32 v[36:37], v[36:37], v[128:129]
v_pk_add_f32 v[36:37], v[36:37], v[136:137]
v_pk_add_f32 v[130:131], v[130:131], v[134:135]
v_pk_add_f32 v[138:139], v[138:139], v[142:143]
v_pk_add_f32 v[38:39], v[38:39], v[130:131]
v_pk_add_f32 v[38:39], v[38:39], v[138:139]
s_add_u32 s48, s12, 138346496
s_lshr_b32 s49, s12, 1
s_add_u32 s49, s49, 143589376
v_cvt_pk_bf16_f32 v64, v32, v33
v_cvt_pk_bf16_f32 v65, v34, v35
v_cvt_pk_bf16_f32 v66, v36, v37
v_cvt_pk_bf16_f32 v67, v38, v39
buffer_store_dwordx4 v[64:67], v0, s[40:43], s49 offen sc1
buffer_store_dwordx4 v[32:35], v0, s[40:43], s48 offen sc1
buffer_store_dwordx4 v[36:39], v0, s[40:43], s48 offen offset:1024 sc1
v_pk_add_f32 v[144:145], v[144:145], v[148:149]
s_waitcnt lgkmcnt(12)
v_pk_add_f32 v[152:153], v[152:153], v[156:157]
s_waitcnt vmcnt(46)
v_pk_add_f32 v[40:41], v[40:41], v[144:145]
v_pk_add_f32 v[40:41], v[40:41], v[152:153]
v_pk_add_f32 v[146:147], v[146:147], v[150:151]
v_pk_add_f32 v[154:155], v[154:155], v[158:159]
v_pk_add_f32 v[42:43], v[42:43], v[146:147]
v_pk_add_f32 v[42:43], v[42:43], v[154:155]
s_waitcnt lgkmcnt(10)
v_pk_add_f32 v[160:161], v[160:161], v[164:165]
s_waitcnt lgkmcnt(8)
v_pk_add_f32 v[168:169], v[168:169], v[172:173]
s_waitcnt vmcnt(45)
v_pk_add_f32 v[44:45], v[44:45], v[160:161]
v_pk_add_f32 v[44:45], v[44:45], v[168:169]
v_pk_add_f32 v[162:163], v[162:163], v[166:167]
v_pk_add_f32 v[170:171], v[170:171], v[174:175]
v_pk_add_f32 v[46:47], v[46:47], v[162:163]
v_pk_add_f32 v[46:47], v[46:47], v[170:171]
s_add_u32 s48, s13, 138346496
s_lshr_b32 s49, s13, 1
s_add_u32 s49, s49, 143589376
v_cvt_pk_bf16_f32 v68, v40, v41
v_cvt_pk_bf16_f32 v69, v42, v43
v_cvt_pk_bf16_f32 v70, v44, v45
v_cvt_pk_bf16_f32 v71, v46, v47
buffer_store_dwordx4 v[68:71], v0, s[40:43], s49 offen sc1
buffer_store_dwordx4 v[40:43], v0, s[40:43], s48 offen sc1
buffer_store_dwordx4 v[44:47], v0, s[40:43], s48 offen offset:1024 sc1
s_waitcnt lgkmcnt(6)
v_pk_add_f32 v[176:177], v[176:177], v[180:181]
s_waitcnt lgkmcnt(4)
v_pk_add_f32 v[184:185], v[184:185], v[188:189]
s_waitcnt vmcnt(47)
v_pk_add_f32 v[48:49], v[48:49], v[176:177]
v_pk_add_f32 v[48:49], v[48:49], v[184:185]
v_pk_add_f32 v[178:179], v[178:179], v[182:183]
v_pk_add_f32 v[186:187], v[186:187], v[190:191]
v_pk_add_f32 v[50:51], v[50:51], v[178:179]
v_pk_add_f32 v[50:51], v[50:51], v[186:187]
s_waitcnt lgkmcnt(2)
v_pk_add_f32 v[192:193], v[192:193], v[196:197]
s_waitcnt lgkmcnt(0)
v_pk_add_f32 v[200:201], v[200:201], v[204:205]
s_waitcnt vmcnt(46)
v_pk_add_f32 v[52:53], v[52:53], v[192:193]
v_pk_add_f32 v[52:53], v[52:53], v[200:201]
v_pk_add_f32 v[194:195], v[194:195], v[198:199]
v_pk_add_f32 v[202:203], v[202:203], v[206:207]
v_pk_add_f32 v[54:55], v[54:55], v[194:195]
v_pk_add_f32 v[54:55], v[54:55], v[202:203]
s_add_u32 s48, s14, 138346496
s_lshr_b32 s49, s14, 1
s_add_u32 s49, s49, 143589376
v_cvt_pk_bf16_f32 v72, v48, v49
v_cvt_pk_bf16_f32 v73, v50, v51
v_cvt_pk_bf16_f32 v74, v52, v53
v_cvt_pk_bf16_f32 v75, v54, v55
buffer_store_dwordx4 v[72:75], v0, s[40:43], s49 offen sc1
buffer_store_dwordx4 v[48:51], v0, s[40:43], s48 offen sc1
buffer_store_dwordx4 v[52:55], v0, s[40:43], s48 offen offset:1024 sc1
s_lshr_b32 s54, s63, 1
s_lshl_b32 s54, s54, 7
s_add_u32 s54, s54, 2354176
buffer_load_dwordx4 a[0:3], v1, s[40:43], s30 offen sc1
buffer_load_dwordx4 a[4:7], v1, s[40:43], s31 offen sc1
buffer_load_dwordx4 a[8:11], v1, s[40:43], s34 offen sc1
buffer_load_dwordx4 a[12:15], v1, s[40:43], s35 offen sc1
buffer_load_dwordx4 a[16:19], v1, s[40:43], s36 offen sc1
buffer_load_dwordx4 a[20:23], v1, s[40:43], s37 offen sc1
buffer_load_dwordx4 a[24:27], v1, s[40:43], s38 offen sc1
buffer_load_dwordx4 a[28:31], v1, s[40:43], s39 offen sc1
buffer_load_dwordx4 a[32:35], v1, s[40:43], s96 offen sc1
buffer_load_dwordx4 a[36:39], v1, s[40:43], s97 offen sc1
buffer_load_dwordx4 a[40:43], v1, s[40:43], s30 offen offset:128 sc1
buffer_load_dwordx4 a[44:47], v1, s[40:43], s31 offen offset:128 sc1
buffer_load_dwordx4 a[48:51], v1, s[40:43], s34 offen offset:128 sc1
buffer_load_dwordx4 a[52:55], v1, s[40:43], s35 offen offset:128 sc1
buffer_load_dwordx4 a[56:59], v1, s[40:43], s36 offen offset:128 sc1
buffer_load_dwordx4 a[60:63], v1, s[40:43], s37 offen offset:128 sc1
buffer_load_dwordx4 a[64:67], v1, s[40:43], s38 offen offset:128 sc1
buffer_load_dwordx4 a[68:71], v1, s[40:43], s39 offen offset:128 sc1
buffer_load_dwordx4 a[72:75], v1, s[40:43], s96 offen offset:128 sc1
buffer_load_dwordx4 a[76:79], v1, s[40:43], s97 offen offset:128 sc1
buffer_load_dwordx4 a[240:243], v1, s[40:43], s30 offen offset:3072 sc1
buffer_load_dwordx4 a[244:247], v1, s[40:43], s31 offen offset:3072 sc1
buffer_load_dwordx4 a[248:251], v1, s[40:43], s34 offen offset:3072 sc1
buffer_load_dwordx4 a[252:255], v1, s[40:43], s35 offen offset:3072 sc1
buffer_load_dwordx4 v[192:195], v1, s[40:43], s36 offen offset:3072 sc1
buffer_load_dwordx4 v[196:199], v1, s[40:43], s37 offen offset:3072 sc1
buffer_load_dwordx4 v[200:203], v1, s[40:43], s38 offen offset:3072 sc1
buffer_load_dwordx4 v[204:207], v1, s[40:43], s39 offen offset:3072 sc1
buffer_load_dwordx4 v[208:211], v1, s[40:43], s96 offen offset:3072 sc1
buffer_load_dwordx4 v[212:215], v1, s[40:43], s97 offen offset:3072 sc1
buffer_load_dwordx4 v[216:219], v1, s[40:43], s30 offen offset:3200 sc1
buffer_load_dwordx4 v[220:223], v1, s[40:43], s31 offen offset:3200 sc1
buffer_load_dwordx4 v[224:227], v1, s[40:43], s34 offen offset:3200 sc1
buffer_load_dwordx4 v[228:231], v1, s[40:43], s35 offen offset:3200 sc1
buffer_load_dwordx4 v[232:235], v1, s[40:43], s36 offen offset:3200 sc1
buffer_load_dwordx4 v[236:239], v1, s[40:43], s37 offen offset:3200 sc1
buffer_load_dwordx4 v[240:243], v1, s[40:43], s38 offen offset:3200 sc1
buffer_load_dwordx4 v[244:247], v1, s[40:43], s39 offen offset:3200 sc1
buffer_load_dwordx4 v[248:251], v1, s[40:43], s96 offen offset:3200 sc1
buffer_load_dwordx4 v[252:255], v1, s[40:43], s97 offen offset:3200 sc1
s_waitcnt vmcnt(40)
s_waitcnt lgkmcnt(0)
s_barrier
buffer_atomic_add v6, v3, s[40:43], s54 offen
s_add_u32 s63, s63, 4
s_cmp_lt_u32 s47, 52
s_cbranch_scc1 hpt_loop_%=
s_branch hp_ret_%=
hp_tf_%=:
v_and_b32 v4, 0x370, v0
v_bfe_u32 v5, v15, 3, 1
v_lshl_add_u32 v1, v5, 15, v4
v_lshl_add_u32 v3, v5, 12, v4
s_lshl_b32 s28, s21, 6
s_add_u32 s28, s28, s26
s_lshl_b32 s54, s28, 7
s_add_u32 s54, s54, 2124032
s_cmp_lg_u32 s45, 0
s_cbranch_scc1 hptf_u_ok_%=
v_cmp_eq_u32 vcc, 0, v15
s_nop 1
v_cndmask_b32 v14, 0, 32, vcc
s_mov_b32 s60, 0
hptf_u_%=:
v_mov_b32 v4, 0
buffer_atomic_add v4, v2, s[40:43], s54 offen sc0
s_waitcnt vmcnt(0)
v_cmp_gt_u32 vcc, v14, v4
s_nop 4
s_cbranch_vccz hptf_u_ok_%=
s_sleep 1
s_add_u32 s60, s60, 1
s_cmp_gt_u32 s60, 65536
s_cbranch_scc1 hp_abort_%=
s_and_b32 s59, s60, 63
s_cbranch_scc1 hptf_u_%=
v_mov_b32 v4, 1024
buffer_load_dword v4, v4, s[40:43], s58 offen sc1
s_waitcnt vmcnt(0)
v_readfirstlane_b32 s59, v4
s_nop 0
s_cmp_lg_u32 s59, 0
s_nop 2
s_cbranch_scc1 hp_end_%=
s_branch hptf_u_%=
hptf_u_ok_%=:
s_barrier
)"
R"(s_and_b32 s29, s26, 31
s_mul_i32 s62, s29, 80
s_lshl3_add_u32 s62, s27, s62
s_lshl_b32 s50, s62, 15
s_lshl_b32 s48, s45, 12
s_and_b32 s49, s48, 0x2000
s_add_u32 s48, s48, s49
s_add_u32 s50, s50, s48
s_add_u32 s50, s50, 32964608
s_lshr_b32 s59, s50, 8
s_and_b32 s59, s59, 0x2000
s_add_u32 s51, s50, s59
s_add_u32 s50, s50, 65536
buffer_load_dwordx4 v[128:131], v1, s[40:43], s51 offen sc1
buffer_load_dwordx4 v[144:147], v1, s[40:43], s51 offen offset:128 sc1
buffer_load_dwordx4 v[160:163], v1, s[40:43], s51 offen offset:1024 sc1
buffer_load_dwordx4 v[176:179], v1, s[40:43], s51 offen offset:1152 sc1
buffer_load_dwordx4 v[192:195], v1, s[40:43], s51 offen offset:2048 sc1
buffer_load_dwordx4 v[208:211], v1, s[40:43], s51 offen offset:2176 sc1
buffer_load_dwordx4 v[224:227], v1, s[40:43], s51 offen offset:3072 sc1
buffer_load_dwordx4 v[240:243], v1, s[40:43], s51 offen offset:3200 sc1
s_lshr_b32 s59, s50, 8
s_and_b32 s59, s59, 0x2000
s_add_u32 s51, s50, s59
s_add_u32 s50, s50, 65536
buffer_load_dwordx4 v[132:135], v1, s[40:43], s51 offen sc1
buffer_load_dwordx4 v[148:151], v1, s[40:43], s51 offen offset:128 sc1
buffer_load_dwordx4 v[164:167], v1, s[40:43], s51 offen offset:1024 sc1
buffer_load_dwordx4 v[180:183], v1, s[40:43], s51 offen offset:1152 sc1
buffer_load_dwordx4 v[196:199], v1, s[40:43], s51 offen offset:2048 sc1
buffer_load_dwordx4 v[212:215], v1, s[40:43], s51 offen offset:2176 sc1
buffer_load_dwordx4 v[228:231], v1, s[40:43], s51 offen offset:3072 sc1
buffer_load_dwordx4 v[244:247], v1, s[40:43], s51 offen offset:3200 sc1
s_lshr_b32 s59, s50, 8
s_and_b32 s59, s59, 0x2000
s_add_u32 s51, s50, s59
s_add_u32 s50, s50, 65536
buffer_load_dwordx4 v[136:139], v1, s[40:43], s51 offen sc1
buffer_load_dwordx4 v[152:155], v1, s[40:43], s51 offen offset:128 sc1
buffer_load_dwordx4 v[168:171], v1, s[40:43], s51 offen offset:1024 sc1
buffer_load_dwordx4 v[184:187], v1, s[40:43], s51 offen offset:1152 sc1
buffer_load_dwordx4 v[200:203], v1, s[40:43], s51 offen offset:2048 sc1
buffer_load_dwordx4 v[216:219], v1, s[40:43], s51 offen offset:2176 sc1
buffer_load_dwordx4 v[232:235], v1, s[40:43], s51 offen offset:3072 sc1
buffer_load_dwordx4 v[248:251], v1, s[40:43], s51 offen offset:3200 sc1
s_lshr_b32 s59, s50, 8
s_and_b32 s59, s59, 0x2000
s_add_u32 s51, s50, s59
s_add_u32 s50, s50, 65536
buffer_load_dwordx4 v[140:143], v1, s[40:43], s51 offen sc1
buffer_load_dwordx4 v[156:159], v1, s[40:43], s51 offen offset:128 sc1
buffer_load_dwordx4 v[172:175], v1, s[40:43], s51 offen offset:1024 sc1
buffer_load_dwordx4 v[188:191], v1, s[40:43], s51 offen offset:1152 sc1
buffer_load_dwordx4 v[204:207], v1, s[40:43], s51 offen offset:2048 sc1
buffer_load_dwordx4 v[220:223], v1, s[40:43], s51 offen offset:2176 sc1
buffer_load_dwordx4 v[236:239], v1, s[40:43], s51 offen offset:3072 sc1
buffer_load_dwordx4 v[252:255], v1, s[40:43], s51 offen offset:3200 sc1
s_lshr_b32 s48, s45, 1
s_lshl1_add_u32 s48, s26, s48
s_mul_i32 s48, s48, 20
s_lshl1_add_u32 s48, s27, s48
s_and_b32 s49, s45, 1
s_add_u32 s48, s48, s49
s_lshl_b32 s30, s48, 11
s_add_u32 s48, s30, 133103616
buffer_load_dwordx4 a[248:251], v0, s[40:43], s48 offen sc1
buffer_load_dwordx4 a[252:255], v0, s[40:43], s48 offen offset:1024 sc1
v_lshlrev_b32 v6, 7, v15
s_mul_i32 s54, s28, 1536
s_add_u32 s54, s54, 2157568
v_mov_b32 v7, 20
v_cmp_gt_u32 vcc, 8, v15
s_nop 1
v_cndmask_b32 v7, v7, 8, vcc
v_cmp_gt_u32 vcc, 12, v15
s_nop 1
v_cndmask_b32 v7, 0, v7, vcc
v_mov_b32 v4, 2147483632
v_cndmask_b32 v6, v4, v6, vcc
s_lshl_b32 s48, s45, 13
v_add_u32 v8, s48, v0
s_lshl_b32 s48, s45, 11
v_add_u32 v9, s48, v0
s_lshl_b32 s50, s29, 5
s_lshl3_add_u32 s50, s45, s50
s_lshl_b32 s50, s50, 11
s_add_u32 s50, s50, 130482176
s_lshl_b32 s52, s29, 14
s_lshl_b32 s48, s45, 11
s_add_u32 s52, s52, s48
s_add_u32 s52, s52, 132579328
s_add_u32 s53, s52, 8192
s_lshl_b32 s51, s62, 12
s_lshl_b32 s48, s45, 10
s_add_u32 s51, s51, s48
s_add_u32 s51, s51, 116850688
s_waitcnt vmcnt(0)
s_cmp_lg_u32 s45, 0
s_cbranch_scc1 hptf_p_ok_%=
s_mov_b32 s60, 0
hptf_p_%=:
v_mov_b32 v4, 0
buffer_atomic_add v4, v6, s[40:43], s54 offen sc0
s_waitcnt vmcnt(0)
v_cmp_gt_u32 vcc, v7, v4
s_nop 4
s_cbranch_vccz hptf_p_ok_%=
s_sleep 2
s_add_u32 s60, s60, 1
s_cmp_gt_u32 s60, 65536
s_cbranch_scc1 hp_abort_%=
s_and_b32 s59, s60, 63
s_cbranch_scc1 hptf_p_%=
v_mov_b32 v4, 1024
buffer_load_dword v4, v4, s[40:43], s58 offen sc1
s_waitcnt vmcnt(0)
v_readfirstlane_b32 s59, v4
s_nop 0
s_cmp_lg_u32 s59, 0
s_nop 2
s_cbranch_scc1 hp_end_%=
s_branch hptf_p_%=
hptf_p_ok_%=:
s_barrier
buffer_load_dwordx4 v[16:19], v0, s[40:43], s50 offen sc1
buffer_load_dwordx4 v[20:23], v0, s[40:43], s50 offen offset:1024 sc1
buffer_load_dwordx4 v[24:27], v0, s[40:43], s50 offen offset:2048 sc1
buffer_load_dwordx4 v[28:31], v0, s[40:43], s50 offen offset:3072 sc1
s_add_u32 s50, s50, 4096
buffer_load_dwordx4 v[32:35], v0, s[40:43], s50 offen sc1
buffer_load_dwordx4 v[36:39], v0, s[40:43], s50 offen offset:1024 sc1
buffer_load_dwordx4 v[40:43], v0, s[40:43], s50 offen offset:2048 sc1
buffer_load_dwordx4 v[44:47], v0, s[40:43], s50 offen offset:3072 sc1
s_add_u32 s50, s50, 4096
buffer_load_dwordx4 v[48:51], v0, s[40:43], s50 offen sc1
buffer_load_dwordx4 v[52:55], v0, s[40:43], s50 offen offset:1024 sc1
buffer_load_dwordx4 v[56:59], v0, s[40:43], s50 offen offset:2048 sc1
buffer_load_dwordx4 v[60:63], v0, s[40:43], s50 offen offset:3072 sc1
s_add_u32 s50, s50, 4096
buffer_load_dwordx4 v[64:67], v0, s[40:43], s50 offen sc1
buffer_load_dwordx4 v[68:71], v0, s[40:43], s50 offen offset:1024 sc1
buffer_load_dwordx4 v[72:75], v0, s[40:43], s50 offen offset:2048 sc1
buffer_load_dwordx4 v[76:79], v0, s[40:43], s50 offen offset:3072 sc1
buffer_load_dwordx4 v[80:83], v0, s[40:43], s52 offen sc1
buffer_load_dwordx4 v[84:87], v0, s[40:43], s53 offen sc1
buffer_load_dwordx4 v[88:91], v0, s[40:43], s52 offen offset:1024 sc1
buffer_load_dwordx4 v[92:95], v0, s[40:43], s53 offen offset:1024 sc1
s_waitcnt vmcnt(19)
v_mfma_f32_16x16x32_bf16 v[96:99], v[128:131], v[16:19], 0
s_waitcnt vmcnt(18)
v_mfma_f32_16x16x32_bf16 v[112:115], v[128:131], v[20:23], 0
s_add_u32 s48, s51, 0
buffer_load_dwordx4 v[128:131], v3, s[40:43], s48 offen sc1
v_mfma_f32_16x16x32_bf16 v[100:103], v[132:135], v[16:19], 0
v_mfma_f32_16x16x32_bf16 v[116:119], v[132:135], v[20:23], 0
s_add_u32 s48, s51, 8192
buffer_load_dwordx4 v[132:135], v3, s[40:43], s48 offen sc1
v_mfma_f32_16x16x32_bf16 v[104:107], v[136:139], v[16:19], 0
v_mfma_f32_16x16x32_bf16 v[120:123], v[136:139], v[20:23], 0
s_add_u32 s48, s51, 16384
buffer_load_dwordx4 v[136:139], v3, s[40:43], s48 offen sc1
v_mfma_f32_16x16x32_bf16 v[108:111], v[140:143], v[16:19], 0
v_mfma_f32_16x16x32_bf16 v[124:127], v[140:143], v[20:23], 0
s_add_u32 s48, s51, 24576
buffer_load_dwordx4 v[140:143], v3, s[40:43], s48 offen sc1
s_waitcnt vmcnt(21)
v_mfma_f32_16x16x32_bf16 v[96:99], v[144:147], v[24:27], v[96:99]
s_waitcnt vmcnt(20)
v_mfma_f32_16x16x32_bf16 v[112:115], v[144:147], v[28:31], v[112:115]
s_add_u32 s48, s51, 0
buffer_load_dwordx4 v[144:147], v3, s[40:43], s48 offen offset:128 sc1
v_mfma_f32_16x16x32_bf16 v[100:103], v[148:151], v[24:27], v[100:103]
v_mfma_f32_16x16x32_bf16 v[116:119], v[148:151], v[28:31], v[116:119]
s_add_u32 s48, s51, 8192
buffer_load_dwordx4 v[148:151], v3, s[40:43], s48 offen offset:128 sc1
v_mfma_f32_16x16x32_bf16 v[104:107], v[152:155], v[24:27], v[104:107]
v_mfma_f32_16x16x32_bf16 v[120:123], v[152:155], v[28:31], v[120:123]
s_add_u32 s48, s51, 16384
buffer_load_dwordx4 v[152:155], v3, s[40:43], s48 offen offset:128 sc1
v_mfma_f32_16x16x32_bf16 v[108:111], v[156:159], v[24:27], v[108:111]
v_mfma_f32_16x16x32_bf16 v[124:127], v[156:159], v[28:31], v[124:127]
s_add_u32 s48, s51, 24576
buffer_load_dwordx4 v[156:159], v3, s[40:43], s48 offen offset:128 sc1
s_waitcnt vmcnt(23)
v_mfma_f32_16x16x32_bf16 v[96:99], v[160:163], v[32:35], v[96:99]
s_waitcnt vmcnt(22)
v_mfma_f32_16x16x32_bf16 v[112:115], v[160:163], v[36:39], v[112:115]
v_mfma_f32_16x16x32_bf16 v[100:103], v[164:167], v[32:35], v[100:103]
v_mfma_f32_16x16x32_bf16 v[116:119], v[164:167], v[36:39], v[116:119]
v_mfma_f32_16x16x32_bf16 v[104:107], v[168:171], v[32:35], v[104:107]
v_mfma_f32_16x16x32_bf16 v[120:123], v[168:171], v[36:39], v[120:123]
v_mfma_f32_16x16x32_bf16 v[108:111], v[172:175], v[32:35], v[108:111]
v_mfma_f32_16x16x32_bf16 v[124:127], v[172:175], v[36:39], v[124:127]
s_waitcnt vmcnt(21)
v_mfma_f32_16x16x32_bf16 v[96:99], v[176:179], v[40:43], v[96:99]
s_waitcnt vmcnt(20)
v_mfma_f32_16x16x32_bf16 v[112:115], v[176:179], v[44:47], v[112:115]
v_mfma_f32_16x16x32_bf16 v[100:103], v[180:183], v[40:43], v[100:103]
v_mfma_f32_16x16x32_bf16 v[116:119], v[180:183], v[44:47], v[116:119]
v_mfma_f32_16x16x32_bf16 v[104:107], v[184:187], v[40:43], v[104:107]
)"
R"(v_mfma_f32_16x16x32_bf16 v[120:123], v[184:187], v[44:47], v[120:123]
v_mfma_f32_16x16x32_bf16 v[108:111], v[188:191], v[40:43], v[108:111]
v_mfma_f32_16x16x32_bf16 v[124:127], v[188:191], v[44:47], v[124:127]
s_waitcnt vmcnt(19)
v_mfma_f32_16x16x32_bf16 v[96:99], v[192:195], v[48:51], v[96:99]
s_waitcnt vmcnt(18)
v_mfma_f32_16x16x32_bf16 v[112:115], v[192:195], v[52:55], v[112:115]
v_mfma_f32_16x16x32_bf16 v[100:103], v[196:199], v[48:51], v[100:103]
v_mfma_f32_16x16x32_bf16 v[116:119], v[196:199], v[52:55], v[116:119]
v_mfma_f32_16x16x32_bf16 v[104:107], v[200:203], v[48:51], v[104:107]
v_mfma_f32_16x16x32_bf16 v[120:123], v[200:203], v[52:55], v[120:123]
v_mfma_f32_16x16x32_bf16 v[108:111], v[204:207], v[48:51], v[108:111]
v_mfma_f32_16x16x32_bf16 v[124:127], v[204:207], v[52:55], v[124:127]
s_waitcnt vmcnt(17)
v_mfma_f32_16x16x32_bf16 v[96:99], v[208:211], v[56:59], v[96:99]
s_waitcnt vmcnt(16)
v_mfma_f32_16x16x32_bf16 v[112:115], v[208:211], v[60:63], v[112:115]
v_mfma_f32_16x16x32_bf16 v[100:103], v[212:215], v[56:59], v[100:103]
v_mfma_f32_16x16x32_bf16 v[116:119], v[212:215], v[60:63], v[116:119]
v_mfma_f32_16x16x32_bf16 v[104:107], v[216:219], v[56:59], v[104:107]
v_mfma_f32_16x16x32_bf16 v[120:123], v[216:219], v[60:63], v[120:123]
v_mfma_f32_16x16x32_bf16 v[108:111], v[220:223], v[56:59], v[108:111]
v_mfma_f32_16x16x32_bf16 v[124:127], v[220:223], v[60:63], v[124:127]
s_waitcnt vmcnt(15)
v_mfma_f32_16x16x32_bf16 v[96:99], v[224:227], v[64:67], v[96:99]
s_waitcnt vmcnt(14)
v_mfma_f32_16x16x32_bf16 v[112:115], v[224:227], v[68:71], v[112:115]
v_mfma_f32_16x16x32_bf16 v[100:103], v[228:231], v[64:67], v[100:103]
v_mfma_f32_16x16x32_bf16 v[116:119], v[228:231], v[68:71], v[116:119]
v_mfma_f32_16x16x32_bf16 v[104:107], v[232:235], v[64:67], v[104:107]
v_mfma_f32_16x16x32_bf16 v[120:123], v[232:235], v[68:71], v[120:123]
v_mfma_f32_16x16x32_bf16 v[108:111], v[236:239], v[64:67], v[108:111]
v_mfma_f32_16x16x32_bf16 v[124:127], v[236:239], v[68:71], v[124:127]
s_waitcnt vmcnt(13)
v_mfma_f32_16x16x32_bf16 v[96:99], v[240:243], v[72:75], v[96:99]
s_waitcnt vmcnt(12)
v_mfma_f32_16x16x32_bf16 v[112:115], v[240:243], v[76:79], v[112:115]
v_mfma_f32_16x16x32_bf16 v[100:103], v[244:247], v[72:75], v[100:103]
v_mfma_f32_16x16x32_bf16 v[116:119], v[244:247], v[76:79], v[116:119]
v_mfma_f32_16x16x32_bf16 v[104:107], v[248:251], v[72:75], v[104:107]
v_mfma_f32_16x16x32_bf16 v[120:123], v[248:251], v[76:79], v[120:123]
v_mfma_f32_16x16x32_bf16 v[108:111], v[252:255], v[72:75], v[108:111]
v_mfma_f32_16x16x32_bf16 v[124:127], v[252:255], v[76:79], v[124:127]
s_waitcnt vmcnt(7)
v_mfma_f32_16x16x32_bf16 v[96:99], v[128:131], v[80:83], v[96:99]
v_mfma_f32_16x16x32_bf16 v[112:115], v[128:131], v[84:87], v[112:115]
s_waitcnt vmcnt(6)
v_mfma_f32_16x16x32_bf16 v[100:103], v[132:135], v[80:83], v[100:103]
v_mfma_f32_16x16x32_bf16 v[116:119], v[132:135], v[84:87], v[116:119]
s_waitcnt vmcnt(5)
v_mfma_f32_16x16x32_bf16 v[104:107], v[136:139], v[80:83], v[104:107]
v_mfma_f32_16x16x32_bf16 v[120:123], v[136:139], v[84:87], v[120:123]
s_waitcnt vmcnt(4)
v_mfma_f32_16x16x32_bf16 v[108:111], v[140:143], v[80:83], v[108:111]
v_mfma_f32_16x16x32_bf16 v[124:127], v[140:143], v[84:87], v[124:127]
s_waitcnt vmcnt(3)
v_mfma_f32_16x16x32_bf16 v[96:99], v[144:147], v[88:91], v[96:99]
v_mfma_f32_16x16x32_bf16 v[112:115], v[144:147], v[92:95], v[112:115]
s_waitcnt vmcnt(2)
v_mfma_f32_16x16x32_bf16 v[100:103], v[148:151], v[88:91], v[100:103]
v_mfma_f32_16x16x32_bf16 v[116:119], v[148:151], v[92:95], v[116:119]
s_waitcnt vmcnt(1)
v_mfma_f32_16x16x32_bf16 v[104:107], v[152:155], v[88:91], v[104:107]
v_mfma_f32_16x16x32_bf16 v[120:123], v[152:155], v[92:95], v[120:123]
s_waitcnt vmcnt(0)
v_mfma_f32_16x16x32_bf16 v[108:111], v[156:159], v[88:91], v[108:111]
v_mfma_f32_16x16x32_bf16 v[124:127], v[156:159], v[92:95], v[124:127]
ds_write_b128 v8, v[96:99] offset:0
ds_write_b128 v8, v[100:103] offset:1024
s_nop 1
ds_write_b128 v8, v[104:107] offset:2048
s_nop 1
ds_write_b128 v8, v[108:111] offset:3072
ds_write_b128 v8, v[112:115] offset:4096
ds_write_b128 v8, v[116:119] offset:5120
ds_write_b128 v8, v[120:123] offset:6144
ds_write_b128 v8, v[124:127] offset:7168
v_accvgpr_read_b32 v16, a248
v_accvgpr_read_b32 v17, a249
v_accvgpr_read_b32 v18, a250
v_accvgpr_read_b32 v19, a251
v_accvgpr_read_b32 v20, a252
v_accvgpr_read_b32 v21, a253
v_accvgpr_read_b32 v22, a254
v_accvgpr_read_b32 v23, a255
s_waitcnt lgkmcnt(0)
s_barrier
ds_read_b128 v[128:131], v9 offset:0
ds_read_b128 v[132:135], v9 offset:8192
ds_read_b128 v[136:139], v9 offset:16384
ds_read_b128 v[140:143], v9 offset:24576
ds_read_b128 v[144:147], v9 offset:1024
ds_read_b128 v[148:151], v9 offset:9216
ds_read_b128 v[152:155], v9 offset:17408
ds_read_b128 v[156:159], v9 offset:25600
s_waitcnt lgkmcnt(6)
v_pk_add_f32 v[128:129], v[128:129], v[132:133]
s_waitcnt lgkmcnt(4)
v_pk_add_f32 v[136:137], v[136:137], v[140:141]
v_pk_add_f32 v[16:17], v[16:17], v[128:129]
v_pk_add_f32 v[16:17], v[16:17], v[136:137]
v_pk_add_f32 v[130:131], v[130:131], v[134:135]
v_pk_add_f32 v[138:139], v[138:139], v[142:143]
v_pk_add_f32 v[18:19], v[18:19], v[130:131]
v_pk_add_f32 v[18:19], v[18:19], v[138:139]
s_waitcnt lgkmcnt(2)
v_pk_add_f32 v[144:145], v[144:145], v[148:149]
s_waitcnt lgkmcnt(0)
v_pk_add_f32 v[152:153], v[152:153], v[156:157]
v_pk_add_f32 v[20:21], v[20:21], v[144:145]
v_pk_add_f32 v[20:21], v[20:21], v[152:153]
v_pk_add_f32 v[146:147], v[146:147], v[150:151]
v_pk_add_f32 v[154:155], v[154:155], v[158:159]
v_pk_add_f32 v[22:23], v[22:23], v[146:147]
v_pk_add_f32 v[22:23], v[22:23], v[154:155]
s_add_u32 s48, s30, 138346496
s_lshr_b32 s49, s30, 1
s_add_u32 s49, s49, 143589376
v_cvt_pk_bf16_f32 v24, v16, v17
v_cvt_pk_bf16_f32 v25, v18, v19
v_cvt_pk_bf16_f32 v26, v20, v21
v_cvt_pk_bf16_f32 v27, v22, v23
buffer_store_dwordx4 v[24:27], v0, s[40:43], s49 offen sc1
buffer_store_dwordx4 v[16:19], v0, s[40:43], s48 offen sc1
buffer_store_dwordx4 v[20:23], v0, s[40:43], s48 offen offset:1024 sc1
s_and_b32 s54, s26, 7
s_lshl3_add_u32 s54, s21, s54
s_lshl_b32 s54, s54, 7
s_add_u32 s54, s54, 2372608
s_branch hp_done_%=
hp_bf_%=:
s_and_b32 s29, s45, 1
s_lshr_b32 s30, s45, 1
s_lshl1_add_u32 s28, s26, s29
s_mul_i32 s48, s29, 7168
s_mul_i32 s49, s30, 15360
s_add_u32 s61, s48, s49
s_mul_i32 s50, s27, 307200
s_add_u32 s50, s50, s48
s_mul_i32 s48, s30, 153600
s_add_u32 s50, s50, s48
s_add_u32 s50, s50, 146210816
s_add_u32 m0, s61, 0
s_add_u32 s51, s50, 0
buffer_load_dwordx4 v0, s[40:43], s51 offen offset:0 lds
buffer_load_dwordx4 v0, s[40:43], s51 offen offset:1024 lds
buffer_load_dwordx4 v0, s[40:43], s51 offen offset:2048 lds
buffer_load_dwordx4 v0, s[40:43], s51 offen offset:3072 lds
s_add_u32 m0, s61, 4096
s_add_u32 s51, s50, 4096
buffer_load_dwordx4 v0, s[40:43], s51 offen offset:0 lds
buffer_load_dwordx4 v0, s[40:43], s51 offen offset:1024 lds
buffer_load_dwordx4 v0, s[40:43], s51 offen offset:2048 lds
buffer_load_dwordx4 v0, s[40:43], s51 offen offset:3072 lds
s_add_u32 m0, s61, 30720
s_add_u32 s51, s50, 15360
buffer_load_dwordx4 v0, s[40:43], s51 offen offset:0 lds
buffer_load_dwordx4 v0, s[40:43], s51 offen offset:1024 lds
buffer_load_dwordx4 v0, s[40:43], s51 offen offset:2048 lds
buffer_load_dwordx4 v0, s[40:43], s51 offen offset:3072 lds
s_add_u32 m0, s61, 34816
s_add_u32 s51, s50, 19456
buffer_load_dwordx4 v0, s[40:43], s51 offen offset:0 lds
buffer_load_dwordx4 v0, s[40:43], s51 offen offset:1024 lds
buffer_load_dwordx4 v0, s[40:43], s51 offen offset:2048 lds
buffer_load_dwordx4 v0, s[40:43], s51 offen offset:3072 lds
s_add_u32 m0, s61, 61440
s_add_u32 s51, s50, 30720
buffer_load_dwordx4 v0, s[40:43], s51 offen offset:0 lds
buffer_load_dwordx4 v0, s[40:43], s51 offen offset:1024 lds
buffer_load_dwordx4 v0, s[40:43], s51 offen offset:2048 lds
buffer_load_dwordx4 v0, s[40:43], s51 offen offset:3072 lds
s_add_u32 m0, s61, 65536
s_add_u32 s51, s50, 34816
buffer_load_dwordx4 v0, s[40:43], s51 offen offset:0 lds
buffer_load_dwordx4 v0, s[40:43], s51 offen offset:1024 lds
buffer_load_dwordx4 v0, s[40:43], s51 offen offset:2048 lds
buffer_load_dwordx4 v0, s[40:43], s51 offen offset:3072 lds
s_add_u32 m0, s61, 92160
s_add_u32 s51, s50, 46080
buffer_load_dwordx4 v0, s[40:43], s51 offen offset:0 lds
buffer_load_dwordx4 v0, s[40:43], s51 offen offset:1024 lds
buffer_load_dwordx4 v0, s[40:43], s51 offen offset:2048 lds
buffer_load_dwordx4 v0, s[40:43], s51 offen offset:3072 lds
s_add_u32 m0, s61, 96256
s_add_u32 s51, s50, 50176
buffer_load_dwordx4 v0, s[40:43], s51 offen offset:0 lds
buffer_load_dwordx4 v0, s[40:43], s51 offen offset:1024 lds
buffer_load_dwordx4 v0, s[40:43], s51 offen offset:2048 lds
buffer_load_dwordx4 v0, s[40:43], s51 offen offset:3072 lds
s_add_u32 m0, s61, 122880
s_add_u32 s51, s50, 61440
buffer_load_dwordx4 v0, s[40:43], s51 offen offset:0 lds
buffer_load_dwordx4 v0, s[40:43], s51 offen offset:1024 lds
buffer_load_dwordx4 v0, s[40:43], s51 offen offset:2048 lds
buffer_load_dwordx4 v0, s[40:43], s51 offen offset:3072 lds
s_add_u32 m0, s61, 126976
s_add_u32 s51, s50, 65536
buffer_load_dwordx4 v0, s[40:43], s51 offen offset:0 lds
buffer_load_dwordx4 v0, s[40:43], s51 offen offset:1024 lds
)"
R"(buffer_load_dwordx4 v0, s[40:43], s51 offen offset:2048 lds
buffer_load_dwordx4 v0, s[40:43], s51 offen offset:3072 lds
v_lshlrev_b32 v4, 7, v15
s_mul_i32 s48, s29, 29696
s_add_u32 s48, s48, s50
s_add_u32 s48, s48, 76800
buffer_load_dword v16, v4, s[40:43], s48 offen
s_add_u32 s48, s48, 8192
buffer_load_dword v17, v4, s[40:43], s48 offen
s_add_u32 s48, s48, 8192
buffer_load_dword v18, v4, s[40:43], s48 offen
s_add_u32 s48, s48, 8192
buffer_load_dword v19, v4, s[40:43], s48 offen
s_add_u32 s48, s48, 8192
buffer_load_dword v20, v4, s[40:43], s48 offen
s_add_u32 s48, s48, 8192
v_mov_b32 v188, 0x3f803f80
v_mov_b32 v189, 0x3f803f80
v_mov_b32 v190, 0x3f803f80
v_mov_b32 v191, 0x3f803f80
v_add_u32 v3, s49, v0
v_add_u32 v6, 61440, v3
v_add_u32 v7, 122880, v3
v_and_b32 v4, 48, v15
s_mul_i32 s52, s27, 960
s_cmp_eq_u32 s30, 0
s_cselect_b32 s48, 320, 0
s_add_u32 s52, s52, s48
s_add_u32 s52, s52, 148668416
s_and_b32 s31, s26, 7
s_lshl3_add_u32 s54, s21, s31
s_lshl_b32 s54, s54, 7
s_add_u32 s54, s54, 2372608
s_waitcnt vmcnt(0)
s_cmp_lg_u32 s45, 0
s_cbranch_scc1 hpbf_dep_ok_%=
v_cmp_eq_u32 vcc, 0, v15
s_nop 1
v_cndmask_b32 v14, 0, 10, vcc
s_mov_b32 s60, 0
hpbf_dep_%=:
v_mov_b32 v5, 0
buffer_atomic_add v5, v2, s[40:43], s54 offen sc0
s_waitcnt vmcnt(0)
v_cmp_gt_u32 vcc, v14, v5
s_nop 4
s_cbranch_vccz hpbf_dep_ok_%=
s_sleep 1
s_add_u32 s60, s60, 1
s_cmp_gt_u32 s60, 65536
s_cbranch_scc1 hp_abort_%=
s_and_b32 s59, s60, 63
s_cbranch_scc1 hpbf_dep_%=
v_mov_b32 v5, 1024
buffer_load_dword v5, v5, s[40:43], s58 offen sc1
s_waitcnt vmcnt(0)
v_readfirstlane_b32 s59, v5
s_nop 0
s_cmp_lg_u32 s59, 0
s_nop 2
s_cbranch_scc1 hp_end_%=
s_branch hpbf_dep_%=
hpbf_dep_ok_%=:
s_barrier
s_mul_i32 s48, s28, 20480
s_mul_i32 s49, s30, 10240
s_add_u32 s48, s48, s49
s_add_u32 s48, s48, 143589376
buffer_load_dwordx4 v[16:19], v0, s[40:43], s48 offen sc1
buffer_load_dwordx4 v[20:23], v0, s[40:43], s48 offen offset:1024 sc1
buffer_load_dwordx4 v[24:27], v0, s[40:43], s48 offen offset:2048 sc1
buffer_load_dwordx4 v[28:31], v0, s[40:43], s48 offen offset:3072 sc1
s_add_u32 s48, s48, 4096
buffer_load_dwordx4 v[32:35], v0, s[40:43], s48 offen sc1
buffer_load_dwordx4 v[36:39], v0, s[40:43], s48 offen offset:1024 sc1
buffer_load_dwordx4 v[40:43], v0, s[40:43], s48 offen offset:2048 sc1
buffer_load_dwordx4 v[44:47], v0, s[40:43], s48 offen offset:3072 sc1
s_add_u32 s48, s48, 4096
buffer_load_dwordx4 v[48:51], v0, s[40:43], s48 offen sc1
buffer_load_dwordx4 v[52:55], v0, s[40:43], s48 offen offset:1024 sc1
buffer_load_dwordx4 v[56:59], v4, s[40:43], s52 offen sc1
buffer_load_dwordx4 v[60:63], v4, s[40:43], s52 offen offset:64 sc1
buffer_load_dwordx4 v[64:67], v4, s[40:43], s52 offen offset:128 sc1
buffer_load_dwordx4 v[68:71], v4, s[40:43], s52 offen offset:192 sc1
buffer_load_dwordx4 v[72:75], v4, s[40:43], s52 offen offset:256 sc1
buffer_load_dwordx4 v[76:79], v4, s[40:43], s52 offen offset:320 sc1
buffer_load_dwordx4 v[80:83], v4, s[40:43], s52 offen offset:384 sc1
buffer_load_dwordx4 v[84:87], v4, s[40:43], s52 offen offset:448 sc1
buffer_load_dwordx4 v[88:91], v4, s[40:43], s52 offen offset:512 sc1
buffer_load_dwordx4 v[92:95], v4, s[40:43], s52 offen offset:576 sc1
ds_read_b128 v[164:167], v3 offset:0
ds_read_b128 v[168:171], v3 offset:1024
ds_read_b128 v[172:175], v3 offset:2048
ds_read_b128 v[176:179], v3 offset:3072
ds_read_b128 v[180:183], v3 offset:4096
s_waitcnt vmcnt(19) lgkmcnt(4)
v_mfma_f32_16x16x32_bf16 v[96:99], v[164:167], v[16:19], 0
ds_read_b128 v[164:167], v3 offset:5120
s_waitcnt lgkmcnt(4)
v_mfma_f32_16x16x32_bf16 v[100:103], v[168:171], v[16:19], 0
ds_read_b128 v[168:171], v3 offset:6144
s_waitcnt lgkmcnt(4)
v_mfma_f32_16x16x32_bf16 v[104:107], v[172:175], v[16:19], 0
ds_read_b128 v[172:175], v3 offset:7168
s_waitcnt lgkmcnt(4)
v_mfma_f32_16x16x32_bf16 v[108:111], v[176:179], v[16:19], 0
ds_read_b128 v[176:179], v3 offset:8192
s_waitcnt lgkmcnt(4)
v_mfma_f32_16x16x32_bf16 v[112:115], v[180:183], v[16:19], 0
ds_read_b128 v[180:183], v3 offset:9216
s_waitcnt lgkmcnt(4)
v_mfma_f32_16x16x32_bf16 v[116:119], v[164:167], v[16:19], 0
ds_read_b128 v[164:167], v3 offset:10240
s_waitcnt lgkmcnt(4)
v_mfma_f32_16x16x32_bf16 v[120:123], v[168:171], v[16:19], 0
ds_read_b128 v[168:171], v3 offset:11264
s_waitcnt lgkmcnt(4)
v_mfma_f32_16x16x32_bf16 v[124:127], v[172:175], v[16:19], 0
ds_read_b128 v[172:175], v3 offset:12288
v_mfma_f32_16x16x32_bf16 v[156:159], v[188:191], v[16:19], 0
s_waitcnt lgkmcnt(4)
v_mfma_f32_16x16x32_bf16 v[128:131], v[176:179], v[16:19], 0
ds_read_b128 v[176:179], v3 offset:13312
s_waitcnt lgkmcnt(4)
v_mfma_f32_16x16x32_bf16 v[132:135], v[180:183], v[16:19], 0
ds_read_b128 v[180:183], v3 offset:14336
s_waitcnt lgkmcnt(4)
v_mfma_f32_16x16x32_bf16 v[136:139], v[164:167], v[16:19], 0
ds_read_b128 v[164:167], v3 offset:30720
s_waitcnt lgkmcnt(4)
v_mfma_f32_16x16x32_bf16 v[140:143], v[168:171], v[16:19], 0
ds_read_b128 v[168:171], v3 offset:31744
s_waitcnt lgkmcnt(4)
v_mfma_f32_16x16x32_bf16 v[144:147], v[172:175], v[16:19], 0
ds_read_b128 v[172:175], v3 offset:32768
s_waitcnt lgkmcnt(4)
v_mfma_f32_16x16x32_bf16 v[148:151], v[176:179], v[16:19], 0
ds_read_b128 v[176:179], v3 offset:33792
s_waitcnt lgkmcnt(4)
v_mfma_f32_16x16x32_bf16 v[152:155], v[180:183], v[16:19], 0
ds_read_b128 v[180:183], v3 offset:34816
v_mfma_f32_16x16x32_bf16 v[160:163], v[16:19], v[16:19], 0
s_waitcnt vmcnt(18) lgkmcnt(4)
v_mfma_f32_16x16x32_bf16 v[96:99], v[164:167], v[20:23], v[96:99]
s_barrier
ds_read_b128 v[164:167], v3 offset:35840
s_add_u32 m0, s61, 0
s_add_u32 s51, s50, 76800
buffer_load_dwordx4 v0, s[40:43], s51 offen offset:0 lds
s_waitcnt lgkmcnt(4)
v_mfma_f32_16x16x32_bf16 v[100:103], v[168:171], v[20:23], v[100:103]
ds_read_b128 v[168:171], v3 offset:36864
buffer_load_dwordx4 v0, s[40:43], s51 offen offset:1024 lds
s_waitcnt lgkmcnt(4)
v_mfma_f32_16x16x32_bf16 v[104:107], v[172:175], v[20:23], v[104:107]
ds_read_b128 v[172:175], v3 offset:37888
buffer_load_dwordx4 v0, s[40:43], s51 offen offset:2048 lds
s_waitcnt lgkmcnt(4)
v_mfma_f32_16x16x32_bf16 v[108:111], v[176:179], v[20:23], v[108:111]
ds_read_b128 v[176:179], v3 offset:38912
buffer_load_dwordx4 v0, s[40:43], s51 offen offset:3072 lds
s_waitcnt lgkmcnt(4)
v_mfma_f32_16x16x32_bf16 v[112:115], v[180:183], v[20:23], v[112:115]
ds_read_b128 v[180:183], v3 offset:39936
s_add_u32 m0, s61, 4096
s_add_u32 s51, s50, 80896
buffer_load_dwordx4 v0, s[40:43], s51 offen offset:0 lds
s_waitcnt lgkmcnt(4)
v_mfma_f32_16x16x32_bf16 v[116:119], v[164:167], v[20:23], v[116:119]
ds_read_b128 v[164:167], v3 offset:40960
buffer_load_dwordx4 v0, s[40:43], s51 offen offset:1024 lds
s_waitcnt lgkmcnt(4)
v_mfma_f32_16x16x32_bf16 v[120:123], v[168:171], v[20:23], v[120:123]
ds_read_b128 v[168:171], v3 offset:41984
buffer_load_dwordx4 v0, s[40:43], s51 offen offset:2048 lds
s_waitcnt lgkmcnt(4)
v_mfma_f32_16x16x32_bf16 v[124:127], v[172:175], v[20:23], v[124:127]
ds_read_b128 v[172:175], v3 offset:43008
buffer_load_dwordx4 v0, s[40:43], s51 offen offset:3072 lds
v_mfma_f32_16x16x32_bf16 v[156:159], v[188:191], v[20:23], v[156:159]
s_waitcnt lgkmcnt(4)
v_mfma_f32_16x16x32_bf16 v[128:131], v[176:179], v[20:23], v[128:131]
ds_read_b128 v[176:179], v3 offset:44032
s_waitcnt lgkmcnt(4)
v_mfma_f32_16x16x32_bf16 v[132:135], v[180:183], v[20:23], v[132:135]
ds_read_b128 v[180:183], v3 offset:45056
s_waitcnt lgkmcnt(4)
v_mfma_f32_16x16x32_bf16 v[136:139], v[164:167], v[20:23], v[136:139]
ds_read_b128 v[164:167], v6 offset:0
s_waitcnt lgkmcnt(4)
v_mfma_f32_16x16x32_bf16 v[140:143], v[168:171], v[20:23], v[140:143]
ds_read_b128 v[168:171], v6 offset:1024
s_waitcnt lgkmcnt(4)
v_mfma_f32_16x16x32_bf16 v[144:147], v[172:175], v[20:23], v[144:147]
ds_read_b128 v[172:175], v6 offset:2048
s_waitcnt lgkmcnt(4)
v_mfma_f32_16x16x32_bf16 v[148:151], v[176:179], v[20:23], v[148:151]
ds_read_b128 v[176:179], v6 offset:3072
s_waitcnt lgkmcnt(4)
v_mfma_f32_16x16x32_bf16 v[152:155], v[180:183], v[20:23], v[152:155]
)"
R"(ds_read_b128 v[180:183], v6 offset:4096
v_mfma_f32_16x16x32_bf16 v[160:163], v[20:23], v[20:23], v[160:163]
s_waitcnt vmcnt(25) lgkmcnt(4)
v_mfma_f32_16x16x32_bf16 v[96:99], v[164:167], v[24:27], v[96:99]
s_barrier
ds_read_b128 v[164:167], v6 offset:5120
s_add_u32 m0, s61, 30720
s_add_u32 s51, s50, 92160
buffer_load_dwordx4 v0, s[40:43], s51 offen offset:0 lds
s_waitcnt lgkmcnt(4)
v_mfma_f32_16x16x32_bf16 v[100:103], v[168:171], v[24:27], v[100:103]
ds_read_b128 v[168:171], v6 offset:6144
buffer_load_dwordx4 v0, s[40:43], s51 offen offset:1024 lds
s_waitcnt lgkmcnt(4)
v_mfma_f32_16x16x32_bf16 v[104:107], v[172:175], v[24:27], v[104:107]
ds_read_b128 v[172:175], v6 offset:7168
buffer_load_dwordx4 v0, s[40:43], s51 offen offset:2048 lds
s_waitcnt lgkmcnt(4)
v_mfma_f32_16x16x32_bf16 v[108:111], v[176:179], v[24:27], v[108:111]
ds_read_b128 v[176:179], v6 offset:8192
buffer_load_dwordx4 v0, s[40:43], s51 offen offset:3072 lds
s_waitcnt lgkmcnt(4)
v_mfma_f32_16x16x32_bf16 v[112:115], v[180:183], v[24:27], v[112:115]
ds_read_b128 v[180:183], v6 offset:9216
s_add_u32 m0, s61, 34816
s_add_u32 s51, s50, 96256
buffer_load_dwordx4 v0, s[40:43], s51 offen offset:0 lds
s_waitcnt lgkmcnt(4)
v_mfma_f32_16x16x32_bf16 v[116:119], v[164:167], v[24:27], v[116:119]
ds_read_b128 v[164:167], v6 offset:10240
buffer_load_dwordx4 v0, s[40:43], s51 offen offset:1024 lds
s_waitcnt lgkmcnt(4)
v_mfma_f32_16x16x32_bf16 v[120:123], v[168:171], v[24:27], v[120:123]
ds_read_b128 v[168:171], v6 offset:11264
buffer_load_dwordx4 v0, s[40:43], s51 offen offset:2048 lds
s_waitcnt lgkmcnt(4)
v_mfma_f32_16x16x32_bf16 v[124:127], v[172:175], v[24:27], v[124:127]
ds_read_b128 v[172:175], v6 offset:12288
buffer_load_dwordx4 v0, s[40:43], s51 offen offset:3072 lds
v_mfma_f32_16x16x32_bf16 v[156:159], v[188:191], v[24:27], v[156:159]
s_waitcnt lgkmcnt(4)
v_mfma_f32_16x16x32_bf16 v[128:131], v[176:179], v[24:27], v[128:131]
ds_read_b128 v[176:179], v6 offset:13312
s_waitcnt lgkmcnt(4)
v_mfma_f32_16x16x32_bf16 v[132:135], v[180:183], v[24:27], v[132:135]
ds_read_b128 v[180:183], v6 offset:14336
s_waitcnt lgkmcnt(4)
v_mfma_f32_16x16x32_bf16 v[136:139], v[164:167], v[24:27], v[136:139]
ds_read_b128 v[164:167], v6 offset:30720
s_waitcnt lgkmcnt(4)
v_mfma_f32_16x16x32_bf16 v[140:143], v[168:171], v[24:27], v[140:143]
ds_read_b128 v[168:171], v6 offset:31744
s_waitcnt lgkmcnt(4)
v_mfma_f32_16x16x32_bf16 v[144:147], v[172:175], v[24:27], v[144:147]
ds_read_b128 v[172:175], v6 offset:32768
s_waitcnt lgkmcnt(4)
v_mfma_f32_16x16x32_bf16 v[148:151], v[176:179], v[24:27], v[148:151]
ds_read_b128 v[176:179], v6 offset:33792
s_waitcnt lgkmcnt(4)
v_mfma_f32_16x16x32_bf16 v[152:155], v[180:183], v[24:27], v[152:155]
ds_read_b128 v[180:183], v6 offset:34816
v_mfma_f32_16x16x32_bf16 v[160:163], v[24:27], v[24:27], v[160:163]
s_waitcnt vmcnt(32) lgkmcnt(4)
v_mfma_f32_16x16x32_bf16 v[96:99], v[164:167], v[28:31], v[96:99]
s_barrier
ds_read_b128 v[164:167], v6 offset:35840
s_add_u32 m0, s61, 61440
s_add_u32 s51, s50, 107520
buffer_load_dwordx4 v0, s[40:43], s51 offen offset:0 lds
s_waitcnt lgkmcnt(4)
v_mfma_f32_16x16x32_bf16 v[100:103], v[168:171], v[28:31], v[100:103]
ds_read_b128 v[168:171], v6 offset:36864
buffer_load_dwordx4 v0, s[40:43], s51 offen offset:1024 lds
s_waitcnt lgkmcnt(4)
v_mfma_f32_16x16x32_bf16 v[104:107], v[172:175], v[28:31], v[104:107]
ds_read_b128 v[172:175], v6 offset:37888
buffer_load_dwordx4 v0, s[40:43], s51 offen offset:2048 lds
s_waitcnt lgkmcnt(4)
v_mfma_f32_16x16x32_bf16 v[108:111], v[176:179], v[28:31], v[108:111]
ds_read_b128 v[176:179], v6 offset:38912
buffer_load_dwordx4 v0, s[40:43], s51 offen offset:3072 lds
s_waitcnt lgkmcnt(4)
v_mfma_f32_16x16x32_bf16 v[112:115], v[180:183], v[28:31], v[112:115]
ds_read_b128 v[180:183], v6 offset:39936
s_add_u32 m0, s61, 65536
s_add_u32 s51, s50, 111616
buffer_load_dwordx4 v0, s[40:43], s51 offen offset:0 lds
s_waitcnt lgkmcnt(4)
v_mfma_f32_16x16x32_bf16 v[116:119], v[164:167], v[28:31], v[116:119]
ds_read_b128 v[164:167], v6 offset:40960
buffer_load_dwordx4 v0, s[40:43], s51 offen offset:1024 lds
s_waitcnt lgkmcnt(4)
v_mfma_f32_16x16x32_bf16 v[120:123], v[168:171], v[28:31], v[120:123]
ds_read_b128 v[168:171], v6 offset:41984
buffer_load_dwordx4 v0, s[40:43], s51 offen offset:2048 lds
s_waitcnt lgkmcnt(4)
v_mfma_f32_16x16x32_bf16 v[124:127], v[172:175], v[28:31], v[124:127]
ds_read_b128 v[172:175], v6 offset:43008
buffer_load_dwordx4 v0, s[40:43], s51 offen offset:3072 lds
v_mfma_f32_16x16x32_bf16 v[156:159], v[188:191], v[28:31], v[156:159]
s_waitcnt lgkmcnt(4)
v_mfma_f32_16x16x32_bf16 v[128:131], v[176:179], v[28:31], v[128:131]
ds_read_b128 v[176:179], v6 offset:44032
s_waitcnt lgkmcnt(4)
v_mfma_f32_16x16x32_bf16 v[132:135], v[180:183], v[28:31], v[132:135]
ds_read_b128 v[180:183], v6 offset:45056
s_waitcnt lgkmcnt(4)
v_mfma_f32_16x16x32_bf16 v[136:139], v[164:167], v[28:31], v[136:139]
ds_read_b128 v[164:167], v7 offset:0
s_waitcnt lgkmcnt(4)
v_mfma_f32_16x16x32_bf16 v[140:143], v[168:171], v[28:31], v[140:143]
ds_read_b128 v[168:171], v7 offset:1024
s_waitcnt lgkmcnt(4)
v_mfma_f32_16x16x32_bf16 v[144:147], v[172:175], v[28:31], v[144:147]
ds_read_b128 v[172:175], v7 offset:2048
s_waitcnt lgkmcnt(4)
v_mfma_f32_16x16x32_bf16 v[148:151], v[176:179], v[28:31], v[148:151]
ds_read_b128 v[176:179], v7 offset:3072
s_waitcnt lgkmcnt(4)
v_mfma_f32_16x16x32_bf16 v[152:155], v[180:183], v[28:31], v[152:155]
ds_read_b128 v[180:183], v7 offset:4096
v_mfma_f32_16x16x32_bf16 v[160:163], v[28:31], v[28:31], v[160:163]
s_waitcnt vmcnt(39) lgkmcnt(4)
v_mfma_f32_16x16x32_bf16 v[96:99], v[164:167], v[32:35], v[96:99]
s_barrier
ds_read_b128 v[164:167], v7 offset:5120
s_add_u32 m0, s61, 92160
s_add_u32 s51, s50, 122880
buffer_load_dwordx4 v0, s[40:43], s51 offen offset:0 lds
s_waitcnt lgkmcnt(4)
v_mfma_f32_16x16x32_bf16 v[100:103], v[168:171], v[32:35], v[100:103]
ds_read_b128 v[168:171], v7 offset:6144
buffer_load_dwordx4 v0, s[40:43], s51 offen offset:1024 lds
s_waitcnt lgkmcnt(4)
v_mfma_f32_16x16x32_bf16 v[104:107], v[172:175], v[32:35], v[104:107]
ds_read_b128 v[172:175], v7 offset:7168
buffer_load_dwordx4 v0, s[40:43], s51 offen offset:2048 lds
s_waitcnt lgkmcnt(4)
v_mfma_f32_16x16x32_bf16 v[108:111], v[176:179], v[32:35], v[108:111]
ds_read_b128 v[176:179], v7 offset:8192
buffer_load_dwordx4 v0, s[40:43], s51 offen offset:3072 lds
s_waitcnt lgkmcnt(4)
v_mfma_f32_16x16x32_bf16 v[112:115], v[180:183], v[32:35], v[112:115]
ds_read_b128 v[180:183], v7 offset:9216
s_add_u32 m0, s61, 96256
s_add_u32 s51, s50, 126976
buffer_load_dwordx4 v0, s[40:43], s51 offen offset:0 lds
s_waitcnt lgkmcnt(4)
v_mfma_f32_16x16x32_bf16 v[116:119], v[164:167], v[32:35], v[116:119]
ds_read_b128 v[164:167], v7 offset:10240
buffer_load_dwordx4 v0, s[40:43], s51 offen offset:1024 lds
s_waitcnt lgkmcnt(4)
v_mfma_f32_16x16x32_bf16 v[120:123], v[168:171], v[32:35], v[120:123]
ds_read_b128 v[168:171], v7 offset:11264
buffer_load_dwordx4 v0, s[40:43], s51 offen offset:2048 lds
s_waitcnt lgkmcnt(4)
v_mfma_f32_16x16x32_bf16 v[124:127], v[172:175], v[32:35], v[124:127]
ds_read_b128 v[172:175], v7 offset:12288
buffer_load_dwordx4 v0, s[40:43], s51 offen offset:3072 lds
v_mfma_f32_16x16x32_bf16 v[156:159], v[188:191], v[32:35], v[156:159]
s_waitcnt lgkmcnt(4)
v_mfma_f32_16x16x32_bf16 v[128:131], v[176:179], v[32:35], v[128:131]
ds_read_b128 v[176:179], v7 offset:13312
s_waitcnt lgkmcnt(4)
v_mfma_f32_16x16x32_bf16 v[132:135], v[180:183], v[32:35], v[132:135]
ds_read_b128 v[180:183], v7 offset:14336
s_waitcnt lgkmcnt(4)
v_mfma_f32_16x16x32_bf16 v[136:139], v[164:167], v[32:35], v[136:139]
s_waitcnt vmcnt(24)
s_barrier
ds_read_b128 v[164:167], v3 offset:0
s_waitcnt lgkmcnt(4)
v_mfma_f32_16x16x32_bf16 v[140:143], v[168:171], v[32:35], v[140:143]
ds_read_b128 v[168:171], v3 offset:1024
s_waitcnt lgkmcnt(4)
v_mfma_f32_16x16x32_bf16 v[144:147], v[172:175], v[32:35], v[144:147]
ds_read_b128 v[172:175], v3 offset:2048
s_waitcnt lgkmcnt(4)
v_mfma_f32_16x16x32_bf16 v[148:151], v[176:179], v[32:35], v[148:151]
ds_read_b128 v[176:179], v3 offset:3072
s_waitcnt lgkmcnt(4)
v_mfma_f32_16x16x32_bf16 v[152:155], v[180:183], v[32:35], v[152:155]
ds_read_b128 v[180:183], v3 offset:4096
v_mfma_f32_16x16x32_bf16 v[160:163], v[32:35], v[32:35], v[160:163]
s_waitcnt lgkmcnt(4)
v_mfma_f32_16x16x32_bf16 v[96:99], v[164:167], v[36:39], v[96:99]
s_barrier
ds_read_b128 v[164:167], v3 offset:5120
s_add_u32 m0, s61, 122880
s_add_u32 s51, s50, 138240
buffer_load_dwordx4 v0, s[40:43], s51 offen offset:0 lds
s_waitcnt lgkmcnt(4)
v_mfma_f32_16x16x32_bf16 v[100:103], v[168:171], v[36:39], v[100:103]
ds_read_b128 v[168:171], v3 offset:6144
buffer_load_dwordx4 v0, s[40:43], s51 offen offset:1024 lds
s_waitcnt lgkmcnt(4)
v_mfma_f32_16x16x32_bf16 v[104:107], v[172:175], v[36:39], v[104:107]
ds_read_b128 v[172:175], v3 offset:7168
buffer_load_dwordx4 v0, s[40:43], s51 offen offset:2048 lds
s_waitcnt lgkmcnt(4)
)"
R"(v_mfma_f32_16x16x32_bf16 v[108:111], v[176:179], v[36:39], v[108:111]
ds_read_b128 v[176:179], v3 offset:8192
buffer_load_dwordx4 v0, s[40:43], s51 offen offset:3072 lds
s_waitcnt lgkmcnt(4)
v_mfma_f32_16x16x32_bf16 v[112:115], v[180:183], v[36:39], v[112:115]
ds_read_b128 v[180:183], v3 offset:9216
s_add_u32 m0, s61, 126976
s_add_u32 s51, s50, 142336
buffer_load_dwordx4 v0, s[40:43], s51 offen offset:0 lds
s_waitcnt lgkmcnt(4)
v_mfma_f32_16x16x32_bf16 v[116:119], v[164:167], v[36:39], v[116:119]
ds_read_b128 v[164:167], v3 offset:10240
buffer_load_dwordx4 v0, s[40:43], s51 offen offset:1024 lds
s_waitcnt lgkmcnt(4)
v_mfma_f32_16x16x32_bf16 v[120:123], v[168:171], v[36:39], v[120:123]
ds_read_b128 v[168:171], v3 offset:11264
buffer_load_dwordx4 v0, s[40:43], s51 offen offset:2048 lds
s_waitcnt lgkmcnt(4)
v_mfma_f32_16x16x32_bf16 v[124:127], v[172:175], v[36:39], v[124:127]
ds_read_b128 v[172:175], v3 offset:12288
buffer_load_dwordx4 v0, s[40:43], s51 offen offset:3072 lds
v_mfma_f32_16x16x32_bf16 v[156:159], v[188:191], v[36:39], v[156:159]
s_waitcnt lgkmcnt(4)
v_mfma_f32_16x16x32_bf16 v[128:131], v[176:179], v[36:39], v[128:131]
ds_read_b128 v[176:179], v3 offset:13312
s_waitcnt lgkmcnt(4)
v_mfma_f32_16x16x32_bf16 v[132:135], v[180:183], v[36:39], v[132:135]
ds_read_b128 v[180:183], v3 offset:14336
s_waitcnt lgkmcnt(4)
v_mfma_f32_16x16x32_bf16 v[136:139], v[164:167], v[36:39], v[136:139]
s_waitcnt vmcnt(24)
s_barrier
ds_read_b128 v[164:167], v3 offset:30720
s_waitcnt lgkmcnt(4)
v_mfma_f32_16x16x32_bf16 v[140:143], v[168:171], v[36:39], v[140:143]
ds_read_b128 v[168:171], v3 offset:31744
s_waitcnt lgkmcnt(4)
v_mfma_f32_16x16x32_bf16 v[144:147], v[172:175], v[36:39], v[144:147]
ds_read_b128 v[172:175], v3 offset:32768
s_waitcnt lgkmcnt(4)
v_mfma_f32_16x16x32_bf16 v[148:151], v[176:179], v[36:39], v[148:151]
ds_read_b128 v[176:179], v3 offset:33792
s_waitcnt lgkmcnt(4)
v_mfma_f32_16x16x32_bf16 v[152:155], v[180:183], v[36:39], v[152:155]
ds_read_b128 v[180:183], v3 offset:34816
v_mfma_f32_16x16x32_bf16 v[160:163], v[36:39], v[36:39], v[160:163]
s_waitcnt lgkmcnt(4)
v_mfma_f32_16x16x32_bf16 v[96:99], v[164:167], v[40:43], v[96:99]
ds_read_b128 v[164:167], v3 offset:35840
s_waitcnt lgkmcnt(4)
v_mfma_f32_16x16x32_bf16 v[100:103], v[168:171], v[40:43], v[100:103]
ds_read_b128 v[168:171], v3 offset:36864
s_waitcnt lgkmcnt(4)
v_mfma_f32_16x16x32_bf16 v[104:107], v[172:175], v[40:43], v[104:107]
ds_read_b128 v[172:175], v3 offset:37888
s_waitcnt lgkmcnt(4)
v_mfma_f32_16x16x32_bf16 v[108:111], v[176:179], v[40:43], v[108:111]
ds_read_b128 v[176:179], v3 offset:38912
s_waitcnt lgkmcnt(4)
v_mfma_f32_16x16x32_bf16 v[112:115], v[180:183], v[40:43], v[112:115]
ds_read_b128 v[180:183], v3 offset:39936
s_waitcnt lgkmcnt(4)
v_mfma_f32_16x16x32_bf16 v[116:119], v[164:167], v[40:43], v[116:119]
ds_read_b128 v[164:167], v3 offset:40960
s_waitcnt lgkmcnt(4)
v_mfma_f32_16x16x32_bf16 v[120:123], v[168:171], v[40:43], v[120:123]
ds_read_b128 v[168:171], v3 offset:41984
s_waitcnt lgkmcnt(4)
v_mfma_f32_16x16x32_bf16 v[124:127], v[172:175], v[40:43], v[124:127]
ds_read_b128 v[172:175], v3 offset:43008
v_mfma_f32_16x16x32_bf16 v[156:159], v[188:191], v[40:43], v[156:159]
s_waitcnt lgkmcnt(4)
v_mfma_f32_16x16x32_bf16 v[128:131], v[176:179], v[40:43], v[128:131]
ds_read_b128 v[176:179], v3 offset:44032
s_waitcnt lgkmcnt(4)
v_mfma_f32_16x16x32_bf16 v[132:135], v[180:183], v[40:43], v[132:135]
ds_read_b128 v[180:183], v3 offset:45056
s_waitcnt lgkmcnt(4)
v_mfma_f32_16x16x32_bf16 v[136:139], v[164:167], v[40:43], v[136:139]
s_waitcnt vmcnt(16)
s_barrier
ds_read_b128 v[164:167], v6 offset:0
s_waitcnt lgkmcnt(4)
v_mfma_f32_16x16x32_bf16 v[140:143], v[168:171], v[40:43], v[140:143]
ds_read_b128 v[168:171], v6 offset:1024
s_waitcnt lgkmcnt(4)
v_mfma_f32_16x16x32_bf16 v[144:147], v[172:175], v[40:43], v[144:147]
ds_read_b128 v[172:175], v6 offset:2048
s_waitcnt lgkmcnt(4)
v_mfma_f32_16x16x32_bf16 v[148:151], v[176:179], v[40:43], v[148:151]
ds_read_b128 v[176:179], v6 offset:3072
s_waitcnt lgkmcnt(4)
v_mfma_f32_16x16x32_bf16 v[152:155], v[180:183], v[40:43], v[152:155]
ds_read_b128 v[180:183], v6 offset:4096
v_mfma_f32_16x16x32_bf16 v[160:163], v[40:43], v[40:43], v[160:163]
s_waitcnt lgkmcnt(4)
v_mfma_f32_16x16x32_bf16 v[96:99], v[164:167], v[44:47], v[96:99]
ds_read_b128 v[164:167], v6 offset:5120
s_waitcnt lgkmcnt(4)
v_mfma_f32_16x16x32_bf16 v[100:103], v[168:171], v[44:47], v[100:103]
ds_read_b128 v[168:171], v6 offset:6144
s_waitcnt lgkmcnt(4)
v_mfma_f32_16x16x32_bf16 v[104:107], v[172:175], v[44:47], v[104:107]
ds_read_b128 v[172:175], v6 offset:7168
s_waitcnt lgkmcnt(4)
v_mfma_f32_16x16x32_bf16 v[108:111], v[176:179], v[44:47], v[108:111]
ds_read_b128 v[176:179], v6 offset:8192
s_waitcnt lgkmcnt(4)
v_mfma_f32_16x16x32_bf16 v[112:115], v[180:183], v[44:47], v[112:115]
ds_read_b128 v[180:183], v6 offset:9216
s_waitcnt lgkmcnt(4)
v_mfma_f32_16x16x32_bf16 v[116:119], v[164:167], v[44:47], v[116:119]
ds_read_b128 v[164:167], v6 offset:10240
s_waitcnt lgkmcnt(4)
v_mfma_f32_16x16x32_bf16 v[120:123], v[168:171], v[44:47], v[120:123]
ds_read_b128 v[168:171], v6 offset:11264
s_waitcnt lgkmcnt(4)
v_mfma_f32_16x16x32_bf16 v[124:127], v[172:175], v[44:47], v[124:127]
ds_read_b128 v[172:175], v6 offset:12288
v_mfma_f32_16x16x32_bf16 v[156:159], v[188:191], v[44:47], v[156:159]
s_waitcnt lgkmcnt(4)
v_mfma_f32_16x16x32_bf16 v[128:131], v[176:179], v[44:47], v[128:131]
ds_read_b128 v[176:179], v6 offset:13312
s_waitcnt lgkmcnt(4)
v_mfma_f32_16x16x32_bf16 v[132:135], v[180:183], v[44:47], v[132:135]
ds_read_b128 v[180:183], v6 offset:14336
s_waitcnt lgkmcnt(4)
v_mfma_f32_16x16x32_bf16 v[136:139], v[164:167], v[44:47], v[136:139]
s_waitcnt vmcnt(8)
s_barrier
ds_read_b128 v[164:167], v6 offset:30720
s_waitcnt lgkmcnt(4)
v_mfma_f32_16x16x32_bf16 v[140:143], v[168:171], v[44:47], v[140:143]
ds_read_b128 v[168:171], v6 offset:31744
s_waitcnt lgkmcnt(4)
v_mfma_f32_16x16x32_bf16 v[144:147], v[172:175], v[44:47], v[144:147]
ds_read_b128 v[172:175], v6 offset:32768
s_waitcnt lgkmcnt(4)
v_mfma_f32_16x16x32_bf16 v[148:151], v[176:179], v[44:47], v[148:151]
ds_read_b128 v[176:179], v6 offset:33792
s_waitcnt lgkmcnt(4)
v_mfma_f32_16x16x32_bf16 v[152:155], v[180:183], v[44:47], v[152:155]
ds_read_b128 v[180:183], v6 offset:34816
v_mfma_f32_16x16x32_bf16 v[160:163], v[44:47], v[44:47], v[160:163]
s_waitcnt lgkmcnt(4)
v_mfma_f32_16x16x32_bf16 v[96:99], v[164:167], v[48:51], v[96:99]
ds_read_b128 v[164:167], v6 offset:35840
s_waitcnt lgkmcnt(4)
v_mfma_f32_16x16x32_bf16 v[100:103], v[168:171], v[48:51], v[100:103]
ds_read_b128 v[168:171], v6 offset:36864
s_waitcnt lgkmcnt(4)
v_mfma_f32_16x16x32_bf16 v[104:107], v[172:175], v[48:51], v[104:107]
ds_read_b128 v[172:175], v6 offset:37888
s_waitcnt lgkmcnt(4)
v_mfma_f32_16x16x32_bf16 v[108:111], v[176:179], v[48:51], v[108:111]
ds_read_b128 v[176:179], v6 offset:38912
s_waitcnt lgkmcnt(4)
v_mfma_f32_16x16x32_bf16 v[112:115], v[180:183], v[48:51], v[112:115]
ds_read_b128 v[180:183], v6 offset:39936
s_waitcnt lgkmcnt(4)
v_mfma_f32_16x16x32_bf16 v[116:119], v[164:167], v[48:51], v[116:119]
ds_read_b128 v[164:167], v6 offset:40960
s_waitcnt lgkmcnt(4)
v_mfma_f32_16x16x32_bf16 v[120:123], v[168:171], v[48:51], v[120:123]
ds_read_b128 v[168:171], v6 offset:41984
s_waitcnt lgkmcnt(4)
v_mfma_f32_16x16x32_bf16 v[124:127], v[172:175], v[48:51], v[124:127]
ds_read_b128 v[172:175], v6 offset:43008
v_mfma_f32_16x16x32_bf16 v[156:159], v[188:191], v[48:51], v[156:159]
s_waitcnt lgkmcnt(4)
v_mfma_f32_16x16x32_bf16 v[128:131], v[176:179], v[48:51], v[128:131]
ds_read_b128 v[176:179], v6 offset:44032
s_waitcnt lgkmcnt(4)
v_mfma_f32_16x16x32_bf16 v[132:135], v[180:183], v[48:51], v[132:135]
ds_read_b128 v[180:183], v6 offset:45056
s_waitcnt lgkmcnt(4)
v_mfma_f32_16x16x32_bf16 v[136:139], v[164:167], v[48:51], v[136:139]
s_waitcnt vmcnt(0)
s_barrier
ds_read_b128 v[164:167], v7 offset:0
s_waitcnt lgkmcnt(4)
v_mfma_f32_16x16x32_bf16 v[140:143], v[168:171], v[48:51], v[140:143]
ds_read_b128 v[168:171], v7 offset:1024
s_waitcnt lgkmcnt(4)
v_mfma_f32_16x16x32_bf16 v[144:147], v[172:175], v[48:51], v[144:147]
ds_read_b128 v[172:175], v7 offset:2048
s_waitcnt lgkmcnt(4)
v_mfma_f32_16x16x32_bf16 v[148:151], v[176:179], v[48:51], v[148:151]
ds_read_b128 v[176:179], v7 offset:3072
s_waitcnt lgkmcnt(4)
v_mfma_f32_16x16x32_bf16 v[152:155], v[180:183], v[48:51], v[152:155]
ds_read_b128 v[180:183], v7 offset:4096
v_mfma_f32_16x16x32_bf16 v[160:163], v[48:51], v[48:51], v[160:163]
s_waitcnt lgkmcnt(4)
v_mfma_f32_16x16x32_bf16 v[96:99], v[164:167], v[52:55], v[96:99]
ds_read_b128 v[164:167], v7 offset:5120
s_waitcnt lgkmcnt(4)
v_mfma_f32_16x16x32_bf16 v[100:103], v[168:171], v[52:55], v[100:103]
ds_read_b128 v[168:171], v7 offset:6144
s_waitcnt lgkmcnt(4)
)"
R"(v_mfma_f32_16x16x32_bf16 v[104:107], v[172:175], v[52:55], v[104:107]
ds_read_b128 v[172:175], v7 offset:7168
s_waitcnt lgkmcnt(4)
v_mfma_f32_16x16x32_bf16 v[108:111], v[176:179], v[52:55], v[108:111]
ds_read_b128 v[176:179], v7 offset:8192
s_waitcnt lgkmcnt(4)
v_mfma_f32_16x16x32_bf16 v[112:115], v[180:183], v[52:55], v[112:115]
ds_read_b128 v[180:183], v7 offset:9216
s_waitcnt lgkmcnt(4)
v_mfma_f32_16x16x32_bf16 v[116:119], v[164:167], v[52:55], v[116:119]
ds_read_b128 v[164:167], v7 offset:10240
s_waitcnt lgkmcnt(4)
v_mfma_f32_16x16x32_bf16 v[120:123], v[168:171], v[52:55], v[120:123]
ds_read_b128 v[168:171], v7 offset:11264
s_waitcnt lgkmcnt(4)
v_mfma_f32_16x16x32_bf16 v[124:127], v[172:175], v[52:55], v[124:127]
ds_read_b128 v[172:175], v7 offset:12288
v_mfma_f32_16x16x32_bf16 v[156:159], v[188:191], v[52:55], v[156:159]
s_waitcnt lgkmcnt(4)
v_mfma_f32_16x16x32_bf16 v[128:131], v[176:179], v[52:55], v[128:131]
ds_read_b128 v[176:179], v7 offset:13312
s_waitcnt lgkmcnt(4)
v_mfma_f32_16x16x32_bf16 v[132:135], v[180:183], v[52:55], v[132:135]
ds_read_b128 v[180:183], v7 offset:14336
s_waitcnt lgkmcnt(4)
v_mfma_f32_16x16x32_bf16 v[136:139], v[164:167], v[52:55], v[136:139]
s_waitcnt lgkmcnt(3)
v_mfma_f32_16x16x32_bf16 v[140:143], v[168:171], v[52:55], v[140:143]
s_waitcnt lgkmcnt(2)
v_mfma_f32_16x16x32_bf16 v[144:147], v[172:175], v[52:55], v[144:147]
s_waitcnt lgkmcnt(1)
v_mfma_f32_16x16x32_bf16 v[148:151], v[176:179], v[52:55], v[148:151]
s_waitcnt lgkmcnt(0)
v_mfma_f32_16x16x32_bf16 v[152:155], v[180:183], v[52:55], v[152:155]
v_mfma_f32_16x16x32_bf16 v[160:163], v[52:55], v[52:55], v[160:163]
s_mul_i32 s48, s45, 12288
s_add_u32 s48, s48, 32768
v_add_u32 v6, s48, v0
s_xor_b32 s48, s45, 2
s_mul_i32 s48, s48, 12288
s_add_u32 s48, s48, 32768
v_add_u32 v7, s48, v0
s_add_u32 s48, s31, 26
s_lshl_b32 s48, s48, 5
s_lshl2_add_u32 s48, s27, s48
s_add_u32 s48, s48, s45
s_mul_i32 s34, s48, 6144
s_add_u32 s34, s34, 154820608
s_lshl3_add_u32 s48, s21, s27
s_lshl_b32 s48, s48, 7
s_add_u32 s36, s48, 267264
v_lshlrev_b32 v12, 11, v15
v_add_u32 v12, s36, v12
s_cmp_eq_u32 s31, 7
s_cselect_b32 s48, 4, 0
v_mov_b32 v13, s48
s_lshr_b32 s48, s31, 1
v_mov_b32 v4, s48
s_and_b32 s37, s31, 1
v_cmp_eq_u32 vcc, s37, v15
s_nop 1
v_cndmask_b32 v13, v13, v4, vcc
v_cmp_gt_u32 vcc, 3, v15
s_nop 1
v_cndmask_b32 v13, 0, v13, vcc
v_mov_b32 v4, 2147483632
v_cndmask_b32 v12, v4, v12, vcc
v_mov_b32 v14, 0
buffer_atomic_add v14, v12, s[40:43], s58 offen sc0
s_cmp_eq_u32 s30, 0
s_nop 0
s_cbranch_scc1 hpbf_kv_%=
ds_write_b128 v6, v[116:119] offset:0
ds_write_b128 v6, v[120:123] offset:1024
ds_write_b128 v6, v[124:127] offset:2048
ds_write_b128 v6, v[128:131] offset:3072
ds_write_b128 v6, v[132:135] offset:4096
ds_write_b128 v6, v[136:139] offset:5120
ds_write_b128 v6, v[140:143] offset:6144
ds_write_b128 v6, v[144:147] offset:7168
ds_write_b128 v6, v[148:151] offset:8192
ds_write_b128 v6, v[152:155] offset:9216
ds_write_b128 v6, v[156:159] offset:10240
ds_write_b128 v6, v[160:163] offset:11264
s_waitcnt lgkmcnt(0)
s_barrier
ds_read_b128 v[16:19], v7 offset:0
ds_read_b128 v[20:23], v7 offset:1024
ds_read_b128 v[24:27], v7 offset:2048
ds_read_b128 v[28:31], v7 offset:3072
ds_read_b128 v[32:35], v7 offset:4096
ds_read_b128 v[36:39], v7 offset:5120
ds_read_b128 v[40:43], v7 offset:6144
s_waitcnt lgkmcnt(6)
v_pk_add_f32 v[96:97], v[96:97], v[16:17]
v_pk_add_f32 v[98:99], v[98:99], v[18:19]
s_waitcnt lgkmcnt(5)
v_pk_add_f32 v[100:101], v[100:101], v[20:21]
v_pk_add_f32 v[102:103], v[102:103], v[22:23]
s_waitcnt lgkmcnt(4)
v_pk_add_f32 v[104:105], v[104:105], v[24:25]
v_pk_add_f32 v[106:107], v[106:107], v[26:27]
s_waitcnt lgkmcnt(3)
v_pk_add_f32 v[108:109], v[108:109], v[28:29]
v_pk_add_f32 v[110:111], v[110:111], v[30:31]
s_waitcnt lgkmcnt(2)
v_pk_add_f32 v[112:113], v[112:113], v[32:33]
v_pk_add_f32 v[114:115], v[114:115], v[34:35]
s_waitcnt lgkmcnt(1)
v_pk_add_f32 v[156:157], v[156:157], v[36:37]
v_pk_add_f32 v[158:159], v[158:159], v[38:39]
s_waitcnt lgkmcnt(0)
v_pk_add_f32 v[160:161], v[160:161], v[40:41]
v_pk_add_f32 v[162:163], v[162:163], v[42:43]
v_and_b32 v4, 1, v15
v_cmp_eq_u32 vcc, 1, v4
s_nop 1
v_cndmask_b32 v160, v160, v161, vcc
v_cndmask_b32 v162, v162, v163, vcc
v_and_b32 v4, 2, v15
v_cmp_eq_u32 vcc, 2, v4
s_nop 1
v_cndmask_b32 v160, v160, v162, vcc
v_bfe_u32 v4, v15, 2, 2
v_lshrrev_b32 v5, 4, v15
v_cmp_eq_u32 vcc, v4, v5
s_nop 1
v_cndmask_b32 v160, 0, v160, vcc
v_mov_b32 v161, v160
s_nop 1
v_permlane16_swap_b32 v160, v161
v_add_f32 v160, v160, v161
v_mov_b32 v161, v160
s_nop 1
v_permlane32_swap_b32 v160, v161
v_add_f32 v160, v160, v161
v_mul_f32 v8, 0x3acccccd, v156
v_mul_f32 v160, 0x3acccccd, v160
v_fma_f32 v160, -v8, v8, v160
v_max_f32 v160, 0, v160
v_add_f32 v160, 0x3727c5ac, v160
v_rsq_f32 v10, v160
v_sub_f32 v8, 0, v8
v_pk_fma_f32 v[96:97], v[56:57], v[8:9], v[96:97] op_sel_hi:[1,0,1]
v_pk_mul_f32 v[96:97], v[96:97], v[10:11] op_sel_hi:[1,0]
v_pk_fma_f32 v[98:99], v[58:59], v[8:9], v[98:99] op_sel_hi:[1,0,1]
v_pk_mul_f32 v[98:99], v[98:99], v[10:11] op_sel_hi:[1,0]
v_pk_fma_f32 v[100:101], v[60:61], v[8:9], v[100:101] op_sel_hi:[1,0,1]
v_pk_mul_f32 v[100:101], v[100:101], v[10:11] op_sel_hi:[1,0]
v_pk_fma_f32 v[102:103], v[62:63], v[8:9], v[102:103] op_sel_hi:[1,0,1]
v_pk_mul_f32 v[102:103], v[102:103], v[10:11] op_sel_hi:[1,0]
v_pk_fma_f32 v[104:105], v[64:65], v[8:9], v[104:105] op_sel_hi:[1,0,1]
v_pk_mul_f32 v[104:105], v[104:105], v[10:11] op_sel_hi:[1,0]
v_pk_fma_f32 v[106:107], v[66:67], v[8:9], v[106:107] op_sel_hi:[1,0,1]
v_pk_mul_f32 v[106:107], v[106:107], v[10:11] op_sel_hi:[1,0]
v_pk_fma_f32 v[108:109], v[68:69], v[8:9], v[108:109] op_sel_hi:[1,0,1]
v_pk_mul_f32 v[108:109], v[108:109], v[10:11] op_sel_hi:[1,0]
v_pk_fma_f32 v[110:111], v[70:71], v[8:9], v[110:111] op_sel_hi:[1,0,1]
v_pk_mul_f32 v[110:111], v[110:111], v[10:11] op_sel_hi:[1,0]
v_pk_fma_f32 v[112:113], v[72:73], v[8:9], v[112:113] op_sel_hi:[1,0,1]
v_pk_mul_f32 v[112:113], v[112:113], v[10:11] op_sel_hi:[1,0]
v_pk_fma_f32 v[114:115], v[74:75], v[8:9], v[114:115] op_sel_hi:[1,0,1]
v_pk_mul_f32 v[114:115], v[114:115], v[10:11] op_sel_hi:[1,0]
s_waitcnt vmcnt(0)
v_cmp_gt_u32 vcc, v13, v14
s_nop 0
s_cmp_eq_u64 vcc, 0
s_cselect_b32 s35, 0, 0x40000000
s_add_u32 s48, s34, 0
s_cmp_ge_u32 s31, 2
s_cselect_b32 s48, s48, 0x40000000
s_or_b32 s48, s48, s35
s_add_u32 s49, s48, 4096
buffer_load_dwordx4 v[192:195], v0, s[40:43], s48 offen sc1
buffer_load_dwordx4 v[196:199], v0, s[40:43], s48 offen offset:1024 sc1
buffer_load_dwordx4 v[200:203], v0, s[40:43], s48 offen offset:2048 sc1
buffer_load_dwordx4 v[204:207], v0, s[40:43], s48 offen offset:3072 sc1
buffer_load_dwordx4 v[208:211], v0, s[40:43], s49 offen sc1
buffer_load_dwordx4 v[212:215], v0, s[40:43], s49 offen offset:1024 sc1
s_add_u32 s48, s34, 196608
s_cmp_eq_u32 s31, 7
s_cselect_b32 s48, s48, 0x40000000
s_or_b32 s48, s48, s35
s_add_u32 s49, s48, 4096
buffer_load_dwordx4 v[56:59], v0, s[40:43], s48 offen sc1
buffer_load_dwordx4 v[60:63], v0, s[40:43], s48 offen offset:1024 sc1
buffer_load_dwordx4 v[64:67], v0, s[40:43], s48 offen offset:2048 sc1
buffer_load_dwordx4 v[68:71], v0, s[40:43], s48 offen offset:3072 sc1
buffer_load_dwordx4 v[72:75], v0, s[40:43], s49 offen sc1
buffer_load_dwordx4 v[76:79], v0, s[40:43], s49 offen offset:1024 sc1
s_add_u32 s48, s34, 589824
s_cmp_eq_u32 s31, 7
s_cselect_b32 s48, s48, 0x40000000
s_or_b32 s48, s48, s35
s_add_u32 s49, s48, 4096
buffer_load_dwordx4 v[164:167], v0, s[40:43], s48 offen sc1
buffer_load_dwordx4 v[168:171], v0, s[40:43], s48 offen offset:1024 sc1
buffer_load_dwordx4 v[172:175], v0, s[40:43], s48 offen offset:2048 sc1
buffer_load_dwordx4 v[176:179], v0, s[40:43], s48 offen offset:3072 sc1
buffer_load_dwordx4 v[180:183], v0, s[40:43], s49 offen sc1
)"
R"(buffer_load_dwordx4 v[184:187], v0, s[40:43], s49 offen offset:1024 sc1
v_max3_f32 v8, v96, v97, v98
v_max3_f32 v8, v8, v99, v100
v_max3_f32 v8, v8, v101, v102
v_max3_f32 v8, v8, v103, v104
v_max3_f32 v8, v8, v105, v106
v_max3_f32 v8, v8, v107, v108
v_max3_f32 v8, v8, v109, v110
v_max3_f32 v8, v8, v111, v112
v_max3_f32 v8, v8, v113, v114
v_max_f32 v8, v8, v115
v_min3_f32 v9, v96, v97, v98
v_min3_f32 v9, v9, v99, v100
v_min3_f32 v9, v9, v101, v102
v_min3_f32 v9, v9, v103, v104
v_min3_f32 v9, v9, v105, v106
v_min3_f32 v9, v9, v107, v108
v_min3_f32 v9, v9, v109, v110
v_min3_f32 v9, v9, v111, v112
v_min3_f32 v9, v9, v113, v114
v_min_f32 v9, v9, v115
v_mul_f32 v8, 0xbfb8aa3b, v8
v_mul_f32 v10, 0x3fb8aa3b, v9
v_pk_fma_f32 v[16:17], v[96:97], s[22:23], v[8:9] op_sel_hi:[1,1,0]
v_pk_fma_f32 v[36:37], v[96:97], s[24:25], v[10:11] op_sel_hi:[1,1,0]
v_pk_fma_f32 v[18:19], v[98:99], s[22:23], v[8:9] op_sel_hi:[1,1,0]
v_pk_fma_f32 v[38:39], v[98:99], s[24:25], v[10:11] op_sel_hi:[1,1,0]
v_pk_fma_f32 v[20:21], v[100:101], s[22:23], v[8:9] op_sel_hi:[1,1,0]
v_pk_fma_f32 v[40:41], v[100:101], s[24:25], v[10:11] op_sel_hi:[1,1,0]
v_pk_fma_f32 v[22:23], v[102:103], s[22:23], v[8:9] op_sel_hi:[1,1,0]
v_pk_fma_f32 v[42:43], v[102:103], s[24:25], v[10:11] op_sel_hi:[1,1,0]
v_pk_fma_f32 v[24:25], v[104:105], s[22:23], v[8:9] op_sel_hi:[1,1,0]
v_pk_fma_f32 v[44:45], v[104:105], s[24:25], v[10:11] op_sel_hi:[1,1,0]
v_pk_fma_f32 v[26:27], v[106:107], s[22:23], v[8:9] op_sel_hi:[1,1,0]
v_pk_fma_f32 v[46:47], v[106:107], s[24:25], v[10:11] op_sel_hi:[1,1,0]
v_pk_fma_f32 v[28:29], v[108:109], s[22:23], v[8:9] op_sel_hi:[1,1,0]
v_pk_fma_f32 v[48:49], v[108:109], s[24:25], v[10:11] op_sel_hi:[1,1,0]
v_pk_fma_f32 v[30:31], v[110:111], s[22:23], v[8:9] op_sel_hi:[1,1,0]
v_pk_fma_f32 v[50:51], v[110:111], s[24:25], v[10:11] op_sel_hi:[1,1,0]
v_pk_fma_f32 v[32:33], v[112:113], s[22:23], v[8:9] op_sel_hi:[1,1,0]
v_pk_fma_f32 v[52:53], v[112:113], s[24:25], v[10:11] op_sel_hi:[1,1,0]
v_pk_fma_f32 v[34:35], v[114:115], s[22:23], v[8:9] op_sel_hi:[1,1,0]
v_pk_fma_f32 v[54:55], v[114:115], s[24:25], v[10:11] op_sel_hi:[1,1,0]
v_exp_f32 v16, v16
v_exp_f32 v17, v17
v_exp_f32 v18, v18
v_exp_f32 v19, v19
v_exp_f32 v20, v20
v_exp_f32 v21, v21
v_exp_f32 v22, v22
v_exp_f32 v23, v23
v_exp_f32 v24, v24
v_exp_f32 v25, v25
v_exp_f32 v26, v26
v_exp_f32 v27, v27
v_exp_f32 v28, v28
v_exp_f32 v29, v29
v_exp_f32 v30, v30
v_exp_f32 v31, v31
v_exp_f32 v32, v32
v_exp_f32 v33, v33
v_exp_f32 v34, v34
v_exp_f32 v35, v35
v_exp_f32 v36, v36
v_exp_f32 v37, v37
v_exp_f32 v38, v38
v_exp_f32 v39, v39
v_exp_f32 v40, v40
v_exp_f32 v41, v41
v_exp_f32 v42, v42
v_exp_f32 v43, v43
v_exp_f32 v44, v44
v_exp_f32 v45, v45
v_exp_f32 v46, v46
v_exp_f32 v47, v47
v_exp_f32 v48, v48
v_exp_f32 v49, v49
v_exp_f32 v50, v50
v_exp_f32 v51, v51
v_exp_f32 v52, v52
v_exp_f32 v53, v53
v_exp_f32 v54, v54
v_exp_f32 v55, v55
v_pk_add_f32 v[8:9], v[16:17], v[18:19]
v_pk_add_f32 v[8:9], v[8:9], v[20:21]
v_pk_add_f32 v[8:9], v[8:9], v[22:23]
v_pk_add_f32 v[8:9], v[8:9], v[24:25]
v_pk_add_f32 v[8:9], v[8:9], v[26:27]
v_pk_add_f32 v[8:9], v[8:9], v[28:29]
v_pk_add_f32 v[8:9], v[8:9], v[30:31]
v_pk_add_f32 v[8:9], v[8:9], v[32:33]
v_pk_add_f32 v[8:9], v[8:9], v[34:35]
v_add_f32 v8, v8, v9
v_pk_add_f32 v[10:11], v[36:37], v[38:39]
v_pk_add_f32 v[10:11], v[10:11], v[40:41]
v_pk_add_f32 v[10:11], v[10:11], v[42:43]
v_pk_add_f32 v[10:11], v[10:11], v[44:45]
v_pk_add_f32 v[10:11], v[10:11], v[46:47]
v_pk_add_f32 v[10:11], v[10:11], v[48:49]
v_pk_add_f32 v[10:11], v[10:11], v[50:51]
v_pk_add_f32 v[10:11], v[10:11], v[52:53]
v_pk_add_f32 v[10:11], v[10:11], v[54:55]
v_add_f32 v10, v10, v11
v_rcp_f32 v8, v8
v_rcp_f32 v10, v10
v_pk_mul_f32 v[16:17], v[16:17], v[8:9] op_sel_hi:[1,0]
v_pk_mul_f32 v[18:19], v[18:19], v[8:9] op_sel_hi:[1,0]
v_pk_mul_f32 v[20:21], v[20:21], v[8:9] op_sel_hi:[1,0]
v_pk_mul_f32 v[22:23], v[22:23], v[8:9] op_sel_hi:[1,0]
v_pk_mul_f32 v[24:25], v[24:25], v[8:9] op_sel_hi:[1,0]
v_pk_mul_f32 v[26:27], v[26:27], v[8:9] op_sel_hi:[1,0]
v_pk_mul_f32 v[28:29], v[28:29], v[8:9] op_sel_hi:[1,0]
v_pk_mul_f32 v[30:31], v[30:31], v[8:9] op_sel_hi:[1,0]
v_pk_mul_f32 v[32:33], v[32:33], v[8:9] op_sel_hi:[1,0]
v_pk_mul_f32 v[34:35], v[34:35], v[8:9] op_sel_hi:[1,0]
v_pk_mul_f32 v[36:37], v[36:37], v[10:11] op_sel_hi:[1,0]
v_pk_mul_f32 v[38:39], v[38:39], v[10:11] op_sel_hi:[1,0]
v_pk_mul_f32 v[40:41], v[40:41], v[10:11] op_sel_hi:[1,0]
v_pk_mul_f32 v[42:43], v[42:43], v[10:11] op_sel_hi:[1,0]
v_pk_mul_f32 v[44:45], v[44:45], v[10:11] op_sel_hi:[1,0]
v_pk_mul_f32 v[46:47], v[46:47], v[10:11] op_sel_hi:[1,0]
v_pk_mul_f32 v[48:49], v[48:49], v[10:11] op_sel_hi:[1,0]
v_pk_mul_f32 v[50:51], v[50:51], v[10:11] op_sel_hi:[1,0]
v_pk_mul_f32 v[52:53], v[52:53], v[10:11] op_sel_hi:[1,0]
v_pk_mul_f32 v[54:55], v[54:55], v[10:11] op_sel_hi:[1,0]
v_cvt_pk_bf16_f32 v216, v16, v17
v_cvt_pk_bf16_f32 v217, v18, v19
v_cvt_pk_bf16_f32 v218, v20, v21
v_cvt_pk_bf16_f32 v219, v22, v23
v_cvt_pk_bf16_f32 v220, v24, v25
v_cvt_pk_bf16_f32 v221, v26, v27
v_cvt_pk_bf16_f32 v222, v28, v29
v_cvt_pk_bf16_f32 v223, v30, v31
v_cvt_pk_bf16_f32 v224, v32, v33
v_cvt_pk_bf16_f32 v225, v34, v35
v_cvt_pk_bf16_f32 v226, v36, v37
v_cvt_pk_bf16_f32 v227, v38, v39
v_cvt_pk_bf16_f32 v228, v40, v41
v_cvt_pk_bf16_f32 v229, v42, v43
v_cvt_pk_bf16_f32 v230, v44, v45
v_cvt_pk_bf16_f32 v231, v46, v47
v_cvt_pk_bf16_f32 v232, v48, v49
v_cvt_pk_bf16_f32 v233, v50, v51
v_cvt_pk_bf16_f32 v234, v52, v53
v_cvt_pk_bf16_f32 v235, v54, v55
s_lshl3_add_u32 s48, s28, s27
s_mul_i32 s48, s48, 5120
s_add_u32 s48, s48, 149577728
s_add_u32 s49, s48, 4096
buffer_store_dwordx4 v[216:219], v0, s[40:43], s48 offen sc1
buffer_store_dwordx4 v[220:223], v0, s[40:43], s48 offen offset:1024 sc1
buffer_store_dwordx4 v[224:227], v0, s[40:43], s48 offen offset:2048 sc1
buffer_store_dwordx4 v[228:231], v0, s[40:43], s48 offen offset:3072 sc1
buffer_store_dwordx4 v[232:235], v0, s[40:43], s49 offen sc1
s_nop 1
s_branch hpbf_join_%=
hpbf_kv_%=:
ds_write_b128 v6, v[96:99] offset:0
ds_write_b128 v6, v[100:103] offset:1024
ds_write_b128 v6, v[104:107] offset:2048
ds_write_b128 v6, v[108:111] offset:3072
ds_write_b128 v6, v[112:115] offset:4096
ds_write_b128 v6, v[156:159] offset:5120
ds_write_b128 v6, v[160:163] offset:6144
s_waitcnt lgkmcnt(0)
s_barrier
ds_read_b128 v[16:19], v7 offset:0
ds_read_b128 v[20:23], v7 offset:1024
ds_read_b128 v[24:27], v7 offset:2048
ds_read_b128 v[28:31], v7 offset:3072
ds_read_b128 v[32:35], v7 offset:4096
ds_read_b128 v[36:39], v7 offset:5120
ds_read_b128 v[40:43], v7 offset:6144
ds_read_b128 v[44:47], v7 offset:7168
ds_read_b128 v[48:51], v7 offset:8192
ds_read_b128 v[52:55], v7 offset:9216
ds_read_b128 v[164:167], v7 offset:10240
ds_read_b128 v[168:171], v7 offset:11264
s_waitcnt lgkmcnt(11)
v_pk_add_f32 v[116:117], v[116:117], v[16:17]
v_pk_add_f32 v[118:119], v[118:119], v[18:19]
s_waitcnt lgkmcnt(10)
v_pk_add_f32 v[120:121], v[120:121], v[20:21]
v_pk_add_f32 v[122:123], v[122:123], v[22:23]
s_waitcnt lgkmcnt(9)
v_pk_add_f32 v[124:125], v[124:125], v[24:25]
v_pk_add_f32 v[126:127], v[126:127], v[26:27]
s_waitcnt lgkmcnt(8)
v_pk_add_f32 v[128:129], v[128:129], v[28:29]
v_pk_add_f32 v[130:131], v[130:131], v[30:31]
s_waitcnt lgkmcnt(7)
v_pk_add_f32 v[132:133], v[132:133], v[32:33]
v_pk_add_f32 v[134:135], v[134:135], v[34:35]
s_waitcnt lgkmcnt(6)
v_pk_add_f32 v[136:137], v[136:137], v[36:37]
v_pk_add_f32 v[138:139], v[138:139], v[38:39]
s_waitcnt lgkmcnt(5)
v_pk_add_f32 v[140:141], v[140:141], v[40:41]
v_pk_add_f32 v[142:143], v[142:143], v[42:43]
s_waitcnt lgkmcnt(4)
)"
R"(v_pk_add_f32 v[144:145], v[144:145], v[44:45]
v_pk_add_f32 v[146:147], v[146:147], v[46:47]
s_waitcnt lgkmcnt(3)
v_pk_add_f32 v[148:149], v[148:149], v[48:49]
v_pk_add_f32 v[150:151], v[150:151], v[50:51]
s_waitcnt lgkmcnt(2)
v_pk_add_f32 v[152:153], v[152:153], v[52:53]
v_pk_add_f32 v[154:155], v[154:155], v[54:55]
s_waitcnt lgkmcnt(1)
v_pk_add_f32 v[156:157], v[156:157], v[164:165]
v_pk_add_f32 v[158:159], v[158:159], v[166:167]
s_waitcnt lgkmcnt(0)
v_pk_add_f32 v[160:161], v[160:161], v[168:169]
v_pk_add_f32 v[162:163], v[162:163], v[170:171]
v_and_b32 v4, 1, v15
v_cmp_eq_u32 vcc, 1, v4
s_nop 1
v_cndmask_b32 v160, v160, v161, vcc
v_cndmask_b32 v162, v162, v163, vcc
v_and_b32 v4, 2, v15
v_cmp_eq_u32 vcc, 2, v4
s_nop 1
v_cndmask_b32 v160, v160, v162, vcc
v_bfe_u32 v4, v15, 2, 2
v_lshrrev_b32 v5, 4, v15
v_cmp_eq_u32 vcc, v4, v5
s_nop 1
v_cndmask_b32 v160, 0, v160, vcc
v_mov_b32 v161, v160
s_nop 1
v_permlane16_swap_b32 v160, v161
v_add_f32 v160, v160, v161
v_mov_b32 v161, v160
s_nop 1
v_permlane32_swap_b32 v160, v161
v_add_f32 v160, v160, v161
v_mul_f32 v8, 0x3acccccd, v156
v_mul_f32 v160, 0x3acccccd, v160
v_fma_f32 v160, -v8, v8, v160
v_max_f32 v160, 0, v160
v_add_f32 v160, 0x3727c5ac, v160
v_rsq_f32 v10, v160
v_sub_f32 v8, 0, v8
v_pk_fma_f32 v[116:117], v[56:57], v[8:9], v[116:117] op_sel_hi:[1,0,1]
v_pk_mul_f32 v[116:117], v[116:117], v[10:11] op_sel_hi:[1,0]
v_pk_fma_f32 v[118:119], v[58:59], v[8:9], v[118:119] op_sel_hi:[1,0,1]
v_pk_mul_f32 v[118:119], v[118:119], v[10:11] op_sel_hi:[1,0]
v_pk_fma_f32 v[120:121], v[60:61], v[8:9], v[120:121] op_sel_hi:[1,0,1]
v_pk_mul_f32 v[120:121], v[120:121], v[10:11] op_sel_hi:[1,0]
v_pk_fma_f32 v[122:123], v[62:63], v[8:9], v[122:123] op_sel_hi:[1,0,1]
v_pk_mul_f32 v[122:123], v[122:123], v[10:11] op_sel_hi:[1,0]
v_pk_fma_f32 v[124:125], v[64:65], v[8:9], v[124:125] op_sel_hi:[1,0,1]
v_pk_mul_f32 v[124:125], v[124:125], v[10:11] op_sel_hi:[1,0]
v_pk_fma_f32 v[126:127], v[66:67], v[8:9], v[126:127] op_sel_hi:[1,0,1]
v_pk_mul_f32 v[126:127], v[126:127], v[10:11] op_sel_hi:[1,0]
v_pk_fma_f32 v[128:129], v[68:69], v[8:9], v[128:129] op_sel_hi:[1,0,1]
v_pk_mul_f32 v[128:129], v[128:129], v[10:11] op_sel_hi:[1,0]
v_pk_fma_f32 v[130:131], v[70:71], v[8:9], v[130:131] op_sel_hi:[1,0,1]
v_pk_mul_f32 v[130:131], v[130:131], v[10:11] op_sel_hi:[1,0]
v_pk_fma_f32 v[132:133], v[72:73], v[8:9], v[132:133] op_sel_hi:[1,0,1]
v_pk_mul_f32 v[132:133], v[132:133], v[10:11] op_sel_hi:[1,0]
v_pk_fma_f32 v[134:135], v[74:75], v[8:9], v[134:135] op_sel_hi:[1,0,1]
v_pk_mul_f32 v[134:135], v[134:135], v[10:11] op_sel_hi:[1,0]
v_pk_fma_f32 v[136:137], v[76:77], v[8:9], v[136:137] op_sel_hi:[1,0,1]
v_pk_mul_f32 v[136:137], v[136:137], v[10:11] op_sel_hi:[1,0]
v_pk_fma_f32 v[138:139], v[78:79], v[8:9], v[138:139] op_sel_hi:[1,0,1]
v_pk_mul_f32 v[138:139], v[138:139], v[10:11] op_sel_hi:[1,0]
v_pk_fma_f32 v[140:141], v[80:81], v[8:9], v[140:141] op_sel_hi:[1,0,1]
v_pk_mul_f32 v[140:141], v[140:141], v[10:11] op_sel_hi:[1,0]
v_pk_fma_f32 v[142:143], v[82:83], v[8:9], v[142:143] op_sel_hi:[1,0,1]
v_pk_mul_f32 v[142:143], v[142:143], v[10:11] op_sel_hi:[1,0]
v_pk_fma_f32 v[144:145], v[84:85], v[8:9], v[144:145] op_sel_hi:[1,0,1]
v_pk_mul_f32 v[144:145], v[144:145], v[10:11] op_sel_hi:[1,0]
v_pk_fma_f32 v[146:147], v[86:87], v[8:9], v[146:147] op_sel_hi:[1,0,1]
v_pk_mul_f32 v[146:147], v[146:147], v[10:11] op_sel_hi:[1,0]
v_pk_fma_f32 v[148:149], v[88:89], v[8:9], v[148:149] op_sel_hi:[1,0,1]
v_pk_mul_f32 v[148:149], v[148:149], v[10:11] op_sel_hi:[1,0]
v_pk_fma_f32 v[150:151], v[90:91], v[8:9], v[150:151] op_sel_hi:[1,0,1]
v_pk_mul_f32 v[150:151], v[150:151], v[10:11] op_sel_hi:[1,0]
v_pk_fma_f32 v[152:153], v[92:93], v[8:9], v[152:153] op_sel_hi:[1,0,1]
v_pk_mul_f32 v[152:153], v[152:153], v[10:11] op_sel_hi:[1,0]
v_pk_fma_f32 v[154:155], v[94:95], v[8:9], v[154:155] op_sel_hi:[1,0,1]
v_pk_mul_f32 v[154:155], v[154:155], v[10:11] op_sel_hi:[1,0]
s_waitcnt vmcnt(0)
v_cmp_gt_u32 vcc, v13, v14
s_nop 0
s_cmp_eq_u64 vcc, 0
s_cselect_b32 s35, 0, 0x40000000
s_add_u32 s48, s34, 0
s_cmp_ge_u32 s31, 2
s_cselect_b32 s48, s48, 0x40000000
s_or_b32 s48, s48, s35
s_add_u32 s49, s48, 4096
buffer_load_dwordx4 v[192:195], v0, s[40:43], s48 offen sc1
buffer_load_dwordx4 v[196:199], v0, s[40:43], s48 offen offset:1024 sc1
buffer_load_dwordx4 v[200:203], v0, s[40:43], s48 offen offset:2048 sc1
buffer_load_dwordx4 v[204:207], v0, s[40:43], s48 offen offset:3072 sc1
buffer_load_dwordx4 v[208:211], v0, s[40:43], s49 offen sc1
buffer_load_dwordx4 v[212:215], v0, s[40:43], s49 offen offset:1024 sc1
s_add_u32 s48, s34, 196608
s_cmp_eq_u32 s31, 7
s_cselect_b32 s48, s48, 0x40000000
s_or_b32 s48, s48, s35
s_add_u32 s49, s48, 4096
buffer_load_dwordx4 v[56:59], v0, s[40:43], s48 offen sc1
buffer_load_dwordx4 v[60:63], v0, s[40:43], s48 offen offset:1024 sc1
buffer_load_dwordx4 v[64:67], v0, s[40:43], s48 offen offset:2048 sc1
buffer_load_dwordx4 v[68:71], v0, s[40:43], s48 offen offset:3072 sc1
buffer_load_dwordx4 v[72:75], v0, s[40:43], s49 offen sc1
buffer_load_dwordx4 v[76:79], v0, s[40:43], s49 offen offset:1024 sc1
s_add_u32 s48, s34, 589824
s_cmp_eq_u32 s31, 7
s_cselect_b32 s48, s48, 0x40000000
s_or_b32 s48, s48, s35
s_add_u32 s49, s48, 4096
buffer_load_dwordx4 v[164:167], v0, s[40:43], s48 offen sc1
buffer_load_dwordx4 v[168:171], v0, s[40:43], s48 offen offset:1024 sc1
buffer_load_dwordx4 v[172:175], v0, s[40:43], s48 offen offset:2048 sc1
buffer_load_dwordx4 v[176:179], v0, s[40:43], s48 offen offset:3072 sc1
buffer_load_dwordx4 v[180:183], v0, s[40:43], s49 offen sc1
buffer_load_dwordx4 v[184:187], v0, s[40:43], s49 offen offset:1024 sc1
v_max3_f32 v8, v116, v117, v118
v_max3_f32 v8, v8, v119, v120
v_max3_f32 v8, v8, v121, v122
v_max3_f32 v8, v8, v123, v124
v_max3_f32 v8, v8, v125, v126
v_max3_f32 v8, v8, v127, v128
v_max3_f32 v8, v8, v129, v130
v_max3_f32 v8, v8, v131, v132
v_max3_f32 v8, v8, v133, v134
v_max_f32 v8, v8, v135
v_min3_f32 v9, v116, v117, v118
v_min3_f32 v9, v9, v119, v120
v_min3_f32 v9, v9, v121, v122
v_min3_f32 v9, v9, v123, v124
v_min3_f32 v9, v9, v125, v126
v_min3_f32 v9, v9, v127, v128
v_min3_f32 v9, v9, v129, v130
v_min3_f32 v9, v9, v131, v132
v_min3_f32 v9, v9, v133, v134
v_min_f32 v9, v9, v135
v_mul_f32 v8, 0xbfb8aa3b, v8
v_mul_f32 v10, 0x3fb8aa3b, v9
v_pk_fma_f32 v[16:17], v[116:117], s[22:23], v[8:9] op_sel_hi:[1,1,0]
v_pk_fma_f32 v[36:37], v[116:117], s[24:25], v[10:11] op_sel_hi:[1,1,0]
v_pk_fma_f32 v[18:19], v[118:119], s[22:23], v[8:9] op_sel_hi:[1,1,0]
v_pk_fma_f32 v[38:39], v[118:119], s[24:25], v[10:11] op_sel_hi:[1,1,0]
v_pk_fma_f32 v[20:21], v[120:121], s[22:23], v[8:9] op_sel_hi:[1,1,0]
v_pk_fma_f32 v[40:41], v[120:121], s[24:25], v[10:11] op_sel_hi:[1,1,0]
v_pk_fma_f32 v[22:23], v[122:123], s[22:23], v[8:9] op_sel_hi:[1,1,0]
v_pk_fma_f32 v[42:43], v[122:123], s[24:25], v[10:11] op_sel_hi:[1,1,0]
v_pk_fma_f32 v[24:25], v[124:125], s[22:23], v[8:9] op_sel_hi:[1,1,0]
v_pk_fma_f32 v[44:45], v[124:125], s[24:25], v[10:11] op_sel_hi:[1,1,0]
v_pk_fma_f32 v[26:27], v[126:127], s[22:23], v[8:9] op_sel_hi:[1,1,0]
v_pk_fma_f32 v[46:47], v[126:127], s[24:25], v[10:11] op_sel_hi:[1,1,0]
v_pk_fma_f32 v[28:29], v[128:129], s[22:23], v[8:9] op_sel_hi:[1,1,0]
v_pk_fma_f32 v[48:49], v[128:129], s[24:25], v[10:11] op_sel_hi:[1,1,0]
v_pk_fma_f32 v[30:31], v[130:131], s[22:23], v[8:9] op_sel_hi:[1,1,0]
v_pk_fma_f32 v[50:51], v[130:131], s[24:25], v[10:11] op_sel_hi:[1,1,0]
v_pk_fma_f32 v[32:33], v[132:133], s[22:23], v[8:9] op_sel_hi:[1,1,0]
v_pk_fma_f32 v[52:53], v[132:133], s[24:25], v[10:11] op_sel_hi:[1,1,0]
v_pk_fma_f32 v[34:35], v[134:135], s[22:23], v[8:9] op_sel_hi:[1,1,0]
v_pk_fma_f32 v[54:55], v[134:135], s[24:25], v[10:11] op_sel_hi:[1,1,0]
v_exp_f32 v16, v16
v_exp_f32 v17, v17
v_exp_f32 v18, v18
v_exp_f32 v19, v19
v_exp_f32 v20, v20
v_exp_f32 v21, v21
v_exp_f32 v22, v22
v_exp_f32 v23, v23
v_exp_f32 v24, v24
v_exp_f32 v25, v25
v_exp_f32 v26, v26
v_exp_f32 v27, v27
v_exp_f32 v28, v28
v_exp_f32 v29, v29
v_exp_f32 v30, v30
v_exp_f32 v31, v31
v_exp_f32 v32, v32
v_exp_f32 v33, v33
v_exp_f32 v34, v34
v_exp_f32 v35, v35
v_exp_f32 v36, v36
v_exp_f32 v37, v37
v_exp_f32 v38, v38
v_exp_f32 v39, v39
v_exp_f32 v40, v40
v_exp_f32 v41, v41
v_exp_f32 v42, v42
v_exp_f32 v43, v43
v_exp_f32 v44, v44
v_exp_f32 v45, v45
v_exp_f32 v46, v46
v_exp_f32 v47, v47
v_exp_f32 v48, v48
v_exp_f32 v49, v49
v_exp_f32 v50, v50
v_exp_f32 v51, v51
v_exp_f32 v52, v52
)"
R"(v_exp_f32 v53, v53
v_exp_f32 v54, v54
v_exp_f32 v55, v55
v_pk_add_f32 v[8:9], v[16:17], v[18:19]
v_pk_add_f32 v[8:9], v[8:9], v[20:21]
v_pk_add_f32 v[8:9], v[8:9], v[22:23]
v_pk_add_f32 v[8:9], v[8:9], v[24:25]
v_pk_add_f32 v[8:9], v[8:9], v[26:27]
v_pk_add_f32 v[8:9], v[8:9], v[28:29]
v_pk_add_f32 v[8:9], v[8:9], v[30:31]
v_pk_add_f32 v[8:9], v[8:9], v[32:33]
v_pk_add_f32 v[8:9], v[8:9], v[34:35]
v_add_f32 v8, v8, v9
v_pk_add_f32 v[10:11], v[36:37], v[38:39]
v_pk_add_f32 v[10:11], v[10:11], v[40:41]
v_pk_add_f32 v[10:11], v[10:11], v[42:43]
v_pk_add_f32 v[10:11], v[10:11], v[44:45]
v_pk_add_f32 v[10:11], v[10:11], v[46:47]
v_pk_add_f32 v[10:11], v[10:11], v[48:49]
v_pk_add_f32 v[10:11], v[10:11], v[50:51]
v_pk_add_f32 v[10:11], v[10:11], v[52:53]
v_pk_add_f32 v[10:11], v[10:11], v[54:55]
v_add_f32 v10, v10, v11
v_rcp_f32 v8, v8
v_rcp_f32 v10, v10
v_pk_mul_f32 v[16:17], v[16:17], v[8:9] op_sel_hi:[1,0]
v_pk_mul_f32 v[18:19], v[18:19], v[8:9] op_sel_hi:[1,0]
v_pk_mul_f32 v[20:21], v[20:21], v[8:9] op_sel_hi:[1,0]
v_pk_mul_f32 v[22:23], v[22:23], v[8:9] op_sel_hi:[1,0]
v_pk_mul_f32 v[24:25], v[24:25], v[8:9] op_sel_hi:[1,0]
v_pk_mul_f32 v[26:27], v[26:27], v[8:9] op_sel_hi:[1,0]
v_pk_mul_f32 v[28:29], v[28:29], v[8:9] op_sel_hi:[1,0]
v_pk_mul_f32 v[30:31], v[30:31], v[8:9] op_sel_hi:[1,0]
v_pk_mul_f32 v[32:33], v[32:33], v[8:9] op_sel_hi:[1,0]
v_pk_mul_f32 v[34:35], v[34:35], v[8:9] op_sel_hi:[1,0]
v_pk_mul_f32 v[36:37], v[36:37], v[10:11] op_sel_hi:[1,0]
v_pk_mul_f32 v[38:39], v[38:39], v[10:11] op_sel_hi:[1,0]
v_pk_mul_f32 v[40:41], v[40:41], v[10:11] op_sel_hi:[1,0]
v_pk_mul_f32 v[42:43], v[42:43], v[10:11] op_sel_hi:[1,0]
v_pk_mul_f32 v[44:45], v[44:45], v[10:11] op_sel_hi:[1,0]
v_pk_mul_f32 v[46:47], v[46:47], v[10:11] op_sel_hi:[1,0]
v_pk_mul_f32 v[48:49], v[48:49], v[10:11] op_sel_hi:[1,0]
v_pk_mul_f32 v[50:51], v[50:51], v[10:11] op_sel_hi:[1,0]
v_pk_mul_f32 v[52:53], v[52:53], v[10:11] op_sel_hi:[1,0]
v_pk_mul_f32 v[54:55], v[54:55], v[10:11] op_sel_hi:[1,0]
v_cvt_pk_bf16_f32 v216, v16, v17
v_cvt_pk_bf16_f32 v217, v18, v19
v_cvt_pk_bf16_f32 v218, v20, v21
v_cvt_pk_bf16_f32 v219, v22, v23
v_cvt_pk_bf16_f32 v220, v24, v25
v_cvt_pk_bf16_f32 v221, v26, v27
v_cvt_pk_bf16_f32 v222, v28, v29
v_cvt_pk_bf16_f32 v223, v30, v31
v_cvt_pk_bf16_f32 v224, v32, v33
v_cvt_pk_bf16_f32 v225, v34, v35
v_cvt_pk_bf16_f32 v226, v36, v37
v_cvt_pk_bf16_f32 v227, v38, v39
v_cvt_pk_bf16_f32 v228, v40, v41
v_cvt_pk_bf16_f32 v229, v42, v43
v_cvt_pk_bf16_f32 v230, v44, v45
v_cvt_pk_bf16_f32 v231, v46, v47
v_cvt_pk_bf16_f32 v232, v48, v49
v_cvt_pk_bf16_f32 v233, v50, v51
v_cvt_pk_bf16_f32 v234, v52, v53
v_cvt_pk_bf16_f32 v235, v54, v55
s_lshl_b32 s48, s29, 4
v_mov_b32 v236, 0
v_mov_b32 v237, 0
v_mov_b32 v238, 0
v_mov_b32 v239, 0
v_cvt_pk_bf16_f32 v240, v136, v137
v_cvt_pk_bf16_f32 v241, v138, v139
v_cvt_pk_bf16_f32 v242, v140, v141
v_cvt_pk_bf16_f32 v243, v142, v143
v_cvt_pk_bf16_f32 v244, v144, v145
v_cvt_pk_bf16_f32 v245, v146, v147
v_cvt_pk_bf16_f32 v246, v148, v149
v_cvt_pk_bf16_f32 v247, v150, v151
v_cvt_pk_bf16_f32 v248, v152, v153
v_cvt_pk_bf16_f32 v249, v154, v155
v_mov_b32 v250, 0x3f80
v_mov_b32 v251, 0
v_and_b32 v4, 15, v15
v_lshrrev_b32 v5, 4, v15
v_lshl_add_u32 v4, v5, 5, v4
v_add_u32 v4, s48, v4
v_mul_u32_u24 v4, 176, v4
ds_write_b128 v4, v[216:219] offset:0
ds_write_b128 v4, v[220:223] offset:16
ds_write_b128 v4, v[224:227] offset:32
ds_write_b128 v4, v[228:231] offset:48
ds_write_b128 v4, v[232:235] offset:64
ds_write_b128 v4, v[236:239] offset:80
ds_write_b128 v4, v[240:243] offset:96
ds_write_b128 v4, v[244:247] offset:112
ds_write_b128 v4, v[248:251] offset:128
ds_write_b128 v4, v[236:239] offset:144
hpbf_join_%=:
s_cmp_eq_u32 s35, 0
s_cbranch_scc1 hpbf_have_%=
s_mov_b32 s60, 0
hpbf_prev_%=:
v_mov_b32 v4, 0
buffer_atomic_add v4, v12, s[40:43], s58 offen sc0
s_waitcnt vmcnt(0)
v_cmp_gt_u32 vcc, v13, v4
s_nop 4
s_cbranch_vccz hpbf_prev_ok_%=
s_sleep 1
s_add_u32 s60, s60, 1
s_cmp_gt_u32 s60, 65536
s_cbranch_scc1 hp_abort_%=
s_and_b32 s59, s60, 63
s_cbranch_scc1 hpbf_prev_%=
v_mov_b32 v4, 1024
buffer_load_dword v4, v4, s[40:43], s58 offen sc1
s_waitcnt vmcnt(0)
v_readfirstlane_b32 s59, v4
s_nop 0
s_cmp_lg_u32 s59, 0
s_nop 2
s_cbranch_scc1 hp_end_%=
s_branch hpbf_prev_%=
hpbf_prev_ok_%=:
s_mov_b32 s35, 0
s_add_u32 s48, s34, 0
s_cmp_ge_u32 s31, 2
s_cselect_b32 s48, s48, 0x40000000
s_or_b32 s48, s48, s35
s_add_u32 s49, s48, 4096
buffer_load_dwordx4 v[192:195], v0, s[40:43], s48 offen sc1
buffer_load_dwordx4 v[196:199], v0, s[40:43], s48 offen offset:1024 sc1
buffer_load_dwordx4 v[200:203], v0, s[40:43], s48 offen offset:2048 sc1
buffer_load_dwordx4 v[204:207], v0, s[40:43], s48 offen offset:3072 sc1
buffer_load_dwordx4 v[208:211], v0, s[40:43], s49 offen sc1
buffer_load_dwordx4 v[212:215], v0, s[40:43], s49 offen offset:1024 sc1
s_add_u32 s48, s34, 196608
s_cmp_eq_u32 s31, 7
s_cselect_b32 s48, s48, 0x40000000
s_or_b32 s48, s48, s35
s_add_u32 s49, s48, 4096
buffer_load_dwordx4 v[56:59], v0, s[40:43], s48 offen sc1
buffer_load_dwordx4 v[60:63], v0, s[40:43], s48 offen offset:1024 sc1
buffer_load_dwordx4 v[64:67], v0, s[40:43], s48 offen offset:2048 sc1
buffer_load_dwordx4 v[68:71], v0, s[40:43], s48 offen offset:3072 sc1
buffer_load_dwordx4 v[72:75], v0, s[40:43], s49 offen sc1
buffer_load_dwordx4 v[76:79], v0, s[40:43], s49 offen offset:1024 sc1
s_add_u32 s48, s34, 589824
s_cmp_eq_u32 s31, 7
s_cselect_b32 s48, s48, 0x40000000
s_or_b32 s48, s48, s35
s_add_u32 s49, s48, 4096
buffer_load_dwordx4 v[164:167], v0, s[40:43], s48 offen sc1
buffer_load_dwordx4 v[168:171], v0, s[40:43], s48 offen offset:1024 sc1
buffer_load_dwordx4 v[172:175], v0, s[40:43], s48 offen offset:2048 sc1
buffer_load_dwordx4 v[176:179], v0, s[40:43], s48 offen offset:3072 sc1
buffer_load_dwordx4 v[180:183], v0, s[40:43], s49 offen sc1
buffer_load_dwordx4 v[184:187], v0, s[40:43], s49 offen offset:1024 sc1
hpbf_have_%=:
v_lshrrev_b32 v5, 2, v15
s_lshl_b32 s48, s45, 5
v_add_u32 v5, s48, v5
v_mul_u32_u24 v5, 176, v5
v_and_b32 v6, 3, v15
v_lshl_add_u32 v6, v6, 3, v5
s_waitcnt lgkmcnt(0)
s_barrier
ds_read_b64_tr_b16 v[96:97], v6 offset:0
ds_read_b64_tr_b16 v[98:99], v6 offset:2816
ds_read_b64_tr_b16 v[100:101], v6 offset:32
ds_read_b64_tr_b16 v[102:103], v6 offset:2848
ds_read_b64_tr_b16 v[104:105], v6 offset:64
ds_read_b64_tr_b16 v[106:107], v6 offset:2880
ds_read_b64_tr_b16 v[108:109], v6 offset:96
ds_read_b64_tr_b16 v[110:111], v6 offset:2912
ds_read_b64_tr_b16 v[112:113], v6 offset:128
ds_read_b64_tr_b16 v[114:115], v6 offset:2944
s_waitcnt lgkmcnt(2)
v_mfma_f32_16x16x32_bf16 v[120:123], v[96:99], v[108:111], 0
s_waitcnt lgkmcnt(0)
v_mfma_f32_16x16x32_bf16 v[124:127], v[96:99], v[112:115], 0
v_mfma_f32_16x16x32_bf16 v[128:131], v[100:103], v[108:111], 0
v_mfma_f32_16x16x32_bf16 v[132:135], v[100:103], v[112:115], 0
v_mfma_f32_16x16x32_bf16 v[136:139], v[104:107], v[108:111], 0
v_mfma_f32_16x16x32_bf16 v[140:143], v[104:107], v[112:115], 0
s_waitcnt vmcnt(0)
v_pk_add_f32 v[192:193], v[192:193], v[56:57]
v_pk_add_f32 v[194:195], v[194:195], v[58:59]
v_pk_add_f32 v[196:197], v[196:197], v[60:61]
v_pk_add_f32 v[198:199], v[198:199], v[62:63]
v_pk_add_f32 v[200:201], v[200:201], v[64:65]
v_pk_add_f32 v[202:203], v[202:203], v[66:67]
v_pk_add_f32 v[204:205], v[204:205], v[68:69]
v_pk_add_f32 v[206:207], v[206:207], v[70:71]
v_pk_add_f32 v[208:209], v[208:209], v[72:73]
v_pk_add_f32 v[210:211], v[210:211], v[74:75]
v_pk_add_f32 v[212:213], v[212:213], v[76:77]
v_pk_add_f32 v[214:215], v[214:215], v[78:79]
v_pk_add_f32 v[120:121], v[120:121], v[164:165]
v_pk_add_f32 v[122:123], v[122:123], v[166:167]
)"
R"(v_pk_add_f32 v[124:125], v[124:125], v[168:169]
v_pk_add_f32 v[126:127], v[126:127], v[170:171]
v_pk_add_f32 v[128:129], v[128:129], v[172:173]
v_pk_add_f32 v[130:131], v[130:131], v[174:175]
v_pk_add_f32 v[132:133], v[132:133], v[176:177]
v_pk_add_f32 v[134:135], v[134:135], v[178:179]
v_pk_add_f32 v[136:137], v[136:137], v[180:181]
v_pk_add_f32 v[138:139], v[138:139], v[182:183]
v_pk_add_f32 v[140:141], v[140:141], v[184:185]
v_pk_add_f32 v[142:143], v[142:143], v[186:187]
v_pk_add_f32 v[192:193], v[192:193], v[120:121]
v_pk_add_f32 v[194:195], v[194:195], v[122:123]
v_pk_add_f32 v[196:197], v[196:197], v[124:125]
v_pk_add_f32 v[198:199], v[198:199], v[126:127]
v_pk_add_f32 v[200:201], v[200:201], v[128:129]
v_pk_add_f32 v[202:203], v[202:203], v[130:131]
v_pk_add_f32 v[204:205], v[204:205], v[132:133]
v_pk_add_f32 v[206:207], v[206:207], v[134:135]
v_pk_add_f32 v[208:209], v[208:209], v[136:137]
v_pk_add_f32 v[210:211], v[210:211], v[138:139]
v_pk_add_f32 v[212:213], v[212:213], v[140:141]
v_pk_add_f32 v[214:215], v[214:215], v[142:143]
s_cmp_eq_u32 s31, 7
s_nop 0
s_cbranch_scc1 hpbf_fin_%=
s_add_u32 s48, s34, 393216
s_add_u32 s49, s48, 4096
buffer_store_dwordx4 v[192:195], v0, s[40:43], s48 offen sc1
buffer_store_dwordx4 v[196:199], v0, s[40:43], s48 offen offset:1024 sc1
buffer_store_dwordx4 v[200:203], v0, s[40:43], s48 offen offset:2048 sc1
buffer_store_dwordx4 v[204:207], v0, s[40:43], s48 offen offset:3072 sc1
buffer_store_dwordx4 v[208:211], v0, s[40:43], s49 offen sc1
buffer_store_dwordx4 v[212:215], v0, s[40:43], s49 offen offset:1024 sc1
s_lshl_b32 s54, s37, 11
s_add_u32 s54, s54, s36
s_add_u32 s54, s54, s58
s_branch hp_done_%=
hpbf_fin_%=:
v_bfe_u32 v4, v15, 2, 2
v_mul_u32_u24 v4, 1280, v4
v_lshrrev_b32 v5, 5, v15
v_lshl_add_u32 v4, v5, 8, v4
v_and_b32 v5, 3, v15
v_lshl_add_u32 v4, v5, 4, v4
v_bfe_u32 v5, v15, 4, 1
v_lshl_add_u32 v8, v5, 3, v4
v_and_b32 v4, 8, v15
v_cmp_eq_u32 vcc, 0, v4
v_mov_b32 v9, v8
v_mov_b32 v4, 2147483632
v_cndmask_b32 v9, v4, v9, vcc
v_cmp_gt_u32 vcc, 32, v15
v_mov_b32 v10, v8
v_mov_b32 v4, 2147483632
v_cndmask_b32 v10, v4, v10, vcc
v_mov_b32 v11, v9
v_mov_b32 v4, 2147483632
v_cndmask_b32 v11, v4, v11, vcc
s_mul_i32 s48, s27, 7680
s_lshl_b32 s49, s45, 6
s_add_u32 s48, s48, s49
s_add_u32 s48, s48, 162095104
s_add_u32 s49, s48, 5120
v_cvt_pk_bf16_f32 v96, v192, v193
v_cvt_pk_bf16_f32 v97, v194, v195
buffer_store_dwordx2 v[96:97], v8, s[40:43], s48 offen sc1
v_cvt_pk_bf16_f32 v98, v196, v197
v_cvt_pk_bf16_f32 v99, v198, v199
buffer_store_dwordx2 v[98:99], v9, s[40:43], s49 offen sc1
v_cvt_pk_bf16_f32 v100, v200, v201
v_cvt_pk_bf16_f32 v101, v202, v203
buffer_store_dwordx2 v[100:101], v8, s[40:43], s48 offen offset:512 sc1
v_cvt_pk_bf16_f32 v102, v204, v205
v_cvt_pk_bf16_f32 v103, v206, v207
buffer_store_dwordx2 v[102:103], v9, s[40:43], s49 offen offset:512 sc1
v_cvt_pk_bf16_f32 v104, v208, v209
v_cvt_pk_bf16_f32 v105, v210, v211
buffer_store_dwordx2 v[104:105], v10, s[40:43], s48 offen offset:1024 sc1
v_cvt_pk_bf16_f32 v106, v212, v213
v_cvt_pk_bf16_f32 v107, v214, v215
buffer_store_dwordx2 v[106:107], v11, s[40:43], s49 offen offset:1024 sc1
s_lshl_b32 s54, s21, 3
s_lshl_b32 s54, s54, 7
s_add_u32 s54, s54, 2370560
s_mov_b32 s55, 8
s_branch hp_donen_%=
hp_b1_%=:
s_and_b32 s26, s20, 7
s_lshr_b32 s27, s20, 3
s_lshl2_add_u32 s28, s27, s45
s_lshl_b32 s48, s45, 3
s_min_u32 s48, s48, 22
s_lshl_b32 s61, s48, 10
s_mul_i32 s50, s26, 307200
s_add_u32 s50, s50, s61
s_add_u32 s50, s50, 146210816
s_add_u32 m0, s61, 0
s_add_u32 s51, s50, 0
buffer_load_dwordx4 v0, s[40:43], s51 offen offset:0 lds
buffer_load_dwordx4 v0, s[40:43], s51 offen offset:1024 lds
buffer_load_dwordx4 v0, s[40:43], s51 offen offset:2048 lds
buffer_load_dwordx4 v0, s[40:43], s51 offen offset:3072 lds
s_add_u32 m0, s61, 4096
s_add_u32 s51, s50, 4096
buffer_load_dwordx4 v0, s[40:43], s51 offen offset:0 lds
buffer_load_dwordx4 v0, s[40:43], s51 offen offset:1024 lds
buffer_load_dwordx4 v0, s[40:43], s51 offen offset:2048 lds
buffer_load_dwordx4 v0, s[40:43], s51 offen offset:3072 lds
s_add_u32 m0, s61, 30720
s_add_u32 s51, s50, 30720
buffer_load_dwordx4 v0, s[40:43], s51 offen offset:0 lds
buffer_load_dwordx4 v0, s[40:43], s51 offen offset:1024 lds
buffer_load_dwordx4 v0, s[40:43], s51 offen offset:2048 lds
buffer_load_dwordx4 v0, s[40:43], s51 offen offset:3072 lds
s_add_u32 m0, s61, 34816
s_add_u32 s51, s50, 34816
buffer_load_dwordx4 v0, s[40:43], s51 offen offset:0 lds
buffer_load_dwordx4 v0, s[40:43], s51 offen offset:1024 lds
buffer_load_dwordx4 v0, s[40:43], s51 offen offset:2048 lds
buffer_load_dwordx4 v0, s[40:43], s51 offen offset:3072 lds
s_add_u32 m0, s61, 61440
s_add_u32 s51, s50, 61440
buffer_load_dwordx4 v0, s[40:43], s51 offen offset:0 lds
buffer_load_dwordx4 v0, s[40:43], s51 offen offset:1024 lds
buffer_load_dwordx4 v0, s[40:43], s51 offen offset:2048 lds
buffer_load_dwordx4 v0, s[40:43], s51 offen offset:3072 lds
s_add_u32 m0, s61, 65536
s_add_u32 s51, s50, 65536
buffer_load_dwordx4 v0, s[40:43], s51 offen offset:0 lds
buffer_load_dwordx4 v0, s[40:43], s51 offen offset:1024 lds
buffer_load_dwordx4 v0, s[40:43], s51 offen offset:2048 lds
buffer_load_dwordx4 v0, s[40:43], s51 offen offset:3072 lds
s_add_u32 m0, s61, 92160
s_add_u32 s51, s50, 92160
buffer_load_dwordx4 v0, s[40:43], s51 offen offset:0 lds
buffer_load_dwordx4 v0, s[40:43], s51 offen offset:1024 lds
buffer_load_dwordx4 v0, s[40:43], s51 offen offset:2048 lds
buffer_load_dwordx4 v0, s[40:43], s51 offen offset:3072 lds
s_add_u32 m0, s61, 96256
s_add_u32 s51, s50, 96256
buffer_load_dwordx4 v0, s[40:43], s51 offen offset:0 lds
buffer_load_dwordx4 v0, s[40:43], s51 offen offset:1024 lds
buffer_load_dwordx4 v0, s[40:43], s51 offen offset:2048 lds
buffer_load_dwordx4 v0, s[40:43], s51 offen offset:3072 lds
s_add_u32 m0, s61, 122880
s_add_u32 s51, s50, 122880
buffer_load_dwordx4 v0, s[40:43], s51 offen offset:0 lds
buffer_load_dwordx4 v0, s[40:43], s51 offen offset:1024 lds
buffer_load_dwordx4 v0, s[40:43], s51 offen offset:2048 lds
buffer_load_dwordx4 v0, s[40:43], s51 offen offset:3072 lds
s_add_u32 m0, s61, 126976
s_add_u32 s51, s50, 126976
buffer_load_dwordx4 v0, s[40:43], s51 offen offset:0 lds
buffer_load_dwordx4 v0, s[40:43], s51 offen offset:1024 lds
buffer_load_dwordx4 v0, s[40:43], s51 offen offset:2048 lds
buffer_load_dwordx4 v0, s[40:43], s51 offen offset:3072 lds
v_mov_b32 v188, 0x3f803f80
v_mov_b32 v189, 0x3f803f80
v_mov_b32 v190, 0x3f803f80
v_mov_b32 v191, 0x3f803f80
v_add_u32 v6, 61440, v0
v_add_u32 v7, 122880, v0
s_lshl_b32 s54, s21, 5
s_add_u32 s54, s54, s27
s_lshl_b32 s54, s54, 7
s_add_u32 s54, s54, 2354176
s_waitcnt vmcnt(0)
s_cmp_lg_u32 s45, 0
s_cbranch_scc1 hpb1_dep_ok_%=
v_cmp_eq_u32 vcc, 0, v15
s_nop 1
v_cndmask_b32 v14, 0, 8, vcc
s_mov_b32 s60, 0
hpb1_dep_%=:
v_mov_b32 v4, 0
buffer_atomic_add v4, v2, s[40:43], s54 offen sc0
s_waitcnt vmcnt(0)
v_cmp_gt_u32 vcc, v14, v4
s_nop 4
s_cbranch_vccz hpb1_dep_ok_%=
s_sleep 1
s_add_u32 s60, s60, 1
s_cmp_gt_u32 s60, 65536
s_cbranch_scc1 hp_abort_%=
s_and_b32 s59, s60, 63
s_cbranch_scc1 hpb1_dep_%=
v_mov_b32 v4, 1024
buffer_load_dword v4, v4, s[40:43], s58 offen sc1
s_waitcnt vmcnt(0)
v_readfirstlane_b32 s59, v4
s_nop 0
s_cmp_lg_u32 s59, 0
s_nop 2
s_cbranch_scc1 hp_end_%=
s_branch hpb1_dep_%=
hpb1_dep_ok_%=:
s_barrier
s_mul_i32 s52, s28, 20480
s_add_u32 s52, s52, 143589376
buffer_load_dwordx4 v[16:19], v0, s[40:43], s52 offen sc1
)"
R"(buffer_load_dwordx4 v[20:23], v0, s[40:43], s52 offen offset:1024 sc1
buffer_load_dwordx4 v[24:27], v0, s[40:43], s52 offen offset:2048 sc1
buffer_load_dwordx4 v[28:31], v0, s[40:43], s52 offen offset:3072 sc1
s_add_u32 s52, s52, 4096
buffer_load_dwordx4 v[32:35], v0, s[40:43], s52 offen sc1
buffer_load_dwordx4 v[36:39], v0, s[40:43], s52 offen offset:1024 sc1
buffer_load_dwordx4 v[40:43], v0, s[40:43], s52 offen offset:2048 sc1
buffer_load_dwordx4 v[44:47], v0, s[40:43], s52 offen offset:3072 sc1
s_add_u32 s52, s52, 4096
buffer_load_dwordx4 v[48:51], v0, s[40:43], s52 offen sc1
buffer_load_dwordx4 v[52:55], v0, s[40:43], s52 offen offset:1024 sc1
buffer_load_dwordx4 v[56:59], v0, s[40:43], s52 offen offset:2048 sc1
buffer_load_dwordx4 v[60:63], v0, s[40:43], s52 offen offset:3072 sc1
s_add_u32 s52, s52, 4096
buffer_load_dwordx4 v[64:67], v0, s[40:43], s52 offen sc1
buffer_load_dwordx4 v[68:71], v0, s[40:43], s52 offen offset:1024 sc1
buffer_load_dwordx4 v[72:75], v0, s[40:43], s52 offen offset:2048 sc1
buffer_load_dwordx4 v[76:79], v0, s[40:43], s52 offen offset:3072 sc1
s_add_u32 s52, s52, 4096
buffer_load_dwordx4 v[80:83], v0, s[40:43], s52 offen sc1
buffer_load_dwordx4 v[84:87], v0, s[40:43], s52 offen offset:1024 sc1
buffer_load_dwordx4 v[88:91], v0, s[40:43], s52 offen offset:2048 sc1
buffer_load_dwordx4 v[92:95], v0, s[40:43], s52 offen offset:3072 sc1
ds_read_b128 v[164:167], v0 offset:0
ds_read_b128 v[168:171], v0 offset:1024
ds_read_b128 v[172:175], v0 offset:2048
ds_read_b128 v[176:179], v0 offset:3072
ds_read_b128 v[180:183], v0 offset:4096
ds_read_b128 v[184:187], v0 offset:5120
s_waitcnt vmcnt(19) lgkmcnt(5)
v_mfma_f32_16x16x32_bf16 v[96:99], v[164:167], v[16:19], 0
ds_read_b128 v[164:167], v0 offset:6144
s_waitcnt lgkmcnt(5)
v_mfma_f32_16x16x32_bf16 v[100:103], v[168:171], v[16:19], 0
ds_read_b128 v[168:171], v0 offset:7168
s_waitcnt lgkmcnt(5)
v_mfma_f32_16x16x32_bf16 v[104:107], v[172:175], v[16:19], 0
ds_read_b128 v[172:175], v0 offset:8192
s_waitcnt lgkmcnt(5)
v_mfma_f32_16x16x32_bf16 v[108:111], v[176:179], v[16:19], 0
ds_read_b128 v[176:179], v0 offset:9216
s_waitcnt lgkmcnt(5)
v_mfma_f32_16x16x32_bf16 v[112:115], v[180:183], v[16:19], 0
ds_read_b128 v[180:183], v0 offset:10240
s_waitcnt lgkmcnt(5)
v_mfma_f32_16x16x32_bf16 v[116:119], v[184:187], v[16:19], 0
ds_read_b128 v[184:187], v0 offset:11264
s_waitcnt lgkmcnt(5)
v_mfma_f32_16x16x32_bf16 v[120:123], v[164:167], v[16:19], 0
ds_read_b128 v[164:167], v0 offset:12288
s_waitcnt lgkmcnt(5)
v_mfma_f32_16x16x32_bf16 v[124:127], v[168:171], v[16:19], 0
ds_read_b128 v[168:171], v0 offset:13312
v_mfma_f32_16x16x32_bf16 v[156:159], v[188:191], v[16:19], 0
s_waitcnt lgkmcnt(5)
v_mfma_f32_16x16x32_bf16 v[128:131], v[172:175], v[16:19], 0
ds_read_b128 v[172:175], v0 offset:14336
s_waitcnt lgkmcnt(5)
v_mfma_f32_16x16x32_bf16 v[132:135], v[176:179], v[16:19], 0
ds_read_b128 v[176:179], v0 offset:15360
s_waitcnt lgkmcnt(5)
v_mfma_f32_16x16x32_bf16 v[136:139], v[180:183], v[16:19], 0
ds_read_b128 v[180:183], v0 offset:16384
s_waitcnt lgkmcnt(5)
v_mfma_f32_16x16x32_bf16 v[140:143], v[184:187], v[16:19], 0
ds_read_b128 v[184:187], v0 offset:17408
s_waitcnt lgkmcnt(5)
v_mfma_f32_16x16x32_bf16 v[144:147], v[164:167], v[16:19], 0
ds_read_b128 v[164:167], v0 offset:18432
s_waitcnt lgkmcnt(5)
v_mfma_f32_16x16x32_bf16 v[148:151], v[168:171], v[16:19], 0
ds_read_b128 v[168:171], v0 offset:19456
s_waitcnt lgkmcnt(5)
v_mfma_f32_16x16x32_bf16 v[152:155], v[172:175], v[16:19], 0
ds_read_b128 v[172:175], v0 offset:20480
v_mfma_f32_16x16x32_bf16 v[160:163], v[16:19], v[16:19], 0
s_waitcnt vmcnt(18) lgkmcnt(5)
v_mfma_f32_16x16x32_bf16 v[96:99], v[176:179], v[20:23], v[96:99]
ds_read_b128 v[176:179], v0 offset:21504
s_waitcnt lgkmcnt(5)
v_mfma_f32_16x16x32_bf16 v[100:103], v[180:183], v[20:23], v[100:103]
ds_read_b128 v[180:183], v0 offset:22528
s_waitcnt lgkmcnt(5)
v_mfma_f32_16x16x32_bf16 v[104:107], v[184:187], v[20:23], v[104:107]
ds_read_b128 v[184:187], v0 offset:23552
s_waitcnt lgkmcnt(5)
v_mfma_f32_16x16x32_bf16 v[108:111], v[164:167], v[20:23], v[108:111]
ds_read_b128 v[164:167], v0 offset:24576
s_waitcnt lgkmcnt(5)
v_mfma_f32_16x16x32_bf16 v[112:115], v[168:171], v[20:23], v[112:115]
ds_read_b128 v[168:171], v0 offset:25600
s_waitcnt lgkmcnt(5)
v_mfma_f32_16x16x32_bf16 v[116:119], v[172:175], v[20:23], v[116:119]
ds_read_b128 v[172:175], v0 offset:26624
s_waitcnt lgkmcnt(5)
v_mfma_f32_16x16x32_bf16 v[120:123], v[176:179], v[20:23], v[120:123]
ds_read_b128 v[176:179], v0 offset:27648
s_waitcnt lgkmcnt(5)
v_mfma_f32_16x16x32_bf16 v[124:127], v[180:183], v[20:23], v[124:127]
ds_read_b128 v[180:183], v0 offset:28672
v_mfma_f32_16x16x32_bf16 v[156:159], v[188:191], v[20:23], v[156:159]
s_waitcnt lgkmcnt(5)
v_mfma_f32_16x16x32_bf16 v[128:131], v[184:187], v[20:23], v[128:131]
ds_read_b128 v[184:187], v0 offset:29696
s_waitcnt lgkmcnt(5)
v_mfma_f32_16x16x32_bf16 v[132:135], v[164:167], v[20:23], v[132:135]
ds_read_b128 v[164:167], v0 offset:30720
s_waitcnt lgkmcnt(5)
v_mfma_f32_16x16x32_bf16 v[136:139], v[168:171], v[20:23], v[136:139]
ds_read_b128 v[168:171], v0 offset:31744
s_waitcnt lgkmcnt(5)
v_mfma_f32_16x16x32_bf16 v[140:143], v[172:175], v[20:23], v[140:143]
ds_read_b128 v[172:175], v0 offset:32768
s_waitcnt lgkmcnt(5)
v_mfma_f32_16x16x32_bf16 v[144:147], v[176:179], v[20:23], v[144:147]
ds_read_b128 v[176:179], v0 offset:33792
s_waitcnt lgkmcnt(5)
v_mfma_f32_16x16x32_bf16 v[148:151], v[180:183], v[20:23], v[148:151]
ds_read_b128 v[180:183], v0 offset:34816
s_waitcnt lgkmcnt(5)
v_mfma_f32_16x16x32_bf16 v[152:155], v[184:187], v[20:23], v[152:155]
ds_read_b128 v[184:187], v0 offset:35840
v_mfma_f32_16x16x32_bf16 v[160:163], v[20:23], v[20:23], v[160:163]
s_waitcnt vmcnt(17) lgkmcnt(5)
v_mfma_f32_16x16x32_bf16 v[96:99], v[164:167], v[24:27], v[96:99]
s_barrier
ds_read_b128 v[164:167], v0 offset:36864
s_waitcnt lgkmcnt(5)
v_mfma_f32_16x16x32_bf16 v[100:103], v[168:171], v[24:27], v[100:103]
ds_read_b128 v[168:171], v0 offset:37888
s_add_u32 m0, s61, 0
s_add_u32 s51, s50, 153600
buffer_load_dwordx4 v0, s[40:43], s51 offen offset:0 lds
s_waitcnt lgkmcnt(5)
v_mfma_f32_16x16x32_bf16 v[104:107], v[172:175], v[24:27], v[104:107]
ds_read_b128 v[172:175], v0 offset:38912
s_waitcnt lgkmcnt(5)
v_mfma_f32_16x16x32_bf16 v[108:111], v[176:179], v[24:27], v[108:111]
ds_read_b128 v[176:179], v0 offset:39936
s_waitcnt lgkmcnt(5)
v_mfma_f32_16x16x32_bf16 v[112:115], v[180:183], v[24:27], v[112:115]
ds_read_b128 v[180:183], v0 offset:40960
buffer_load_dwordx4 v0, s[40:43], s51 offen offset:1024 lds
s_waitcnt lgkmcnt(5)
v_mfma_f32_16x16x32_bf16 v[116:119], v[184:187], v[24:27], v[116:119]
ds_read_b128 v[184:187], v0 offset:41984
s_waitcnt lgkmcnt(5)
v_mfma_f32_16x16x32_bf16 v[120:123], v[164:167], v[24:27], v[120:123]
ds_read_b128 v[164:167], v0 offset:43008
s_waitcnt lgkmcnt(5)
v_mfma_f32_16x16x32_bf16 v[124:127], v[168:171], v[24:27], v[124:127]
ds_read_b128 v[168:171], v0 offset:44032
buffer_load_dwordx4 v0, s[40:43], s51 offen offset:2048 lds
v_mfma_f32_16x16x32_bf16 v[156:159], v[188:191], v[24:27], v[156:159]
s_waitcnt lgkmcnt(5)
v_mfma_f32_16x16x32_bf16 v[128:131], v[172:175], v[24:27], v[128:131]
ds_read_b128 v[172:175], v0 offset:45056
s_waitcnt lgkmcnt(5)
v_mfma_f32_16x16x32_bf16 v[132:135], v[176:179], v[24:27], v[132:135]
ds_read_b128 v[176:179], v0 offset:46080
s_waitcnt lgkmcnt(5)
v_mfma_f32_16x16x32_bf16 v[136:139], v[180:183], v[24:27], v[136:139]
ds_read_b128 v[180:183], v0 offset:47104
buffer_load_dwordx4 v0, s[40:43], s51 offen offset:3072 lds
s_waitcnt lgkmcnt(5)
v_mfma_f32_16x16x32_bf16 v[140:143], v[184:187], v[24:27], v[140:143]
ds_read_b128 v[184:187], v0 offset:48128
s_waitcnt lgkmcnt(5)
v_mfma_f32_16x16x32_bf16 v[144:147], v[164:167], v[24:27], v[144:147]
ds_read_b128 v[164:167], v0 offset:49152
s_waitcnt lgkmcnt(5)
v_mfma_f32_16x16x32_bf16 v[148:151], v[168:171], v[24:27], v[148:151]
ds_read_b128 v[168:171], v0 offset:50176
s_add_u32 m0, s61, 4096
s_add_u32 s51, s50, 157696
buffer_load_dwordx4 v0, s[40:43], s51 offen offset:0 lds
s_waitcnt lgkmcnt(5)
v_mfma_f32_16x16x32_bf16 v[152:155], v[172:175], v[24:27], v[152:155]
ds_read_b128 v[172:175], v0 offset:51200
v_mfma_f32_16x16x32_bf16 v[160:163], v[24:27], v[24:27], v[160:163]
s_waitcnt vmcnt(21) lgkmcnt(5)
v_mfma_f32_16x16x32_bf16 v[96:99], v[176:179], v[28:31], v[96:99]
ds_read_b128 v[176:179], v0 offset:52224
s_waitcnt lgkmcnt(5)
v_mfma_f32_16x16x32_bf16 v[100:103], v[180:183], v[28:31], v[100:103]
ds_read_b128 v[180:183], v0 offset:53248
buffer_load_dwordx4 v0, s[40:43], s51 offen offset:1024 lds
s_waitcnt lgkmcnt(5)
v_mfma_f32_16x16x32_bf16 v[104:107], v[184:187], v[28:31], v[104:107]
ds_read_b128 v[184:187], v0 offset:54272
s_waitcnt lgkmcnt(5)
v_mfma_f32_16x16x32_bf16 v[108:111], v[164:167], v[28:31], v[108:111]
ds_read_b128 v[164:167], v0 offset:55296
s_waitcnt lgkmcnt(5)
v_mfma_f32_16x16x32_bf16 v[112:115], v[168:171], v[28:31], v[112:115]
ds_read_b128 v[168:171], v0 offset:56320
buffer_load_dwordx4 v0, s[40:43], s51 offen offset:2048 lds
s_waitcnt lgkmcnt(5)
v_mfma_f32_16x16x32_bf16 v[116:119], v[172:175], v[28:31], v[116:119]
ds_read_b128 v[172:175], v0 offset:57344
)"
R"(s_waitcnt lgkmcnt(5)
v_mfma_f32_16x16x32_bf16 v[120:123], v[176:179], v[28:31], v[120:123]
ds_read_b128 v[176:179], v0 offset:58368
s_waitcnt lgkmcnt(5)
v_mfma_f32_16x16x32_bf16 v[124:127], v[180:183], v[28:31], v[124:127]
ds_read_b128 v[180:183], v0 offset:59392
buffer_load_dwordx4 v0, s[40:43], s51 offen offset:3072 lds
v_mfma_f32_16x16x32_bf16 v[156:159], v[188:191], v[28:31], v[156:159]
s_waitcnt lgkmcnt(5)
v_mfma_f32_16x16x32_bf16 v[128:131], v[184:187], v[28:31], v[128:131]
ds_read_b128 v[184:187], v0 offset:60416
s_waitcnt lgkmcnt(5)
v_mfma_f32_16x16x32_bf16 v[132:135], v[164:167], v[28:31], v[132:135]
ds_read_b128 v[164:167], v6 offset:0
s_waitcnt lgkmcnt(5)
v_mfma_f32_16x16x32_bf16 v[136:139], v[168:171], v[28:31], v[136:139]
ds_read_b128 v[168:171], v6 offset:1024
s_waitcnt lgkmcnt(5)
v_mfma_f32_16x16x32_bf16 v[140:143], v[172:175], v[28:31], v[140:143]
ds_read_b128 v[172:175], v6 offset:2048
s_waitcnt lgkmcnt(5)
v_mfma_f32_16x16x32_bf16 v[144:147], v[176:179], v[28:31], v[144:147]
ds_read_b128 v[176:179], v6 offset:3072
s_waitcnt lgkmcnt(5)
v_mfma_f32_16x16x32_bf16 v[148:151], v[180:183], v[28:31], v[148:151]
ds_read_b128 v[180:183], v6 offset:4096
s_waitcnt lgkmcnt(5)
v_mfma_f32_16x16x32_bf16 v[152:155], v[184:187], v[28:31], v[152:155]
ds_read_b128 v[184:187], v6 offset:5120
v_mfma_f32_16x16x32_bf16 v[160:163], v[28:31], v[28:31], v[160:163]
s_waitcnt vmcnt(23) lgkmcnt(5)
v_mfma_f32_16x16x32_bf16 v[96:99], v[164:167], v[32:35], v[96:99]
s_barrier
ds_read_b128 v[164:167], v6 offset:6144
s_waitcnt lgkmcnt(5)
v_mfma_f32_16x16x32_bf16 v[100:103], v[168:171], v[32:35], v[100:103]
ds_read_b128 v[168:171], v6 offset:7168
s_add_u32 m0, s61, 30720
s_add_u32 s51, s50, 184320
buffer_load_dwordx4 v0, s[40:43], s51 offen offset:0 lds
s_waitcnt lgkmcnt(5)
v_mfma_f32_16x16x32_bf16 v[104:107], v[172:175], v[32:35], v[104:107]
ds_read_b128 v[172:175], v6 offset:8192
s_waitcnt lgkmcnt(5)
v_mfma_f32_16x16x32_bf16 v[108:111], v[176:179], v[32:35], v[108:111]
ds_read_b128 v[176:179], v6 offset:9216
s_waitcnt lgkmcnt(5)
v_mfma_f32_16x16x32_bf16 v[112:115], v[180:183], v[32:35], v[112:115]
ds_read_b128 v[180:183], v6 offset:10240
buffer_load_dwordx4 v0, s[40:43], s51 offen offset:1024 lds
s_waitcnt lgkmcnt(5)
v_mfma_f32_16x16x32_bf16 v[116:119], v[184:187], v[32:35], v[116:119]
ds_read_b128 v[184:187], v6 offset:11264
s_waitcnt lgkmcnt(5)
v_mfma_f32_16x16x32_bf16 v[120:123], v[164:167], v[32:35], v[120:123]
ds_read_b128 v[164:167], v6 offset:12288
s_waitcnt lgkmcnt(5)
v_mfma_f32_16x16x32_bf16 v[124:127], v[168:171], v[32:35], v[124:127]
ds_read_b128 v[168:171], v6 offset:13312
buffer_load_dwordx4 v0, s[40:43], s51 offen offset:2048 lds
v_mfma_f32_16x16x32_bf16 v[156:159], v[188:191], v[32:35], v[156:159]
s_waitcnt lgkmcnt(5)
v_mfma_f32_16x16x32_bf16 v[128:131], v[172:175], v[32:35], v[128:131]
ds_read_b128 v[172:175], v6 offset:14336
s_waitcnt lgkmcnt(5)
v_mfma_f32_16x16x32_bf16 v[132:135], v[176:179], v[32:35], v[132:135]
ds_read_b128 v[176:179], v6 offset:15360
s_waitcnt lgkmcnt(5)
v_mfma_f32_16x16x32_bf16 v[136:139], v[180:183], v[32:35], v[136:139]
ds_read_b128 v[180:183], v6 offset:16384
buffer_load_dwordx4 v0, s[40:43], s51 offen offset:3072 lds
s_waitcnt lgkmcnt(5)
v_mfma_f32_16x16x32_bf16 v[140:143], v[184:187], v[32:35], v[140:143]
ds_read_b128 v[184:187], v6 offset:17408
s_waitcnt lgkmcnt(5)
v_mfma_f32_16x16x32_bf16 v[144:147], v[164:167], v[32:35], v[144:147]
ds_read_b128 v[164:167], v6 offset:18432
s_waitcnt lgkmcnt(5)
v_mfma_f32_16x16x32_bf16 v[148:151], v[168:171], v[32:35], v[148:151]
ds_read_b128 v[168:171], v6 offset:19456
s_add_u32 m0, s61, 34816
s_add_u32 s51, s50, 188416
buffer_load_dwordx4 v0, s[40:43], s51 offen offset:0 lds
s_waitcnt lgkmcnt(5)
v_mfma_f32_16x16x32_bf16 v[152:155], v[172:175], v[32:35], v[152:155]
ds_read_b128 v[172:175], v6 offset:20480
v_mfma_f32_16x16x32_bf16 v[160:163], v[32:35], v[32:35], v[160:163]
s_waitcnt vmcnt(27) lgkmcnt(5)
v_mfma_f32_16x16x32_bf16 v[96:99], v[176:179], v[36:39], v[96:99]
ds_read_b128 v[176:179], v6 offset:21504
s_waitcnt lgkmcnt(5)
v_mfma_f32_16x16x32_bf16 v[100:103], v[180:183], v[36:39], v[100:103]
ds_read_b128 v[180:183], v6 offset:22528
buffer_load_dwordx4 v0, s[40:43], s51 offen offset:1024 lds
s_waitcnt lgkmcnt(5)
v_mfma_f32_16x16x32_bf16 v[104:107], v[184:187], v[36:39], v[104:107]
ds_read_b128 v[184:187], v6 offset:23552
s_waitcnt lgkmcnt(5)
v_mfma_f32_16x16x32_bf16 v[108:111], v[164:167], v[36:39], v[108:111]
ds_read_b128 v[164:167], v6 offset:24576
s_waitcnt lgkmcnt(5)
v_mfma_f32_16x16x32_bf16 v[112:115], v[168:171], v[36:39], v[112:115]
ds_read_b128 v[168:171], v6 offset:25600
buffer_load_dwordx4 v0, s[40:43], s51 offen offset:2048 lds
s_waitcnt lgkmcnt(5)
v_mfma_f32_16x16x32_bf16 v[116:119], v[172:175], v[36:39], v[116:119]
ds_read_b128 v[172:175], v6 offset:26624
s_waitcnt lgkmcnt(5)
v_mfma_f32_16x16x32_bf16 v[120:123], v[176:179], v[36:39], v[120:123]
ds_read_b128 v[176:179], v6 offset:27648
s_waitcnt lgkmcnt(5)
v_mfma_f32_16x16x32_bf16 v[124:127], v[180:183], v[36:39], v[124:127]
ds_read_b128 v[180:183], v6 offset:28672
buffer_load_dwordx4 v0, s[40:43], s51 offen offset:3072 lds
v_mfma_f32_16x16x32_bf16 v[156:159], v[188:191], v[36:39], v[156:159]
s_waitcnt lgkmcnt(5)
v_mfma_f32_16x16x32_bf16 v[128:131], v[184:187], v[36:39], v[128:131]
ds_read_b128 v[184:187], v6 offset:29696
s_waitcnt lgkmcnt(5)
v_mfma_f32_16x16x32_bf16 v[132:135], v[164:167], v[36:39], v[132:135]
ds_read_b128 v[164:167], v6 offset:30720
s_waitcnt lgkmcnt(5)
v_mfma_f32_16x16x32_bf16 v[136:139], v[168:171], v[36:39], v[136:139]
ds_read_b128 v[168:171], v6 offset:31744
s_waitcnt lgkmcnt(5)
v_mfma_f32_16x16x32_bf16 v[140:143], v[172:175], v[36:39], v[140:143]
ds_read_b128 v[172:175], v6 offset:32768
s_waitcnt lgkmcnt(5)
v_mfma_f32_16x16x32_bf16 v[144:147], v[176:179], v[36:39], v[144:147]
ds_read_b128 v[176:179], v6 offset:33792
s_waitcnt lgkmcnt(5)
v_mfma_f32_16x16x32_bf16 v[148:151], v[180:183], v[36:39], v[148:151]
ds_read_b128 v[180:183], v6 offset:34816
s_waitcnt lgkmcnt(5)
v_mfma_f32_16x16x32_bf16 v[152:155], v[184:187], v[36:39], v[152:155]
ds_read_b128 v[184:187], v6 offset:35840
v_mfma_f32_16x16x32_bf16 v[160:163], v[36:39], v[36:39], v[160:163]
s_waitcnt vmcnt(29) lgkmcnt(5)
v_mfma_f32_16x16x32_bf16 v[96:99], v[164:167], v[40:43], v[96:99]
s_barrier
ds_read_b128 v[164:167], v6 offset:36864
s_waitcnt lgkmcnt(5)
v_mfma_f32_16x16x32_bf16 v[100:103], v[168:171], v[40:43], v[100:103]
ds_read_b128 v[168:171], v6 offset:37888
s_add_u32 m0, s61, 61440
s_add_u32 s51, s50, 215040
buffer_load_dwordx4 v0, s[40:43], s51 offen offset:0 lds
s_waitcnt lgkmcnt(5)
v_mfma_f32_16x16x32_bf16 v[104:107], v[172:175], v[40:43], v[104:107]
ds_read_b128 v[172:175], v6 offset:38912
s_waitcnt lgkmcnt(5)
v_mfma_f32_16x16x32_bf16 v[108:111], v[176:179], v[40:43], v[108:111]
ds_read_b128 v[176:179], v6 offset:39936
s_waitcnt lgkmcnt(5)
v_mfma_f32_16x16x32_bf16 v[112:115], v[180:183], v[40:43], v[112:115]
ds_read_b128 v[180:183], v6 offset:40960
buffer_load_dwordx4 v0, s[40:43], s51 offen offset:1024 lds
s_waitcnt lgkmcnt(5)
v_mfma_f32_16x16x32_bf16 v[116:119], v[184:187], v[40:43], v[116:119]
ds_read_b128 v[184:187], v6 offset:41984
s_waitcnt lgkmcnt(5)
v_mfma_f32_16x16x32_bf16 v[120:123], v[164:167], v[40:43], v[120:123]
ds_read_b128 v[164:167], v6 offset:43008
s_waitcnt lgkmcnt(5)
v_mfma_f32_16x16x32_bf16 v[124:127], v[168:171], v[40:43], v[124:127]
ds_read_b128 v[168:171], v6 offset:44032
buffer_load_dwordx4 v0, s[40:43], s51 offen offset:2048 lds
v_mfma_f32_16x16x32_bf16 v[156:159], v[188:191], v[40:43], v[156:159]
s_waitcnt lgkmcnt(5)
v_mfma_f32_16x16x32_bf16 v[128:131], v[172:175], v[40:43], v[128:131]
ds_read_b128 v[172:175], v6 offset:45056
s_waitcnt lgkmcnt(5)
v_mfma_f32_16x16x32_bf16 v[132:135], v[176:179], v[40:43], v[132:135]
ds_read_b128 v[176:179], v6 offset:46080
s_waitcnt lgkmcnt(5)
v_mfma_f32_16x16x32_bf16 v[136:139], v[180:183], v[40:43], v[136:139]
ds_read_b128 v[180:183], v6 offset:47104
buffer_load_dwordx4 v0, s[40:43], s51 offen offset:3072 lds
s_waitcnt lgkmcnt(5)
v_mfma_f32_16x16x32_bf16 v[140:143], v[184:187], v[40:43], v[140:143]
ds_read_b128 v[184:187], v6 offset:48128
s_waitcnt lgkmcnt(5)
v_mfma_f32_16x16x32_bf16 v[144:147], v[164:167], v[40:43], v[144:147]
ds_read_b128 v[164:167], v6 offset:49152
s_waitcnt lgkmcnt(5)
v_mfma_f32_16x16x32_bf16 v[148:151], v[168:171], v[40:43], v[148:151]
ds_read_b128 v[168:171], v6 offset:50176
s_add_u32 m0, s61, 65536
s_add_u32 s51, s50, 219136
buffer_load_dwordx4 v0, s[40:43], s51 offen offset:0 lds
s_waitcnt lgkmcnt(5)
v_mfma_f32_16x16x32_bf16 v[152:155], v[172:175], v[40:43], v[152:155]
ds_read_b128 v[172:175], v6 offset:51200
v_mfma_f32_16x16x32_bf16 v[160:163], v[40:43], v[40:43], v[160:163]
s_waitcnt vmcnt(33) lgkmcnt(5)
v_mfma_f32_16x16x32_bf16 v[96:99], v[176:179], v[44:47], v[96:99]
ds_read_b128 v[176:179], v6 offset:52224
s_waitcnt lgkmcnt(5)
v_mfma_f32_16x16x32_bf16 v[100:103], v[180:183], v[44:47], v[100:103]
ds_read_b128 v[180:183], v6 offset:53248
)"
R"(buffer_load_dwordx4 v0, s[40:43], s51 offen offset:1024 lds
s_waitcnt lgkmcnt(5)
v_mfma_f32_16x16x32_bf16 v[104:107], v[184:187], v[44:47], v[104:107]
ds_read_b128 v[184:187], v6 offset:54272
s_waitcnt lgkmcnt(5)
v_mfma_f32_16x16x32_bf16 v[108:111], v[164:167], v[44:47], v[108:111]
ds_read_b128 v[164:167], v6 offset:55296
s_waitcnt lgkmcnt(5)
v_mfma_f32_16x16x32_bf16 v[112:115], v[168:171], v[44:47], v[112:115]
ds_read_b128 v[168:171], v6 offset:56320
buffer_load_dwordx4 v0, s[40:43], s51 offen offset:2048 lds
s_waitcnt lgkmcnt(5)
v_mfma_f32_16x16x32_bf16 v[116:119], v[172:175], v[44:47], v[116:119]
ds_read_b128 v[172:175], v6 offset:57344
s_waitcnt lgkmcnt(5)
v_mfma_f32_16x16x32_bf16 v[120:123], v[176:179], v[44:47], v[120:123]
ds_read_b128 v[176:179], v6 offset:58368
s_waitcnt lgkmcnt(5)
v_mfma_f32_16x16x32_bf16 v[124:127], v[180:183], v[44:47], v[124:127]
ds_read_b128 v[180:183], v6 offset:59392
buffer_load_dwordx4 v0, s[40:43], s51 offen offset:3072 lds
v_mfma_f32_16x16x32_bf16 v[156:159], v[188:191], v[44:47], v[156:159]
s_waitcnt lgkmcnt(5)
v_mfma_f32_16x16x32_bf16 v[128:131], v[184:187], v[44:47], v[128:131]
ds_read_b128 v[184:187], v6 offset:60416
s_waitcnt lgkmcnt(5)
v_mfma_f32_16x16x32_bf16 v[132:135], v[164:167], v[44:47], v[132:135]
ds_read_b128 v[164:167], v7 offset:0
s_waitcnt lgkmcnt(5)
v_mfma_f32_16x16x32_bf16 v[136:139], v[168:171], v[44:47], v[136:139]
ds_read_b128 v[168:171], v7 offset:1024
s_waitcnt lgkmcnt(5)
v_mfma_f32_16x16x32_bf16 v[140:143], v[172:175], v[44:47], v[140:143]
ds_read_b128 v[172:175], v7 offset:2048
s_waitcnt lgkmcnt(5)
v_mfma_f32_16x16x32_bf16 v[144:147], v[176:179], v[44:47], v[144:147]
ds_read_b128 v[176:179], v7 offset:3072
s_waitcnt lgkmcnt(5)
v_mfma_f32_16x16x32_bf16 v[148:151], v[180:183], v[44:47], v[148:151]
ds_read_b128 v[180:183], v7 offset:4096
s_waitcnt lgkmcnt(5)
v_mfma_f32_16x16x32_bf16 v[152:155], v[184:187], v[44:47], v[152:155]
ds_read_b128 v[184:187], v7 offset:5120
v_mfma_f32_16x16x32_bf16 v[160:163], v[44:47], v[44:47], v[160:163]
s_waitcnt vmcnt(35) lgkmcnt(5)
v_mfma_f32_16x16x32_bf16 v[96:99], v[164:167], v[48:51], v[96:99]
s_barrier
ds_read_b128 v[164:167], v7 offset:6144
s_waitcnt lgkmcnt(5)
v_mfma_f32_16x16x32_bf16 v[100:103], v[168:171], v[48:51], v[100:103]
ds_read_b128 v[168:171], v7 offset:7168
s_add_u32 m0, s61, 92160
s_add_u32 s51, s50, 245760
buffer_load_dwordx4 v0, s[40:43], s51 offen offset:0 lds
s_waitcnt lgkmcnt(5)
v_mfma_f32_16x16x32_bf16 v[104:107], v[172:175], v[48:51], v[104:107]
ds_read_b128 v[172:175], v7 offset:8192
s_waitcnt lgkmcnt(5)
v_mfma_f32_16x16x32_bf16 v[108:111], v[176:179], v[48:51], v[108:111]
ds_read_b128 v[176:179], v7 offset:9216
s_waitcnt lgkmcnt(5)
v_mfma_f32_16x16x32_bf16 v[112:115], v[180:183], v[48:51], v[112:115]
ds_read_b128 v[180:183], v7 offset:10240
buffer_load_dwordx4 v0, s[40:43], s51 offen offset:1024 lds
s_waitcnt lgkmcnt(5)
v_mfma_f32_16x16x32_bf16 v[116:119], v[184:187], v[48:51], v[116:119]
ds_read_b128 v[184:187], v7 offset:11264
s_waitcnt lgkmcnt(5)
v_mfma_f32_16x16x32_bf16 v[120:123], v[164:167], v[48:51], v[120:123]
ds_read_b128 v[164:167], v7 offset:12288
s_waitcnt lgkmcnt(5)
v_mfma_f32_16x16x32_bf16 v[124:127], v[168:171], v[48:51], v[124:127]
ds_read_b128 v[168:171], v7 offset:13312
buffer_load_dwordx4 v0, s[40:43], s51 offen offset:2048 lds
v_mfma_f32_16x16x32_bf16 v[156:159], v[188:191], v[48:51], v[156:159]
s_waitcnt lgkmcnt(5)
v_mfma_f32_16x16x32_bf16 v[128:131], v[172:175], v[48:51], v[128:131]
ds_read_b128 v[172:175], v7 offset:14336
s_waitcnt lgkmcnt(5)
v_mfma_f32_16x16x32_bf16 v[132:135], v[176:179], v[48:51], v[132:135]
ds_read_b128 v[176:179], v7 offset:15360
s_waitcnt lgkmcnt(5)
v_mfma_f32_16x16x32_bf16 v[136:139], v[180:183], v[48:51], v[136:139]
ds_read_b128 v[180:183], v7 offset:16384
buffer_load_dwordx4 v0, s[40:43], s51 offen offset:3072 lds
s_waitcnt lgkmcnt(5)
v_mfma_f32_16x16x32_bf16 v[140:143], v[184:187], v[48:51], v[140:143]
ds_read_b128 v[184:187], v7 offset:17408
s_waitcnt lgkmcnt(5)
v_mfma_f32_16x16x32_bf16 v[144:147], v[164:167], v[48:51], v[144:147]
ds_read_b128 v[164:167], v7 offset:18432
s_waitcnt lgkmcnt(5)
v_mfma_f32_16x16x32_bf16 v[148:151], v[168:171], v[48:51], v[148:151]
ds_read_b128 v[168:171], v7 offset:19456
s_add_u32 m0, s61, 96256
s_add_u32 s51, s50, 249856
buffer_load_dwordx4 v0, s[40:43], s51 offen offset:0 lds
s_waitcnt lgkmcnt(5)
v_mfma_f32_16x16x32_bf16 v[152:155], v[172:175], v[48:51], v[152:155]
ds_read_b128 v[172:175], v7 offset:20480
v_mfma_f32_16x16x32_bf16 v[160:163], v[48:51], v[48:51], v[160:163]
s_waitcnt vmcnt(39) lgkmcnt(5)
v_mfma_f32_16x16x32_bf16 v[96:99], v[176:179], v[52:55], v[96:99]
ds_read_b128 v[176:179], v7 offset:21504
s_waitcnt lgkmcnt(5)
v_mfma_f32_16x16x32_bf16 v[100:103], v[180:183], v[52:55], v[100:103]
ds_read_b128 v[180:183], v7 offset:22528
buffer_load_dwordx4 v0, s[40:43], s51 offen offset:1024 lds
s_waitcnt lgkmcnt(5)
v_mfma_f32_16x16x32_bf16 v[104:107], v[184:187], v[52:55], v[104:107]
ds_read_b128 v[184:187], v7 offset:23552
s_waitcnt lgkmcnt(5)
v_mfma_f32_16x16x32_bf16 v[108:111], v[164:167], v[52:55], v[108:111]
ds_read_b128 v[164:167], v7 offset:24576
s_waitcnt lgkmcnt(5)
v_mfma_f32_16x16x32_bf16 v[112:115], v[168:171], v[52:55], v[112:115]
ds_read_b128 v[168:171], v7 offset:25600
buffer_load_dwordx4 v0, s[40:43], s51 offen offset:2048 lds
s_waitcnt lgkmcnt(5)
v_mfma_f32_16x16x32_bf16 v[116:119], v[172:175], v[52:55], v[116:119]
ds_read_b128 v[172:175], v7 offset:26624
s_waitcnt lgkmcnt(5)
v_mfma_f32_16x16x32_bf16 v[120:123], v[176:179], v[52:55], v[120:123]
ds_read_b128 v[176:179], v7 offset:27648
s_waitcnt lgkmcnt(5)
v_mfma_f32_16x16x32_bf16 v[124:127], v[180:183], v[52:55], v[124:127]
ds_read_b128 v[180:183], v7 offset:28672
buffer_load_dwordx4 v0, s[40:43], s51 offen offset:3072 lds
v_mfma_f32_16x16x32_bf16 v[156:159], v[188:191], v[52:55], v[156:159]
s_waitcnt lgkmcnt(5)
v_mfma_f32_16x16x32_bf16 v[128:131], v[184:187], v[52:55], v[128:131]
ds_read_b128 v[184:187], v7 offset:29696
s_waitcnt lgkmcnt(5)
v_mfma_f32_16x16x32_bf16 v[132:135], v[164:167], v[52:55], v[132:135]
s_waitcnt vmcnt(16)
s_barrier
ds_read_b128 v[164:167], v0 offset:0
s_waitcnt lgkmcnt(5)
v_mfma_f32_16x16x32_bf16 v[136:139], v[168:171], v[52:55], v[136:139]
ds_read_b128 v[168:171], v0 offset:1024
s_waitcnt lgkmcnt(5)
v_mfma_f32_16x16x32_bf16 v[140:143], v[172:175], v[52:55], v[140:143]
ds_read_b128 v[172:175], v0 offset:2048
s_waitcnt lgkmcnt(5)
v_mfma_f32_16x16x32_bf16 v[144:147], v[176:179], v[52:55], v[144:147]
ds_read_b128 v[176:179], v0 offset:3072
s_waitcnt lgkmcnt(5)
v_mfma_f32_16x16x32_bf16 v[148:151], v[180:183], v[52:55], v[148:151]
ds_read_b128 v[180:183], v0 offset:4096
s_waitcnt lgkmcnt(5)
v_mfma_f32_16x16x32_bf16 v[152:155], v[184:187], v[52:55], v[152:155]
ds_read_b128 v[184:187], v0 offset:5120
v_mfma_f32_16x16x32_bf16 v[160:163], v[52:55], v[52:55], v[160:163]
s_waitcnt lgkmcnt(5)
v_mfma_f32_16x16x32_bf16 v[96:99], v[164:167], v[56:59], v[96:99]
s_barrier
ds_read_b128 v[164:167], v0 offset:6144
s_waitcnt lgkmcnt(5)
v_mfma_f32_16x16x32_bf16 v[100:103], v[168:171], v[56:59], v[100:103]
ds_read_b128 v[168:171], v0 offset:7168
s_add_u32 m0, s61, 122880
s_add_u32 s51, s50, 276480
buffer_load_dwordx4 v0, s[40:43], s51 offen offset:0 lds
s_waitcnt lgkmcnt(5)
v_mfma_f32_16x16x32_bf16 v[104:107], v[172:175], v[56:59], v[104:107]
ds_read_b128 v[172:175], v0 offset:8192
s_waitcnt lgkmcnt(5)
v_mfma_f32_16x16x32_bf16 v[108:111], v[176:179], v[56:59], v[108:111]
ds_read_b128 v[176:179], v0 offset:9216
s_waitcnt lgkmcnt(5)
v_mfma_f32_16x16x32_bf16 v[112:115], v[180:183], v[56:59], v[112:115]
ds_read_b128 v[180:183], v0 offset:10240
buffer_load_dwordx4 v0, s[40:43], s51 offen offset:1024 lds
s_waitcnt lgkmcnt(5)
v_mfma_f32_16x16x32_bf16 v[116:119], v[184:187], v[56:59], v[116:119]
ds_read_b128 v[184:187], v0 offset:11264
s_waitcnt lgkmcnt(5)
v_mfma_f32_16x16x32_bf16 v[120:123], v[164:167], v[56:59], v[120:123]
ds_read_b128 v[164:167], v0 offset:12288
s_waitcnt lgkmcnt(5)
v_mfma_f32_16x16x32_bf16 v[124:127], v[168:171], v[56:59], v[124:127]
ds_read_b128 v[168:171], v0 offset:13312
buffer_load_dwordx4 v0, s[40:43], s51 offen offset:2048 lds
v_mfma_f32_16x16x32_bf16 v[156:159], v[188:191], v[56:59], v[156:159]
s_waitcnt lgkmcnt(5)
v_mfma_f32_16x16x32_bf16 v[128:131], v[172:175], v[56:59], v[128:131]
ds_read_b128 v[172:175], v0 offset:14336
s_waitcnt lgkmcnt(5)
v_mfma_f32_16x16x32_bf16 v[132:135], v[176:179], v[56:59], v[132:135]
ds_read_b128 v[176:179], v0 offset:15360
s_waitcnt lgkmcnt(5)
v_mfma_f32_16x16x32_bf16 v[136:139], v[180:183], v[56:59], v[136:139]
ds_read_b128 v[180:183], v0 offset:16384
buffer_load_dwordx4 v0, s[40:43], s51 offen offset:3072 lds
s_waitcnt lgkmcnt(5)
v_mfma_f32_16x16x32_bf16 v[140:143], v[184:187], v[56:59], v[140:143]
ds_read_b128 v[184:187], v0 offset:17408
s_waitcnt lgkmcnt(5)
v_mfma_f32_16x16x32_bf16 v[144:147], v[164:167], v[56:59], v[144:147]
ds_read_b128 v[164:167], v0 offset:18432
)"
R"(s_waitcnt lgkmcnt(5)
v_mfma_f32_16x16x32_bf16 v[148:151], v[168:171], v[56:59], v[148:151]
ds_read_b128 v[168:171], v0 offset:19456
s_add_u32 m0, s61, 126976
s_add_u32 s51, s50, 280576
buffer_load_dwordx4 v0, s[40:43], s51 offen offset:0 lds
s_waitcnt lgkmcnt(5)
v_mfma_f32_16x16x32_bf16 v[152:155], v[172:175], v[56:59], v[152:155]
ds_read_b128 v[172:175], v0 offset:20480
v_mfma_f32_16x16x32_bf16 v[160:163], v[56:59], v[56:59], v[160:163]
s_waitcnt lgkmcnt(5)
v_mfma_f32_16x16x32_bf16 v[96:99], v[176:179], v[60:63], v[96:99]
ds_read_b128 v[176:179], v0 offset:21504
s_waitcnt lgkmcnt(5)
v_mfma_f32_16x16x32_bf16 v[100:103], v[180:183], v[60:63], v[100:103]
ds_read_b128 v[180:183], v0 offset:22528
buffer_load_dwordx4 v0, s[40:43], s51 offen offset:1024 lds
s_waitcnt lgkmcnt(5)
v_mfma_f32_16x16x32_bf16 v[104:107], v[184:187], v[60:63], v[104:107]
ds_read_b128 v[184:187], v0 offset:23552
s_waitcnt lgkmcnt(5)
v_mfma_f32_16x16x32_bf16 v[108:111], v[164:167], v[60:63], v[108:111]
ds_read_b128 v[164:167], v0 offset:24576
s_waitcnt lgkmcnt(5)
v_mfma_f32_16x16x32_bf16 v[112:115], v[168:171], v[60:63], v[112:115]
ds_read_b128 v[168:171], v0 offset:25600
buffer_load_dwordx4 v0, s[40:43], s51 offen offset:2048 lds
s_waitcnt lgkmcnt(5)
v_mfma_f32_16x16x32_bf16 v[116:119], v[172:175], v[60:63], v[116:119]
ds_read_b128 v[172:175], v0 offset:26624
s_waitcnt lgkmcnt(5)
v_mfma_f32_16x16x32_bf16 v[120:123], v[176:179], v[60:63], v[120:123]
ds_read_b128 v[176:179], v0 offset:27648
s_waitcnt lgkmcnt(5)
v_mfma_f32_16x16x32_bf16 v[124:127], v[180:183], v[60:63], v[124:127]
ds_read_b128 v[180:183], v0 offset:28672
buffer_load_dwordx4 v0, s[40:43], s51 offen offset:3072 lds
v_mfma_f32_16x16x32_bf16 v[156:159], v[188:191], v[60:63], v[156:159]
s_waitcnt lgkmcnt(5)
v_mfma_f32_16x16x32_bf16 v[128:131], v[184:187], v[60:63], v[128:131]
ds_read_b128 v[184:187], v0 offset:29696
s_waitcnt lgkmcnt(5)
v_mfma_f32_16x16x32_bf16 v[132:135], v[164:167], v[60:63], v[132:135]
s_waitcnt vmcnt(16)
s_barrier
ds_read_b128 v[164:167], v0 offset:30720
s_waitcnt lgkmcnt(5)
v_mfma_f32_16x16x32_bf16 v[136:139], v[168:171], v[60:63], v[136:139]
ds_read_b128 v[168:171], v0 offset:31744
s_waitcnt lgkmcnt(5)
v_mfma_f32_16x16x32_bf16 v[140:143], v[172:175], v[60:63], v[140:143]
ds_read_b128 v[172:175], v0 offset:32768
s_waitcnt lgkmcnt(5)
v_mfma_f32_16x16x32_bf16 v[144:147], v[176:179], v[60:63], v[144:147]
ds_read_b128 v[176:179], v0 offset:33792
s_waitcnt lgkmcnt(5)
v_mfma_f32_16x16x32_bf16 v[148:151], v[180:183], v[60:63], v[148:151]
ds_read_b128 v[180:183], v0 offset:34816
s_waitcnt lgkmcnt(5)
v_mfma_f32_16x16x32_bf16 v[152:155], v[184:187], v[60:63], v[152:155]
ds_read_b128 v[184:187], v0 offset:35840
v_mfma_f32_16x16x32_bf16 v[160:163], v[60:63], v[60:63], v[160:163]
s_waitcnt lgkmcnt(5)
v_mfma_f32_16x16x32_bf16 v[96:99], v[164:167], v[64:67], v[96:99]
ds_read_b128 v[164:167], v0 offset:36864
s_waitcnt lgkmcnt(5)
v_mfma_f32_16x16x32_bf16 v[100:103], v[168:171], v[64:67], v[100:103]
ds_read_b128 v[168:171], v0 offset:37888
s_waitcnt lgkmcnt(5)
v_mfma_f32_16x16x32_bf16 v[104:107], v[172:175], v[64:67], v[104:107]
ds_read_b128 v[172:175], v0 offset:38912
s_waitcnt lgkmcnt(5)
v_mfma_f32_16x16x32_bf16 v[108:111], v[176:179], v[64:67], v[108:111]
ds_read_b128 v[176:179], v0 offset:39936
s_waitcnt lgkmcnt(5)
v_mfma_f32_16x16x32_bf16 v[112:115], v[180:183], v[64:67], v[112:115]
ds_read_b128 v[180:183], v0 offset:40960
s_waitcnt lgkmcnt(5)
v_mfma_f32_16x16x32_bf16 v[116:119], v[184:187], v[64:67], v[116:119]
ds_read_b128 v[184:187], v0 offset:41984
s_waitcnt lgkmcnt(5)
v_mfma_f32_16x16x32_bf16 v[120:123], v[164:167], v[64:67], v[120:123]
ds_read_b128 v[164:167], v0 offset:43008
s_waitcnt lgkmcnt(5)
v_mfma_f32_16x16x32_bf16 v[124:127], v[168:171], v[64:67], v[124:127]
ds_read_b128 v[168:171], v0 offset:44032
v_mfma_f32_16x16x32_bf16 v[156:159], v[188:191], v[64:67], v[156:159]
s_waitcnt lgkmcnt(5)
v_mfma_f32_16x16x32_bf16 v[128:131], v[172:175], v[64:67], v[128:131]
ds_read_b128 v[172:175], v0 offset:45056
s_waitcnt lgkmcnt(5)
v_mfma_f32_16x16x32_bf16 v[132:135], v[176:179], v[64:67], v[132:135]
ds_read_b128 v[176:179], v0 offset:46080
s_waitcnt lgkmcnt(5)
v_mfma_f32_16x16x32_bf16 v[136:139], v[180:183], v[64:67], v[136:139]
ds_read_b128 v[180:183], v0 offset:47104
s_waitcnt lgkmcnt(5)
v_mfma_f32_16x16x32_bf16 v[140:143], v[184:187], v[64:67], v[140:143]
ds_read_b128 v[184:187], v0 offset:48128
s_waitcnt lgkmcnt(5)
v_mfma_f32_16x16x32_bf16 v[144:147], v[164:167], v[64:67], v[144:147]
ds_read_b128 v[164:167], v0 offset:49152
s_waitcnt lgkmcnt(5)
v_mfma_f32_16x16x32_bf16 v[148:151], v[168:171], v[64:67], v[148:151]
ds_read_b128 v[168:171], v0 offset:50176
s_waitcnt lgkmcnt(5)
v_mfma_f32_16x16x32_bf16 v[152:155], v[172:175], v[64:67], v[152:155]
ds_read_b128 v[172:175], v0 offset:51200
v_mfma_f32_16x16x32_bf16 v[160:163], v[64:67], v[64:67], v[160:163]
s_waitcnt lgkmcnt(5)
v_mfma_f32_16x16x32_bf16 v[96:99], v[176:179], v[68:71], v[96:99]
ds_read_b128 v[176:179], v0 offset:52224
s_waitcnt lgkmcnt(5)
v_mfma_f32_16x16x32_bf16 v[100:103], v[180:183], v[68:71], v[100:103]
ds_read_b128 v[180:183], v0 offset:53248
s_waitcnt lgkmcnt(5)
v_mfma_f32_16x16x32_bf16 v[104:107], v[184:187], v[68:71], v[104:107]
ds_read_b128 v[184:187], v0 offset:54272
s_waitcnt lgkmcnt(5)
v_mfma_f32_16x16x32_bf16 v[108:111], v[164:167], v[68:71], v[108:111]
ds_read_b128 v[164:167], v0 offset:55296
s_waitcnt lgkmcnt(5)
v_mfma_f32_16x16x32_bf16 v[112:115], v[168:171], v[68:71], v[112:115]
ds_read_b128 v[168:171], v0 offset:56320
s_waitcnt lgkmcnt(5)
v_mfma_f32_16x16x32_bf16 v[116:119], v[172:175], v[68:71], v[116:119]
ds_read_b128 v[172:175], v0 offset:57344
s_waitcnt lgkmcnt(5)
v_mfma_f32_16x16x32_bf16 v[120:123], v[176:179], v[68:71], v[120:123]
ds_read_b128 v[176:179], v0 offset:58368
s_waitcnt lgkmcnt(5)
v_mfma_f32_16x16x32_bf16 v[124:127], v[180:183], v[68:71], v[124:127]
ds_read_b128 v[180:183], v0 offset:59392
v_mfma_f32_16x16x32_bf16 v[156:159], v[188:191], v[68:71], v[156:159]
s_waitcnt lgkmcnt(5)
v_mfma_f32_16x16x32_bf16 v[128:131], v[184:187], v[68:71], v[128:131]
ds_read_b128 v[184:187], v0 offset:60416
s_waitcnt lgkmcnt(5)
v_mfma_f32_16x16x32_bf16 v[132:135], v[164:167], v[68:71], v[132:135]
s_waitcnt vmcnt(16)
s_barrier
ds_read_b128 v[164:167], v6 offset:0
s_waitcnt lgkmcnt(5)
v_mfma_f32_16x16x32_bf16 v[136:139], v[168:171], v[68:71], v[136:139]
ds_read_b128 v[168:171], v6 offset:1024
s_waitcnt lgkmcnt(5)
v_mfma_f32_16x16x32_bf16 v[140:143], v[172:175], v[68:71], v[140:143]
ds_read_b128 v[172:175], v6 offset:2048
s_waitcnt lgkmcnt(5)
v_mfma_f32_16x16x32_bf16 v[144:147], v[176:179], v[68:71], v[144:147]
ds_read_b128 v[176:179], v6 offset:3072
s_waitcnt lgkmcnt(5)
v_mfma_f32_16x16x32_bf16 v[148:151], v[180:183], v[68:71], v[148:151]
ds_read_b128 v[180:183], v6 offset:4096
s_waitcnt lgkmcnt(5)
v_mfma_f32_16x16x32_bf16 v[152:155], v[184:187], v[68:71], v[152:155]
ds_read_b128 v[184:187], v6 offset:5120
v_mfma_f32_16x16x32_bf16 v[160:163], v[68:71], v[68:71], v[160:163]
s_waitcnt lgkmcnt(5)
v_mfma_f32_16x16x32_bf16 v[96:99], v[164:167], v[72:75], v[96:99]
ds_read_b128 v[164:167], v6 offset:6144
s_waitcnt lgkmcnt(5)
v_mfma_f32_16x16x32_bf16 v[100:103], v[168:171], v[72:75], v[100:103]
ds_read_b128 v[168:171], v6 offset:7168
s_waitcnt lgkmcnt(5)
v_mfma_f32_16x16x32_bf16 v[104:107], v[172:175], v[72:75], v[104:107]
ds_read_b128 v[172:175], v6 offset:8192
s_waitcnt lgkmcnt(5)
v_mfma_f32_16x16x32_bf16 v[108:111], v[176:179], v[72:75], v[108:111]
ds_read_b128 v[176:179], v6 offset:9216
s_waitcnt lgkmcnt(5)
v_mfma_f32_16x16x32_bf16 v[112:115], v[180:183], v[72:75], v[112:115]
ds_read_b128 v[180:183], v6 offset:10240
s_waitcnt lgkmcnt(5)
v_mfma_f32_16x16x32_bf16 v[116:119], v[184:187], v[72:75], v[116:119]
ds_read_b128 v[184:187], v6 offset:11264
s_waitcnt lgkmcnt(5)
v_mfma_f32_16x16x32_bf16 v[120:123], v[164:167], v[72:75], v[120:123]
ds_read_b128 v[164:167], v6 offset:12288
s_waitcnt lgkmcnt(5)
v_mfma_f32_16x16x32_bf16 v[124:127], v[168:171], v[72:75], v[124:127]
ds_read_b128 v[168:171], v6 offset:13312
v_mfma_f32_16x16x32_bf16 v[156:159], v[188:191], v[72:75], v[156:159]
s_waitcnt lgkmcnt(5)
v_mfma_f32_16x16x32_bf16 v[128:131], v[172:175], v[72:75], v[128:131]
ds_read_b128 v[172:175], v6 offset:14336
s_waitcnt lgkmcnt(5)
v_mfma_f32_16x16x32_bf16 v[132:135], v[176:179], v[72:75], v[132:135]
ds_read_b128 v[176:179], v6 offset:15360
s_waitcnt lgkmcnt(5)
v_mfma_f32_16x16x32_bf16 v[136:139], v[180:183], v[72:75], v[136:139]
ds_read_b128 v[180:183], v6 offset:16384
s_waitcnt lgkmcnt(5)
v_mfma_f32_16x16x32_bf16 v[140:143], v[184:187], v[72:75], v[140:143]
ds_read_b128 v[184:187], v6 offset:17408
s_waitcnt lgkmcnt(5)
v_mfma_f32_16x16x32_bf16 v[144:147], v[164:167], v[72:75], v[144:147]
ds_read_b128 v[164:167], v6 offset:18432
s_waitcnt lgkmcnt(5)
v_mfma_f32_16x16x32_bf16 v[148:151], v[168:171], v[72:75], v[148:151]
)"
R"(ds_read_b128 v[168:171], v6 offset:19456
s_waitcnt lgkmcnt(5)
v_mfma_f32_16x16x32_bf16 v[152:155], v[172:175], v[72:75], v[152:155]
ds_read_b128 v[172:175], v6 offset:20480
v_mfma_f32_16x16x32_bf16 v[160:163], v[72:75], v[72:75], v[160:163]
s_waitcnt lgkmcnt(5)
v_mfma_f32_16x16x32_bf16 v[96:99], v[176:179], v[76:79], v[96:99]
ds_read_b128 v[176:179], v6 offset:21504
s_waitcnt lgkmcnt(5)
v_mfma_f32_16x16x32_bf16 v[100:103], v[180:183], v[76:79], v[100:103]
ds_read_b128 v[180:183], v6 offset:22528
s_waitcnt lgkmcnt(5)
v_mfma_f32_16x16x32_bf16 v[104:107], v[184:187], v[76:79], v[104:107]
ds_read_b128 v[184:187], v6 offset:23552
s_waitcnt lgkmcnt(5)
v_mfma_f32_16x16x32_bf16 v[108:111], v[164:167], v[76:79], v[108:111]
ds_read_b128 v[164:167], v6 offset:24576
s_waitcnt lgkmcnt(5)
v_mfma_f32_16x16x32_bf16 v[112:115], v[168:171], v[76:79], v[112:115]
ds_read_b128 v[168:171], v6 offset:25600
s_waitcnt lgkmcnt(5)
v_mfma_f32_16x16x32_bf16 v[116:119], v[172:175], v[76:79], v[116:119]
ds_read_b128 v[172:175], v6 offset:26624
s_waitcnt lgkmcnt(5)
v_mfma_f32_16x16x32_bf16 v[120:123], v[176:179], v[76:79], v[120:123]
ds_read_b128 v[176:179], v6 offset:27648
s_waitcnt lgkmcnt(5)
v_mfma_f32_16x16x32_bf16 v[124:127], v[180:183], v[76:79], v[124:127]
ds_read_b128 v[180:183], v6 offset:28672
v_mfma_f32_16x16x32_bf16 v[156:159], v[188:191], v[76:79], v[156:159]
s_waitcnt lgkmcnt(5)
v_mfma_f32_16x16x32_bf16 v[128:131], v[184:187], v[76:79], v[128:131]
ds_read_b128 v[184:187], v6 offset:29696
s_waitcnt lgkmcnt(5)
v_mfma_f32_16x16x32_bf16 v[132:135], v[164:167], v[76:79], v[132:135]
s_waitcnt vmcnt(8)
s_barrier
ds_read_b128 v[164:167], v6 offset:30720
s_waitcnt lgkmcnt(5)
v_mfma_f32_16x16x32_bf16 v[136:139], v[168:171], v[76:79], v[136:139]
ds_read_b128 v[168:171], v6 offset:31744
s_waitcnt lgkmcnt(5)
v_mfma_f32_16x16x32_bf16 v[140:143], v[172:175], v[76:79], v[140:143]
ds_read_b128 v[172:175], v6 offset:32768
s_waitcnt lgkmcnt(5)
v_mfma_f32_16x16x32_bf16 v[144:147], v[176:179], v[76:79], v[144:147]
ds_read_b128 v[176:179], v6 offset:33792
s_waitcnt lgkmcnt(5)
v_mfma_f32_16x16x32_bf16 v[148:151], v[180:183], v[76:79], v[148:151]
ds_read_b128 v[180:183], v6 offset:34816
s_waitcnt lgkmcnt(5)
v_mfma_f32_16x16x32_bf16 v[152:155], v[184:187], v[76:79], v[152:155]
ds_read_b128 v[184:187], v6 offset:35840
v_mfma_f32_16x16x32_bf16 v[160:163], v[76:79], v[76:79], v[160:163]
s_waitcnt lgkmcnt(5)
v_mfma_f32_16x16x32_bf16 v[96:99], v[164:167], v[80:83], v[96:99]
ds_read_b128 v[164:167], v6 offset:36864
v_and_b32 v4, 48, v15
s_mul_i32 s52, s26, 960
s_add_u32 s52, s52, 148668416
s_waitcnt lgkmcnt(5)
v_mfma_f32_16x16x32_bf16 v[100:103], v[168:171], v[80:83], v[100:103]
ds_read_b128 v[168:171], v6 offset:37888
buffer_load_dwordx4 v[16:19], v4, s[40:43], s52 offen sc1
s_waitcnt lgkmcnt(5)
v_mfma_f32_16x16x32_bf16 v[104:107], v[172:175], v[80:83], v[104:107]
ds_read_b128 v[172:175], v6 offset:38912
buffer_load_dwordx4 v[20:23], v4, s[40:43], s52 offen offset:64 sc1
s_waitcnt lgkmcnt(5)
v_mfma_f32_16x16x32_bf16 v[108:111], v[176:179], v[80:83], v[108:111]
ds_read_b128 v[176:179], v6 offset:39936
buffer_load_dwordx4 v[24:27], v4, s[40:43], s52 offen offset:128 sc1
s_waitcnt lgkmcnt(5)
v_mfma_f32_16x16x32_bf16 v[112:115], v[180:183], v[80:83], v[112:115]
ds_read_b128 v[180:183], v6 offset:40960
buffer_load_dwordx4 v[28:31], v4, s[40:43], s52 offen offset:192 sc1
s_waitcnt lgkmcnt(5)
v_mfma_f32_16x16x32_bf16 v[116:119], v[184:187], v[80:83], v[116:119]
ds_read_b128 v[184:187], v6 offset:41984
buffer_load_dwordx4 v[32:35], v4, s[40:43], s52 offen offset:256 sc1
s_waitcnt lgkmcnt(5)
v_mfma_f32_16x16x32_bf16 v[120:123], v[164:167], v[80:83], v[120:123]
ds_read_b128 v[164:167], v6 offset:43008
buffer_load_dwordx4 v[36:39], v4, s[40:43], s52 offen offset:320 sc1
s_waitcnt lgkmcnt(5)
v_mfma_f32_16x16x32_bf16 v[124:127], v[168:171], v[80:83], v[124:127]
ds_read_b128 v[168:171], v6 offset:44032
v_mfma_f32_16x16x32_bf16 v[156:159], v[188:191], v[80:83], v[156:159]
buffer_load_dwordx4 v[40:43], v4, s[40:43], s52 offen offset:384 sc1
s_waitcnt lgkmcnt(5)
v_mfma_f32_16x16x32_bf16 v[128:131], v[172:175], v[80:83], v[128:131]
ds_read_b128 v[172:175], v6 offset:45056
buffer_load_dwordx4 v[44:47], v4, s[40:43], s52 offen offset:448 sc1
s_waitcnt lgkmcnt(5)
v_mfma_f32_16x16x32_bf16 v[132:135], v[176:179], v[80:83], v[132:135]
ds_read_b128 v[176:179], v6 offset:46080
buffer_load_dwordx4 v[48:51], v4, s[40:43], s52 offen offset:512 sc1
s_waitcnt lgkmcnt(5)
v_mfma_f32_16x16x32_bf16 v[136:139], v[180:183], v[80:83], v[136:139]
ds_read_b128 v[180:183], v6 offset:47104
buffer_load_dwordx4 v[52:55], v4, s[40:43], s52 offen offset:576 sc1
s_waitcnt lgkmcnt(5)
v_mfma_f32_16x16x32_bf16 v[140:143], v[184:187], v[80:83], v[140:143]
ds_read_b128 v[184:187], v6 offset:48128
buffer_load_dwordx4 v[56:59], v4, s[40:43], s52 offen offset:640 sc1
s_waitcnt lgkmcnt(5)
v_mfma_f32_16x16x32_bf16 v[144:147], v[164:167], v[80:83], v[144:147]
ds_read_b128 v[164:167], v6 offset:49152
buffer_load_dwordx4 v[60:63], v4, s[40:43], s52 offen offset:704 sc1
s_waitcnt lgkmcnt(5)
v_mfma_f32_16x16x32_bf16 v[148:151], v[168:171], v[80:83], v[148:151]
ds_read_b128 v[168:171], v6 offset:50176
buffer_load_dwordx4 v[64:67], v4, s[40:43], s52 offen offset:768 sc1
s_waitcnt lgkmcnt(5)
v_mfma_f32_16x16x32_bf16 v[152:155], v[172:175], v[80:83], v[152:155]
ds_read_b128 v[172:175], v6 offset:51200
v_mfma_f32_16x16x32_bf16 v[160:163], v[80:83], v[80:83], v[160:163]
buffer_load_dwordx4 v[68:71], v4, s[40:43], s52 offen offset:832 sc1
s_waitcnt lgkmcnt(5)
v_mfma_f32_16x16x32_bf16 v[96:99], v[176:179], v[84:87], v[96:99]
ds_read_b128 v[176:179], v6 offset:52224
buffer_load_dwordx4 v[72:75], v4, s[40:43], s52 offen offset:896 sc1
s_waitcnt lgkmcnt(5)
v_mfma_f32_16x16x32_bf16 v[100:103], v[180:183], v[84:87], v[100:103]
ds_read_b128 v[180:183], v6 offset:53248
s_waitcnt lgkmcnt(5)
v_mfma_f32_16x16x32_bf16 v[104:107], v[184:187], v[84:87], v[104:107]
ds_read_b128 v[184:187], v6 offset:54272
s_waitcnt lgkmcnt(5)
v_mfma_f32_16x16x32_bf16 v[108:111], v[164:167], v[84:87], v[108:111]
ds_read_b128 v[164:167], v6 offset:55296
s_waitcnt lgkmcnt(5)
v_mfma_f32_16x16x32_bf16 v[112:115], v[168:171], v[84:87], v[112:115]
ds_read_b128 v[168:171], v6 offset:56320
s_waitcnt lgkmcnt(5)
v_mfma_f32_16x16x32_bf16 v[116:119], v[172:175], v[84:87], v[116:119]
ds_read_b128 v[172:175], v6 offset:57344
s_waitcnt lgkmcnt(5)
v_mfma_f32_16x16x32_bf16 v[120:123], v[176:179], v[84:87], v[120:123]
ds_read_b128 v[176:179], v6 offset:58368
s_waitcnt lgkmcnt(5)
v_mfma_f32_16x16x32_bf16 v[124:127], v[180:183], v[84:87], v[124:127]
ds_read_b128 v[180:183], v6 offset:59392
v_mfma_f32_16x16x32_bf16 v[156:159], v[188:191], v[84:87], v[156:159]
s_waitcnt lgkmcnt(5)
v_mfma_f32_16x16x32_bf16 v[128:131], v[184:187], v[84:87], v[128:131]
ds_read_b128 v[184:187], v6 offset:60416
s_waitcnt lgkmcnt(5)
v_mfma_f32_16x16x32_bf16 v[132:135], v[164:167], v[84:87], v[132:135]
s_waitcnt vmcnt(0)
s_barrier
ds_read_b128 v[164:167], v7 offset:0
s_waitcnt lgkmcnt(5)
v_mfma_f32_16x16x32_bf16 v[136:139], v[168:171], v[84:87], v[136:139]
ds_read_b128 v[168:171], v7 offset:1024
s_waitcnt lgkmcnt(5)
v_mfma_f32_16x16x32_bf16 v[140:143], v[172:175], v[84:87], v[140:143]
ds_read_b128 v[172:175], v7 offset:2048
s_waitcnt lgkmcnt(5)
v_mfma_f32_16x16x32_bf16 v[144:147], v[176:179], v[84:87], v[144:147]
ds_read_b128 v[176:179], v7 offset:3072
s_waitcnt lgkmcnt(5)
v_mfma_f32_16x16x32_bf16 v[148:151], v[180:183], v[84:87], v[148:151]
ds_read_b128 v[180:183], v7 offset:4096
s_waitcnt lgkmcnt(5)
v_mfma_f32_16x16x32_bf16 v[152:155], v[184:187], v[84:87], v[152:155]
ds_read_b128 v[184:187], v7 offset:5120
v_mfma_f32_16x16x32_bf16 v[160:163], v[84:87], v[84:87], v[160:163]
s_waitcnt lgkmcnt(5)
v_mfma_f32_16x16x32_bf16 v[96:99], v[164:167], v[88:91], v[96:99]
ds_read_b128 v[164:167], v7 offset:6144
s_waitcnt lgkmcnt(5)
v_mfma_f32_16x16x32_bf16 v[100:103], v[168:171], v[88:91], v[100:103]
ds_read_b128 v[168:171], v7 offset:7168
s_waitcnt lgkmcnt(5)
v_mfma_f32_16x16x32_bf16 v[104:107], v[172:175], v[88:91], v[104:107]
ds_read_b128 v[172:175], v7 offset:8192
s_waitcnt lgkmcnt(5)
v_mfma_f32_16x16x32_bf16 v[108:111], v[176:179], v[88:91], v[108:111]
ds_read_b128 v[176:179], v7 offset:9216
s_waitcnt lgkmcnt(5)
v_mfma_f32_16x16x32_bf16 v[112:115], v[180:183], v[88:91], v[112:115]
ds_read_b128 v[180:183], v7 offset:10240
s_waitcnt lgkmcnt(5)
v_mfma_f32_16x16x32_bf16 v[116:119], v[184:187], v[88:91], v[116:119]
ds_read_b128 v[184:187], v7 offset:11264
s_waitcnt lgkmcnt(5)
v_mfma_f32_16x16x32_bf16 v[120:123], v[164:167], v[88:91], v[120:123]
ds_read_b128 v[164:167], v7 offset:12288
s_waitcnt lgkmcnt(5)
v_mfma_f32_16x16x32_bf16 v[124:127], v[168:171], v[88:91], v[124:127]
ds_read_b128 v[168:171], v7 offset:13312
v_mfma_f32_16x16x32_bf16 v[156:159], v[188:191], v[88:91], v[156:159]
s_waitcnt lgkmcnt(5)
v_mfma_f32_16x16x32_bf16 v[128:131], v[172:175], v[88:91], v[128:131]
ds_read_b128 v[172:175], v7 offset:14336
s_waitcnt lgkmcnt(5)
v_mfma_f32_16x16x32_bf16 v[132:135], v[176:179], v[88:91], v[132:135]
ds_read_b128 v[176:179], v7 offset:15360
s_waitcnt lgkmcnt(5)
)"
R"(v_mfma_f32_16x16x32_bf16 v[136:139], v[180:183], v[88:91], v[136:139]
ds_read_b128 v[180:183], v7 offset:16384
s_waitcnt lgkmcnt(5)
v_mfma_f32_16x16x32_bf16 v[140:143], v[184:187], v[88:91], v[140:143]
ds_read_b128 v[184:187], v7 offset:17408
s_waitcnt lgkmcnt(5)
v_mfma_f32_16x16x32_bf16 v[144:147], v[164:167], v[88:91], v[144:147]
ds_read_b128 v[164:167], v7 offset:18432
s_waitcnt lgkmcnt(5)
v_mfma_f32_16x16x32_bf16 v[148:151], v[168:171], v[88:91], v[148:151]
ds_read_b128 v[168:171], v7 offset:19456
s_waitcnt lgkmcnt(5)
v_mfma_f32_16x16x32_bf16 v[152:155], v[172:175], v[88:91], v[152:155]
ds_read_b128 v[172:175], v7 offset:20480
v_mfma_f32_16x16x32_bf16 v[160:163], v[88:91], v[88:91], v[160:163]
s_waitcnt lgkmcnt(5)
v_mfma_f32_16x16x32_bf16 v[96:99], v[176:179], v[92:95], v[96:99]
ds_read_b128 v[176:179], v7 offset:21504
s_waitcnt lgkmcnt(5)
v_mfma_f32_16x16x32_bf16 v[100:103], v[180:183], v[92:95], v[100:103]
ds_read_b128 v[180:183], v7 offset:22528
s_waitcnt lgkmcnt(5)
v_mfma_f32_16x16x32_bf16 v[104:107], v[184:187], v[92:95], v[104:107]
ds_read_b128 v[184:187], v7 offset:23552
s_waitcnt lgkmcnt(5)
v_mfma_f32_16x16x32_bf16 v[108:111], v[164:167], v[92:95], v[108:111]
ds_read_b128 v[164:167], v7 offset:24576
s_waitcnt lgkmcnt(5)
v_mfma_f32_16x16x32_bf16 v[112:115], v[168:171], v[92:95], v[112:115]
ds_read_b128 v[168:171], v7 offset:25600
s_waitcnt lgkmcnt(5)
v_mfma_f32_16x16x32_bf16 v[116:119], v[172:175], v[92:95], v[116:119]
ds_read_b128 v[172:175], v7 offset:26624
s_waitcnt lgkmcnt(5)
v_mfma_f32_16x16x32_bf16 v[120:123], v[176:179], v[92:95], v[120:123]
ds_read_b128 v[176:179], v7 offset:27648
s_waitcnt lgkmcnt(5)
v_mfma_f32_16x16x32_bf16 v[124:127], v[180:183], v[92:95], v[124:127]
ds_read_b128 v[180:183], v7 offset:28672
v_mfma_f32_16x16x32_bf16 v[156:159], v[188:191], v[92:95], v[156:159]
s_waitcnt lgkmcnt(5)
v_mfma_f32_16x16x32_bf16 v[128:131], v[184:187], v[92:95], v[128:131]
ds_read_b128 v[184:187], v7 offset:29696
s_waitcnt lgkmcnt(5)
v_mfma_f32_16x16x32_bf16 v[132:135], v[164:167], v[92:95], v[132:135]
s_waitcnt lgkmcnt(4)
v_mfma_f32_16x16x32_bf16 v[136:139], v[168:171], v[92:95], v[136:139]
s_waitcnt lgkmcnt(3)
v_mfma_f32_16x16x32_bf16 v[140:143], v[172:175], v[92:95], v[140:143]
s_waitcnt lgkmcnt(2)
v_mfma_f32_16x16x32_bf16 v[144:147], v[176:179], v[92:95], v[144:147]
s_waitcnt lgkmcnt(1)
v_mfma_f32_16x16x32_bf16 v[148:151], v[180:183], v[92:95], v[148:151]
s_waitcnt lgkmcnt(0)
v_mfma_f32_16x16x32_bf16 v[152:155], v[184:187], v[92:95], v[152:155]
v_mfma_f32_16x16x32_bf16 v[160:163], v[92:95], v[92:95], v[160:163]
v_and_b32 v4, 1, v15
v_cmp_eq_u32 vcc, 1, v4
s_nop 5
v_cndmask_b32 v160, v160, v161, vcc
v_cndmask_b32 v162, v162, v163, vcc
v_and_b32 v4, 2, v15
v_cmp_eq_u32 vcc, 2, v4
s_nop 1
v_cndmask_b32 v160, v160, v162, vcc
v_bfe_u32 v4, v15, 2, 2
v_lshrrev_b32 v5, 4, v15
v_cmp_eq_u32 vcc, v4, v5
s_nop 1
v_cndmask_b32 v160, 0, v160, vcc
v_mov_b32 v161, v160
s_nop 1
v_permlane16_swap_b32 v160, v161
v_add_f32 v160, v160, v161
v_mov_b32 v161, v160
s_nop 1
v_permlane32_swap_b32 v160, v161
v_add_f32 v160, v160, v161
v_mul_f32 v8, 0x3acccccd, v156
v_mul_f32 v160, 0x3acccccd, v160
v_fma_f32 v160, -v8, v8, v160
v_max_f32 v160, 0, v160
v_add_f32 v160, 0x3727c5ac, v160
v_rsq_f32 v10, v160
v_sub_f32 v8, 0, v8
v_pk_fma_f32 v[96:97], v[16:17], v[8:9], v[96:97] op_sel_hi:[1,0,1]
v_pk_mul_f32 v[96:97], v[96:97], v[10:11] op_sel_hi:[1,0]
v_pk_fma_f32 v[98:99], v[18:19], v[8:9], v[98:99] op_sel_hi:[1,0,1]
v_pk_mul_f32 v[98:99], v[98:99], v[10:11] op_sel_hi:[1,0]
v_pk_fma_f32 v[100:101], v[20:21], v[8:9], v[100:101] op_sel_hi:[1,0,1]
v_pk_mul_f32 v[100:101], v[100:101], v[10:11] op_sel_hi:[1,0]
v_pk_fma_f32 v[102:103], v[22:23], v[8:9], v[102:103] op_sel_hi:[1,0,1]
v_pk_mul_f32 v[102:103], v[102:103], v[10:11] op_sel_hi:[1,0]
v_pk_fma_f32 v[104:105], v[24:25], v[8:9], v[104:105] op_sel_hi:[1,0,1]
v_pk_mul_f32 v[104:105], v[104:105], v[10:11] op_sel_hi:[1,0]
v_pk_fma_f32 v[106:107], v[26:27], v[8:9], v[106:107] op_sel_hi:[1,0,1]
v_pk_mul_f32 v[106:107], v[106:107], v[10:11] op_sel_hi:[1,0]
v_pk_fma_f32 v[108:109], v[28:29], v[8:9], v[108:109] op_sel_hi:[1,0,1]
v_pk_mul_f32 v[108:109], v[108:109], v[10:11] op_sel_hi:[1,0]
v_pk_fma_f32 v[110:111], v[30:31], v[8:9], v[110:111] op_sel_hi:[1,0,1]
v_pk_mul_f32 v[110:111], v[110:111], v[10:11] op_sel_hi:[1,0]
v_pk_fma_f32 v[112:113], v[32:33], v[8:9], v[112:113] op_sel_hi:[1,0,1]
v_pk_mul_f32 v[112:113], v[112:113], v[10:11] op_sel_hi:[1,0]
v_pk_fma_f32 v[114:115], v[34:35], v[8:9], v[114:115] op_sel_hi:[1,0,1]
v_pk_mul_f32 v[114:115], v[114:115], v[10:11] op_sel_hi:[1,0]
v_pk_fma_f32 v[116:117], v[36:37], v[8:9], v[116:117] op_sel_hi:[1,0,1]
v_pk_mul_f32 v[116:117], v[116:117], v[10:11] op_sel_hi:[1,0]
v_pk_fma_f32 v[118:119], v[38:39], v[8:9], v[118:119] op_sel_hi:[1,0,1]
v_pk_mul_f32 v[118:119], v[118:119], v[10:11] op_sel_hi:[1,0]
v_pk_fma_f32 v[120:121], v[40:41], v[8:9], v[120:121] op_sel_hi:[1,0,1]
v_pk_mul_f32 v[120:121], v[120:121], v[10:11] op_sel_hi:[1,0]
v_pk_fma_f32 v[122:123], v[42:43], v[8:9], v[122:123] op_sel_hi:[1,0,1]
v_pk_mul_f32 v[122:123], v[122:123], v[10:11] op_sel_hi:[1,0]
v_pk_fma_f32 v[124:125], v[44:45], v[8:9], v[124:125] op_sel_hi:[1,0,1]
v_pk_mul_f32 v[124:125], v[124:125], v[10:11] op_sel_hi:[1,0]
v_pk_fma_f32 v[126:127], v[46:47], v[8:9], v[126:127] op_sel_hi:[1,0,1]
v_pk_mul_f32 v[126:127], v[126:127], v[10:11] op_sel_hi:[1,0]
v_pk_fma_f32 v[128:129], v[48:49], v[8:9], v[128:129] op_sel_hi:[1,0,1]
v_pk_mul_f32 v[128:129], v[128:129], v[10:11] op_sel_hi:[1,0]
v_pk_fma_f32 v[130:131], v[50:51], v[8:9], v[130:131] op_sel_hi:[1,0,1]
v_pk_mul_f32 v[130:131], v[130:131], v[10:11] op_sel_hi:[1,0]
v_pk_fma_f32 v[132:133], v[52:53], v[8:9], v[132:133] op_sel_hi:[1,0,1]
v_pk_mul_f32 v[132:133], v[132:133], v[10:11] op_sel_hi:[1,0]
v_pk_fma_f32 v[134:135], v[54:55], v[8:9], v[134:135] op_sel_hi:[1,0,1]
v_pk_mul_f32 v[134:135], v[134:135], v[10:11] op_sel_hi:[1,0]
v_pk_fma_f32 v[136:137], v[56:57], v[8:9], v[136:137] op_sel_hi:[1,0,1]
v_pk_mul_f32 v[136:137], v[136:137], v[10:11] op_sel_hi:[1,0]
v_pk_fma_f32 v[138:139], v[58:59], v[8:9], v[138:139] op_sel_hi:[1,0,1]
v_pk_mul_f32 v[138:139], v[138:139], v[10:11] op_sel_hi:[1,0]
v_pk_fma_f32 v[140:141], v[60:61], v[8:9], v[140:141] op_sel_hi:[1,0,1]
v_pk_mul_f32 v[140:141], v[140:141], v[10:11] op_sel_hi:[1,0]
v_pk_fma_f32 v[142:143], v[62:63], v[8:9], v[142:143] op_sel_hi:[1,0,1]
v_pk_mul_f32 v[142:143], v[142:143], v[10:11] op_sel_hi:[1,0]
v_pk_fma_f32 v[144:145], v[64:65], v[8:9], v[144:145] op_sel_hi:[1,0,1]
v_pk_mul_f32 v[144:145], v[144:145], v[10:11] op_sel_hi:[1,0]
v_pk_fma_f32 v[146:147], v[66:67], v[8:9], v[146:147] op_sel_hi:[1,0,1]
v_pk_mul_f32 v[146:147], v[146:147], v[10:11] op_sel_hi:[1,0]
v_pk_fma_f32 v[148:149], v[68:69], v[8:9], v[148:149] op_sel_hi:[1,0,1]
v_pk_mul_f32 v[148:149], v[148:149], v[10:11] op_sel_hi:[1,0]
v_pk_fma_f32 v[150:151], v[70:71], v[8:9], v[150:151] op_sel_hi:[1,0,1]
v_pk_mul_f32 v[150:151], v[150:151], v[10:11] op_sel_hi:[1,0]
v_pk_fma_f32 v[152:153], v[72:73], v[8:9], v[152:153] op_sel_hi:[1,0,1]
v_pk_mul_f32 v[152:153], v[152:153], v[10:11] op_sel_hi:[1,0]
v_pk_fma_f32 v[154:155], v[74:75], v[8:9], v[154:155] op_sel_hi:[1,0,1]
v_pk_mul_f32 v[154:155], v[154:155], v[10:11] op_sel_hi:[1,0]
v_max3_f32 v8, v116, v117, v118
v_max3_f32 v8, v8, v119, v120
v_max3_f32 v8, v8, v121, v122
v_max3_f32 v8, v8, v123, v124
v_max3_f32 v8, v8, v125, v126
v_max3_f32 v8, v8, v127, v128
v_max3_f32 v8, v8, v129, v130
v_max3_f32 v8, v8, v131, v132
v_max3_f32 v8, v8, v133, v134
v_max_f32 v8, v8, v135
v_min3_f32 v9, v116, v117, v118
v_min3_f32 v9, v9, v119, v120
v_min3_f32 v9, v9, v121, v122
v_min3_f32 v9, v9, v123, v124
v_min3_f32 v9, v9, v125, v126
v_min3_f32 v9, v9, v127, v128
v_min3_f32 v9, v9, v129, v130
v_min3_f32 v9, v9, v131, v132
v_min3_f32 v9, v9, v133, v134
v_min_f32 v9, v9, v135
v_mul_f32 v8, 0xbfb8aa3b, v8
v_mul_f32 v10, 0x3fb8aa3b, v9
v_pk_fma_f32 v[16:17], v[116:117], s[22:23], v[8:9] op_sel_hi:[1,1,0]
v_pk_fma_f32 v[36:37], v[116:117], s[24:25], v[10:11] op_sel_hi:[1,1,0]
v_pk_fma_f32 v[18:19], v[118:119], s[22:23], v[8:9] op_sel_hi:[1,1,0]
v_pk_fma_f32 v[38:39], v[118:119], s[24:25], v[10:11] op_sel_hi:[1,1,0]
v_pk_fma_f32 v[20:21], v[120:121], s[22:23], v[8:9] op_sel_hi:[1,1,0]
v_pk_fma_f32 v[40:41], v[120:121], s[24:25], v[10:11] op_sel_hi:[1,1,0]
v_pk_fma_f32 v[22:23], v[122:123], s[22:23], v[8:9] op_sel_hi:[1,1,0]
v_pk_fma_f32 v[42:43], v[122:123], s[24:25], v[10:11] op_sel_hi:[1,1,0]
v_pk_fma_f32 v[24:25], v[124:125], s[22:23], v[8:9] op_sel_hi:[1,1,0]
v_pk_fma_f32 v[44:45], v[124:125], s[24:25], v[10:11] op_sel_hi:[1,1,0]
v_pk_fma_f32 v[26:27], v[126:127], s[22:23], v[8:9] op_sel_hi:[1,1,0]
v_pk_fma_f32 v[46:47], v[126:127], s[24:25], v[10:11] op_sel_hi:[1,1,0]
v_pk_fma_f32 v[28:29], v[128:129], s[22:23], v[8:9] op_sel_hi:[1,1,0]
v_pk_fma_f32 v[48:49], v[128:129], s[24:25], v[10:11] op_sel_hi:[1,1,0]
v_pk_fma_f32 v[30:31], v[130:131], s[22:23], v[8:9] op_sel_hi:[1,1,0]
v_pk_fma_f32 v[50:51], v[130:131], s[24:25], v[10:11] op_sel_hi:[1,1,0]
v_pk_fma_f32 v[32:33], v[132:133], s[22:23], v[8:9] op_sel_hi:[1,1,0]
v_pk_fma_f32 v[52:53], v[132:133], s[24:25], v[10:11] op_sel_hi:[1,1,0]
v_pk_fma_f32 v[34:35], v[134:135], s[22:23], v[8:9] op_sel_hi:[1,1,0]
v_pk_fma_f32 v[54:55], v[134:135], s[24:25], v[10:11] op_sel_hi:[1,1,0]
v_exp_f32 v16, v16
v_exp_f32 v17, v17
v_exp_f32 v18, v18
v_exp_f32 v19, v19
v_exp_f32 v20, v20
v_exp_f32 v21, v21
v_exp_f32 v22, v22
v_exp_f32 v23, v23
v_exp_f32 v24, v24
v_exp_f32 v25, v25
v_exp_f32 v26, v26
v_exp_f32 v27, v27
v_exp_f32 v28, v28
)"
R"(v_exp_f32 v29, v29
v_exp_f32 v30, v30
v_exp_f32 v31, v31
v_exp_f32 v32, v32
v_exp_f32 v33, v33
v_exp_f32 v34, v34
v_exp_f32 v35, v35
v_exp_f32 v36, v36
v_exp_f32 v37, v37
v_exp_f32 v38, v38
v_exp_f32 v39, v39
v_exp_f32 v40, v40
v_exp_f32 v41, v41
v_exp_f32 v42, v42
v_exp_f32 v43, v43
v_exp_f32 v44, v44
v_exp_f32 v45, v45
v_exp_f32 v46, v46
v_exp_f32 v47, v47
v_exp_f32 v48, v48
v_exp_f32 v49, v49
v_exp_f32 v50, v50
v_exp_f32 v51, v51
v_exp_f32 v52, v52
v_exp_f32 v53, v53
v_exp_f32 v54, v54
v_exp_f32 v55, v55
v_pk_add_f32 v[8:9], v[16:17], v[18:19]
v_pk_add_f32 v[8:9], v[8:9], v[20:21]
v_pk_add_f32 v[8:9], v[8:9], v[22:23]
v_pk_add_f32 v[8:9], v[8:9], v[24:25]
v_pk_add_f32 v[8:9], v[8:9], v[26:27]
v_pk_add_f32 v[8:9], v[8:9], v[28:29]
v_pk_add_f32 v[8:9], v[8:9], v[30:31]
v_pk_add_f32 v[8:9], v[8:9], v[32:33]
v_pk_add_f32 v[8:9], v[8:9], v[34:35]
v_add_f32 v8, v8, v9
v_pk_add_f32 v[10:11], v[36:37], v[38:39]
v_pk_add_f32 v[10:11], v[10:11], v[40:41]
v_pk_add_f32 v[10:11], v[10:11], v[42:43]
v_pk_add_f32 v[10:11], v[10:11], v[44:45]
v_pk_add_f32 v[10:11], v[10:11], v[46:47]
v_pk_add_f32 v[10:11], v[10:11], v[48:49]
v_pk_add_f32 v[10:11], v[10:11], v[50:51]
v_pk_add_f32 v[10:11], v[10:11], v[52:53]
v_pk_add_f32 v[10:11], v[10:11], v[54:55]
v_add_f32 v10, v10, v11
v_rcp_f32 v8, v8
v_rcp_f32 v10, v10
v_pk_mul_f32 v[16:17], v[16:17], v[8:9] op_sel_hi:[1,0]
v_pk_mul_f32 v[18:19], v[18:19], v[8:9] op_sel_hi:[1,0]
v_pk_mul_f32 v[20:21], v[20:21], v[8:9] op_sel_hi:[1,0]
v_pk_mul_f32 v[22:23], v[22:23], v[8:9] op_sel_hi:[1,0]
v_pk_mul_f32 v[24:25], v[24:25], v[8:9] op_sel_hi:[1,0]
v_pk_mul_f32 v[26:27], v[26:27], v[8:9] op_sel_hi:[1,0]
v_pk_mul_f32 v[28:29], v[28:29], v[8:9] op_sel_hi:[1,0]
v_pk_mul_f32 v[30:31], v[30:31], v[8:9] op_sel_hi:[1,0]
v_pk_mul_f32 v[32:33], v[32:33], v[8:9] op_sel_hi:[1,0]
v_pk_mul_f32 v[34:35], v[34:35], v[8:9] op_sel_hi:[1,0]
v_pk_mul_f32 v[36:37], v[36:37], v[10:11] op_sel_hi:[1,0]
v_pk_mul_f32 v[38:39], v[38:39], v[10:11] op_sel_hi:[1,0]
v_pk_mul_f32 v[40:41], v[40:41], v[10:11] op_sel_hi:[1,0]
v_pk_mul_f32 v[42:43], v[42:43], v[10:11] op_sel_hi:[1,0]
v_pk_mul_f32 v[44:45], v[44:45], v[10:11] op_sel_hi:[1,0]
v_pk_mul_f32 v[46:47], v[46:47], v[10:11] op_sel_hi:[1,0]
v_pk_mul_f32 v[48:49], v[48:49], v[10:11] op_sel_hi:[1,0]
v_pk_mul_f32 v[50:51], v[50:51], v[10:11] op_sel_hi:[1,0]
v_pk_mul_f32 v[52:53], v[52:53], v[10:11] op_sel_hi:[1,0]
v_pk_mul_f32 v[54:55], v[54:55], v[10:11] op_sel_hi:[1,0]
v_cvt_pk_bf16_f32 v56, v16, v17
v_cvt_pk_bf16_f32 v57, v18, v19
v_cvt_pk_bf16_f32 v58, v20, v21
v_cvt_pk_bf16_f32 v59, v22, v23
v_cvt_pk_bf16_f32 v60, v24, v25
v_cvt_pk_bf16_f32 v61, v26, v27
v_cvt_pk_bf16_f32 v62, v28, v29
v_cvt_pk_bf16_f32 v63, v30, v31
v_cvt_pk_bf16_f32 v64, v32, v33
v_cvt_pk_bf16_f32 v65, v34, v35
v_cvt_pk_bf16_f32 v66, v36, v37
v_cvt_pk_bf16_f32 v67, v38, v39
v_cvt_pk_bf16_f32 v68, v40, v41
v_cvt_pk_bf16_f32 v69, v42, v43
v_cvt_pk_bf16_f32 v70, v44, v45
v_cvt_pk_bf16_f32 v71, v46, v47
v_cvt_pk_bf16_f32 v72, v48, v49
v_cvt_pk_bf16_f32 v73, v50, v51
v_cvt_pk_bf16_f32 v74, v52, v53
v_cvt_pk_bf16_f32 v75, v54, v55
s_lshl_b32 s48, s45, 4
v_mov_b32 v76, 0
v_mov_b32 v77, 0
v_mov_b32 v78, 0
v_mov_b32 v79, 0
v_cvt_pk_bf16_f32 v80, v136, v137
v_cvt_pk_bf16_f32 v81, v138, v139
v_cvt_pk_bf16_f32 v82, v140, v141
v_cvt_pk_bf16_f32 v83, v142, v143
v_cvt_pk_bf16_f32 v84, v144, v145
v_cvt_pk_bf16_f32 v85, v146, v147
v_cvt_pk_bf16_f32 v86, v148, v149
v_cvt_pk_bf16_f32 v87, v150, v151
v_cvt_pk_bf16_f32 v88, v152, v153
v_cvt_pk_bf16_f32 v89, v154, v155
v_mov_b32 v90, 0x3f80
v_mov_b32 v91, 0
v_and_b32 v4, 15, v15
v_lshrrev_b32 v5, 4, v15
v_lshl_add_u32 v4, v5, 6, v4
v_add_u32 v4, s48, v4
v_mul_u32_u24 v4, 176, v4
ds_write_b128 v4, v[56:59] offset:0
ds_write_b128 v4, v[60:63] offset:16
ds_write_b128 v4, v[64:67] offset:32
ds_write_b128 v4, v[68:71] offset:48
ds_write_b128 v4, v[72:75] offset:64
ds_write_b128 v4, v[76:79] offset:80
ds_write_b128 v4, v[80:83] offset:96
ds_write_b128 v4, v[84:87] offset:112
ds_write_b128 v4, v[88:91] offset:128
ds_write_b128 v4, v[76:79] offset:144
v_lshrrev_b32 v5, 2, v15
s_lshl_b32 s48, s45, 6
v_add_u32 v5, s48, v5
v_mul_u32_u24 v5, 176, v5
v_and_b32 v6, 3, v15
v_lshl_add_u32 v6, v6, 3, v5
s_waitcnt lgkmcnt(0)
s_barrier
ds_read_b64_tr_b16 v[116:117], v6 offset:0
ds_read_b64_tr_b16 v[118:119], v6 offset:2816
ds_read_b64_tr_b16 v[120:121], v6 offset:32
ds_read_b64_tr_b16 v[122:123], v6 offset:2848
ds_read_b64_tr_b16 v[124:125], v6 offset:64
ds_read_b64_tr_b16 v[126:127], v6 offset:2880
ds_read_b64_tr_b16 v[128:129], v6 offset:96
ds_read_b64_tr_b16 v[130:131], v6 offset:2912
ds_read_b64_tr_b16 v[132:133], v6 offset:128
ds_read_b64_tr_b16 v[134:135], v6 offset:2944
ds_read_b64_tr_b16 v[136:137], v6 offset:5632
ds_read_b64_tr_b16 v[138:139], v6 offset:8448
ds_read_b64_tr_b16 v[140:141], v6 offset:5664
ds_read_b64_tr_b16 v[142:143], v6 offset:8480
ds_read_b64_tr_b16 v[144:145], v6 offset:5696
ds_read_b64_tr_b16 v[146:147], v6 offset:8512
s_waitcnt lgkmcnt(14)
ds_read_b64_tr_b16 v[148:149], v6 offset:5728
ds_read_b64_tr_b16 v[150:151], v6 offset:8544
s_waitcnt lgkmcnt(14)
ds_read_b64_tr_b16 v[152:153], v6 offset:5760
ds_read_b64_tr_b16 v[154:155], v6 offset:8576
s_waitcnt lgkmcnt(14)
s_waitcnt lgkmcnt(12)
v_mfma_f32_16x16x32_bf16 v[192:195], v[116:119], v[128:131], 0
s_waitcnt lgkmcnt(10)
v_mfma_f32_16x16x32_bf16 v[196:199], v[116:119], v[132:135], 0
v_mfma_f32_16x16x32_bf16 v[200:203], v[120:123], v[128:131], 0
v_mfma_f32_16x16x32_bf16 v[204:207], v[120:123], v[132:135], 0
v_mfma_f32_16x16x32_bf16 v[208:211], v[124:127], v[128:131], 0
v_mfma_f32_16x16x32_bf16 v[212:215], v[124:127], v[132:135], 0
s_waitcnt lgkmcnt(2)
v_mfma_f32_16x16x32_bf16 v[192:195], v[136:139], v[148:151], v[192:195]
s_waitcnt lgkmcnt(0)
v_mfma_f32_16x16x32_bf16 v[196:199], v[136:139], v[152:155], v[196:199]
v_mfma_f32_16x16x32_bf16 v[200:203], v[140:143], v[148:151], v[200:203]
v_mfma_f32_16x16x32_bf16 v[204:207], v[140:143], v[152:155], v[204:207]
v_mfma_f32_16x16x32_bf16 v[208:211], v[144:147], v[148:151], v[208:211]
v_mfma_f32_16x16x32_bf16 v[212:215], v[144:147], v[152:155], v[212:215]
s_lshl_b32 s48, s27, 5
s_lshl2_add_u32 s48, s26, s48
s_add_u32 s48, s48, s45
s_mul_i32 s48, s48, 6144
s_add_u32 s48, s48, 154820608
s_add_u32 s49, s48, 4096
buffer_store_dwordx4 v[192:195], v0, s[40:43], s48 offen sc1
buffer_store_dwordx4 v[196:199], v0, s[40:43], s48 offen offset:1024 sc1
buffer_store_dwordx4 v[200:203], v0, s[40:43], s48 offen offset:2048 sc1
buffer_store_dwordx4 v[204:207], v0, s[40:43], s48 offen offset:3072 sc1
buffer_store_dwordx4 v[208:211], v0, s[40:43], s49 offen sc1
buffer_store_dwordx4 v[212:215], v0, s[40:43], s49 offen offset:1024 sc1
v_max3_f32 v8, v96, v97, v98
v_max3_f32 v8, v8, v99, v100
v_max3_f32 v8, v8, v101, v102
v_max3_f32 v8, v8, v103, v104
v_max3_f32 v8, v8, v105, v106
v_max3_f32 v8, v8, v107, v108
v_max3_f32 v8, v8, v109, v110
v_max3_f32 v8, v8, v111, v112
v_max3_f32 v8, v8, v113, v114
v_max_f32 v8, v8, v115
v_min3_f32 v9, v96, v97, v98
v_min3_f32 v9, v9, v99, v100
v_min3_f32 v9, v9, v101, v102
v_min3_f32 v9, v9, v103, v104
v_min3_f32 v9, v9, v105, v106
v_min3_f32 v9, v9, v107, v108
v_min3_f32 v9, v9, v109, v110
v_min3_f32 v9, v9, v111, v112
v_min3_f32 v9, v9, v113, v114
v_min_f32 v9, v9, v115
)"
R"(v_mul_f32 v8, 0xbfb8aa3b, v8
v_mul_f32 v10, 0x3fb8aa3b, v9
v_pk_fma_f32 v[16:17], v[96:97], s[22:23], v[8:9] op_sel_hi:[1,1,0]
v_pk_fma_f32 v[36:37], v[96:97], s[24:25], v[10:11] op_sel_hi:[1,1,0]
v_pk_fma_f32 v[18:19], v[98:99], s[22:23], v[8:9] op_sel_hi:[1,1,0]
v_pk_fma_f32 v[38:39], v[98:99], s[24:25], v[10:11] op_sel_hi:[1,1,0]
v_pk_fma_f32 v[20:21], v[100:101], s[22:23], v[8:9] op_sel_hi:[1,1,0]
v_pk_fma_f32 v[40:41], v[100:101], s[24:25], v[10:11] op_sel_hi:[1,1,0]
v_pk_fma_f32 v[22:23], v[102:103], s[22:23], v[8:9] op_sel_hi:[1,1,0]
v_pk_fma_f32 v[42:43], v[102:103], s[24:25], v[10:11] op_sel_hi:[1,1,0]
v_pk_fma_f32 v[24:25], v[104:105], s[22:23], v[8:9] op_sel_hi:[1,1,0]
v_pk_fma_f32 v[44:45], v[104:105], s[24:25], v[10:11] op_sel_hi:[1,1,0]
v_pk_fma_f32 v[26:27], v[106:107], s[22:23], v[8:9] op_sel_hi:[1,1,0]
v_pk_fma_f32 v[46:47], v[106:107], s[24:25], v[10:11] op_sel_hi:[1,1,0]
v_pk_fma_f32 v[28:29], v[108:109], s[22:23], v[8:9] op_sel_hi:[1,1,0]
v_pk_fma_f32 v[48:49], v[108:109], s[24:25], v[10:11] op_sel_hi:[1,1,0]
v_pk_fma_f32 v[30:31], v[110:111], s[22:23], v[8:9] op_sel_hi:[1,1,0]
v_pk_fma_f32 v[50:51], v[110:111], s[24:25], v[10:11] op_sel_hi:[1,1,0]
v_pk_fma_f32 v[32:33], v[112:113], s[22:23], v[8:9] op_sel_hi:[1,1,0]
v_pk_fma_f32 v[52:53], v[112:113], s[24:25], v[10:11] op_sel_hi:[1,1,0]
v_pk_fma_f32 v[34:35], v[114:115], s[22:23], v[8:9] op_sel_hi:[1,1,0]
v_pk_fma_f32 v[54:55], v[114:115], s[24:25], v[10:11] op_sel_hi:[1,1,0]
v_exp_f32 v16, v16
v_exp_f32 v17, v17
v_exp_f32 v18, v18
v_exp_f32 v19, v19
v_exp_f32 v20, v20
v_exp_f32 v21, v21
v_exp_f32 v22, v22
v_exp_f32 v23, v23
v_exp_f32 v24, v24
v_exp_f32 v25, v25
v_exp_f32 v26, v26
v_exp_f32 v27, v27
v_exp_f32 v28, v28
v_exp_f32 v29, v29
v_exp_f32 v30, v30
v_exp_f32 v31, v31
v_exp_f32 v32, v32
v_exp_f32 v33, v33
v_exp_f32 v34, v34
v_exp_f32 v35, v35
v_exp_f32 v36, v36
v_exp_f32 v37, v37
v_exp_f32 v38, v38
v_exp_f32 v39, v39
v_exp_f32 v40, v40
v_exp_f32 v41, v41
v_exp_f32 v42, v42
v_exp_f32 v43, v43
v_exp_f32 v44, v44
v_exp_f32 v45, v45
v_exp_f32 v46, v46
v_exp_f32 v47, v47
v_exp_f32 v48, v48
v_exp_f32 v49, v49
v_exp_f32 v50, v50
v_exp_f32 v51, v51
v_exp_f32 v52, v52
v_exp_f32 v53, v53
v_exp_f32 v54, v54
v_exp_f32 v55, v55
v_pk_add_f32 v[8:9], v[16:17], v[18:19]
v_pk_add_f32 v[8:9], v[8:9], v[20:21]
v_pk_add_f32 v[8:9], v[8:9], v[22:23]
v_pk_add_f32 v[8:9], v[8:9], v[24:25]
v_pk_add_f32 v[8:9], v[8:9], v[26:27]
v_pk_add_f32 v[8:9], v[8:9], v[28:29]
v_pk_add_f32 v[8:9], v[8:9], v[30:31]
v_pk_add_f32 v[8:9], v[8:9], v[32:33]
v_pk_add_f32 v[8:9], v[8:9], v[34:35]
v_add_f32 v8, v8, v9
v_pk_add_f32 v[10:11], v[36:37], v[38:39]
v_pk_add_f32 v[10:11], v[10:11], v[40:41]
v_pk_add_f32 v[10:11], v[10:11], v[42:43]
v_pk_add_f32 v[10:11], v[10:11], v[44:45]
v_pk_add_f32 v[10:11], v[10:11], v[46:47]
v_pk_add_f32 v[10:11], v[10:11], v[48:49]
v_pk_add_f32 v[10:11], v[10:11], v[50:51]
v_pk_add_f32 v[10:11], v[10:11], v[52:53]
v_pk_add_f32 v[10:11], v[10:11], v[54:55]
v_add_f32 v10, v10, v11
v_rcp_f32 v8, v8
v_rcp_f32 v10, v10
v_pk_mul_f32 v[16:17], v[16:17], v[8:9] op_sel_hi:[1,0]
v_pk_mul_f32 v[18:19], v[18:19], v[8:9] op_sel_hi:[1,0]
v_pk_mul_f32 v[20:21], v[20:21], v[8:9] op_sel_hi:[1,0]
v_pk_mul_f32 v[22:23], v[22:23], v[8:9] op_sel_hi:[1,0]
v_pk_mul_f32 v[24:25], v[24:25], v[8:9] op_sel_hi:[1,0]
v_pk_mul_f32 v[26:27], v[26:27], v[8:9] op_sel_hi:[1,0]
v_pk_mul_f32 v[28:29], v[28:29], v[8:9] op_sel_hi:[1,0]
v_pk_mul_f32 v[30:31], v[30:31], v[8:9] op_sel_hi:[1,0]
v_pk_mul_f32 v[32:33], v[32:33], v[8:9] op_sel_hi:[1,0]
v_pk_mul_f32 v[34:35], v[34:35], v[8:9] op_sel_hi:[1,0]
v_pk_mul_f32 v[36:37], v[36:37], v[10:11] op_sel_hi:[1,0]
v_pk_mul_f32 v[38:39], v[38:39], v[10:11] op_sel_hi:[1,0]
v_pk_mul_f32 v[40:41], v[40:41], v[10:11] op_sel_hi:[1,0]
v_pk_mul_f32 v[42:43], v[42:43], v[10:11] op_sel_hi:[1,0]
v_pk_mul_f32 v[44:45], v[44:45], v[10:11] op_sel_hi:[1,0]
v_pk_mul_f32 v[46:47], v[46:47], v[10:11] op_sel_hi:[1,0]
v_pk_mul_f32 v[48:49], v[48:49], v[10:11] op_sel_hi:[1,0]
v_pk_mul_f32 v[50:51], v[50:51], v[10:11] op_sel_hi:[1,0]
v_pk_mul_f32 v[52:53], v[52:53], v[10:11] op_sel_hi:[1,0]
v_pk_mul_f32 v[54:55], v[54:55], v[10:11] op_sel_hi:[1,0]
v_cvt_pk_bf16_f32 v56, v16, v17
v_cvt_pk_bf16_f32 v57, v18, v19
v_cvt_pk_bf16_f32 v58, v20, v21
v_cvt_pk_bf16_f32 v59, v22, v23
v_cvt_pk_bf16_f32 v60, v24, v25
v_cvt_pk_bf16_f32 v61, v26, v27
v_cvt_pk_bf16_f32 v62, v28, v29
v_cvt_pk_bf16_f32 v63, v30, v31
v_cvt_pk_bf16_f32 v64, v32, v33
v_cvt_pk_bf16_f32 v65, v34, v35
v_cvt_pk_bf16_f32 v66, v36, v37
v_cvt_pk_bf16_f32 v67, v38, v39
v_cvt_pk_bf16_f32 v68, v40, v41
v_cvt_pk_bf16_f32 v69, v42, v43
v_cvt_pk_bf16_f32 v70, v44, v45
v_cvt_pk_bf16_f32 v71, v46, v47
v_cvt_pk_bf16_f32 v72, v48, v49
v_cvt_pk_bf16_f32 v73, v50, v51
v_cvt_pk_bf16_f32 v74, v52, v53
v_cvt_pk_bf16_f32 v75, v54, v55
s_lshl3_add_u32 s48, s28, s26
s_mul_i32 s48, s48, 5120
s_add_u32 s48, s48, 149577728
s_add_u32 s49, s48, 4096
buffer_store_dwordx4 v[56:59], v0, s[40:43], s48 offen sc1
buffer_store_dwordx4 v[60:63], v0, s[40:43], s48 offen offset:1024 sc1
buffer_store_dwordx4 v[64:67], v0, s[40:43], s48 offen offset:2048 sc1
buffer_store_dwordx4 v[68:71], v0, s[40:43], s48 offen offset:3072 sc1
buffer_store_dwordx4 v[72:75], v0, s[40:43], s49 offen sc1
s_lshl3_add_u32 s54, s21, s26
s_lshl_b32 s54, s54, 7
s_add_u32 s54, s54, 2362368
s_branch hp_done_%=
hp_rd_%=:
s_sub_u32 s26, s20, 368
s_lshr_b32 s27, s26, 2
s_lshl3_add_u32 s28, s21, s27
s_lshl_b32 s54, s28, 7
s_add_u32 s54, s54, 2362368
s_cmp_lg_u32 s45, 0
s_cbranch_scc1 hprd_dep_ok_%=
v_cmp_eq_u32 vcc, 0, v15
s_nop 1
v_cndmask_b32 v14, 0, 28, vcc
s_mov_b32 s60, 0
hprd_dep_%=:
v_mov_b32 v4, 0
buffer_atomic_add v4, v2, s[40:43], s54 offen sc0
s_waitcnt vmcnt(0)
v_cmp_gt_u32 vcc, v14, v4
s_nop 4
s_cbranch_vccz hprd_dep_ok_%=
s_sleep 1
s_add_u32 s60, s60, 1
s_cmp_gt_u32 s60, 65536
s_cbranch_scc1 hp_abort_%=
s_and_b32 s59, s60, 63
s_cbranch_scc1 hprd_dep_%=
v_mov_b32 v4, 1024
buffer_load_dword v4, v4, s[40:43], s58 offen sc1
s_waitcnt vmcnt(0)
v_readfirstlane_b32 s59, v4
s_nop 0
s_cmp_lg_u32 s59, 0
s_nop 2
s_cbranch_scc1 hp_end_%=
s_branch hprd_dep_%=
hprd_dep_ok_%=:
s_barrier
s_mul_i32 s48, s45, 224
s_add_u32 s48, s48, s26
s_mul_i32 s48, s48, 6144
s_add_u32 s48, s48, 154820608
s_add_u32 s49, s48, 4096
buffer_load_dwordx4 v[16:19], v0, s[40:43], s48 offen sc1
buffer_load_dwordx4 v[20:23], v0, s[40:43], s48 offen offset:1024 sc1
buffer_load_dwordx4 v[24:27], v0, s[40:43], s48 offen offset:2048 sc1
buffer_load_dwordx4 v[28:31], v0, s[40:43], s48 offen offset:3072 sc1
buffer_load_dwordx4 v[32:35], v0, s[40:43], s49 offen sc1
buffer_load_dwordx4 v[36:39], v0, s[40:43], s49 offen offset:1024 sc1
s_add_u32 s48, s48, 196608
s_add_u32 s49, s48, 4096
buffer_load_dwordx4 v[40:43], v0, s[40:43], s48 offen sc1
buffer_load_dwordx4 v[44:47], v0, s[40:43], s48 offen offset:1024 sc1
buffer_load_dwordx4 v[48:51], v0, s[40:43], s48 offen offset:2048 sc1
buffer_load_dwordx4 v[52:55], v0, s[40:43], s48 offen offset:3072 sc1
buffer_load_dwordx4 v[56:59], v0, s[40:43], s49 offen sc1
buffer_load_dwordx4 v[60:63], v0, s[40:43], s49 offen offset:1024 sc1
s_add_u32 s48, s48, 196608
s_add_u32 s49, s48, 4096
buffer_load_dwordx4 v[64:67], v0, s[40:43], s48 offen sc1
buffer_load_dwordx4 v[68:71], v0, s[40:43], s48 offen offset:1024 sc1
buffer_load_dwordx4 v[72:75], v0, s[40:43], s48 offen offset:2048 sc1
buffer_load_dwordx4 v[76:79], v0, s[40:43], s48 offen offset:3072 sc1
buffer_load_dwordx4 v[80:83], v0, s[40:43], s49 offen sc1
buffer_load_dwordx4 v[84:87], v0, s[40:43], s49 offen offset:1024 sc1
)"
R"(s_add_u32 s48, s48, 196608
s_add_u32 s49, s48, 4096
buffer_load_dwordx4 v[88:91], v0, s[40:43], s48 offen sc1
buffer_load_dwordx4 v[92:95], v0, s[40:43], s48 offen offset:1024 sc1
buffer_load_dwordx4 v[96:99], v0, s[40:43], s48 offen offset:2048 sc1
buffer_load_dwordx4 v[100:103], v0, s[40:43], s48 offen offset:3072 sc1
buffer_load_dwordx4 v[104:107], v0, s[40:43], s49 offen sc1
buffer_load_dwordx4 v[108:111], v0, s[40:43], s49 offen offset:1024 sc1
s_add_u32 s48, s48, 196608
s_add_u32 s49, s48, 4096
buffer_load_dwordx4 v[112:115], v0, s[40:43], s48 offen sc1
buffer_load_dwordx4 v[116:119], v0, s[40:43], s48 offen offset:1024 sc1
buffer_load_dwordx4 v[120:123], v0, s[40:43], s48 offen offset:2048 sc1
buffer_load_dwordx4 v[124:127], v0, s[40:43], s48 offen offset:3072 sc1
buffer_load_dwordx4 v[128:131], v0, s[40:43], s49 offen sc1
buffer_load_dwordx4 v[132:135], v0, s[40:43], s49 offen offset:1024 sc1
s_add_u32 s48, s48, 196608
s_add_u32 s49, s48, 4096
buffer_load_dwordx4 v[136:139], v0, s[40:43], s48 offen sc1
buffer_load_dwordx4 v[140:143], v0, s[40:43], s48 offen offset:1024 sc1
buffer_load_dwordx4 v[144:147], v0, s[40:43], s48 offen offset:2048 sc1
buffer_load_dwordx4 v[148:151], v0, s[40:43], s48 offen offset:3072 sc1
buffer_load_dwordx4 v[152:155], v0, s[40:43], s49 offen sc1
buffer_load_dwordx4 v[156:159], v0, s[40:43], s49 offen offset:1024 sc1
s_add_u32 s48, s48, 196608
s_add_u32 s49, s48, 4096
buffer_load_dwordx4 v[160:163], v0, s[40:43], s48 offen sc1
buffer_load_dwordx4 v[164:167], v0, s[40:43], s48 offen offset:1024 sc1
buffer_load_dwordx4 v[168:171], v0, s[40:43], s48 offen offset:2048 sc1
buffer_load_dwordx4 v[172:175], v0, s[40:43], s48 offen offset:3072 sc1
buffer_load_dwordx4 v[176:179], v0, s[40:43], s49 offen sc1
buffer_load_dwordx4 v[180:183], v0, s[40:43], s49 offen offset:1024 sc1
s_add_u32 s48, s48, 196608
s_mul_i32 s48, s45, 6144
v_add_u32 v6, s48, v0
s_waitcnt vmcnt(35)
v_pk_add_f32 v[16:17], v[16:17], v[40:41]
v_pk_add_f32 v[18:19], v[18:19], v[42:43]
s_waitcnt vmcnt(34)
v_pk_add_f32 v[20:21], v[20:21], v[44:45]
v_pk_add_f32 v[22:23], v[22:23], v[46:47]
s_waitcnt vmcnt(33)
v_pk_add_f32 v[24:25], v[24:25], v[48:49]
v_pk_add_f32 v[26:27], v[26:27], v[50:51]
s_waitcnt vmcnt(32)
v_pk_add_f32 v[28:29], v[28:29], v[52:53]
v_pk_add_f32 v[30:31], v[30:31], v[54:55]
s_waitcnt vmcnt(31)
v_pk_add_f32 v[32:33], v[32:33], v[56:57]
v_pk_add_f32 v[34:35], v[34:35], v[58:59]
s_waitcnt vmcnt(30)
v_pk_add_f32 v[36:37], v[36:37], v[60:61]
v_pk_add_f32 v[38:39], v[38:39], v[62:63]
s_waitcnt vmcnt(29)
v_pk_add_f32 v[16:17], v[16:17], v[64:65]
v_pk_add_f32 v[18:19], v[18:19], v[66:67]
s_waitcnt vmcnt(28)
v_pk_add_f32 v[20:21], v[20:21], v[68:69]
v_pk_add_f32 v[22:23], v[22:23], v[70:71]
s_waitcnt vmcnt(27)
v_pk_add_f32 v[24:25], v[24:25], v[72:73]
v_pk_add_f32 v[26:27], v[26:27], v[74:75]
s_waitcnt vmcnt(26)
v_pk_add_f32 v[28:29], v[28:29], v[76:77]
v_pk_add_f32 v[30:31], v[30:31], v[78:79]
s_waitcnt vmcnt(25)
v_pk_add_f32 v[32:33], v[32:33], v[80:81]
v_pk_add_f32 v[34:35], v[34:35], v[82:83]
s_waitcnt vmcnt(24)
v_pk_add_f32 v[36:37], v[36:37], v[84:85]
v_pk_add_f32 v[38:39], v[38:39], v[86:87]
s_waitcnt vmcnt(23)
v_pk_add_f32 v[16:17], v[16:17], v[88:89]
v_pk_add_f32 v[18:19], v[18:19], v[90:91]
s_waitcnt vmcnt(22)
v_pk_add_f32 v[20:21], v[20:21], v[92:93]
v_pk_add_f32 v[22:23], v[22:23], v[94:95]
s_waitcnt vmcnt(21)
v_pk_add_f32 v[24:25], v[24:25], v[96:97]
v_pk_add_f32 v[26:27], v[26:27], v[98:99]
s_waitcnt vmcnt(20)
v_pk_add_f32 v[28:29], v[28:29], v[100:101]
v_pk_add_f32 v[30:31], v[30:31], v[102:103]
s_waitcnt vmcnt(19)
v_pk_add_f32 v[32:33], v[32:33], v[104:105]
v_pk_add_f32 v[34:35], v[34:35], v[106:107]
s_waitcnt vmcnt(18)
v_pk_add_f32 v[36:37], v[36:37], v[108:109]
v_pk_add_f32 v[38:39], v[38:39], v[110:111]
s_waitcnt vmcnt(17)
v_pk_add_f32 v[16:17], v[16:17], v[112:113]
v_pk_add_f32 v[18:19], v[18:19], v[114:115]
s_waitcnt vmcnt(16)
v_pk_add_f32 v[20:21], v[20:21], v[116:117]
v_pk_add_f32 v[22:23], v[22:23], v[118:119]
s_waitcnt vmcnt(15)
v_pk_add_f32 v[24:25], v[24:25], v[120:121]
v_pk_add_f32 v[26:27], v[26:27], v[122:123]
s_waitcnt vmcnt(14)
v_pk_add_f32 v[28:29], v[28:29], v[124:125]
v_pk_add_f32 v[30:31], v[30:31], v[126:127]
s_waitcnt vmcnt(13)
v_pk_add_f32 v[32:33], v[32:33], v[128:129]
v_pk_add_f32 v[34:35], v[34:35], v[130:131]
s_waitcnt vmcnt(12)
v_pk_add_f32 v[36:37], v[36:37], v[132:133]
v_pk_add_f32 v[38:39], v[38:39], v[134:135]
s_waitcnt vmcnt(11)
v_pk_add_f32 v[16:17], v[16:17], v[136:137]
v_pk_add_f32 v[18:19], v[18:19], v[138:139]
s_waitcnt vmcnt(10)
v_pk_add_f32 v[20:21], v[20:21], v[140:141]
v_pk_add_f32 v[22:23], v[22:23], v[142:143]
s_waitcnt vmcnt(9)
v_pk_add_f32 v[24:25], v[24:25], v[144:145]
v_pk_add_f32 v[26:27], v[26:27], v[146:147]
s_waitcnt vmcnt(8)
v_pk_add_f32 v[28:29], v[28:29], v[148:149]
v_pk_add_f32 v[30:31], v[30:31], v[150:151]
s_waitcnt vmcnt(7)
v_pk_add_f32 v[32:33], v[32:33], v[152:153]
v_pk_add_f32 v[34:35], v[34:35], v[154:155]
s_waitcnt vmcnt(6)
v_pk_add_f32 v[36:37], v[36:37], v[156:157]
v_pk_add_f32 v[38:39], v[38:39], v[158:159]
s_waitcnt vmcnt(5)
v_pk_add_f32 v[16:17], v[16:17], v[160:161]
v_pk_add_f32 v[18:19], v[18:19], v[162:163]
s_waitcnt vmcnt(4)
v_pk_add_f32 v[20:21], v[20:21], v[164:165]
v_pk_add_f32 v[22:23], v[22:23], v[166:167]
s_waitcnt vmcnt(3)
v_pk_add_f32 v[24:25], v[24:25], v[168:169]
v_pk_add_f32 v[26:27], v[26:27], v[170:171]
s_waitcnt vmcnt(2)
v_pk_add_f32 v[28:29], v[28:29], v[172:173]
v_pk_add_f32 v[30:31], v[30:31], v[174:175]
s_waitcnt vmcnt(1)
v_pk_add_f32 v[32:33], v[32:33], v[176:177]
v_pk_add_f32 v[34:35], v[34:35], v[178:179]
s_waitcnt vmcnt(0)
v_pk_add_f32 v[36:37], v[36:37], v[180:181]
v_pk_add_f32 v[38:39], v[38:39], v[182:183]
ds_write_b128 v6, v[16:19] offset:0
ds_write_b128 v6, v[20:23] offset:1024
ds_write_b128 v6, v[24:27] offset:2048
ds_write_b128 v6, v[28:31] offset:3072
ds_write_b128 v6, v[32:35] offset:4096
ds_write_b128 v6, v[36:39] offset:5120
s_waitcnt lgkmcnt(0)
s_barrier
s_mul_i32 s50, s26, 6144
s_add_u32 s50, s50, 161898496
s_add_u32 s48, s45, 0
s_min_u32 s48, s48, 5
s_lshl_b32 s49, s48, 10
v_add_u32 v6, s49, v0
ds_read_b128 v[16:19], v6 offset:0
ds_read_b128 v[20:23], v6 offset:6144
ds_read_b128 v[24:27], v6 offset:12288
ds_read_b128 v[28:31], v6 offset:18432
s_waitcnt lgkmcnt(2)
v_pk_add_f32 v[16:17], v[16:17], v[20:21]
s_waitcnt lgkmcnt(0)
v_pk_add_f32 v[24:25], v[24:25], v[28:29]
v_pk_add_f32 v[16:17], v[16:17], v[24:25]
v_pk_add_f32 v[18:19], v[18:19], v[22:23]
v_pk_add_f32 v[26:27], v[26:27], v[30:31]
v_pk_add_f32 v[18:19], v[18:19], v[26:27]
buffer_store_dwordx4 v[16:19], v6, s[40:43], s50 offen sc1
s_add_u32 s48, s45, 4
s_min_u32 s48, s48, 5
s_lshl_b32 s49, s48, 10
v_add_u32 v7, s49, v0
ds_read_b128 v[48:51], v7 offset:0
ds_read_b128 v[52:55], v7 offset:6144
ds_read_b128 v[56:59], v7 offset:12288
ds_read_b128 v[60:63], v7 offset:18432
s_waitcnt lgkmcnt(2)
v_pk_add_f32 v[48:49], v[48:49], v[52:53]
s_waitcnt lgkmcnt(0)
v_pk_add_f32 v[56:57], v[56:57], v[60:61]
v_pk_add_f32 v[48:49], v[48:49], v[56:57]
v_pk_add_f32 v[50:51], v[50:51], v[54:55]
v_pk_add_f32 v[58:59], v[58:59], v[62:63]
v_pk_add_f32 v[50:51], v[50:51], v[58:59]
buffer_store_dwordx4 v[48:51], v7, s[40:43], s50 offen sc1
s_lshl_b32 s54, s28, 7
s_add_u32 s54, s54, 2368512
s_branch hp_done_%=
hp_b2_%=:
s_sub_u32 s26, s20, 400
s_and_b32 s29, s26, 1
s_lshr_b32 s26, s26, 1
s_and_b32 s30, s45, 1
s_lshr_b32 s31, s45, 1
s_lshl1_add_u32 s31, s29, s31
s_lshl2_add_u32 s50, s29, s45
s_mul_i32 s50, s50, 102400
s_add_u32 s50, s50, 148758528
)"
R"(s_mul_i32 s61, s45, 12288
s_add_u32 s61, s61, 102656
s_add_u32 m0, s61, 0
s_add_u32 s51, s50, 90112
buffer_load_dwordx4 v0, s[40:43], s51 offen offset:0 lds
buffer_load_dwordx4 v0, s[40:43], s51 offen offset:1024 lds
buffer_load_dwordx4 v0, s[40:43], s51 offen offset:2048 lds
buffer_load_dwordx4 v0, s[40:43], s51 offen offset:3072 lds
s_add_u32 m0, s61, 4096
s_add_u32 s51, s50, 94208
buffer_load_dwordx4 v0, s[40:43], s51 offen offset:0 lds
buffer_load_dwordx4 v0, s[40:43], s51 offen offset:1024 lds
buffer_load_dwordx4 v0, s[40:43], s51 offen offset:2048 lds
buffer_load_dwordx4 v0, s[40:43], s51 offen offset:3072 lds
s_add_u32 m0, s61, 8192
s_add_u32 s51, s50, 98304
buffer_load_dwordx4 v0, s[40:43], s51 offen offset:0 lds
buffer_load_dwordx4 v0, s[40:43], s51 offen offset:1024 lds
buffer_load_dwordx4 v0, s[40:43], s51 offen offset:2048 lds
buffer_load_dwordx4 v0, s[40:43], s51 offen offset:3072 lds
buffer_load_dwordx4 v[128:131], v0, s[40:43], s50 offen
buffer_load_dwordx4 v[132:135], v0, s[40:43], s50 offen offset:1024
buffer_load_dwordx4 v[136:139], v0, s[40:43], s50 offen offset:2048
buffer_load_dwordx4 v[140:143], v0, s[40:43], s50 offen offset:3072
s_add_u32 s50, s50, 4096
buffer_load_dwordx4 v[144:147], v0, s[40:43], s50 offen
buffer_load_dwordx4 v[148:151], v0, s[40:43], s50 offen offset:1024
buffer_load_dwordx4 v[152:155], v0, s[40:43], s50 offen offset:2048
buffer_load_dwordx4 v[156:159], v0, s[40:43], s50 offen offset:3072
s_add_u32 s50, s50, 4096
buffer_load_dwordx4 v[160:163], v0, s[40:43], s50 offen
buffer_load_dwordx4 v[164:167], v0, s[40:43], s50 offen offset:1024
buffer_load_dwordx4 v[168:171], v0, s[40:43], s50 offen offset:2048
buffer_load_dwordx4 v[172:175], v0, s[40:43], s50 offen offset:3072
s_add_u32 s50, s50, 4096
buffer_load_dwordx4 v[176:179], v0, s[40:43], s50 offen
buffer_load_dwordx4 v[180:183], v0, s[40:43], s50 offen offset:1024
buffer_load_dwordx4 v[184:187], v0, s[40:43], s50 offen offset:2048
buffer_load_dwordx4 v[188:191], v0, s[40:43], s50 offen offset:3072
s_add_u32 s50, s50, 4096
buffer_load_dwordx4 v[192:195], v0, s[40:43], s50 offen
buffer_load_dwordx4 v[196:199], v0, s[40:43], s50 offen offset:1024
buffer_load_dwordx4 v[200:203], v0, s[40:43], s50 offen offset:2048
buffer_load_dwordx4 v[204:207], v0, s[40:43], s50 offen offset:3072
s_add_u32 s50, s50, 4096
buffer_load_dwordx4 v[208:211], v0, s[40:43], s50 offen
buffer_load_dwordx4 v[212:215], v0, s[40:43], s50 offen offset:1024
buffer_load_dwordx4 v[216:219], v0, s[40:43], s50 offen offset:2048
buffer_load_dwordx4 v[220:223], v0, s[40:43], s50 offen offset:3072
s_add_u32 s50, s50, 4096
buffer_load_dwordx4 v[224:227], v0, s[40:43], s50 offen
buffer_load_dwordx4 v[228:231], v0, s[40:43], s50 offen offset:1024
buffer_load_dwordx4 v[232:235], v0, s[40:43], s50 offen offset:2048
buffer_load_dwordx4 v[236:239], v0, s[40:43], s50 offen offset:3072
s_add_u32 s50, s50, 4096
buffer_load_dwordx4 v[240:243], v0, s[40:43], s50 offen
buffer_load_dwordx4 v[244:247], v0, s[40:43], s50 offen offset:1024
buffer_load_dwordx4 v[248:251], v0, s[40:43], s50 offen offset:2048
buffer_load_dwordx4 v[252:255], v0, s[40:43], s50 offen offset:3072
v_lshlrev_b32 v8, 7, v15
s_waitcnt vmcnt(0)
s_cmp_eq_u32 s21, 0
s_cbranch_scc1 hpb2_warm_%=
s_add_u32 s50, s50, 4096
buffer_load_dwordx4 a[0:3], v0, s[40:43], s50 offen
buffer_load_dwordx4 a[4:7], v0, s[40:43], s50 offen offset:1024
buffer_load_dwordx4 a[8:11], v0, s[40:43], s50 offen offset:2048
buffer_load_dwordx4 a[12:15], v0, s[40:43], s50 offen offset:3072
s_add_u32 s50, s50, 4096
buffer_load_dwordx4 a[16:19], v0, s[40:43], s50 offen
buffer_load_dwordx4 a[20:23], v0, s[40:43], s50 offen offset:1024
buffer_load_dwordx4 a[24:27], v0, s[40:43], s50 offen offset:2048
buffer_load_dwordx4 a[28:31], v0, s[40:43], s50 offen offset:3072
s_add_u32 s50, s50, 4096
buffer_load_dwordx4 a[32:35], v0, s[40:43], s50 offen
buffer_load_dwordx4 a[36:39], v0, s[40:43], s50 offen offset:1024
buffer_load_dwordx4 a[40:43], v0, s[40:43], s50 offen offset:2048
buffer_load_dwordx4 a[44:47], v0, s[40:43], s50 offen offset:3072
s_add_u32 s50, s50, 4096
buffer_load_dwordx4 a[48:51], v0, s[40:43], s50 offen
buffer_load_dwordx4 a[52:55], v0, s[40:43], s50 offen offset:1024
buffer_load_dwordx4 a[56:59], v0, s[40:43], s50 offen offset:2048
buffer_load_dwordx4 a[60:63], v0, s[40:43], s50 offen offset:3072
s_add_u32 s50, s50, 4096
buffer_load_dwordx4 a[64:67], v0, s[40:43], s50 offen
buffer_load_dwordx4 a[68:71], v0, s[40:43], s50 offen offset:1024
buffer_load_dwordx4 a[72:75], v0, s[40:43], s50 offen offset:2048
buffer_load_dwordx4 a[76:79], v0, s[40:43], s50 offen offset:3072
s_add_u32 s50, s50, 4096
buffer_load_dwordx4 a[80:83], v0, s[40:43], s50 offen
buffer_load_dwordx4 a[84:87], v0, s[40:43], s50 offen offset:1024
buffer_load_dwordx4 a[88:91], v0, s[40:43], s50 offen offset:2048
buffer_load_dwordx4 a[92:95], v0, s[40:43], s50 offen offset:3072
s_add_u32 s50, s50, 4096
buffer_load_dwordx4 a[96:99], v0, s[40:43], s50 offen
buffer_load_dwordx4 a[100:103], v0, s[40:43], s50 offen offset:1024
buffer_load_dwordx4 a[104:107], v0, s[40:43], s50 offen offset:2048
buffer_load_dwordx4 a[108:111], v0, s[40:43], s50 offen offset:3072
s_add_u32 s50, s50, 4096
buffer_load_dwordx4 a[112:115], v0, s[40:43], s50 offen
buffer_load_dwordx4 a[116:119], v0, s[40:43], s50 offen offset:1024
buffer_load_dwordx4 a[120:123], v0, s[40:43], s50 offen offset:2048
buffer_load_dwordx4 a[124:127], v0, s[40:43], s50 offen offset:3072
s_add_u32 s50, s50, 4096
buffer_load_dwordx4 a[128:131], v0, s[40:43], s50 offen
buffer_load_dwordx4 a[132:135], v0, s[40:43], s50 offen offset:1024
buffer_load_dwordx4 a[136:139], v0, s[40:43], s50 offen offset:2048
buffer_load_dwordx4 a[140:143], v0, s[40:43], s50 offen offset:3072
s_add_u32 s50, s50, 4096
buffer_load_dwordx4 a[144:147], v0, s[40:43], s50 offen
buffer_load_dwordx4 a[148:151], v0, s[40:43], s50 offen offset:1024
buffer_load_dwordx4 a[152:155], v0, s[40:43], s50 offen offset:2048
buffer_load_dwordx4 a[156:159], v0, s[40:43], s50 offen offset:3072
s_add_u32 s50, s50, 4096
buffer_load_dwordx4 a[160:163], v0, s[40:43], s50 offen
buffer_load_dwordx4 a[164:167], v0, s[40:43], s50 offen offset:1024
buffer_load_dwordx4 a[168:171], v0, s[40:43], s50 offen offset:2048
buffer_load_dwordx4 a[172:175], v0, s[40:43], s50 offen offset:3072
s_add_u32 s50, s50, 4096
buffer_load_dwordx4 a[176:179], v0, s[40:43], s50 offen
buffer_load_dwordx4 a[180:183], v0, s[40:43], s50 offen offset:1024
buffer_load_dwordx4 a[184:187], v0, s[40:43], s50 offen offset:2048
buffer_load_dwordx4 a[188:191], v0, s[40:43], s50 offen offset:3072
s_add_u32 s50, s50, 4096
buffer_load_dwordx4 a[192:195], v0, s[40:43], s50 offen
buffer_load_dwordx4 a[196:199], v0, s[40:43], s50 offen offset:1024
buffer_load_dwordx4 a[200:203], v0, s[40:43], s50 offen offset:2048
buffer_load_dwordx4 a[204:207], v0, s[40:43], s50 offen offset:3072
s_add_u32 s50, s50, 4096
buffer_load_dwordx4 a[208:211], v0, s[40:43], s50 offen
buffer_load_dwordx4 a[212:215], v0, s[40:43], s50 offen offset:1024
buffer_load_dwordx4 a[216:219], v0, s[40:43], s50 offen offset:2048
buffer_load_dwordx4 a[220:223], v0, s[40:43], s50 offen offset:3072
s_waitcnt vmcnt(0)
s_nop 0
s_branch hpb2_pre_%=
hpb2_warm_%=:
s_add_u32 s48, s50, 4096
buffer_load_dword v24, v8, s[40:43], s48 offen
s_add_u32 s48, s48, 8192
buffer_load_dword v25, v8, s[40:43], s48 offen
s_add_u32 s48, s48, 8192
buffer_load_dword v26, v8, s[40:43], s48 offen
s_add_u32 s48, s48, 8192
buffer_load_dword v27, v8, s[40:43], s48 offen
s_add_u32 s48, s48, 8192
buffer_load_dword v28, v8, s[40:43], s48 offen
s_add_u32 s48, s48, 8192
buffer_load_dword v29, v8, s[40:43], s48 offen
s_add_u32 s48, s48, 8192
buffer_load_dword v30, v8, s[40:43], s48 offen
s_add_u32 s48, s48, 8192
s_waitcnt vmcnt(0)
hpb2_pre_%=:
v_mov_b32 v16, 0
v_lshlrev_b32 v4, 2, v15
ds_write_b32 v4, v16 offset:61440
s_mul_i32 s27, s45, 15360
v_add_u32 v6, s27, v0
v_and_b32 v7, 0xf0, v0
v_add_u32 v7, s27, v7
v_mov_b32 v9, 61440
v_mov_b32 v3, 256
v_bfe_u32 v4, v15, 2, 2
v_lshrrev_b32 v5, 4, v15
v_cmp_eq_u32 vcc, v4, v5
s_nop 1
v_cndmask_b32 v7, v9, v7, vcc
v_cndmask_b32 v3, 0, v3, vcc
s_mul_i32 s48, s45, 5120
s_add_u32 s48, s48, 61696
v_add_u32 v9, s48, v0
s_lshl3_add_u32 s28, s26, s45
s_mul_i32 s28, s28, 10240
s_lshl1_add_u32 s34, s26, s30
s_lshl2_add_u32 s34, s34, s31
s_mul_i32 s34, s34, 10240
s_cmp_ge_u32 s26, 56
s_cbranch_scc1 hpb2_cold_%=
v_and_b32 v10, 7, v15
v_lshlrev_b32 v10, 7, v10
s_lshl_b32 s48, s21, 10
s_add_u32 s48, s48, 265216
v_add_u32 v10, s48, v10
v_mov_b32 v11, 0
s_mov_b32 s48, 28
v_cmp_gt_u32 vcc, s48, v11
s_cbranch_vccz hpb2_early_ok_%=
s_mov_b32 s60, 0
s_nop 2
hpb2_early_poll_%=:
buffer_load_dword v11, v10, s[40:43], s58 offen sc1
s_waitcnt vmcnt(0)
v_cmp_gt_u32 vcc, s48, v11
s_nop 4
s_cbranch_vccz hpb2_early_ok_%=
s_sleep 20
s_add_u32 s60, s60, 1
s_cmp_gt_u32 s60, 65536
s_cbranch_scc1 hp_abort_%=
)"
R"(s_and_b32 s59, s60, 63
s_cbranch_scc1 hpb2_early_poll_%=
v_mov_b32 v4, 1024
buffer_load_dword v4, v4, s[40:43], s58 offen sc1
s_waitcnt vmcnt(0)
v_readfirstlane_b32 s59, v4
s_nop 0
s_cmp_lg_u32 s59, 0
s_nop 2
s_cbranch_scc1 hp_end_%=
s_branch hpb2_early_poll_%=
hpb2_early_ok_%=:
s_add_u32 s48, s28, 149577728
buffer_load_dword v24, v8, s[40:43], s48 offen sc1
s_add_u32 s48, s28, 149579776
buffer_load_dword v25, v8, s[40:43], s48 offen sc1
s_add_u32 s48, s28, 149618688
buffer_load_dword v26, v8, s[40:43], s48 offen sc1
s_add_u32 s48, s28, 149620736
buffer_load_dword v27, v8, s[40:43], s48 offen sc1
s_add_u32 s48, s34, 138346496
buffer_load_dword v28, v8, s[40:43], s48 offen sc1
s_add_u32 s48, s34, 138348544
buffer_load_dword v29, v8, s[40:43], s48 offen sc1
s_waitcnt vmcnt(0)
hpb2_cold_%=:
v_and_b32 v8, 0x1f0, v0
v_lshrrev_b32 v4, 5, v15
v_lshl_add_u32 v8, v4, 10, v8
s_and_b32 s54, s20, 7
s_lshl3_add_u32 s54, s21, s54
s_lshl_b32 s54, s54, 7
s_add_u32 s54, s54, 2370560
s_waitcnt lgkmcnt(0)
s_cmp_lg_u32 s45, 0
s_cbranch_scc1 hpb2_dep_ok_%=
v_cmp_eq_u32 vcc, 0, v15
s_nop 1
v_cndmask_b32 v14, 0, 8, vcc
s_mov_b32 s60, 0
hpb2_dep_%=:
v_mov_b32 v4, 0
buffer_atomic_add v4, v2, s[40:43], s54 offen sc0
s_waitcnt vmcnt(0)
v_cmp_gt_u32 vcc, v14, v4
s_nop 4
s_cbranch_vccz hpb2_dep_ok_%=
s_sleep 1
s_add_u32 s60, s60, 1
s_cmp_gt_u32 s60, 65536
s_cbranch_scc1 hp_abort_%=
s_and_b32 s59, s60, 63
s_cbranch_scc1 hpb2_dep_%=
v_mov_b32 v4, 1024
buffer_load_dword v4, v4, s[40:43], s58 offen sc1
s_waitcnt vmcnt(0)
v_readfirstlane_b32 s59, v4
s_nop 0
s_cmp_lg_u32 s59, 0
s_nop 2
s_cbranch_scc1 hp_end_%=
s_branch hpb2_dep_%=
hpb2_dep_ok_%=:
s_barrier
s_add_u32 s48, s27, 162095104
buffer_load_dwordx4 v[56:59], v0, s[40:43], s48 offen sc1
buffer_load_dwordx4 v[60:63], v0, s[40:43], s48 offen offset:1024 sc1
buffer_load_dwordx4 v[64:67], v0, s[40:43], s48 offen offset:2048 sc1
buffer_load_dwordx4 v[68:71], v0, s[40:43], s48 offen offset:3072 sc1
s_add_u32 s48, s48, 4096
buffer_load_dwordx4 v[72:75], v0, s[40:43], s48 offen sc1
buffer_load_dwordx4 v[76:79], v0, s[40:43], s48 offen offset:1024 sc1
buffer_load_dwordx4 v[80:83], v0, s[40:43], s48 offen offset:2048 sc1
buffer_load_dwordx4 v[84:87], v0, s[40:43], s48 offen offset:3072 sc1
s_add_u32 s48, s48, 4096
buffer_load_dwordx4 v[88:91], v0, s[40:43], s48 offen sc1
buffer_load_dwordx4 v[92:95], v0, s[40:43], s48 offen offset:1024 sc1
buffer_load_dwordx4 v[96:99], v0, s[40:43], s48 offen offset:2048 sc1
buffer_load_dwordx4 v[100:103], v0, s[40:43], s48 offen offset:3072 sc1
s_add_u32 s48, s48, 4096
buffer_load_dwordx4 v[104:107], v0, s[40:43], s48 offen sc1
buffer_load_dwordx4 v[108:111], v0, s[40:43], s48 offen offset:1024 sc1
buffer_load_dwordx4 v[112:115], v0, s[40:43], s48 offen offset:2048 sc1
s_add_u32 s48, s28, 149577728
s_add_u32 s49, s48, 4096
buffer_load_dwordx4 v[16:19], v0, s[40:43], s48 offen sc1
buffer_load_dwordx4 v[20:23], v0, s[40:43], s48 offen offset:1024 sc1
buffer_load_dwordx4 v[24:27], v0, s[40:43], s48 offen offset:2048 sc1
buffer_load_dwordx4 v[28:31], v0, s[40:43], s48 offen offset:3072 sc1
buffer_load_dwordx4 v[32:35], v0, s[40:43], s49 offen sc1
s_add_u32 s48, s28, 149582848
s_add_u32 s49, s48, 4096
buffer_load_dwordx4 v[36:39], v0, s[40:43], s48 offen sc1
buffer_load_dwordx4 v[40:43], v0, s[40:43], s48 offen offset:1024 sc1
buffer_load_dwordx4 v[44:47], v0, s[40:43], s48 offen offset:2048 sc1
buffer_load_dwordx4 v[48:51], v0, s[40:43], s48 offen offset:3072 sc1
buffer_load_dwordx4 v[52:55], v0, s[40:43], s49 offen sc1
s_waitcnt vmcnt(24)
ds_write_b128 v6, v[56:59] offset:0
s_waitcnt vmcnt(23)
ds_write_b128 v6, v[60:63] offset:1024
s_waitcnt vmcnt(22)
ds_write_b128 v6, v[64:67] offset:2048
s_waitcnt vmcnt(21)
ds_write_b128 v6, v[68:71] offset:3072
s_waitcnt vmcnt(20)
ds_write_b128 v6, v[72:75] offset:4096
s_waitcnt vmcnt(19)
ds_write_b128 v6, v[76:79] offset:5120
s_waitcnt vmcnt(18)
ds_write_b128 v6, v[80:83] offset:6144
s_waitcnt vmcnt(17)
ds_write_b128 v6, v[84:87] offset:7168
s_waitcnt vmcnt(16)
ds_write_b128 v6, v[88:91] offset:8192
s_waitcnt vmcnt(15)
ds_write_b128 v6, v[92:95] offset:9216
s_waitcnt vmcnt(14)
ds_write_b128 v6, v[96:99] offset:10240
s_waitcnt vmcnt(13)
ds_write_b128 v6, v[100:103] offset:11264
s_waitcnt vmcnt(12)
ds_write_b128 v6, v[104:107] offset:12288
s_waitcnt vmcnt(11)
ds_write_b128 v6, v[108:111] offset:13312
s_waitcnt vmcnt(10)
ds_write_b128 v6, v[112:115] offset:14336
v_mov_b32 v6, v7
s_waitcnt lgkmcnt(0)
ds_read_b128 v[80:83], v7
v_add_u32 v7, v7, v3
ds_read_b128 v[84:87], v7
v_add_u32 v7, v7, v3
ds_read_b128 v[88:91], v7
v_add_u32 v7, v7, v3
ds_read_b128 v[92:95], v7
v_add_u32 v7, v7, v3
ds_read_b128 v[96:99], v7
v_add_u32 v7, v7, v3
s_waitcnt vmcnt(9) lgkmcnt(4)
v_mfma_f32_16x16x32_bf16 v[56:59], v[80:83], v[16:19], 0
ds_read_b128 v[80:83], v7
v_add_u32 v7, v7, v3
s_waitcnt vmcnt(8) lgkmcnt(4)
v_mfma_f32_16x16x32_bf16 v[56:59], v[84:87], v[20:23], v[56:59]
ds_read_b128 v[84:87], v7
v_add_u32 v7, v7, v3
s_waitcnt vmcnt(7) lgkmcnt(4)
v_mfma_f32_16x16x32_bf16 v[56:59], v[88:91], v[24:27], v[56:59]
ds_read_b128 v[88:91], v7
v_add_u32 v7, v7, v3
s_waitcnt vmcnt(6) lgkmcnt(4)
v_mfma_f32_16x16x32_bf16 v[56:59], v[92:95], v[28:31], v[56:59]
ds_read_b128 v[92:95], v7
v_add_u32 v7, v7, v3
s_waitcnt vmcnt(5) lgkmcnt(4)
v_mfma_f32_16x16x32_bf16 v[56:59], v[96:99], v[32:35], v[56:59]
ds_read_b128 v[96:99], v7
v_add_u32 v7, v7, v3
s_waitcnt lgkmcnt(4)
v_mfma_f32_16x16x32_bf16 v[60:63], v[80:83], v[16:19], 0
ds_read_b128 v[80:83], v7
v_add_u32 v7, v7, v3
s_waitcnt lgkmcnt(4)
v_mfma_f32_16x16x32_bf16 v[60:63], v[84:87], v[20:23], v[60:63]
ds_read_b128 v[84:87], v7
v_add_u32 v7, v7, v3
s_waitcnt lgkmcnt(4)
v_mfma_f32_16x16x32_bf16 v[60:63], v[88:91], v[24:27], v[60:63]
ds_read_b128 v[88:91], v7
v_add_u32 v7, v7, v3
s_waitcnt lgkmcnt(4)
v_mfma_f32_16x16x32_bf16 v[60:63], v[92:95], v[28:31], v[60:63]
ds_read_b128 v[92:95], v7
v_add_u32 v7, v7, v3
s_waitcnt lgkmcnt(4)
v_mfma_f32_16x16x32_bf16 v[60:63], v[96:99], v[32:35], v[60:63]
ds_read_b128 v[96:99], v7
v_add_u32 v7, v7, v3
s_waitcnt lgkmcnt(4)
v_mfma_f32_16x16x32_bf16 v[64:67], v[80:83], v[16:19], 0
ds_read_b128 v[80:83], v7
v_add_u32 v7, v7, v3
s_waitcnt lgkmcnt(4)
v_mfma_f32_16x16x32_bf16 v[64:67], v[84:87], v[20:23], v[64:67]
ds_read_b128 v[84:87], v7
v_add_u32 v7, v7, v3
s_waitcnt lgkmcnt(4)
v_mfma_f32_16x16x32_bf16 v[64:67], v[88:91], v[24:27], v[64:67]
ds_read_b128 v[88:91], v7
v_add_u32 v7, v7, v3
s_waitcnt lgkmcnt(4)
v_mfma_f32_16x16x32_bf16 v[64:67], v[92:95], v[28:31], v[64:67]
ds_read_b128 v[92:95], v7
v_add_u32 v7, v7, v3
s_waitcnt lgkmcnt(4)
v_mfma_f32_16x16x32_bf16 v[64:67], v[96:99], v[32:35], v[64:67]
ds_read_b128 v[96:99], v7
v_add_u32 v7, v7, v3
s_waitcnt lgkmcnt(4)
)"
R"(v_mfma_f32_16x16x32_bf16 v[68:71], v[80:83], v[16:19], 0
ds_read_b128 v[80:83], v7
v_add_u32 v7, v7, v3
s_waitcnt lgkmcnt(4)
v_mfma_f32_16x16x32_bf16 v[68:71], v[84:87], v[20:23], v[68:71]
ds_read_b128 v[84:87], v7
v_add_u32 v7, v7, v3
s_waitcnt lgkmcnt(4)
v_mfma_f32_16x16x32_bf16 v[68:71], v[88:91], v[24:27], v[68:71]
ds_read_b128 v[88:91], v7
v_add_u32 v7, v7, v3
s_waitcnt lgkmcnt(4)
v_mfma_f32_16x16x32_bf16 v[68:71], v[92:95], v[28:31], v[68:71]
ds_read_b128 v[92:95], v7
v_add_u32 v7, v7, v3
s_waitcnt lgkmcnt(4)
v_mfma_f32_16x16x32_bf16 v[68:71], v[96:99], v[32:35], v[68:71]
ds_read_b128 v[96:99], v7
v_add_u32 v7, v7, v3
s_waitcnt lgkmcnt(4)
v_mfma_f32_16x16x32_bf16 v[72:75], v[80:83], v[16:19], 0
ds_read_b128 v[80:83], v7
v_add_u32 v7, v7, v3
s_waitcnt lgkmcnt(4)
v_mfma_f32_16x16x32_bf16 v[72:75], v[84:87], v[20:23], v[72:75]
ds_read_b128 v[84:87], v7
v_add_u32 v7, v7, v3
s_waitcnt lgkmcnt(4)
v_mfma_f32_16x16x32_bf16 v[72:75], v[88:91], v[24:27], v[72:75]
ds_read_b128 v[88:91], v7
v_add_u32 v7, v7, v3
s_waitcnt lgkmcnt(4)
v_mfma_f32_16x16x32_bf16 v[72:75], v[92:95], v[28:31], v[72:75]
ds_read_b128 v[92:95], v7
v_add_u32 v7, v7, v3
s_waitcnt lgkmcnt(4)
v_mfma_f32_16x16x32_bf16 v[72:75], v[96:99], v[32:35], v[72:75]
ds_read_b128 v[96:99], v7
v_add_u32 v7, v7, v3
s_waitcnt lgkmcnt(4)
v_mfma_f32_16x16x32_bf16 v[76:79], v[80:83], v[16:19], 0
ds_read_b128 v[80:83], v7
v_add_u32 v7, v7, v3
s_waitcnt lgkmcnt(4)
v_mfma_f32_16x16x32_bf16 v[76:79], v[84:87], v[20:23], v[76:79]
ds_read_b128 v[84:87], v7
v_add_u32 v7, v7, v3
s_waitcnt lgkmcnt(4)
v_mfma_f32_16x16x32_bf16 v[76:79], v[88:91], v[24:27], v[76:79]
ds_read_b128 v[88:91], v7
v_add_u32 v7, v7, v3
s_waitcnt lgkmcnt(4)
v_mfma_f32_16x16x32_bf16 v[76:79], v[92:95], v[28:31], v[76:79]
ds_read_b128 v[92:95], v7
v_add_u32 v7, v7, v3
s_waitcnt lgkmcnt(4)
v_mfma_f32_16x16x32_bf16 v[76:79], v[96:99], v[32:35], v[76:79]
ds_read_b128 v[96:99], v7
v_add_u32 v7, v7, v3
s_add_u32 s48, s28, 149618688
s_add_u32 s49, s48, 4096
buffer_load_dwordx4 v[16:19], v0, s[40:43], s48 offen sc1
buffer_load_dwordx4 v[20:23], v0, s[40:43], s48 offen offset:1024 sc1
buffer_load_dwordx4 v[24:27], v0, s[40:43], s48 offen offset:2048 sc1
buffer_load_dwordx4 v[28:31], v0, s[40:43], s48 offen offset:3072 sc1
buffer_load_dwordx4 v[32:35], v0, s[40:43], s49 offen sc1
v_add_f32 v76, 0x358637bd, v76
v_rcp_f32 v76, v76
s_nop 0
v_pk_mul_f32 v[56:57], v[56:57], v[76:77] op_sel_hi:[1,0]
v_pk_mul_f32 v[58:59], v[58:59], v[76:77] op_sel_hi:[1,0]
v_pk_mul_f32 v[60:61], v[60:61], v[76:77] op_sel_hi:[1,0]
v_pk_mul_f32 v[62:63], v[62:63], v[76:77] op_sel_hi:[1,0]
v_pk_mul_f32 v[64:65], v[64:65], v[76:77] op_sel_hi:[1,0]
v_pk_mul_f32 v[66:67], v[66:67], v[76:77] op_sel_hi:[1,0]
v_pk_mul_f32 v[68:69], v[68:69], v[76:77] op_sel_hi:[1,0]
v_pk_mul_f32 v[70:71], v[70:71], v[76:77] op_sel_hi:[1,0]
v_pk_mul_f32 v[72:73], v[72:73], v[76:77] op_sel_hi:[1,0]
v_pk_mul_f32 v[74:75], v[74:75], v[76:77] op_sel_hi:[1,0]
v_cvt_pk_bf16_f32 v100, v56, v57
v_cvt_pk_bf16_f32 v101, v58, v59
v_cvt_pk_bf16_f32 v102, v60, v61
v_cvt_pk_bf16_f32 v103, v62, v63
v_cvt_pk_bf16_f32 v104, v64, v65
v_cvt_pk_bf16_f32 v105, v66, v67
v_cvt_pk_bf16_f32 v106, v68, v69
v_cvt_pk_bf16_f32 v107, v70, v71
v_cvt_pk_bf16_f32 v108, v72, v73
v_cvt_pk_bf16_f32 v109, v74, v75
s_waitcnt vmcnt(9) lgkmcnt(4)
v_mfma_f32_16x16x32_bf16 v[56:59], v[80:83], v[36:39], 0
ds_read_b128 v[80:83], v7
v_add_u32 v7, v7, v3
s_waitcnt vmcnt(8) lgkmcnt(4)
v_mfma_f32_16x16x32_bf16 v[56:59], v[84:87], v[40:43], v[56:59]
ds_read_b128 v[84:87], v7
v_add_u32 v7, v7, v3
s_waitcnt vmcnt(7) lgkmcnt(4)
v_mfma_f32_16x16x32_bf16 v[56:59], v[88:91], v[44:47], v[56:59]
ds_read_b128 v[88:91], v7
v_add_u32 v7, v7, v3
s_waitcnt vmcnt(6) lgkmcnt(4)
v_mfma_f32_16x16x32_bf16 v[56:59], v[92:95], v[48:51], v[56:59]
ds_read_b128 v[92:95], v7
v_add_u32 v7, v7, v3
s_waitcnt vmcnt(5) lgkmcnt(4)
v_mfma_f32_16x16x32_bf16 v[56:59], v[96:99], v[52:55], v[56:59]
ds_read_b128 v[96:99], v7
v_add_u32 v7, v7, v3
s_waitcnt lgkmcnt(4)
v_mfma_f32_16x16x32_bf16 v[60:63], v[80:83], v[36:39], 0
ds_read_b128 v[80:83], v7
v_add_u32 v7, v7, v3
s_waitcnt lgkmcnt(4)
v_mfma_f32_16x16x32_bf16 v[60:63], v[84:87], v[40:43], v[60:63]
ds_read_b128 v[84:87], v7
v_add_u32 v7, v7, v3
s_waitcnt lgkmcnt(4)
v_mfma_f32_16x16x32_bf16 v[60:63], v[88:91], v[44:47], v[60:63]
ds_read_b128 v[88:91], v7
v_add_u32 v7, v7, v3
s_waitcnt lgkmcnt(4)
v_mfma_f32_16x16x32_bf16 v[60:63], v[92:95], v[48:51], v[60:63]
ds_read_b128 v[92:95], v7
v_add_u32 v7, v7, v3
s_waitcnt lgkmcnt(4)
v_mfma_f32_16x16x32_bf16 v[60:63], v[96:99], v[52:55], v[60:63]
ds_read_b128 v[96:99], v7
v_add_u32 v7, v7, v3
s_waitcnt lgkmcnt(4)
v_mfma_f32_16x16x32_bf16 v[64:67], v[80:83], v[36:39], 0
ds_read_b128 v[80:83], v7
v_add_u32 v7, v7, v3
s_waitcnt lgkmcnt(4)
v_mfma_f32_16x16x32_bf16 v[64:67], v[84:87], v[40:43], v[64:67]
ds_read_b128 v[84:87], v7
v_add_u32 v7, v7, v3
s_waitcnt lgkmcnt(4)
v_mfma_f32_16x16x32_bf16 v[64:67], v[88:91], v[44:47], v[64:67]
ds_read_b128 v[88:91], v7
v_add_u32 v7, v7, v3
s_waitcnt lgkmcnt(4)
v_mfma_f32_16x16x32_bf16 v[64:67], v[92:95], v[48:51], v[64:67]
ds_read_b128 v[92:95], v7
v_add_u32 v7, v7, v3
s_waitcnt lgkmcnt(4)
v_mfma_f32_16x16x32_bf16 v[64:67], v[96:99], v[52:55], v[64:67]
ds_read_b128 v[96:99], v7
v_add_u32 v7, v7, v3
s_waitcnt lgkmcnt(4)
v_mfma_f32_16x16x32_bf16 v[68:71], v[80:83], v[36:39], 0
ds_read_b128 v[80:83], v7
v_add_u32 v7, v7, v3
s_waitcnt lgkmcnt(4)
v_mfma_f32_16x16x32_bf16 v[68:71], v[84:87], v[40:43], v[68:71]
ds_read_b128 v[84:87], v7
v_add_u32 v7, v7, v3
s_waitcnt lgkmcnt(4)
v_mfma_f32_16x16x32_bf16 v[68:71], v[88:91], v[44:47], v[68:71]
ds_read_b128 v[88:91], v7
v_add_u32 v7, v7, v3
s_waitcnt lgkmcnt(4)
v_mfma_f32_16x16x32_bf16 v[68:71], v[92:95], v[48:51], v[68:71]
ds_read_b128 v[92:95], v7
v_add_u32 v7, v7, v3
s_waitcnt lgkmcnt(4)
v_mfma_f32_16x16x32_bf16 v[68:71], v[96:99], v[52:55], v[68:71]
ds_read_b128 v[96:99], v7
v_add_u32 v7, v7, v3
s_waitcnt lgkmcnt(4)
v_mfma_f32_16x16x32_bf16 v[72:75], v[80:83], v[36:39], 0
ds_read_b128 v[80:83], v7
v_add_u32 v7, v7, v3
s_waitcnt lgkmcnt(4)
v_mfma_f32_16x16x32_bf16 v[72:75], v[84:87], v[40:43], v[72:75]
ds_read_b128 v[84:87], v7
v_add_u32 v7, v7, v3
s_waitcnt lgkmcnt(4)
v_mfma_f32_16x16x32_bf16 v[72:75], v[88:91], v[44:47], v[72:75]
ds_read_b128 v[88:91], v7
v_add_u32 v7, v7, v3
s_waitcnt lgkmcnt(4)
v_mfma_f32_16x16x32_bf16 v[72:75], v[92:95], v[48:51], v[72:75]
ds_read_b128 v[92:95], v7
v_add_u32 v7, v7, v3
s_waitcnt lgkmcnt(4)
v_mfma_f32_16x16x32_bf16 v[72:75], v[96:99], v[52:55], v[72:75]
ds_read_b128 v[96:99], v7
v_mov_b32 v7, v6
s_waitcnt lgkmcnt(4)
v_mfma_f32_16x16x32_bf16 v[76:79], v[80:83], v[36:39], 0
ds_read_b128 v[80:83], v7
v_add_u32 v7, v7, v3
s_waitcnt lgkmcnt(4)
v_mfma_f32_16x16x32_bf16 v[76:79], v[84:87], v[40:43], v[76:79]
ds_read_b128 v[84:87], v7
v_add_u32 v7, v7, v3
s_waitcnt lgkmcnt(4)
v_mfma_f32_16x16x32_bf16 v[76:79], v[88:91], v[44:47], v[76:79]
ds_read_b128 v[88:91], v7
)"
R"(v_add_u32 v7, v7, v3
s_waitcnt lgkmcnt(4)
v_mfma_f32_16x16x32_bf16 v[76:79], v[92:95], v[48:51], v[76:79]
ds_read_b128 v[92:95], v7
v_add_u32 v7, v7, v3
s_waitcnt lgkmcnt(4)
v_mfma_f32_16x16x32_bf16 v[76:79], v[96:99], v[52:55], v[76:79]
ds_read_b128 v[96:99], v7
v_add_u32 v7, v7, v3
s_add_u32 s48, s28, 149623808
s_add_u32 s49, s48, 4096
buffer_load_dwordx4 v[36:39], v0, s[40:43], s48 offen sc1
buffer_load_dwordx4 v[40:43], v0, s[40:43], s48 offen offset:1024 sc1
buffer_load_dwordx4 v[44:47], v0, s[40:43], s48 offen offset:2048 sc1
buffer_load_dwordx4 v[48:51], v0, s[40:43], s48 offen offset:3072 sc1
buffer_load_dwordx4 v[52:55], v0, s[40:43], s49 offen sc1
v_add_f32 v76, 0x358637bd, v76
v_rcp_f32 v76, v76
s_nop 0
v_pk_mul_f32 v[56:57], v[56:57], v[76:77] op_sel_hi:[1,0]
v_pk_mul_f32 v[58:59], v[58:59], v[76:77] op_sel_hi:[1,0]
v_pk_mul_f32 v[60:61], v[60:61], v[76:77] op_sel_hi:[1,0]
v_pk_mul_f32 v[62:63], v[62:63], v[76:77] op_sel_hi:[1,0]
v_pk_mul_f32 v[64:65], v[64:65], v[76:77] op_sel_hi:[1,0]
v_pk_mul_f32 v[66:67], v[66:67], v[76:77] op_sel_hi:[1,0]
v_pk_mul_f32 v[68:69], v[68:69], v[76:77] op_sel_hi:[1,0]
v_pk_mul_f32 v[70:71], v[70:71], v[76:77] op_sel_hi:[1,0]
v_pk_mul_f32 v[72:73], v[72:73], v[76:77] op_sel_hi:[1,0]
v_pk_mul_f32 v[74:75], v[74:75], v[76:77] op_sel_hi:[1,0]
v_cvt_pk_bf16_f32 v110, v56, v57
v_cvt_pk_bf16_f32 v111, v58, v59
v_cvt_pk_bf16_f32 v112, v60, v61
v_cvt_pk_bf16_f32 v113, v62, v63
v_cvt_pk_bf16_f32 v114, v64, v65
v_cvt_pk_bf16_f32 v115, v66, v67
v_cvt_pk_bf16_f32 v116, v68, v69
v_cvt_pk_bf16_f32 v117, v70, v71
v_cvt_pk_bf16_f32 v118, v72, v73
v_cvt_pk_bf16_f32 v119, v74, v75
ds_write_b128 v9, v[100:103] offset:0
ds_write_b128 v9, v[104:107] offset:1024
ds_write_b128 v9, v[108:111] offset:2048
ds_write_b128 v9, v[112:115] offset:3072
ds_write_b128 v9, v[116:119] offset:4096
s_waitcnt vmcnt(9) lgkmcnt(9)
v_mfma_f32_16x16x32_bf16 v[56:59], v[80:83], v[16:19], 0
ds_read_b128 v[80:83], v7
v_add_u32 v7, v7, v3
s_waitcnt vmcnt(8) lgkmcnt(9)
v_mfma_f32_16x16x32_bf16 v[56:59], v[84:87], v[20:23], v[56:59]
ds_read_b128 v[84:87], v7
v_add_u32 v7, v7, v3
s_waitcnt vmcnt(7) lgkmcnt(9)
v_mfma_f32_16x16x32_bf16 v[56:59], v[88:91], v[24:27], v[56:59]
ds_read_b128 v[88:91], v7
v_add_u32 v7, v7, v3
s_waitcnt vmcnt(6) lgkmcnt(9)
v_mfma_f32_16x16x32_bf16 v[56:59], v[92:95], v[28:31], v[56:59]
ds_read_b128 v[92:95], v7
v_add_u32 v7, v7, v3
s_waitcnt vmcnt(5) lgkmcnt(9)
v_mfma_f32_16x16x32_bf16 v[56:59], v[96:99], v[32:35], v[56:59]
ds_read_b128 v[96:99], v7
v_add_u32 v7, v7, v3
s_waitcnt lgkmcnt(4)
v_mfma_f32_16x16x32_bf16 v[60:63], v[80:83], v[16:19], 0
ds_read_b128 v[80:83], v7
v_add_u32 v7, v7, v3
s_waitcnt lgkmcnt(4)
v_mfma_f32_16x16x32_bf16 v[60:63], v[84:87], v[20:23], v[60:63]
ds_read_b128 v[84:87], v7
v_add_u32 v7, v7, v3
s_waitcnt lgkmcnt(4)
v_mfma_f32_16x16x32_bf16 v[60:63], v[88:91], v[24:27], v[60:63]
ds_read_b128 v[88:91], v7
v_add_u32 v7, v7, v3
s_waitcnt lgkmcnt(4)
v_mfma_f32_16x16x32_bf16 v[60:63], v[92:95], v[28:31], v[60:63]
ds_read_b128 v[92:95], v7
v_add_u32 v7, v7, v3
s_waitcnt lgkmcnt(4)
v_mfma_f32_16x16x32_bf16 v[60:63], v[96:99], v[32:35], v[60:63]
ds_read_b128 v[96:99], v7
v_add_u32 v7, v7, v3
s_waitcnt lgkmcnt(4)
v_mfma_f32_16x16x32_bf16 v[64:67], v[80:83], v[16:19], 0
ds_read_b128 v[80:83], v7
v_add_u32 v7, v7, v3
s_waitcnt lgkmcnt(4)
v_mfma_f32_16x16x32_bf16 v[64:67], v[84:87], v[20:23], v[64:67]
ds_read_b128 v[84:87], v7
v_add_u32 v7, v7, v3
s_waitcnt lgkmcnt(4)
v_mfma_f32_16x16x32_bf16 v[64:67], v[88:91], v[24:27], v[64:67]
ds_read_b128 v[88:91], v7
v_add_u32 v7, v7, v3
s_waitcnt lgkmcnt(4)
v_mfma_f32_16x16x32_bf16 v[64:67], v[92:95], v[28:31], v[64:67]
ds_read_b128 v[92:95], v7
v_add_u32 v7, v7, v3
s_waitcnt lgkmcnt(4)
v_mfma_f32_16x16x32_bf16 v[64:67], v[96:99], v[32:35], v[64:67]
ds_read_b128 v[96:99], v7
v_add_u32 v7, v7, v3
s_waitcnt lgkmcnt(4)
v_mfma_f32_16x16x32_bf16 v[68:71], v[80:83], v[16:19], 0
ds_read_b128 v[80:83], v7
v_add_u32 v7, v7, v3
s_waitcnt lgkmcnt(4)
v_mfma_f32_16x16x32_bf16 v[68:71], v[84:87], v[20:23], v[68:71]
ds_read_b128 v[84:87], v7
v_add_u32 v7, v7, v3
s_waitcnt lgkmcnt(4)
v_mfma_f32_16x16x32_bf16 v[68:71], v[88:91], v[24:27], v[68:71]
ds_read_b128 v[88:91], v7
v_add_u32 v7, v7, v3
s_waitcnt lgkmcnt(4)
v_mfma_f32_16x16x32_bf16 v[68:71], v[92:95], v[28:31], v[68:71]
ds_read_b128 v[92:95], v7
v_add_u32 v7, v7, v3
s_waitcnt lgkmcnt(4)
v_mfma_f32_16x16x32_bf16 v[68:71], v[96:99], v[32:35], v[68:71]
ds_read_b128 v[96:99], v7
v_add_u32 v7, v7, v3
s_waitcnt lgkmcnt(4)
v_mfma_f32_16x16x32_bf16 v[72:75], v[80:83], v[16:19], 0
ds_read_b128 v[80:83], v7
v_add_u32 v7, v7, v3
s_waitcnt lgkmcnt(4)
v_mfma_f32_16x16x32_bf16 v[72:75], v[84:87], v[20:23], v[72:75]
ds_read_b128 v[84:87], v7
v_add_u32 v7, v7, v3
s_waitcnt lgkmcnt(4)
v_mfma_f32_16x16x32_bf16 v[72:75], v[88:91], v[24:27], v[72:75]
ds_read_b128 v[88:91], v7
v_add_u32 v7, v7, v3
s_waitcnt lgkmcnt(4)
v_mfma_f32_16x16x32_bf16 v[72:75], v[92:95], v[28:31], v[72:75]
ds_read_b128 v[92:95], v7
v_add_u32 v7, v7, v3
s_waitcnt lgkmcnt(4)
v_mfma_f32_16x16x32_bf16 v[72:75], v[96:99], v[32:35], v[72:75]
ds_read_b128 v[96:99], v7
v_add_u32 v7, v7, v3
s_waitcnt lgkmcnt(4)
v_mfma_f32_16x16x32_bf16 v[76:79], v[80:83], v[16:19], 0
ds_read_b128 v[80:83], v7
v_add_u32 v7, v7, v3
s_waitcnt lgkmcnt(4)
v_mfma_f32_16x16x32_bf16 v[76:79], v[84:87], v[20:23], v[76:79]
ds_read_b128 v[84:87], v7
v_add_u32 v7, v7, v3
s_waitcnt lgkmcnt(4)
v_mfma_f32_16x16x32_bf16 v[76:79], v[88:91], v[24:27], v[76:79]
ds_read_b128 v[88:91], v7
v_add_u32 v7, v7, v3
s_waitcnt lgkmcnt(4)
v_mfma_f32_16x16x32_bf16 v[76:79], v[92:95], v[28:31], v[76:79]
ds_read_b128 v[92:95], v7
v_add_u32 v7, v7, v3
s_waitcnt lgkmcnt(4)
v_mfma_f32_16x16x32_bf16 v[76:79], v[96:99], v[32:35], v[76:79]
ds_read_b128 v[96:99], v7
v_add_u32 v7, v7, v3
s_nop 5
v_add_f32 v76, 0x358637bd, v76
v_rcp_f32 v76, v76
s_nop 0
v_pk_mul_f32 v[56:57], v[56:57], v[76:77] op_sel_hi:[1,0]
v_pk_mul_f32 v[58:59], v[58:59], v[76:77] op_sel_hi:[1,0]
v_pk_mul_f32 v[60:61], v[60:61], v[76:77] op_sel_hi:[1,0]
v_pk_mul_f32 v[62:63], v[62:63], v[76:77] op_sel_hi:[1,0]
v_pk_mul_f32 v[64:65], v[64:65], v[76:77] op_sel_hi:[1,0]
v_pk_mul_f32 v[66:67], v[66:67], v[76:77] op_sel_hi:[1,0]
v_pk_mul_f32 v[68:69], v[68:69], v[76:77] op_sel_hi:[1,0]
v_pk_mul_f32 v[70:71], v[70:71], v[76:77] op_sel_hi:[1,0]
v_pk_mul_f32 v[72:73], v[72:73], v[76:77] op_sel_hi:[1,0]
v_pk_mul_f32 v[74:75], v[74:75], v[76:77] op_sel_hi:[1,0]
v_cvt_pk_bf16_f32 v100, v56, v57
v_cvt_pk_bf16_f32 v101, v58, v59
v_cvt_pk_bf16_f32 v102, v60, v61
v_cvt_pk_bf16_f32 v103, v62, v63
v_cvt_pk_bf16_f32 v104, v64, v65
v_cvt_pk_bf16_f32 v105, v66, v67
v_cvt_pk_bf16_f32 v106, v68, v69
v_cvt_pk_bf16_f32 v107, v70, v71
v_cvt_pk_bf16_f32 v108, v72, v73
v_cvt_pk_bf16_f32 v109, v74, v75
s_waitcnt vmcnt(4) lgkmcnt(4)
v_mfma_f32_16x16x32_bf16 v[56:59], v[80:83], v[36:39], 0
ds_read_b128 v[80:83], v7
v_add_u32 v7, v7, v3
s_waitcnt vmcnt(3) lgkmcnt(4)
v_mfma_f32_16x16x32_bf16 v[56:59], v[84:87], v[40:43], v[56:59]
ds_read_b128 v[84:87], v7
v_add_u32 v7, v7, v3
s_waitcnt vmcnt(2) lgkmcnt(4)
v_mfma_f32_16x16x32_bf16 v[56:59], v[88:91], v[44:47], v[56:59]
ds_read_b128 v[88:91], v7
v_add_u32 v7, v7, v3
)"
R"(s_waitcnt vmcnt(1) lgkmcnt(4)
v_mfma_f32_16x16x32_bf16 v[56:59], v[92:95], v[48:51], v[56:59]
ds_read_b128 v[92:95], v7
v_add_u32 v7, v7, v3
s_waitcnt vmcnt(0) lgkmcnt(4)
v_mfma_f32_16x16x32_bf16 v[56:59], v[96:99], v[52:55], v[56:59]
ds_read_b128 v[96:99], v7
v_add_u32 v7, v7, v3
s_waitcnt lgkmcnt(4)
v_mfma_f32_16x16x32_bf16 v[60:63], v[80:83], v[36:39], 0
ds_read_b128 v[80:83], v7
v_add_u32 v7, v7, v3
s_waitcnt lgkmcnt(4)
v_mfma_f32_16x16x32_bf16 v[60:63], v[84:87], v[40:43], v[60:63]
ds_read_b128 v[84:87], v7
v_add_u32 v7, v7, v3
s_waitcnt lgkmcnt(4)
v_mfma_f32_16x16x32_bf16 v[60:63], v[88:91], v[44:47], v[60:63]
ds_read_b128 v[88:91], v7
v_add_u32 v7, v7, v3
s_waitcnt lgkmcnt(4)
v_mfma_f32_16x16x32_bf16 v[60:63], v[92:95], v[48:51], v[60:63]
ds_read_b128 v[92:95], v7
v_add_u32 v7, v7, v3
s_waitcnt lgkmcnt(4)
v_mfma_f32_16x16x32_bf16 v[60:63], v[96:99], v[52:55], v[60:63]
ds_read_b128 v[96:99], v7
v_add_u32 v7, v7, v3
s_waitcnt lgkmcnt(4)
v_mfma_f32_16x16x32_bf16 v[64:67], v[80:83], v[36:39], 0
ds_read_b128 v[80:83], v7
v_add_u32 v7, v7, v3
s_waitcnt lgkmcnt(4)
v_mfma_f32_16x16x32_bf16 v[64:67], v[84:87], v[40:43], v[64:67]
ds_read_b128 v[84:87], v7
v_add_u32 v7, v7, v3
s_waitcnt lgkmcnt(4)
v_mfma_f32_16x16x32_bf16 v[64:67], v[88:91], v[44:47], v[64:67]
ds_read_b128 v[88:91], v7
v_add_u32 v7, v7, v3
s_waitcnt lgkmcnt(4)
v_mfma_f32_16x16x32_bf16 v[64:67], v[92:95], v[48:51], v[64:67]
ds_read_b128 v[92:95], v7
v_add_u32 v7, v7, v3
s_waitcnt lgkmcnt(4)
v_mfma_f32_16x16x32_bf16 v[64:67], v[96:99], v[52:55], v[64:67]
ds_read_b128 v[96:99], v7
v_add_u32 v7, v7, v3
s_waitcnt lgkmcnt(4)
v_mfma_f32_16x16x32_bf16 v[68:71], v[80:83], v[36:39], 0
ds_read_b128 v[80:83], v7
v_add_u32 v7, v7, v3
s_waitcnt lgkmcnt(4)
v_mfma_f32_16x16x32_bf16 v[68:71], v[84:87], v[40:43], v[68:71]
ds_read_b128 v[84:87], v7
v_add_u32 v7, v7, v3
s_waitcnt lgkmcnt(4)
v_mfma_f32_16x16x32_bf16 v[68:71], v[88:91], v[44:47], v[68:71]
ds_read_b128 v[88:91], v7
v_add_u32 v7, v7, v3
s_waitcnt lgkmcnt(4)
v_mfma_f32_16x16x32_bf16 v[68:71], v[92:95], v[48:51], v[68:71]
ds_read_b128 v[92:95], v7
v_add_u32 v7, v7, v3
s_waitcnt lgkmcnt(4)
v_mfma_f32_16x16x32_bf16 v[68:71], v[96:99], v[52:55], v[68:71]
ds_read_b128 v[96:99], v7
v_add_u32 v7, v7, v3
s_waitcnt lgkmcnt(4)
v_mfma_f32_16x16x32_bf16 v[72:75], v[80:83], v[36:39], 0
ds_read_b128 v[80:83], v7
v_add_u32 v7, v7, v3
s_waitcnt lgkmcnt(4)
v_mfma_f32_16x16x32_bf16 v[72:75], v[84:87], v[40:43], v[72:75]
ds_read_b128 v[84:87], v7
v_add_u32 v7, v7, v3
s_waitcnt lgkmcnt(4)
v_mfma_f32_16x16x32_bf16 v[72:75], v[88:91], v[44:47], v[72:75]
ds_read_b128 v[88:91], v7
v_add_u32 v7, v7, v3
s_waitcnt lgkmcnt(4)
v_mfma_f32_16x16x32_bf16 v[72:75], v[92:95], v[48:51], v[72:75]
ds_read_b128 v[92:95], v7
v_add_u32 v7, v7, v3
s_waitcnt lgkmcnt(4)
v_mfma_f32_16x16x32_bf16 v[72:75], v[96:99], v[52:55], v[72:75]
ds_read_b128 v[96:99], v7
v_add_u32 v7, v7, v3
s_waitcnt lgkmcnt(4)
v_mfma_f32_16x16x32_bf16 v[76:79], v[80:83], v[36:39], 0
s_waitcnt lgkmcnt(3)
v_mfma_f32_16x16x32_bf16 v[76:79], v[84:87], v[40:43], v[76:79]
s_waitcnt lgkmcnt(2)
v_mfma_f32_16x16x32_bf16 v[76:79], v[88:91], v[44:47], v[76:79]
s_waitcnt lgkmcnt(1)
v_mfma_f32_16x16x32_bf16 v[76:79], v[92:95], v[48:51], v[76:79]
s_waitcnt lgkmcnt(0)
v_mfma_f32_16x16x32_bf16 v[76:79], v[96:99], v[52:55], v[76:79]
s_nop 7
v_add_f32 v76, 0x358637bd, v76
v_rcp_f32 v76, v76
s_nop 0
v_pk_mul_f32 v[56:57], v[56:57], v[76:77] op_sel_hi:[1,0]
v_pk_mul_f32 v[58:59], v[58:59], v[76:77] op_sel_hi:[1,0]
v_pk_mul_f32 v[60:61], v[60:61], v[76:77] op_sel_hi:[1,0]
v_pk_mul_f32 v[62:63], v[62:63], v[76:77] op_sel_hi:[1,0]
v_pk_mul_f32 v[64:65], v[64:65], v[76:77] op_sel_hi:[1,0]
v_pk_mul_f32 v[66:67], v[66:67], v[76:77] op_sel_hi:[1,0]
v_pk_mul_f32 v[68:69], v[68:69], v[76:77] op_sel_hi:[1,0]
v_pk_mul_f32 v[70:71], v[70:71], v[76:77] op_sel_hi:[1,0]
v_pk_mul_f32 v[72:73], v[72:73], v[76:77] op_sel_hi:[1,0]
v_pk_mul_f32 v[74:75], v[74:75], v[76:77] op_sel_hi:[1,0]
v_cvt_pk_bf16_f32 v110, v56, v57
v_cvt_pk_bf16_f32 v111, v58, v59
v_cvt_pk_bf16_f32 v112, v60, v61
v_cvt_pk_bf16_f32 v113, v62, v63
v_cvt_pk_bf16_f32 v114, v64, v65
v_cvt_pk_bf16_f32 v115, v66, v67
v_cvt_pk_bf16_f32 v116, v68, v69
v_cvt_pk_bf16_f32 v117, v70, v71
v_cvt_pk_bf16_f32 v118, v72, v73
v_cvt_pk_bf16_f32 v119, v74, v75
ds_write_b128 v9, v[100:103] offset:20480
ds_write_b128 v9, v[104:107] offset:21504
ds_write_b128 v9, v[108:111] offset:22528
ds_write_b128 v9, v[112:115] offset:23552
ds_write_b128 v9, v[116:119] offset:24576
s_mul_i32 s48, s30, 30720
s_add_u32 s48, s48, 61696
v_add_u32 v9, s48, v0
s_mul_i32 s48, s30, -10240
s_add_u32 s48, s48, 82176
v_add_u32 v7, s48, v0
v_add_u32 v6, s61, v0
s_waitcnt lgkmcnt(0)
s_barrier
s_cmp_eq_u32 s21, 0
s_cbranch_scc1 hpb2_mid_%=
ds_read_b128 v[96:99], v9 offset:0
ds_read_b128 v[100:103], v7 offset:0
ds_read_b128 v[104:107], v9 offset:1024
ds_read_b128 v[108:111], v7 offset:1024
ds_read_b128 v[112:115], v9 offset:2048
ds_read_b128 v[116:119], v7 offset:2048
s_waitcnt lgkmcnt(5)
v_mfma_f32_16x16x32_bf16 v[16:19], v[128:131], v[96:99], 0
s_waitcnt lgkmcnt(4)
v_mfma_f32_16x16x32_bf16 v[56:59], v[128:131], v[100:103], 0
ds_read_b128 v[120:123], v9 offset:3072
v_mfma_f32_16x16x32_bf16 v[20:23], v[132:135], v[96:99], 0
v_mfma_f32_16x16x32_bf16 v[60:63], v[132:135], v[100:103], 0
v_mfma_f32_16x16x32_bf16 v[24:27], v[136:139], v[96:99], 0
v_mfma_f32_16x16x32_bf16 v[64:67], v[136:139], v[100:103], 0
v_mfma_f32_16x16x32_bf16 v[28:31], v[140:143], v[96:99], 0
v_mfma_f32_16x16x32_bf16 v[68:71], v[140:143], v[100:103], 0
v_mfma_f32_16x16x32_bf16 v[32:35], v[144:147], v[96:99], 0
v_mfma_f32_16x16x32_bf16 v[72:75], v[144:147], v[100:103], 0
v_mfma_f32_16x16x32_bf16 v[36:39], v[148:151], v[96:99], 0
v_mfma_f32_16x16x32_bf16 v[76:79], v[148:151], v[100:103], 0
ds_read_b128 v[124:127], v7 offset:3072
v_mfma_f32_16x16x32_bf16 v[40:43], v[152:155], v[96:99], 0
v_mfma_f32_16x16x32_bf16 v[80:83], v[152:155], v[100:103], 0
v_mfma_f32_16x16x32_bf16 v[44:47], v[156:159], v[96:99], 0
v_mfma_f32_16x16x32_bf16 v[84:87], v[156:159], v[100:103], 0
v_mfma_f32_16x16x32_bf16 v[48:51], v[160:163], v[96:99], 0
v_mfma_f32_16x16x32_bf16 v[88:91], v[160:163], v[100:103], 0
v_mfma_f32_16x16x32_bf16 v[52:55], v[164:167], v[96:99], 0
v_mfma_f32_16x16x32_bf16 v[92:95], v[164:167], v[100:103], 0
s_waitcnt lgkmcnt(5)
v_mfma_f32_16x16x32_bf16 v[16:19], v[168:171], v[104:107], v[16:19]
s_waitcnt lgkmcnt(4)
v_mfma_f32_16x16x32_bf16 v[56:59], v[168:171], v[108:111], v[56:59]
ds_read_b128 v[96:99], v9 offset:4096
v_mfma_f32_16x16x32_bf16 v[20:23], v[172:175], v[104:107], v[20:23]
v_mfma_f32_16x16x32_bf16 v[60:63], v[172:175], v[108:111], v[60:63]
v_mfma_f32_16x16x32_bf16 v[24:27], v[176:179], v[104:107], v[24:27]
v_mfma_f32_16x16x32_bf16 v[64:67], v[176:179], v[108:111], v[64:67]
v_mfma_f32_16x16x32_bf16 v[28:31], v[180:183], v[104:107], v[28:31]
v_mfma_f32_16x16x32_bf16 v[68:71], v[180:183], v[108:111], v[68:71]
v_mfma_f32_16x16x32_bf16 v[32:35], v[184:187], v[104:107], v[32:35]
v_mfma_f32_16x16x32_bf16 v[72:75], v[184:187], v[108:111], v[72:75]
v_mfma_f32_16x16x32_bf16 v[36:39], v[188:191], v[104:107], v[36:39]
v_mfma_f32_16x16x32_bf16 v[76:79], v[188:191], v[108:111], v[76:79]
ds_read_b128 v[100:103], v7 offset:4096
v_mfma_f32_16x16x32_bf16 v[40:43], v[192:195], v[104:107], v[40:43]
v_mfma_f32_16x16x32_bf16 v[80:83], v[192:195], v[108:111], v[80:83]
v_mfma_f32_16x16x32_bf16 v[44:47], v[196:199], v[104:107], v[44:47]
v_mfma_f32_16x16x32_bf16 v[84:87], v[196:199], v[108:111], v[84:87]
v_mfma_f32_16x16x32_bf16 v[48:51], v[200:203], v[104:107], v[48:51]
v_mfma_f32_16x16x32_bf16 v[88:91], v[200:203], v[108:111], v[88:91]
v_mfma_f32_16x16x32_bf16 v[52:55], v[204:207], v[104:107], v[52:55]
v_mfma_f32_16x16x32_bf16 v[92:95], v[204:207], v[108:111], v[92:95]
s_waitcnt lgkmcnt(5)
v_mfma_f32_16x16x32_bf16 v[16:19], v[208:211], v[112:115], v[16:19]
s_waitcnt lgkmcnt(4)
v_mfma_f32_16x16x32_bf16 v[56:59], v[208:211], v[116:119], v[56:59]
ds_read_b128 v[104:107], v9 offset:5120
v_mfma_f32_16x16x32_bf16 v[20:23], v[212:215], v[112:115], v[20:23]
v_mfma_f32_16x16x32_bf16 v[60:63], v[212:215], v[116:119], v[60:63]
v_mfma_f32_16x16x32_bf16 v[24:27], v[216:219], v[112:115], v[24:27]
)"
R"(v_mfma_f32_16x16x32_bf16 v[64:67], v[216:219], v[116:119], v[64:67]
v_mfma_f32_16x16x32_bf16 v[28:31], v[220:223], v[112:115], v[28:31]
v_mfma_f32_16x16x32_bf16 v[68:71], v[220:223], v[116:119], v[68:71]
v_mfma_f32_16x16x32_bf16 v[32:35], v[224:227], v[112:115], v[32:35]
v_mfma_f32_16x16x32_bf16 v[72:75], v[224:227], v[116:119], v[72:75]
v_mfma_f32_16x16x32_bf16 v[36:39], v[228:231], v[112:115], v[36:39]
v_mfma_f32_16x16x32_bf16 v[76:79], v[228:231], v[116:119], v[76:79]
ds_read_b128 v[108:111], v7 offset:5120
v_mfma_f32_16x16x32_bf16 v[40:43], v[232:235], v[112:115], v[40:43]
v_mfma_f32_16x16x32_bf16 v[80:83], v[232:235], v[116:119], v[80:83]
v_mfma_f32_16x16x32_bf16 v[44:47], v[236:239], v[112:115], v[44:47]
v_mfma_f32_16x16x32_bf16 v[84:87], v[236:239], v[116:119], v[84:87]
v_mfma_f32_16x16x32_bf16 v[48:51], v[240:243], v[112:115], v[48:51]
v_mfma_f32_16x16x32_bf16 v[88:91], v[240:243], v[116:119], v[88:91]
v_mfma_f32_16x16x32_bf16 v[52:55], v[244:247], v[112:115], v[52:55]
v_mfma_f32_16x16x32_bf16 v[92:95], v[244:247], v[116:119], v[92:95]
s_waitcnt lgkmcnt(5)
v_mfma_f32_16x16x32_bf16 v[16:19], v[248:251], v[120:123], v[16:19]
s_waitcnt lgkmcnt(4)
v_mfma_f32_16x16x32_bf16 v[56:59], v[248:251], v[124:127], v[56:59]
ds_read_b128 v[112:115], v9 offset:6144
v_mfma_f32_16x16x32_bf16 v[20:23], v[252:255], v[120:123], v[20:23]
v_mfma_f32_16x16x32_bf16 v[60:63], v[252:255], v[124:127], v[60:63]
v_mfma_f32_16x16x32_bf16 v[24:27], a[0:3], v[120:123], v[24:27]
v_mfma_f32_16x16x32_bf16 v[64:67], a[0:3], v[124:127], v[64:67]
v_mfma_f32_16x16x32_bf16 v[28:31], a[4:7], v[120:123], v[28:31]
v_mfma_f32_16x16x32_bf16 v[68:71], a[4:7], v[124:127], v[68:71]
v_mfma_f32_16x16x32_bf16 v[32:35], a[8:11], v[120:123], v[32:35]
v_mfma_f32_16x16x32_bf16 v[72:75], a[8:11], v[124:127], v[72:75]
v_mfma_f32_16x16x32_bf16 v[36:39], a[12:15], v[120:123], v[36:39]
v_mfma_f32_16x16x32_bf16 v[76:79], a[12:15], v[124:127], v[76:79]
ds_read_b128 v[116:119], v7 offset:6144
v_mfma_f32_16x16x32_bf16 v[40:43], a[16:19], v[120:123], v[40:43]
v_mfma_f32_16x16x32_bf16 v[80:83], a[16:19], v[124:127], v[80:83]
v_mfma_f32_16x16x32_bf16 v[44:47], a[20:23], v[120:123], v[44:47]
v_mfma_f32_16x16x32_bf16 v[84:87], a[20:23], v[124:127], v[84:87]
v_mfma_f32_16x16x32_bf16 v[48:51], a[24:27], v[120:123], v[48:51]
v_mfma_f32_16x16x32_bf16 v[88:91], a[24:27], v[124:127], v[88:91]
v_mfma_f32_16x16x32_bf16 v[52:55], a[28:31], v[120:123], v[52:55]
v_mfma_f32_16x16x32_bf16 v[92:95], a[28:31], v[124:127], v[92:95]
s_waitcnt lgkmcnt(5)
v_mfma_f32_16x16x32_bf16 v[16:19], a[32:35], v[96:99], v[16:19]
s_waitcnt lgkmcnt(4)
v_mfma_f32_16x16x32_bf16 v[56:59], a[32:35], v[100:103], v[56:59]
ds_read_b128 v[120:123], v9 offset:7168
v_mfma_f32_16x16x32_bf16 v[20:23], a[36:39], v[96:99], v[20:23]
v_mfma_f32_16x16x32_bf16 v[60:63], a[36:39], v[100:103], v[60:63]
v_mfma_f32_16x16x32_bf16 v[24:27], a[40:43], v[96:99], v[24:27]
v_mfma_f32_16x16x32_bf16 v[64:67], a[40:43], v[100:103], v[64:67]
v_mfma_f32_16x16x32_bf16 v[28:31], a[44:47], v[96:99], v[28:31]
v_mfma_f32_16x16x32_bf16 v[68:71], a[44:47], v[100:103], v[68:71]
v_mfma_f32_16x16x32_bf16 v[32:35], a[48:51], v[96:99], v[32:35]
v_mfma_f32_16x16x32_bf16 v[72:75], a[48:51], v[100:103], v[72:75]
v_mfma_f32_16x16x32_bf16 v[36:39], a[52:55], v[96:99], v[36:39]
v_mfma_f32_16x16x32_bf16 v[76:79], a[52:55], v[100:103], v[76:79]
ds_read_b128 v[124:127], v7 offset:7168
v_mfma_f32_16x16x32_bf16 v[40:43], a[56:59], v[96:99], v[40:43]
v_mfma_f32_16x16x32_bf16 v[80:83], a[56:59], v[100:103], v[80:83]
v_mfma_f32_16x16x32_bf16 v[44:47], a[60:63], v[96:99], v[44:47]
v_mfma_f32_16x16x32_bf16 v[84:87], a[60:63], v[100:103], v[84:87]
v_mfma_f32_16x16x32_bf16 v[48:51], a[64:67], v[96:99], v[48:51]
v_mfma_f32_16x16x32_bf16 v[88:91], a[64:67], v[100:103], v[88:91]
v_mfma_f32_16x16x32_bf16 v[52:55], a[68:71], v[96:99], v[52:55]
v_mfma_f32_16x16x32_bf16 v[92:95], a[68:71], v[100:103], v[92:95]
s_waitcnt lgkmcnt(5)
v_mfma_f32_16x16x32_bf16 v[16:19], a[72:75], v[104:107], v[16:19]
s_waitcnt lgkmcnt(4)
v_mfma_f32_16x16x32_bf16 v[56:59], a[72:75], v[108:111], v[56:59]
ds_read_b128 v[96:99], v9 offset:8192
v_mfma_f32_16x16x32_bf16 v[20:23], a[76:79], v[104:107], v[20:23]
v_mfma_f32_16x16x32_bf16 v[60:63], a[76:79], v[108:111], v[60:63]
v_mfma_f32_16x16x32_bf16 v[24:27], a[80:83], v[104:107], v[24:27]
v_mfma_f32_16x16x32_bf16 v[64:67], a[80:83], v[108:111], v[64:67]
v_mfma_f32_16x16x32_bf16 v[28:31], a[84:87], v[104:107], v[28:31]
v_mfma_f32_16x16x32_bf16 v[68:71], a[84:87], v[108:111], v[68:71]
v_mfma_f32_16x16x32_bf16 v[32:35], a[88:91], v[104:107], v[32:35]
v_mfma_f32_16x16x32_bf16 v[72:75], a[88:91], v[108:111], v[72:75]
v_mfma_f32_16x16x32_bf16 v[36:39], a[92:95], v[104:107], v[36:39]
v_mfma_f32_16x16x32_bf16 v[76:79], a[92:95], v[108:111], v[76:79]
ds_read_b128 v[100:103], v7 offset:8192
v_mfma_f32_16x16x32_bf16 v[40:43], a[96:99], v[104:107], v[40:43]
v_mfma_f32_16x16x32_bf16 v[80:83], a[96:99], v[108:111], v[80:83]
ds_read_b128 v[224:227], v6 offset:0
v_mfma_f32_16x16x32_bf16 v[44:47], a[100:103], v[104:107], v[44:47]
v_mfma_f32_16x16x32_bf16 v[84:87], a[100:103], v[108:111], v[84:87]
ds_read_b128 v[228:231], v6 offset:1024
v_mfma_f32_16x16x32_bf16 v[48:51], a[104:107], v[104:107], v[48:51]
v_mfma_f32_16x16x32_bf16 v[88:91], a[104:107], v[108:111], v[88:91]
ds_read_b128 v[232:235], v6 offset:2048
v_mfma_f32_16x16x32_bf16 v[52:55], a[108:111], v[104:107], v[52:55]
v_mfma_f32_16x16x32_bf16 v[92:95], a[108:111], v[108:111], v[92:95]
ds_read_b128 v[236:239], v6 offset:3072
s_waitcnt lgkmcnt(9)
v_mfma_f32_16x16x32_bf16 v[16:19], a[112:115], v[112:115], v[16:19]
s_waitcnt lgkmcnt(8)
v_mfma_f32_16x16x32_bf16 v[56:59], a[112:115], v[116:119], v[56:59]
ds_read_b128 v[240:243], v6 offset:4096
ds_read_b128 v[104:107], v9 offset:9216
v_mfma_f32_16x16x32_bf16 v[20:23], a[116:119], v[112:115], v[20:23]
v_mfma_f32_16x16x32_bf16 v[60:63], a[116:119], v[116:119], v[60:63]
ds_read_b128 v[244:247], v6 offset:5120
v_mfma_f32_16x16x32_bf16 v[24:27], a[120:123], v[112:115], v[24:27]
v_mfma_f32_16x16x32_bf16 v[64:67], a[120:123], v[116:119], v[64:67]
ds_read_b128 v[248:251], v6 offset:6144
v_mfma_f32_16x16x32_bf16 v[28:31], a[124:127], v[112:115], v[28:31]
v_mfma_f32_16x16x32_bf16 v[68:71], a[124:127], v[116:119], v[68:71]
ds_read_b128 v[252:255], v6 offset:7168
v_mfma_f32_16x16x32_bf16 v[32:35], a[128:131], v[112:115], v[32:35]
v_mfma_f32_16x16x32_bf16 v[72:75], a[128:131], v[116:119], v[72:75]
ds_read_b128 v[128:131], v6 offset:8192
v_mfma_f32_16x16x32_bf16 v[36:39], a[132:135], v[112:115], v[36:39]
v_mfma_f32_16x16x32_bf16 v[76:79], a[132:135], v[116:119], v[76:79]
ds_read_b128 v[132:135], v6 offset:9216
ds_read_b128 v[108:111], v7 offset:9216
s_waitcnt lgkmcnt(14)
v_mfma_f32_16x16x32_bf16 v[40:43], a[136:139], v[112:115], v[40:43]
v_mfma_f32_16x16x32_bf16 v[80:83], a[136:139], v[116:119], v[80:83]
ds_read_b128 v[136:139], v6 offset:10240
v_mfma_f32_16x16x32_bf16 v[44:47], a[140:143], v[112:115], v[44:47]
v_mfma_f32_16x16x32_bf16 v[84:87], a[140:143], v[116:119], v[84:87]
ds_read_b128 v[140:143], v6 offset:11264
s_waitcnt lgkmcnt(14)
v_mfma_f32_16x16x32_bf16 v[48:51], a[144:147], v[112:115], v[48:51]
v_mfma_f32_16x16x32_bf16 v[88:91], a[144:147], v[116:119], v[88:91]
v_mfma_f32_16x16x32_bf16 v[52:55], a[148:151], v[112:115], v[52:55]
v_mfma_f32_16x16x32_bf16 v[92:95], a[148:151], v[116:119], v[92:95]
v_mfma_f32_16x16x32_bf16 v[16:19], a[152:155], v[120:123], v[16:19]
v_mfma_f32_16x16x32_bf16 v[56:59], a[152:155], v[124:127], v[56:59]
v_mfma_f32_16x16x32_bf16 v[20:23], a[156:159], v[120:123], v[20:23]
v_mfma_f32_16x16x32_bf16 v[60:63], a[156:159], v[124:127], v[60:63]
v_mfma_f32_16x16x32_bf16 v[24:27], a[160:163], v[120:123], v[24:27]
v_mfma_f32_16x16x32_bf16 v[64:67], a[160:163], v[124:127], v[64:67]
s_add_u32 s48, s34, 138346496
buffer_load_dwordx4 v[160:163], v8, s[40:43], s48 offen sc1
v_mfma_f32_16x16x32_bf16 v[28:31], a[164:167], v[120:123], v[28:31]
v_mfma_f32_16x16x32_bf16 v[68:71], a[164:167], v[124:127], v[68:71]
s_add_u32 s48, s34, 138346496
buffer_load_dwordx4 v[164:167], v8, s[40:43], s48 offen offset:512 sc1
v_mfma_f32_16x16x32_bf16 v[32:35], a[168:171], v[120:123], v[32:35]
v_mfma_f32_16x16x32_bf16 v[72:75], a[168:171], v[124:127], v[72:75]
s_add_u32 s48, s34, 138348544
buffer_load_dwordx4 v[168:171], v8, s[40:43], s48 offen sc1
v_mfma_f32_16x16x32_bf16 v[36:39], a[172:175], v[120:123], v[36:39]
v_mfma_f32_16x16x32_bf16 v[76:79], a[172:175], v[124:127], v[76:79]
s_add_u32 s48, s34, 138348544
buffer_load_dwordx4 v[172:175], v8, s[40:43], s48 offen offset:512 sc1
v_mfma_f32_16x16x32_bf16 v[40:43], a[176:179], v[120:123], v[40:43]
v_mfma_f32_16x16x32_bf16 v[80:83], a[176:179], v[124:127], v[80:83]
s_add_u32 s48, s34, 138350592
buffer_load_dwordx4 v[176:179], v8, s[40:43], s48 offen sc1
v_mfma_f32_16x16x32_bf16 v[44:47], a[180:183], v[120:123], v[44:47]
v_mfma_f32_16x16x32_bf16 v[84:87], a[180:183], v[124:127], v[84:87]
s_add_u32 s48, s34, 138350592
buffer_load_dwordx4 v[180:183], v8, s[40:43], s48 offen offset:512 sc1
v_mfma_f32_16x16x32_bf16 v[48:51], a[184:187], v[120:123], v[48:51]
v_mfma_f32_16x16x32_bf16 v[88:91], a[184:187], v[124:127], v[88:91]
s_add_u32 s48, s34, 138352640
buffer_load_dwordx4 v[184:187], v8, s[40:43], s48 offen sc1
v_mfma_f32_16x16x32_bf16 v[52:55], a[188:191], v[120:123], v[52:55]
v_mfma_f32_16x16x32_bf16 v[92:95], a[188:191], v[124:127], v[92:95]
s_add_u32 s48, s34, 138352640
buffer_load_dwordx4 v[188:191], v8, s[40:43], s48 offen offset:512 sc1
v_mfma_f32_16x16x32_bf16 v[16:19], a[192:195], v[96:99], v[16:19]
v_mfma_f32_16x16x32_bf16 v[56:59], a[192:195], v[100:103], v[56:59]
s_add_u32 s48, s34, 138354688
buffer_load_dwordx4 v[192:195], v8, s[40:43], s48 offen sc1
v_mfma_f32_16x16x32_bf16 v[20:23], a[196:199], v[96:99], v[20:23]
v_mfma_f32_16x16x32_bf16 v[60:63], a[196:199], v[100:103], v[60:63]
s_add_u32 s48, s34, 138354688
buffer_load_dwordx4 v[196:199], v8, s[40:43], s48 offen offset:512 sc1
v_mfma_f32_16x16x32_bf16 v[24:27], a[200:203], v[96:99], v[24:27]
v_mfma_f32_16x16x32_bf16 v[64:67], a[200:203], v[100:103], v[64:67]
v_mfma_f32_16x16x32_bf16 v[28:31], a[204:207], v[96:99], v[28:31]
v_mfma_f32_16x16x32_bf16 v[68:71], a[204:207], v[100:103], v[68:71]
v_mfma_f32_16x16x32_bf16 v[32:35], a[208:211], v[96:99], v[32:35]
v_mfma_f32_16x16x32_bf16 v[72:75], a[208:211], v[100:103], v[72:75]
v_mfma_f32_16x16x32_bf16 v[36:39], a[212:215], v[96:99], v[36:39]
v_mfma_f32_16x16x32_bf16 v[76:79], a[212:215], v[100:103], v[76:79]
v_mfma_f32_16x16x32_bf16 v[40:43], a[216:219], v[96:99], v[40:43]
v_mfma_f32_16x16x32_bf16 v[80:83], a[216:219], v[100:103], v[80:83]
v_mfma_f32_16x16x32_bf16 v[44:47], a[220:223], v[96:99], v[44:47]
v_mfma_f32_16x16x32_bf16 v[84:87], a[220:223], v[100:103], v[84:87]
s_waitcnt lgkmcnt(13)
v_mfma_f32_16x16x32_bf16 v[48:51], v[224:227], v[96:99], v[48:51]
v_mfma_f32_16x16x32_bf16 v[88:91], v[224:227], v[100:103], v[88:91]
s_waitcnt lgkmcnt(12)
v_mfma_f32_16x16x32_bf16 v[52:55], v[228:231], v[96:99], v[52:55]
v_mfma_f32_16x16x32_bf16 v[92:95], v[228:231], v[100:103], v[92:95]
s_waitcnt lgkmcnt(8)
v_mfma_f32_16x16x32_bf16 v[16:19], v[232:235], v[104:107], v[16:19]
s_waitcnt lgkmcnt(2)
v_mfma_f32_16x16x32_bf16 v[56:59], v[232:235], v[108:111], v[56:59]
v_mfma_f32_16x16x32_bf16 v[20:23], v[236:239], v[104:107], v[20:23]
v_mfma_f32_16x16x32_bf16 v[60:63], v[236:239], v[108:111], v[60:63]
v_mfma_f32_16x16x32_bf16 v[24:27], v[240:243], v[104:107], v[24:27]
v_mfma_f32_16x16x32_bf16 v[64:67], v[240:243], v[108:111], v[64:67]
v_mfma_f32_16x16x32_bf16 v[28:31], v[244:247], v[104:107], v[28:31]
v_mfma_f32_16x16x32_bf16 v[68:71], v[244:247], v[108:111], v[68:71]
v_mfma_f32_16x16x32_bf16 v[32:35], v[248:251], v[104:107], v[32:35]
v_mfma_f32_16x16x32_bf16 v[72:75], v[248:251], v[108:111], v[72:75]
)"
R"(v_mfma_f32_16x16x32_bf16 v[36:39], v[252:255], v[104:107], v[36:39]
v_mfma_f32_16x16x32_bf16 v[76:79], v[252:255], v[108:111], v[76:79]
v_mfma_f32_16x16x32_bf16 v[40:43], v[128:131], v[104:107], v[40:43]
v_mfma_f32_16x16x32_bf16 v[80:83], v[128:131], v[108:111], v[80:83]
v_mfma_f32_16x16x32_bf16 v[44:47], v[132:135], v[104:107], v[44:47]
v_mfma_f32_16x16x32_bf16 v[84:87], v[132:135], v[108:111], v[84:87]
s_waitcnt lgkmcnt(1)
v_mfma_f32_16x16x32_bf16 v[48:51], v[136:139], v[104:107], v[48:51]
v_mfma_f32_16x16x32_bf16 v[88:91], v[136:139], v[108:111], v[88:91]
s_waitcnt lgkmcnt(0)
v_mfma_f32_16x16x32_bf16 v[52:55], v[140:143], v[104:107], v[52:55]
v_mfma_f32_16x16x32_bf16 v[92:95], v[140:143], v[108:111], v[92:95]
s_mul_i32 s48, s45, 10240
v_add_u32 v6, s48, v0
s_xor_b32 s48, s45, 1
s_mul_i32 s48, s48, 10240
v_add_u32 v7, s48, v0
ds_write_b128 v6, v[56:59] offset:0
ds_write_b128 v6, v[60:63] offset:1024
ds_write_b128 v6, v[64:67] offset:2048
ds_write_b128 v6, v[68:71] offset:3072
ds_write_b128 v6, v[72:75] offset:4096
ds_write_b128 v6, v[76:79] offset:5120
ds_write_b128 v6, v[80:83] offset:6144
ds_write_b128 v6, v[84:87] offset:7168
ds_write_b128 v6, v[88:91] offset:8192
ds_write_b128 v6, v[92:95] offset:9216
s_waitcnt vmcnt(9)
v_pk_add_f32 v[160:161], v[160:161], v[16:17]
v_pk_add_f32 v[162:163], v[162:163], v[20:21]
s_waitcnt vmcnt(8)
v_pk_add_f32 v[164:165], v[164:165], v[18:19]
v_pk_add_f32 v[166:167], v[166:167], v[22:23]
s_waitcnt vmcnt(7)
v_pk_add_f32 v[168:169], v[168:169], v[24:25]
v_pk_add_f32 v[170:171], v[170:171], v[28:29]
s_waitcnt vmcnt(6)
v_pk_add_f32 v[172:173], v[172:173], v[26:27]
v_pk_add_f32 v[174:175], v[174:175], v[30:31]
s_waitcnt vmcnt(5)
v_pk_add_f32 v[176:177], v[176:177], v[32:33]
v_pk_add_f32 v[178:179], v[178:179], v[36:37]
s_waitcnt vmcnt(4)
v_pk_add_f32 v[180:181], v[180:181], v[34:35]
v_pk_add_f32 v[182:183], v[182:183], v[38:39]
s_waitcnt vmcnt(3)
v_pk_add_f32 v[184:185], v[184:185], v[40:41]
v_pk_add_f32 v[186:187], v[186:187], v[44:45]
s_waitcnt vmcnt(2)
v_pk_add_f32 v[188:189], v[188:189], v[42:43]
v_pk_add_f32 v[190:191], v[190:191], v[46:47]
s_waitcnt vmcnt(1)
v_pk_add_f32 v[192:193], v[192:193], v[48:49]
v_pk_add_f32 v[194:195], v[194:195], v[52:53]
s_waitcnt vmcnt(0)
v_pk_add_f32 v[196:197], v[196:197], v[50:51]
v_pk_add_f32 v[198:199], v[198:199], v[54:55]
s_waitcnt lgkmcnt(0)
s_barrier
ds_read_b128 v[16:19], v7 offset:0
ds_read_b128 v[20:23], v7 offset:1024
ds_read_b128 v[24:27], v7 offset:2048
ds_read_b128 v[28:31], v7 offset:3072
ds_read_b128 v[32:35], v7 offset:4096
ds_read_b128 v[36:39], v7 offset:5120
ds_read_b128 v[40:43], v7 offset:6144
ds_read_b128 v[44:47], v7 offset:7168
ds_read_b128 v[48:51], v7 offset:8192
ds_read_b128 v[52:55], v7 offset:9216
s_waitcnt lgkmcnt(9)
v_pk_add_f32 v[160:161], v[160:161], v[16:17]
s_waitcnt lgkmcnt(8)
v_pk_add_f32 v[162:163], v[162:163], v[20:21]
v_pk_add_f32 v[164:165], v[164:165], v[18:19]
v_pk_add_f32 v[166:167], v[166:167], v[22:23]
s_waitcnt lgkmcnt(7)
v_pk_add_f32 v[168:169], v[168:169], v[24:25]
s_waitcnt lgkmcnt(6)
v_pk_add_f32 v[170:171], v[170:171], v[28:29]
v_pk_add_f32 v[172:173], v[172:173], v[26:27]
v_pk_add_f32 v[174:175], v[174:175], v[30:31]
s_waitcnt lgkmcnt(5)
v_pk_add_f32 v[176:177], v[176:177], v[32:33]
s_waitcnt lgkmcnt(4)
v_pk_add_f32 v[178:179], v[178:179], v[36:37]
v_pk_add_f32 v[180:181], v[180:181], v[34:35]
v_pk_add_f32 v[182:183], v[182:183], v[38:39]
s_waitcnt lgkmcnt(3)
v_pk_add_f32 v[184:185], v[184:185], v[40:41]
s_waitcnt lgkmcnt(2)
v_pk_add_f32 v[186:187], v[186:187], v[44:45]
v_pk_add_f32 v[188:189], v[188:189], v[42:43]
v_pk_add_f32 v[190:191], v[190:191], v[46:47]
s_waitcnt lgkmcnt(1)
v_pk_add_f32 v[192:193], v[192:193], v[48:49]
s_waitcnt lgkmcnt(0)
v_pk_add_f32 v[194:195], v[194:195], v[52:53]
v_pk_add_f32 v[196:197], v[196:197], v[50:51]
v_pk_add_f32 v[198:199], v[198:199], v[54:55]
s_mov_b32 s36, s66
s_mov_b32 s37, s67
s_mov_b32 s38, 2621440
s_mov_b32 s39, 0x20000
v_and_b32 v4, 15, v15
v_mul_u32_u24 v4, 1280, v4
v_and_b32 v5, 48, v15
v_add_u32 v4, v4, v5
s_lshl1_add_u32 s48, s26, s30
s_mul_i32 s48, s48, 20480
s_mul_i32 s49, s31, 320
s_add_u32 s48, s48, s49
v_cvt_pk_bf16_f32 v16, v160, v161
v_cvt_pk_bf16_f32 v17, v162, v163
v_cvt_pk_bf16_f32 v18, v164, v165
v_cvt_pk_bf16_f32 v19, v166, v167
v_cvt_pk_bf16_f32 v20, v168, v169
v_cvt_pk_bf16_f32 v21, v170, v171
v_cvt_pk_bf16_f32 v22, v172, v173
v_cvt_pk_bf16_f32 v23, v174, v175
v_cvt_pk_bf16_f32 v24, v176, v177
v_cvt_pk_bf16_f32 v25, v178, v179
v_cvt_pk_bf16_f32 v26, v180, v181
v_cvt_pk_bf16_f32 v27, v182, v183
v_cvt_pk_bf16_f32 v28, v184, v185
v_cvt_pk_bf16_f32 v29, v186, v187
v_cvt_pk_bf16_f32 v30, v188, v189
v_cvt_pk_bf16_f32 v31, v190, v191
v_cvt_pk_bf16_f32 v32, v192, v193
v_cvt_pk_bf16_f32 v33, v194, v195
v_cvt_pk_bf16_f32 v34, v196, v197
v_cvt_pk_bf16_f32 v35, v198, v199
v_permlane16_swap_b32 v16, v18
v_permlane16_swap_b32 v17, v19
buffer_store_dwordx4 v[16:19], v4, s[36:39], s48 offen offset:0
v_permlane16_swap_b32 v20, v22
v_permlane16_swap_b32 v21, v23
buffer_store_dwordx4 v[20:23], v4, s[36:39], s48 offen offset:64
v_permlane16_swap_b32 v24, v26
v_permlane16_swap_b32 v25, v27
buffer_store_dwordx4 v[24:27], v4, s[36:39], s48 offen offset:128
v_permlane16_swap_b32 v28, v30
v_permlane16_swap_b32 v29, v31
buffer_store_dwordx4 v[28:31], v4, s[36:39], s48 offen offset:192
v_permlane16_swap_b32 v32, v34
v_permlane16_swap_b32 v33, v35
buffer_store_dwordx4 v[32:35], v4, s[36:39], s48 offen offset:256
s_mov_b32 s54, 0x40000000
s_nop 0
s_branch hp_done_%=
hpb2_mid_%=:
ds_read_b128 v[96:99], v9 offset:0
ds_read_b128 v[100:103], v7 offset:0
ds_read_b128 v[104:107], v9 offset:1024
ds_read_b128 v[108:111], v7 offset:1024
ds_read_b128 v[112:115], v9 offset:2048
ds_read_b128 v[116:119], v7 offset:2048
s_waitcnt lgkmcnt(5)
v_mfma_f32_16x16x32_bf16 v[16:19], v[128:131], v[96:99], 0
s_waitcnt lgkmcnt(4)
v_mfma_f32_16x16x32_bf16 v[56:59], v[128:131], v[100:103], 0
s_add_u32 s50, s50, 4096
buffer_load_dwordx4 v[128:131], v0, s[40:43], s50 offen
ds_read_b128 v[120:123], v9 offset:3072
v_mfma_f32_16x16x32_bf16 v[20:23], v[132:135], v[96:99], 0
v_mfma_f32_16x16x32_bf16 v[60:63], v[132:135], v[100:103], 0
buffer_load_dwordx4 v[132:135], v0, s[40:43], s50 offen offset:1024
v_mfma_f32_16x16x32_bf16 v[24:27], v[136:139], v[96:99], 0
v_mfma_f32_16x16x32_bf16 v[64:67], v[136:139], v[100:103], 0
buffer_load_dwordx4 v[136:139], v0, s[40:43], s50 offen offset:2048
v_mfma_f32_16x16x32_bf16 v[28:31], v[140:143], v[96:99], 0
v_mfma_f32_16x16x32_bf16 v[68:71], v[140:143], v[100:103], 0
buffer_load_dwordx4 v[140:143], v0, s[40:43], s50 offen offset:3072
v_mfma_f32_16x16x32_bf16 v[32:35], v[144:147], v[96:99], 0
v_mfma_f32_16x16x32_bf16 v[72:75], v[144:147], v[100:103], 0
s_add_u32 s50, s50, 4096
buffer_load_dwordx4 v[144:147], v0, s[40:43], s50 offen
v_mfma_f32_16x16x32_bf16 v[36:39], v[148:151], v[96:99], 0
v_mfma_f32_16x16x32_bf16 v[76:79], v[148:151], v[100:103], 0
buffer_load_dwordx4 v[148:151], v0, s[40:43], s50 offen offset:1024
ds_read_b128 v[124:127], v7 offset:3072
v_mfma_f32_16x16x32_bf16 v[40:43], v[152:155], v[96:99], 0
v_mfma_f32_16x16x32_bf16 v[80:83], v[152:155], v[100:103], 0
buffer_load_dwordx4 v[152:155], v0, s[40:43], s50 offen offset:2048
v_mfma_f32_16x16x32_bf16 v[44:47], v[156:159], v[96:99], 0
v_mfma_f32_16x16x32_bf16 v[84:87], v[156:159], v[100:103], 0
buffer_load_dwordx4 v[156:159], v0, s[40:43], s50 offen offset:3072
v_mfma_f32_16x16x32_bf16 v[48:51], v[160:163], v[96:99], 0
v_mfma_f32_16x16x32_bf16 v[88:91], v[160:163], v[100:103], 0
s_add_u32 s50, s50, 4096
buffer_load_dwordx4 v[160:163], v0, s[40:43], s50 offen
v_mfma_f32_16x16x32_bf16 v[52:55], v[164:167], v[96:99], 0
v_mfma_f32_16x16x32_bf16 v[92:95], v[164:167], v[100:103], 0
buffer_load_dwordx4 v[164:167], v0, s[40:43], s50 offen offset:1024
s_waitcnt lgkmcnt(5)
v_mfma_f32_16x16x32_bf16 v[16:19], v[168:171], v[104:107], v[16:19]
s_waitcnt lgkmcnt(4)
v_mfma_f32_16x16x32_bf16 v[56:59], v[168:171], v[108:111], v[56:59]
buffer_load_dwordx4 v[168:171], v0, s[40:43], s50 offen offset:2048
ds_read_b128 v[96:99], v9 offset:4096
v_mfma_f32_16x16x32_bf16 v[20:23], v[172:175], v[104:107], v[20:23]
)"
R"(v_mfma_f32_16x16x32_bf16 v[60:63], v[172:175], v[108:111], v[60:63]
buffer_load_dwordx4 v[172:175], v0, s[40:43], s50 offen offset:3072
v_mfma_f32_16x16x32_bf16 v[24:27], v[176:179], v[104:107], v[24:27]
v_mfma_f32_16x16x32_bf16 v[64:67], v[176:179], v[108:111], v[64:67]
s_add_u32 s50, s50, 4096
buffer_load_dwordx4 v[176:179], v0, s[40:43], s50 offen
v_mfma_f32_16x16x32_bf16 v[28:31], v[180:183], v[104:107], v[28:31]
v_mfma_f32_16x16x32_bf16 v[68:71], v[180:183], v[108:111], v[68:71]
buffer_load_dwordx4 v[180:183], v0, s[40:43], s50 offen offset:1024
v_mfma_f32_16x16x32_bf16 v[32:35], v[184:187], v[104:107], v[32:35]
v_mfma_f32_16x16x32_bf16 v[72:75], v[184:187], v[108:111], v[72:75]
buffer_load_dwordx4 v[184:187], v0, s[40:43], s50 offen offset:2048
v_mfma_f32_16x16x32_bf16 v[36:39], v[188:191], v[104:107], v[36:39]
v_mfma_f32_16x16x32_bf16 v[76:79], v[188:191], v[108:111], v[76:79]
buffer_load_dwordx4 v[188:191], v0, s[40:43], s50 offen offset:3072
ds_read_b128 v[100:103], v7 offset:4096
v_mfma_f32_16x16x32_bf16 v[40:43], v[192:195], v[104:107], v[40:43]
v_mfma_f32_16x16x32_bf16 v[80:83], v[192:195], v[108:111], v[80:83]
s_add_u32 s50, s50, 4096
buffer_load_dwordx4 v[192:195], v0, s[40:43], s50 offen
v_mfma_f32_16x16x32_bf16 v[44:47], v[196:199], v[104:107], v[44:47]
v_mfma_f32_16x16x32_bf16 v[84:87], v[196:199], v[108:111], v[84:87]
buffer_load_dwordx4 v[196:199], v0, s[40:43], s50 offen offset:1024
v_mfma_f32_16x16x32_bf16 v[48:51], v[200:203], v[104:107], v[48:51]
v_mfma_f32_16x16x32_bf16 v[88:91], v[200:203], v[108:111], v[88:91]
buffer_load_dwordx4 v[200:203], v0, s[40:43], s50 offen offset:2048
v_mfma_f32_16x16x32_bf16 v[52:55], v[204:207], v[104:107], v[52:55]
v_mfma_f32_16x16x32_bf16 v[92:95], v[204:207], v[108:111], v[92:95]
buffer_load_dwordx4 v[204:207], v0, s[40:43], s50 offen offset:3072
s_waitcnt lgkmcnt(5)
v_mfma_f32_16x16x32_bf16 v[16:19], v[208:211], v[112:115], v[16:19]
s_waitcnt lgkmcnt(4)
v_mfma_f32_16x16x32_bf16 v[56:59], v[208:211], v[116:119], v[56:59]
s_add_u32 s50, s50, 4096
buffer_load_dwordx4 v[208:211], v0, s[40:43], s50 offen
ds_read_b128 v[104:107], v9 offset:5120
v_mfma_f32_16x16x32_bf16 v[20:23], v[212:215], v[112:115], v[20:23]
v_mfma_f32_16x16x32_bf16 v[60:63], v[212:215], v[116:119], v[60:63]
buffer_load_dwordx4 v[212:215], v0, s[40:43], s50 offen offset:1024
v_mfma_f32_16x16x32_bf16 v[24:27], v[216:219], v[112:115], v[24:27]
v_mfma_f32_16x16x32_bf16 v[64:67], v[216:219], v[116:119], v[64:67]
buffer_load_dwordx4 v[216:219], v0, s[40:43], s50 offen offset:2048
v_mfma_f32_16x16x32_bf16 v[28:31], v[220:223], v[112:115], v[28:31]
v_mfma_f32_16x16x32_bf16 v[68:71], v[220:223], v[116:119], v[68:71]
buffer_load_dwordx4 v[220:223], v0, s[40:43], s50 offen offset:3072
v_mfma_f32_16x16x32_bf16 v[32:35], v[224:227], v[112:115], v[32:35]
v_mfma_f32_16x16x32_bf16 v[72:75], v[224:227], v[116:119], v[72:75]
s_add_u32 s50, s50, 4096
buffer_load_dwordx4 v[224:227], v0, s[40:43], s50 offen
v_mfma_f32_16x16x32_bf16 v[36:39], v[228:231], v[112:115], v[36:39]
v_mfma_f32_16x16x32_bf16 v[76:79], v[228:231], v[116:119], v[76:79]
buffer_load_dwordx4 v[228:231], v0, s[40:43], s50 offen offset:1024
ds_read_b128 v[108:111], v7 offset:5120
v_mfma_f32_16x16x32_bf16 v[40:43], v[232:235], v[112:115], v[40:43]
v_mfma_f32_16x16x32_bf16 v[80:83], v[232:235], v[116:119], v[80:83]
buffer_load_dwordx4 v[232:235], v0, s[40:43], s50 offen offset:2048
v_mfma_f32_16x16x32_bf16 v[44:47], v[236:239], v[112:115], v[44:47]
v_mfma_f32_16x16x32_bf16 v[84:87], v[236:239], v[116:119], v[84:87]
buffer_load_dwordx4 v[236:239], v0, s[40:43], s50 offen offset:3072
v_mfma_f32_16x16x32_bf16 v[48:51], v[240:243], v[112:115], v[48:51]
v_mfma_f32_16x16x32_bf16 v[88:91], v[240:243], v[116:119], v[88:91]
s_add_u32 s50, s50, 4096
buffer_load_dwordx4 v[240:243], v0, s[40:43], s50 offen
v_mfma_f32_16x16x32_bf16 v[52:55], v[244:247], v[112:115], v[52:55]
v_mfma_f32_16x16x32_bf16 v[92:95], v[244:247], v[116:119], v[92:95]
buffer_load_dwordx4 v[244:247], v0, s[40:43], s50 offen offset:1024
s_waitcnt lgkmcnt(5)
v_mfma_f32_16x16x32_bf16 v[16:19], v[248:251], v[120:123], v[16:19]
s_waitcnt lgkmcnt(4)
v_mfma_f32_16x16x32_bf16 v[56:59], v[248:251], v[124:127], v[56:59]
buffer_load_dwordx4 v[248:251], v0, s[40:43], s50 offen offset:2048
ds_read_b128 v[112:115], v9 offset:6144
v_mfma_f32_16x16x32_bf16 v[20:23], v[252:255], v[120:123], v[20:23]
v_mfma_f32_16x16x32_bf16 v[60:63], v[252:255], v[124:127], v[60:63]
buffer_load_dwordx4 v[252:255], v0, s[40:43], s50 offen offset:3072
s_waitcnt vmcnt(31)
v_mfma_f32_16x16x32_bf16 v[24:27], v[128:131], v[120:123], v[24:27]
v_mfma_f32_16x16x32_bf16 v[64:67], v[128:131], v[124:127], v[64:67]
s_add_u32 s50, s50, 4096
buffer_load_dwordx4 v[128:131], v0, s[40:43], s50 offen
s_waitcnt vmcnt(31)
v_mfma_f32_16x16x32_bf16 v[28:31], v[132:135], v[120:123], v[28:31]
v_mfma_f32_16x16x32_bf16 v[68:71], v[132:135], v[124:127], v[68:71]
buffer_load_dwordx4 v[132:135], v0, s[40:43], s50 offen offset:1024
s_waitcnt vmcnt(31)
v_mfma_f32_16x16x32_bf16 v[32:35], v[136:139], v[120:123], v[32:35]
v_mfma_f32_16x16x32_bf16 v[72:75], v[136:139], v[124:127], v[72:75]
buffer_load_dwordx4 v[136:139], v0, s[40:43], s50 offen offset:2048
s_waitcnt vmcnt(31)
v_mfma_f32_16x16x32_bf16 v[36:39], v[140:143], v[120:123], v[36:39]
v_mfma_f32_16x16x32_bf16 v[76:79], v[140:143], v[124:127], v[76:79]
buffer_load_dwordx4 v[140:143], v0, s[40:43], s50 offen offset:3072
ds_read_b128 v[116:119], v7 offset:6144
s_waitcnt vmcnt(31)
v_mfma_f32_16x16x32_bf16 v[40:43], v[144:147], v[120:123], v[40:43]
v_mfma_f32_16x16x32_bf16 v[80:83], v[144:147], v[124:127], v[80:83]
s_add_u32 s50, s50, 4096
buffer_load_dwordx4 v[144:147], v0, s[40:43], s50 offen
s_waitcnt vmcnt(31)
v_mfma_f32_16x16x32_bf16 v[44:47], v[148:151], v[120:123], v[44:47]
v_mfma_f32_16x16x32_bf16 v[84:87], v[148:151], v[124:127], v[84:87]
buffer_load_dwordx4 v[148:151], v0, s[40:43], s50 offen offset:1024
s_waitcnt vmcnt(31)
v_mfma_f32_16x16x32_bf16 v[48:51], v[152:155], v[120:123], v[48:51]
v_mfma_f32_16x16x32_bf16 v[88:91], v[152:155], v[124:127], v[88:91]
buffer_load_dwordx4 v[152:155], v0, s[40:43], s50 offen offset:2048
s_waitcnt vmcnt(31)
v_mfma_f32_16x16x32_bf16 v[52:55], v[156:159], v[120:123], v[52:55]
v_mfma_f32_16x16x32_bf16 v[92:95], v[156:159], v[124:127], v[92:95]
buffer_load_dwordx4 v[156:159], v0, s[40:43], s50 offen offset:3072
s_waitcnt vmcnt(31) lgkmcnt(5)
v_mfma_f32_16x16x32_bf16 v[16:19], v[160:163], v[96:99], v[16:19]
s_waitcnt lgkmcnt(4)
v_mfma_f32_16x16x32_bf16 v[56:59], v[160:163], v[100:103], v[56:59]
s_add_u32 s50, s50, 4096
buffer_load_dwordx4 v[160:163], v0, s[40:43], s50 offen
ds_read_b128 v[120:123], v9 offset:7168
s_waitcnt vmcnt(31)
v_mfma_f32_16x16x32_bf16 v[20:23], v[164:167], v[96:99], v[20:23]
v_mfma_f32_16x16x32_bf16 v[60:63], v[164:167], v[100:103], v[60:63]
buffer_load_dwordx4 v[164:167], v0, s[40:43], s50 offen offset:1024
s_waitcnt vmcnt(31)
v_mfma_f32_16x16x32_bf16 v[24:27], v[168:171], v[96:99], v[24:27]
v_mfma_f32_16x16x32_bf16 v[64:67], v[168:171], v[100:103], v[64:67]
buffer_load_dwordx4 v[168:171], v0, s[40:43], s50 offen offset:2048
s_waitcnt vmcnt(31)
v_mfma_f32_16x16x32_bf16 v[28:31], v[172:175], v[96:99], v[28:31]
v_mfma_f32_16x16x32_bf16 v[68:71], v[172:175], v[100:103], v[68:71]
buffer_load_dwordx4 v[172:175], v0, s[40:43], s50 offen offset:3072
s_waitcnt vmcnt(31)
v_mfma_f32_16x16x32_bf16 v[32:35], v[176:179], v[96:99], v[32:35]
v_mfma_f32_16x16x32_bf16 v[72:75], v[176:179], v[100:103], v[72:75]
s_add_u32 s50, s50, 4096
buffer_load_dwordx4 v[176:179], v0, s[40:43], s50 offen
s_waitcnt vmcnt(31)
v_mfma_f32_16x16x32_bf16 v[36:39], v[180:183], v[96:99], v[36:39]
v_mfma_f32_16x16x32_bf16 v[76:79], v[180:183], v[100:103], v[76:79]
buffer_load_dwordx4 v[180:183], v0, s[40:43], s50 offen offset:1024
ds_read_b128 v[124:127], v7 offset:7168
s_waitcnt vmcnt(31)
v_mfma_f32_16x16x32_bf16 v[40:43], v[184:187], v[96:99], v[40:43]
v_mfma_f32_16x16x32_bf16 v[80:83], v[184:187], v[100:103], v[80:83]
buffer_load_dwordx4 v[184:187], v0, s[40:43], s50 offen offset:2048
s_waitcnt vmcnt(31)
v_mfma_f32_16x16x32_bf16 v[44:47], v[188:191], v[96:99], v[44:47]
v_mfma_f32_16x16x32_bf16 v[84:87], v[188:191], v[100:103], v[84:87]
buffer_load_dwordx4 v[188:191], v0, s[40:43], s50 offen offset:3072
s_waitcnt vmcnt(31)
v_mfma_f32_16x16x32_bf16 v[48:51], v[192:195], v[96:99], v[48:51]
v_mfma_f32_16x16x32_bf16 v[88:91], v[192:195], v[100:103], v[88:91]
s_add_u32 s50, s50, 4096
buffer_load_dwordx4 v[192:195], v0, s[40:43], s50 offen
s_waitcnt vmcnt(31)
v_mfma_f32_16x16x32_bf16 v[52:55], v[196:199], v[96:99], v[52:55]
v_mfma_f32_16x16x32_bf16 v[92:95], v[196:199], v[100:103], v[92:95]
buffer_load_dwordx4 v[196:199], v0, s[40:43], s50 offen offset:1024
s_waitcnt vmcnt(31) lgkmcnt(5)
v_mfma_f32_16x16x32_bf16 v[16:19], v[200:203], v[104:107], v[16:19]
s_waitcnt lgkmcnt(4)
v_mfma_f32_16x16x32_bf16 v[56:59], v[200:203], v[108:111], v[56:59]
buffer_load_dwordx4 v[200:203], v0, s[40:43], s50 offen offset:2048
ds_read_b128 v[96:99], v9 offset:8192
s_waitcnt vmcnt(31)
v_mfma_f32_16x16x32_bf16 v[20:23], v[204:207], v[104:107], v[20:23]
v_mfma_f32_16x16x32_bf16 v[60:63], v[204:207], v[108:111], v[60:63]
buffer_load_dwordx4 v[204:207], v0, s[40:43], s50 offen offset:3072
s_waitcnt vmcnt(31)
v_mfma_f32_16x16x32_bf16 v[24:27], v[208:211], v[104:107], v[24:27]
v_mfma_f32_16x16x32_bf16 v[64:67], v[208:211], v[108:111], v[64:67]
s_add_u32 s50, s50, 4096
buffer_load_dwordx4 v[208:211], v0, s[40:43], s50 offen
s_waitcnt vmcnt(31)
v_mfma_f32_16x16x32_bf16 v[28:31], v[212:215], v[104:107], v[28:31]
v_mfma_f32_16x16x32_bf16 v[68:71], v[212:215], v[108:111], v[68:71]
buffer_load_dwordx4 v[212:215], v0, s[40:43], s50 offen offset:1024
s_waitcnt vmcnt(31)
v_mfma_f32_16x16x32_bf16 v[32:35], v[216:219], v[104:107], v[32:35]
v_mfma_f32_16x16x32_bf16 v[72:75], v[216:219], v[108:111], v[72:75]
buffer_load_dwordx4 v[216:219], v0, s[40:43], s50 offen offset:2048
s_waitcnt vmcnt(31)
v_mfma_f32_16x16x32_bf16 v[36:39], v[220:223], v[104:107], v[36:39]
v_mfma_f32_16x16x32_bf16 v[76:79], v[220:223], v[108:111], v[76:79]
buffer_load_dwordx4 v[220:223], v0, s[40:43], s50 offen offset:3072
ds_read_b128 v[100:103], v7 offset:8192
s_waitcnt vmcnt(31)
v_mfma_f32_16x16x32_bf16 v[40:43], v[224:227], v[104:107], v[40:43]
v_mfma_f32_16x16x32_bf16 v[80:83], v[224:227], v[108:111], v[80:83]
ds_read_b128 v[224:227], v6 offset:0
s_waitcnt vmcnt(30)
v_mfma_f32_16x16x32_bf16 v[44:47], v[228:231], v[104:107], v[44:47]
v_mfma_f32_16x16x32_bf16 v[84:87], v[228:231], v[108:111], v[84:87]
ds_read_b128 v[228:231], v6 offset:1024
s_waitcnt vmcnt(29)
v_mfma_f32_16x16x32_bf16 v[48:51], v[232:235], v[104:107], v[48:51]
v_mfma_f32_16x16x32_bf16 v[88:91], v[232:235], v[108:111], v[88:91]
ds_read_b128 v[232:235], v6 offset:2048
s_waitcnt vmcnt(28)
v_mfma_f32_16x16x32_bf16 v[52:55], v[236:239], v[104:107], v[52:55]
v_mfma_f32_16x16x32_bf16 v[92:95], v[236:239], v[108:111], v[92:95]
ds_read_b128 v[236:239], v6 offset:3072
)"
R"(s_waitcnt vmcnt(27) lgkmcnt(9)
v_mfma_f32_16x16x32_bf16 v[16:19], v[240:243], v[112:115], v[16:19]
s_waitcnt lgkmcnt(8)
v_mfma_f32_16x16x32_bf16 v[56:59], v[240:243], v[116:119], v[56:59]
ds_read_b128 v[240:243], v6 offset:4096
ds_read_b128 v[104:107], v9 offset:9216
s_waitcnt vmcnt(26)
v_mfma_f32_16x16x32_bf16 v[20:23], v[244:247], v[112:115], v[20:23]
v_mfma_f32_16x16x32_bf16 v[60:63], v[244:247], v[116:119], v[60:63]
ds_read_b128 v[244:247], v6 offset:5120
s_waitcnt vmcnt(25)
v_mfma_f32_16x16x32_bf16 v[24:27], v[248:251], v[112:115], v[24:27]
v_mfma_f32_16x16x32_bf16 v[64:67], v[248:251], v[116:119], v[64:67]
ds_read_b128 v[248:251], v6 offset:6144
s_waitcnt vmcnt(24)
v_mfma_f32_16x16x32_bf16 v[28:31], v[252:255], v[112:115], v[28:31]
v_mfma_f32_16x16x32_bf16 v[68:71], v[252:255], v[116:119], v[68:71]
ds_read_b128 v[252:255], v6 offset:7168
s_waitcnt vmcnt(23)
v_mfma_f32_16x16x32_bf16 v[32:35], v[128:131], v[112:115], v[32:35]
v_mfma_f32_16x16x32_bf16 v[72:75], v[128:131], v[116:119], v[72:75]
ds_read_b128 v[128:131], v6 offset:8192
s_waitcnt vmcnt(22)
v_mfma_f32_16x16x32_bf16 v[36:39], v[132:135], v[112:115], v[36:39]
v_mfma_f32_16x16x32_bf16 v[76:79], v[132:135], v[116:119], v[76:79]
ds_read_b128 v[132:135], v6 offset:9216
ds_read_b128 v[108:111], v7 offset:9216
s_waitcnt lgkmcnt(14)
s_waitcnt vmcnt(21)
v_mfma_f32_16x16x32_bf16 v[40:43], v[136:139], v[112:115], v[40:43]
v_mfma_f32_16x16x32_bf16 v[80:83], v[136:139], v[116:119], v[80:83]
ds_read_b128 v[136:139], v6 offset:10240
s_waitcnt vmcnt(20)
v_mfma_f32_16x16x32_bf16 v[44:47], v[140:143], v[112:115], v[44:47]
v_mfma_f32_16x16x32_bf16 v[84:87], v[140:143], v[116:119], v[84:87]
ds_read_b128 v[140:143], v6 offset:11264
s_waitcnt lgkmcnt(14)
s_waitcnt vmcnt(19)
v_mfma_f32_16x16x32_bf16 v[48:51], v[144:147], v[112:115], v[48:51]
v_mfma_f32_16x16x32_bf16 v[88:91], v[144:147], v[116:119], v[88:91]
s_waitcnt vmcnt(18)
v_mfma_f32_16x16x32_bf16 v[52:55], v[148:151], v[112:115], v[52:55]
v_mfma_f32_16x16x32_bf16 v[92:95], v[148:151], v[116:119], v[92:95]
s_waitcnt vmcnt(17)
v_mfma_f32_16x16x32_bf16 v[16:19], v[152:155], v[120:123], v[16:19]
v_mfma_f32_16x16x32_bf16 v[56:59], v[152:155], v[124:127], v[56:59]
s_waitcnt vmcnt(16)
v_mfma_f32_16x16x32_bf16 v[20:23], v[156:159], v[120:123], v[20:23]
v_mfma_f32_16x16x32_bf16 v[60:63], v[156:159], v[124:127], v[60:63]
s_waitcnt vmcnt(15)
v_mfma_f32_16x16x32_bf16 v[24:27], v[160:163], v[120:123], v[24:27]
v_mfma_f32_16x16x32_bf16 v[64:67], v[160:163], v[124:127], v[64:67]
s_add_u32 s48, s34, 138346496
buffer_load_dwordx4 v[160:163], v8, s[40:43], s48 offen sc1
s_waitcnt vmcnt(15)
v_mfma_f32_16x16x32_bf16 v[28:31], v[164:167], v[120:123], v[28:31]
v_mfma_f32_16x16x32_bf16 v[68:71], v[164:167], v[124:127], v[68:71]
s_add_u32 s48, s34, 138346496
buffer_load_dwordx4 v[164:167], v8, s[40:43], s48 offen offset:512 sc1
s_waitcnt vmcnt(15)
v_mfma_f32_16x16x32_bf16 v[32:35], v[168:171], v[120:123], v[32:35]
v_mfma_f32_16x16x32_bf16 v[72:75], v[168:171], v[124:127], v[72:75]
s_add_u32 s48, s34, 138348544
buffer_load_dwordx4 v[168:171], v8, s[40:43], s48 offen sc1
s_waitcnt vmcnt(15)
v_mfma_f32_16x16x32_bf16 v[36:39], v[172:175], v[120:123], v[36:39]
v_mfma_f32_16x16x32_bf16 v[76:79], v[172:175], v[124:127], v[76:79]
s_add_u32 s48, s34, 138348544
buffer_load_dwordx4 v[172:175], v8, s[40:43], s48 offen offset:512 sc1
s_waitcnt vmcnt(15)
v_mfma_f32_16x16x32_bf16 v[40:43], v[176:179], v[120:123], v[40:43]
v_mfma_f32_16x16x32_bf16 v[80:83], v[176:179], v[124:127], v[80:83]
s_add_u32 s48, s34, 138350592
buffer_load_dwordx4 v[176:179], v8, s[40:43], s48 offen sc1
s_waitcnt vmcnt(15)
v_mfma_f32_16x16x32_bf16 v[44:47], v[180:183], v[120:123], v[44:47]
v_mfma_f32_16x16x32_bf16 v[84:87], v[180:183], v[124:127], v[84:87]
s_add_u32 s48, s34, 138350592
buffer_load_dwordx4 v[180:183], v8, s[40:43], s48 offen offset:512 sc1
s_waitcnt vmcnt(15)
v_mfma_f32_16x16x32_bf16 v[48:51], v[184:187], v[120:123], v[48:51]
v_mfma_f32_16x16x32_bf16 v[88:91], v[184:187], v[124:127], v[88:91]
s_add_u32 s48, s34, 138352640
buffer_load_dwordx4 v[184:187], v8, s[40:43], s48 offen sc1
s_waitcnt vmcnt(15)
v_mfma_f32_16x16x32_bf16 v[52:55], v[188:191], v[120:123], v[52:55]
v_mfma_f32_16x16x32_bf16 v[92:95], v[188:191], v[124:127], v[92:95]
s_add_u32 s48, s34, 138352640
buffer_load_dwordx4 v[188:191], v8, s[40:43], s48 offen offset:512 sc1
s_waitcnt vmcnt(15)
v_mfma_f32_16x16x32_bf16 v[16:19], v[192:195], v[96:99], v[16:19]
v_mfma_f32_16x16x32_bf16 v[56:59], v[192:195], v[100:103], v[56:59]
s_add_u32 s48, s34, 138354688
buffer_load_dwordx4 v[192:195], v8, s[40:43], s48 offen sc1
s_waitcnt vmcnt(15)
v_mfma_f32_16x16x32_bf16 v[20:23], v[196:199], v[96:99], v[20:23]
v_mfma_f32_16x16x32_bf16 v[60:63], v[196:199], v[100:103], v[60:63]
s_add_u32 s48, s34, 138354688
buffer_load_dwordx4 v[196:199], v8, s[40:43], s48 offen offset:512 sc1
s_waitcnt vmcnt(15)
v_mfma_f32_16x16x32_bf16 v[24:27], v[200:203], v[96:99], v[24:27]
v_mfma_f32_16x16x32_bf16 v[64:67], v[200:203], v[100:103], v[64:67]
s_waitcnt vmcnt(14)
v_mfma_f32_16x16x32_bf16 v[28:31], v[204:207], v[96:99], v[28:31]
v_mfma_f32_16x16x32_bf16 v[68:71], v[204:207], v[100:103], v[68:71]
s_waitcnt vmcnt(13)
v_mfma_f32_16x16x32_bf16 v[32:35], v[208:211], v[96:99], v[32:35]
v_mfma_f32_16x16x32_bf16 v[72:75], v[208:211], v[100:103], v[72:75]
s_waitcnt vmcnt(12)
v_mfma_f32_16x16x32_bf16 v[36:39], v[212:215], v[96:99], v[36:39]
v_mfma_f32_16x16x32_bf16 v[76:79], v[212:215], v[100:103], v[76:79]
s_waitcnt vmcnt(11)
v_mfma_f32_16x16x32_bf16 v[40:43], v[216:219], v[96:99], v[40:43]
v_mfma_f32_16x16x32_bf16 v[80:83], v[216:219], v[100:103], v[80:83]
s_waitcnt vmcnt(10)
v_mfma_f32_16x16x32_bf16 v[44:47], v[220:223], v[96:99], v[44:47]
v_mfma_f32_16x16x32_bf16 v[84:87], v[220:223], v[100:103], v[84:87]
s_waitcnt lgkmcnt(13)
v_mfma_f32_16x16x32_bf16 v[48:51], v[224:227], v[96:99], v[48:51]
v_mfma_f32_16x16x32_bf16 v[88:91], v[224:227], v[100:103], v[88:91]
s_waitcnt lgkmcnt(12)
v_mfma_f32_16x16x32_bf16 v[52:55], v[228:231], v[96:99], v[52:55]
v_mfma_f32_16x16x32_bf16 v[92:95], v[228:231], v[100:103], v[92:95]
s_waitcnt lgkmcnt(8)
v_mfma_f32_16x16x32_bf16 v[16:19], v[232:235], v[104:107], v[16:19]
s_waitcnt lgkmcnt(2)
v_mfma_f32_16x16x32_bf16 v[56:59], v[232:235], v[108:111], v[56:59]
v_mfma_f32_16x16x32_bf16 v[20:23], v[236:239], v[104:107], v[20:23]
v_mfma_f32_16x16x32_bf16 v[60:63], v[236:239], v[108:111], v[60:63]
v_mfma_f32_16x16x32_bf16 v[24:27], v[240:243], v[104:107], v[24:27]
v_mfma_f32_16x16x32_bf16 v[64:67], v[240:243], v[108:111], v[64:67]
v_mfma_f32_16x16x32_bf16 v[28:31], v[244:247], v[104:107], v[28:31]
v_mfma_f32_16x16x32_bf16 v[68:71], v[244:247], v[108:111], v[68:71]
v_mfma_f32_16x16x32_bf16 v[32:35], v[248:251], v[104:107], v[32:35]
v_mfma_f32_16x16x32_bf16 v[72:75], v[248:251], v[108:111], v[72:75]
v_mfma_f32_16x16x32_bf16 v[36:39], v[252:255], v[104:107], v[36:39]
v_mfma_f32_16x16x32_bf16 v[76:79], v[252:255], v[108:111], v[76:79]
v_mfma_f32_16x16x32_bf16 v[40:43], v[128:131], v[104:107], v[40:43]
v_mfma_f32_16x16x32_bf16 v[80:83], v[128:131], v[108:111], v[80:83]
v_mfma_f32_16x16x32_bf16 v[44:47], v[132:135], v[104:107], v[44:47]
v_mfma_f32_16x16x32_bf16 v[84:87], v[132:135], v[108:111], v[84:87]
s_waitcnt lgkmcnt(1)
v_mfma_f32_16x16x32_bf16 v[48:51], v[136:139], v[104:107], v[48:51]
v_mfma_f32_16x16x32_bf16 v[88:91], v[136:139], v[108:111], v[88:91]
s_waitcnt lgkmcnt(0)
v_mfma_f32_16x16x32_bf16 v[52:55], v[140:143], v[104:107], v[52:55]
v_mfma_f32_16x16x32_bf16 v[92:95], v[140:143], v[108:111], v[92:95]
s_mul_i32 s48, s45, 10240
v_add_u32 v6, s48, v0
s_xor_b32 s48, s45, 1
s_mul_i32 s48, s48, 10240
v_add_u32 v7, s48, v0
ds_write_b128 v6, v[56:59] offset:0
ds_write_b128 v6, v[60:63] offset:1024
ds_write_b128 v6, v[64:67] offset:2048
ds_write_b128 v6, v[68:71] offset:3072
ds_write_b128 v6, v[72:75] offset:4096
ds_write_b128 v6, v[76:79] offset:5120
ds_write_b128 v6, v[80:83] offset:6144
ds_write_b128 v6, v[84:87] offset:7168
ds_write_b128 v6, v[88:91] offset:8192
ds_write_b128 v6, v[92:95] offset:9216
s_waitcnt vmcnt(9)
v_pk_add_f32 v[160:161], v[160:161], v[16:17]
v_pk_add_f32 v[162:163], v[162:163], v[20:21]
s_waitcnt vmcnt(8)
v_pk_add_f32 v[164:165], v[164:165], v[18:19]
v_pk_add_f32 v[166:167], v[166:167], v[22:23]
s_waitcnt vmcnt(7)
v_pk_add_f32 v[168:169], v[168:169], v[24:25]
v_pk_add_f32 v[170:171], v[170:171], v[28:29]
s_waitcnt vmcnt(6)
v_pk_add_f32 v[172:173], v[172:173], v[26:27]
v_pk_add_f32 v[174:175], v[174:175], v[30:31]
s_waitcnt vmcnt(5)
v_pk_add_f32 v[176:177], v[176:177], v[32:33]
v_pk_add_f32 v[178:179], v[178:179], v[36:37]
s_waitcnt vmcnt(4)
v_pk_add_f32 v[180:181], v[180:181], v[34:35]
v_pk_add_f32 v[182:183], v[182:183], v[38:39]
s_waitcnt vmcnt(3)
v_pk_add_f32 v[184:185], v[184:185], v[40:41]
v_pk_add_f32 v[186:187], v[186:187], v[44:45]
s_waitcnt vmcnt(2)
v_pk_add_f32 v[188:189], v[188:189], v[42:43]
v_pk_add_f32 v[190:191], v[190:191], v[46:47]
s_waitcnt vmcnt(1)
v_pk_add_f32 v[192:193], v[192:193], v[48:49]
v_pk_add_f32 v[194:195], v[194:195], v[52:53]
s_waitcnt vmcnt(0)
v_pk_add_f32 v[196:197], v[196:197], v[50:51]
v_pk_add_f32 v[198:199], v[198:199], v[54:55]
s_waitcnt lgkmcnt(0)
s_barrier
ds_read_b128 v[16:19], v7 offset:0
ds_read_b128 v[20:23], v7 offset:1024
ds_read_b128 v[24:27], v7 offset:2048
ds_read_b128 v[28:31], v7 offset:3072
ds_read_b128 v[32:35], v7 offset:4096
ds_read_b128 v[36:39], v7 offset:5120
)"
R"(ds_read_b128 v[40:43], v7 offset:6144
ds_read_b128 v[44:47], v7 offset:7168
ds_read_b128 v[48:51], v7 offset:8192
ds_read_b128 v[52:55], v7 offset:9216
s_waitcnt lgkmcnt(9)
v_pk_add_f32 v[160:161], v[160:161], v[16:17]
s_waitcnt lgkmcnt(8)
v_pk_add_f32 v[162:163], v[162:163], v[20:21]
v_pk_add_f32 v[164:165], v[164:165], v[18:19]
v_pk_add_f32 v[166:167], v[166:167], v[22:23]
s_waitcnt lgkmcnt(7)
v_pk_add_f32 v[168:169], v[168:169], v[24:25]
s_waitcnt lgkmcnt(6)
v_pk_add_f32 v[170:171], v[170:171], v[28:29]
v_pk_add_f32 v[172:173], v[172:173], v[26:27]
v_pk_add_f32 v[174:175], v[174:175], v[30:31]
s_waitcnt lgkmcnt(5)
v_pk_add_f32 v[176:177], v[176:177], v[32:33]
s_waitcnt lgkmcnt(4)
v_pk_add_f32 v[178:179], v[178:179], v[36:37]
v_pk_add_f32 v[180:181], v[180:181], v[34:35]
v_pk_add_f32 v[182:183], v[182:183], v[38:39]
s_waitcnt lgkmcnt(3)
v_pk_add_f32 v[184:185], v[184:185], v[40:41]
s_waitcnt lgkmcnt(2)
v_pk_add_f32 v[186:187], v[186:187], v[44:45]
v_pk_add_f32 v[188:189], v[188:189], v[42:43]
v_pk_add_f32 v[190:191], v[190:191], v[46:47]
s_waitcnt lgkmcnt(1)
v_pk_add_f32 v[192:193], v[192:193], v[48:49]
s_waitcnt lgkmcnt(0)
v_pk_add_f32 v[194:195], v[194:195], v[52:53]
v_pk_add_f32 v[196:197], v[196:197], v[50:51]
v_pk_add_f32 v[198:199], v[198:199], v[54:55]
v_pk_add_f32 v[12:13], v[160:161], v[162:163]
v_pk_add_f32 v[12:13], v[12:13], v[164:165]
v_pk_add_f32 v[12:13], v[12:13], v[166:167]
v_pk_add_f32 v[12:13], v[12:13], v[168:169]
v_pk_add_f32 v[12:13], v[12:13], v[170:171]
v_pk_add_f32 v[12:13], v[12:13], v[172:173]
v_pk_add_f32 v[12:13], v[12:13], v[174:175]
v_pk_add_f32 v[12:13], v[12:13], v[176:177]
v_pk_add_f32 v[12:13], v[12:13], v[178:179]
v_pk_add_f32 v[12:13], v[12:13], v[180:181]
v_pk_add_f32 v[12:13], v[12:13], v[182:183]
v_pk_add_f32 v[12:13], v[12:13], v[184:185]
v_pk_add_f32 v[12:13], v[12:13], v[186:187]
v_pk_add_f32 v[12:13], v[12:13], v[188:189]
v_pk_add_f32 v[12:13], v[12:13], v[190:191]
v_pk_add_f32 v[12:13], v[12:13], v[192:193]
v_pk_add_f32 v[12:13], v[12:13], v[194:195]
v_pk_add_f32 v[12:13], v[12:13], v[196:197]
v_pk_add_f32 v[12:13], v[12:13], v[198:199]
v_add_f32 v10, v12, v13
v_mov_b32 v12, v10
s_nop 1
v_permlane16_swap_b32 v10, v12
v_add_f32 v10, v10, v12
v_mov_b32 v12, v10
s_nop 1
v_permlane32_swap_b32 v10, v12
v_add_f32 v10, v10, v12
v_mul_f32 v10, 0xbbcccccd, v10
v_pk_add_f32 v[16:17], v[160:161], v[10:11] op_sel_hi:[1,0]
v_pk_add_f32 v[18:19], v[162:163], v[10:11] op_sel_hi:[1,0]
v_pk_add_f32 v[20:21], v[164:165], v[10:11] op_sel_hi:[1,0]
v_pk_add_f32 v[22:23], v[166:167], v[10:11] op_sel_hi:[1,0]
v_pk_add_f32 v[24:25], v[168:169], v[10:11] op_sel_hi:[1,0]
v_pk_add_f32 v[26:27], v[170:171], v[10:11] op_sel_hi:[1,0]
v_pk_add_f32 v[28:29], v[172:173], v[10:11] op_sel_hi:[1,0]
v_pk_add_f32 v[30:31], v[174:175], v[10:11] op_sel_hi:[1,0]
v_pk_add_f32 v[32:33], v[176:177], v[10:11] op_sel_hi:[1,0]
v_pk_add_f32 v[34:35], v[178:179], v[10:11] op_sel_hi:[1,0]
v_pk_add_f32 v[36:37], v[180:181], v[10:11] op_sel_hi:[1,0]
v_pk_add_f32 v[38:39], v[182:183], v[10:11] op_sel_hi:[1,0]
v_pk_add_f32 v[40:41], v[184:185], v[10:11] op_sel_hi:[1,0]
v_pk_add_f32 v[42:43], v[186:187], v[10:11] op_sel_hi:[1,0]
v_pk_add_f32 v[44:45], v[188:189], v[10:11] op_sel_hi:[1,0]
v_pk_add_f32 v[46:47], v[190:191], v[10:11] op_sel_hi:[1,0]
v_pk_add_f32 v[48:49], v[192:193], v[10:11] op_sel_hi:[1,0]
v_pk_add_f32 v[50:51], v[194:195], v[10:11] op_sel_hi:[1,0]
v_pk_add_f32 v[52:53], v[196:197], v[10:11] op_sel_hi:[1,0]
v_pk_add_f32 v[54:55], v[198:199], v[10:11] op_sel_hi:[1,0]
v_pk_mul_f32 v[12:13], v[16:17], v[16:17]
v_pk_fma_f32 v[12:13], v[18:19], v[18:19], v[12:13]
v_pk_fma_f32 v[12:13], v[20:21], v[20:21], v[12:13]
v_pk_fma_f32 v[12:13], v[22:23], v[22:23], v[12:13]
v_pk_fma_f32 v[12:13], v[24:25], v[24:25], v[12:13]
v_pk_fma_f32 v[12:13], v[26:27], v[26:27], v[12:13]
v_pk_fma_f32 v[12:13], v[28:29], v[28:29], v[12:13]
v_pk_fma_f32 v[12:13], v[30:31], v[30:31], v[12:13]
v_pk_fma_f32 v[12:13], v[32:33], v[32:33], v[12:13]
v_pk_fma_f32 v[12:13], v[34:35], v[34:35], v[12:13]
v_pk_fma_f32 v[12:13], v[36:37], v[36:37], v[12:13]
v_pk_fma_f32 v[12:13], v[38:39], v[38:39], v[12:13]
v_pk_fma_f32 v[12:13], v[40:41], v[40:41], v[12:13]
v_pk_fma_f32 v[12:13], v[42:43], v[42:43], v[12:13]
v_pk_fma_f32 v[12:13], v[44:45], v[44:45], v[12:13]
v_pk_fma_f32 v[12:13], v[46:47], v[46:47], v[12:13]
v_pk_fma_f32 v[12:13], v[48:49], v[48:49], v[12:13]
v_pk_fma_f32 v[12:13], v[50:51], v[50:51], v[12:13]
v_pk_fma_f32 v[12:13], v[52:53], v[52:53], v[12:13]
v_pk_fma_f32 v[12:13], v[54:55], v[54:55], v[12:13]
v_add_f32 v11, v12, v13
v_mov_b32 v12, v11
s_nop 1
v_permlane16_swap_b32 v11, v12
v_add_f32 v11, v11, v12
v_mov_b32 v12, v11
s_nop 1
v_permlane32_swap_b32 v11, v12
v_add_f32 v11, v11, v12
v_min_u32 v10, 0xfffffffe, v10
v_min_u32 v11, 0xfffffffe, v11
s_lshl1_add_u32 s48, s26, s30
s_lshl_b32 s48, s48, 9
s_and_b32 s49, s48, 0xffffe000
s_add_u32 s48, s48, s49
s_add_u32 s35, s48, 1441792
v_lshlrev_b32 v6, 3, v15
v_mov_b32 v7, v6
v_lshrrev_b32 v4, 4, v15
v_cmp_eq_u32 vcc, s31, v4
v_mov_b32 v4, 2147483632
s_nop 0
v_cndmask_b32 v7, v4, v7, vcc
buffer_store_dwordx2 v[10:11], v7, s[40:43], s35 offen sc1
s_add_u32 s48, s34, 133103616
buffer_store_dwordx4 v[160:163], v8, s[40:43], s48 offen sc1
buffer_store_dwordx4 v[164:167], v8, s[40:43], s48 offen offset:512 sc1
s_add_u32 s48, s34, 133105664
buffer_store_dwordx4 v[168:171], v8, s[40:43], s48 offen sc1
buffer_store_dwordx4 v[172:175], v8, s[40:43], s48 offen offset:512 sc1
s_add_u32 s48, s34, 133107712
buffer_store_dwordx4 v[176:179], v8, s[40:43], s48 offen sc1
buffer_store_dwordx4 v[180:183], v8, s[40:43], s48 offen offset:512 sc1
s_add_u32 s48, s34, 133109760
buffer_store_dwordx4 v[184:187], v8, s[40:43], s48 offen sc1
buffer_store_dwordx4 v[188:191], v8, s[40:43], s48 offen offset:512 sc1
s_add_u32 s48, s34, 133111808
buffer_store_dwordx4 v[192:195], v8, s[40:43], s48 offen sc1
buffer_store_dwordx4 v[196:199], v8, s[40:43], s48 offen offset:512 sc1
s_mov_b32 s60, 0
hpb2_ln_%=:
buffer_load_dwordx2 v[96:97], v6, s[40:43], s35 offen sc1
s_waitcnt vmcnt(0)
v_max_u32 v4, v96, v97
v_cmp_eq_u32 vcc, -1, v4
s_nop 4
s_cbranch_vccz hpb2_ln_ok_%=
s_sleep 1
s_add_u32 s60, s60, 1
s_cmp_gt_u32 s60, 65536
s_cbranch_scc1 hp_abort_%=
s_and_b32 s59, s60, 63
s_cbranch_scc1 hpb2_ln_%=
v_mov_b32 v4, 1024
buffer_load_dword v4, v4, s[40:43], s58 offen sc1
s_waitcnt vmcnt(0)
v_readfirstlane_b32 s59, v4
s_nop 0
s_cmp_lg_u32 s59, 0
s_nop 2
s_cbranch_scc1 hp_end_%=
s_branch hpb2_ln_%=
hpb2_ln_ok_%=:
v_mov_b32 v12, v96
v_mov_b32 v13, v12
s_nop 1
v_permlane16_swap_b32 v12, v13
v_add_f32 v12, v12, v13
v_mov_b32 v13, v12
s_nop 1
v_permlane32_swap_b32 v12, v13
v_add_f32 v12, v12, v13
v_mul_f32 v12, 0.25, v12
v_sub_f32 v96, v96, v12
v_mul_f32 v96, v96, v96
v_fmamk_f32 v96, v96, 0x43200000, v97
v_mov_b32 v97, v96
s_nop 1
v_permlane16_swap_b32 v96, v97
v_add_f32 v96, v96, v97
v_mov_b32 v97, v96
s_nop 1
v_permlane32_swap_b32 v96, v97
v_add_f32 v96, v96, v97
v_mov_b32 v97, 0x3727c5ac
v_fmamk_f32 v96, v96, 0x3acccccd, v97
v_rsq_f32 v96, v96
v_sub_f32 v12, v12, v10
v_mul_f32 v12, v12, v96
v_pk_fma_f32 v[16:17], v[16:17], v[96:97], v[12:13] op_sel_hi:[1,0,0]
v_pk_fma_f32 v[18:19], v[18:19], v[96:97], v[12:13] op_sel_hi:[1,0,0]
v_pk_fma_f32 v[20:21], v[20:21], v[96:97], v[12:13] op_sel_hi:[1,0,0]
v_pk_fma_f32 v[22:23], v[22:23], v[96:97], v[12:13] op_sel_hi:[1,0,0]
v_pk_fma_f32 v[24:25], v[24:25], v[96:97], v[12:13] op_sel_hi:[1,0,0]
v_pk_fma_f32 v[26:27], v[26:27], v[96:97], v[12:13] op_sel_hi:[1,0,0]
v_pk_fma_f32 v[28:29], v[28:29], v[96:97], v[12:13] op_sel_hi:[1,0,0]
v_pk_fma_f32 v[30:31], v[30:31], v[96:97], v[12:13] op_sel_hi:[1,0,0]
)"
R"(v_pk_fma_f32 v[32:33], v[32:33], v[96:97], v[12:13] op_sel_hi:[1,0,0]
v_pk_fma_f32 v[34:35], v[34:35], v[96:97], v[12:13] op_sel_hi:[1,0,0]
v_pk_fma_f32 v[36:37], v[36:37], v[96:97], v[12:13] op_sel_hi:[1,0,0]
v_pk_fma_f32 v[38:39], v[38:39], v[96:97], v[12:13] op_sel_hi:[1,0,0]
v_pk_fma_f32 v[40:41], v[40:41], v[96:97], v[12:13] op_sel_hi:[1,0,0]
v_pk_fma_f32 v[42:43], v[42:43], v[96:97], v[12:13] op_sel_hi:[1,0,0]
v_pk_fma_f32 v[44:45], v[44:45], v[96:97], v[12:13] op_sel_hi:[1,0,0]
v_pk_fma_f32 v[46:47], v[46:47], v[96:97], v[12:13] op_sel_hi:[1,0,0]
v_pk_fma_f32 v[48:49], v[48:49], v[96:97], v[12:13] op_sel_hi:[1,0,0]
v_pk_fma_f32 v[50:51], v[50:51], v[96:97], v[12:13] op_sel_hi:[1,0,0]
v_pk_fma_f32 v[52:53], v[52:53], v[96:97], v[12:13] op_sel_hi:[1,0,0]
v_pk_fma_f32 v[54:55], v[54:55], v[96:97], v[12:13] op_sel_hi:[1,0,0]
s_lshr_b32 s48, s34, 1
s_add_u32 s48, s48, 5144576
s_add_u32 s49, s48, 4096
v_cvt_pk_bf16_f32 v56, v16, v17
v_cvt_pk_bf16_f32 v57, v20, v21
v_cvt_pk_bf16_f32 v58, v18, v19
v_cvt_pk_bf16_f32 v59, v22, v23
buffer_store_dwordx4 v[56:59], v0, s[40:43], s48 offen sc1
v_cvt_pk_bf16_f32 v60, v24, v25
v_cvt_pk_bf16_f32 v61, v28, v29
v_cvt_pk_bf16_f32 v62, v26, v27
v_cvt_pk_bf16_f32 v63, v30, v31
buffer_store_dwordx4 v[60:63], v0, s[40:43], s48 offen offset:1024 sc1
v_cvt_pk_bf16_f32 v64, v32, v33
v_cvt_pk_bf16_f32 v65, v36, v37
v_cvt_pk_bf16_f32 v66, v34, v35
v_cvt_pk_bf16_f32 v67, v38, v39
buffer_store_dwordx4 v[64:67], v0, s[40:43], s48 offen offset:2048 sc1
v_cvt_pk_bf16_f32 v68, v40, v41
v_cvt_pk_bf16_f32 v69, v44, v45
v_cvt_pk_bf16_f32 v70, v42, v43
v_cvt_pk_bf16_f32 v71, v46, v47
buffer_store_dwordx4 v[68:71], v0, s[40:43], s48 offen offset:3072 sc1
v_cvt_pk_bf16_f32 v72, v48, v49
v_cvt_pk_bf16_f32 v73, v52, v53
v_cvt_pk_bf16_f32 v74, v50, v51
v_cvt_pk_bf16_f32 v75, v54, v55
buffer_store_dwordx4 v[72:75], v0, s[40:43], s49 offen sc1
s_add_u32 s54, s26, 64
s_lshl_b32 s54, s54, 7
s_add_u32 s54, s54, 2140672
hp_done_%=:
s_mov_b32 s55, 1
hp_donen_%=:
s_cmp_eq_u32 s45, 0
s_cselect_b32 s59, s55, 0
v_lshlrev_b32 v2, 7, v15
v_cmp_gt_u32 vcc, s59, v15
v_mov_b32 v4, 2147483632
s_nop 0
v_cndmask_b32 v2, v4, v2, vcc
v_mov_b32 v1, 1
s_waitcnt vmcnt(0)
s_waitcnt lgkmcnt(0)
s_barrier
buffer_atomic_add v1, v2, s[40:43], s54 offen
s_cmp_ge_u32 s20, 400
s_nop 0
s_cbranch_scc1 hp_over_%=
s_branch hp_next_%=
hp_ret_%=:
s_waitcnt vmcnt(0)
s_waitcnt lgkmcnt(0)
s_barrier
hp_next_%=:
v_mov_b32 v3, 155648
s_cmp_lg_u32 s45, 0
s_nop 0
s_cbranch_scc1 hp_tk_%=
s_cmp_eq_u32 s45, 0
s_cselect_b32 s59, 1, 0
v_lshlrev_b32 v2, 7, v15
v_cmp_gt_u32 vcc, s59, v15
v_mov_b32 v4, 2147483632
s_nop 0
v_cndmask_b32 v2, v4, v2, vcc
v_mov_b32 v1, 1
s_lshl_b32 s54, s21, 7
s_add_u32 s54, s54, 2374656
buffer_atomic_add v1, v2, s[40:43], s54 offen sc0
s_waitcnt vmcnt(0)
v_readfirstlane_b32 s48, v1
s_nop 1
v_mov_b32 v1, s48
ds_write_b32 v3, v1
s_nop 0
hp_tk_%=:
s_waitcnt lgkmcnt(0)
s_barrier
ds_read_b32 v1, v3
s_waitcnt lgkmcnt(0)
v_readfirstlane_b32 s20, v1
s_nop 0
s_cmp_lt_u32 s20, 224
s_nop 2
s_cbranch_scc1 hp_b1_%=
s_cmp_ge_u32 s20, 400
s_cbranch_scc1 hp_hi_%=
s_sub_u32 s48, s20, 368
s_cmp_lt_u32 s48, 32
s_cbranch_scc1 hp_rd_%=
s_cmp_ge_u32 s20, 400
s_cselect_b32 s48, 32, 0
s_sub_u32 s27, s20, s48
s_sub_u32 s27, s27, 224
s_mul_i32 s26, s27, 3641
s_lshr_b32 s26, s26, 16
s_mul_i32 s48, s26, 18
s_sub_u32 s27, s27, s48
s_lshl_b32 s26, s26, 2
s_lshr_b32 s26, 1985229328, s26
s_and_b32 s26, s26, 7
s_add_u32 s26, s26, 56
s_cmp_lt_u32 s27, 10
s_cbranch_scc1 hp_tf_%=
s_sub_u32 s27, s27, 10
s_branch hp_bf_%=
hp_hi_%=:
s_cmp_lt_u32 s20, 528
s_cbranch_scc1 hp_b2_%=
hp_over_%=:
s_add_u32 s21, s21, 1
s_cmp_lt_u32 s21, 2
s_cbranch_scc1 hp_layer_%=
s_branch hp_end_%=
hp_abort_%=:
v_mov_b32 v4, 1
v_mov_b32 v5, 1024
buffer_store_dword v4, v5, s[40:43], s58 offen sc1
hp_end_%=:
s_endpgm
)"
    :: CHAIN_INPUTS : CHAIN_CLOBBERS);
    __builtin_unreachable();
}

__global__ void __launch_bounds__(256, 1) mega(char* ws, u16* out, const u16* Wq, const u16* Wk, const u16* Wv, const u16* Wo, Tune tn) {
    extern __shared__ __attribute__((aligned(16))) char smem[];
    LAS char* lds = (LAS char*)smem;
    rsrc_t r = __builtin_amdgcn_make_buffer_rsrc(ws, 0, WS_BYTES, 0x00020000);
    const int w = __builtin_amdgcn_readfirstlane(threadIdx.x >> 6);
    const unsigned xcc = xcc_id();
    if (threadIdx.x == 0) {
        const unsigned nearg = ld4(r, 0, OFF_FLAGS + FL_NEAR) & 1u;
        const unsigned pos = ((((xcc >> 2) ^ nearg) & 1u) << 2) | (xcc & 3u);
        unsigned role = pos * 32 + ((blockIdx.x >> 3) & 31);
        unsigned* claim = (unsigned*)(ws + OFF_FLAGS + FL_CLAIM);
#pragma unroll 1
        for (int n = 0; n < 256; ++n, role = (role + 1) & 255)
            if (__hip_atomic_exchange(claim + role, 1u, __ATOMIC_RELAXED, __HIP_MEMORY_SCOPE_AGENT) == 0u) break;
        *(LAS unsigned*)lds = role;
    }
    __syncthreads();
    const int role = __builtin_amdgcn_readfirstlane(*(LAS unsigned*)lds);
    __syncthreads();
    if ((role >> 2) == 7) {
        f4 p[2][10];
#pragma unroll
        for (int q = 0; q < 20; ++q) p[q / 10][q % 10] = __builtin_bit_cast(f4, ld16p(r, threadIdx.x * 16 + (q / 10) * 4096, OFF_CSP + (q % 10) * 8192));
#pragma unroll
        for (int y = 0; y < 2; ++y)
            st16(__builtin_bit_cast(u4, ((p[y][0] + p[y][1]) + (p[y][2] + p[y][3])) + ((p[y][4] + p[y][5]) + (p[y][6] + p[y][7])) + (p[y][8] + p[y][9])), r, threadIdx.x * 16 + y * 4096, OFF_CS);
        drain();
    }
    const int pos = role >> 5, idx = role & 31;
    const unsigned lo = (unsigned)(uintptr_t)ws, hi = (unsigned)((uintptr_t)ws >> 32) & 0xffffu;
    if (pos < 4 && idx < 8) role_head(lo, hi, pos * 8 + idx, w, xcc & 3, tn, Wq, Wk, 0);
    else if (pos < 4 && idx < 28) role_col(lo, hi, pos * 20 + idx - 8, w, xcc & 3, tn, Wo, Wv, pos * 24 + idx - 8);
    else {
        tn.p[0] = (int)plo(out);
        tn.p[1] = (int)phi(out);
        tn.p[2] = (int)plo(Wq);
        tn.p[3] = (int)phi(Wq);
        const int kind = pos < 4 ? 4 : idx >> 3, id = pos < 4 ? pos * 4 + idx - 28 : (pos - 4) * 8 + (idx & 7);
        role_helper(lo, hi, id, w, kind, tn, kind == 1 ? Wk : (kind == 2 ? Wv : Wo), Wv, pos < 4 ? pos * 24 + idx - 8 : 96 + id);
    }
}

extern "C" void kernel_launch(void* const* d_in, const int* in_sizes, int n_in, void* d_out, int out_size, void* d_ws, size_t ws_size, hipStream_t stream) {
    const u16* x = (const u16*)d_in[0];
    const u16* mem0 = (const u16*)d_in[1];
    const u16 *Wq = (const u16*)d_in[2], *Wk = (const u16*)d_in[3], *Wv = (const u16*)d_in[4], *Wo = (const u16*)d_in[5];
    const u16 *wqkv = (const u16*)d_in[8], *wphi = (const u16*)d_in[9], *wo = (const u16*)d_in[10];
    char* ws = (char*)(((uintptr_t)d_ws + 0x1FFFFF) & ~(uintptr_t)0x1FFFFF);
    (void)hipFuncSetAttribute((const void*)mega, hipFuncAttributeMaxDynamicSharedMemorySize, LDS_BYTES);
    Tune tn = {{51,126,30,74,22,80,110,40}};
    prep<<<PB_END, 256, 0, stream>>>(x, mem0, wqkv, wphi, wo, ws);
    mega<<<256, 256, LDS_BYTES, stream>>>(ws, (u16*)d_out, Wq, Wk, Wv, Wo, tn);
}
```

```cpp
#include <hip/hip_runtime.h>
#include <hip/hip_bf16.h>
#include <stdint.h>

typedef __bf16 bf2 __attribute__((ext_vector_type(2)));
typedef __bf16 bf8 __attribute__((ext_vector_type(8)));
typedef __bf16 bf16v __attribute__((ext_vector_type(16)));
typedef float f4 __attribute__((ext_vector_type(4)));
typedef unsigned u2 __attribute__((ext_vector_type(2)));
typedef unsigned u4 __attribute__((ext_vector_type(4)));
typedef unsigned u8v __attribute__((ext_vector_type(8)));
typedef short s4 __attribute__((ext_vector_type(4)));
typedef unsigned short u16;
#define LAS __attribute__((address_space(3)))
#define DEV __device__ __forceinline__
typedef __amdgpu_buffer_rsrc_t rsrc_t;

constexpr unsigned SENT = 0xFFFFFFFFu;
constexpr int SC1 = 16;
constexpr unsigned SPIN_CAP = 1u << 16;

constexpr unsigned RING = 4u;
constexpr unsigned MCOPY = 4u;
constexpr unsigned PSLOT = 49152u;
constexpr unsigned MSLOT = 32768u;
constexpr unsigned OFF_PRING = 0u;
constexpr unsigned OFF_MRING = 786432u;
constexpr unsigned OFF_SCHED = 1310720u;
constexpr unsigned OFF_LNX = 1441792u;
constexpr unsigned RING_BYTES = 1572864u;
constexpr unsigned OFF_PROBE = 2031616u;
constexpr unsigned OFF_FLAGS = 2097152u;
constexpr unsigned FL_CLAIM = 0u;
constexpr unsigned FL_ABORT = 1024u;
constexpr unsigned FL_NEAR = 442240u;
constexpr unsigned FL_VX = 2048u;
constexpr unsigned FL_KM = 6144u;
constexpr unsigned FL_CK = 10240u;
constexpr unsigned FL_CU = 26880u;
constexpr unsigned FL_CX = 43520u;
constexpr unsigned FL_CT = 60416u;
constexpr unsigned FL_CD = 257024u;
constexpr unsigned FL_CB = 265216u;
constexpr unsigned FL_CL = 267264u;
constexpr unsigned FL_CE = 271360u;
constexpr unsigned FL_CR = 273408u;
constexpr unsigned FL_CF = 275456u;
constexpr unsigned FL_TK = 277504u;
constexpr unsigned FL_BG = 277760u;
constexpr unsigned FL_KH = 278784u;
constexpr unsigned FL_UR = 282880u;
constexpr unsigned FL_RP = 286976u;
constexpr unsigned FL_END = 420096u;
constexpr unsigned OFF_XF = 2539520u;
constexpr unsigned OFF_KX = 7782400u;
constexpr unsigned OFF_VX = 20365312u;
constexpr unsigned OFF_UX = 32964608u;
constexpr unsigned OFF_BT = 64421888u;
constexpr unsigned OFF_VO = 116850688u;
constexpr unsigned OFF_KM = 127336448u;
constexpr unsigned OFF_PTX = 130482176u;
constexpr unsigned OFF_PTM = 132579328u;
constexpr unsigned OFF_XA = 133103616u;
constexpr unsigned OFF_XB = 138346496u;
constexpr unsigned OFF_XR = 143589376u;
constexpr unsigned OFF_WB = 146210816u;
constexpr unsigned OFF_CS = 148668416u;
constexpr unsigned OFF_WO2 = 148676608u;
constexpr unsigned OFF_PHI = 149495808u;
constexpr unsigned OFF_SLAB = 154738688u;
constexpr unsigned OFF_KVC = 162013184u;
constexpr unsigned OFF_PROF = 162078720u;
constexpr unsigned WS_BYTES = 164175872u;
constexpr unsigned OOB = 2147483632u;
constexpr unsigned NSEG = 64u;

DEV unsigned ringphys(unsigned o) { return ((o >> 13) << 14) | (o & 8191); }
DEV unsigned pk2(float a, float b) { bf2 v = {(__bf16)a, (__bf16)b}; return __builtin_bit_cast(unsigned, v); }
DEV float asf(unsigned v) { return __builtin_bit_cast(float, v); }
DEV float bf2f(u16 v) { return __builtin_bit_cast(float, (unsigned)v << 16); }
DEV f4 mfma(u4 a, u4 b, f4 c) { return __builtin_amdgcn_mfma_f32_16x16x32_bf16(__builtin_bit_cast(bf8, a), __builtin_bit_cast(bf8, b), c, 0, 0, 0); }
DEV f4 smfmac(u4 a, u8v b, f4 c, int idx) { return __builtin_amdgcn_smfmac_f32_16x16x64_bf16(__builtin_bit_cast(bf8, a), __builtin_bit_cast(bf16v, b), c, idx, 0, 0); }
DEV u4 ld16(rsrc_t r, unsigned v, unsigned s) { return __builtin_amdgcn_raw_buffer_load_b128(r, v, s, SC1); }
DEV u4 ld16p(rsrc_t r, unsigned v, unsigned s) { return __builtin_amdgcn_raw_buffer_load_b128(r, v, s, 0); }
DEV u2 ld8(rsrc_t r, unsigned v, unsigned s) { return __builtin_amdgcn_raw_buffer_load_b64(r, v, s, SC1); }
DEV unsigned ld4(rsrc_t r, unsigned v, unsigned s) { return __builtin_amdgcn_raw_buffer_load_b32(r, v, s, SC1); }
DEV void st16(u4 d, rsrc_t r, unsigned v, unsigned s) { __builtin_amdgcn_raw_buffer_store_b128(d, r, v, s, SC1); asm volatile("s_nop 1" :: "v"(d) : "memory"); }
DEV void st8(u2 d, rsrc_t r, unsigned v, unsigned s) { __builtin_amdgcn_raw_buffer_store_b64(d, r, v, s, SC1); }
DEV void st4(unsigned d, rsrc_t r, unsigned v, unsigned s) { __builtin_amdgcn_raw_buffer_store_b32(d, r, v, s, SC1); }
DEV u8v cat(u4 a, u4 b) { return __builtin_shufflevector(a, b, 0, 1, 2, 3, 4, 5, 6, 7); }
DEV unsigned umax3(unsigned a, unsigned b, unsigned c) { return max(max(a, b), c); }
DEV unsigned umax4(u4 v, unsigned m) { return umax3(umax3(v.x, v.y, v.z), v.w, m); }
DEV void memfence() { asm volatile("" ::: "memory"); __builtin_amdgcn_sched_barrier(0); }
DEV void drain() { asm volatile("s_waitcnt vmcnt(0)" ::: "memory"); }
DEV void bar() { asm volatile("s_waitcnt lgkmcnt(0)\n\ts_barrier" ::: "memory"); }
DEV unsigned xcc_id() { return (unsigned)__builtin_amdgcn_s_getreg(20 | (3 << 11)) & 0xfu; }
DEV float fold32(float x, float y) {
    auto r = __builtin_amdgcn_permlane32_swap(__builtin_bit_cast(unsigned, x), __builtin_bit_cast(unsigned, y), false, false);
    return asf(r[0]) + asf(r[1]);
}
DEV u2 trread(LAS char* p) { return __builtin_bit_cast(u2, __builtin_amdgcn_ds_read_tr16_b64_v4i16((LAS s4*)p)); }
template <int CTRL, int RM, int BM> DEV float dppf(float old, float v) {
    return __builtin_bit_cast(float, __builtin_amdgcn_update_dpp(__builtin_bit_cast(int, old), __builtin_bit_cast(int, v), CTRL, RM, BM, false));
}
DEV float xmax16(float v) { auto r = __builtin_amdgcn_permlane16_swap(__builtin_bit_cast(unsigned, v), __builtin_bit_cast(unsigned, v), false, false); return fmaxf(asf(r[0]), asf(r[1])); }
DEV float xmax32(float v) { auto r = __builtin_amdgcn_permlane32_swap(__builtin_bit_cast(unsigned, v), __builtin_bit_cast(unsigned, v), false, false); return fmaxf(asf(r[0]), asf(r[1])); }
DEV float xadd16(float v) { auto r = __builtin_amdgcn_permlane16_swap(__builtin_bit_cast(unsigned, v), __builtin_bit_cast(unsigned, v), false, false); return asf(r[0]) + asf(r[1]); }
DEV float xadd32(float v) { auto r = __builtin_amdgcn_permlane32_swap(__builtin_bit_cast(unsigned, v), __builtin_bit_cast(unsigned, v), false, false); return asf(r[0]) + asf(r[1]); }

DEV int dperm(int g, int j) { return 16 * (g >> 1) + 8 * ((j >> 1) & 1) + 4 * (g & 1) + 2 * (j >> 2) + (j & 1); }

DEV void prep_ln(const u16* __restrict__ x, char* ws, LAS char* lds, int b, int w, int lane) {
    const int m = w * 4 + (lane >> 4), k = lane & 15;
    float v[5][8], sum = 0.f, var = 0.f;
#pragma unroll
    for (int j = 0; j < 5; ++j) {
        const u4 q = *(const u4*)(x + (size_t)(b * 16 + m) * 640 + (j * 16 + k) * 8);
#pragma unroll
        for (int e = 0; e < 4; ++e) { const unsigned d = q[e]; v[j][2 * e] = asf(d << 16); v[j][2 * e + 1] = asf(d & 0xffff0000u); sum += v[j][2 * e] + v[j][2 * e + 1]; }
    }
    for (int o = 8; o; o >>= 1) sum += __shfl_xor(sum, o);
    const float mean = sum * (1.f / 640.f);
#pragma unroll
    for (int j = 0; j < 5; ++j)
#pragma unroll
        for (int e = 0; e < 8; ++e) { const float d = v[j][e] - mean; var += d * d; }
    for (int o = 8; o; o >>= 1) var += __shfl_xor(var, o);
    const float rs = rsqrtf(var * (1.f / 640.f) + 1e-5f);
#pragma unroll
    for (int j = 0; j < 5; ++j) {
        f4* xa = (f4*)(ws + OFF_XA + ((size_t)(b * 160 + (j * 16 + k) * 2) * 16 + m) * 16);
        xa[0] = (f4){v[j][0], v[j][1], v[j][2], v[j][3]}; xa[16] = (f4){v[j][4], v[j][5], v[j][6], v[j][7]};
        *(LAS u4*)(lds + m * 1296 + (j * 16 + k) * 16) = (u4){pk2((v[j][0] - mean) * rs, (v[j][1] - mean) * rs), pk2((v[j][2] - mean) * rs, (v[j][3] - mean) * rs),
                                                              pk2((v[j][4] - mean) * rs, (v[j][5] - mean) * rs), pk2((v[j][6] - mean) * rs, (v[j][7] - mean) * rs)};
    }
    bar();
    const int g = lane >> 4;
#pragma unroll
    for (int q = 0; q < 5; ++q) {
        const int kk = w * 5 + q;
        LAS char* row = lds + k * 1296 + (32 * kk + 16 * (g >> 1) + 4 * (g & 1)) * 2;
        const u2 a = *(LAS u2*)row, c = *(LAS u2*)(row + 16);
        *(u4*)(ws + OFF_XF + (size_t)(b * 20 + kk) * 1024 + lane * 16) = (u4){a.x, c.x, a.y, c.y};
    }
}
DEV void prep_layer_b(const u16* __restrict__ wqkv, const u16* __restrict__ wphi, const u16* __restrict__ wo, rsrc_t r, LAS char* lds, int id, int w, int lane) {
    const int g = lane >> 4, m = lane & 15, hg = id & 7, part = (id >> 3) / 5, t = (id >> 3) % 5;
    rsrc_t rw = __builtin_amdgcn_make_buffer_rsrc((void*)wqkv, 0, 640 * 1920 * 2, 0x00020000);
    u4 s[25], b[3];
    unsigned v[2][8], p[24];
#pragma unroll
    for (int q = 0; q < 25; ++q) { const int c = lane + 64 * (q % 5); s[q] = ld16p(rw, (c / 10) * 3840 + (c % 10) * 16, 32 * (4 * (q / 5) + w) * 3840 + part * 1280 + hg * 160); }
#pragma unroll
    for (int k = 0; k < 24; ++k) p[k] = wphi[(4 * hg + (m >> 2)) * 400 + min(max(32 * (k >> 3) + 8 * g + (k & 7) - 20 * (m >> 2), 0), 19) * 20 + 4 * t + (m & 3)];
#pragma unroll
    for (int q = 0; q < 2; ++q) {
        const int f = min(id * 4 + w + 480 * q, 799), ct = (f / 200) * 10 + f % 10, kq = (f % 200) / 10, c = 32 * (ct >> 1) + dperm(m >> 2, 4 * (ct & 1) + (m & 3));
#pragma unroll
        for (int j = 0; j < 8; ++j) { const int idx = 8 * (kq % 5) + j; v[q][j] = wo[(size_t)((4 * (2 * (kq / 5) + idx / 20) + g) * 20 + idx % 20) * 640 + c]; }
    }
    memfence();
    LAS char* st = lds + 1024 + w * 28160;
#pragma unroll
    for (int q = 0; q < 25; ++q) { const int c = lane + 64 * (q % 5); *(LAS u4*)(st + (32 * (q / 5) + c / 10) * 176 + (c % 10) * 16) = s[q]; }
#pragma unroll
    for (int k = 0; k < 24; ++k) {
        const int d = 32 * (k >> 3) + 8 * g + (k & 7) - 20 * (m >> 2);
        p[k] = (d < 0 || d >= 20) ? 0u : (part < 2 ? p[k] : (d == 4 * t + (m & 3) ? 0x3f80u : 0u));
    }
#pragma unroll
    for (int ks = 0; ks < 3; ++ks) b[ks] = (u4){p[8 * ks] | p[8 * ks + 1] << 16, p[8 * ks + 2] | p[8 * ks + 3] << 16, p[8 * ks + 4] | p[8 * ks + 5] << 16, p[8 * ks + 6] | p[8 * ks + 7] << 16};
    f4 c[10];
#pragma unroll
    for (int ks = 0; ks < 3; ++ks)
#pragma unroll
        for (int q = 0; q < 10; ++q) c[q] = mfma(*(LAS u4*)(st + (16 * q + m) * 176 + min(ks * 64 + g * 16, 144)), b[ks], ks ? c[q] : (f4){0, 0, 0, 0});
    float cs = 0.f;
#pragma unroll
    for (int q = 0; q < 5; ++q) {
        const u4 o = {pk2(c[2 * q][0], c[2 * q][1]), pk2(c[2 * q][2], c[2 * q][3]), pk2(c[2 * q + 1][0], c[2 * q + 1][1]), pk2(c[2 * q + 1][2], c[2 * q + 1][3])};
        cs += ((asf(o.x << 16) + asf(o.x & 0xffff0000u)) + (asf(o.y << 16) + asf(o.y & 0xffff0000u))) + ((asf(o.z << 16) + asf(o.z & 0xffff0000u)) + (asf(o.w << 16) + asf(o.w & 0xffff0000u)));
        st16(o, r, lane * 16, OFF_WB + (unsigned)((hg * 20 + 4 * q + w) * 15 + part * 5 + t) * 1024);
    }
#pragma unroll
    for (int q = 0; q < 2; ++q)
        st16((u4){v[q][0] | v[q][1] << 16, v[q][2] | v[q][3] << 16, v[q][4] | v[q][5] << 16, v[q][6] | v[q][7] << 16}, r, lane * 16, OFF_WO2 + (unsigned)min(id * 4 + w + 480 * q, 799) * 1024);
    LAS float* red = (LAS float*)lds;
    red[w * 64 + lane] = xadd32(xadd16(cs));
    bar();
    st4(__builtin_bit_cast(unsigned, (red[m] + red[64 + m]) + (red[128 + m] + red[192 + m])), r, w * 64 + lane < 16 ? m * 4 : OOB, OFF_CS + (hg * 15 + part * 5 + t) * 64);
    bar();
}
constexpr int PB_INIT = 1, PB_LN = PB_INIT + 127, PB_END = PB_LN + 128;
__global__ void __launch_bounds__(256) prep(const u16* __restrict__ x, const u16* __restrict__ mem0, char* ws) {
    __shared__ __attribute__((aligned(16))) char smem[20736];
    LAS char* lds = (LAS char*)smem;
    const int tid = threadIdx.x, w = __builtin_amdgcn_readfirstlane(tid >> 6), lane = tid & 63, b = blockIdx.x;
    rsrc_t r = __builtin_amdgcn_make_buffer_rsrc(ws, 0, WS_BYTES, 0x00020000);
    if (b < PB_INIT) {
        if (w) return;
        unsigned best[2] = {1u << 30, 1u << 30};
        for (int rep = 0; rep < 4; ++rep) for (int c = 0; c < 2; ++c) {
            drain();
            const unsigned long long t0 = __builtin_amdgcn_s_memtime();
            asm volatile("s_waitcnt lgkmcnt(0)" ::: "memory");
            st16((u4){0, 0, 0, 0}, r, lane < 8 ? lane * 16 : OOB, OFF_PROBE + c * 8192 + (rep & 1) * 4096);
            drain();
            best[c] = min(best[c], (unsigned)(__builtin_amdgcn_s_memtime() - t0));
        }
        st4((xcc_id() >> 2) ^ (best[0] > best[1] ? 1u : 0u), r, lane == 0 ? 0 : OOB, OFF_FLAGS + FL_NEAR);
    } else if (b < PB_LN) {
        const int q = b - PB_INIT, n = PB_LN - PB_INIT;
        for (unsigned lo = (q * 256 + tid) * 16; lo < RING_BYTES / 2; lo += n * 4096) {
            const unsigned o = ringphys(lo), p = (lo - OFF_MRING / 2) % (MSLOT / 2);
            u4 v = {SENT, SENT, SENT, SENT};
            if (o >= OFF_MRING && o < OFF_MRING + MCOPY * MSLOT && p < 10240) v = *(const u4*)(mem0 + ((p >> 4) & 7) * 640 + (p >> 7) * 8);
            st16(v, r, o, 0);
        }
        for (unsigned o = (q * 256 + tid) * 16; o < FL_END; o += n * 4096) st16((u4){(o >= FL_CX && o < FL_CX + 64 * 128) ? 2u : 0u, 0, 0, 0}, r, o, OFF_FLAGS);
    } else prep_ln(x, ws, lds, b - PB_LN, w, lane);
}

constexpr int LDS_BYTES = 152 * 1024 + 64;
struct Tune { int p[8]; };
DEV unsigned plo(const void* p) { return (unsigned)(uintptr_t)p; }
DEV unsigned phi(const void* p) { return (unsigned)((uintptr_t)p >> 32) & 0xffffu; }
#define ROLE_CLOBBERS "v255", "a255", "s40", "s41", "s42", "s43", "s44", "s45", "s46", "s47", "s66", "s67", "s68", "s69", "s70", "s71", "s72", "s73", "s76", "s77", "s78", "s79", "s80", "s81", "s82", "s83", "s84", "s99", "vcc", "scc", "memory"
#define ROLE_INPUTS "s"(lo), "s"(hi), "s"(WS_BYTES), "s"(0x00020000u), "s"(id), "s"(w), "s"(aux), "s"(tn.p[0]), "s"(tn.p[1]), "s"(tn.p[2]), "s"(tn.p[3]), "s"(tn.p[4]), "s"(tn.p[5]), "s"(tn.p[6]), "s"(tn.p[7]), "s"(plo(wa)), "s"(phi(wa)), "s"(plo(wb)), "s"(phi(wb)), "s"(bid)
DEV void role_head(unsigned lo, unsigned hi, int id, int w, int aux, const Tune& tn, const void* wa, const void* wb, int bid) {
    asm volatile(
R"(s_mov_b32 s40, %0
s_mov_b32 s41, %1
s_mov_b32 s42, %2
s_mov_b32 s43, %3
s_mov_b32 s44, %4
s_mov_b32 s45, %5
s_mov_b32 s46, %6
s_mov_b32 s66, %7
s_mov_b32 s67, %8
s_mov_b32 s68, %9
s_mov_b32 s69, %10
s_mov_b32 s70, %11
s_mov_b32 s71, %12
s_mov_b32 s72, %13
s_mov_b32 s73, %14
s_mov_b32 s76, %15
s_mov_b32 s77, %16
s_mov_b32 s78, 7864320
s_mov_b32 s79, 0x20000
s_mov_b32 s80, %17
s_mov_b32 s81, %18
s_mov_b32 s82, 7864320
s_mov_b32 s83, 0x20000
s_mov_b32 s84, %19
s_mov_b64 exec, -1
v_mbcnt_lo_u32_b32 v15, -1, 0
v_mbcnt_hi_u32_b32 v15, -1, v15
v_lshlrev_b32 v0, 4, v15
s_mov_b32 s58, 2097152
s_mov_b32 s92, 0
s_lshl_b32 s48, s45, 15
v_add_u32 v2, s48, v0
s_mov_b32 s20, 0
s_mov_b32 s21, 1024
s_mov_b32 s22, 2048
s_mov_b32 s23, 3072
s_mov_b32 s24, 4096
s_mov_b32 s25, 5120
s_mov_b32 s26, 6144
s_mov_b32 s27, 7168
s_mov_b32 s28, 16384
s_mov_b32 s29, 17408
v_add_u32 v150, 0, v15
v_mul_u32_u24 v144, 2731, v150
v_lshrrev_b32 v144, 16, v144
v_mul_u32_u24 v144, 11904, v144
v_lshl_add_u32 v144, v150, 4, v144
v_add_u32 v150, 64, v15
v_mul_u32_u24 v145, 2731, v150
v_lshrrev_b32 v145, 16, v145
v_mul_u32_u24 v145, 11904, v145
v_lshl_add_u32 v145, v150, 4, v145
v_add_u32 v150, 128, v15
v_mul_u32_u24 v146, 2731, v150
v_lshrrev_b32 v146, 16, v146
v_mul_u32_u24 v146, 11904, v146
v_lshl_add_u32 v146, v150, 4, v146
v_bfe_u32 v147, v15, 2, 1
v_and_b32 v148, 8, v15
v_add_u32 v147, v147, v148
v_bfe_u32 v148, v15, 4, 1
v_lshl_add_u32 v147, v148, 2, v147
v_lshrrev_b32 v148, 5, v15
v_lshl_add_u32 v147, v148, 4, v147
v_mul_u32_u24 v147, 384, v147
v_and_b32 v148, 3, v15
v_lshl_add_u32 v147, v148, 3, v147
s_mul_i32 s48, s45, 96
v_add_u32 v147, s48, v147
v_add_u32 v148, 49152, v147
v_add_u32 v149, 98304, v147
s_mul_i32 s50, s45, 196608
s_mul_i32 s48, s44, 384
s_add_u32 s50, s50, s48
s_mul_i32 s51, s45, 6144
s_add_u32 m0, s51, 0
s_add_u32 s49, s50, 0
buffer_load_dwordx4 v144, s[76:79], s49 offen lds
s_add_u32 m0, s51, 1024
s_add_u32 s49, s50, 0
buffer_load_dwordx4 v145, s[76:79], s49 offen lds
s_add_u32 m0, s51, 2048
s_add_u32 s49, s50, 0
buffer_load_dwordx4 v146, s[76:79], s49 offen lds
s_add_u32 m0, s51, 3072
s_add_u32 s49, s50, 98304
buffer_load_dwordx4 v144, s[76:79], s49 offen lds
s_add_u32 m0, s51, 4096
s_add_u32 s49, s50, 98304
buffer_load_dwordx4 v145, s[76:79], s49 offen lds
s_add_u32 m0, s51, 5120
s_add_u32 s49, s50, 98304
buffer_load_dwordx4 v146, s[76:79], s49 offen lds
s_add_u32 m0, s51, 24576
s_add_u32 s49, s50, 0
buffer_load_dwordx4 v144, s[80:83], s49 offen lds
s_add_u32 m0, s51, 25600
s_add_u32 s49, s50, 0
buffer_load_dwordx4 v145, s[80:83], s49 offen lds
s_add_u32 m0, s51, 26624
s_add_u32 s49, s50, 0
buffer_load_dwordx4 v146, s[80:83], s49 offen lds
s_add_u32 m0, s51, 27648
s_add_u32 s49, s50, 98304
buffer_load_dwordx4 v144, s[80:83], s49 offen lds
s_add_u32 m0, s51, 28672
s_add_u32 s49, s50, 98304
buffer_load_dwordx4 v145, s[80:83], s49 offen lds
s_add_u32 m0, s51, 29696
s_add_u32 s49, s50, 98304
buffer_load_dwordx4 v146, s[80:83], s49 offen lds
s_add_u32 m0, s51, 49152
s_add_u32 s49, s50, 786432
buffer_load_dwordx4 v144, s[76:79], s49 offen lds
s_add_u32 m0, s51, 50176
s_add_u32 s49, s50, 786432
buffer_load_dwordx4 v145, s[76:79], s49 offen lds
s_add_u32 m0, s51, 51200
s_add_u32 s49, s50, 786432
buffer_load_dwordx4 v146, s[76:79], s49 offen lds
s_add_u32 m0, s51, 52224
s_add_u32 s49, s50, 884736
buffer_load_dwordx4 v144, s[76:79], s49 offen lds
s_add_u32 m0, s51, 53248
s_add_u32 s49, s50, 884736
buffer_load_dwordx4 v145, s[76:79], s49 offen lds
s_add_u32 m0, s51, 54272
s_add_u32 s49, s50, 884736
buffer_load_dwordx4 v146, s[76:79], s49 offen lds
s_add_u32 m0, s51, 73728
s_add_u32 s49, s50, 786432
buffer_load_dwordx4 v144, s[80:83], s49 offen lds
s_add_u32 m0, s51, 74752
s_add_u32 s49, s50, 786432
buffer_load_dwordx4 v145, s[80:83], s49 offen lds
s_add_u32 m0, s51, 75776
s_add_u32 s49, s50, 786432
buffer_load_dwordx4 v146, s[80:83], s49 offen lds
s_add_u32 m0, s51, 76800
s_add_u32 s49, s50, 884736
buffer_load_dwordx4 v144, s[80:83], s49 offen lds
s_add_u32 m0, s51, 77824
s_add_u32 s49, s50, 884736
buffer_load_dwordx4 v145, s[80:83], s49 offen lds
s_add_u32 m0, s51, 78848
s_add_u32 s49, s50, 884736
buffer_load_dwordx4 v146, s[80:83], s49 offen lds
s_add_u32 m0, s51, 98304
s_add_u32 s49, s50, 1572864
buffer_load_dwordx4 v144, s[76:79], s49 offen lds
s_add_u32 m0, s51, 99328
s_add_u32 s49, s50, 1572864
buffer_load_dwordx4 v145, s[76:79], s49 offen lds
s_add_u32 m0, s51, 100352
s_add_u32 s49, s50, 1572864
buffer_load_dwordx4 v146, s[76:79], s49 offen lds
s_add_u32 m0, s51, 101376
s_add_u32 s49, s50, 1671168
buffer_load_dwordx4 v144, s[76:79], s49 offen lds
s_add_u32 m0, s51, 102400
s_add_u32 s49, s50, 1671168
buffer_load_dwordx4 v145, s[76:79], s49 offen lds
s_add_u32 m0, s51, 103424
s_add_u32 s49, s50, 1671168
buffer_load_dwordx4 v146, s[76:79], s49 offen lds
s_waitcnt vmcnt(24) lgkmcnt(0)
s_barrier
s_add_u32 m0, s51, 122880
s_add_u32 s49, s50, 1572864
buffer_load_dwordx4 v144, s[80:83], s49 offen lds
s_add_u32 m0, s51, 123904
s_add_u32 s49, s50, 1572864
buffer_load_dwordx4 v145, s[80:83], s49 offen lds
s_add_u32 m0, s51, 124928
s_add_u32 s49, s50, 1572864
buffer_load_dwordx4 v146, s[80:83], s49 offen lds
s_add_u32 m0, s51, 125952
s_add_u32 s49, s50, 1671168
buffer_load_dwordx4 v144, s[80:83], s49 offen lds
s_add_u32 m0, s51, 126976
s_add_u32 s49, s50, 1671168
buffer_load_dwordx4 v145, s[80:83], s49 offen lds
s_add_u32 m0, s51, 128000
s_add_u32 s49, s50, 1671168
buffer_load_dwordx4 v146, s[80:83], s49 offen lds
ds_read_b64_tr_b16 a[0:1], v147 offset:0
ds_read_b64_tr_b16 a[2:3], v147 offset:768
ds_read_b64_tr_b16 a[4:5], v147 offset:12288
ds_read_b64_tr_b16 a[6:7], v147 offset:13056
ds_read_b64_tr_b16 a[8:9], v147 offset:32
ds_read_b64_tr_b16 a[10:11], v147 offset:800
ds_read_b64_tr_b16 a[12:13], v147 offset:12320
ds_read_b64_tr_b16 a[14:15], v147 offset:13088
ds_read_b64_tr_b16 a[216:217], v147 offset:64
ds_read_b64_tr_b16 a[218:219], v147 offset:832
ds_read_b64_tr_b16 a[220:221], v147 offset:12352
ds_read_b64_tr_b16 a[222:223], v147 offset:13120
s_waitcnt vmcnt(24) lgkmcnt(0)
s_barrier
s_add_u32 m0, s51, 0
)"
R"(s_add_u32 s49, s50, 2359296
buffer_load_dwordx4 v144, s[76:79], s49 offen lds
s_add_u32 m0, s51, 1024
s_add_u32 s49, s50, 2359296
buffer_load_dwordx4 v145, s[76:79], s49 offen lds
s_add_u32 m0, s51, 2048
s_add_u32 s49, s50, 2359296
buffer_load_dwordx4 v146, s[76:79], s49 offen lds
s_add_u32 m0, s51, 3072
s_add_u32 s49, s50, 2457600
buffer_load_dwordx4 v144, s[76:79], s49 offen lds
s_add_u32 m0, s51, 4096
s_add_u32 s49, s50, 2457600
buffer_load_dwordx4 v145, s[76:79], s49 offen lds
s_add_u32 m0, s51, 5120
s_add_u32 s49, s50, 2457600
buffer_load_dwordx4 v146, s[76:79], s49 offen lds
ds_read_b64_tr_b16 a[16:17], v147 offset:24576
ds_read_b64_tr_b16 a[18:19], v147 offset:25344
ds_read_b64_tr_b16 a[20:21], v147 offset:36864
ds_read_b64_tr_b16 a[22:23], v147 offset:37632
ds_read_b64_tr_b16 v[16:17], v147 offset:24608
ds_read_b64_tr_b16 v[18:19], v147 offset:25376
ds_read_b64_tr_b16 v[20:21], v147 offset:36896
ds_read_b64_tr_b16 v[22:23], v147 offset:37664
ds_read_b64_tr_b16 v[120:121], v147 offset:24640
ds_read_b64_tr_b16 v[122:123], v147 offset:25408
ds_read_b64_tr_b16 v[124:125], v147 offset:36928
ds_read_b64_tr_b16 v[126:127], v147 offset:37696
s_waitcnt vmcnt(24) lgkmcnt(0)
s_barrier
s_add_u32 m0, s51, 24576
s_add_u32 s49, s50, 2359296
buffer_load_dwordx4 v144, s[80:83], s49 offen lds
s_add_u32 m0, s51, 25600
s_add_u32 s49, s50, 2359296
buffer_load_dwordx4 v145, s[80:83], s49 offen lds
s_add_u32 m0, s51, 26624
s_add_u32 s49, s50, 2359296
buffer_load_dwordx4 v146, s[80:83], s49 offen lds
s_add_u32 m0, s51, 27648
s_add_u32 s49, s50, 2457600
buffer_load_dwordx4 v144, s[80:83], s49 offen lds
s_add_u32 m0, s51, 28672
s_add_u32 s49, s50, 2457600
buffer_load_dwordx4 v145, s[80:83], s49 offen lds
s_add_u32 m0, s51, 29696
s_add_u32 s49, s50, 2457600
buffer_load_dwordx4 v146, s[80:83], s49 offen lds
ds_read_b64_tr_b16 a[24:25], v148 offset:0
ds_read_b64_tr_b16 a[26:27], v148 offset:768
ds_read_b64_tr_b16 a[28:29], v148 offset:12288
ds_read_b64_tr_b16 a[30:31], v148 offset:13056
ds_read_b64_tr_b16 v[32:33], v148 offset:32
ds_read_b64_tr_b16 v[34:35], v148 offset:800
ds_read_b64_tr_b16 v[36:37], v148 offset:12320
ds_read_b64_tr_b16 v[38:39], v148 offset:13088
ds_read_b64_tr_b16 a[224:225], v148 offset:64
ds_read_b64_tr_b16 a[226:227], v148 offset:832
ds_read_b64_tr_b16 a[228:229], v148 offset:12352
ds_read_b64_tr_b16 a[230:231], v148 offset:13120
s_waitcnt vmcnt(24) lgkmcnt(0)
s_barrier
s_add_u32 m0, s51, 49152
s_add_u32 s49, s50, 3145728
buffer_load_dwordx4 v144, s[76:79], s49 offen lds
s_add_u32 m0, s51, 50176
s_add_u32 s49, s50, 3145728
buffer_load_dwordx4 v145, s[76:79], s49 offen lds
s_add_u32 m0, s51, 51200
s_add_u32 s49, s50, 3145728
buffer_load_dwordx4 v146, s[76:79], s49 offen lds
s_add_u32 m0, s51, 52224
s_add_u32 s49, s50, 3244032
buffer_load_dwordx4 v144, s[76:79], s49 offen lds
s_add_u32 m0, s51, 53248
s_add_u32 s49, s50, 3244032
buffer_load_dwordx4 v145, s[76:79], s49 offen lds
s_add_u32 m0, s51, 54272
s_add_u32 s49, s50, 3244032
buffer_load_dwordx4 v146, s[76:79], s49 offen lds
ds_read_b64_tr_b16 a[32:33], v148 offset:24576
ds_read_b64_tr_b16 a[34:35], v148 offset:25344
ds_read_b64_tr_b16 a[36:37], v148 offset:36864
ds_read_b64_tr_b16 a[38:39], v148 offset:37632
ds_read_b64_tr_b16 v[24:25], v148 offset:24608
ds_read_b64_tr_b16 v[26:27], v148 offset:25376
ds_read_b64_tr_b16 v[28:29], v148 offset:36896
ds_read_b64_tr_b16 v[30:31], v148 offset:37664
ds_read_b64_tr_b16 v[128:129], v148 offset:24640
ds_read_b64_tr_b16 v[130:131], v148 offset:25408
ds_read_b64_tr_b16 v[132:133], v148 offset:36928
ds_read_b64_tr_b16 v[134:135], v148 offset:37696
s_waitcnt vmcnt(24) lgkmcnt(0)
s_barrier
s_add_u32 m0, s51, 73728
s_add_u32 s49, s50, 3145728
buffer_load_dwordx4 v144, s[80:83], s49 offen lds
s_add_u32 m0, s51, 74752
s_add_u32 s49, s50, 3145728
buffer_load_dwordx4 v145, s[80:83], s49 offen lds
s_add_u32 m0, s51, 75776
s_add_u32 s49, s50, 3145728
buffer_load_dwordx4 v146, s[80:83], s49 offen lds
s_add_u32 m0, s51, 76800
s_add_u32 s49, s50, 3244032
buffer_load_dwordx4 v144, s[80:83], s49 offen lds
s_add_u32 m0, s51, 77824
s_add_u32 s49, s50, 3244032
buffer_load_dwordx4 v145, s[80:83], s49 offen lds
s_add_u32 m0, s51, 78848
s_add_u32 s49, s50, 3244032
buffer_load_dwordx4 v146, s[80:83], s49 offen lds
ds_read_b64_tr_b16 a[40:41], v149 offset:0
ds_read_b64_tr_b16 a[42:43], v149 offset:768
ds_read_b64_tr_b16 a[44:45], v149 offset:12288
ds_read_b64_tr_b16 a[46:47], v149 offset:13056
ds_read_b64_tr_b16 a[48:49], v149 offset:32
ds_read_b64_tr_b16 a[50:51], v149 offset:800
ds_read_b64_tr_b16 a[52:53], v149 offset:12320
ds_read_b64_tr_b16 a[54:55], v149 offset:13088
ds_read_b64_tr_b16 a[232:233], v149 offset:64
ds_read_b64_tr_b16 a[234:235], v149 offset:832
ds_read_b64_tr_b16 a[236:237], v149 offset:12352
ds_read_b64_tr_b16 a[238:239], v149 offset:13120
s_waitcnt vmcnt(24) lgkmcnt(0)
s_barrier
s_add_u32 m0, s51, 98304
s_add_u32 s49, s50, 3932160
buffer_load_dwordx4 v144, s[76:79], s49 offen lds
s_add_u32 m0, s51, 99328
s_add_u32 s49, s50, 3932160
buffer_load_dwordx4 v145, s[76:79], s49 offen lds
s_add_u32 m0, s51, 100352
s_add_u32 s49, s50, 3932160
buffer_load_dwordx4 v146, s[76:79], s49 offen lds
s_add_u32 m0, s51, 101376
s_add_u32 s49, s50, 4030464
buffer_load_dwordx4 v144, s[76:79], s49 offen lds
s_add_u32 m0, s51, 102400
s_add_u32 s49, s50, 4030464
buffer_load_dwordx4 v145, s[76:79], s49 offen lds
s_add_u32 m0, s51, 103424
s_add_u32 s49, s50, 4030464
buffer_load_dwordx4 v146, s[76:79], s49 offen lds
ds_read_b64_tr_b16 a[56:57], v149 offset:24576
ds_read_b64_tr_b16 a[58:59], v149 offset:25344
ds_read_b64_tr_b16 a[60:61], v149 offset:36864
ds_read_b64_tr_b16 a[62:63], v149 offset:37632
ds_read_b64_tr_b16 v[40:41], v149 offset:24608
ds_read_b64_tr_b16 v[42:43], v149 offset:25376
ds_read_b64_tr_b16 v[44:45], v149 offset:36896
ds_read_b64_tr_b16 v[46:47], v149 offset:37664
ds_read_b64_tr_b16 v[136:137], v149 offset:24640
ds_read_b64_tr_b16 v[138:139], v149 offset:25408
ds_read_b64_tr_b16 v[140:141], v149 offset:36928
ds_read_b64_tr_b16 v[142:143], v149 offset:37696
s_waitcnt vmcnt(24) lgkmcnt(0)
s_barrier
s_add_u32 m0, s51, 122880
s_add_u32 s49, s50, 3932160
buffer_load_dwordx4 v144, s[80:83], s49 offen lds
s_add_u32 m0, s51, 123904
s_add_u32 s49, s50, 3932160
buffer_load_dwordx4 v145, s[80:83], s49 offen lds
s_add_u32 m0, s51, 124928
s_add_u32 s49, s50, 3932160
buffer_load_dwordx4 v146, s[80:83], s49 offen lds
s_add_u32 m0, s51, 125952
s_add_u32 s49, s50, 4030464
buffer_load_dwordx4 v144, s[80:83], s49 offen lds
s_add_u32 m0, s51, 126976
s_add_u32 s49, s50, 4030464
buffer_load_dwordx4 v145, s[80:83], s49 offen lds
s_add_u32 m0, s51, 128000
s_add_u32 s49, s50, 4030464
buffer_load_dwordx4 v146, s[80:83], s49 offen lds
ds_read_b64_tr_b16 a[64:65], v147 offset:0
ds_read_b64_tr_b16 a[66:67], v147 offset:768
ds_read_b64_tr_b16 a[68:69], v147 offset:12288
ds_read_b64_tr_b16 a[70:71], v147 offset:13056
ds_read_b64_tr_b16 a[72:73], v147 offset:32
ds_read_b64_tr_b16 a[74:75], v147 offset:800
ds_read_b64_tr_b16 a[76:77], v147 offset:12320
ds_read_b64_tr_b16 a[78:79], v147 offset:13088
ds_read_b64_tr_b16 a[240:241], v147 offset:64
ds_read_b64_tr_b16 a[242:243], v147 offset:832
ds_read_b64_tr_b16 a[244:245], v147 offset:12352
ds_read_b64_tr_b16 a[246:247], v147 offset:13120
s_waitcnt vmcnt(24) lgkmcnt(0)
s_barrier
s_add_u32 m0, s51, 0
s_add_u32 s49, s50, 4718592
buffer_load_dwordx4 v144, s[76:79], s49 offen lds
s_add_u32 m0, s51, 1024
s_add_u32 s49, s50, 4718592
buffer_load_dwordx4 v145, s[76:79], s49 offen lds
s_add_u32 m0, s51, 2048
s_add_u32 s49, s50, 4718592
buffer_load_dwordx4 v146, s[76:79], s49 offen lds
)"
R"(s_add_u32 m0, s51, 3072
s_add_u32 s49, s50, 4816896
buffer_load_dwordx4 v144, s[76:79], s49 offen lds
s_add_u32 m0, s51, 4096
s_add_u32 s49, s50, 4816896
buffer_load_dwordx4 v145, s[76:79], s49 offen lds
s_add_u32 m0, s51, 5120
s_add_u32 s49, s50, 4816896
buffer_load_dwordx4 v146, s[76:79], s49 offen lds
ds_read_b64_tr_b16 a[80:81], v147 offset:24576
ds_read_b64_tr_b16 a[82:83], v147 offset:25344
ds_read_b64_tr_b16 a[84:85], v147 offset:36864
ds_read_b64_tr_b16 a[86:87], v147 offset:37632
ds_read_b64_tr_b16 v[48:49], v147 offset:24608
ds_read_b64_tr_b16 v[50:51], v147 offset:25376
ds_read_b64_tr_b16 v[52:53], v147 offset:36896
ds_read_b64_tr_b16 v[54:55], v147 offset:37664
ds_read_b64_tr_b16 a[248:249], v147 offset:24640
ds_read_b64_tr_b16 a[250:251], v147 offset:25408
ds_read_b64_tr_b16 a[252:253], v147 offset:36928
ds_read_b64_tr_b16 a[254:255], v147 offset:37696
s_waitcnt vmcnt(24) lgkmcnt(0)
s_barrier
s_add_u32 m0, s51, 24576
s_add_u32 s49, s50, 4718592
buffer_load_dwordx4 v144, s[80:83], s49 offen lds
s_add_u32 m0, s51, 25600
s_add_u32 s49, s50, 4718592
buffer_load_dwordx4 v145, s[80:83], s49 offen lds
s_add_u32 m0, s51, 26624
s_add_u32 s49, s50, 4718592
buffer_load_dwordx4 v146, s[80:83], s49 offen lds
s_add_u32 m0, s51, 27648
s_add_u32 s49, s50, 4816896
buffer_load_dwordx4 v144, s[80:83], s49 offen lds
s_add_u32 m0, s51, 28672
s_add_u32 s49, s50, 4816896
buffer_load_dwordx4 v145, s[80:83], s49 offen lds
s_add_u32 m0, s51, 29696
s_add_u32 s49, s50, 4816896
buffer_load_dwordx4 v146, s[80:83], s49 offen lds
ds_read_b64_tr_b16 a[88:89], v148 offset:0
ds_read_b64_tr_b16 a[90:91], v148 offset:768
ds_read_b64_tr_b16 a[92:93], v148 offset:12288
ds_read_b64_tr_b16 a[94:95], v148 offset:13056
ds_read_b64_tr_b16 v[64:65], v148 offset:32
ds_read_b64_tr_b16 v[66:67], v148 offset:800
ds_read_b64_tr_b16 v[68:69], v148 offset:12320
ds_read_b64_tr_b16 v[70:71], v148 offset:13088
ds_read_b64_tr_b16 v[160:161], v148 offset:64
ds_read_b64_tr_b16 v[162:163], v148 offset:832
ds_read_b64_tr_b16 v[164:165], v148 offset:12352
ds_read_b64_tr_b16 v[166:167], v148 offset:13120
s_waitcnt vmcnt(24) lgkmcnt(0)
s_barrier
s_add_u32 m0, s51, 49152
s_add_u32 s49, s50, 5505024
buffer_load_dwordx4 v144, s[76:79], s49 offen lds
s_add_u32 m0, s51, 50176
s_add_u32 s49, s50, 5505024
buffer_load_dwordx4 v145, s[76:79], s49 offen lds
s_add_u32 m0, s51, 51200
s_add_u32 s49, s50, 5505024
buffer_load_dwordx4 v146, s[76:79], s49 offen lds
s_add_u32 m0, s51, 52224
s_add_u32 s49, s50, 5603328
buffer_load_dwordx4 v144, s[76:79], s49 offen lds
s_add_u32 m0, s51, 53248
s_add_u32 s49, s50, 5603328
buffer_load_dwordx4 v145, s[76:79], s49 offen lds
s_add_u32 m0, s51, 54272
s_add_u32 s49, s50, 5603328
buffer_load_dwordx4 v146, s[76:79], s49 offen lds
ds_read_b64_tr_b16 a[96:97], v148 offset:24576
ds_read_b64_tr_b16 a[98:99], v148 offset:25344
ds_read_b64_tr_b16 a[100:101], v148 offset:36864
ds_read_b64_tr_b16 a[102:103], v148 offset:37632
ds_read_b64_tr_b16 v[56:57], v148 offset:24608
ds_read_b64_tr_b16 v[58:59], v148 offset:25376
ds_read_b64_tr_b16 v[60:61], v148 offset:36896
ds_read_b64_tr_b16 v[62:63], v148 offset:37664
ds_read_b64_tr_b16 v[168:169], v148 offset:24640
ds_read_b64_tr_b16 v[170:171], v148 offset:25408
ds_read_b64_tr_b16 v[172:173], v148 offset:36928
ds_read_b64_tr_b16 v[174:175], v148 offset:37696
s_waitcnt vmcnt(24) lgkmcnt(0)
s_barrier
s_add_u32 m0, s51, 73728
s_add_u32 s49, s50, 5505024
buffer_load_dwordx4 v144, s[80:83], s49 offen lds
s_add_u32 m0, s51, 74752
s_add_u32 s49, s50, 5505024
buffer_load_dwordx4 v145, s[80:83], s49 offen lds
s_add_u32 m0, s51, 75776
s_add_u32 s49, s50, 5505024
buffer_load_dwordx4 v146, s[80:83], s49 offen lds
s_add_u32 m0, s51, 76800
s_add_u32 s49, s50, 5603328
buffer_load_dwordx4 v144, s[80:83], s49 offen lds
s_add_u32 m0, s51, 77824
s_add_u32 s49, s50, 5603328
buffer_load_dwordx4 v145, s[80:83], s49 offen lds
s_add_u32 m0, s51, 78848
s_add_u32 s49, s50, 5603328
buffer_load_dwordx4 v146, s[80:83], s49 offen lds
ds_read_b64_tr_b16 a[104:105], v149 offset:0
ds_read_b64_tr_b16 a[106:107], v149 offset:768
ds_read_b64_tr_b16 a[108:109], v149 offset:12288
ds_read_b64_tr_b16 a[110:111], v149 offset:13056
ds_read_b64_tr_b16 a[112:113], v149 offset:32
ds_read_b64_tr_b16 a[114:115], v149 offset:800
ds_read_b64_tr_b16 a[116:117], v149 offset:12320
ds_read_b64_tr_b16 a[118:119], v149 offset:13088
ds_read_b64_tr_b16 v[176:177], v149 offset:64
ds_read_b64_tr_b16 v[178:179], v149 offset:832
ds_read_b64_tr_b16 v[180:181], v149 offset:12352
ds_read_b64_tr_b16 v[182:183], v149 offset:13120
s_waitcnt vmcnt(24) lgkmcnt(0)
s_barrier
s_add_u32 m0, s51, 98304
s_add_u32 s49, s50, 6291456
buffer_load_dwordx4 v144, s[76:79], s49 offen lds
s_add_u32 m0, s51, 99328
s_add_u32 s49, s50, 6291456
buffer_load_dwordx4 v145, s[76:79], s49 offen lds
s_add_u32 m0, s51, 100352
s_add_u32 s49, s50, 6291456
buffer_load_dwordx4 v146, s[76:79], s49 offen lds
s_add_u32 m0, s51, 101376
s_add_u32 s49, s50, 6389760
buffer_load_dwordx4 v144, s[76:79], s49 offen lds
s_add_u32 m0, s51, 102400
s_add_u32 s49, s50, 6389760
buffer_load_dwordx4 v145, s[76:79], s49 offen lds
s_add_u32 m0, s51, 103424
s_add_u32 s49, s50, 6389760
buffer_load_dwordx4 v146, s[76:79], s49 offen lds
ds_read_b64_tr_b16 a[120:121], v149 offset:24576
ds_read_b64_tr_b16 a[122:123], v149 offset:25344
ds_read_b64_tr_b16 a[124:125], v149 offset:36864
ds_read_b64_tr_b16 a[126:127], v149 offset:37632
ds_read_b64_tr_b16 v[72:73], v149 offset:24608
ds_read_b64_tr_b16 v[74:75], v149 offset:25376
ds_read_b64_tr_b16 v[76:77], v149 offset:36896
ds_read_b64_tr_b16 v[78:79], v149 offset:37664
ds_read_b64_tr_b16 v[184:185], v149 offset:24640
ds_read_b64_tr_b16 v[186:187], v149 offset:25408
ds_read_b64_tr_b16 v[188:189], v149 offset:36928
ds_read_b64_tr_b16 v[190:191], v149 offset:37696
s_waitcnt vmcnt(24) lgkmcnt(0)
s_barrier
s_add_u32 m0, s51, 122880
s_add_u32 s49, s50, 6291456
buffer_load_dwordx4 v144, s[80:83], s49 offen lds
s_add_u32 m0, s51, 123904
s_add_u32 s49, s50, 6291456
buffer_load_dwordx4 v145, s[80:83], s49 offen lds
s_add_u32 m0, s51, 124928
s_add_u32 s49, s50, 6291456
buffer_load_dwordx4 v146, s[80:83], s49 offen lds
s_add_u32 m0, s51, 125952
s_add_u32 s49, s50, 6389760
buffer_load_dwordx4 v144, s[80:83], s49 offen lds
s_add_u32 m0, s51, 126976
s_add_u32 s49, s50, 6389760
buffer_load_dwordx4 v145, s[80:83], s49 offen lds
s_add_u32 m0, s51, 128000
s_add_u32 s49, s50, 6389760
buffer_load_dwordx4 v146, s[80:83], s49 offen lds
ds_read_b64_tr_b16 a[128:129], v147 offset:0
ds_read_b64_tr_b16 a[130:131], v147 offset:768
ds_read_b64_tr_b16 a[132:133], v147 offset:12288
ds_read_b64_tr_b16 a[134:135], v147 offset:13056
ds_read_b64_tr_b16 a[136:137], v147 offset:32
ds_read_b64_tr_b16 a[138:139], v147 offset:800
ds_read_b64_tr_b16 a[140:141], v147 offset:12320
ds_read_b64_tr_b16 a[142:143], v147 offset:13088
ds_read_b64_tr_b16 v[192:193], v147 offset:64
ds_read_b64_tr_b16 v[194:195], v147 offset:832
ds_read_b64_tr_b16 v[196:197], v147 offset:12352
ds_read_b64_tr_b16 v[198:199], v147 offset:13120
s_waitcnt vmcnt(24) lgkmcnt(0)
s_barrier
s_add_u32 m0, s51, 0
s_add_u32 s49, s50, 7077888
buffer_load_dwordx4 v144, s[76:79], s49 offen lds
s_add_u32 m0, s51, 1024
s_add_u32 s49, s50, 7077888
buffer_load_dwordx4 v145, s[76:79], s49 offen lds
s_add_u32 m0, s51, 2048
s_add_u32 s49, s50, 7077888
buffer_load_dwordx4 v146, s[76:79], s49 offen lds
s_add_u32 m0, s51, 3072
s_add_u32 s49, s50, 7176192
buffer_load_dwordx4 v144, s[76:79], s49 offen lds
s_add_u32 m0, s51, 4096
s_add_u32 s49, s50, 7176192
buffer_load_dwordx4 v145, s[76:79], s49 offen lds
s_add_u32 m0, s51, 5120
s_add_u32 s49, s50, 7176192
)"
R"(buffer_load_dwordx4 v146, s[76:79], s49 offen lds
ds_read_b64_tr_b16 a[144:145], v147 offset:24576
ds_read_b64_tr_b16 a[146:147], v147 offset:25344
ds_read_b64_tr_b16 a[148:149], v147 offset:36864
ds_read_b64_tr_b16 a[150:151], v147 offset:37632
ds_read_b64_tr_b16 v[80:81], v147 offset:24608
ds_read_b64_tr_b16 v[82:83], v147 offset:25376
ds_read_b64_tr_b16 v[84:85], v147 offset:36896
ds_read_b64_tr_b16 v[86:87], v147 offset:37664
ds_read_b64_tr_b16 v[200:201], v147 offset:24640
ds_read_b64_tr_b16 v[202:203], v147 offset:25408
ds_read_b64_tr_b16 v[204:205], v147 offset:36928
ds_read_b64_tr_b16 v[206:207], v147 offset:37696
s_waitcnt vmcnt(24) lgkmcnt(0)
s_barrier
s_add_u32 m0, s51, 24576
s_add_u32 s49, s50, 7077888
buffer_load_dwordx4 v144, s[80:83], s49 offen lds
s_add_u32 m0, s51, 25600
s_add_u32 s49, s50, 7077888
buffer_load_dwordx4 v145, s[80:83], s49 offen lds
s_add_u32 m0, s51, 26624
s_add_u32 s49, s50, 7077888
buffer_load_dwordx4 v146, s[80:83], s49 offen lds
s_add_u32 m0, s51, 27648
s_add_u32 s49, s50, 7176192
buffer_load_dwordx4 v144, s[80:83], s49 offen lds
s_add_u32 m0, s51, 28672
s_add_u32 s49, s50, 7176192
buffer_load_dwordx4 v145, s[80:83], s49 offen lds
s_add_u32 m0, s51, 29696
s_add_u32 s49, s50, 7176192
buffer_load_dwordx4 v146, s[80:83], s49 offen lds
ds_read_b64_tr_b16 a[152:153], v148 offset:0
ds_read_b64_tr_b16 a[154:155], v148 offset:768
ds_read_b64_tr_b16 a[156:157], v148 offset:12288
ds_read_b64_tr_b16 a[158:159], v148 offset:13056
ds_read_b64_tr_b16 v[96:97], v148 offset:32
ds_read_b64_tr_b16 v[98:99], v148 offset:800
ds_read_b64_tr_b16 v[100:101], v148 offset:12320
ds_read_b64_tr_b16 v[102:103], v148 offset:13088
ds_read_b64_tr_b16 v[208:209], v148 offset:64
ds_read_b64_tr_b16 v[210:211], v148 offset:832
ds_read_b64_tr_b16 v[212:213], v148 offset:12352
ds_read_b64_tr_b16 v[214:215], v148 offset:13120
s_waitcnt vmcnt(24) lgkmcnt(0)
s_barrier
ds_read_b64_tr_b16 a[160:161], v148 offset:24576
ds_read_b64_tr_b16 a[162:163], v148 offset:25344
ds_read_b64_tr_b16 a[164:165], v148 offset:36864
ds_read_b64_tr_b16 a[166:167], v148 offset:37632
ds_read_b64_tr_b16 v[88:89], v148 offset:24608
ds_read_b64_tr_b16 v[90:91], v148 offset:25376
ds_read_b64_tr_b16 v[92:93], v148 offset:36896
ds_read_b64_tr_b16 v[94:95], v148 offset:37664
ds_read_b64_tr_b16 v[216:217], v148 offset:24640
ds_read_b64_tr_b16 v[218:219], v148 offset:25408
ds_read_b64_tr_b16 v[220:221], v148 offset:36928
ds_read_b64_tr_b16 v[222:223], v148 offset:37696
s_waitcnt vmcnt(18) lgkmcnt(0)
s_barrier
ds_read_b64_tr_b16 a[168:169], v149 offset:0
ds_read_b64_tr_b16 a[170:171], v149 offset:768
ds_read_b64_tr_b16 a[172:173], v149 offset:12288
ds_read_b64_tr_b16 a[174:175], v149 offset:13056
ds_read_b64_tr_b16 a[176:177], v149 offset:32
ds_read_b64_tr_b16 a[178:179], v149 offset:800
ds_read_b64_tr_b16 a[180:181], v149 offset:12320
ds_read_b64_tr_b16 a[182:183], v149 offset:13088
ds_read_b64_tr_b16 v[224:225], v149 offset:64
ds_read_b64_tr_b16 v[226:227], v149 offset:832
ds_read_b64_tr_b16 v[228:229], v149 offset:12352
ds_read_b64_tr_b16 v[230:231], v149 offset:13120
s_waitcnt vmcnt(12) lgkmcnt(0)
s_barrier
ds_read_b64_tr_b16 a[184:185], v149 offset:24576
ds_read_b64_tr_b16 a[186:187], v149 offset:25344
ds_read_b64_tr_b16 a[188:189], v149 offset:36864
ds_read_b64_tr_b16 a[190:191], v149 offset:37632
ds_read_b64_tr_b16 v[104:105], v149 offset:24608
ds_read_b64_tr_b16 v[106:107], v149 offset:25376
ds_read_b64_tr_b16 v[108:109], v149 offset:36896
ds_read_b64_tr_b16 v[110:111], v149 offset:37664
ds_read_b64_tr_b16 v[232:233], v149 offset:24640
ds_read_b64_tr_b16 v[234:235], v149 offset:25408
ds_read_b64_tr_b16 v[236:237], v149 offset:36928
ds_read_b64_tr_b16 v[238:239], v149 offset:37696
s_waitcnt vmcnt(6) lgkmcnt(0)
s_barrier
ds_read_b64_tr_b16 a[192:193], v147 offset:0
ds_read_b64_tr_b16 a[194:195], v147 offset:768
ds_read_b64_tr_b16 a[196:197], v147 offset:12288
ds_read_b64_tr_b16 a[198:199], v147 offset:13056
ds_read_b64_tr_b16 a[200:201], v147 offset:32
ds_read_b64_tr_b16 a[202:203], v147 offset:800
ds_read_b64_tr_b16 a[204:205], v147 offset:12320
ds_read_b64_tr_b16 a[206:207], v147 offset:13088
ds_read_b64_tr_b16 v[240:241], v147 offset:64
ds_read_b64_tr_b16 v[242:243], v147 offset:832
ds_read_b64_tr_b16 v[244:245], v147 offset:12352
ds_read_b64_tr_b16 v[246:247], v147 offset:13120
s_waitcnt vmcnt(0) lgkmcnt(0)
s_barrier
ds_read_b64_tr_b16 a[208:209], v147 offset:24576
ds_read_b64_tr_b16 a[210:211], v147 offset:25344
ds_read_b64_tr_b16 a[212:213], v147 offset:36864
ds_read_b64_tr_b16 a[214:215], v147 offset:37632
ds_read_b64_tr_b16 v[112:113], v147 offset:24608
ds_read_b64_tr_b16 v[114:115], v147 offset:25376
ds_read_b64_tr_b16 v[116:117], v147 offset:36896
ds_read_b64_tr_b16 v[118:119], v147 offset:37664
ds_read_b64_tr_b16 v[248:249], v147 offset:24640
ds_read_b64_tr_b16 v[250:251], v147 offset:25408
ds_read_b64_tr_b16 v[252:253], v147 offset:36928
ds_read_b64_tr_b16 v[254:255], v147 offset:37696
s_waitcnt lgkmcnt(0)
s_barrier
ds_write_b128 v2, v[16:19] offset:0
ds_write_b128 v2, v[20:23] offset:1024
ds_write_b128 v2, v[24:27] offset:2048
ds_write_b128 v2, v[28:31] offset:3072
ds_write_b128 v2, v[32:35] offset:4096
ds_write_b128 v2, v[36:39] offset:5120
ds_write_b128 v2, v[40:43] offset:6144
ds_write_b128 v2, v[44:47] offset:7168
ds_write_b128 v2, v[48:51] offset:8192
ds_write_b128 v2, v[52:55] offset:9216
ds_write_b128 v2, v[56:59] offset:10240
ds_write_b128 v2, v[60:63] offset:11264
ds_write_b128 v2, v[64:67] offset:12288
ds_write_b128 v2, v[68:71] offset:13312
ds_write_b128 v2, v[72:75] offset:14336
ds_write_b128 v2, v[76:79] offset:15360
s_waitcnt lgkmcnt(14)
ds_write_b128 v2, v[80:83] offset:16384
ds_write_b128 v2, v[84:87] offset:17408
s_waitcnt lgkmcnt(14)
ds_write_b128 v2, v[88:91] offset:18432
ds_write_b128 v2, v[92:95] offset:19456
s_waitcnt lgkmcnt(14)
ds_write_b128 v2, v[96:99] offset:20480
ds_write_b128 v2, v[100:103] offset:21504
s_waitcnt lgkmcnt(14)
ds_write_b128 v2, v[104:107] offset:22528
ds_write_b128 v2, v[108:111] offset:23552
s_waitcnt lgkmcnt(14)
ds_write_b128 v2, v[112:115] offset:24576
ds_write_b128 v2, v[116:119] offset:25600
s_waitcnt lgkmcnt(14)
ds_write_b128 v2, v[120:123] offset:26624
ds_write_b128 v2, v[124:127] offset:27648
s_waitcnt lgkmcnt(14)
ds_write_b128 v2, v[128:131] offset:28672
ds_write_b128 v2, v[132:135] offset:29696
s_waitcnt lgkmcnt(14)
ds_write_b128 v2, v[136:139] offset:30720
ds_write_b128 v2, v[140:143] offset:31744
s_waitcnt lgkmcnt(14)
s_waitcnt vmcnt(0) lgkmcnt(0)
v_lshrrev_b32 v152, 4, v15
v_and_b32 v153, 7, v15
v_bfe_u32 v154, v15, 3, 1
v_cmp_eq_u32 vcc, 1, v154
v_mov_b32 v13, 0x4444
v_mov_b32 v1, 0xeeee
v_cndmask_b32 v1, v13, v1, vcc
v_mov_b32 v13, 0xff800000
v_cndmask_b32 v8, 0, v13, vcc
v_cndmask_b32 v9, 0, v13, vcc
v_cndmask_b32 v10, 0, v13, vcc
v_cndmask_b32 v11, 0, v13, vcc
v_bfe_u32 v13, v15, 2, 2
v_xor_b32 v13, v13, v152
v_lshlrev_b32 v13, 3, v13
v_and_b32 v3, 15, v15
v_lshl_add_u32 v3, v3, 5, v13
s_mul_i32 s48, s45, 1536
s_add_u32 s48, s48, 143360
v_add_u32 v3, s48, v3
v_xor_b32 v4, v15, v152
v_lshlrev_b32 v4, 3, v4
v_add_u32 v4, 143360, v4
v_add_u32 v5, 131072, v0
s_lshr_b32 s48, s44, 1
s_lshl_b32 s48, s48, 10
s_and_b32 s49, s44, 1
s_lshl_b32 s49, s49, 7
s_add_u32 s50, s48, s49
s_lshl_b32 s51, s44, 7
s_add_u32 s51, s51, 16384
s_mov_b32 s47, 0
s_mov_b32 s61, 6144
s_cmp_lg_u32 s45, 0
s_cbranch_scc1 hw_wx_%=
v_lshlrev_b32 v6, 8, v152
v_add_u32 v6, s50, v6
v_mov_b32 v7, s51
v_cmp_eq_u32 vcc, 0, v154
s_nop 1
v_cndmask_b32 v6, v7, v6, vcc
)"
R"(v_lshl_add_u32 v6, v153, 4, v6
v_and_b32 v13, 0xffffe000, v6
v_add_u32 v6, v6, v13
v_lshrrev_b32 v7, 3, v15
v_cmp_eq_u32 s[56:57], 5, v7
s_nop 0
s_or_b64 vcc, vcc, s[56:57]
v_mov_b32 v13, 2147483632
v_cndmask_b32 v6, v13, v6, vcc
v_mov_b32 v14, 0
v_cmp_eq_u32 vcc, 0, v15
v_mov_b32 v13, 2147483632
s_nop 0
v_cndmask_b32 v14, v13, v14, vcc
s_cmp_eq_u32 s44, 0
s_cselect_b32 s59, 4, 0
v_lshlrev_b32 v7, 15, v15
v_cmp_gt_u32 vcc, s59, v15
v_mov_b32 v13, 2147483632
s_nop 0
v_cndmask_b32 v7, v13, v7, vcc
s_lshl_b32 s59, s59, 1
v_lshlrev_b32 v155, 7, v15
v_cmp_gt_u32 vcc, s59, v15
v_mov_b32 v13, 2147483632
s_nop 0
v_cndmask_b32 v155, v13, v155, vcc
s_mov_b32 s64, 0x3dd53b95
s_mov_b32 s65, s64
s_mov_b32 s8, 0xff00ff00
s_mov_b32 s9, s8
s_lshr_b32 s48, s44, 3
s_lshl_b32 s48, s48, 7
v_add_u32 v159, s48, v14
s_mul_i32 s10, s67, 23
s_sub_u32 s10, s10, s71
s_mul_i32 s11, s66, 23
s_sub_u32 s11, s11, s72
hw_loopa_%=:
s_and_b32 s48, s47, 3
s_mul_i32 s53, s48, 196608
s_lshl_b32 s50, s48, 2
s_add_u32 s50, s50, s46
s_mul_i32 s50, s50, 32768
s_add_u32 s50, s50, 786432
v_add_u32 v158, s50, v0
s_add_u32 s50, s53, 49152
s_add_u32 s51, s53, 98304
s_add_u32 s52, s53, 147456
v_mov_b32 v56, 0
v_mov_b32 v57, 0
v_mov_b32 v58, 0
v_mov_b32 v59, 0
v_mov_b32 v60, 0
v_mov_b32 v61, 0
v_mov_b32 v62, 0
v_mov_b32 v63, 0
v_mov_b32 v64, 0
v_mov_b32 v65, 0
v_mov_b32 v66, 0
v_mov_b32 v67, 0
v_mov_b32 v68, 0
v_mov_b32 v69, 0
v_mov_b32 v70, 0
v_mov_b32 v71, 0
v_mov_b32 v72, 0
v_mov_b32 v73, 0
v_mov_b32 v74, 0
v_mov_b32 v75, 0
v_mov_b32 v76, 0
v_mov_b32 v77, 0
v_mov_b32 v78, 0
v_mov_b32 v79, 0
ds_read_b128 v[80:83], v2 offset:0
ds_read_b128 v[84:87], v2 offset:1024
ds_read_b128 v[88:91], v2 offset:2048
ds_read_b128 v[92:95], v2 offset:3072
ds_read_b128 v[96:99], v2 offset:4096
ds_read_b128 v[100:103], v2 offset:5120
s_and_b32 s59, s47, 64
s_add_u32 s63, s47, s59
s_lshl_b32 s63, s63, 7
s_add_u32 s63, s63, 1310720
s_lshl_b32 s59, s46, 15
s_add_u32 s59, s59, s63
s_add_u32 s14, s40, s59
s_addc_u32 s15, s41, 0
s_lshl_b32 s12, s47, 10
s_add_u32 s12, s12, 2384128
s_waitcnt lgkmcnt(0)
s_and_b32 s59, s47, 63
s_cbranch_scc1 hw_timed_%=
s_add_u32 s75, s47, 6
s_barrier
s_memrealtime s[56:57]
s_waitcnt lgkmcnt(0)
s_add_u32 s62, s56, s68
s_add_u32 s59, s62, s67
v_mov_b32 v152, s59
s_add_u32 s59, s59, s66
v_mov_b32 v153, s59
s_nop 1
s_branch hw_go_%=
hw_timed_%=:
s_mov_b32 s62, s17
s_cmp_lg_u32 s62, -1
s_cbranch_scc1 hw_rs_ok_%=
s_sub_u32 s55, s47, 1
s_and_b32 s59, s55, 64
s_add_u32 s54, s55, s59
s_lshl_b32 s54, s54, 7
s_add_u32 s54, s54, 1310720
s_mov_b32 s60, 0
hw_rs_%=:
buffer_load_dwordx2 v[152:153], v14, s[40:43], s54 offen sc1
s_waitcnt vmcnt(0)
v_readfirstlane_b32 s55, v152
v_readfirstlane_b32 s62, v153
s_nop 0
s_max_u32 s59, s55, s62
s_cmp_lg_u32 s59, -1
s_nop 1
s_cbranch_scc1 hw_rs_ok_%=
s_sleep 4
s_add_u32 s60, s60, 1
s_cmp_gt_u32 s60, 65536
s_cbranch_scc1 hw_abort_%=
s_and_b32 s59, s60, 63
s_cbranch_scc1 hw_rs_%=
v_mov_b32 v152, 1024
buffer_load_dword v152, v152, s[40:43], s58 offen sc1
s_waitcnt vmcnt(0)
v_readfirstlane_b32 s59, v152
s_nop 0
s_cmp_lg_u32 s59, 0
s_nop 2
s_cbranch_scc1 hw_end_%=
s_branch hw_rs_%=
hw_rs_ok_%=:
s_add_u32 s59, s62, s67
v_mov_b32 v152, s59
s_add_u32 s59, s59, s66
v_mov_b32 v153, s59
s_mov_b32 s60, 0
s_memrealtime s[56:57]
s_waitcnt lgkmcnt(0)
s_sub_u32 s59, s62, s56
s_cmp_gt_u32 s59, 1000
s_cbranch_scc1 hw_wu_go_%=
hw_wu_%=:
s_memrealtime s[56:57]
s_waitcnt lgkmcnt(0)
s_sub_u32 s59, s62, s56
s_cmp_gt_i32 s59, 15
s_cbranch_scc1 hw_wu_%=
s_memrealtime s[30:31]
s_nop 1
s_memrealtime s[18:19]
s_nop 1
s_memrealtime s[34:35]
s_nop 1
s_memrealtime s[36:37]
s_nop 1
s_memrealtime s[38:39]
s_nop 1
s_memrealtime s[94:95]
s_nop 1
s_memrealtime s[96:97]
s_nop 1
s_memrealtime s[98:99]
s_nop 1
s_getpc_b64 s[56:57]
hw_wu_pc_%=:
s_waitcnt lgkmcnt(0)
s_add_u32 s18, s18, s34
s_add_u32 s18, s18, s36
s_add_u32 s18, s18, s38
s_add_u32 s18, s18, s94
s_add_u32 s18, s18, s96
s_add_u32 s18, s18, s98
s_mul_i32 s34, s30, 7
s_sub_u32 s18, s18, s34
s_mul_i32 s18, s18, 3
s_sub_u32 s59, s62, s30
s_mul_i32 s59, s59, 23
s_sub_u32 s59, s59, s18
s_sub_u32 s59, s59, 170
s_max_i32 s59, s59, 0
s_min_i32 s59, s59, 252
s_andn2_b32 s59, s59, 3
s_sub_u32 s59, hw_wu_e_%=-hw_wu_pc_%=, s59
s_add_u32 s56, s56, s59
s_addc_u32 s57, s57, 0
s_setpc_b64 s[56:57]
s_nop 0
s_nop 0
s_nop 0
s_nop 0
s_nop 0
s_nop 0
)"
R"(s_nop 0
s_nop 0
s_nop 0
s_nop 0
s_nop 0
s_nop 0
s_nop 0
s_nop 0
s_nop 0
s_nop 0
s_nop 0
s_nop 0
s_nop 0
s_nop 0
s_nop 0
s_nop 0
s_nop 0
s_nop 0
s_nop 0
s_nop 0
s_nop 0
s_nop 0
s_nop 0
s_nop 0
s_nop 0
s_nop 0
s_nop 0
s_nop 0
s_nop 0
s_nop 0
s_nop 0
s_nop 0
s_nop 0
s_nop 0
s_nop 0
s_nop 0
s_nop 0
s_nop 0
s_nop 0
s_nop 0
s_nop 0
s_nop 0
s_nop 0
s_nop 0
s_nop 0
s_nop 0
s_nop 0
s_nop 0
s_nop 0
s_nop 0
s_nop 0
s_nop 0
s_nop 0
s_nop 0
s_nop 0
s_nop 0
s_nop 0
hw_wu_e_%=:
hw_wu_go_%=:
buffer_load_dwordx4 v[16:19], v158, s[40:43], s20 offen sc1
s_barrier
s_branch hw_second_%=
hw_go_%=:
s_mov_b32 s60, 0
hw_retrya_%=:
buffer_load_dwordx4 v[16:19], v158, s[40:43], s20 offen sc1
hw_second_%=:
s_nop 5
buffer_load_dwordx4 v[20:23], v158, s[40:43], s21 offen sc1
s_nop 5
buffer_load_dwordx4 v[24:27], v158, s[40:43], s22 offen sc1
s_nop 5
buffer_load_dwordx4 v[28:31], v158, s[40:43], s23 offen sc1
s_nop 5
buffer_load_dwordx4 v[32:35], v158, s[40:43], s24 offen sc1
s_nop 5
buffer_load_dwordx4 v[36:39], v158, s[40:43], s25 offen sc1
s_nop 5
buffer_load_dwordx4 v[40:43], v158, s[40:43], s26 offen sc1
s_nop 5
buffer_load_dwordx4 v[44:47], v158, s[40:43], s27 offen sc1
s_nop 5
buffer_load_dwordx4 v[48:51], v158, s[40:43], s28 offen sc1
s_nop 5
buffer_load_dwordx4 v[52:55], v158, s[40:43], s29 offen sc1
ds_read_b128 v[104:107], v5 offset:0
ds_read_b128 v[108:111], v5 offset:1024
ds_read_b128 v[112:115], v5 offset:2048
ds_read_b128 v[116:119], v5 offset:3072
ds_read_b128 v[120:123], v5 offset:4096
ds_read_b128 v[124:127], v5 offset:5120
ds_read_b128 v[128:131], v5 offset:6144
ds_read_b128 v[132:135], v5 offset:7168
s_waitcnt vmcnt(9)
v_smfmac_f32_16x16x64_bf16 v[68:71], v[16:19], v[80:87], v1
ds_read_b128 v[80:83], v2 offset:6144
v_smfmac_f32_16x16x64_bf16 v[56:59], v[16:19], a[0:7], v1
ds_read_b128 v[84:87], v2 offset:7168
v_smfmac_f32_16x16x64_bf16 v[64:67], v[16:19], a[8:15], v1
v_smfmac_f32_16x16x64_bf16 v[60:63], v[16:19], a[16:23], v1
s_waitcnt vmcnt(8)
v_smfmac_f32_16x16x64_bf16 v[68:71], v[20:23], v[88:95], v1
ds_read_b128 v[88:91], v2 offset:8192
v_smfmac_f32_16x16x64_bf16 v[56:59], v[20:23], a[24:31], v1
ds_read_b128 v[92:95], v2 offset:9216
v_smfmac_f32_16x16x64_bf16 v[64:67], v[20:23], v[96:103], v1
ds_read_b128 v[96:99], v2 offset:10240
v_smfmac_f32_16x16x64_bf16 v[60:63], v[20:23], a[32:39], v1
ds_read_b128 v[100:103], v2 offset:11264
s_waitcnt vmcnt(7) lgkmcnt(4)
v_smfmac_f32_16x16x64_bf16 v[68:71], v[24:27], v[80:87], v1
ds_read_b128 v[80:83], v2 offset:12288
v_smfmac_f32_16x16x64_bf16 v[56:59], v[24:27], a[40:47], v1
ds_read_b128 v[84:87], v2 offset:13312
v_smfmac_f32_16x16x64_bf16 v[64:67], v[24:27], a[48:55], v1
v_smfmac_f32_16x16x64_bf16 v[60:63], v[24:27], a[56:63], v1
v_max3_u32 v12, v16, v20, v24
s_waitcnt vmcnt(6) lgkmcnt(4)
v_smfmac_f32_16x16x64_bf16 v[68:71], v[28:31], v[88:95], v1
ds_read_b128 v[88:91], v2 offset:14336
v_smfmac_f32_16x16x64_bf16 v[56:59], v[28:31], a[64:71], v1
ds_read_b128 v[92:95], v2 offset:15360
v_smfmac_f32_16x16x64_bf16 v[64:67], v[28:31], a[72:79], v1
v_cmp_eq_u32 vcc, -1, v12
v_smfmac_f32_16x16x64_bf16 v[60:63], v[28:31], a[80:87], v1
s_cbranch_vccnz hw_polla_%=
s_waitcnt vmcnt(5) lgkmcnt(4)
v_smfmac_f32_16x16x64_bf16 v[68:71], v[32:35], v[96:103], v1
ds_read_b128 v[96:99], v2 offset:16384
v_smfmac_f32_16x16x64_bf16 v[56:59], v[32:35], a[88:95], v1
ds_read_b128 v[100:103], v2 offset:17408
s_waitcnt lgkmcnt(4)
v_smfmac_f32_16x16x64_bf16 v[64:67], v[32:35], v[80:87], v1
ds_read_b128 v[80:83], v2 offset:18432
v_smfmac_f32_16x16x64_bf16 v[60:63], v[32:35], a[96:103], v1
ds_read_b128 v[84:87], v2 offset:19456
s_waitcnt vmcnt(4) lgkmcnt(4)
v_smfmac_f32_16x16x64_bf16 v[68:71], v[36:39], v[88:95], v1
ds_read_b128 v[88:91], v2 offset:20480
v_smfmac_f32_16x16x64_bf16 v[56:59], v[36:39], a[104:111], v1
ds_read_b128 v[92:95], v2 offset:21504
v_smfmac_f32_16x16x64_bf16 v[64:67], v[36:39], a[112:119], v1
v_smfmac_f32_16x16x64_bf16 v[60:63], v[36:39], a[120:127], v1
s_waitcnt vmcnt(3) lgkmcnt(4)
v_smfmac_f32_16x16x64_bf16 v[68:71], v[40:43], v[96:103], v1
ds_read_b128 v[96:99], v2 offset:22528
v_smfmac_f32_16x16x64_bf16 v[56:59], v[40:43], a[128:135], v1
ds_read_b128 v[100:103], v2 offset:23552
v_smfmac_f32_16x16x64_bf16 v[64:67], v[40:43], a[136:143], v1
v_smfmac_f32_16x16x64_bf16 v[60:63], v[40:43], a[144:151], v1
s_waitcnt vmcnt(2) lgkmcnt(4)
v_smfmac_f32_16x16x64_bf16 v[68:71], v[44:47], v[80:87], v1
ds_read_b128 v[80:83], v2 offset:24576
v_smfmac_f32_16x16x64_bf16 v[56:59], v[44:47], a[152:159], v1
ds_read_b128 v[84:87], v2 offset:25600
s_waitcnt lgkmcnt(4)
v_smfmac_f32_16x16x64_bf16 v[64:67], v[44:47], v[88:95], v1
ds_read_b128 v[88:91], v2 offset:26624
v_smfmac_f32_16x16x64_bf16 v[60:63], v[44:47], a[160:167], v1
ds_read_b128 v[92:95], v2 offset:27648
s_waitcnt vmcnt(1) lgkmcnt(4)
v_smfmac_f32_16x16x64_bf16 v[68:71], v[48:51], v[96:103], v1
ds_read_b128 v[96:99], v2 offset:28672
v_smfmac_f32_16x16x64_bf16 v[56:59], v[48:51], a[168:175], v1
ds_read_b128 v[100:103], v2 offset:29696
v_smfmac_f32_16x16x64_bf16 v[64:67], v[48:51], a[176:183], v1
v_smfmac_f32_16x16x64_bf16 v[60:63], v[48:51], a[184:191], v1
s_waitcnt vmcnt(0) lgkmcnt(4)
v_smfmac_f32_16x16x64_bf16 v[68:71], v[52:55], v[80:87], v1
ds_read_b128 v[80:83], v2 offset:30720
v_smfmac_f32_16x16x64_bf16 v[56:59], v[52:55], a[192:199], v1
ds_read_b128 v[84:87], v2 offset:31744
v_smfmac_f32_16x16x64_bf16 v[64:67], v[52:55], a[200:207], v1
v_smfmac_f32_16x16x64_bf16 v[60:63], v[52:55], a[208:215], v1
v_smfmac_f32_16x16x64_bf16 v[72:75], v[16:19], a[216:223], v1
v_mov_b32 v13, v13
v_mov_b32 v13, v13
v_mov_b32 v13, v13
v_mov_b32 v13, v13
v_mov_b32 v13, v13
v_mov_b32 v13, v13
v_mov_b32 v13, v13
v_permlane32_swap_b32 v56, v60
v_permlane32_swap_b32 v57, v61
s_waitcnt lgkmcnt(4)
v_smfmac_f32_16x16x64_bf16 v[76:79], v[16:19], v[88:95], v1
v_permlane32_swap_b32 v58, v62
v_permlane32_swap_b32 v59, v63
v_smfmac_f32_16x16x64_bf16 v[72:75], v[20:23], a[224:231], v1
v_add_f32 v56, v56, v60
v_add_f32 v57, v57, v61
s_waitcnt lgkmcnt(2)
v_smfmac_f32_16x16x64_bf16 v[76:79], v[20:23], v[96:103], v1
v_add_f32 v58, v58, v62
v_add_f32 v59, v59, v63
v_smfmac_f32_16x16x64_bf16 v[72:75], v[24:27], a[232:239], v1
v_cmp_u_f32 vcc, v56, v57
v_cmp_u_f32 s[56:57], v58, v59
s_waitcnt lgkmcnt(0)
v_smfmac_f32_16x16x64_bf16 v[76:79], v[24:27], v[80:87], v1
)"
R"(s_or_b64 vcc, vcc, s[56:57]
s_cbranch_vccnz hw_faila_%=
hw_resumea_%=:
buffer_store_dwordx2 v[152:153], v7, s[40:43], s63 offen sc1
v_cvt_pk_bf16_f32 v60, v56, v57
v_smfmac_f32_16x16x64_bf16 v[72:75], v[28:31], a[240:247], v1
v_cvt_pk_bf16_f32 v61, v58, v59
ds_write_b64 v3, v[60:61] offset:0
v_smfmac_f32_16x16x64_bf16 v[76:79], v[28:31], a[248:255], v1
v_permlane32_swap_b32 v64, v68
v_permlane32_swap_b32 v65, v69
v_smfmac_f32_16x16x64_bf16 v[72:75], v[32:35], v[160:167], v1
v_permlane32_swap_b32 v66, v70
v_permlane32_swap_b32 v67, v71
v_smfmac_f32_16x16x64_bf16 v[76:79], v[32:35], v[168:175], v1
v_add_f32 v64, v64, v68
v_add_f32 v65, v65, v69
v_smfmac_f32_16x16x64_bf16 v[72:75], v[36:39], v[176:183], v1
v_add_f32 v66, v66, v70
v_add_f32 v67, v67, v71
v_smfmac_f32_16x16x64_bf16 v[76:79], v[36:39], v[184:191], v1
v_cvt_pk_bf16_f32 v68, v64, v65
v_cvt_pk_bf16_f32 v69, v66, v67
v_smfmac_f32_16x16x64_bf16 v[72:75], v[40:43], v[192:199], v1
ds_write_b64 v3, v[68:69] offset:512
v_smfmac_f32_16x16x64_bf16 v[76:79], v[40:43], v[200:207], v1
ds_read_b128 v[16:19], v5 offset:8192
v_smfmac_f32_16x16x64_bf16 v[72:75], v[44:47], v[208:215], v1
ds_read_b128 v[20:23], v5 offset:9216
v_smfmac_f32_16x16x64_bf16 v[76:79], v[44:47], v[216:223], v1
ds_read_b128 v[24:27], v5 offset:10240
buffer_load_dword v154, v155, s[40:43], s12 offen sc1
v_smfmac_f32_16x16x64_bf16 v[72:75], v[48:51], v[224:231], v1
ds_read_b128 v[28:31], v5 offset:11264
s_waitcnt lgkmcnt(4)
s_barrier
v_smfmac_f32_16x16x64_bf16 v[76:79], v[48:51], v[232:239], v1
ds_read_b64_tr_b16 v[136:137], v4 offset:0
ds_read_b64_tr_b16 v[138:139], v4 offset:512
v_smfmac_f32_16x16x64_bf16 v[72:75], v[52:55], v[240:247], v1
ds_read_b64_tr_b16 v[140:141], v4 offset:1536
ds_read_b64_tr_b16 v[142:143], v4 offset:2048
v_smfmac_f32_16x16x64_bf16 v[76:79], v[52:55], v[248:255], v1
ds_read_b64_tr_b16 v[144:145], v4 offset:3072
ds_read_b64_tr_b16 v[146:147], v4 offset:3584
ds_read_b64_tr_b16 v[148:149], v4 offset:4608
ds_read_b64_tr_b16 v[150:151], v4 offset:5120
v_mov_b32 v13, v13
s_waitcnt lgkmcnt(6)
v_mfma_f32_16x16x32_bf16 v[56:59], v[104:107], v[136:139], v[8:11]
v_mfma_f32_16x16x32_bf16 v[60:63], v[108:111], v[136:139], v[8:11]
v_permlane32_swap_b32 v72, v76
v_permlane32_swap_b32 v73, v77
v_permlane32_swap_b32 v74, v78
v_permlane32_swap_b32 v75, v79
v_pk_add_f32 v[72:73], v[72:73], v[76:77]
v_pk_add_f32 v[74:75], v[74:75], v[78:79]
v_cvt_pk_bf16_f32 v76, v72, v73
v_cvt_pk_bf16_f32 v77, v74, v75
ds_write_b64 v3, v[76:77] offset:1024
v_mfma_f32_16x16x32_bf16 v[64:67], v[136:139], v[136:139], 0
s_waitcnt lgkmcnt(5)
v_mfma_f32_16x16x32_bf16 v[56:59], v[112:115], v[140:143], v[56:59]
v_mfma_f32_16x16x32_bf16 v[60:63], v[116:119], v[140:143], v[60:63]
v_mfma_f32_16x16x32_bf16 v[64:67], v[140:143], v[140:143], v[64:67]
s_waitcnt lgkmcnt(3)
v_mfma_f32_16x16x32_bf16 v[56:59], v[120:123], v[144:147], v[56:59]
s_waitcnt lgkmcnt(0)
s_barrier
ds_read_b64_tr_b16 v[32:33], v4 offset:1024
ds_read_b64_tr_b16 v[34:35], v4 offset:2560
ds_read_b64_tr_b16 v[36:37], v4 offset:4096
ds_read_b64_tr_b16 v[38:39], v4 offset:5632
v_mfma_f32_16x16x32_bf16 v[60:63], v[124:127], v[144:147], v[60:63]
v_mfma_f32_16x16x32_bf16 v[64:67], v[144:147], v[144:147], v[64:67]
v_mfma_f32_16x16x32_bf16 v[56:59], v[128:131], v[148:151], v[56:59]
v_mfma_f32_16x16x32_bf16 v[60:63], v[132:135], v[148:151], v[60:63]
v_mfma_f32_16x16x32_bf16 v[64:67], v[148:151], v[148:151], v[64:67]
s_waitcnt lgkmcnt(2)
v_mfma_f32_16x16x32_bf16 v[64:67], v[32:35], v[32:35], v[64:67]
s_waitcnt lgkmcnt(0)
v_mfma_f32_16x16x32_bf16 v[64:67], v[36:39], v[36:39], v[64:67]
v_mfma_f32_16x16x32_bf16 v[56:59], v[16:19], v[32:35], v[56:59]
v_mfma_f32_16x16x32_bf16 v[56:59], v[24:27], v[36:39], v[56:59]
v_mfma_f32_16x16x32_bf16 v[60:63], v[20:23], v[32:35], v[60:63]
v_mfma_f32_16x16x32_bf16 v[60:63], v[28:31], v[36:39], v[60:63]
s_load_dwordx2 s[16:17], s[14:15], 0x0
v_mov_b32 v13, v13
v_mov_b32 v13, v13
v_mov_b32 v13, v13
v_mov_b32 v13, v13
v_mov_b32 v13, v13
v_mov_b32_dpp v56, v64 row_shr:8 row_mask:0xc bank_mask:0xc
v_mov_b32_dpp v57, v65 row_shr:8 row_mask:0xc bank_mask:0xc
v_mov_b32_dpp v58, v66 row_shr:8 row_mask:0xc bank_mask:0xc
v_mov_b32_dpp v59, v67 row_shr:8 row_mask:0xc bank_mask:0xc
v_max3_f32 v69, v56, v57, v58
v_max3_f32 v68, v60, v61, v62
v_max3_f32 v68, v68, v63, v59
v_max_f32 v68, v68, v69
s_nop 1
v_max_f32_dpp v68, v68, v68 row_ror:8 row_mask:0xf bank_mask:0xf
v_mov_b32 v69, v68
s_nop 1
v_permlane16_swap_b32 v68, v69
v_max_f32 v68, v68, v69
v_mov_b32 v69, v68
s_nop 1
v_permlane32_swap_b32 v68, v69
v_max_f32 v68, v68, v69
v_mul_f32 v68, 0xbdd53b95, v68
v_pk_fma_f32 v[56:57], v[56:57], s[64:65], v[68:69] op_sel_hi:[1,1,0]
v_pk_fma_f32 v[58:59], v[58:59], s[64:65], v[68:69] op_sel_hi:[1,1,0]
v_pk_fma_f32 v[60:61], v[60:61], s[64:65], v[68:69] op_sel_hi:[1,1,0]
v_pk_fma_f32 v[62:63], v[62:63], s[64:65], v[68:69] op_sel_hi:[1,1,0]
v_exp_f32 v56, v56
v_exp_f32 v57, v57
v_exp_f32 v58, v58
v_exp_f32 v59, v59
v_exp_f32 v60, v60
v_exp_f32 v61, v61
v_exp_f32 v62, v62
v_exp_f32 v63, v63
v_pk_add_f32 v[64:65], v[56:57], v[58:59]
v_pk_add_f32 v[66:67], v[60:61], v[62:63]
v_pk_add_f32 v[64:65], v[64:65], v[66:67]
v_add_f32 v68, v64, v65
s_nop 1
v_add_f32_dpp v68, v68, v68 row_ror:8 row_mask:0xf bank_mask:0xf
v_mov_b32 v69, v68
s_nop 1
v_permlane16_swap_b32 v68, v69
v_add_f32 v68, v68, v69
v_mov_b32 v69, v68
s_nop 1
v_permlane32_swap_b32 v68, v69
v_add_f32 v68, v68, v69
v_rcp_f32 v68, v68
s_nop 0
v_pk_mul_f32 v[56:57], v[56:57], v[68:69] op_sel_hi:[1,0]
v_pk_mul_f32 v[58:59], v[58:59], v[68:69] op_sel_hi:[1,0]
v_pk_mul_f32 v[60:61], v[60:61], v[68:69] op_sel_hi:[1,0]
v_pk_mul_f32 v[62:63], v[62:63], v[68:69] op_sel_hi:[1,0]
v_cvt_pk_bf16_f32 v64, v56, v57
v_cvt_pk_bf16_f32 v66, v60, v61
v_cvt_pk_bf16_f32 v65, v58, v59
v_cvt_pk_bf16_f32 v67, v62, v63
s_mov_b64 exec, s[8:9]
v_permlane16_swap_b32 v64, v66
v_permlane16_swap_b32 v65, v67
s_mov_b64 exec, -1
buffer_store_dwordx4 v[64:67], v6, s[40:43], s53 offen sc1
buffer_store_dwordx4 v[64:67], v6, s[40:43], s50 offen sc1
buffer_store_dwordx4 v[64:67], v6, s[40:43], s51 offen sc1
buffer_store_dwordx4 v[64:67], v6, s[40:43], s52 offen sc1
s_memrealtime s[30:31]
s_nop 1
s_memrealtime s[18:19]
s_nop 1
s_memrealtime s[34:35]
s_nop 1
s_memrealtime s[36:37]
s_nop 1
s_memrealtime s[38:39]
s_nop 1
s_memrealtime s[94:95]
s_nop 1
s_memrealtime s[96:97]
s_nop 1
s_memrealtime s[98:99]
s_nop 1
s_waitcnt lgkmcnt(0)
s_add_u32 s18, s18, s34
s_add_u32 s18, s18, s36
s_add_u32 s18, s18, s38
s_add_u32 s18, s18, s94
s_add_u32 s18, s18, s96
s_add_u32 s18, s18, s98
s_mul_i32 s34, s30, 7
s_sub_u32 s18, s18, s34
s_mul_i32 s18, s18, 3
s_sub_u32 s59, s30, s62
s_mul_i32 s59, s59, 23
s_add_u32 s59, s59, s18
v_mov_b32 v13, s59
s_lshl_b32 s34, s47, 10
s_add_u32 s34, s34, 2386176
s_cmp_eq_u32 s60, 0
s_cselect_b32 s34, s34, 0x40000000
buffer_atomic_umax v13, v159, s[40:43], s34 offen
s_waitcnt vmcnt(5)
v_readlane_b32 s0, v154, 0
v_readlane_b32 s1, v154, 1
v_readlane_b32 s2, v154, 2
v_readlane_b32 s3, v154, 3
v_readlane_b32 s4, v154, 4
v_readlane_b32 s5, v154, 5
v_readlane_b32 s6, v154, 6
v_readlane_b32 s7, v154, 7
s_max_u32 s0, s0, s1
)"
R"(s_max_u32 s0, s0, s2
s_max_u32 s0, s0, s3
s_sub_u32 s59, s0, s10
s_add_u32 s57, s59, 500
s_cmp_lt_u32 s57, 900
s_cselect_b32 s59, s59, 0
s_ashr_i32 s57, s59, 4
s_max_i32 s59, s59, s57
s_min_i32 s59, s59, s73
s_add_u32 s10, s10, s59
s_add_u32 s59, s10, s71
s_mul_i32 s59, s59, 2850
s_lshr_b32 s67, s59, 16
s_max_u32 s4, s4, s5
s_max_u32 s4, s4, s6
s_max_u32 s4, s4, s7
s_sub_u32 s59, s4, s11
s_add_u32 s57, s59, 500
s_cmp_lt_u32 s57, 900
s_cselect_b32 s59, s59, 0
s_ashr_i32 s57, s59, 4
s_max_i32 s59, s59, s57
s_min_i32 s59, s59, s73
s_add_u32 s11, s11, s59
s_add_u32 s59, s11, s72
s_mul_i32 s59, s59, 2850
s_lshr_b32 s66, s59, 16
v_add_u32 v3, s61, v3
v_add_u32 v4, s61, v4
s_sub_u32 s61, 0, s61
s_add_u32 s47, s47, 1
s_cmp_lt_u32 s47, 128
s_cbranch_scc1 hw_loopa_%=
s_branch hw_end_%=
hw_faila_%=:
v_max_u32 v12, v16, v17
v_max3_u32 v12, v18, v19, v12
v_max3_u32 v12, v20, v21, v12
v_max3_u32 v12, v22, v23, v12
v_max3_u32 v12, v24, v25, v12
v_max3_u32 v12, v26, v27, v12
v_max3_u32 v12, v28, v29, v12
v_max3_u32 v12, v30, v31, v12
v_max3_u32 v12, v32, v33, v12
v_max3_u32 v12, v34, v35, v12
v_max3_u32 v12, v36, v37, v12
v_max3_u32 v12, v38, v39, v12
v_max3_u32 v12, v40, v41, v12
v_max3_u32 v12, v42, v43, v12
v_max3_u32 v12, v44, v45, v12
v_max3_u32 v12, v46, v47, v12
v_max3_u32 v12, v48, v49, v12
v_max3_u32 v12, v50, v51, v12
v_max3_u32 v12, v52, v53, v12
v_max3_u32 v12, v54, v55, v12
v_cmp_eq_u32 vcc, -1, v12
s_nop 4
s_cbranch_vccz hw_resumea_%=
hw_polla_%=:
s_nop 7
s_waitcnt vmcnt(0) lgkmcnt(0)
v_mov_b32 v56, 0
v_mov_b32 v57, 0
v_mov_b32 v58, 0
v_mov_b32 v59, 0
v_mov_b32 v60, 0
v_mov_b32 v61, 0
v_mov_b32 v62, 0
v_mov_b32 v63, 0
v_mov_b32 v64, 0
v_mov_b32 v65, 0
v_mov_b32 v66, 0
v_mov_b32 v67, 0
v_mov_b32 v68, 0
v_mov_b32 v69, 0
v_mov_b32 v70, 0
v_mov_b32 v71, 0
v_mov_b32 v72, 0
v_mov_b32 v73, 0
v_mov_b32 v74, 0
v_mov_b32 v75, 0
v_mov_b32 v76, 0
v_mov_b32 v77, 0
v_mov_b32 v78, 0
v_mov_b32 v79, 0
ds_read_b128 v[80:83], v2 offset:0
ds_read_b128 v[84:87], v2 offset:1024
ds_read_b128 v[88:91], v2 offset:2048
ds_read_b128 v[92:95], v2 offset:3072
ds_read_b128 v[96:99], v2 offset:4096
ds_read_b128 v[100:103], v2 offset:5120
v_sub_u32 v12, v158, v0
v_lshl_add_u32 v12, v15, 7, v12
v_and_b32 v18, 15, v15
v_sub_u32 v18, v18, v15
v_lshl_add_u32 v18, v18, 7, v12
s_movk_i32 s55, 0x4000
s_waitcnt vmcnt(0) lgkmcnt(0)
s_add_u32 s60, s60, 1
s_cmp_gt_u32 s60, 65536
s_cbranch_scc1 hw_abort_%=
s_and_b32 s59, s60, 63
s_cbranch_scc1 hw_againa_%=
v_mov_b32 v13, 1024
buffer_load_dword v13, v13, s[40:43], s58 offen sc1
s_waitcnt vmcnt(0)
v_readfirstlane_b32 s59, v13
s_nop 0
s_cmp_lg_u32 s59, 0
s_nop 2
s_cbranch_scc1 hw_end_%=
s_branch hw_againa_%=
hw_againa_%=:
buffer_load_dword v16, v12, s[40:43], 0 offen sc1
buffer_load_dword v17, v18, s[40:43], s55 offen sc1
s_waitcnt vmcnt(0)
v_max_u32 v16, v16, v17
v_cmp_eq_u32 vcc, -1, v16
s_nop 4
s_cbranch_vccz hw_relocka_%=
s_add_u32 s60, s60, 1
s_cmp_gt_u32 s60, 65536
s_cbranch_scc1 hw_abort_%=
s_and_b32 s59, s60, 63
s_cbranch_scc1 hw_againa_%=
v_mov_b32 v13, 1024
buffer_load_dword v13, v13, s[40:43], s58 offen sc1
s_waitcnt vmcnt(0)
v_readfirstlane_b32 s59, v13
s_nop 0
s_cmp_lg_u32 s59, 0
s_nop 2
s_cbranch_scc1 hw_end_%=
s_branch hw_againa_%=
hw_relocka_%=:
s_memrealtime s[56:57]
s_waitcnt lgkmcnt(0)
s_cmp_lt_u32 s47, s75
s_cbranch_scc1 hw_nobump_%=
s_add_u32 s71, s71, 6
s_add_u32 s72, s72, 6
hw_nobump_%=:
s_add_u32 s59, s47, 2
s_max_u32 s75, s75, s59
s_add_u32 s62, s56, s68
s_add_u32 s59, s62, s67
v_mov_b32 v152, s59
s_add_u32 s59, s59, s66
v_mov_b32 v153, s59
s_nop 1
s_branch hw_retrya_%=
hw_wx_%=:
v_lshrrev_b32 v6, 3, v15
v_lshlrev_b32 v6, 8, v6
v_add_u32 v6, s50, v6
v_mov_b32 v13, s51
v_cmp_gt_u32 vcc, 32, v15
s_nop 1
v_cndmask_b32 v6, v13, v6, vcc
v_lshl_add_u32 v6, v153, 4, v6
v_and_b32 v13, 0xffffe000, v6
v_add_u32 v6, v6, v13
v_cmp_gt_u32 vcc, 40, v15
v_mov_b32 v13, 2147483632
s_nop 0
v_cndmask_b32 v6, v13, v6, vcc
v_lshlrev_b32 v7, 7, v152
v_lshl_add_u32 v7, v153, 4, v7
v_cmp_eq_u32 vcc, 1, v154
v_mov_b32 v13, 2147483632
s_nop 0
v_cndmask_b32 v7, v13, v7, vcc
s_cmp_eq_u32 s45, 1
s_cselect_b64 vcc, -1, 0
v_cndmask_b32 v6, v6, v7, vcc
s_cmp_eq_u32 s45, 1
s_cselect_b32 s59, 1, 0
v_lshlrev_b32 v133, 7, v15
v_cmp_gt_u32 vcc, s59, v15
v_mov_b32 v13, 2147483632
s_nop 0
v_cndmask_b32 v133, v13, v133, vcc
s_lshl_b32 s64, s44, 7
s_add_u32 s64, s64, 2103296
s_lshl_b32 s49, s45, 10
s_sub_u32 s49, s49, 1024
s_cmp_eq_u32 s45, 3
s_cselect_b32 s49, 8192, s49
s_cselect_b32 s48, 8, 2048
s_cselect_b32 s30, 1024, 8
v_add_u32 v129, s49, v5
v_add_u32 v130, s48, v129
s_cselect_b32 s48, 2048, 0
s_sub_u32 s48, 4096, s48
v_add_u32 v131, s48, v129
v_add_u32 v132, s48, v130
v_mov_b32 v104, -1
v_mov_b32 v105, -1
v_mov_b32 v106, -1
v_mov_b32 v107, -1
)"
R"(v_mov_b32 v108, -1
v_mov_b32 v109, -1
v_mov_b32 v110, -1
v_mov_b32 v111, -1
v_mov_b32 v112, -1
v_mov_b32 v113, -1
v_mov_b32 v114, -1
v_mov_b32 v115, -1
v_mov_b32 v116, -1
v_mov_b32 v117, -1
v_mov_b32 v118, -1
v_mov_b32 v119, -1
v_mov_b32 v120, -1
v_mov_b32 v121, -1
v_mov_b32 v122, -1
v_mov_b32 v123, -1
v_mov_b32 v124, -1
v_mov_b32 v125, -1
v_mov_b32 v126, -1
v_mov_b32 v127, -1
v_mov_b32 v8, 0
v_add_u32 v10, 6144, v0
s_mov_b32 s65, 32
s_mov_b32 s63, 0x40000000
hw_first_%=:
s_mul_i32 s62, s44, 12288
s_lshl_b32 s49, s45, 10
s_add_u32 s62, s62, s49
s_add_u32 s62, s62, 7781376
s_mov_b32 s31, s62
s_lshl_b32 s49, s47, 7
s_add_u32 s49, s49, 10240
v_mov_b32 v128, s49
v_mov_b32 v14, 0
s_lshl_b32 s49, s44, 7
s_add_u32 s19, s49, 278784
v_mov_b32 v134, s19
s_lshr_b32 s49, s47, 5
s_add_u32 s49, s49, 1
v_cmp_gt_u32 vcc, s49, v14
s_cbranch_vccz hw_kx0_ok_%=
s_mov_b32 s60, 0
s_nop 2
hw_kx0_poll_%=:
buffer_load_dword v14, v134, s[40:43], s58 offen sc1
s_waitcnt vmcnt(0)
v_cmp_gt_u32 vcc, s49, v14
s_nop 4
s_cbranch_vccz hw_kx0_ok_%=
s_sleep 3
s_add_u32 s60, s60, 1
s_cmp_gt_u32 s60, 65536
s_cbranch_scc1 hw_abort_%=
s_and_b32 s59, s60, 63
s_cbranch_scc1 hw_kx0_poll_%=
v_mov_b32 v13, 1024
buffer_load_dword v13, v13, s[40:43], s58 offen sc1
s_waitcnt vmcnt(0)
v_readfirstlane_b32 s59, v13
s_nop 0
s_cmp_lg_u32 s59, 0
s_nop 2
s_cbranch_scc1 hw_end_%=
s_branch hw_kx0_poll_%=
hw_kx0_ok_%=:
buffer_load_dwordx4 v[136:139], v0, s[40:43], s62 offen sc1
buffer_load_dwordx4 v[140:143], v0, s[40:43], s62 offen offset:3072 sc1
buffer_load_dwordx4 v[144:147], v10, s[40:43], s62 offen sc1
buffer_load_dwordx4 v[148:151], v10, s[40:43], s62 offen offset:3072 sc1
v_add_u32 v9, s30, v129
s_waitcnt vmcnt(3)
ds_write_b64 v129, v[136:137]
ds_write_b64 v9, v[138:139]
v_add_u32 v9, s30, v130
s_waitcnt vmcnt(2)
ds_write_b64 v130, v[140:141]
ds_write_b64 v9, v[142:143]
v_add_u32 v9, s30, v131
s_waitcnt vmcnt(1)
ds_write_b64 v131, v[144:145]
ds_write_b64 v9, v[146:147]
v_add_u32 v9, s30, v132
s_waitcnt vmcnt(0)
ds_write_b64 v132, v[148:149]
ds_write_b64 v9, v[150:151]
v_add_u32 v128, 128, v128
v_mov_b32 v14, 0
s_nop 1
hw_loopb_%=:
s_add_u32 s59, s47, 0
s_and_b32 s59, s59, 63
s_cmp_eq_u32 s59, 31
s_cselect_b32 s59, -31, 1
s_mul_i32 s57, s59, 393216
s_add_u32 s31, s31, s57
v_cmp_gt_u32 vcc, s65, v14
s_nop 0
s_cmp_eq_u64 vcc, 0
s_cselect_b32 s62, s31, 0x40000000
v_mov_b32 v134, v128
v_add_u32 v128, 128, v128
s_and_b32 s48, s47, 3
s_mul_i32 s53, s48, 196608
s_lshl_b32 s50, s48, 2
s_add_u32 s50, s50, s46
s_mul_i32 s50, s50, 32768
s_add_u32 s50, s50, 786432
v_add_u32 v158, s50, v0
s_add_u32 s50, s53, 49152
s_add_u32 s51, s53, 98304
s_add_u32 s52, s53, 147456
v_mov_b32 v56, 0
v_mov_b32 v57, 0
v_mov_b32 v58, 0
v_mov_b32 v59, 0
v_mov_b32 v60, 0
v_mov_b32 v61, 0
v_mov_b32 v62, 0
v_mov_b32 v63, 0
v_mov_b32 v64, 0
v_mov_b32 v65, 0
v_mov_b32 v66, 0
v_mov_b32 v67, 0
v_mov_b32 v68, 0
v_mov_b32 v69, 0
v_mov_b32 v70, 0
v_mov_b32 v71, 0
v_mov_b32 v72, 0
v_mov_b32 v73, 0
v_mov_b32 v74, 0
v_mov_b32 v75, 0
v_mov_b32 v76, 0
v_mov_b32 v77, 0
v_mov_b32 v78, 0
v_mov_b32 v79, 0
ds_read_b128 v[80:83], v2 offset:0
ds_read_b128 v[84:87], v2 offset:1024
ds_read_b128 v[88:91], v2 offset:2048
ds_read_b128 v[92:95], v2 offset:3072
ds_read_b128 v[96:99], v2 offset:4096
ds_read_b128 v[100:103], v2 offset:5120
s_xor_b32 s48, s48, 2
s_mul_i32 s54, s48, 196608
s_and_b32 s63, s47, 31
s_lshl_b32 s63, s63, 5
s_add_u32 s63, s63, s44
s_mul_i32 s63, s63, 3072
s_add_u32 s63, s63, 127336448
s_cmp_eq_u32 s45, 1
s_cselect_b32 s63, s63, 0x40000000
s_movk_i32 s49, 512
s_cmp_eq_u32 s45, 2
s_cselect_b32 s48, 49152, s49
s_cselect_b32 s34, s54, s63
s_cselect_b32 s49, 0x40000000, 0
s_add_u32 s35, s34, s48
s_add_u32 s36, s35, s48
s_add_u32 s37, s36, s48
s_add_u32 s38, s37, s48
s_add_u32 s39, s38, s48
s_or_b32 s38, s38, s49
s_or_b32 s39, s39, s49
s_waitcnt lgkmcnt(0)
s_barrier
s_mov_b32 s60, 0
hw_retryb_%=:
buffer_load_dwordx4 v[16:19], v158, s[40:43], s20 offen sc1
s_nop 7
buffer_load_dwordx4 v[20:23], v158, s[40:43], s21 offen sc1
s_nop 7
buffer_load_dwordx4 v[24:27], v158, s[40:43], s22 offen sc1
s_nop 7
buffer_load_dwordx4 v[28:31], v158, s[40:43], s23 offen sc1
s_nop 7
buffer_load_dwordx4 v[32:35], v158, s[40:43], s24 offen sc1
s_nop 7
buffer_load_dwordx4 v[36:39], v158, s[40:43], s25 offen sc1
s_nop 7
buffer_load_dwordx4 v[40:43], v158, s[40:43], s26 offen sc1
s_nop 7
buffer_load_dwordx4 v[44:47], v158, s[40:43], s27 offen sc1
s_nop 7
buffer_load_dwordx4 v[48:51], v158, s[40:43], s28 offen sc1
s_nop 7
buffer_load_dwordx4 v[52:55], v158, s[40:43], s29 offen sc1
s_waitcnt vmcnt(9)
v_smfmac_f32_16x16x64_bf16 v[68:71], v[16:19], v[80:87], v1
ds_read_b128 v[80:83], v2 offset:6144
v_smfmac_f32_16x16x64_bf16 v[56:59], v[16:19], a[0:7], v1
ds_read_b128 v[84:87], v2 offset:7168
v_smfmac_f32_16x16x64_bf16 v[64:67], v[16:19], a[8:15], v1
v_smfmac_f32_16x16x64_bf16 v[60:63], v[16:19], a[16:23], v1
buffer_store_dword v8, v133, s[40:43], s64 offen sc1
s_waitcnt vmcnt(9)
v_smfmac_f32_16x16x64_bf16 v[68:71], v[20:23], v[88:95], v1
ds_read_b128 v[88:91], v2 offset:8192
v_smfmac_f32_16x16x64_bf16 v[56:59], v[20:23], a[24:31], v1
ds_read_b128 v[92:95], v2 offset:9216
v_smfmac_f32_16x16x64_bf16 v[64:67], v[20:23], v[96:103], v1
ds_read_b128 v[96:99], v2 offset:10240
)"
R"(v_smfmac_f32_16x16x64_bf16 v[60:63], v[20:23], a[32:39], v1
ds_read_b128 v[100:103], v2 offset:11264
s_waitcnt vmcnt(8) lgkmcnt(4)
v_smfmac_f32_16x16x64_bf16 v[68:71], v[24:27], v[80:87], v1
ds_read_b128 v[80:83], v2 offset:12288
v_smfmac_f32_16x16x64_bf16 v[56:59], v[24:27], a[40:47], v1
ds_read_b128 v[84:87], v2 offset:13312
v_smfmac_f32_16x16x64_bf16 v[64:67], v[24:27], a[48:55], v1
v_smfmac_f32_16x16x64_bf16 v[60:63], v[24:27], a[56:63], v1
v_max3_u32 v12, v16, v20, v24
s_waitcnt vmcnt(7) lgkmcnt(4)
v_smfmac_f32_16x16x64_bf16 v[68:71], v[28:31], v[88:95], v1
ds_read_b128 v[88:91], v2 offset:14336
v_smfmac_f32_16x16x64_bf16 v[56:59], v[28:31], a[64:71], v1
ds_read_b128 v[92:95], v2 offset:15360
v_smfmac_f32_16x16x64_bf16 v[64:67], v[28:31], a[72:79], v1
v_cmp_eq_u32 vcc, -1, v12
v_smfmac_f32_16x16x64_bf16 v[60:63], v[28:31], a[80:87], v1
s_cbranch_vccnz hw_pollb_%=
s_waitcnt vmcnt(6) lgkmcnt(4)
v_smfmac_f32_16x16x64_bf16 v[68:71], v[32:35], v[96:103], v1
ds_read_b128 v[96:99], v2 offset:16384
v_smfmac_f32_16x16x64_bf16 v[56:59], v[32:35], a[88:95], v1
ds_read_b128 v[100:103], v2 offset:17408
s_waitcnt lgkmcnt(4)
v_smfmac_f32_16x16x64_bf16 v[64:67], v[32:35], v[80:87], v1
ds_read_b128 v[80:83], v2 offset:18432
v_smfmac_f32_16x16x64_bf16 v[60:63], v[32:35], a[96:103], v1
ds_read_b128 v[84:87], v2 offset:19456
s_waitcnt vmcnt(5) lgkmcnt(4)
v_smfmac_f32_16x16x64_bf16 v[68:71], v[36:39], v[88:95], v1
ds_read_b128 v[88:91], v2 offset:20480
v_smfmac_f32_16x16x64_bf16 v[56:59], v[36:39], a[104:111], v1
ds_read_b128 v[92:95], v2 offset:21504
v_smfmac_f32_16x16x64_bf16 v[64:67], v[36:39], a[112:119], v1
buffer_load_dwordx4 v[136:139], v0, s[40:43], s62 offen offset:0 sc1
v_smfmac_f32_16x16x64_bf16 v[60:63], v[36:39], a[120:127], v1
buffer_load_dwordx4 v[140:143], v0, s[40:43], s62 offen offset:3072 sc1
s_waitcnt vmcnt(6) lgkmcnt(4)
v_smfmac_f32_16x16x64_bf16 v[68:71], v[40:43], v[96:103], v1
ds_read_b128 v[96:99], v2 offset:22528
v_smfmac_f32_16x16x64_bf16 v[56:59], v[40:43], a[128:135], v1
ds_read_b128 v[100:103], v2 offset:23552
v_smfmac_f32_16x16x64_bf16 v[64:67], v[40:43], a[136:143], v1
buffer_load_dwordx4 v[144:147], v10, s[40:43], s62 offen offset:0 sc1
v_smfmac_f32_16x16x64_bf16 v[60:63], v[40:43], a[144:151], v1
buffer_load_dwordx4 v[148:151], v10, s[40:43], s62 offen offset:3072 sc1
s_waitcnt vmcnt(7) lgkmcnt(4)
v_smfmac_f32_16x16x64_bf16 v[68:71], v[44:47], v[80:87], v1
ds_read_b128 v[80:83], v2 offset:24576
v_smfmac_f32_16x16x64_bf16 v[56:59], v[44:47], a[152:159], v1
ds_read_b128 v[84:87], v2 offset:25600
s_waitcnt lgkmcnt(4)
v_smfmac_f32_16x16x64_bf16 v[64:67], v[44:47], v[88:95], v1
ds_read_b128 v[88:91], v2 offset:26624
v_smfmac_f32_16x16x64_bf16 v[60:63], v[44:47], a[160:167], v1
ds_read_b128 v[92:95], v2 offset:27648
s_waitcnt vmcnt(6) lgkmcnt(4)
v_smfmac_f32_16x16x64_bf16 v[68:71], v[48:51], v[96:103], v1
ds_read_b128 v[96:99], v2 offset:28672
v_smfmac_f32_16x16x64_bf16 v[56:59], v[48:51], a[168:175], v1
ds_read_b128 v[100:103], v2 offset:29696
v_smfmac_f32_16x16x64_bf16 v[64:67], v[48:51], a[176:183], v1
buffer_load_dword v14, v128, s[40:43], s58 offen sc1
v_smfmac_f32_16x16x64_bf16 v[60:63], v[48:51], a[184:191], v1
s_waitcnt vmcnt(6) lgkmcnt(4)
v_smfmac_f32_16x16x64_bf16 v[68:71], v[52:55], v[80:87], v1
ds_read_b128 v[80:83], v2 offset:30720
v_smfmac_f32_16x16x64_bf16 v[56:59], v[52:55], a[192:199], v1
ds_read_b128 v[84:87], v2 offset:31744
v_smfmac_f32_16x16x64_bf16 v[64:67], v[52:55], a[200:207], v1
v_smfmac_f32_16x16x64_bf16 v[60:63], v[52:55], a[208:215], v1
v_smfmac_f32_16x16x64_bf16 v[72:75], v[16:19], a[216:223], v1
v_mov_b32 v13, v13
v_mov_b32 v13, v13
v_mov_b32 v13, v13
v_mov_b32 v13, v13
v_mov_b32 v13, v13
v_mov_b32 v13, v13
v_mov_b32 v13, v13
v_permlane32_swap_b32 v56, v60
v_permlane32_swap_b32 v57, v61
s_waitcnt lgkmcnt(4)
v_smfmac_f32_16x16x64_bf16 v[76:79], v[16:19], v[88:95], v1
v_permlane32_swap_b32 v58, v62
v_permlane32_swap_b32 v59, v63
v_smfmac_f32_16x16x64_bf16 v[72:75], v[20:23], a[224:231], v1
v_add_f32 v56, v56, v60
v_add_f32 v57, v57, v61
s_waitcnt lgkmcnt(2)
v_smfmac_f32_16x16x64_bf16 v[76:79], v[20:23], v[96:103], v1
v_add_f32 v58, v58, v62
v_add_f32 v59, v59, v63
v_smfmac_f32_16x16x64_bf16 v[72:75], v[24:27], a[232:239], v1
v_cmp_u_f32 vcc, v56, v57
v_cmp_u_f32 s[56:57], v58, v59
s_waitcnt lgkmcnt(0)
v_smfmac_f32_16x16x64_bf16 v[76:79], v[24:27], v[80:87], v1
s_or_b64 vcc, vcc, s[56:57]
s_cbranch_vccnz hw_failb_%=
hw_resumeb_%=:
v_cvt_pk_bf16_f32 v60, v56, v57
v_smfmac_f32_16x16x64_bf16 v[72:75], v[28:31], a[240:247], v1
v_cvt_pk_bf16_f32 v61, v58, v59
ds_write_b64 v3, v[60:61] offset:0
v_smfmac_f32_16x16x64_bf16 v[76:79], v[28:31], a[248:255], v1
v_permlane32_swap_b32 v64, v68
v_permlane32_swap_b32 v65, v69
v_smfmac_f32_16x16x64_bf16 v[72:75], v[32:35], v[160:167], v1
v_permlane32_swap_b32 v66, v70
v_permlane32_swap_b32 v67, v71
v_smfmac_f32_16x16x64_bf16 v[76:79], v[32:35], v[168:175], v1
v_add_f32 v64, v64, v68
v_add_f32 v65, v65, v69
v_smfmac_f32_16x16x64_bf16 v[72:75], v[36:39], v[176:183], v1
v_add_f32 v66, v66, v70
v_add_f32 v67, v67, v71
v_smfmac_f32_16x16x64_bf16 v[76:79], v[36:39], v[184:191], v1
v_cvt_pk_bf16_f32 v68, v64, v65
v_cvt_pk_bf16_f32 v69, v66, v67
v_smfmac_f32_16x16x64_bf16 v[72:75], v[40:43], v[192:199], v1
ds_write_b64 v3, v[68:69] offset:512
v_smfmac_f32_16x16x64_bf16 v[76:79], v[40:43], v[200:207], v1
v_smfmac_f32_16x16x64_bf16 v[72:75], v[44:47], v[208:215], v1
v_smfmac_f32_16x16x64_bf16 v[76:79], v[44:47], v[216:223], v1
v_smfmac_f32_16x16x64_bf16 v[72:75], v[48:51], v[224:231], v1
s_waitcnt lgkmcnt(0)
s_barrier
v_smfmac_f32_16x16x64_bf16 v[76:79], v[48:51], v[232:239], v1
v_smfmac_f32_16x16x64_bf16 v[72:75], v[52:55], v[240:247], v1
v_smfmac_f32_16x16x64_bf16 v[76:79], v[52:55], v[248:255], v1
v_mov_b32 v13, v13
v_mov_b32 v13, v13
v_mov_b32 v13, v13
v_mov_b32 v13, v13
v_mov_b32 v13, v13
v_mov_b32 v13, v13
v_mov_b32 v13, v13
v_mov_b32 v13, v13
v_permlane32_swap_b32 v72, v76
v_permlane32_swap_b32 v73, v77
v_permlane32_swap_b32 v74, v78
v_permlane32_swap_b32 v75, v79
v_pk_add_f32 v[72:73], v[72:73], v[76:77]
v_pk_add_f32 v[74:75], v[74:75], v[78:79]
v_cvt_pk_bf16_f32 v76, v72, v73
v_cvt_pk_bf16_f32 v77, v74, v75
ds_write_b64 v3, v[76:77] offset:1024
s_waitcnt lgkmcnt(0)
s_barrier
s_cmp_lg_u32 s45, 1
s_cbranch_scc1 hw_n1_%=
ds_read_b64_tr_b16 v[104:105], v4 offset:0
ds_read_b64_tr_b16 v[106:107], v4 offset:512
ds_read_b64_tr_b16 v[108:109], v4 offset:1536
ds_read_b64_tr_b16 v[110:111], v4 offset:2048
ds_read_b64_tr_b16 v[112:113], v4 offset:3072
ds_read_b64_tr_b16 v[114:115], v4 offset:3584
ds_read_b64_tr_b16 v[116:117], v4 offset:4608
ds_read_b64_tr_b16 v[118:119], v4 offset:5120
ds_read_b64_tr_b16 v[120:121], v4 offset:1024
ds_read_b64_tr_b16 v[122:123], v4 offset:2560
ds_read_b64_tr_b16 v[124:125], v4 offset:4096
ds_read_b64_tr_b16 v[126:127], v4 offset:5632
s_waitcnt lgkmcnt(0)
hw_n1_%=:
buffer_store_dwordx4 v[104:107], v6, s[40:43], s34 offen sc1
buffer_store_dwordx4 v[108:111], v6, s[40:43], s35 offen sc1
buffer_store_dwordx4 v[112:115], v6, s[40:43], s36 offen sc1
buffer_store_dwordx4 v[116:119], v6, s[40:43], s37 offen sc1
buffer_store_dwordx4 v[120:123], v6, s[40:43], s38 offen sc1
buffer_store_dwordx4 v[124:127], v6, s[40:43], s39 offen sc1
v_add_u32 v9, s30, v129
s_waitcnt vmcnt(10)
ds_write_b64 v129, v[136:137]
ds_write_b64 v9, v[138:139]
v_add_u32 v9, s30, v130
s_waitcnt vmcnt(9)
ds_write_b64 v130, v[140:141]
ds_write_b64 v9, v[142:143]
v_add_u32 v9, s30, v131
s_waitcnt vmcnt(8)
ds_write_b64 v131, v[144:145]
ds_write_b64 v9, v[146:147]
v_add_u32 v9, s30, v132
s_waitcnt vmcnt(7)
ds_write_b64 v132, v[148:149]
ds_write_b64 v9, v[150:151]
s_add_u32 s49, s47, 1
v_mov_b32 v8, s49
s_waitcnt vmcnt(6)
v_add_u32 v3, s61, v3
v_add_u32 v4, s61, v4
s_sub_u32 s61, 0, s61
s_add_u32 s47, s47, 1
s_and_b32 s59, s47, 63
s_cbranch_scc0 hw_flush_%=
s_cmp_eq_u32 s62, s31
s_cbranch_scc1 hw_loopb_%=
v_mov_b32 v135, s19
)"
R"(s_lshr_b32 s49, s47, 5
s_add_u32 s49, s49, 2
s_and_b32 s59, s47, 63
s_cmp_eq_u32 s59, 1
s_cselect_b32 s49, s49, s65
s_cselect_b64 vcc, -1, 0
v_cndmask_b32 v134, v134, v135, vcc
v_mov_b32 v135, 0
v_cmp_gt_u32 vcc, s49, v135
s_cbranch_vccz hw_kx1_ok_%=
s_mov_b32 s60, 0
s_nop 2
hw_kx1_poll_%=:
buffer_load_dword v135, v134, s[40:43], s58 offen sc1
s_waitcnt vmcnt(0)
v_cmp_gt_u32 vcc, s49, v135
s_nop 4
s_cbranch_vccz hw_kx1_ok_%=
s_sleep 4
s_add_u32 s60, s60, 1
s_cmp_gt_u32 s60, 65536
s_cbranch_scc1 hw_abort_%=
s_and_b32 s59, s60, 63
s_cbranch_scc1 hw_kx1_poll_%=
v_mov_b32 v13, 1024
buffer_load_dword v13, v13, s[40:43], s58 offen sc1
s_waitcnt vmcnt(0)
v_readfirstlane_b32 s59, v13
s_nop 0
s_cmp_lg_u32 s59, 0
s_nop 2
s_cbranch_scc1 hw_end_%=
s_branch hw_kx1_poll_%=
hw_kx1_ok_%=:
buffer_load_dwordx4 v[136:139], v0, s[40:43], s31 offen sc1
buffer_load_dwordx4 v[140:143], v0, s[40:43], s31 offen offset:3072 sc1
buffer_load_dwordx4 v[144:147], v10, s[40:43], s31 offen sc1
buffer_load_dwordx4 v[148:151], v10, s[40:43], s31 offen offset:3072 sc1
v_add_u32 v9, s30, v129
s_waitcnt vmcnt(3)
ds_write_b64 v129, v[136:137]
ds_write_b64 v9, v[138:139]
v_add_u32 v9, s30, v130
s_waitcnt vmcnt(2)
ds_write_b64 v130, v[140:141]
ds_write_b64 v9, v[142:143]
v_add_u32 v9, s30, v131
s_waitcnt vmcnt(1)
ds_write_b64 v131, v[144:145]
ds_write_b64 v9, v[146:147]
v_add_u32 v9, s30, v132
s_waitcnt vmcnt(0)
ds_write_b64 v132, v[148:149]
ds_write_b64 v9, v[150:151]
s_branch hw_loopb_%=
hw_flush_%=:
s_waitcnt vmcnt(0)
buffer_store_dword v8, v133, s[40:43], s64 offen sc1
s_cmp_lt_u32 s47, 128
s_nop 0
s_cbranch_scc1 hw_first_%=
s_branch hw_end_%=
hw_failb_%=:
v_max_u32 v12, v16, v17
v_max3_u32 v12, v18, v19, v12
v_max3_u32 v12, v20, v21, v12
v_max3_u32 v12, v22, v23, v12
v_max3_u32 v12, v24, v25, v12
v_max3_u32 v12, v26, v27, v12
v_max3_u32 v12, v28, v29, v12
v_max3_u32 v12, v30, v31, v12
v_max3_u32 v12, v32, v33, v12
v_max3_u32 v12, v34, v35, v12
v_max3_u32 v12, v36, v37, v12
v_max3_u32 v12, v38, v39, v12
v_max3_u32 v12, v40, v41, v12
v_max3_u32 v12, v42, v43, v12
v_max3_u32 v12, v44, v45, v12
v_max3_u32 v12, v46, v47, v12
v_max3_u32 v12, v48, v49, v12
v_max3_u32 v12, v50, v51, v12
v_max3_u32 v12, v52, v53, v12
v_max3_u32 v12, v54, v55, v12
v_cmp_eq_u32 vcc, -1, v12
s_nop 4
s_cbranch_vccz hw_resumeb_%=
hw_pollb_%=:
s_nop 7
s_waitcnt vmcnt(0) lgkmcnt(0)
v_mov_b32 v56, 0
v_mov_b32 v57, 0
v_mov_b32 v58, 0
v_mov_b32 v59, 0
v_mov_b32 v60, 0
v_mov_b32 v61, 0
v_mov_b32 v62, 0
v_mov_b32 v63, 0
v_mov_b32 v64, 0
v_mov_b32 v65, 0
v_mov_b32 v66, 0
v_mov_b32 v67, 0
v_mov_b32 v68, 0
v_mov_b32 v69, 0
v_mov_b32 v70, 0
v_mov_b32 v71, 0
v_mov_b32 v72, 0
v_mov_b32 v73, 0
v_mov_b32 v74, 0
v_mov_b32 v75, 0
v_mov_b32 v76, 0
v_mov_b32 v77, 0
v_mov_b32 v78, 0
v_mov_b32 v79, 0
ds_read_b128 v[80:83], v2 offset:0
ds_read_b128 v[84:87], v2 offset:1024
ds_read_b128 v[88:91], v2 offset:2048
ds_read_b128 v[92:95], v2 offset:3072
ds_read_b128 v[96:99], v2 offset:4096
ds_read_b128 v[100:103], v2 offset:5120
v_sub_u32 v12, v158, v0
v_lshl_add_u32 v12, v15, 7, v12
v_and_b32 v18, 15, v15
v_sub_u32 v18, v18, v15
v_lshl_add_u32 v18, v18, 7, v12
s_movk_i32 s55, 0x4000
s_waitcnt vmcnt(0) lgkmcnt(0)
s_add_u32 s60, s60, 1
s_cmp_gt_u32 s60, 65536
s_cbranch_scc1 hw_abort_%=
s_and_b32 s59, s60, 63
s_cbranch_scc1 hw_againb_%=
v_mov_b32 v13, 1024
buffer_load_dword v13, v13, s[40:43], s58 offen sc1
s_waitcnt vmcnt(0)
v_readfirstlane_b32 s59, v13
s_nop 0
s_cmp_lg_u32 s59, 0
s_nop 2
s_cbranch_scc1 hw_end_%=
s_branch hw_againb_%=
hw_againb_%=:
buffer_load_dword v16, v12, s[40:43], 0 offen sc1
buffer_load_dword v17, v18, s[40:43], s55 offen sc1
s_waitcnt vmcnt(0)
v_max_u32 v16, v16, v17
v_cmp_eq_u32 vcc, -1, v16
s_nop 4
s_cbranch_vccz hw_retryb_%=
s_add_u32 s60, s60, 1
s_cmp_gt_u32 s60, 65536
s_cbranch_scc1 hw_abort_%=
s_and_b32 s59, s60, 63
s_cbranch_scc1 hw_againb_%=
v_mov_b32 v13, 1024
buffer_load_dword v13, v13, s[40:43], s58 offen sc1
s_waitcnt vmcnt(0)
v_readfirstlane_b32 s59, v13
s_nop 0
s_cmp_lg_u32 s59, 0
s_nop 2
s_cbranch_scc1 hw_end_%=
s_branch hw_againb_%=
s_branch hw_end_%=
hw_abort_%=:
v_mov_b32 v13, 1
v_mov_b32 v14, 1024
buffer_store_dword v13, v14, s[40:43], s58 offen sc1
hw_end_%=:
s_endpgm
)"
    :: ROLE_INPUTS : ROLE_CLOBBERS);
    __builtin_unreachable();
}
DEV void role_col(unsigned lo, unsigned hi, int id, int w, int aux, const Tune& tn, const void* wa, const void* wb, int bid) {
    asm volatile(
R"(s_mov_b32 s40, %0
s_mov_b32 s41, %1
s_mov_b32 s42, %2
s_mov_b32 s43, %3
s_mov_b32 s44, %4
s_mov_b32 s45, %5
s_mov_b32 s46, %6
s_mov_b32 s66, %7
s_mov_b32 s67, %8
s_mov_b32 s68, %9
s_mov_b32 s69, %10
s_mov_b32 s70, %11
s_mov_b32 s71, %12
s_mov_b32 s72, %13
s_mov_b32 s73, %14
s_mov_b32 s76, %15
s_mov_b32 s77, %16
s_mov_b32 s78, 7864320
s_mov_b32 s79, 0x20000
s_mov_b32 s80, %17
s_mov_b32 s81, %18
s_mov_b32 s82, 7864320
s_mov_b32 s83, 0x20000
s_mov_b32 s84, %19
s_mov_b64 exec, -1
v_mbcnt_lo_u32_b32 v15, -1, 0
v_mbcnt_hi_u32_b32 v15, -1, v15
v_lshlrev_b32 v0, 4, v15
s_mov_b32 s58, 2097152
s_mov_b32 s92, 0
s_lshr_b32 s85, s84, 2
s_and_b32 s86, s84, 3
v_add_u32 v26, 0, v15
v_mul_u32_u24 v27, 2731, v26
v_lshrrev_b32 v27, 16, v27
v_mul_u32_u24 v16, 11904, v27
v_lshl_add_u32 v16, v26, 4, v16
v_add_u32 v26, v26, v27
s_mul_i32 s48, s45, 3200
v_lshl_add_u32 v19, v26, 4, s48
v_add_u32 v26, 64, v15
v_mul_u32_u24 v27, 2731, v26
v_lshrrev_b32 v27, 16, v27
v_mul_u32_u24 v17, 11904, v27
v_lshl_add_u32 v17, v26, 4, v17
v_add_u32 v26, v26, v27
s_mul_i32 s48, s45, 3200
v_lshl_add_u32 v20, v26, 4, s48
v_add_u32 v26, 128, v15
v_mul_u32_u24 v27, 2731, v26
v_lshrrev_b32 v27, 16, v27
v_mul_u32_u24 v18, 11904, v27
v_lshl_add_u32 v18, v26, 4, v18
v_add_u32 v26, v26, v27
s_mul_i32 s48, s45, 3200
v_lshl_add_u32 v21, v26, 4, s48
s_mul_i32 s48, s86, 160
s_lshl3_add_u32 s48, s45, s48
s_mul_i32 s48, s48, 12288
s_mul_i32 s49, s85, 384
s_add_u32 s48, s48, s49
buffer_load_dwordx4 v[32:35], v16, s[80:83], s48 offen
buffer_load_dwordx4 v[36:39], v17, s[80:83], s48 offen
buffer_load_dwordx4 v[40:43], v18, s[80:83], s48 offen
s_add_u32 s48, s48, 393216
buffer_load_dwordx4 v[44:47], v16, s[80:83], s48 offen
buffer_load_dwordx4 v[48:51], v17, s[80:83], s48 offen
buffer_load_dwordx4 v[52:55], v18, s[80:83], s48 offen
s_add_u32 s48, s48, 393216
buffer_load_dwordx4 v[56:59], v16, s[80:83], s48 offen
buffer_load_dwordx4 v[60:63], v17, s[80:83], s48 offen
buffer_load_dwordx4 v[64:67], v18, s[80:83], s48 offen
s_add_u32 s48, s48, 393216
buffer_load_dwordx4 v[68:71], v16, s[80:83], s48 offen
buffer_load_dwordx4 v[72:75], v17, s[80:83], s48 offen
buffer_load_dwordx4 v[76:79], v18, s[80:83], s48 offen
s_add_u32 s48, s48, 393216
buffer_load_dwordx4 v[80:83], v16, s[80:83], s48 offen
buffer_load_dwordx4 v[84:87], v17, s[80:83], s48 offen
buffer_load_dwordx4 v[88:91], v18, s[80:83], s48 offen
s_add_u32 s48, s48, 393216
v_add_u32 v116, 0, v15
v_mul_u32_u24 v117, 820, v116
v_lshrrev_b32 v117, 16, v117
v_lshlrev_b32 v117, 1, v117
v_xor_b32 v116, v116, v117
v_lshlrev_b32 v96, 4, v116
v_add_u32 v116, 64, v15
v_mul_u32_u24 v117, 820, v116
v_lshrrev_b32 v117, 16, v117
v_lshlrev_b32 v117, 1, v117
v_xor_b32 v116, v116, v117
v_lshlrev_b32 v97, 4, v116
v_add_u32 v116, 128, v15
v_mul_u32_u24 v117, 820, v116
v_lshrrev_b32 v117, 16, v117
v_lshlrev_b32 v117, 1, v117
v_xor_b32 v116, v116, v117
v_lshlrev_b32 v98, 4, v116
v_add_u32 v116, 192, v15
v_mul_u32_u24 v117, 820, v116
v_lshrrev_b32 v117, 16, v117
v_lshlrev_b32 v117, 1, v117
v_xor_b32 v116, v116, v117
v_lshlrev_b32 v99, 4, v116
v_add_u32 v116, 256, v15
v_mul_u32_u24 v117, 820, v116
v_lshrrev_b32 v117, 16, v117
v_lshlrev_b32 v117, 1, v117
v_xor_b32 v116, v116, v117
v_lshlrev_b32 v100, 4, v116
v_add_u32 v116, 320, v15
v_mul_u32_u24 v117, 820, v116
v_lshrrev_b32 v117, 16, v117
v_lshlrev_b32 v117, 1, v117
v_xor_b32 v116, v116, v117
v_lshlrev_b32 v101, 4, v116
v_add_u32 v116, 384, v15
v_mul_u32_u24 v117, 820, v116
v_lshrrev_b32 v117, 16, v117
v_lshlrev_b32 v117, 1, v117
v_xor_b32 v116, v116, v117
v_lshlrev_b32 v102, 4, v116
v_add_u32 v116, 448, v15
v_mul_u32_u24 v117, 820, v116
v_lshrrev_b32 v117, 16, v117
v_lshlrev_b32 v117, 1, v117
v_xor_b32 v116, v116, v117
v_lshlrev_b32 v103, 4, v116
v_add_u32 v116, 512, v15
v_mul_u32_u24 v117, 820, v116
v_lshrrev_b32 v117, 16, v117
v_lshlrev_b32 v117, 1, v117
v_xor_b32 v116, v116, v117
v_lshlrev_b32 v104, 4, v116
v_add_u32 v116, 576, v15
v_mul_u32_u24 v117, 820, v116
v_lshrrev_b32 v117, 16, v117
v_lshlrev_b32 v117, 1, v117
v_xor_b32 v116, v116, v117
v_lshlrev_b32 v105, 4, v116
v_lshrrev_b32 v116, 2, v15
v_mul_u32_u24 v116, 1280, v116
v_and_b32 v117, 3, v15
v_lshl_add_u32 v116, v117, 3, v116
v_bfe_u32 v117, v15, 2, 3
s_mul_i32 s48, s45, 10
s_add_u32 s48, s48, 0
v_xor_b32 v106, s48, v117
v_lshl_add_u32 v106, v106, 5, v116
s_mul_i32 s48, s45, 10
s_add_u32 s48, s48, 1
v_xor_b32 v107, s48, v117
v_lshl_add_u32 v107, v107, 5, v116
s_mul_i32 s48, s45, 10
s_add_u32 s48, s48, 2
v_xor_b32 v108, s48, v117
v_lshl_add_u32 v108, v108, 5, v116
s_mul_i32 s48, s45, 10
s_add_u32 s48, s48, 3
v_xor_b32 v109, s48, v117
v_lshl_add_u32 v109, v109, 5, v116
s_mul_i32 s48, s45, 10
s_add_u32 s48, s48, 4
v_xor_b32 v110, s48, v117
v_lshl_add_u32 v110, v110, 5, v116
s_mul_i32 s48, s45, 10
s_add_u32 s48, s48, 5
v_xor_b32 v111, s48, v117
v_lshl_add_u32 v111, v111, 5, v116
s_mul_i32 s48, s45, 10
s_add_u32 s48, s48, 6
v_xor_b32 v112, s48, v117
v_lshl_add_u32 v112, v112, 5, v116
s_mul_i32 s48, s45, 10
s_add_u32 s48, s48, 7
v_xor_b32 v113, s48, v117
v_lshl_add_u32 v113, v113, 5, v116
s_mul_i32 s48, s45, 10
s_add_u32 s48, s48, 8
v_xor_b32 v114, s48, v117
v_lshl_add_u32 v114, v114, 5, v116
s_mul_i32 s48, s45, 10
s_add_u32 s48, s48, 9
v_xor_b32 v115, s48, v117
v_lshl_add_u32 v115, v115, 5, v116
s_and_b32 s51, s45, 1
s_mul_i32 s51, s51, 10240
s_lshr_b32 s49, s45, 1
s_mul_i32 s49, s49, 5120
s_add_u32 s51, s51, s49
s_mul_i32 s50, s85, 245760
s_and_b32 s49, s45, 1
s_mul_i32 s49, s49, 10240
s_add_u32 s50, s50, s49
s_cmp_ge_u32 s45, 2
s_cselect_b64 vcc, -1, 0
v_cndmask_b32 v96, v96, v101, vcc
v_cndmask_b32 v97, v97, v102, vcc
v_cndmask_b32 v98, v98, v103, vcc
)"
R"(v_cndmask_b32 v99, v99, v104, vcc
v_cndmask_b32 v100, v100, v105, vcc
s_add_u32 s49, s50, 0
s_add_u32 m0, s51, 0
s_nop 0
buffer_load_dwordx4 v96, s[76:79], s49 offen lds
s_add_u32 m0, s51, 1024
s_nop 0
buffer_load_dwordx4 v97, s[76:79], s49 offen lds
s_add_u32 m0, s51, 2048
s_nop 0
buffer_load_dwordx4 v98, s[76:79], s49 offen lds
s_add_u32 m0, s51, 3072
s_nop 0
buffer_load_dwordx4 v99, s[76:79], s49 offen lds
s_add_u32 m0, s51, 4096
s_nop 0
buffer_load_dwordx4 v100, s[76:79], s49 offen lds
s_add_u32 s49, s50, 20480
s_add_u32 m0, s51, 20480
s_nop 0
buffer_load_dwordx4 v96, s[76:79], s49 offen lds
s_add_u32 m0, s51, 21504
s_nop 0
buffer_load_dwordx4 v97, s[76:79], s49 offen lds
s_add_u32 m0, s51, 22528
s_nop 0
buffer_load_dwordx4 v98, s[76:79], s49 offen lds
s_add_u32 m0, s51, 23552
s_nop 0
buffer_load_dwordx4 v99, s[76:79], s49 offen lds
s_add_u32 m0, s51, 24576
s_nop 0
buffer_load_dwordx4 v100, s[76:79], s49 offen lds
s_add_u32 s49, s50, 61440
s_add_u32 m0, s51, 40960
s_nop 0
buffer_load_dwordx4 v96, s[76:79], s49 offen lds
s_add_u32 m0, s51, 41984
s_nop 0
buffer_load_dwordx4 v97, s[76:79], s49 offen lds
s_add_u32 m0, s51, 43008
s_nop 0
buffer_load_dwordx4 v98, s[76:79], s49 offen lds
s_add_u32 m0, s51, 44032
s_nop 0
buffer_load_dwordx4 v99, s[76:79], s49 offen lds
s_add_u32 m0, s51, 45056
s_nop 0
buffer_load_dwordx4 v100, s[76:79], s49 offen lds
s_add_u32 s49, s50, 81920
s_add_u32 m0, s51, 61440
s_nop 0
buffer_load_dwordx4 v96, s[76:79], s49 offen lds
s_add_u32 m0, s51, 62464
s_nop 0
buffer_load_dwordx4 v97, s[76:79], s49 offen lds
s_add_u32 m0, s51, 63488
s_nop 0
buffer_load_dwordx4 v98, s[76:79], s49 offen lds
s_add_u32 m0, s51, 64512
s_nop 0
buffer_load_dwordx4 v99, s[76:79], s49 offen lds
s_add_u32 m0, s51, 65536
s_nop 0
buffer_load_dwordx4 v100, s[76:79], s49 offen lds
s_add_u32 s49, s50, 122880
s_add_u32 m0, s51, 81920
s_nop 0
buffer_load_dwordx4 v96, s[76:79], s49 offen lds
s_add_u32 m0, s51, 82944
s_nop 0
buffer_load_dwordx4 v97, s[76:79], s49 offen lds
s_add_u32 m0, s51, 83968
s_nop 0
buffer_load_dwordx4 v98, s[76:79], s49 offen lds
s_add_u32 m0, s51, 84992
s_nop 0
buffer_load_dwordx4 v99, s[76:79], s49 offen lds
s_add_u32 m0, s51, 86016
s_nop 0
buffer_load_dwordx4 v100, s[76:79], s49 offen lds
s_add_u32 s49, s50, 143360
s_add_u32 m0, s51, 102400
s_nop 0
buffer_load_dwordx4 v96, s[76:79], s49 offen lds
s_add_u32 m0, s51, 103424
s_nop 0
buffer_load_dwordx4 v97, s[76:79], s49 offen lds
s_add_u32 m0, s51, 104448
s_nop 0
buffer_load_dwordx4 v98, s[76:79], s49 offen lds
s_add_u32 m0, s51, 105472
s_nop 0
buffer_load_dwordx4 v99, s[76:79], s49 offen lds
s_add_u32 m0, s51, 106496
s_nop 0
buffer_load_dwordx4 v100, s[76:79], s49 offen lds
s_waitcnt vmcnt(25) lgkmcnt(0)
s_barrier
s_add_u32 s49, s50, 184320
s_add_u32 m0, s51, 122880
s_nop 0
buffer_load_dwordx4 v96, s[76:79], s49 offen lds
s_add_u32 m0, s51, 123904
s_nop 0
buffer_load_dwordx4 v97, s[76:79], s49 offen lds
s_add_u32 m0, s51, 124928
s_nop 0
buffer_load_dwordx4 v98, s[76:79], s49 offen lds
s_add_u32 m0, s51, 125952
s_nop 0
buffer_load_dwordx4 v99, s[76:79], s49 offen lds
s_add_u32 m0, s51, 126976
s_nop 0
buffer_load_dwordx4 v100, s[76:79], s49 offen lds
ds_read_b64_tr_b16 a[0:1], v106 offset:0
ds_read_b64_tr_b16 a[24:25], v107 offset:0
ds_read_b64_tr_b16 a[48:49], v108 offset:0
ds_read_b64_tr_b16 a[72:73], v109 offset:0
ds_read_b64_tr_b16 a[96:97], v110 offset:0
ds_read_b64_tr_b16 a[120:121], v111 offset:0
ds_read_b64_tr_b16 a[144:145], v112 offset:0
ds_read_b64_tr_b16 a[168:169], v113 offset:0
ds_read_b64_tr_b16 a[192:193], v114 offset:0
ds_read_b64_tr_b16 a[216:217], v115 offset:0
s_waitcnt vmcnt(25) lgkmcnt(0)
s_barrier
s_add_u32 s49, s50, 204800
s_add_u32 m0, s51, 0
s_nop 0
buffer_load_dwordx4 v96, s[76:79], s49 offen lds
s_add_u32 m0, s51, 1024
s_nop 0
buffer_load_dwordx4 v97, s[76:79], s49 offen lds
s_add_u32 m0, s51, 2048
s_nop 0
buffer_load_dwordx4 v98, s[76:79], s49 offen lds
s_add_u32 m0, s51, 3072
s_nop 0
buffer_load_dwordx4 v99, s[76:79], s49 offen lds
s_add_u32 m0, s51, 4096
s_nop 0
buffer_load_dwordx4 v100, s[76:79], s49 offen lds
ds_read_b64_tr_b16 a[2:3], v106 offset:20480
ds_read_b64_tr_b16 a[26:27], v107 offset:20480
ds_read_b64_tr_b16 a[50:51], v108 offset:20480
ds_read_b64_tr_b16 a[74:75], v109 offset:20480
ds_read_b64_tr_b16 a[98:99], v110 offset:20480
ds_read_b64_tr_b16 a[122:123], v111 offset:20480
ds_read_b64_tr_b16 a[146:147], v112 offset:20480
ds_read_b64_tr_b16 a[170:171], v113 offset:20480
ds_read_b64_tr_b16 a[194:195], v114 offset:20480
ds_read_b64_tr_b16 a[218:219], v115 offset:20480
s_waitcnt vmcnt(25) lgkmcnt(0)
s_barrier
s_add_u32 s49, s50, 40960
s_add_u32 m0, s51, 20480
s_nop 0
buffer_load_dwordx4 v96, s[76:79], s49 offen lds
s_add_u32 m0, s51, 21504
s_nop 0
buffer_load_dwordx4 v97, s[76:79], s49 offen lds
s_add_u32 m0, s51, 22528
s_nop 0
buffer_load_dwordx4 v98, s[76:79], s49 offen lds
s_add_u32 m0, s51, 23552
s_nop 0
buffer_load_dwordx4 v99, s[76:79], s49 offen lds
s_add_u32 m0, s51, 24576
s_nop 0
buffer_load_dwordx4 v100, s[76:79], s49 offen lds
ds_read_b64_tr_b16 a[4:5], v106 offset:40960
ds_read_b64_tr_b16 a[28:29], v107 offset:40960
ds_read_b64_tr_b16 a[52:53], v108 offset:40960
ds_read_b64_tr_b16 a[76:77], v109 offset:40960
ds_read_b64_tr_b16 a[100:101], v110 offset:40960
ds_read_b64_tr_b16 a[124:125], v111 offset:40960
ds_read_b64_tr_b16 a[148:149], v112 offset:40960
ds_read_b64_tr_b16 a[172:173], v113 offset:40960
ds_read_b64_tr_b16 a[196:197], v114 offset:40960
ds_read_b64_tr_b16 a[220:221], v115 offset:40960
s_waitcnt vmcnt(25) lgkmcnt(0)
s_barrier
s_add_u32 s49, s50, 102400
s_add_u32 m0, s51, 40960
s_nop 0
buffer_load_dwordx4 v96, s[76:79], s49 offen lds
s_add_u32 m0, s51, 41984
s_nop 0
buffer_load_dwordx4 v97, s[76:79], s49 offen lds
s_add_u32 m0, s51, 43008
s_nop 0
buffer_load_dwordx4 v98, s[76:79], s49 offen lds
s_add_u32 m0, s51, 44032
s_nop 0
buffer_load_dwordx4 v99, s[76:79], s49 offen lds
s_add_u32 m0, s51, 45056
s_nop 0
buffer_load_dwordx4 v100, s[76:79], s49 offen lds
)"
R"(v_add_u32 v106, 61440, v106
v_add_u32 v107, 61440, v107
v_add_u32 v108, 61440, v108
v_add_u32 v109, 61440, v109
v_add_u32 v110, 61440, v110
v_add_u32 v111, 61440, v111
v_add_u32 v112, 61440, v112
v_add_u32 v113, 61440, v113
v_add_u32 v114, 61440, v114
v_add_u32 v115, 61440, v115
ds_read_b64_tr_b16 a[6:7], v106 offset:0
ds_read_b64_tr_b16 a[30:31], v107 offset:0
ds_read_b64_tr_b16 a[54:55], v108 offset:0
ds_read_b64_tr_b16 a[78:79], v109 offset:0
ds_read_b64_tr_b16 a[102:103], v110 offset:0
ds_read_b64_tr_b16 a[126:127], v111 offset:0
ds_read_b64_tr_b16 a[150:151], v112 offset:0
ds_read_b64_tr_b16 a[174:175], v113 offset:0
ds_read_b64_tr_b16 a[198:199], v114 offset:0
ds_read_b64_tr_b16 a[222:223], v115 offset:0
s_waitcnt vmcnt(25) lgkmcnt(0)
s_barrier
s_add_u32 s49, s50, 163840
s_add_u32 m0, s51, 61440
s_nop 0
buffer_load_dwordx4 v96, s[76:79], s49 offen lds
s_add_u32 m0, s51, 62464
s_nop 0
buffer_load_dwordx4 v97, s[76:79], s49 offen lds
s_add_u32 m0, s51, 63488
s_nop 0
buffer_load_dwordx4 v98, s[76:79], s49 offen lds
s_add_u32 m0, s51, 64512
s_nop 0
buffer_load_dwordx4 v99, s[76:79], s49 offen lds
s_add_u32 m0, s51, 65536
s_nop 0
buffer_load_dwordx4 v100, s[76:79], s49 offen lds
ds_read_b64_tr_b16 a[8:9], v106 offset:20480
ds_read_b64_tr_b16 a[32:33], v107 offset:20480
ds_read_b64_tr_b16 a[56:57], v108 offset:20480
ds_read_b64_tr_b16 a[80:81], v109 offset:20480
ds_read_b64_tr_b16 a[104:105], v110 offset:20480
ds_read_b64_tr_b16 a[128:129], v111 offset:20480
ds_read_b64_tr_b16 a[152:153], v112 offset:20480
ds_read_b64_tr_b16 a[176:177], v113 offset:20480
ds_read_b64_tr_b16 a[200:201], v114 offset:20480
ds_read_b64_tr_b16 a[224:225], v115 offset:20480
s_waitcnt vmcnt(25) lgkmcnt(0)
s_barrier
s_add_u32 s49, s50, 225280
s_add_u32 m0, s51, 81920
s_nop 0
buffer_load_dwordx4 v96, s[76:79], s49 offen lds
s_add_u32 m0, s51, 82944
s_nop 0
buffer_load_dwordx4 v97, s[76:79], s49 offen lds
s_add_u32 m0, s51, 83968
s_nop 0
buffer_load_dwordx4 v98, s[76:79], s49 offen lds
s_add_u32 m0, s51, 84992
s_nop 0
buffer_load_dwordx4 v99, s[76:79], s49 offen lds
s_add_u32 m0, s51, 86016
s_nop 0
buffer_load_dwordx4 v100, s[76:79], s49 offen lds
ds_read_b64_tr_b16 a[10:11], v106 offset:40960
ds_read_b64_tr_b16 a[34:35], v107 offset:40960
ds_read_b64_tr_b16 a[58:59], v108 offset:40960
ds_read_b64_tr_b16 a[82:83], v109 offset:40960
ds_read_b64_tr_b16 a[106:107], v110 offset:40960
ds_read_b64_tr_b16 a[130:131], v111 offset:40960
ds_read_b64_tr_b16 a[154:155], v112 offset:40960
ds_read_b64_tr_b16 a[178:179], v113 offset:40960
ds_read_b64_tr_b16 a[202:203], v114 offset:40960
ds_read_b64_tr_b16 a[226:227], v115 offset:40960
s_waitcnt vmcnt(25) lgkmcnt(0)
s_barrier
v_add_u32 v106, 61440, v106
v_add_u32 v107, 61440, v107
v_add_u32 v108, 61440, v108
v_add_u32 v109, 61440, v109
v_add_u32 v110, 61440, v110
v_add_u32 v111, 61440, v111
v_add_u32 v112, 61440, v112
v_add_u32 v113, 61440, v113
v_add_u32 v114, 61440, v114
v_add_u32 v115, 61440, v115
ds_read_b64_tr_b16 a[12:13], v106 offset:0
ds_read_b64_tr_b16 a[36:37], v107 offset:0
ds_read_b64_tr_b16 a[60:61], v108 offset:0
ds_read_b64_tr_b16 a[84:85], v109 offset:0
ds_read_b64_tr_b16 a[108:109], v110 offset:0
ds_read_b64_tr_b16 a[132:133], v111 offset:0
ds_read_b64_tr_b16 a[156:157], v112 offset:0
ds_read_b64_tr_b16 a[180:181], v113 offset:0
ds_read_b64_tr_b16 a[204:205], v114 offset:0
ds_read_b64_tr_b16 a[228:229], v115 offset:0
s_waitcnt vmcnt(20) lgkmcnt(0)
s_barrier
v_add_u32 v106, -122880, v106
v_add_u32 v107, -122880, v107
v_add_u32 v108, -122880, v108
v_add_u32 v109, -122880, v109
v_add_u32 v110, -122880, v110
v_add_u32 v111, -122880, v111
v_add_u32 v112, -122880, v112
v_add_u32 v113, -122880, v113
v_add_u32 v114, -122880, v114
v_add_u32 v115, -122880, v115
ds_read_b64_tr_b16 a[14:15], v106 offset:0
ds_read_b64_tr_b16 a[38:39], v107 offset:0
ds_read_b64_tr_b16 a[62:63], v108 offset:0
ds_read_b64_tr_b16 a[86:87], v109 offset:0
ds_read_b64_tr_b16 a[110:111], v110 offset:0
ds_read_b64_tr_b16 a[134:135], v111 offset:0
ds_read_b64_tr_b16 a[158:159], v112 offset:0
ds_read_b64_tr_b16 a[182:183], v113 offset:0
ds_read_b64_tr_b16 a[206:207], v114 offset:0
ds_read_b64_tr_b16 a[230:231], v115 offset:0
s_waitcnt vmcnt(15) lgkmcnt(0)
s_barrier
ds_read_b64_tr_b16 a[16:17], v106 offset:20480
ds_read_b64_tr_b16 a[40:41], v107 offset:20480
ds_read_b64_tr_b16 a[64:65], v108 offset:20480
ds_read_b64_tr_b16 a[88:89], v109 offset:20480
ds_read_b64_tr_b16 a[112:113], v110 offset:20480
ds_read_b64_tr_b16 a[136:137], v111 offset:20480
ds_read_b64_tr_b16 a[160:161], v112 offset:20480
ds_read_b64_tr_b16 a[184:185], v113 offset:20480
ds_read_b64_tr_b16 a[208:209], v114 offset:20480
ds_read_b64_tr_b16 a[232:233], v115 offset:20480
s_waitcnt vmcnt(10) lgkmcnt(0)
s_barrier
ds_read_b64_tr_b16 a[18:19], v106 offset:40960
ds_read_b64_tr_b16 a[42:43], v107 offset:40960
ds_read_b64_tr_b16 a[66:67], v108 offset:40960
ds_read_b64_tr_b16 a[90:91], v109 offset:40960
ds_read_b64_tr_b16 a[114:115], v110 offset:40960
ds_read_b64_tr_b16 a[138:139], v111 offset:40960
ds_read_b64_tr_b16 a[162:163], v112 offset:40960
ds_read_b64_tr_b16 a[186:187], v113 offset:40960
ds_read_b64_tr_b16 a[210:211], v114 offset:40960
ds_read_b64_tr_b16 a[234:235], v115 offset:40960
s_waitcnt vmcnt(5) lgkmcnt(0)
s_barrier
v_add_u32 v106, 61440, v106
v_add_u32 v107, 61440, v107
v_add_u32 v108, 61440, v108
v_add_u32 v109, 61440, v109
v_add_u32 v110, 61440, v110
v_add_u32 v111, 61440, v111
v_add_u32 v112, 61440, v112
v_add_u32 v113, 61440, v113
v_add_u32 v114, 61440, v114
v_add_u32 v115, 61440, v115
ds_read_b64_tr_b16 a[20:21], v106 offset:0
ds_read_b64_tr_b16 a[44:45], v107 offset:0
ds_read_b64_tr_b16 a[68:69], v108 offset:0
ds_read_b64_tr_b16 a[92:93], v109 offset:0
ds_read_b64_tr_b16 a[116:117], v110 offset:0
ds_read_b64_tr_b16 a[140:141], v111 offset:0
ds_read_b64_tr_b16 a[164:165], v112 offset:0
ds_read_b64_tr_b16 a[188:189], v113 offset:0
ds_read_b64_tr_b16 a[212:213], v114 offset:0
ds_read_b64_tr_b16 a[236:237], v115 offset:0
s_waitcnt vmcnt(0) lgkmcnt(0)
s_barrier
ds_read_b64_tr_b16 a[22:23], v106 offset:20480
ds_read_b64_tr_b16 a[46:47], v107 offset:20480
ds_read_b64_tr_b16 a[70:71], v108 offset:20480
ds_read_b64_tr_b16 a[94:95], v109 offset:20480
ds_read_b64_tr_b16 a[118:119], v110 offset:20480
ds_read_b64_tr_b16 a[142:143], v111 offset:20480
ds_read_b64_tr_b16 a[166:167], v112 offset:20480
ds_read_b64_tr_b16 a[190:191], v113 offset:20480
ds_read_b64_tr_b16 a[214:215], v114 offset:20480
ds_read_b64_tr_b16 a[238:239], v115 offset:20480
s_waitcnt lgkmcnt(0)
s_barrier
ds_write_b128 v19, v[32:35] offset:0
ds_write_b128 v20, v[36:39] offset:0
ds_write_b128 v21, v[40:43] offset:0
ds_write_b128 v19, v[44:47] offset:12800
ds_write_b128 v20, v[48:51] offset:12800
ds_write_b128 v21, v[52:55] offset:12800
ds_write_b128 v19, v[56:59] offset:25600
ds_write_b128 v20, v[60:63] offset:25600
ds_write_b128 v21, v[64:67] offset:25600
ds_write_b128 v19, v[68:71] offset:38400
ds_write_b128 v20, v[72:75] offset:38400
ds_write_b128 v21, v[76:79] offset:38400
ds_write_b128 v19, v[80:83] offset:51200
ds_write_b128 v20, v[84:87] offset:51200
ds_write_b128 v21, v[88:91] offset:51200
v_and_b32 v22, 1, v15
v_and_b32 v26, 2, v15
v_lshl_add_u32 v22, v26, 2, v22
v_and_b32 v26, 4, v15
v_add_u32 v22, v22, v26
)"
R"(v_and_b32 v26, 8, v15
v_lshl_add_u32 v22, v26, 1, v22
v_mul_u32_u24 v22, 400, v22
v_lshrrev_b32 v26, 4, v15
v_lshl_add_u32 v22, v26, 3, v22
v_bfe_u32 v26, v15, 3, 1
v_mul_u32_u24 v26, 655360, v26
v_and_b32 v24, 0x370, v0
v_add_u32 v24, v24, v26
s_mul_i32 s48, s45, 13107200
s_add_u32 s48, s48, 64421888
v_add_u32 v24, s48, v24
s_lshr_b32 s50, s85, 1
s_mul_i32 s50, s50, 20480
s_and_b32 s48, s85, 1
s_lshl_b32 s48, s48, 7
s_add_u32 s50, s50, s48
s_mul_i32 s48, s86, 5120
s_add_u32 s50, s50, s48
s_movk_i32 s52, 8192
s_mov_b32 s51, 5
s_waitcnt lgkmcnt(0)
s_barrier
cw_bg_%=:
ds_read2_b64 v[96:99], v22 offset0:0 offset1:4
ds_read2_b64 v[100:103], v22 offset0:100 offset1:104
ds_read2_b64 v[104:107], v22 offset0:12 offset1:16
ds_read2_b64 v[108:111], v22 offset0:112 offset1:116
ds_read2_b64 v[112:115], v22 offset0:24 offset1:28
ds_read2_b64 v[116:119], v22 offset0:124 offset1:128
ds_read2_b64 v[120:123], v22 offset0:36 offset1:40
ds_read2_b64 v[124:127], v22 offset0:136 offset1:140
ds_read2_b64 v[128:131], v22 offset0:8 offset1:20
ds_read2_b64 v[132:135], v22 offset0:108 offset1:120
ds_read2_b64 v[136:139], v22 offset0:32 offset1:44
ds_read2_b64 v[140:143], v22 offset0:132 offset1:144
v_add_u32 v22, 12800, v22
s_and_b32 s53, s50, 4294959104
s_add_u32 s53, s53, s50
s_waitcnt lgkmcnt(11)
v_mfma_f32_16x16x32_bf16 v[144:147], v[96:99], a[0:3], 0
s_waitcnt lgkmcnt(10)
v_mfma_f32_16x16x32_bf16 v[148:151], v[100:103], a[0:3], 0
s_waitcnt lgkmcnt(9)
v_mfma_f32_16x16x32_bf16 v[144:147], v[104:107], a[4:7], v[144:147]
s_waitcnt lgkmcnt(8)
v_mfma_f32_16x16x32_bf16 v[148:151], v[108:111], a[4:7], v[148:151]
s_waitcnt lgkmcnt(7)
v_mfma_f32_16x16x32_bf16 v[144:147], v[112:115], a[8:11], v[144:147]
s_waitcnt lgkmcnt(6)
v_mfma_f32_16x16x32_bf16 v[148:151], v[116:119], a[8:11], v[148:151]
s_waitcnt lgkmcnt(5)
v_mfma_f32_16x16x32_bf16 v[144:147], v[120:123], a[12:15], v[144:147]
s_waitcnt lgkmcnt(4)
v_mfma_f32_16x16x32_bf16 v[148:151], v[124:127], a[12:15], v[148:151]
s_waitcnt lgkmcnt(3)
v_mfma_f32_16x16x32_bf16 v[144:147], v[128:131], a[16:19], v[144:147]
s_waitcnt lgkmcnt(2)
v_mfma_f32_16x16x32_bf16 v[148:151], v[132:135], a[16:19], v[148:151]
s_waitcnt lgkmcnt(1)
v_mfma_f32_16x16x32_bf16 v[144:147], v[136:139], a[20:23], v[144:147]
s_waitcnt lgkmcnt(0)
v_mfma_f32_16x16x32_bf16 v[148:151], v[140:143], a[20:23], v[148:151]
v_mfma_f32_16x16x32_bf16 v[152:155], v[96:99], a[24:27], 0
v_add_u32 v25, s53, v24
v_mfma_f32_16x16x32_bf16 v[156:159], v[100:103], a[24:27], 0
v_lshrrev_b32 v26, 8, v25
v_mfma_f32_16x16x32_bf16 v[152:155], v[104:107], a[28:31], v[152:155]
v_and_or_b32 v25, v26, s52, v25
v_mfma_f32_16x16x32_bf16 v[156:159], v[108:111], a[28:31], v[156:159]
s_add_u32 s53, s53, 1310720
v_mfma_f32_16x16x32_bf16 v[152:155], v[112:115], a[32:35], v[152:155]
v_mfma_f32_16x16x32_bf16 v[156:159], v[116:119], a[32:35], v[156:159]
v_mfma_f32_16x16x32_bf16 v[152:155], v[120:123], a[36:39], v[152:155]
v_cvt_pk_bf16_f32 v160, v144, v145
v_mfma_f32_16x16x32_bf16 v[156:159], v[124:127], a[36:39], v[156:159]
v_cvt_pk_bf16_f32 v161, v146, v147
v_mfma_f32_16x16x32_bf16 v[152:155], v[128:131], a[40:43], v[152:155]
v_cvt_pk_bf16_f32 v162, v148, v149
v_mfma_f32_16x16x32_bf16 v[156:159], v[132:135], a[40:43], v[156:159]
v_cvt_pk_bf16_f32 v163, v150, v151
v_mfma_f32_16x16x32_bf16 v[152:155], v[136:139], a[44:47], v[152:155]
buffer_store_dwordx4 v[160:163], v25, s[40:43], 0 offen sc1
v_mfma_f32_16x16x32_bf16 v[156:159], v[140:143], a[44:47], v[156:159]
v_mfma_f32_16x16x32_bf16 v[144:147], v[96:99], a[48:51], 0
v_add_u32 v25, s53, v24
v_mfma_f32_16x16x32_bf16 v[148:151], v[100:103], a[48:51], 0
v_lshrrev_b32 v26, 8, v25
v_mfma_f32_16x16x32_bf16 v[144:147], v[104:107], a[52:55], v[144:147]
v_and_or_b32 v25, v26, s52, v25
v_mfma_f32_16x16x32_bf16 v[148:151], v[108:111], a[52:55], v[148:151]
s_add_u32 s53, s53, 1310720
v_mfma_f32_16x16x32_bf16 v[144:147], v[112:115], a[56:59], v[144:147]
v_mfma_f32_16x16x32_bf16 v[148:151], v[116:119], a[56:59], v[148:151]
v_mfma_f32_16x16x32_bf16 v[144:147], v[120:123], a[60:63], v[144:147]
v_cvt_pk_bf16_f32 v164, v152, v153
v_mfma_f32_16x16x32_bf16 v[148:151], v[124:127], a[60:63], v[148:151]
v_cvt_pk_bf16_f32 v165, v154, v155
v_mfma_f32_16x16x32_bf16 v[144:147], v[128:131], a[64:67], v[144:147]
v_cvt_pk_bf16_f32 v166, v156, v157
v_mfma_f32_16x16x32_bf16 v[148:151], v[132:135], a[64:67], v[148:151]
v_cvt_pk_bf16_f32 v167, v158, v159
v_mfma_f32_16x16x32_bf16 v[144:147], v[136:139], a[68:71], v[144:147]
buffer_store_dwordx4 v[164:167], v25, s[40:43], 0 offen sc1
v_mfma_f32_16x16x32_bf16 v[148:151], v[140:143], a[68:71], v[148:151]
v_mfma_f32_16x16x32_bf16 v[152:155], v[96:99], a[72:75], 0
v_add_u32 v25, s53, v24
v_mfma_f32_16x16x32_bf16 v[156:159], v[100:103], a[72:75], 0
v_lshrrev_b32 v26, 8, v25
v_mfma_f32_16x16x32_bf16 v[152:155], v[104:107], a[76:79], v[152:155]
v_and_or_b32 v25, v26, s52, v25
v_mfma_f32_16x16x32_bf16 v[156:159], v[108:111], a[76:79], v[156:159]
s_add_u32 s53, s53, 1310720
v_mfma_f32_16x16x32_bf16 v[152:155], v[112:115], a[80:83], v[152:155]
v_mfma_f32_16x16x32_bf16 v[156:159], v[116:119], a[80:83], v[156:159]
v_mfma_f32_16x16x32_bf16 v[152:155], v[120:123], a[84:87], v[152:155]
v_cvt_pk_bf16_f32 v160, v144, v145
v_mfma_f32_16x16x32_bf16 v[156:159], v[124:127], a[84:87], v[156:159]
v_cvt_pk_bf16_f32 v161, v146, v147
v_mfma_f32_16x16x32_bf16 v[152:155], v[128:131], a[88:91], v[152:155]
v_cvt_pk_bf16_f32 v162, v148, v149
v_mfma_f32_16x16x32_bf16 v[156:159], v[132:135], a[88:91], v[156:159]
v_cvt_pk_bf16_f32 v163, v150, v151
v_mfma_f32_16x16x32_bf16 v[152:155], v[136:139], a[92:95], v[152:155]
buffer_store_dwordx4 v[160:163], v25, s[40:43], 0 offen sc1
v_mfma_f32_16x16x32_bf16 v[156:159], v[140:143], a[92:95], v[156:159]
v_mfma_f32_16x16x32_bf16 v[144:147], v[96:99], a[96:99], 0
v_add_u32 v25, s53, v24
v_mfma_f32_16x16x32_bf16 v[148:151], v[100:103], a[96:99], 0
v_lshrrev_b32 v26, 8, v25
v_mfma_f32_16x16x32_bf16 v[144:147], v[104:107], a[100:103], v[144:147]
v_and_or_b32 v25, v26, s52, v25
v_mfma_f32_16x16x32_bf16 v[148:151], v[108:111], a[100:103], v[148:151]
s_add_u32 s53, s53, 1310720
v_mfma_f32_16x16x32_bf16 v[144:147], v[112:115], a[104:107], v[144:147]
v_mfma_f32_16x16x32_bf16 v[148:151], v[116:119], a[104:107], v[148:151]
v_mfma_f32_16x16x32_bf16 v[144:147], v[120:123], a[108:111], v[144:147]
v_cvt_pk_bf16_f32 v164, v152, v153
v_mfma_f32_16x16x32_bf16 v[148:151], v[124:127], a[108:111], v[148:151]
v_cvt_pk_bf16_f32 v165, v154, v155
v_mfma_f32_16x16x32_bf16 v[144:147], v[128:131], a[112:115], v[144:147]
v_cvt_pk_bf16_f32 v166, v156, v157
v_mfma_f32_16x16x32_bf16 v[148:151], v[132:135], a[112:115], v[148:151]
v_cvt_pk_bf16_f32 v167, v158, v159
v_mfma_f32_16x16x32_bf16 v[144:147], v[136:139], a[116:119], v[144:147]
buffer_store_dwordx4 v[164:167], v25, s[40:43], 0 offen sc1
v_mfma_f32_16x16x32_bf16 v[148:151], v[140:143], a[116:119], v[148:151]
v_mfma_f32_16x16x32_bf16 v[152:155], v[96:99], a[120:123], 0
v_add_u32 v25, s53, v24
v_mfma_f32_16x16x32_bf16 v[156:159], v[100:103], a[120:123], 0
v_lshrrev_b32 v26, 8, v25
v_mfma_f32_16x16x32_bf16 v[152:155], v[104:107], a[124:127], v[152:155]
v_and_or_b32 v25, v26, s52, v25
v_mfma_f32_16x16x32_bf16 v[156:159], v[108:111], a[124:127], v[156:159]
s_add_u32 s53, s53, 1310720
v_mfma_f32_16x16x32_bf16 v[152:155], v[112:115], a[128:131], v[152:155]
v_mfma_f32_16x16x32_bf16 v[156:159], v[116:119], a[128:131], v[156:159]
v_mfma_f32_16x16x32_bf16 v[152:155], v[120:123], a[132:135], v[152:155]
v_cvt_pk_bf16_f32 v160, v144, v145
v_mfma_f32_16x16x32_bf16 v[156:159], v[124:127], a[132:135], v[156:159]
v_cvt_pk_bf16_f32 v161, v146, v147
v_mfma_f32_16x16x32_bf16 v[152:155], v[128:131], a[136:139], v[152:155]
v_cvt_pk_bf16_f32 v162, v148, v149
v_mfma_f32_16x16x32_bf16 v[156:159], v[132:135], a[136:139], v[156:159]
v_cvt_pk_bf16_f32 v163, v150, v151
v_mfma_f32_16x16x32_bf16 v[152:155], v[136:139], a[140:143], v[152:155]
buffer_store_dwordx4 v[160:163], v25, s[40:43], 0 offen sc1
v_mfma_f32_16x16x32_bf16 v[156:159], v[140:143], a[140:143], v[156:159]
v_mfma_f32_16x16x32_bf16 v[144:147], v[96:99], a[144:147], 0
v_add_u32 v25, s53, v24
v_mfma_f32_16x16x32_bf16 v[148:151], v[100:103], a[144:147], 0
v_lshrrev_b32 v26, 8, v25
v_mfma_f32_16x16x32_bf16 v[144:147], v[104:107], a[148:151], v[144:147]
v_and_or_b32 v25, v26, s52, v25
v_mfma_f32_16x16x32_bf16 v[148:151], v[108:111], a[148:151], v[148:151]
s_add_u32 s53, s53, 1310720
v_mfma_f32_16x16x32_bf16 v[144:147], v[112:115], a[152:155], v[144:147]
v_mfma_f32_16x16x32_bf16 v[148:151], v[116:119], a[152:155], v[148:151]
v_mfma_f32_16x16x32_bf16 v[144:147], v[120:123], a[156:159], v[144:147]
v_cvt_pk_bf16_f32 v164, v152, v153
v_mfma_f32_16x16x32_bf16 v[148:151], v[124:127], a[156:159], v[148:151]
v_cvt_pk_bf16_f32 v165, v154, v155
v_mfma_f32_16x16x32_bf16 v[144:147], v[128:131], a[160:163], v[144:147]
v_cvt_pk_bf16_f32 v166, v156, v157
v_mfma_f32_16x16x32_bf16 v[148:151], v[132:135], a[160:163], v[148:151]
v_cvt_pk_bf16_f32 v167, v158, v159
v_mfma_f32_16x16x32_bf16 v[144:147], v[136:139], a[164:167], v[144:147]
buffer_store_dwordx4 v[164:167], v25, s[40:43], 0 offen sc1
v_mfma_f32_16x16x32_bf16 v[148:151], v[140:143], a[164:167], v[148:151]
v_mfma_f32_16x16x32_bf16 v[152:155], v[96:99], a[168:171], 0
v_add_u32 v25, s53, v24
v_mfma_f32_16x16x32_bf16 v[156:159], v[100:103], a[168:171], 0
v_lshrrev_b32 v26, 8, v25
v_mfma_f32_16x16x32_bf16 v[152:155], v[104:107], a[172:175], v[152:155]
v_and_or_b32 v25, v26, s52, v25
v_mfma_f32_16x16x32_bf16 v[156:159], v[108:111], a[172:175], v[156:159]
s_add_u32 s53, s53, 1310720
v_mfma_f32_16x16x32_bf16 v[152:155], v[112:115], a[176:179], v[152:155]
v_mfma_f32_16x16x32_bf16 v[156:159], v[116:119], a[176:179], v[156:159]
v_mfma_f32_16x16x32_bf16 v[152:155], v[120:123], a[180:183], v[152:155]
)"
R"(v_cvt_pk_bf16_f32 v160, v144, v145
v_mfma_f32_16x16x32_bf16 v[156:159], v[124:127], a[180:183], v[156:159]
v_cvt_pk_bf16_f32 v161, v146, v147
v_mfma_f32_16x16x32_bf16 v[152:155], v[128:131], a[184:187], v[152:155]
v_cvt_pk_bf16_f32 v162, v148, v149
v_mfma_f32_16x16x32_bf16 v[156:159], v[132:135], a[184:187], v[156:159]
v_cvt_pk_bf16_f32 v163, v150, v151
v_mfma_f32_16x16x32_bf16 v[152:155], v[136:139], a[188:191], v[152:155]
buffer_store_dwordx4 v[160:163], v25, s[40:43], 0 offen sc1
v_mfma_f32_16x16x32_bf16 v[156:159], v[140:143], a[188:191], v[156:159]
v_mfma_f32_16x16x32_bf16 v[144:147], v[96:99], a[192:195], 0
v_add_u32 v25, s53, v24
v_mfma_f32_16x16x32_bf16 v[148:151], v[100:103], a[192:195], 0
v_lshrrev_b32 v26, 8, v25
v_mfma_f32_16x16x32_bf16 v[144:147], v[104:107], a[196:199], v[144:147]
v_and_or_b32 v25, v26, s52, v25
v_mfma_f32_16x16x32_bf16 v[148:151], v[108:111], a[196:199], v[148:151]
s_add_u32 s53, s53, 1310720
v_mfma_f32_16x16x32_bf16 v[144:147], v[112:115], a[200:203], v[144:147]
v_mfma_f32_16x16x32_bf16 v[148:151], v[116:119], a[200:203], v[148:151]
v_mfma_f32_16x16x32_bf16 v[144:147], v[120:123], a[204:207], v[144:147]
v_cvt_pk_bf16_f32 v164, v152, v153
v_mfma_f32_16x16x32_bf16 v[148:151], v[124:127], a[204:207], v[148:151]
v_cvt_pk_bf16_f32 v165, v154, v155
v_mfma_f32_16x16x32_bf16 v[144:147], v[128:131], a[208:211], v[144:147]
v_cvt_pk_bf16_f32 v166, v156, v157
v_mfma_f32_16x16x32_bf16 v[148:151], v[132:135], a[208:211], v[148:151]
v_cvt_pk_bf16_f32 v167, v158, v159
v_mfma_f32_16x16x32_bf16 v[144:147], v[136:139], a[212:215], v[144:147]
buffer_store_dwordx4 v[164:167], v25, s[40:43], 0 offen sc1
v_mfma_f32_16x16x32_bf16 v[148:151], v[140:143], a[212:215], v[148:151]
v_mfma_f32_16x16x32_bf16 v[152:155], v[96:99], a[216:219], 0
v_add_u32 v25, s53, v24
v_mfma_f32_16x16x32_bf16 v[156:159], v[100:103], a[216:219], 0
v_lshrrev_b32 v26, 8, v25
v_mfma_f32_16x16x32_bf16 v[152:155], v[104:107], a[220:223], v[152:155]
v_and_or_b32 v25, v26, s52, v25
v_mfma_f32_16x16x32_bf16 v[156:159], v[108:111], a[220:223], v[156:159]
s_add_u32 s53, s53, 1310720
v_mfma_f32_16x16x32_bf16 v[152:155], v[112:115], a[224:227], v[152:155]
v_mfma_f32_16x16x32_bf16 v[156:159], v[116:119], a[224:227], v[156:159]
v_mfma_f32_16x16x32_bf16 v[152:155], v[120:123], a[228:231], v[152:155]
v_cvt_pk_bf16_f32 v160, v144, v145
v_mfma_f32_16x16x32_bf16 v[156:159], v[124:127], a[228:231], v[156:159]
v_cvt_pk_bf16_f32 v161, v146, v147
v_mfma_f32_16x16x32_bf16 v[152:155], v[128:131], a[232:235], v[152:155]
v_cvt_pk_bf16_f32 v162, v148, v149
v_mfma_f32_16x16x32_bf16 v[156:159], v[132:135], a[232:235], v[156:159]
v_cvt_pk_bf16_f32 v163, v150, v151
v_mfma_f32_16x16x32_bf16 v[152:155], v[136:139], a[236:239], v[152:155]
buffer_store_dwordx4 v[160:163], v25, s[40:43], 0 offen sc1
v_mfma_f32_16x16x32_bf16 v[156:159], v[140:143], a[236:239], v[156:159]
v_add_u32 v25, s53, v24
v_lshrrev_b32 v26, 8, v25
v_and_or_b32 v25, v26, s52, v25
s_add_u32 s53, s53, 1310720
s_nop 1
v_cvt_pk_bf16_f32 v164, v152, v153
v_cvt_pk_bf16_f32 v165, v154, v155
v_cvt_pk_bf16_f32 v166, v156, v157
v_cvt_pk_bf16_f32 v167, v158, v159
buffer_store_dwordx4 v[164:167], v25, s[40:43], 0 offen sc1
s_add_u32 s50, s50, 1024
s_sub_u32 s51, s51, 1
s_cmp_lg_u32 s51, 0
s_cbranch_scc1 cw_bg_%=
s_cmp_eq_u32 s45, 0
s_cselect_b32 s59, 1, 0
v_lshlrev_b32 v26, 7, v15
v_cmp_gt_u32 vcc, s59, v15
v_mov_b32 v27, 2147483632
s_nop 0
v_cndmask_b32 v26, v27, v26, vcc
v_mov_b32 v27, 1
s_and_b32 s54, s84, 7
s_lshl_b32 s54, s54, 7
s_add_u32 s54, s54, 2374912
s_waitcnt vmcnt(0)
s_waitcnt lgkmcnt(0)
s_barrier
buffer_atomic_add v27, v26, s[40:43], s54 offen
v_lshrrev_b32 v12, 4, v15
v_and_b32 v13, 7, v15
v_and_b32 v10, 8, v15
v_cmp_eq_u32 vcc, 0, v10
v_mov_b32 v10, 0xeeee
v_mov_b32 v1, 0x4444
v_cndmask_b32 v1, v10, v1, vcc
s_lshl_b32 s48, s44, 7
v_lshlrev_b32 v3, 4, v13
v_add_u32 v3, s48, v3
v_and_b32 v10, 0xffffe000, v3
v_add_u32 v3, v3, v10
v_lshl_add_u32 v3, v12, 15, v3
v_mov_b32 v10, 2147483632
v_cndmask_b32 v3, v10, v3, vcc
s_mov_b32 s47, 0
s_cmp_lg_u32 s45, 0
s_cbranch_scc1 cw_wx_%=
v_mov_b32 v5, 0
v_cmp_eq_u32 vcc, 0, v15
v_mov_b32 v10, 2147483632
s_nop 0
v_cndmask_b32 v5, v10, v5, vcc
s_mov_b32 s65, 0
v_and_b32 v2, 31, v15
v_lshlrev_b32 v2, 7, v2
v_mov_b32 v7, 0
v_mov_b32 v8, 26880
s_mov_b32 s55, 32
v_cmp_eq_u32 vcc, 0, v15
s_nop 1
v_cndmask_b32 v18, 0, 32, vcc
s_mul_i32 s48, s44, 3277
s_lshr_b32 s48, s48, 16
s_lshl_b32 s48, s48, 7
s_add_u32 s48, s48, 512
v_add_u32 v4, s48, v5
s_mov_b32 s48, 786432
buffer_load_dwordx4 v[192:195], v3, s[40:43], s48 offen sc1
s_waitcnt vmcnt(0)
s_nop 0
cw_loopa_%=:
s_and_b32 s48, s47, 3
s_lshl_b32 s50, s48, 2
s_add_u32 s50, s50, s46
s_mul_i32 s50, s50, 32768
s_add_u32 s50, s50, 786432
s_add_u32 s51, s50, 4096
s_add_u32 s52, s50, 16384
s_add_u32 s64, s65, s70
s_and_b32 s61, s47, 31
s_mul_i32 s61, s61, 80
s_add_u32 s61, s61, s44
s_lshl_b32 s61, s61, 15
s_add_u32 s61, s61, 32964608
s_lshr_b32 s59, s61, 8
s_and_b32 s59, s59, 0x2000
s_add_u32 s61, s61, s59
s_and_b32 s59, s47, 63
s_cbranch_scc0 cw_start_%=
s_cmp_eq_u32 s59, 1
s_cbranch_scc0 cw_u1_ok_%=
s_lshr_b32 s49, s47, 5
s_and_b32 s54, s47, 1
s_add_u32 s49, s49, s54
s_and_b32 s54, s44, 7
s_lshl3_add_u32 s54, s49, s54
s_lshl_b32 s54, s54, 7
s_add_u32 s54, s54, 2380032
s_mov_b32 s60, 0
cw_u1_%=:
v_mov_b32 v10, 0
buffer_atomic_add v10, v5, s[40:43], s54 offen sc0
s_waitcnt vmcnt(0)
v_cmp_gt_u32 vcc, v18, v10
s_nop 4
s_cbranch_vccz cw_u1x_%=
s_sleep 3
s_add_u32 s60, s60, 1
s_cmp_gt_u32 s60, 65536
s_cbranch_scc1 cw_abort_%=
s_and_b32 s59, s60, 63
s_cbranch_scc1 cw_u1_%=
v_mov_b32 v10, 1024
buffer_load_dword v10, v10, s[40:43], s58 offen sc1
s_waitcnt vmcnt(0)
v_readfirstlane_b32 s59, v10
s_nop 0
s_cmp_lg_u32 s59, 0
s_nop 2
s_cbranch_scc1 cw_end_%=
s_branch cw_u1_%=
cw_u1x_%=:
v_mov_b32 v7, 32
cw_u1_ok_%=:
v_cmp_gt_u32 vcc, s55, v7
s_cbranch_vccz cw_ux_ok_%=
s_mov_b32 s60, 0
s_nop 2
cw_ux_poll_%=:
buffer_load_dword v7, v8, s[40:43], s58 offen sc1
s_waitcnt vmcnt(0)
v_cmp_gt_u32 vcc, s55, v7
s_nop 4
s_cbranch_vccz cw_ux_ok_%=
s_sleep 10
s_add_u32 s60, s60, 1
s_cmp_gt_u32 s60, 65536
s_cbranch_scc1 cw_abort_%=
s_and_b32 s59, s60, 63
s_cbranch_scc1 cw_ux_poll_%=
v_mov_b32 v10, 1024
buffer_load_dword v10, v10, s[40:43], s58 offen sc1
s_waitcnt vmcnt(0)
v_readfirstlane_b32 s59, v10
s_nop 0
s_cmp_lg_u32 s59, 0
s_nop 2
s_cbranch_scc1 cw_end_%=
)"
R"(s_branch cw_ux_poll_%=
cw_ux_ok_%=:
v_add_u32 v8, 128, v8
s_memrealtime s[56:57]
s_waitcnt lgkmcnt(0)
s_sub_u32 s59, s64, s56
s_cmp_gt_u32 s59, 1000
s_cbranch_scc1 cw_wm_go_%=
cw_wm_%=:
s_memrealtime s[56:57]
s_waitcnt lgkmcnt(0)
s_sub_u32 s59, s64, s56
s_cmp_gt_i32 s59, 3
s_cbranch_scc1 cw_wm_%=
cw_wm_f_%=:
s_sub_u32 s59, s59, 1
s_cmp_ge_i32 s59, 0
s_cbranch_scc1 cw_wm_f_%=
cw_wm_go_%=:
s_mov_b32 s60, 0
buffer_load_dwordx4 v[32:35], v0, s[40:43], s50 offen sc1
buffer_load_dwordx4 v[36:39], v0, s[40:43], s50 offen offset:1024 sc1
buffer_load_dwordx4 v[40:43], v0, s[40:43], s50 offen offset:2048 sc1
buffer_load_dwordx4 v[44:47], v0, s[40:43], s50 offen offset:3072 sc1
buffer_load_dwordx4 v[48:51], v0, s[40:43], s51 offen sc1
buffer_load_dwordx4 v[52:55], v0, s[40:43], s51 offen offset:1024 sc1
buffer_load_dwordx4 v[56:59], v0, s[40:43], s51 offen offset:2048 sc1
buffer_load_dwordx4 v[60:63], v0, s[40:43], s51 offen offset:3072 sc1
buffer_load_dwordx4 v[64:67], v0, s[40:43], s52 offen sc1
buffer_load_dwordx4 v[68:71], v0, s[40:43], s52 offen offset:1024 sc1
buffer_load_dwordx4 v[112:115], v0, s[40:43], s61 offen sc1
buffer_load_dwordx4 v[116:119], v0, s[40:43], s61 offen offset:1024 sc1
buffer_load_dwordx4 v[120:123], v0, s[40:43], s61 offen offset:2048 sc1
buffer_load_dwordx4 v[124:127], v0, s[40:43], s61 offen offset:3072 sc1
s_add_u32 s61, s61, 4096
buffer_load_dwordx4 v[128:131], v0, s[40:43], s61 offen sc1
buffer_load_dwordx4 v[132:135], v0, s[40:43], s61 offen offset:1024 sc1
buffer_load_dwordx4 v[136:139], v0, s[40:43], s61 offen offset:2048 sc1
buffer_load_dwordx4 v[140:143], v0, s[40:43], s61 offen offset:3072 sc1
s_add_u32 s61, s61, 12288
buffer_load_dwordx4 v[144:147], v0, s[40:43], s61 offen sc1
buffer_load_dwordx4 v[148:151], v0, s[40:43], s61 offen offset:1024 sc1
buffer_load_dwordx4 v[152:155], v0, s[40:43], s61 offen offset:2048 sc1
buffer_load_dwordx4 v[156:159], v0, s[40:43], s61 offen offset:3072 sc1
s_add_u32 s61, s61, 4096
buffer_load_dwordx4 v[160:163], v0, s[40:43], s61 offen sc1
buffer_load_dwordx4 v[164:167], v0, s[40:43], s61 offen offset:1024 sc1
buffer_load_dwordx4 v[168:171], v0, s[40:43], s61 offen offset:2048 sc1
buffer_load_dwordx4 v[172:175], v0, s[40:43], s61 offen offset:3072 sc1
s_add_u32 s61, s61, 4096
buffer_load_dword v7, v8, s[40:43], s58 offen sc1
cw_mcomp_%=:
s_waitcnt vmcnt(26)
ds_write_b128 v0, v[32:35] offset:4096
s_waitcnt vmcnt(25)
ds_write_b128 v0, v[36:39] offset:5120
s_waitcnt vmcnt(24)
ds_write_b128 v0, v[40:43] offset:6144
s_waitcnt vmcnt(23)
ds_write_b128 v0, v[44:47] offset:7168
s_waitcnt vmcnt(22)
ds_write_b128 v0, v[48:51] offset:8192
s_waitcnt vmcnt(21)
ds_write_b128 v0, v[52:55] offset:9216
s_waitcnt vmcnt(20)
ds_write_b128 v0, v[56:59] offset:10240
s_waitcnt vmcnt(19)
ds_write_b128 v0, v[60:63] offset:11264
s_waitcnt vmcnt(18)
ds_write_b128 v0, v[64:67] offset:12288
s_waitcnt vmcnt(17)
ds_write_b128 v0, v[68:71] offset:13312
v_max_u32 v6, v32, v33
v_max3_u32 v6, v34, v35, v6
v_max3_u32 v6, v36, v37, v6
v_max3_u32 v6, v38, v39, v6
v_max3_u32 v6, v40, v41, v6
v_max3_u32 v6, v42, v43, v6
v_max3_u32 v6, v44, v45, v6
v_max3_u32 v6, v46, v47, v6
v_max3_u32 v6, v48, v49, v6
v_max3_u32 v6, v50, v51, v6
v_max3_u32 v6, v52, v53, v6
v_max3_u32 v6, v54, v55, v6
v_max3_u32 v6, v56, v57, v6
v_max3_u32 v6, v58, v59, v6
v_max3_u32 v6, v60, v61, v6
v_max3_u32 v6, v62, v63, v6
v_max3_u32 v6, v64, v65, v6
v_max3_u32 v6, v66, v67, v6
v_max3_u32 v6, v68, v69, v6
v_max3_u32 v6, v70, v71, v6
v_cmp_eq_u32 vcc, -1, v6
s_nop 4
s_cbranch_vccnz cw_mfail_%=
s_waitcnt lgkmcnt(0)
s_barrier
s_mul_i32 s50, s48, 4
s_and_b32 s51, s46, 3
s_add_u32 s50, s50, s51
s_mul_i32 s50, s50, 49152
s_add_u32 s51, s50, 4096
s_add_u32 s52, s50, 16384
s_add_u32 s53, s50, 20480
s_add_u32 s62, s50, 32768
s_add_u32 s49, s47, 1
s_and_b32 s49, s49, 3
s_mul_i32 s63, s49, 131072
s_add_u32 s63, s63, 786432
s_and_b32 s59, s47, 63
s_cbranch_scc1 cw_running_%=
s_lshr_b32 s49, s47, 5
s_and_b32 s54, s47, 1
s_add_u32 s49, s49, s54
s_and_b32 s54, s44, 7
s_lshl3_add_u32 s54, s49, s54
s_lshl_b32 s54, s54, 7
s_add_u32 s54, s54, 2380032
s_mov_b32 s60, 0
cw_u0_%=:
v_mov_b32 v10, 0
buffer_atomic_add v10, v5, s[40:43], s54 offen sc0
s_waitcnt vmcnt(0)
v_cmp_gt_u32 vcc, v18, v10
s_nop 4
s_cbranch_vccz cw_u0_ok_%=
s_sleep 3
s_add_u32 s60, s60, 1
s_cmp_gt_u32 s60, 65536
s_cbranch_scc1 cw_abort_%=
s_and_b32 s59, s60, 63
s_cbranch_scc1 cw_u0_%=
v_mov_b32 v10, 1024
buffer_load_dword v10, v10, s[40:43], s58 offen sc1
s_waitcnt vmcnt(0)
v_readfirstlane_b32 s59, v10
s_nop 0
s_cmp_lg_u32 s59, 0
s_nop 2
s_cbranch_scc1 cw_end_%=
s_branch cw_u0_%=
cw_u0_ok_%=:
v_add_u32 v8, 128, v8
buffer_load_dwordx4 v[112:115], v0, s[40:43], s61 offen sc1
buffer_load_dwordx4 v[116:119], v0, s[40:43], s61 offen offset:1024 sc1
buffer_load_dwordx4 v[120:123], v0, s[40:43], s61 offen offset:2048 sc1
buffer_load_dwordx4 v[124:127], v0, s[40:43], s61 offen offset:3072 sc1
s_add_u32 s61, s61, 4096
buffer_load_dwordx4 v[128:131], v0, s[40:43], s61 offen sc1
buffer_load_dwordx4 v[132:135], v0, s[40:43], s61 offen offset:1024 sc1
buffer_load_dwordx4 v[136:139], v0, s[40:43], s61 offen offset:2048 sc1
buffer_load_dwordx4 v[140:143], v0, s[40:43], s61 offen offset:3072 sc1
s_add_u32 s61, s61, 12288
buffer_load_dwordx4 v[144:147], v0, s[40:43], s61 offen sc1
buffer_load_dwordx4 v[148:151], v0, s[40:43], s61 offen offset:1024 sc1
buffer_load_dwordx4 v[152:155], v0, s[40:43], s61 offen offset:2048 sc1
buffer_load_dwordx4 v[156:159], v0, s[40:43], s61 offen offset:3072 sc1
s_add_u32 s61, s61, 4096
buffer_load_dwordx4 v[160:163], v0, s[40:43], s61 offen sc1
buffer_load_dwordx4 v[164:167], v0, s[40:43], s61 offen offset:1024 sc1
buffer_load_dwordx4 v[168:171], v0, s[40:43], s61 offen offset:2048 sc1
buffer_load_dwordx4 v[172:175], v0, s[40:43], s61 offen offset:3072 sc1
s_add_u32 s61, s61, 4096
buffer_load_dword v7, v8, s[40:43], s58 offen sc1
cw_running_%=:
s_and_b32 s59, s47, 63
s_cmp_eq_u32 s59, 63
s_cbranch_scc1 cw_last_%=
s_add_u32 s64, s65, s69
s_memrealtime s[56:57]
s_waitcnt lgkmcnt(0)
s_sub_u32 s59, s64, s56
s_cmp_gt_u32 s59, 1000
s_cbranch_scc1 cw_ws_go_%=
cw_ws_%=:
s_memrealtime s[56:57]
s_waitcnt lgkmcnt(0)
s_sub_u32 s59, s64, s56
s_cmp_gt_i32 s59, 3
s_cbranch_scc1 cw_ws_%=
cw_ws_f_%=:
s_sub_u32 s59, s59, 1
s_cmp_ge_i32 s59, 0
s_cbranch_scc1 cw_ws_f_%=
cw_ws_go_%=:
s_and_b32 s59, s47, 64
s_add_u32 s54, s47, s59
s_lshl_b32 s54, s54, 7
s_add_u32 s54, s54, 1310720
s_lshl_b32 s59, s46, 15
s_add_u32 s59, s59, s54
s_add_u32 s56, s40, s59
s_addc_u32 s57, s41, 0
s_load_dwordx2 s[64:65], s[56:57], 0x0
s_waitcnt lgkmcnt(0)
s_max_u32 s59, s64, s65
s_cmp_lg_u32 s59, -1
s_cbranch_scc1 cw_rs_ok_%=
s_and_b32 s59, s47, 64
s_add_u32 s54, s47, s59
)"
R"(s_lshl_b32 s54, s54, 7
s_add_u32 s54, s54, 1310720
s_mov_b32 s60, 0
cw_rs_%=:
buffer_load_dwordx2 v[10:11], v5, s[40:43], s54 offen sc1
s_waitcnt vmcnt(0)
v_readfirstlane_b32 s64, v10
v_readfirstlane_b32 s65, v11
s_nop 0
s_max_u32 s59, s64, s65
s_cmp_lg_u32 s59, -1
s_nop 1
s_cbranch_scc1 cw_rs_ok_%=
s_sleep 4
s_add_u32 s60, s60, 1
s_cmp_gt_u32 s60, 65536
s_cbranch_scc1 cw_abort_%=
s_and_b32 s59, s60, 63
s_cbranch_scc1 cw_rs_%=
v_mov_b32 v10, 1024
buffer_load_dword v10, v10, s[40:43], s58 offen sc1
s_waitcnt vmcnt(0)
v_readfirstlane_b32 s59, v10
s_nop 0
s_cmp_lg_u32 s59, 0
s_nop 2
s_cbranch_scc1 cw_end_%=
s_branch cw_rs_%=
cw_rs_ok_%=:
s_mov_b32 s60, 0
s_memrealtime s[56:57]
s_waitcnt lgkmcnt(0)
s_sub_u32 s59, s64, s56
s_cmp_gt_u32 s59, 1000
s_cbranch_scc1 cw_wu_go_%=
cw_wu_%=:
s_memrealtime s[56:57]
s_waitcnt lgkmcnt(0)
s_sub_u32 s59, s64, s56
s_cmp_gt_i32 s59, 15
s_cbranch_scc1 cw_wu_%=
s_memrealtime s[30:31]
s_nop 1
s_memrealtime s[18:19]
s_nop 1
s_memrealtime s[34:35]
s_nop 1
s_memrealtime s[36:37]
s_nop 1
s_memrealtime s[38:39]
s_nop 1
s_memrealtime s[94:95]
s_nop 1
s_memrealtime s[96:97]
s_nop 1
s_memrealtime s[98:99]
s_nop 1
s_getpc_b64 s[56:57]
cw_wu_pc_%=:
s_waitcnt lgkmcnt(0)
s_add_u32 s18, s18, s34
s_add_u32 s18, s18, s36
s_add_u32 s18, s18, s38
s_add_u32 s18, s18, s94
s_add_u32 s18, s18, s96
s_add_u32 s18, s18, s98
s_mul_i32 s34, s30, 7
s_sub_u32 s18, s18, s34
s_mul_i32 s18, s18, 3
s_sub_u32 s59, s64, s30
s_mul_i32 s59, s59, 23
s_sub_u32 s59, s59, s18
s_sub_u32 s59, s59, 170
s_max_i32 s59, s59, 0
s_min_i32 s59, s59, 252
s_andn2_b32 s59, s59, 3
s_sub_u32 s59, cw_wu_e_%=-cw_wu_pc_%=, s59
s_add_u32 s56, s56, s59
s_addc_u32 s57, s57, 0
s_setpc_b64 s[56:57]
s_nop 0
s_nop 0
s_nop 0
s_nop 0
s_nop 0
s_nop 0
s_nop 0
s_nop 0
s_nop 0
s_nop 0
s_nop 0
s_nop 0
s_nop 0
s_nop 0
s_nop 0
s_nop 0
s_nop 0
s_nop 0
s_nop 0
s_nop 0
s_nop 0
s_nop 0
s_nop 0
s_nop 0
s_nop 0
s_nop 0
s_nop 0
s_nop 0
s_nop 0
s_nop 0
s_nop 0
s_nop 0
s_nop 0
s_nop 0
s_nop 0
s_nop 0
s_nop 0
s_nop 0
s_nop 0
s_nop 0
s_nop 0
s_nop 0
s_nop 0
s_nop 0
s_nop 0
s_nop 0
s_nop 0
s_nop 0
s_nop 0
s_nop 0
s_nop 0
s_nop 0
s_nop 0
s_nop 0
s_nop 0
s_nop 0
s_nop 0
s_nop 0
s_nop 0
s_nop 0
s_nop 0
s_nop 0
s_nop 0
cw_wu_e_%=:
cw_wu_go_%=:
buffer_load_dwordx4 v[32:35], v0, s[40:43], s50 offen sc1
buffer_load_dwordx4 v[36:39], v0, s[40:43], s50 offen offset:1024 sc1
buffer_load_dwordx4 v[40:43], v0, s[40:43], s50 offen offset:2048 sc1
buffer_load_dwordx4 v[44:47], v0, s[40:43], s50 offen offset:3072 sc1
buffer_load_dwordx4 v[48:51], v0, s[40:43], s51 offen sc1
buffer_load_dwordx4 v[52:55], v0, s[40:43], s51 offen offset:1024 sc1
buffer_load_dwordx4 v[56:59], v0, s[40:43], s51 offen offset:2048 sc1
buffer_load_dwordx4 v[60:63], v0, s[40:43], s51 offen offset:3072 sc1
buffer_load_dwordx4 v[64:67], v0, s[40:43], s52 offen sc1
buffer_load_dwordx4 v[68:71], v0, s[40:43], s52 offen offset:1024 sc1
buffer_load_dwordx4 v[72:75], v0, s[40:43], s52 offen offset:2048 sc1
buffer_load_dwordx4 v[76:79], v0, s[40:43], s52 offen offset:3072 sc1
buffer_load_dwordx4 v[80:83], v0, s[40:43], s53 offen sc1
buffer_load_dwordx4 v[84:87], v0, s[40:43], s53 offen offset:1024 sc1
buffer_load_dwordx4 v[88:91], v0, s[40:43], s53 offen offset:2048 sc1
buffer_load_dwordx4 v[92:95], v0, s[40:43], s53 offen offset:3072 sc1
buffer_load_dwordx4 v[96:99], v0, s[40:43], s62 offen sc1
buffer_load_dwordx4 v[100:103], v0, s[40:43], s62 offen offset:1024 sc1
buffer_load_dwordx4 v[104:107], v0, s[40:43], s62 offen offset:2048 sc1
buffer_load_dwordx4 v[108:111], v0, s[40:43], s62 offen offset:3072 sc1
s_barrier
ds_read_b128 v[176:179], v0 offset:0
ds_read_b128 v[180:183], v0 offset:1024
ds_read_b128 v[184:187], v0 offset:2048
ds_read_b128 v[188:191], v0 offset:3072
cw_pcomp_%=:
s_waitcnt vmcnt(19)
v_mfma_f32_16x16x32_bf16 v[24:27], v[112:115], v[32:35], 0
s_waitcnt vmcnt(18)
v_mfma_f32_16x16x32_bf16 v[24:27], v[116:119], v[36:39], v[24:27]
s_waitcnt vmcnt(17)
v_mfma_f32_16x16x32_bf16 v[24:27], v[120:123], v[40:43], v[24:27]
s_waitcnt vmcnt(16)
v_mfma_f32_16x16x32_bf16 v[24:27], v[124:127], v[44:47], v[24:27]
s_waitcnt vmcnt(15)
v_mfma_f32_16x16x32_bf16 v[24:27], v[128:131], v[48:51], v[24:27]
s_waitcnt vmcnt(14)
v_mfma_f32_16x16x32_bf16 v[24:27], v[132:135], v[52:55], v[24:27]
s_waitcnt vmcnt(13)
v_mfma_f32_16x16x32_bf16 v[24:27], v[136:139], v[56:59], v[24:27]
s_waitcnt vmcnt(12)
v_mfma_f32_16x16x32_bf16 v[24:27], v[140:143], v[60:63], v[24:27]
s_waitcnt vmcnt(11)
v_mfma_f32_16x16x32_bf16 v[24:27], v[144:147], v[64:67], v[24:27]
s_waitcnt vmcnt(10)
v_mfma_f32_16x16x32_bf16 v[24:27], v[148:151], v[68:71], v[24:27]
s_waitcnt vmcnt(9)
v_mfma_f32_16x16x32_bf16 v[24:27], v[152:155], v[72:75], v[24:27]
s_waitcnt vmcnt(8)
v_mfma_f32_16x16x32_bf16 v[24:27], v[156:159], v[76:79], v[24:27]
s_waitcnt vmcnt(7)
v_mfma_f32_16x16x32_bf16 v[24:27], v[160:163], v[80:83], v[24:27]
s_waitcnt vmcnt(6)
v_mfma_f32_16x16x32_bf16 v[24:27], v[164:167], v[84:87], v[24:27]
s_waitcnt vmcnt(5)
)"
R"(v_mfma_f32_16x16x32_bf16 v[24:27], v[168:171], v[88:91], v[24:27]
s_waitcnt vmcnt(4)
v_mfma_f32_16x16x32_bf16 v[24:27], v[172:175], v[92:95], v[24:27]
s_waitcnt vmcnt(3) lgkmcnt(3)
v_mfma_f32_16x16x32_bf16 v[24:27], v[176:179], v[96:99], v[24:27]
s_waitcnt vmcnt(2) lgkmcnt(2)
v_mfma_f32_16x16x32_bf16 v[24:27], v[180:183], v[100:103], v[24:27]
s_waitcnt vmcnt(1) lgkmcnt(1)
v_mfma_f32_16x16x32_bf16 v[24:27], v[184:187], v[104:107], v[24:27]
s_waitcnt vmcnt(0) lgkmcnt(0)
v_mfma_f32_16x16x32_bf16 v[24:27], v[188:191], v[108:111], v[24:27]
v_mov_b32 v11, v11
v_mov_b32 v11, v11
v_mov_b32 v11, v11
v_mov_b32 v11, v11
v_mov_b32 v11, v11
v_mov_b32 v11, v11
v_mov_b32 v11, v11
v_mov_b32 v11, v11
v_cmp_u_f32 vcc, v24, v24
v_permlane32_swap_b32 v24, v26
v_permlane32_swap_b32 v25, v27
s_nop 0
v_add_f32_dpp v24, v26, v24 row_ror:8 row_mask:0xf bank_mask:0xf
v_add_f32_dpp v25, v27, v25 row_ror:8 row_mask:0xf bank_mask:0xf
v_cvt_pk_bf16_f32 v28, v24, v25
v_cvt_pk_bf16_f32 v30, v24, v25
s_nop 1
v_permlane16_swap_b32 v28, v30
v_mov_b32 v29, v28
v_mov_b32 v31, v30
s_nop 0
v_permlane32_swap_b32 v28, v29
v_permlane32_swap_b32 v30, v31
s_cbranch_vccnz cw_pfail_%=
cw_presume_%=:
buffer_store_dwordx4 v[28:31], v3, s[40:43], s63 offen sc1
s_memrealtime s[30:31]
s_nop 1
s_memrealtime s[18:19]
s_nop 1
s_memrealtime s[34:35]
s_nop 1
s_memrealtime s[36:37]
s_nop 1
s_memrealtime s[38:39]
s_nop 1
s_memrealtime s[94:95]
s_nop 1
s_memrealtime s[96:97]
s_nop 1
s_memrealtime s[98:99]
s_nop 1
s_waitcnt lgkmcnt(0)
s_add_u32 s18, s18, s34
s_add_u32 s18, s18, s36
s_add_u32 s18, s18, s38
s_add_u32 s18, s18, s94
s_add_u32 s18, s18, s96
s_add_u32 s18, s18, s98
s_mul_i32 s34, s30, 7
s_sub_u32 s18, s18, s34
s_mul_i32 s18, s18, 3
s_sub_u32 s59, s30, s64
s_mul_i32 s59, s59, 23
s_add_u32 s59, s59, s18
v_mov_b32 v10, s59
s_lshl_b32 s34, s47, 10
s_add_u32 s34, s34, 2386176
s_cmp_eq_u32 s60, 0
s_cselect_b32 s34, s34, 0x40000000
buffer_atomic_umax v10, v4, s[40:43], s34 offen
s_add_u32 s47, s47, 1
s_nop 0
s_branch cw_loopa_%=
cw_last_%=:
s_cmp_eq_u32 s47, 127
s_cbranch_scc1 cw_end_%=
buffer_store_dwordx4 v[192:195], v3, s[40:43], s63 offen sc1
s_waitcnt vmcnt(0)
s_barrier
s_add_u32 s47, s47, 1
s_branch cw_loopa_%=
cw_pagain_%=:
buffer_load_dwordx4 v[32:35], v0, s[40:43], s50 offen sc1
buffer_load_dwordx4 v[36:39], v0, s[40:43], s50 offen offset:1024 sc1
buffer_load_dwordx4 v[40:43], v0, s[40:43], s50 offen offset:2048 sc1
buffer_load_dwordx4 v[44:47], v0, s[40:43], s50 offen offset:3072 sc1
buffer_load_dwordx4 v[48:51], v0, s[40:43], s51 offen sc1
buffer_load_dwordx4 v[52:55], v0, s[40:43], s51 offen offset:1024 sc1
buffer_load_dwordx4 v[56:59], v0, s[40:43], s51 offen offset:2048 sc1
buffer_load_dwordx4 v[60:63], v0, s[40:43], s51 offen offset:3072 sc1
buffer_load_dwordx4 v[64:67], v0, s[40:43], s52 offen sc1
buffer_load_dwordx4 v[68:71], v0, s[40:43], s52 offen offset:1024 sc1
buffer_load_dwordx4 v[72:75], v0, s[40:43], s52 offen offset:2048 sc1
buffer_load_dwordx4 v[76:79], v0, s[40:43], s52 offen offset:3072 sc1
buffer_load_dwordx4 v[80:83], v0, s[40:43], s53 offen sc1
buffer_load_dwordx4 v[84:87], v0, s[40:43], s53 offen offset:1024 sc1
buffer_load_dwordx4 v[88:91], v0, s[40:43], s53 offen offset:2048 sc1
buffer_load_dwordx4 v[92:95], v0, s[40:43], s53 offen offset:3072 sc1
buffer_load_dwordx4 v[96:99], v0, s[40:43], s62 offen sc1
buffer_load_dwordx4 v[100:103], v0, s[40:43], s62 offen offset:1024 sc1
buffer_load_dwordx4 v[104:107], v0, s[40:43], s62 offen offset:2048 sc1
buffer_load_dwordx4 v[108:111], v0, s[40:43], s62 offen offset:3072 sc1
s_nop 1
s_branch cw_pcomp_%=
cw_mfail_%=:
s_sleep 6
s_add_u32 s60, s60, 1
s_cmp_gt_u32 s60, 65536
s_cbranch_scc1 cw_abort_%=
s_and_b32 s59, s60, 63
s_cbranch_scc1 cw_mretry_%=
v_mov_b32 v10, 1024
buffer_load_dword v10, v10, s[40:43], s58 offen sc1
s_waitcnt vmcnt(0)
v_readfirstlane_b32 s59, v10
s_nop 0
s_cmp_lg_u32 s59, 0
s_nop 2
s_cbranch_scc1 cw_end_%=
s_branch cw_mretry_%=
cw_start_%=:
v_mov_b32 v16, 273408
v_mov_b32 v17, 0
s_lshr_b32 s49, s47, 3
v_cmp_gt_u32 vcc, s49, v17
s_cbranch_vccz cw_cr_ok_%=
s_mov_b32 s60, 0
s_nop 2
cw_cr_poll_%=:
buffer_load_dword v17, v16, s[40:43], s58 offen sc1
s_waitcnt vmcnt(0)
v_cmp_gt_u32 vcc, s49, v17
s_nop 4
s_cbranch_vccz cw_cr_ok_%=
s_sleep 30
s_add_u32 s60, s60, 1
s_cmp_gt_u32 s60, 65536
s_cbranch_scc1 cw_abort_%=
s_and_b32 s59, s60, 63
s_cbranch_scc1 cw_cr_poll_%=
v_mov_b32 v10, 1024
buffer_load_dword v10, v10, s[40:43], s58 offen sc1
s_waitcnt vmcnt(0)
v_readfirstlane_b32 s59, v10
s_nop 0
s_cmp_lg_u32 s59, 0
s_nop 2
s_cbranch_scc1 cw_end_%=
s_branch cw_cr_poll_%=
cw_cr_ok_%=:
s_mov_b32 s60, 0
cw_mretry_%=:
buffer_load_dwordx4 v[32:35], v0, s[40:43], s50 offen sc1
buffer_load_dwordx4 v[36:39], v0, s[40:43], s50 offen offset:1024 sc1
buffer_load_dwordx4 v[40:43], v0, s[40:43], s50 offen offset:2048 sc1
buffer_load_dwordx4 v[44:47], v0, s[40:43], s50 offen offset:3072 sc1
buffer_load_dwordx4 v[48:51], v0, s[40:43], s51 offen sc1
buffer_load_dwordx4 v[52:55], v0, s[40:43], s51 offen offset:1024 sc1
buffer_load_dwordx4 v[56:59], v0, s[40:43], s51 offen offset:2048 sc1
buffer_load_dwordx4 v[60:63], v0, s[40:43], s51 offen offset:3072 sc1
buffer_load_dwordx4 v[64:67], v0, s[40:43], s52 offen sc1
buffer_load_dwordx4 v[68:71], v0, s[40:43], s52 offen offset:1024 sc1
s_waitcnt vmcnt(0)
s_nop 0
s_branch cw_mcomp_%=
cw_pfail_%=:
v_max_u32 v6, v32, v33
v_max3_u32 v6, v34, v35, v6
v_max3_u32 v6, v36, v37, v6
v_max3_u32 v6, v38, v39, v6
v_max3_u32 v6, v40, v41, v6
v_max3_u32 v6, v42, v43, v6
v_max3_u32 v6, v44, v45, v6
v_max3_u32 v6, v46, v47, v6
v_max3_u32 v6, v48, v49, v6
v_max3_u32 v6, v50, v51, v6
v_max3_u32 v6, v52, v53, v6
v_max3_u32 v6, v54, v55, v6
v_max3_u32 v6, v56, v57, v6
v_max3_u32 v6, v58, v59, v6
v_max3_u32 v6, v60, v61, v6
v_max3_u32 v6, v62, v63, v6
v_max3_u32 v6, v64, v65, v6
v_max3_u32 v6, v66, v67, v6
v_max3_u32 v6, v68, v69, v6
v_max3_u32 v6, v70, v71, v6
v_max3_u32 v6, v72, v73, v6
v_max3_u32 v6, v74, v75, v6
v_max3_u32 v6, v76, v77, v6
v_max3_u32 v6, v78, v79, v6
v_max3_u32 v6, v80, v81, v6
v_max3_u32 v6, v82, v83, v6
v_max3_u32 v6, v84, v85, v6
v_max3_u32 v6, v86, v87, v6
v_max3_u32 v6, v88, v89, v6
v_max3_u32 v6, v90, v91, v6
v_max3_u32 v6, v92, v93, v6
v_max3_u32 v6, v94, v95, v6
)"
R"(v_max3_u32 v6, v96, v97, v6
v_max3_u32 v6, v98, v99, v6
v_max3_u32 v6, v100, v101, v6
v_max3_u32 v6, v102, v103, v6
v_max3_u32 v6, v104, v105, v6
v_max3_u32 v6, v106, v107, v6
v_max3_u32 v6, v108, v109, v6
v_max3_u32 v6, v110, v111, v6
v_cmp_eq_u32 vcc, -1, v6
s_nop 4
s_cbranch_vccz cw_presume_%=
s_cmp_lg_u32 s60, 0
s_cbranch_scc1 cw_pspin_%=
s_mov_b32 s60, 1
s_branch cw_pagain_%=
cw_ppoll_%=:
buffer_load_dword v10, v2, s[40:43], s62 offen sc1
s_waitcnt vmcnt(0)
v_cmp_eq_u32 vcc, -1, v10
s_nop 4
s_cbranch_vccz cw_pagain_%=
cw_pspin_%=:
s_add_u32 s60, s60, 1
s_cmp_gt_u32 s60, 65536
s_cbranch_scc1 cw_abort_%=
s_and_b32 s59, s60, 63
s_cbranch_scc1 cw_ppoll_%=
v_mov_b32 v10, 1024
buffer_load_dword v10, v10, s[40:43], s58 offen sc1
s_waitcnt vmcnt(0)
v_readfirstlane_b32 s59, v10
s_nop 0
s_cmp_lg_u32 s59, 0
s_nop 2
s_cbranch_scc1 cw_end_%=
s_branch cw_ppoll_%=
cw_wx_%=:
s_mul_i32 s61, s45, 6
s_sub_u32 s61, s61, 6
s_min_u32 s61, s61, 10
s_lshl_b32 s48, s44, 4
s_add_u32 s48, s48, s61
s_mul_i32 s50, s48, 20480
s_lshl_b32 s49, s45, 15
s_add_u32 s49, s49, -18432
v_add_u32 v14, s49, v0
v_and_b32 v8, 7, v15
v_lshlrev_b32 v8, 7, v8
v_add_u32 v8, 277760, v8
v_mov_b32 v7, 0
s_mov_b32 s55, 16
v_cmp_gt_u32 vcc, s55, v7
s_cbranch_vccz cw_bgw_ok_%=
s_mov_b32 s60, 0
s_nop 2
cw_bgw_poll_%=:
buffer_load_dword v7, v8, s[40:43], s58 offen sc1
s_waitcnt vmcnt(0)
v_cmp_gt_u32 vcc, s55, v7
s_nop 4
s_cbranch_vccz cw_bgw_ok_%=
s_sleep 6
s_add_u32 s60, s60, 1
s_cmp_gt_u32 s60, 65536
s_cbranch_scc1 cw_abort_%=
s_and_b32 s59, s60, 63
s_cbranch_scc1 cw_bgw_poll_%=
v_mov_b32 v10, 1024
buffer_load_dword v10, v10, s[40:43], s58 offen sc1
s_waitcnt vmcnt(0)
v_readfirstlane_b32 s59, v10
s_nop 0
s_cmp_lg_u32 s59, 0
s_nop 2
s_cbranch_scc1 cw_end_%=
s_branch cw_bgw_poll_%=
cw_bgw_ok_%=:
v_mov_b32 v13, v14
v_mov_b32 v14, v13
s_and_b32 s48, s50, 4294959104
s_add_u32 s48, s48, s50
s_add_u32 s48, s48, 64421888
s_lshr_b32 s59, s48, 8
s_and_b32 s59, s59, 0x2000
s_add_u32 s48, s48, s59
s_add_u32 s50, s50, 4096
buffer_load_dwordx4 v[16:19], v0, s[40:43], s48 offen sc1
buffer_load_dwordx4 v[20:23], v0, s[40:43], s48 offen offset:1024 sc1
buffer_load_dwordx4 v[32:35], v0, s[40:43], s48 offen offset:2048 sc1
buffer_load_dwordx4 v[36:39], v0, s[40:43], s48 offen offset:3072 sc1
s_and_b32 s48, s50, 4294959104
s_add_u32 s48, s48, s50
s_add_u32 s48, s48, 64421888
s_lshr_b32 s59, s48, 8
s_and_b32 s59, s59, 0x2000
s_add_u32 s48, s48, s59
s_add_u32 s50, s50, 4096
buffer_load_dwordx4 v[48:51], v0, s[40:43], s48 offen sc1
buffer_load_dwordx4 v[52:55], v0, s[40:43], s48 offen offset:1024 sc1
buffer_load_dwordx4 v[64:67], v0, s[40:43], s48 offen offset:2048 sc1
buffer_load_dwordx4 v[68:71], v0, s[40:43], s48 offen offset:3072 sc1
s_and_b32 s48, s50, 4294959104
s_add_u32 s48, s48, s50
s_add_u32 s48, s48, 64421888
s_lshr_b32 s59, s48, 8
s_and_b32 s59, s59, 0x2000
s_add_u32 s48, s48, s59
s_add_u32 s50, s50, 4096
buffer_load_dwordx4 v[80:83], v0, s[40:43], s48 offen sc1
buffer_load_dwordx4 v[84:87], v0, s[40:43], s48 offen offset:1024 sc1
buffer_load_dwordx4 v[96:99], v0, s[40:43], s48 offen offset:2048 sc1
buffer_load_dwordx4 v[100:103], v0, s[40:43], s48 offen offset:3072 sc1
s_and_b32 s48, s50, 4294959104
s_add_u32 s48, s48, s50
s_add_u32 s48, s48, 64421888
s_lshr_b32 s59, s48, 8
s_and_b32 s59, s59, 0x2000
s_add_u32 s48, s48, s59
s_add_u32 s50, s50, 4096
buffer_load_dwordx4 v[112:115], v0, s[40:43], s48 offen sc1
buffer_load_dwordx4 v[116:119], v0, s[40:43], s48 offen offset:1024 sc1
buffer_load_dwordx4 v[120:123], v0, s[40:43], s48 offen offset:2048 sc1
buffer_load_dwordx4 v[124:127], v0, s[40:43], s48 offen offset:3072 sc1
s_and_b32 s48, s50, 4294959104
s_add_u32 s48, s48, s50
s_add_u32 s48, s48, 64421888
s_lshr_b32 s59, s48, 8
s_and_b32 s59, s59, 0x2000
s_add_u32 s48, s48, s59
s_add_u32 s50, s50, 4096
buffer_load_dwordx4 v[128:131], v0, s[40:43], s48 offen sc1
buffer_load_dwordx4 v[132:135], v0, s[40:43], s48 offen offset:1024 sc1
buffer_load_dwordx4 v[136:139], v0, s[40:43], s48 offen offset:2048 sc1
buffer_load_dwordx4 v[140:143], v0, s[40:43], s48 offen offset:3072 sc1
s_and_b32 s48, s50, 4294959104
s_add_u32 s48, s48, s50
s_add_u32 s48, s48, 64421888
s_lshr_b32 s59, s48, 8
s_and_b32 s59, s59, 0x2000
s_add_u32 s48, s48, s59
s_add_u32 s50, s50, 4096
buffer_load_dwordx4 a[0:3], v0, s[40:43], s48 offen sc1
buffer_load_dwordx4 a[4:7], v0, s[40:43], s48 offen offset:1024 sc1
buffer_load_dwordx4 a[32:35], v0, s[40:43], s48 offen offset:2048 sc1
buffer_load_dwordx4 a[36:39], v0, s[40:43], s48 offen offset:3072 sc1
s_and_b32 s48, s50, 4294959104
s_add_u32 s48, s48, s50
s_add_u32 s48, s48, 64421888
s_lshr_b32 s59, s48, 8
s_and_b32 s59, s59, 0x2000
s_add_u32 s48, s48, s59
s_add_u32 s50, s50, 4096
buffer_load_dwordx4 a[64:67], v0, s[40:43], s48 offen sc1
buffer_load_dwordx4 a[68:71], v0, s[40:43], s48 offen offset:1024 sc1
buffer_load_dwordx4 a[96:99], v0, s[40:43], s48 offen offset:2048 sc1
buffer_load_dwordx4 a[100:103], v0, s[40:43], s48 offen offset:3072 sc1
s_and_b32 s48, s50, 4294959104
s_add_u32 s48, s48, s50
s_add_u32 s48, s48, 64421888
s_lshr_b32 s59, s48, 8
s_and_b32 s59, s59, 0x2000
s_add_u32 s48, s48, s59
s_add_u32 s50, s50, 4096
buffer_load_dwordx4 a[128:131], v0, s[40:43], s48 offen sc1
buffer_load_dwordx4 a[132:135], v0, s[40:43], s48 offen offset:1024 sc1
buffer_load_dwordx4 a[160:163], v0, s[40:43], s48 offen offset:2048 sc1
buffer_load_dwordx4 a[164:167], v0, s[40:43], s48 offen offset:3072 sc1
s_and_b32 s48, s50, 4294959104
s_add_u32 s48, s48, s50
s_add_u32 s48, s48, 64421888
s_lshr_b32 s59, s48, 8
s_and_b32 s59, s59, 0x2000
s_add_u32 s48, s48, s59
s_add_u32 s50, s50, 4096
buffer_load_dwordx4 a[192:195], v0, s[40:43], s48 offen sc1
buffer_load_dwordx4 a[196:199], v0, s[40:43], s48 offen offset:1024 sc1
buffer_load_dwordx4 a[232:235], v0, s[40:43], s48 offen offset:2048 sc1
buffer_load_dwordx4 a[236:239], v0, s[40:43], s48 offen offset:3072 sc1
s_and_b32 s48, s50, 4294959104
s_add_u32 s48, s48, s50
s_add_u32 s48, s48, 64421888
s_lshr_b32 s59, s48, 8
s_and_b32 s59, s59, 0x2000
s_add_u32 s48, s48, s59
s_add_u32 s50, s50, 4096
buffer_load_dwordx4 v[176:179], v0, s[40:43], s48 offen sc1
buffer_load_dwordx4 v[180:183], v0, s[40:43], s48 offen offset:1024 sc1
buffer_load_dwordx4 v[216:219], v0, s[40:43], s48 offen offset:2048 sc1
buffer_load_dwordx4 v[220:223], v0, s[40:43], s48 offen offset:3072 sc1
s_and_b32 s48, s50, 4294959104
s_add_u32 s48, s48, s50
s_add_u32 s48, s48, 64421888
s_lshr_b32 s59, s48, 8
s_and_b32 s59, s59, 0x2000
s_add_u32 s48, s48, s59
s_add_u32 s50, s50, 4096
buffer_load_dwordx4 a[8:11], v0, s[40:43], s48 offen sc1
buffer_load_dwordx4 a[12:15], v0, s[40:43], s48 offen offset:1024 sc1
buffer_load_dwordx4 a[40:43], v0, s[40:43], s48 offen offset:2048 sc1
buffer_load_dwordx4 a[44:47], v0, s[40:43], s48 offen offset:3072 sc1
)"
R"(s_and_b32 s48, s50, 4294959104
s_add_u32 s48, s48, s50
s_add_u32 s48, s48, 64421888
s_lshr_b32 s59, s48, 8
s_and_b32 s59, s59, 0x2000
s_add_u32 s48, s48, s59
s_add_u32 s50, s50, 4096
buffer_load_dwordx4 a[72:75], v0, s[40:43], s48 offen sc1
buffer_load_dwordx4 a[76:79], v0, s[40:43], s48 offen offset:1024 sc1
buffer_load_dwordx4 a[104:107], v0, s[40:43], s48 offen offset:2048 sc1
buffer_load_dwordx4 a[108:111], v0, s[40:43], s48 offen offset:3072 sc1
s_and_b32 s48, s50, 4294959104
s_add_u32 s48, s48, s50
s_add_u32 s48, s48, 64421888
s_lshr_b32 s59, s48, 8
s_and_b32 s59, s59, 0x2000
s_add_u32 s48, s48, s59
s_add_u32 s50, s50, 4096
buffer_load_dwordx4 a[136:139], v0, s[40:43], s48 offen sc1
buffer_load_dwordx4 a[140:143], v0, s[40:43], s48 offen offset:1024 sc1
buffer_load_dwordx4 a[168:171], v0, s[40:43], s48 offen offset:2048 sc1
buffer_load_dwordx4 a[172:175], v0, s[40:43], s48 offen offset:3072 sc1
s_and_b32 s48, s50, 4294959104
s_add_u32 s48, s48, s50
s_add_u32 s48, s48, 64421888
s_lshr_b32 s59, s48, 8
s_and_b32 s59, s59, 0x2000
s_add_u32 s48, s48, s59
s_add_u32 s50, s50, 4096
buffer_load_dwordx4 a[200:203], v0, s[40:43], s48 offen sc1
buffer_load_dwordx4 a[204:207], v0, s[40:43], s48 offen offset:1024 sc1
buffer_load_dwordx4 a[240:243], v0, s[40:43], s48 offen offset:2048 sc1
buffer_load_dwordx4 a[244:247], v0, s[40:43], s48 offen offset:3072 sc1
s_and_b32 s48, s50, 4294959104
s_add_u32 s48, s48, s50
s_add_u32 s48, s48, 64421888
s_lshr_b32 s59, s48, 8
s_and_b32 s59, s59, 0x2000
s_add_u32 s48, s48, s59
s_add_u32 s50, s50, 4096
buffer_load_dwordx4 v[184:187], v0, s[40:43], s48 offen sc1
buffer_load_dwordx4 v[188:191], v0, s[40:43], s48 offen offset:1024 sc1
buffer_load_dwordx4 v[224:227], v0, s[40:43], s48 offen offset:2048 sc1
buffer_load_dwordx4 v[228:231], v0, s[40:43], s48 offen offset:3072 sc1
s_and_b32 s48, s50, 4294959104
s_add_u32 s48, s48, s50
s_add_u32 s48, s48, 64421888
s_lshr_b32 s59, s48, 8
s_and_b32 s59, s59, 0x2000
s_add_u32 s48, s48, s59
s_add_u32 s50, s50, 4096
buffer_load_dwordx4 v[24:27], v0, s[40:43], s48 offen sc1
buffer_load_dwordx4 v[28:31], v0, s[40:43], s48 offen offset:1024 sc1
buffer_load_dwordx4 v[40:43], v0, s[40:43], s48 offen offset:2048 sc1
s_waitcnt vmcnt(61)
buffer_load_dwordx4 v[44:47], v0, s[40:43], s48 offen offset:3072 sc1
s_and_b32 s48, s50, 4294959104
s_add_u32 s48, s48, s50
s_add_u32 s48, s48, 64421888
s_lshr_b32 s59, s48, 8
s_and_b32 s59, s59, 0x2000
s_add_u32 s48, s48, s59
s_add_u32 s50, s50, 4096
buffer_load_dwordx4 v[56:59], v0, s[40:43], s48 offen sc1
s_waitcnt vmcnt(61)
buffer_load_dwordx4 v[60:63], v0, s[40:43], s48 offen offset:1024 sc1
buffer_load_dwordx4 v[72:75], v0, s[40:43], s48 offen offset:2048 sc1
s_waitcnt vmcnt(61)
buffer_load_dwordx4 v[76:79], v0, s[40:43], s48 offen offset:3072 sc1
s_and_b32 s48, s50, 4294959104
s_add_u32 s48, s48, s50
s_add_u32 s48, s48, 64421888
s_lshr_b32 s59, s48, 8
s_and_b32 s59, s59, 0x2000
s_add_u32 s48, s48, s59
s_add_u32 s50, s50, 4096
buffer_load_dwordx4 v[88:91], v0, s[40:43], s48 offen sc1
s_waitcnt vmcnt(61)
buffer_load_dwordx4 v[92:95], v0, s[40:43], s48 offen offset:1024 sc1
buffer_load_dwordx4 v[104:107], v0, s[40:43], s48 offen offset:2048 sc1
s_waitcnt vmcnt(61)
buffer_load_dwordx4 v[108:111], v0, s[40:43], s48 offen offset:3072 sc1
s_and_b32 s48, s50, 4294959104
s_add_u32 s48, s48, s50
s_add_u32 s48, s48, 64421888
s_lshr_b32 s59, s48, 8
s_and_b32 s59, s59, 0x2000
s_add_u32 s48, s48, s59
s_add_u32 s50, s50, 4096
buffer_load_dwordx4 a[208:211], v0, s[40:43], s48 offen sc1
s_waitcnt vmcnt(61)
buffer_load_dwordx4 a[212:215], v0, s[40:43], s48 offen offset:1024 sc1
buffer_load_dwordx4 a[248:251], v0, s[40:43], s48 offen offset:2048 sc1
s_waitcnt vmcnt(61)
buffer_load_dwordx4 a[252:255], v0, s[40:43], s48 offen offset:3072 sc1
s_and_b32 s48, s50, 4294959104
s_add_u32 s48, s48, s50
s_add_u32 s48, s48, 64421888
s_lshr_b32 s59, s48, 8
s_and_b32 s59, s59, 0x2000
s_add_u32 s48, s48, s59
s_add_u32 s50, s50, 4096
buffer_load_dwordx4 v[192:195], v0, s[40:43], s48 offen sc1
s_waitcnt vmcnt(61)
buffer_load_dwordx4 v[196:199], v0, s[40:43], s48 offen offset:1024 sc1
buffer_load_dwordx4 v[232:235], v0, s[40:43], s48 offen offset:2048 sc1
s_waitcnt vmcnt(61)
buffer_load_dwordx4 v[236:239], v0, s[40:43], s48 offen offset:3072 sc1
s_and_b32 s48, s50, 4294959104
s_add_u32 s48, s48, s50
s_add_u32 s48, s48, 64421888
s_lshr_b32 s59, s48, 8
s_and_b32 s59, s59, 0x2000
s_add_u32 s48, s48, s59
s_add_u32 s50, s50, 4096
buffer_load_dwordx4 a[16:19], v0, s[40:43], s48 offen sc1
s_waitcnt vmcnt(61)
buffer_load_dwordx4 a[20:23], v0, s[40:43], s48 offen offset:1024 sc1
buffer_load_dwordx4 a[48:51], v0, s[40:43], s48 offen offset:2048 sc1
s_waitcnt vmcnt(61)
buffer_load_dwordx4 a[52:55], v0, s[40:43], s48 offen offset:3072 sc1
s_and_b32 s48, s50, 4294959104
s_add_u32 s48, s48, s50
s_add_u32 s48, s48, 64421888
s_lshr_b32 s59, s48, 8
s_and_b32 s59, s59, 0x2000
s_add_u32 s48, s48, s59
s_add_u32 s50, s50, 4096
buffer_load_dwordx4 a[80:83], v0, s[40:43], s48 offen sc1
s_waitcnt vmcnt(61)
buffer_load_dwordx4 a[84:87], v0, s[40:43], s48 offen offset:1024 sc1
buffer_load_dwordx4 a[112:115], v0, s[40:43], s48 offen offset:2048 sc1
s_waitcnt vmcnt(61)
buffer_load_dwordx4 a[116:119], v0, s[40:43], s48 offen offset:3072 sc1
s_and_b32 s48, s50, 4294959104
s_add_u32 s48, s48, s50
s_add_u32 s48, s48, 64421888
s_lshr_b32 s59, s48, 8
s_and_b32 s59, s59, 0x2000
s_add_u32 s48, s48, s59
s_add_u32 s50, s50, 4096
buffer_load_dwordx4 a[144:147], v0, s[40:43], s48 offen sc1
s_waitcnt vmcnt(61)
buffer_load_dwordx4 a[148:151], v0, s[40:43], s48 offen offset:1024 sc1
buffer_load_dwordx4 a[176:179], v0, s[40:43], s48 offen offset:2048 sc1
s_waitcnt vmcnt(61)
buffer_load_dwordx4 a[180:183], v0, s[40:43], s48 offen offset:3072 sc1
s_and_b32 s48, s50, 4294959104
s_add_u32 s48, s48, s50
s_add_u32 s48, s48, 64421888
s_lshr_b32 s59, s48, 8
s_and_b32 s59, s59, 0x2000
s_add_u32 s48, s48, s59
s_add_u32 s50, s50, 4096
buffer_load_dwordx4 a[216:219], v0, s[40:43], s48 offen sc1
s_waitcnt vmcnt(61)
buffer_load_dwordx4 a[220:223], v0, s[40:43], s48 offen offset:1024 sc1
buffer_load_dwordx4 v[160:163], v0, s[40:43], s48 offen offset:2048 sc1
s_waitcnt vmcnt(61)
buffer_load_dwordx4 v[164:167], v0, s[40:43], s48 offen offset:3072 sc1
s_and_b32 s48, s50, 4294959104
s_add_u32 s48, s48, s50
s_add_u32 s48, s48, 64421888
s_lshr_b32 s59, s48, 8
s_and_b32 s59, s59, 0x2000
s_add_u32 s48, s48, s59
s_add_u32 s50, s50, 4096
buffer_load_dwordx4 v[200:203], v0, s[40:43], s48 offen sc1
s_waitcnt vmcnt(61)
buffer_load_dwordx4 v[204:207], v0, s[40:43], s48 offen offset:1024 sc1
buffer_load_dwordx4 v[240:243], v0, s[40:43], s48 offen offset:2048 sc1
s_waitcnt vmcnt(61)
buffer_load_dwordx4 v[244:247], v0, s[40:43], s48 offen offset:3072 sc1
s_and_b32 s48, s50, 4294959104
s_add_u32 s48, s48, s50
s_add_u32 s48, s48, 64421888
s_lshr_b32 s59, s48, 8
s_and_b32 s59, s59, 0x2000
s_add_u32 s48, s48, s59
s_add_u32 s50, s50, 4096
buffer_load_dwordx4 a[24:27], v0, s[40:43], s48 offen sc1
s_waitcnt vmcnt(61)
buffer_load_dwordx4 a[28:31], v0, s[40:43], s48 offen offset:1024 sc1
buffer_load_dwordx4 a[56:59], v0, s[40:43], s48 offen offset:2048 sc1
s_waitcnt vmcnt(61)
buffer_load_dwordx4 a[60:63], v0, s[40:43], s48 offen offset:3072 sc1
s_and_b32 s48, s50, 4294959104
s_add_u32 s48, s48, s50
s_add_u32 s48, s48, 64421888
s_lshr_b32 s59, s48, 8
s_and_b32 s59, s59, 0x2000
s_add_u32 s48, s48, s59
s_add_u32 s50, s50, 4096
buffer_load_dwordx4 a[88:91], v0, s[40:43], s48 offen sc1
s_waitcnt vmcnt(61)
buffer_load_dwordx4 a[92:95], v0, s[40:43], s48 offen offset:1024 sc1
buffer_load_dwordx4 a[120:123], v0, s[40:43], s48 offen offset:2048 sc1
s_waitcnt vmcnt(61)
buffer_load_dwordx4 a[124:127], v0, s[40:43], s48 offen offset:3072 sc1
s_and_b32 s48, s50, 4294959104
)"
R"(s_add_u32 s48, s48, s50
s_add_u32 s48, s48, 64421888
s_lshr_b32 s59, s48, 8
s_and_b32 s59, s59, 0x2000
s_add_u32 s48, s48, s59
s_add_u32 s50, s50, 4096
buffer_load_dwordx4 a[152:155], v0, s[40:43], s48 offen sc1
s_waitcnt vmcnt(61)
buffer_load_dwordx4 a[156:159], v0, s[40:43], s48 offen offset:1024 sc1
buffer_load_dwordx4 a[184:187], v0, s[40:43], s48 offen offset:2048 sc1
s_waitcnt vmcnt(61)
buffer_load_dwordx4 a[188:191], v0, s[40:43], s48 offen offset:3072 sc1
s_and_b32 s48, s50, 4294959104
s_add_u32 s48, s48, s50
s_add_u32 s48, s48, 64421888
s_lshr_b32 s59, s48, 8
s_and_b32 s59, s59, 0x2000
s_add_u32 s48, s48, s59
s_add_u32 s50, s50, 4096
buffer_load_dwordx4 a[224:227], v0, s[40:43], s48 offen sc1
s_waitcnt vmcnt(61)
buffer_load_dwordx4 a[228:231], v0, s[40:43], s48 offen offset:1024 sc1
buffer_load_dwordx4 v[168:171], v0, s[40:43], s48 offen offset:2048 sc1
s_waitcnt vmcnt(61)
buffer_load_dwordx4 v[172:175], v0, s[40:43], s48 offen offset:3072 sc1
s_and_b32 s48, s50, 4294959104
s_add_u32 s48, s48, s50
s_add_u32 s48, s48, 64421888
s_lshr_b32 s59, s48, 8
s_and_b32 s59, s59, 0x2000
s_add_u32 s48, s48, s59
s_add_u32 s50, s50, 4096
buffer_load_dwordx4 v[208:211], v0, s[40:43], s48 offen sc1
s_waitcnt vmcnt(61)
buffer_load_dwordx4 v[212:215], v0, s[40:43], s48 offen offset:1024 sc1
buffer_load_dwordx4 v[248:251], v0, s[40:43], s48 offen offset:2048 sc1
s_waitcnt vmcnt(61)
buffer_load_dwordx4 v[252:255], v0, s[40:43], s48 offen offset:3072 sc1
ds_write_b128 v14, v[16:19] offset:0
ds_write_b128 v14, v[20:23] offset:1024
s_waitcnt vmcnt(59)
ds_write_b128 v14, v[24:27] offset:2048
s_waitcnt vmcnt(58)
ds_write_b128 v14, v[28:31] offset:3072
ds_write_b128 v14, v[32:35] offset:4096
ds_write_b128 v14, v[36:39] offset:5120
s_waitcnt vmcnt(57)
ds_write_b128 v14, v[40:43] offset:6144
s_waitcnt vmcnt(56)
ds_write_b128 v14, v[44:47] offset:7168
ds_write_b128 v14, v[48:51] offset:8192
ds_write_b128 v14, v[52:55] offset:9216
s_waitcnt vmcnt(55)
ds_write_b128 v14, v[56:59] offset:10240
s_waitcnt vmcnt(54)
ds_write_b128 v14, v[60:63] offset:11264
ds_write_b128 v14, v[64:67] offset:12288
ds_write_b128 v14, v[68:71] offset:13312
s_waitcnt vmcnt(53)
ds_write_b128 v14, v[72:75] offset:14336
s_waitcnt vmcnt(52)
ds_write_b128 v14, v[76:79] offset:15360
s_waitcnt lgkmcnt(14)
ds_write_b128 v14, v[80:83] offset:16384
ds_write_b128 v14, v[84:87] offset:17408
s_waitcnt lgkmcnt(14)
s_waitcnt vmcnt(51)
ds_write_b128 v14, v[88:91] offset:18432
s_waitcnt vmcnt(50)
ds_write_b128 v14, v[92:95] offset:19456
s_waitcnt lgkmcnt(14)
ds_write_b128 v14, v[96:99] offset:20480
ds_write_b128 v14, v[100:103] offset:21504
s_waitcnt lgkmcnt(14)
s_waitcnt vmcnt(49)
ds_write_b128 v14, v[104:107] offset:22528
s_waitcnt vmcnt(48)
ds_write_b128 v14, v[108:111] offset:23552
s_waitcnt lgkmcnt(14)
ds_write_b128 v14, v[112:115] offset:24576
ds_write_b128 v14, v[116:119] offset:25600
s_waitcnt lgkmcnt(14)
ds_write_b128 v14, v[120:123] offset:26624
ds_write_b128 v14, v[124:127] offset:27648
s_waitcnt lgkmcnt(14)
ds_write_b128 v14, v[128:131] offset:28672
ds_write_b128 v14, v[132:135] offset:29696
s_waitcnt lgkmcnt(14)
ds_write_b128 v14, v[136:139] offset:30720
ds_write_b128 v14, v[140:143] offset:31744
s_waitcnt lgkmcnt(14)
s_waitcnt vmcnt(0) lgkmcnt(0)
v_mov_b32 v134, v14
v_mov_b32 v128, -1
v_mov_b32 v129, -1
v_mov_b32 v130, -1
v_mov_b32 v131, -1
s_cmp_eq_u32 s45, 2
s_cselect_b32 s59, 1, 0
v_lshlrev_b32 v132, 7, v15
v_cmp_gt_u32 vcc, s59, v15
v_mov_b32 v10, 2147483632
s_nop 0
v_cndmask_b32 v132, v10, v132, vcc
v_mov_b32 v133, 1
s_mul_i32 s48, s44, 3277
s_lshr_b32 s48, s48, 16
s_lshl_b32 s48, s48, 7
s_add_u32 s65, s48, 2158592
s_mov_b32 s62, 0x40000000
s_mov_b32 s63, 0x40000000
s_mov_b32 s64, 0x40000000
v_and_b32 v9, 0xf0, v0
v_lshrrev_b32 v10, 1, v12
v_lshl_add_u32 v9, v10, 8, v9
v_and_b32 v10, 1, v12
v_lshl_add_u32 v9, v10, 3, v9
s_lshl_b32 s48, s61, 8
v_add_u32 v9, s48, v9
cw_loopb_%=:
v_mov_b32 v56, 0
v_mov_b32 v57, 0
v_mov_b32 v58, 0
v_mov_b32 v59, 0
v_mov_b32 v60, 0
v_mov_b32 v61, 0
v_mov_b32 v62, 0
v_mov_b32 v63, 0
v_mov_b32 v64, 0
v_mov_b32 v65, 0
v_mov_b32 v66, 0
v_mov_b32 v67, 0
v_mov_b32 v68, 0
v_mov_b32 v69, 0
v_mov_b32 v70, 0
v_mov_b32 v71, 0
v_mov_b32 v72, 0
v_mov_b32 v73, 0
v_mov_b32 v74, 0
v_mov_b32 v75, 0
v_mov_b32 v76, 0
v_mov_b32 v77, 0
v_mov_b32 v78, 0
v_mov_b32 v79, 0
ds_read_b128 v[80:83], v134 offset:0
ds_read_b128 v[84:87], v134 offset:1024
ds_read_b128 v[88:91], v134 offset:2048
ds_read_b128 v[92:95], v134 offset:3072
ds_read_b128 v[96:99], v134 offset:4096
ds_read_b128 v[100:103], v134 offset:5120
s_add_u32 s49, s47, 3
s_and_b32 s49, s49, 3
s_mul_i32 s54, s49, 131072
s_add_u32 s54, s54, 786432
s_cmp_eq_u32 s45, 1
s_cselect_b32 s54, s54, 0x40000000
s_waitcnt vmcnt(0)
s_waitcnt lgkmcnt(0)
s_barrier
ds_read_b128 v[16:19], v0 offset:4096
ds_read_b128 v[20:23], v0 offset:5120
ds_read_b128 v[24:27], v0 offset:6144
ds_read_b128 v[28:31], v0 offset:7168
ds_read_b128 v[32:35], v0 offset:8192
ds_read_b128 v[36:39], v0 offset:9216
ds_read_b128 v[40:43], v0 offset:10240
ds_read_b128 v[44:47], v0 offset:11264
ds_read_b128 v[48:51], v0 offset:12288
ds_read_b128 v[52:55], v0 offset:13312
s_waitcnt lgkmcnt(9)
v_smfmac_f32_16x16x64_bf16 v[56:59], v[16:19], v[80:87], v1
ds_read_b128 v[80:83], v134 offset:6144
v_smfmac_f32_16x16x64_bf16 v[60:63], v[16:19], a[0:7], v1
ds_read_b128 v[84:87], v134 offset:7168
v_smfmac_f32_16x16x64_bf16 v[64:67], v[16:19], a[8:15], v1
buffer_store_dwordx4 v[128:131], v3, s[40:43], s54 offen sc1
v_smfmac_f32_16x16x64_bf16 v[68:71], v[16:19], v[88:95], v1
ds_read_b128 v[88:91], v134 offset:8192
v_smfmac_f32_16x16x64_bf16 v[72:75], v[16:19], a[16:23], v1
ds_read_b128 v[92:95], v134 offset:9216
v_smfmac_f32_16x16x64_bf16 v[76:79], v[16:19], a[24:31], v1
buffer_store_dwordx4 v[104:107], v0, s[40:43], s62 offen offset:0 sc1
s_waitcnt lgkmcnt(12)
v_smfmac_f32_16x16x64_bf16 v[56:59], v[20:23], v[96:103], v1
ds_read_b128 v[96:99], v134 offset:10240
v_smfmac_f32_16x16x64_bf16 v[60:63], v[20:23], a[32:39], v1
ds_read_b128 v[100:103], v134 offset:11264
v_smfmac_f32_16x16x64_bf16 v[64:67], v[20:23], a[40:47], v1
buffer_store_dwordx4 v[108:111], v0, s[40:43], s62 offen offset:1024 sc1
s_waitcnt lgkmcnt(4)
v_smfmac_f32_16x16x64_bf16 v[68:71], v[20:23], v[80:87], v1
ds_read_b128 v[80:83], v134 offset:12288
v_smfmac_f32_16x16x64_bf16 v[72:75], v[20:23], a[48:55], v1
ds_read_b128 v[84:87], v134 offset:13312
v_smfmac_f32_16x16x64_bf16 v[76:79], v[20:23], a[56:63], v1
buffer_store_dwordx4 v[112:115], v0, s[40:43], s62 offen offset:2048 sc1
s_waitcnt lgkmcnt(4)
v_smfmac_f32_16x16x64_bf16 v[56:59], v[24:27], v[88:95], v1
ds_read_b128 v[88:91], v134 offset:14336
v_smfmac_f32_16x16x64_bf16 v[60:63], v[24:27], a[64:71], v1
)"
R"(ds_read_b128 v[92:95], v134 offset:15360
v_smfmac_f32_16x16x64_bf16 v[64:67], v[24:27], a[72:79], v1
buffer_store_dwordx4 v[116:119], v0, s[40:43], s62 offen offset:3072 sc1
s_waitcnt lgkmcnt(4)
v_smfmac_f32_16x16x64_bf16 v[68:71], v[24:27], v[96:103], v1
ds_read_b128 v[96:99], v134 offset:16384
v_smfmac_f32_16x16x64_bf16 v[72:75], v[24:27], a[80:87], v1
ds_read_b128 v[100:103], v134 offset:17408
v_smfmac_f32_16x16x64_bf16 v[76:79], v[24:27], a[88:95], v1
s_waitcnt lgkmcnt(4)
v_smfmac_f32_16x16x64_bf16 v[56:59], v[28:31], v[80:87], v1
ds_read_b128 v[80:83], v134 offset:18432
v_smfmac_f32_16x16x64_bf16 v[60:63], v[28:31], a[96:103], v1
ds_read_b128 v[84:87], v134 offset:19456
v_smfmac_f32_16x16x64_bf16 v[64:67], v[28:31], a[104:111], v1
s_waitcnt lgkmcnt(4)
v_smfmac_f32_16x16x64_bf16 v[68:71], v[28:31], v[88:95], v1
ds_read_b128 v[88:91], v134 offset:20480
v_smfmac_f32_16x16x64_bf16 v[72:75], v[28:31], a[112:119], v1
ds_read_b128 v[92:95], v134 offset:21504
v_smfmac_f32_16x16x64_bf16 v[76:79], v[28:31], a[120:127], v1
s_waitcnt lgkmcnt(4)
v_smfmac_f32_16x16x64_bf16 v[56:59], v[32:35], v[96:103], v1
ds_read_b128 v[96:99], v134 offset:22528
v_smfmac_f32_16x16x64_bf16 v[60:63], v[32:35], a[128:135], v1
ds_read_b128 v[100:103], v134 offset:23552
v_smfmac_f32_16x16x64_bf16 v[64:67], v[32:35], a[136:143], v1
s_waitcnt lgkmcnt(4)
v_smfmac_f32_16x16x64_bf16 v[68:71], v[32:35], v[80:87], v1
ds_read_b128 v[80:83], v134 offset:24576
v_smfmac_f32_16x16x64_bf16 v[72:75], v[32:35], a[144:151], v1
ds_read_b128 v[84:87], v134 offset:25600
v_smfmac_f32_16x16x64_bf16 v[76:79], v[32:35], a[152:159], v1
s_waitcnt lgkmcnt(4)
v_smfmac_f32_16x16x64_bf16 v[56:59], v[36:39], v[88:95], v1
ds_read_b128 v[88:91], v134 offset:26624
v_smfmac_f32_16x16x64_bf16 v[60:63], v[36:39], a[160:167], v1
ds_read_b128 v[92:95], v134 offset:27648
v_smfmac_f32_16x16x64_bf16 v[64:67], v[36:39], a[168:175], v1
s_waitcnt lgkmcnt(4)
v_smfmac_f32_16x16x64_bf16 v[68:71], v[36:39], v[96:103], v1
ds_read_b128 v[96:99], v134 offset:28672
v_smfmac_f32_16x16x64_bf16 v[72:75], v[36:39], a[176:183], v1
ds_read_b128 v[100:103], v134 offset:29696
v_smfmac_f32_16x16x64_bf16 v[76:79], v[36:39], a[184:191], v1
s_waitcnt lgkmcnt(4)
v_smfmac_f32_16x16x64_bf16 v[56:59], v[40:43], v[80:87], v1
ds_read_b128 v[80:83], v134 offset:30720
v_smfmac_f32_16x16x64_bf16 v[60:63], v[40:43], a[192:199], v1
ds_read_b128 v[84:87], v134 offset:31744
v_smfmac_f32_16x16x64_bf16 v[64:67], v[40:43], a[200:207], v1
v_smfmac_f32_16x16x64_bf16 v[68:71], v[40:43], a[208:215], v1
v_smfmac_f32_16x16x64_bf16 v[72:75], v[40:43], a[216:223], v1
v_smfmac_f32_16x16x64_bf16 v[76:79], v[40:43], a[224:231], v1
s_waitcnt lgkmcnt(4)
v_smfmac_f32_16x16x64_bf16 v[56:59], v[44:47], v[88:95], v1
v_smfmac_f32_16x16x64_bf16 v[60:63], v[44:47], a[232:239], v1
v_smfmac_f32_16x16x64_bf16 v[64:67], v[44:47], a[240:247], v1
v_smfmac_f32_16x16x64_bf16 v[68:71], v[44:47], a[248:255], v1
v_smfmac_f32_16x16x64_bf16 v[72:75], v[44:47], v[160:167], v1
v_smfmac_f32_16x16x64_bf16 v[76:79], v[44:47], v[168:175], v1
s_waitcnt lgkmcnt(2)
v_smfmac_f32_16x16x64_bf16 v[56:59], v[48:51], v[96:103], v1
v_smfmac_f32_16x16x64_bf16 v[60:63], v[48:51], v[176:183], v1
v_smfmac_f32_16x16x64_bf16 v[64:67], v[48:51], v[184:191], v1
v_smfmac_f32_16x16x64_bf16 v[68:71], v[48:51], v[192:199], v1
v_smfmac_f32_16x16x64_bf16 v[72:75], v[48:51], v[200:207], v1
v_smfmac_f32_16x16x64_bf16 v[76:79], v[48:51], v[208:215], v1
s_waitcnt lgkmcnt(0)
v_smfmac_f32_16x16x64_bf16 v[56:59], v[52:55], v[80:87], v1
v_smfmac_f32_16x16x64_bf16 v[60:63], v[52:55], v[216:223], v1
v_smfmac_f32_16x16x64_bf16 v[64:67], v[52:55], v[224:231], v1
v_smfmac_f32_16x16x64_bf16 v[68:71], v[52:55], v[232:239], v1
v_smfmac_f32_16x16x64_bf16 v[72:75], v[52:55], v[240:247], v1
v_smfmac_f32_16x16x64_bf16 v[76:79], v[52:55], v[248:255], v1
v_mov_b32 v11, v11
v_mov_b32 v11, v11
v_mov_b32 v11, v11
v_mov_b32 v11, v11
v_permlane32_swap_b32 v56, v60
v_permlane32_swap_b32 v57, v61
v_permlane32_swap_b32 v58, v62
v_permlane32_swap_b32 v59, v63
v_pk_add_f32 v[56:57], v[56:57], v[60:61]
v_pk_add_f32 v[58:59], v[58:59], v[62:63]
v_cvt_pk_bf16_f32 v60, v56, v57
v_cvt_pk_bf16_f32 v61, v58, v59
ds_write_b64 v9, v[60:61] offset:0
v_permlane32_swap_b32 v64, v68
v_permlane32_swap_b32 v65, v69
v_permlane32_swap_b32 v66, v70
v_permlane32_swap_b32 v67, v71
v_pk_add_f32 v[64:65], v[64:65], v[68:69]
v_pk_add_f32 v[66:67], v[66:67], v[70:71]
v_cvt_pk_bf16_f32 v68, v64, v65
v_cvt_pk_bf16_f32 v69, v66, v67
ds_write_b64 v9, v[68:69] offset:512
v_permlane32_swap_b32 v72, v76
v_permlane32_swap_b32 v73, v77
v_permlane32_swap_b32 v74, v78
v_permlane32_swap_b32 v75, v79
v_pk_add_f32 v[72:73], v[72:73], v[76:77]
v_pk_add_f32 v[74:75], v[74:75], v[78:79]
v_cvt_pk_bf16_f32 v76, v72, v73
v_cvt_pk_bf16_f32 v77, v74, v75
ds_write_b64 v9, v[76:77] offset:1024
s_waitcnt lgkmcnt(0)
s_barrier
s_waitcnt vmcnt(0)
buffer_atomic_add v133, v132, s[40:43], s63 offen
ds_read_b128 v[104:107], v0 offset:0
ds_read_b128 v[108:111], v0 offset:1024
ds_read_b128 v[112:115], v0 offset:2048
ds_read_b128 v[116:119], v0 offset:3072
s_and_b32 s62, s47, 31
s_mul_i32 s62, s62, 80
s_add_u32 s62, s62, s44
s_lshl_b32 s62, s62, 12
s_add_u32 s62, s62, 116850688
s_cmp_eq_u32 s45, 2
s_cselect_b32 s62, s62, 0x40000000
s_mul_i32 s63, s47, 1536
s_add_u32 s63, s63, s65
s_waitcnt lgkmcnt(0)
s_add_u32 s47, s47, 1
s_and_b32 s59, s47, 63
s_cbranch_scc1 cw_loopb_%=
buffer_store_dwordx4 v[104:107], v0, s[40:43], s62 offen offset:0 sc1
buffer_store_dwordx4 v[108:111], v0, s[40:43], s62 offen offset:1024 sc1
buffer_store_dwordx4 v[112:115], v0, s[40:43], s62 offen offset:2048 sc1
buffer_store_dwordx4 v[116:119], v0, s[40:43], s62 offen offset:3072 sc1
s_waitcnt vmcnt(0)
buffer_atomic_add v133, v132, s[40:43], s63 offen
s_mov_b32 s62, 0x40000000
s_mov_b32 s63, 0x40000000
s_mov_b32 s64, 0x40000000
s_cmp_lt_u32 s47, 128
s_cbranch_scc1 cw_loopb_%=
s_branch cw_end_%=
cw_abort_%=:
v_mov_b32 v10, 1
v_mov_b32 v11, 1024
buffer_store_dword v10, v11, s[40:43], s58 offen sc1
cw_end_%=:
s_endpgm
)"
    :: ROLE_INPUTS : ROLE_CLOBBERS);
    __builtin_unreachable();
}
DEV void role_helper(unsigned lo, unsigned hi, int id, int w, int aux, const Tune& tn, const void* wa, const void* wb, int bid) {
    asm volatile(
R"(s_mov_b32 s40, %0
s_mov_b32 s41, %1
s_mov_b32 s42, %2
s_mov_b32 s43, %3
s_mov_b32 s44, %4
s_mov_b32 s45, %5
s_mov_b32 s46, %6
s_mov_b32 s66, %7
s_mov_b32 s67, %8
s_mov_b32 s68, %9
s_mov_b32 s69, %10
s_mov_b32 s70, %11
s_mov_b32 s71, %12
s_mov_b32 s72, %13
s_mov_b32 s73, %14
s_mov_b32 s76, %15
s_mov_b32 s77, %16
s_mov_b32 s78, 7864320
s_mov_b32 s79, 0x20000
s_mov_b32 s80, %17
s_mov_b32 s81, %18
s_mov_b32 s82, 7864320
s_mov_b32 s83, 0x20000
s_mov_b32 s84, %19
s_mov_b64 exec, -1
v_mbcnt_lo_u32_b32 v15, -1, 0
v_mbcnt_hi_u32_b32 v15, -1, v15
v_lshlrev_b32 v0, 4, v15
s_mov_b32 s58, 2097152
s_mov_b32 s92, 0
s_mov_b32 s21, 0
s_mov_b32 s22, 0x3fb8aa3b
s_mov_b32 s23, s22
s_mov_b32 s24, 0xbfb8aa3b
s_mov_b32 s25, s24
s_cmp_lt_u32 s46, 3
s_cbranch_scc1 hp_ldw_%=
s_lshr_b32 s85, s84, 2
s_and_b32 s86, s84, 3
v_add_u32 v26, 0, v15
v_mul_u32_u24 v27, 2731, v26
v_lshrrev_b32 v27, 16, v27
v_mul_u32_u24 v16, 11904, v27
v_lshl_add_u32 v16, v26, 4, v16
v_add_u32 v26, v26, v27
s_mul_i32 s48, s45, 3200
v_lshl_add_u32 v19, v26, 4, s48
v_add_u32 v26, 64, v15
v_mul_u32_u24 v27, 2731, v26
v_lshrrev_b32 v27, 16, v27
v_mul_u32_u24 v17, 11904, v27
v_lshl_add_u32 v17, v26, 4, v17
v_add_u32 v26, v26, v27
s_mul_i32 s48, s45, 3200
v_lshl_add_u32 v20, v26, 4, s48
v_add_u32 v26, 128, v15
v_mul_u32_u24 v27, 2731, v26
v_lshrrev_b32 v27, 16, v27
v_mul_u32_u24 v18, 11904, v27
v_lshl_add_u32 v18, v26, 4, v18
v_add_u32 v26, v26, v27
s_mul_i32 s48, s45, 3200
v_lshl_add_u32 v21, v26, 4, s48
s_mul_i32 s48, s86, 160
s_lshl3_add_u32 s48, s45, s48
s_mul_i32 s48, s48, 12288
s_mul_i32 s49, s85, 384
s_add_u32 s48, s48, s49
buffer_load_dwordx4 v[32:35], v16, s[80:83], s48 offen
buffer_load_dwordx4 v[36:39], v17, s[80:83], s48 offen
buffer_load_dwordx4 v[40:43], v18, s[80:83], s48 offen
s_add_u32 s48, s48, 393216
buffer_load_dwordx4 v[44:47], v16, s[80:83], s48 offen
buffer_load_dwordx4 v[48:51], v17, s[80:83], s48 offen
buffer_load_dwordx4 v[52:55], v18, s[80:83], s48 offen
s_add_u32 s48, s48, 393216
buffer_load_dwordx4 v[56:59], v16, s[80:83], s48 offen
buffer_load_dwordx4 v[60:63], v17, s[80:83], s48 offen
buffer_load_dwordx4 v[64:67], v18, s[80:83], s48 offen
s_add_u32 s48, s48, 393216
buffer_load_dwordx4 v[68:71], v16, s[80:83], s48 offen
buffer_load_dwordx4 v[72:75], v17, s[80:83], s48 offen
buffer_load_dwordx4 v[76:79], v18, s[80:83], s48 offen
s_add_u32 s48, s48, 393216
buffer_load_dwordx4 v[80:83], v16, s[80:83], s48 offen
buffer_load_dwordx4 v[84:87], v17, s[80:83], s48 offen
buffer_load_dwordx4 v[88:91], v18, s[80:83], s48 offen
s_add_u32 s48, s48, 393216
v_add_u32 v116, 0, v15
v_mul_u32_u24 v117, 820, v116
v_lshrrev_b32 v117, 16, v117
v_lshlrev_b32 v117, 1, v117
v_xor_b32 v116, v116, v117
v_lshlrev_b32 v96, 4, v116
v_add_u32 v116, 64, v15
v_mul_u32_u24 v117, 820, v116
v_lshrrev_b32 v117, 16, v117
v_lshlrev_b32 v117, 1, v117
v_xor_b32 v116, v116, v117
v_lshlrev_b32 v97, 4, v116
v_add_u32 v116, 128, v15
v_mul_u32_u24 v117, 820, v116
v_lshrrev_b32 v117, 16, v117
v_lshlrev_b32 v117, 1, v117
v_xor_b32 v116, v116, v117
v_lshlrev_b32 v98, 4, v116
v_add_u32 v116, 192, v15
v_mul_u32_u24 v117, 820, v116
v_lshrrev_b32 v117, 16, v117
v_lshlrev_b32 v117, 1, v117
v_xor_b32 v116, v116, v117
v_lshlrev_b32 v99, 4, v116
v_add_u32 v116, 256, v15
v_mul_u32_u24 v117, 820, v116
v_lshrrev_b32 v117, 16, v117
v_lshlrev_b32 v117, 1, v117
v_xor_b32 v116, v116, v117
v_lshlrev_b32 v100, 4, v116
v_add_u32 v116, 320, v15
v_mul_u32_u24 v117, 820, v116
v_lshrrev_b32 v117, 16, v117
v_lshlrev_b32 v117, 1, v117
v_xor_b32 v116, v116, v117
v_lshlrev_b32 v101, 4, v116
v_add_u32 v116, 384, v15
v_mul_u32_u24 v117, 820, v116
v_lshrrev_b32 v117, 16, v117
v_lshlrev_b32 v117, 1, v117
v_xor_b32 v116, v116, v117
v_lshlrev_b32 v102, 4, v116
v_add_u32 v116, 448, v15
v_mul_u32_u24 v117, 820, v116
v_lshrrev_b32 v117, 16, v117
v_lshlrev_b32 v117, 1, v117
v_xor_b32 v116, v116, v117
v_lshlrev_b32 v103, 4, v116
v_add_u32 v116, 512, v15
v_mul_u32_u24 v117, 820, v116
v_lshrrev_b32 v117, 16, v117
v_lshlrev_b32 v117, 1, v117
v_xor_b32 v116, v116, v117
v_lshlrev_b32 v104, 4, v116
v_add_u32 v116, 576, v15
v_mul_u32_u24 v117, 820, v116
v_lshrrev_b32 v117, 16, v117
v_lshlrev_b32 v117, 1, v117
v_xor_b32 v116, v116, v117
v_lshlrev_b32 v105, 4, v116
v_lshrrev_b32 v116, 2, v15
v_mul_u32_u24 v116, 1280, v116
v_and_b32 v117, 3, v15
v_lshl_add_u32 v116, v117, 3, v116
v_bfe_u32 v117, v15, 2, 3
s_mul_i32 s48, s45, 10
s_add_u32 s48, s48, 0
v_xor_b32 v106, s48, v117
v_lshl_add_u32 v106, v106, 5, v116
s_mul_i32 s48, s45, 10
s_add_u32 s48, s48, 1
v_xor_b32 v107, s48, v117
v_lshl_add_u32 v107, v107, 5, v116
s_mul_i32 s48, s45, 10
s_add_u32 s48, s48, 2
v_xor_b32 v108, s48, v117
v_lshl_add_u32 v108, v108, 5, v116
s_mul_i32 s48, s45, 10
s_add_u32 s48, s48, 3
v_xor_b32 v109, s48, v117
v_lshl_add_u32 v109, v109, 5, v116
s_mul_i32 s48, s45, 10
s_add_u32 s48, s48, 4
v_xor_b32 v110, s48, v117
v_lshl_add_u32 v110, v110, 5, v116
s_mul_i32 s48, s45, 10
s_add_u32 s48, s48, 5
v_xor_b32 v111, s48, v117
v_lshl_add_u32 v111, v111, 5, v116
s_mul_i32 s48, s45, 10
s_add_u32 s48, s48, 6
v_xor_b32 v112, s48, v117
v_lshl_add_u32 v112, v112, 5, v116
s_mul_i32 s48, s45, 10
s_add_u32 s48, s48, 7
v_xor_b32 v113, s48, v117
v_lshl_add_u32 v113, v113, 5, v116
s_mul_i32 s48, s45, 10
s_add_u32 s48, s48, 8
v_xor_b32 v114, s48, v117
v_lshl_add_u32 v114, v114, 5, v116
s_mul_i32 s48, s45, 10
s_add_u32 s48, s48, 9
v_xor_b32 v115, s48, v117
v_lshl_add_u32 v115, v115, 5, v116
s_and_b32 s51, s45, 1
s_mul_i32 s51, s51, 10240
s_lshr_b32 s49, s45, 1
s_mul_i32 s49, s49, 5120
s_add_u32 s51, s51, s49
s_mul_i32 s50, s85, 245760
s_and_b32 s49, s45, 1
)"
R"(s_mul_i32 s49, s49, 10240
s_add_u32 s50, s50, s49
s_cmp_ge_u32 s45, 2
s_cselect_b64 vcc, -1, 0
v_cndmask_b32 v96, v96, v101, vcc
v_cndmask_b32 v97, v97, v102, vcc
v_cndmask_b32 v98, v98, v103, vcc
v_cndmask_b32 v99, v99, v104, vcc
v_cndmask_b32 v100, v100, v105, vcc
s_add_u32 s49, s50, 0
s_add_u32 m0, s51, 0
s_nop 0
buffer_load_dwordx4 v96, s[76:79], s49 offen lds
s_add_u32 m0, s51, 1024
s_nop 0
buffer_load_dwordx4 v97, s[76:79], s49 offen lds
s_add_u32 m0, s51, 2048
s_nop 0
buffer_load_dwordx4 v98, s[76:79], s49 offen lds
s_add_u32 m0, s51, 3072
s_nop 0
buffer_load_dwordx4 v99, s[76:79], s49 offen lds
s_add_u32 m0, s51, 4096
s_nop 0
buffer_load_dwordx4 v100, s[76:79], s49 offen lds
s_add_u32 s49, s50, 20480
s_add_u32 m0, s51, 20480
s_nop 0
buffer_load_dwordx4 v96, s[76:79], s49 offen lds
s_add_u32 m0, s51, 21504
s_nop 0
buffer_load_dwordx4 v97, s[76:79], s49 offen lds
s_add_u32 m0, s51, 22528
s_nop 0
buffer_load_dwordx4 v98, s[76:79], s49 offen lds
s_add_u32 m0, s51, 23552
s_nop 0
buffer_load_dwordx4 v99, s[76:79], s49 offen lds
s_add_u32 m0, s51, 24576
s_nop 0
buffer_load_dwordx4 v100, s[76:79], s49 offen lds
s_add_u32 s49, s50, 61440
s_add_u32 m0, s51, 40960
s_nop 0
buffer_load_dwordx4 v96, s[76:79], s49 offen lds
s_add_u32 m0, s51, 41984
s_nop 0
buffer_load_dwordx4 v97, s[76:79], s49 offen lds
s_add_u32 m0, s51, 43008
s_nop 0
buffer_load_dwordx4 v98, s[76:79], s49 offen lds
s_add_u32 m0, s51, 44032
s_nop 0
buffer_load_dwordx4 v99, s[76:79], s49 offen lds
s_add_u32 m0, s51, 45056
s_nop 0
buffer_load_dwordx4 v100, s[76:79], s49 offen lds
s_add_u32 s49, s50, 81920
s_add_u32 m0, s51, 61440
s_nop 0
buffer_load_dwordx4 v96, s[76:79], s49 offen lds
s_add_u32 m0, s51, 62464
s_nop 0
buffer_load_dwordx4 v97, s[76:79], s49 offen lds
s_add_u32 m0, s51, 63488
s_nop 0
buffer_load_dwordx4 v98, s[76:79], s49 offen lds
s_add_u32 m0, s51, 64512
s_nop 0
buffer_load_dwordx4 v99, s[76:79], s49 offen lds
s_add_u32 m0, s51, 65536
s_nop 0
buffer_load_dwordx4 v100, s[76:79], s49 offen lds
s_add_u32 s49, s50, 122880
s_add_u32 m0, s51, 81920
s_nop 0
buffer_load_dwordx4 v96, s[76:79], s49 offen lds
s_add_u32 m0, s51, 82944
s_nop 0
buffer_load_dwordx4 v97, s[76:79], s49 offen lds
s_add_u32 m0, s51, 83968
s_nop 0
buffer_load_dwordx4 v98, s[76:79], s49 offen lds
s_add_u32 m0, s51, 84992
s_nop 0
buffer_load_dwordx4 v99, s[76:79], s49 offen lds
s_add_u32 m0, s51, 86016
s_nop 0
buffer_load_dwordx4 v100, s[76:79], s49 offen lds
s_add_u32 s49, s50, 143360
s_add_u32 m0, s51, 102400
s_nop 0
buffer_load_dwordx4 v96, s[76:79], s49 offen lds
s_add_u32 m0, s51, 103424
s_nop 0
buffer_load_dwordx4 v97, s[76:79], s49 offen lds
s_add_u32 m0, s51, 104448
s_nop 0
buffer_load_dwordx4 v98, s[76:79], s49 offen lds
s_add_u32 m0, s51, 105472
s_nop 0
buffer_load_dwordx4 v99, s[76:79], s49 offen lds
s_add_u32 m0, s51, 106496
s_nop 0
buffer_load_dwordx4 v100, s[76:79], s49 offen lds
s_waitcnt vmcnt(25) lgkmcnt(0)
s_barrier
s_add_u32 s49, s50, 184320
s_add_u32 m0, s51, 122880
s_nop 0
buffer_load_dwordx4 v96, s[76:79], s49 offen lds
s_add_u32 m0, s51, 123904
s_nop 0
buffer_load_dwordx4 v97, s[76:79], s49 offen lds
s_add_u32 m0, s51, 124928
s_nop 0
buffer_load_dwordx4 v98, s[76:79], s49 offen lds
s_add_u32 m0, s51, 125952
s_nop 0
buffer_load_dwordx4 v99, s[76:79], s49 offen lds
s_add_u32 m0, s51, 126976
s_nop 0
buffer_load_dwordx4 v100, s[76:79], s49 offen lds
ds_read_b64_tr_b16 a[0:1], v106 offset:0
ds_read_b64_tr_b16 a[24:25], v107 offset:0
ds_read_b64_tr_b16 a[48:49], v108 offset:0
ds_read_b64_tr_b16 a[72:73], v109 offset:0
ds_read_b64_tr_b16 a[96:97], v110 offset:0
ds_read_b64_tr_b16 a[120:121], v111 offset:0
ds_read_b64_tr_b16 a[144:145], v112 offset:0
ds_read_b64_tr_b16 a[168:169], v113 offset:0
ds_read_b64_tr_b16 a[192:193], v114 offset:0
ds_read_b64_tr_b16 a[216:217], v115 offset:0
s_waitcnt vmcnt(25) lgkmcnt(0)
s_barrier
s_add_u32 s49, s50, 204800
s_add_u32 m0, s51, 0
s_nop 0
buffer_load_dwordx4 v96, s[76:79], s49 offen lds
s_add_u32 m0, s51, 1024
s_nop 0
buffer_load_dwordx4 v97, s[76:79], s49 offen lds
s_add_u32 m0, s51, 2048
s_nop 0
buffer_load_dwordx4 v98, s[76:79], s49 offen lds
s_add_u32 m0, s51, 3072
s_nop 0
buffer_load_dwordx4 v99, s[76:79], s49 offen lds
s_add_u32 m0, s51, 4096
s_nop 0
buffer_load_dwordx4 v100, s[76:79], s49 offen lds
ds_read_b64_tr_b16 a[2:3], v106 offset:20480
ds_read_b64_tr_b16 a[26:27], v107 offset:20480
ds_read_b64_tr_b16 a[50:51], v108 offset:20480
ds_read_b64_tr_b16 a[74:75], v109 offset:20480
ds_read_b64_tr_b16 a[98:99], v110 offset:20480
ds_read_b64_tr_b16 a[122:123], v111 offset:20480
ds_read_b64_tr_b16 a[146:147], v112 offset:20480
ds_read_b64_tr_b16 a[170:171], v113 offset:20480
ds_read_b64_tr_b16 a[194:195], v114 offset:20480
ds_read_b64_tr_b16 a[218:219], v115 offset:20480
s_waitcnt vmcnt(25) lgkmcnt(0)
s_barrier
s_add_u32 s49, s50, 40960
s_add_u32 m0, s51, 20480
s_nop 0
buffer_load_dwordx4 v96, s[76:79], s49 offen lds
s_add_u32 m0, s51, 21504
s_nop 0
buffer_load_dwordx4 v97, s[76:79], s49 offen lds
s_add_u32 m0, s51, 22528
s_nop 0
buffer_load_dwordx4 v98, s[76:79], s49 offen lds
s_add_u32 m0, s51, 23552
s_nop 0
buffer_load_dwordx4 v99, s[76:79], s49 offen lds
s_add_u32 m0, s51, 24576
s_nop 0
buffer_load_dwordx4 v100, s[76:79], s49 offen lds
ds_read_b64_tr_b16 a[4:5], v106 offset:40960
ds_read_b64_tr_b16 a[28:29], v107 offset:40960
ds_read_b64_tr_b16 a[52:53], v108 offset:40960
ds_read_b64_tr_b16 a[76:77], v109 offset:40960
ds_read_b64_tr_b16 a[100:101], v110 offset:40960
ds_read_b64_tr_b16 a[124:125], v111 offset:40960
ds_read_b64_tr_b16 a[148:149], v112 offset:40960
ds_read_b64_tr_b16 a[172:173], v113 offset:40960
ds_read_b64_tr_b16 a[196:197], v114 offset:40960
ds_read_b64_tr_b16 a[220:221], v115 offset:40960
s_waitcnt vmcnt(25) lgkmcnt(0)
s_barrier
s_add_u32 s49, s50, 102400
s_add_u32 m0, s51, 40960
s_nop 0
buffer_load_dwordx4 v96, s[76:79], s49 offen lds
s_add_u32 m0, s51, 41984
s_nop 0
buffer_load_dwordx4 v97, s[76:79], s49 offen lds
s_add_u32 m0, s51, 43008
s_nop 0
)"
R"(buffer_load_dwordx4 v98, s[76:79], s49 offen lds
s_add_u32 m0, s51, 44032
s_nop 0
buffer_load_dwordx4 v99, s[76:79], s49 offen lds
s_add_u32 m0, s51, 45056
s_nop 0
buffer_load_dwordx4 v100, s[76:79], s49 offen lds
v_add_u32 v106, 61440, v106
v_add_u32 v107, 61440, v107
v_add_u32 v108, 61440, v108
v_add_u32 v109, 61440, v109
v_add_u32 v110, 61440, v110
v_add_u32 v111, 61440, v111
v_add_u32 v112, 61440, v112
v_add_u32 v113, 61440, v113
v_add_u32 v114, 61440, v114
v_add_u32 v115, 61440, v115
ds_read_b64_tr_b16 a[6:7], v106 offset:0
ds_read_b64_tr_b16 a[30:31], v107 offset:0
ds_read_b64_tr_b16 a[54:55], v108 offset:0
ds_read_b64_tr_b16 a[78:79], v109 offset:0
ds_read_b64_tr_b16 a[102:103], v110 offset:0
ds_read_b64_tr_b16 a[126:127], v111 offset:0
ds_read_b64_tr_b16 a[150:151], v112 offset:0
ds_read_b64_tr_b16 a[174:175], v113 offset:0
ds_read_b64_tr_b16 a[198:199], v114 offset:0
ds_read_b64_tr_b16 a[222:223], v115 offset:0
s_waitcnt vmcnt(25) lgkmcnt(0)
s_barrier
s_add_u32 s49, s50, 163840
s_add_u32 m0, s51, 61440
s_nop 0
buffer_load_dwordx4 v96, s[76:79], s49 offen lds
s_add_u32 m0, s51, 62464
s_nop 0
buffer_load_dwordx4 v97, s[76:79], s49 offen lds
s_add_u32 m0, s51, 63488
s_nop 0
buffer_load_dwordx4 v98, s[76:79], s49 offen lds
s_add_u32 m0, s51, 64512
s_nop 0
buffer_load_dwordx4 v99, s[76:79], s49 offen lds
s_add_u32 m0, s51, 65536
s_nop 0
buffer_load_dwordx4 v100, s[76:79], s49 offen lds
ds_read_b64_tr_b16 a[8:9], v106 offset:20480
ds_read_b64_tr_b16 a[32:33], v107 offset:20480
ds_read_b64_tr_b16 a[56:57], v108 offset:20480
ds_read_b64_tr_b16 a[80:81], v109 offset:20480
ds_read_b64_tr_b16 a[104:105], v110 offset:20480
ds_read_b64_tr_b16 a[128:129], v111 offset:20480
ds_read_b64_tr_b16 a[152:153], v112 offset:20480
ds_read_b64_tr_b16 a[176:177], v113 offset:20480
ds_read_b64_tr_b16 a[200:201], v114 offset:20480
ds_read_b64_tr_b16 a[224:225], v115 offset:20480
s_waitcnt vmcnt(25) lgkmcnt(0)
s_barrier
s_add_u32 s49, s50, 225280
s_add_u32 m0, s51, 81920
s_nop 0
buffer_load_dwordx4 v96, s[76:79], s49 offen lds
s_add_u32 m0, s51, 82944
s_nop 0
buffer_load_dwordx4 v97, s[76:79], s49 offen lds
s_add_u32 m0, s51, 83968
s_nop 0
buffer_load_dwordx4 v98, s[76:79], s49 offen lds
s_add_u32 m0, s51, 84992
s_nop 0
buffer_load_dwordx4 v99, s[76:79], s49 offen lds
s_add_u32 m0, s51, 86016
s_nop 0
buffer_load_dwordx4 v100, s[76:79], s49 offen lds
ds_read_b64_tr_b16 a[10:11], v106 offset:40960
ds_read_b64_tr_b16 a[34:35], v107 offset:40960
ds_read_b64_tr_b16 a[58:59], v108 offset:40960
ds_read_b64_tr_b16 a[82:83], v109 offset:40960
ds_read_b64_tr_b16 a[106:107], v110 offset:40960
ds_read_b64_tr_b16 a[130:131], v111 offset:40960
ds_read_b64_tr_b16 a[154:155], v112 offset:40960
ds_read_b64_tr_b16 a[178:179], v113 offset:40960
ds_read_b64_tr_b16 a[202:203], v114 offset:40960
ds_read_b64_tr_b16 a[226:227], v115 offset:40960
s_waitcnt vmcnt(25) lgkmcnt(0)
s_barrier
v_add_u32 v106, 61440, v106
v_add_u32 v107, 61440, v107
v_add_u32 v108, 61440, v108
v_add_u32 v109, 61440, v109
v_add_u32 v110, 61440, v110
v_add_u32 v111, 61440, v111
v_add_u32 v112, 61440, v112
v_add_u32 v113, 61440, v113
v_add_u32 v114, 61440, v114
v_add_u32 v115, 61440, v115
ds_read_b64_tr_b16 a[12:13], v106 offset:0
ds_read_b64_tr_b16 a[36:37], v107 offset:0
ds_read_b64_tr_b16 a[60:61], v108 offset:0
ds_read_b64_tr_b16 a[84:85], v109 offset:0
ds_read_b64_tr_b16 a[108:109], v110 offset:0
ds_read_b64_tr_b16 a[132:133], v111 offset:0
ds_read_b64_tr_b16 a[156:157], v112 offset:0
ds_read_b64_tr_b16 a[180:181], v113 offset:0
ds_read_b64_tr_b16 a[204:205], v114 offset:0
ds_read_b64_tr_b16 a[228:229], v115 offset:0
s_waitcnt vmcnt(20) lgkmcnt(0)
s_barrier
v_add_u32 v106, -122880, v106
v_add_u32 v107, -122880, v107
v_add_u32 v108, -122880, v108
v_add_u32 v109, -122880, v109
v_add_u32 v110, -122880, v110
v_add_u32 v111, -122880, v111
v_add_u32 v112, -122880, v112
v_add_u32 v113, -122880, v113
v_add_u32 v114, -122880, v114
v_add_u32 v115, -122880, v115
ds_read_b64_tr_b16 a[14:15], v106 offset:0
ds_read_b64_tr_b16 a[38:39], v107 offset:0
ds_read_b64_tr_b16 a[62:63], v108 offset:0
ds_read_b64_tr_b16 a[86:87], v109 offset:0
ds_read_b64_tr_b16 a[110:111], v110 offset:0
ds_read_b64_tr_b16 a[134:135], v111 offset:0
ds_read_b64_tr_b16 a[158:159], v112 offset:0
ds_read_b64_tr_b16 a[182:183], v113 offset:0
ds_read_b64_tr_b16 a[206:207], v114 offset:0
ds_read_b64_tr_b16 a[230:231], v115 offset:0
s_waitcnt vmcnt(15) lgkmcnt(0)
s_barrier
ds_read_b64_tr_b16 a[16:17], v106 offset:20480
ds_read_b64_tr_b16 a[40:41], v107 offset:20480
ds_read_b64_tr_b16 a[64:65], v108 offset:20480
ds_read_b64_tr_b16 a[88:89], v109 offset:20480
ds_read_b64_tr_b16 a[112:113], v110 offset:20480
ds_read_b64_tr_b16 a[136:137], v111 offset:20480
ds_read_b64_tr_b16 a[160:161], v112 offset:20480
ds_read_b64_tr_b16 a[184:185], v113 offset:20480
ds_read_b64_tr_b16 a[208:209], v114 offset:20480
ds_read_b64_tr_b16 a[232:233], v115 offset:20480
s_waitcnt vmcnt(10) lgkmcnt(0)
s_barrier
ds_read_b64_tr_b16 a[18:19], v106 offset:40960
ds_read_b64_tr_b16 a[42:43], v107 offset:40960
ds_read_b64_tr_b16 a[66:67], v108 offset:40960
ds_read_b64_tr_b16 a[90:91], v109 offset:40960
ds_read_b64_tr_b16 a[114:115], v110 offset:40960
ds_read_b64_tr_b16 a[138:139], v111 offset:40960
ds_read_b64_tr_b16 a[162:163], v112 offset:40960
ds_read_b64_tr_b16 a[186:187], v113 offset:40960
ds_read_b64_tr_b16 a[210:211], v114 offset:40960
ds_read_b64_tr_b16 a[234:235], v115 offset:40960
s_waitcnt vmcnt(5) lgkmcnt(0)
s_barrier
v_add_u32 v106, 61440, v106
v_add_u32 v107, 61440, v107
v_add_u32 v108, 61440, v108
v_add_u32 v109, 61440, v109
v_add_u32 v110, 61440, v110
v_add_u32 v111, 61440, v111
v_add_u32 v112, 61440, v112
v_add_u32 v113, 61440, v113
v_add_u32 v114, 61440, v114
v_add_u32 v115, 61440, v115
ds_read_b64_tr_b16 a[20:21], v106 offset:0
ds_read_b64_tr_b16 a[44:45], v107 offset:0
ds_read_b64_tr_b16 a[68:69], v108 offset:0
ds_read_b64_tr_b16 a[92:93], v109 offset:0
ds_read_b64_tr_b16 a[116:117], v110 offset:0
ds_read_b64_tr_b16 a[140:141], v111 offset:0
ds_read_b64_tr_b16 a[164:165], v112 offset:0
ds_read_b64_tr_b16 a[188:189], v113 offset:0
ds_read_b64_tr_b16 a[212:213], v114 offset:0
ds_read_b64_tr_b16 a[236:237], v115 offset:0
s_waitcnt vmcnt(0) lgkmcnt(0)
s_barrier
ds_read_b64_tr_b16 a[22:23], v106 offset:20480
ds_read_b64_tr_b16 a[46:47], v107 offset:20480
ds_read_b64_tr_b16 a[70:71], v108 offset:20480
ds_read_b64_tr_b16 a[94:95], v109 offset:20480
ds_read_b64_tr_b16 a[118:119], v110 offset:20480
ds_read_b64_tr_b16 a[142:143], v111 offset:20480
ds_read_b64_tr_b16 a[166:167], v112 offset:20480
ds_read_b64_tr_b16 a[190:191], v113 offset:20480
ds_read_b64_tr_b16 a[214:215], v114 offset:20480
ds_read_b64_tr_b16 a[238:239], v115 offset:20480
s_waitcnt lgkmcnt(0)
s_barrier
ds_write_b128 v19, v[32:35] offset:0
ds_write_b128 v20, v[36:39] offset:0
ds_write_b128 v21, v[40:43] offset:0
ds_write_b128 v19, v[44:47] offset:12800
ds_write_b128 v20, v[48:51] offset:12800
ds_write_b128 v21, v[52:55] offset:12800
ds_write_b128 v19, v[56:59] offset:25600
ds_write_b128 v20, v[60:63] offset:25600
ds_write_b128 v21, v[64:67] offset:25600
ds_write_b128 v19, v[68:71] offset:38400
ds_write_b128 v20, v[72:75] offset:38400
ds_write_b128 v21, v[76:79] offset:38400
ds_write_b128 v19, v[80:83] offset:51200
)"
R"(ds_write_b128 v20, v[84:87] offset:51200
ds_write_b128 v21, v[88:91] offset:51200
v_and_b32 v22, 1, v15
v_and_b32 v26, 2, v15
v_lshl_add_u32 v22, v26, 2, v22
v_and_b32 v26, 4, v15
v_add_u32 v22, v22, v26
v_and_b32 v26, 8, v15
v_lshl_add_u32 v22, v26, 1, v22
v_mul_u32_u24 v22, 400, v22
v_lshrrev_b32 v26, 4, v15
v_lshl_add_u32 v22, v26, 3, v22
v_bfe_u32 v26, v15, 3, 1
v_mul_u32_u24 v26, 655360, v26
v_and_b32 v24, 0x370, v0
v_add_u32 v24, v24, v26
s_mul_i32 s48, s45, 13107200
s_add_u32 s48, s48, 64421888
v_add_u32 v24, s48, v24
s_lshr_b32 s50, s85, 1
s_mul_i32 s50, s50, 20480
s_and_b32 s48, s85, 1
s_lshl_b32 s48, s48, 7
s_add_u32 s50, s50, s48
s_mul_i32 s48, s86, 5120
s_add_u32 s50, s50, s48
s_movk_i32 s52, 8192
s_mov_b32 s51, 5
s_waitcnt lgkmcnt(0)
s_barrier
hp_bg_%=:
ds_read2_b64 v[96:99], v22 offset0:0 offset1:4
ds_read2_b64 v[100:103], v22 offset0:100 offset1:104
ds_read2_b64 v[104:107], v22 offset0:12 offset1:16
ds_read2_b64 v[108:111], v22 offset0:112 offset1:116
ds_read2_b64 v[112:115], v22 offset0:24 offset1:28
ds_read2_b64 v[116:119], v22 offset0:124 offset1:128
ds_read2_b64 v[120:123], v22 offset0:36 offset1:40
ds_read2_b64 v[124:127], v22 offset0:136 offset1:140
ds_read2_b64 v[128:131], v22 offset0:8 offset1:20
ds_read2_b64 v[132:135], v22 offset0:108 offset1:120
ds_read2_b64 v[136:139], v22 offset0:32 offset1:44
ds_read2_b64 v[140:143], v22 offset0:132 offset1:144
v_add_u32 v22, 12800, v22
s_and_b32 s53, s50, 4294959104
s_add_u32 s53, s53, s50
s_waitcnt lgkmcnt(11)
v_mfma_f32_16x16x32_bf16 v[144:147], v[96:99], a[0:3], 0
s_waitcnt lgkmcnt(10)
v_mfma_f32_16x16x32_bf16 v[148:151], v[100:103], a[0:3], 0
s_waitcnt lgkmcnt(9)
v_mfma_f32_16x16x32_bf16 v[144:147], v[104:107], a[4:7], v[144:147]
s_waitcnt lgkmcnt(8)
v_mfma_f32_16x16x32_bf16 v[148:151], v[108:111], a[4:7], v[148:151]
s_waitcnt lgkmcnt(7)
v_mfma_f32_16x16x32_bf16 v[144:147], v[112:115], a[8:11], v[144:147]
s_waitcnt lgkmcnt(6)
v_mfma_f32_16x16x32_bf16 v[148:151], v[116:119], a[8:11], v[148:151]
s_waitcnt lgkmcnt(5)
v_mfma_f32_16x16x32_bf16 v[144:147], v[120:123], a[12:15], v[144:147]
s_waitcnt lgkmcnt(4)
v_mfma_f32_16x16x32_bf16 v[148:151], v[124:127], a[12:15], v[148:151]
s_waitcnt lgkmcnt(3)
v_mfma_f32_16x16x32_bf16 v[144:147], v[128:131], a[16:19], v[144:147]
s_waitcnt lgkmcnt(2)
v_mfma_f32_16x16x32_bf16 v[148:151], v[132:135], a[16:19], v[148:151]
s_waitcnt lgkmcnt(1)
v_mfma_f32_16x16x32_bf16 v[144:147], v[136:139], a[20:23], v[144:147]
s_waitcnt lgkmcnt(0)
v_mfma_f32_16x16x32_bf16 v[148:151], v[140:143], a[20:23], v[148:151]
v_mfma_f32_16x16x32_bf16 v[152:155], v[96:99], a[24:27], 0
v_add_u32 v25, s53, v24
v_mfma_f32_16x16x32_bf16 v[156:159], v[100:103], a[24:27], 0
v_lshrrev_b32 v26, 8, v25
v_mfma_f32_16x16x32_bf16 v[152:155], v[104:107], a[28:31], v[152:155]
v_and_or_b32 v25, v26, s52, v25
v_mfma_f32_16x16x32_bf16 v[156:159], v[108:111], a[28:31], v[156:159]
s_add_u32 s53, s53, 1310720
v_mfma_f32_16x16x32_bf16 v[152:155], v[112:115], a[32:35], v[152:155]
v_mfma_f32_16x16x32_bf16 v[156:159], v[116:119], a[32:35], v[156:159]
v_mfma_f32_16x16x32_bf16 v[152:155], v[120:123], a[36:39], v[152:155]
v_cvt_pk_bf16_f32 v160, v144, v145
v_mfma_f32_16x16x32_bf16 v[156:159], v[124:127], a[36:39], v[156:159]
v_cvt_pk_bf16_f32 v161, v146, v147
v_mfma_f32_16x16x32_bf16 v[152:155], v[128:131], a[40:43], v[152:155]
v_cvt_pk_bf16_f32 v162, v148, v149
v_mfma_f32_16x16x32_bf16 v[156:159], v[132:135], a[40:43], v[156:159]
v_cvt_pk_bf16_f32 v163, v150, v151
v_mfma_f32_16x16x32_bf16 v[152:155], v[136:139], a[44:47], v[152:155]
buffer_store_dwordx4 v[160:163], v25, s[40:43], 0 offen sc1
v_mfma_f32_16x16x32_bf16 v[156:159], v[140:143], a[44:47], v[156:159]
v_mfma_f32_16x16x32_bf16 v[144:147], v[96:99], a[48:51], 0
v_add_u32 v25, s53, v24
v_mfma_f32_16x16x32_bf16 v[148:151], v[100:103], a[48:51], 0
v_lshrrev_b32 v26, 8, v25
v_mfma_f32_16x16x32_bf16 v[144:147], v[104:107], a[52:55], v[144:147]
v_and_or_b32 v25, v26, s52, v25
v_mfma_f32_16x16x32_bf16 v[148:151], v[108:111], a[52:55], v[148:151]
s_add_u32 s53, s53, 1310720
v_mfma_f32_16x16x32_bf16 v[144:147], v[112:115], a[56:59], v[144:147]
v_mfma_f32_16x16x32_bf16 v[148:151], v[116:119], a[56:59], v[148:151]
v_mfma_f32_16x16x32_bf16 v[144:147], v[120:123], a[60:63], v[144:147]
v_cvt_pk_bf16_f32 v164, v152, v153
v_mfma_f32_16x16x32_bf16 v[148:151], v[124:127], a[60:63], v[148:151]
v_cvt_pk_bf16_f32 v165, v154, v155
v_mfma_f32_16x16x32_bf16 v[144:147], v[128:131], a[64:67], v[144:147]
v_cvt_pk_bf16_f32 v166, v156, v157
v_mfma_f32_16x16x32_bf16 v[148:151], v[132:135], a[64:67], v[148:151]
v_cvt_pk_bf16_f32 v167, v158, v159
v_mfma_f32_16x16x32_bf16 v[144:147], v[136:139], a[68:71], v[144:147]
buffer_store_dwordx4 v[164:167], v25, s[40:43], 0 offen sc1
v_mfma_f32_16x16x32_bf16 v[148:151], v[140:143], a[68:71], v[148:151]
v_mfma_f32_16x16x32_bf16 v[152:155], v[96:99], a[72:75], 0
v_add_u32 v25, s53, v24
v_mfma_f32_16x16x32_bf16 v[156:159], v[100:103], a[72:75], 0
v_lshrrev_b32 v26, 8, v25
v_mfma_f32_16x16x32_bf16 v[152:155], v[104:107], a[76:79], v[152:155]
v_and_or_b32 v25, v26, s52, v25
v_mfma_f32_16x16x32_bf16 v[156:159], v[108:111], a[76:79], v[156:159]
s_add_u32 s53, s53, 1310720
v_mfma_f32_16x16x32_bf16 v[152:155], v[112:115], a[80:83], v[152:155]
v_mfma_f32_16x16x32_bf16 v[156:159], v[116:119], a[80:83], v[156:159]
v_mfma_f32_16x16x32_bf16 v[152:155], v[120:123], a[84:87], v[152:155]
v_cvt_pk_bf16_f32 v160, v144, v145
v_mfma_f32_16x16x32_bf16 v[156:159], v[124:127], a[84:87], v[156:159]
v_cvt_pk_bf16_f32 v161, v146, v147
v_mfma_f32_16x16x32_bf16 v[152:155], v[128:131], a[88:91], v[152:155]
v_cvt_pk_bf16_f32 v162, v148, v149
v_mfma_f32_16x16x32_bf16 v[156:159], v[132:135], a[88:91], v[156:159]
v_cvt_pk_bf16_f32 v163, v150, v151
v_mfma_f32_16x16x32_bf16 v[152:155], v[136:139], a[92:95], v[152:155]
buffer_store_dwordx4 v[160:163], v25, s[40:43], 0 offen sc1
v_mfma_f32_16x16x32_bf16 v[156:159], v[140:143], a[92:95], v[156:159]
v_mfma_f32_16x16x32_bf16 v[144:147], v[96:99], a[96:99], 0
v_add_u32 v25, s53, v24
v_mfma_f32_16x16x32_bf16 v[148:151], v[100:103], a[96:99], 0
v_lshrrev_b32 v26, 8, v25
v_mfma_f32_16x16x32_bf16 v[144:147], v[104:107], a[100:103], v[144:147]
v_and_or_b32 v25, v26, s52, v25
v_mfma_f32_16x16x32_bf16 v[148:151], v[108:111], a[100:103], v[148:151]
s_add_u32 s53, s53, 1310720
v_mfma_f32_16x16x32_bf16 v[144:147], v[112:115], a[104:107], v[144:147]
v_mfma_f32_16x16x32_bf16 v[148:151], v[116:119], a[104:107], v[148:151]
v_mfma_f32_16x16x32_bf16 v[144:147], v[120:123], a[108:111], v[144:147]
v_cvt_pk_bf16_f32 v164, v152, v153
v_mfma_f32_16x16x32_bf16 v[148:151], v[124:127], a[108:111], v[148:151]
v_cvt_pk_bf16_f32 v165, v154, v155
v_mfma_f32_16x16x32_bf16 v[144:147], v[128:131], a[112:115], v[144:147]
v_cvt_pk_bf16_f32 v166, v156, v157
v_mfma_f32_16x16x32_bf16 v[148:151], v[132:135], a[112:115], v[148:151]
v_cvt_pk_bf16_f32 v167, v158, v159
v_mfma_f32_16x16x32_bf16 v[144:147], v[136:139], a[116:119], v[144:147]
buffer_store_dwordx4 v[164:167], v25, s[40:43], 0 offen sc1
v_mfma_f32_16x16x32_bf16 v[148:151], v[140:143], a[116:119], v[148:151]
v_mfma_f32_16x16x32_bf16 v[152:155], v[96:99], a[120:123], 0
v_add_u32 v25, s53, v24
v_mfma_f32_16x16x32_bf16 v[156:159], v[100:103], a[120:123], 0
v_lshrrev_b32 v26, 8, v25
v_mfma_f32_16x16x32_bf16 v[152:155], v[104:107], a[124:127], v[152:155]
v_and_or_b32 v25, v26, s52, v25
v_mfma_f32_16x16x32_bf16 v[156:159], v[108:111], a[124:127], v[156:159]
s_add_u32 s53, s53, 1310720
v_mfma_f32_16x16x32_bf16 v[152:155], v[112:115], a[128:131], v[152:155]
v_mfma_f32_16x16x32_bf16 v[156:159], v[116:119], a[128:131], v[156:159]
v_mfma_f32_16x16x32_bf16 v[152:155], v[120:123], a[132:135], v[152:155]
v_cvt_pk_bf16_f32 v160, v144, v145
v_mfma_f32_16x16x32_bf16 v[156:159], v[124:127], a[132:135], v[156:159]
v_cvt_pk_bf16_f32 v161, v146, v147
v_mfma_f32_16x16x32_bf16 v[152:155], v[128:131], a[136:139], v[152:155]
v_cvt_pk_bf16_f32 v162, v148, v149
v_mfma_f32_16x16x32_bf16 v[156:159], v[132:135], a[136:139], v[156:159]
v_cvt_pk_bf16_f32 v163, v150, v151
v_mfma_f32_16x16x32_bf16 v[152:155], v[136:139], a[140:143], v[152:155]
buffer_store_dwordx4 v[160:163], v25, s[40:43], 0 offen sc1
v_mfma_f32_16x16x32_bf16 v[156:159], v[140:143], a[140:143], v[156:159]
v_mfma_f32_16x16x32_bf16 v[144:147], v[96:99], a[144:147], 0
v_add_u32 v25, s53, v24
v_mfma_f32_16x16x32_bf16 v[148:151], v[100:103], a[144:147], 0
v_lshrrev_b32 v26, 8, v25
v_mfma_f32_16x16x32_bf16 v[144:147], v[104:107], a[148:151], v[144:147]
v_and_or_b32 v25, v26, s52, v25
v_mfma_f32_16x16x32_bf16 v[148:151], v[108:111], a[148:151], v[148:151]
s_add_u32 s53, s53, 1310720
v_mfma_f32_16x16x32_bf16 v[144:147], v[112:115], a[152:155], v[144:147]
v_mfma_f32_16x16x32_bf16 v[148:151], v[116:119], a[152:155], v[148:151]
v_mfma_f32_16x16x32_bf16 v[144:147], v[120:123], a[156:159], v[144:147]
v_cvt_pk_bf16_f32 v164, v152, v153
v_mfma_f32_16x16x32_bf16 v[148:151], v[124:127], a[156:159], v[148:151]
v_cvt_pk_bf16_f32 v165, v154, v155
v_mfma_f32_16x16x32_bf16 v[144:147], v[128:131], a[160:163], v[144:147]
v_cvt_pk_bf16_f32 v166, v156, v157
v_mfma_f32_16x16x32_bf16 v[148:151], v[132:135], a[160:163], v[148:151]
v_cvt_pk_bf16_f32 v167, v158, v159
v_mfma_f32_16x16x32_bf16 v[144:147], v[136:139], a[164:167], v[144:147]
buffer_store_dwordx4 v[164:167], v25, s[40:43], 0 offen sc1
v_mfma_f32_16x16x32_bf16 v[148:151], v[140:143], a[164:167], v[148:151]
v_mfma_f32_16x16x32_bf16 v[152:155], v[96:99], a[168:171], 0
v_add_u32 v25, s53, v24
v_mfma_f32_16x16x32_bf16 v[156:159], v[100:103], a[168:171], 0
v_lshrrev_b32 v26, 8, v25
)"
R"(v_mfma_f32_16x16x32_bf16 v[152:155], v[104:107], a[172:175], v[152:155]
v_and_or_b32 v25, v26, s52, v25
v_mfma_f32_16x16x32_bf16 v[156:159], v[108:111], a[172:175], v[156:159]
s_add_u32 s53, s53, 1310720
v_mfma_f32_16x16x32_bf16 v[152:155], v[112:115], a[176:179], v[152:155]
v_mfma_f32_16x16x32_bf16 v[156:159], v[116:119], a[176:179], v[156:159]
v_mfma_f32_16x16x32_bf16 v[152:155], v[120:123], a[180:183], v[152:155]
v_cvt_pk_bf16_f32 v160, v144, v145
v_mfma_f32_16x16x32_bf16 v[156:159], v[124:127], a[180:183], v[156:159]
v_cvt_pk_bf16_f32 v161, v146, v147
v_mfma_f32_16x16x32_bf16 v[152:155], v[128:131], a[184:187], v[152:155]
v_cvt_pk_bf16_f32 v162, v148, v149
v_mfma_f32_16x16x32_bf16 v[156:159], v[132:135], a[184:187], v[156:159]
v_cvt_pk_bf16_f32 v163, v150, v151
v_mfma_f32_16x16x32_bf16 v[152:155], v[136:139], a[188:191], v[152:155]
buffer_store_dwordx4 v[160:163], v25, s[40:43], 0 offen sc1
v_mfma_f32_16x16x32_bf16 v[156:159], v[140:143], a[188:191], v[156:159]
v_mfma_f32_16x16x32_bf16 v[144:147], v[96:99], a[192:195], 0
v_add_u32 v25, s53, v24
v_mfma_f32_16x16x32_bf16 v[148:151], v[100:103], a[192:195], 0
v_lshrrev_b32 v26, 8, v25
v_mfma_f32_16x16x32_bf16 v[144:147], v[104:107], a[196:199], v[144:147]
v_and_or_b32 v25, v26, s52, v25
v_mfma_f32_16x16x32_bf16 v[148:151], v[108:111], a[196:199], v[148:151]
s_add_u32 s53, s53, 1310720
v_mfma_f32_16x16x32_bf16 v[144:147], v[112:115], a[200:203], v[144:147]
v_mfma_f32_16x16x32_bf16 v[148:151], v[116:119], a[200:203], v[148:151]
v_mfma_f32_16x16x32_bf16 v[144:147], v[120:123], a[204:207], v[144:147]
v_cvt_pk_bf16_f32 v164, v152, v153
v_mfma_f32_16x16x32_bf16 v[148:151], v[124:127], a[204:207], v[148:151]
v_cvt_pk_bf16_f32 v165, v154, v155
v_mfma_f32_16x16x32_bf16 v[144:147], v[128:131], a[208:211], v[144:147]
v_cvt_pk_bf16_f32 v166, v156, v157
v_mfma_f32_16x16x32_bf16 v[148:151], v[132:135], a[208:211], v[148:151]
v_cvt_pk_bf16_f32 v167, v158, v159
v_mfma_f32_16x16x32_bf16 v[144:147], v[136:139], a[212:215], v[144:147]
buffer_store_dwordx4 v[164:167], v25, s[40:43], 0 offen sc1
v_mfma_f32_16x16x32_bf16 v[148:151], v[140:143], a[212:215], v[148:151]
v_mfma_f32_16x16x32_bf16 v[152:155], v[96:99], a[216:219], 0
v_add_u32 v25, s53, v24
v_mfma_f32_16x16x32_bf16 v[156:159], v[100:103], a[216:219], 0
v_lshrrev_b32 v26, 8, v25
v_mfma_f32_16x16x32_bf16 v[152:155], v[104:107], a[220:223], v[152:155]
v_and_or_b32 v25, v26, s52, v25
v_mfma_f32_16x16x32_bf16 v[156:159], v[108:111], a[220:223], v[156:159]
s_add_u32 s53, s53, 1310720
v_mfma_f32_16x16x32_bf16 v[152:155], v[112:115], a[224:227], v[152:155]
v_mfma_f32_16x16x32_bf16 v[156:159], v[116:119], a[224:227], v[156:159]
v_mfma_f32_16x16x32_bf16 v[152:155], v[120:123], a[228:231], v[152:155]
v_cvt_pk_bf16_f32 v160, v144, v145
v_mfma_f32_16x16x32_bf16 v[156:159], v[124:127], a[228:231], v[156:159]
v_cvt_pk_bf16_f32 v161, v146, v147
v_mfma_f32_16x16x32_bf16 v[152:155], v[128:131], a[232:235], v[152:155]
v_cvt_pk_bf16_f32 v162, v148, v149
v_mfma_f32_16x16x32_bf16 v[156:159], v[132:135], a[232:235], v[156:159]
v_cvt_pk_bf16_f32 v163, v150, v151
v_mfma_f32_16x16x32_bf16 v[152:155], v[136:139], a[236:239], v[152:155]
buffer_store_dwordx4 v[160:163], v25, s[40:43], 0 offen sc1
v_mfma_f32_16x16x32_bf16 v[156:159], v[140:143], a[236:239], v[156:159]
v_add_u32 v25, s53, v24
v_lshrrev_b32 v26, 8, v25
v_and_or_b32 v25, v26, s52, v25
s_add_u32 s53, s53, 1310720
s_nop 1
v_cvt_pk_bf16_f32 v164, v152, v153
v_cvt_pk_bf16_f32 v165, v154, v155
v_cvt_pk_bf16_f32 v166, v156, v157
v_cvt_pk_bf16_f32 v167, v158, v159
buffer_store_dwordx4 v[164:167], v25, s[40:43], 0 offen sc1
s_add_u32 s50, s50, 1024
s_sub_u32 s51, s51, 1
s_cmp_lg_u32 s51, 0
s_cbranch_scc1 hp_bg_%=
s_cmp_eq_u32 s45, 0
s_cselect_b32 s59, 1, 0
v_lshlrev_b32 v26, 7, v15
v_cmp_gt_u32 vcc, s59, v15
v_mov_b32 v27, 2147483632
s_nop 0
v_cndmask_b32 v26, v27, v26, vcc
v_mov_b32 v27, 1
s_and_b32 s54, s84, 7
s_lshl_b32 s54, s54, 7
s_add_u32 s54, s54, 2374912
s_waitcnt vmcnt(0)
s_waitcnt lgkmcnt(0)
s_barrier
buffer_atomic_add v27, v26, s[40:43], s54 offen
s_nop 1
s_branch hp_layer_%=
hp_ldw_%=:
v_add_u32 v22, 0, v15
v_mul_u32_u24 v16, 2731, v22
v_lshrrev_b32 v16, 16, v16
v_mul_u32_u24 v16, 11904, v16
v_lshl_add_u32 v16, v22, 4, v16
v_add_u32 v22, 64, v15
v_mul_u32_u24 v17, 2731, v22
v_lshrrev_b32 v17, 16, v17
v_mul_u32_u24 v17, 11904, v17
v_lshl_add_u32 v17, v22, 4, v17
v_add_u32 v22, 128, v15
v_mul_u32_u24 v18, 2731, v22
v_lshrrev_b32 v18, 16, v18
v_mul_u32_u24 v18, 11904, v18
v_lshl_add_u32 v18, v22, 4, v18
v_bfe_u32 v19, v15, 2, 1
v_and_b32 v20, 8, v15
v_add_u32 v19, v19, v20
v_bfe_u32 v20, v15, 4, 1
v_lshl_add_u32 v19, v20, 2, v19
v_lshrrev_b32 v20, 5, v15
v_lshl_add_u32 v19, v20, 4, v19
v_mul_u32_u24 v19, 384, v19
v_and_b32 v20, 3, v15
v_lshl_add_u32 v19, v20, 3, v19
s_mul_i32 s48, s45, 96
v_add_u32 v19, s48, v19
v_add_u32 v20, 49152, v19
v_add_u32 v21, 98304, v19
s_mul_i32 s50, s45, 196608
s_mul_i32 s48, s44, 384
s_add_u32 s50, s50, s48
s_mul_i32 s51, s45, 6144
s_add_u32 m0, s51, 0
s_add_u32 s49, s50, 0
buffer_load_dwordx4 v16, s[76:79], s49 offen nt lds
s_add_u32 m0, s51, 1024
s_add_u32 s49, s50, 0
buffer_load_dwordx4 v17, s[76:79], s49 offen nt lds
s_add_u32 m0, s51, 2048
s_add_u32 s49, s50, 0
buffer_load_dwordx4 v18, s[76:79], s49 offen nt lds
s_add_u32 m0, s51, 3072
s_add_u32 s49, s50, 98304
buffer_load_dwordx4 v16, s[76:79], s49 offen nt lds
s_add_u32 m0, s51, 4096
s_add_u32 s49, s50, 98304
buffer_load_dwordx4 v17, s[76:79], s49 offen nt lds
s_add_u32 m0, s51, 5120
s_add_u32 s49, s50, 98304
buffer_load_dwordx4 v18, s[76:79], s49 offen nt lds
s_add_u32 m0, s51, 24576
s_add_u32 s49, s50, 786432
buffer_load_dwordx4 v16, s[76:79], s49 offen nt lds
s_add_u32 m0, s51, 25600
s_add_u32 s49, s50, 786432
buffer_load_dwordx4 v17, s[76:79], s49 offen nt lds
s_add_u32 m0, s51, 26624
s_add_u32 s49, s50, 786432
buffer_load_dwordx4 v18, s[76:79], s49 offen nt lds
s_add_u32 m0, s51, 27648
s_add_u32 s49, s50, 884736
buffer_load_dwordx4 v16, s[76:79], s49 offen nt lds
s_add_u32 m0, s51, 28672
s_add_u32 s49, s50, 884736
buffer_load_dwordx4 v17, s[76:79], s49 offen nt lds
s_add_u32 m0, s51, 29696
s_add_u32 s49, s50, 884736
buffer_load_dwordx4 v18, s[76:79], s49 offen nt lds
s_add_u32 m0, s51, 49152
s_add_u32 s49, s50, 1572864
buffer_load_dwordx4 v16, s[76:79], s49 offen nt lds
s_add_u32 m0, s51, 50176
s_add_u32 s49, s50, 1572864
buffer_load_dwordx4 v17, s[76:79], s49 offen nt lds
s_add_u32 m0, s51, 51200
s_add_u32 s49, s50, 1572864
buffer_load_dwordx4 v18, s[76:79], s49 offen nt lds
s_add_u32 m0, s51, 52224
s_add_u32 s49, s50, 1671168
buffer_load_dwordx4 v16, s[76:79], s49 offen nt lds
s_add_u32 m0, s51, 53248
s_add_u32 s49, s50, 1671168
buffer_load_dwordx4 v17, s[76:79], s49 offen nt lds
s_add_u32 m0, s51, 54272
s_add_u32 s49, s50, 1671168
buffer_load_dwordx4 v18, s[76:79], s49 offen nt lds
s_add_u32 m0, s51, 73728
s_add_u32 s49, s50, 2359296
buffer_load_dwordx4 v16, s[76:79], s49 offen nt lds
s_add_u32 m0, s51, 74752
s_add_u32 s49, s50, 2359296
buffer_load_dwordx4 v17, s[76:79], s49 offen nt lds
s_add_u32 m0, s51, 75776
s_add_u32 s49, s50, 2359296
buffer_load_dwordx4 v18, s[76:79], s49 offen nt lds
s_add_u32 m0, s51, 76800
s_add_u32 s49, s50, 2457600
buffer_load_dwordx4 v16, s[76:79], s49 offen nt lds
s_add_u32 m0, s51, 77824
s_add_u32 s49, s50, 2457600
buffer_load_dwordx4 v17, s[76:79], s49 offen nt lds
s_add_u32 m0, s51, 78848
s_add_u32 s49, s50, 2457600
buffer_load_dwordx4 v18, s[76:79], s49 offen nt lds
s_add_u32 m0, s51, 98304
s_add_u32 s49, s50, 3145728
buffer_load_dwordx4 v16, s[76:79], s49 offen nt lds
s_add_u32 m0, s51, 99328
)"
R"(s_add_u32 s49, s50, 3145728
buffer_load_dwordx4 v17, s[76:79], s49 offen nt lds
s_add_u32 m0, s51, 100352
s_add_u32 s49, s50, 3145728
buffer_load_dwordx4 v18, s[76:79], s49 offen nt lds
s_add_u32 m0, s51, 101376
s_add_u32 s49, s50, 3244032
buffer_load_dwordx4 v16, s[76:79], s49 offen nt lds
s_add_u32 m0, s51, 102400
s_add_u32 s49, s50, 3244032
buffer_load_dwordx4 v17, s[76:79], s49 offen nt lds
s_add_u32 m0, s51, 103424
s_add_u32 s49, s50, 3244032
buffer_load_dwordx4 v18, s[76:79], s49 offen nt lds
s_waitcnt vmcnt(24) lgkmcnt(0)
s_barrier
s_add_u32 m0, s51, 122880
s_add_u32 s49, s50, 3932160
buffer_load_dwordx4 v16, s[76:79], s49 offen nt lds
s_add_u32 m0, s51, 123904
s_add_u32 s49, s50, 3932160
buffer_load_dwordx4 v17, s[76:79], s49 offen nt lds
s_add_u32 m0, s51, 124928
s_add_u32 s49, s50, 3932160
buffer_load_dwordx4 v18, s[76:79], s49 offen nt lds
s_add_u32 m0, s51, 125952
s_add_u32 s49, s50, 4030464
buffer_load_dwordx4 v16, s[76:79], s49 offen nt lds
s_add_u32 m0, s51, 126976
s_add_u32 s49, s50, 4030464
buffer_load_dwordx4 v17, s[76:79], s49 offen nt lds
s_add_u32 m0, s51, 128000
s_add_u32 s49, s50, 4030464
buffer_load_dwordx4 v18, s[76:79], s49 offen nt lds
ds_read_b64_tr_b16 a[0:1], v19 offset:0
ds_read_b64_tr_b16 a[2:3], v19 offset:768
ds_read_b64_tr_b16 a[4:5], v19 offset:12288
ds_read_b64_tr_b16 a[6:7], v19 offset:13056
ds_read_b64_tr_b16 a[80:81], v19 offset:32
ds_read_b64_tr_b16 a[82:83], v19 offset:800
ds_read_b64_tr_b16 a[84:85], v19 offset:12320
ds_read_b64_tr_b16 a[86:87], v19 offset:13088
ds_read_b64_tr_b16 a[160:161], v19 offset:64
ds_read_b64_tr_b16 a[162:163], v19 offset:832
ds_read_b64_tr_b16 a[164:165], v19 offset:12352
ds_read_b64_tr_b16 a[166:167], v19 offset:13120
s_waitcnt vmcnt(24) lgkmcnt(0)
s_barrier
s_add_u32 m0, s51, 0
s_add_u32 s49, s50, 4718592
buffer_load_dwordx4 v16, s[76:79], s49 offen nt lds
s_add_u32 m0, s51, 1024
s_add_u32 s49, s50, 4718592
buffer_load_dwordx4 v17, s[76:79], s49 offen nt lds
s_add_u32 m0, s51, 2048
s_add_u32 s49, s50, 4718592
buffer_load_dwordx4 v18, s[76:79], s49 offen nt lds
s_add_u32 m0, s51, 3072
s_add_u32 s49, s50, 4816896
buffer_load_dwordx4 v16, s[76:79], s49 offen nt lds
s_add_u32 m0, s51, 4096
s_add_u32 s49, s50, 4816896
buffer_load_dwordx4 v17, s[76:79], s49 offen nt lds
s_add_u32 m0, s51, 5120
s_add_u32 s49, s50, 4816896
buffer_load_dwordx4 v18, s[76:79], s49 offen nt lds
ds_read_b64_tr_b16 a[8:9], v19 offset:24576
ds_read_b64_tr_b16 a[10:11], v19 offset:25344
ds_read_b64_tr_b16 a[12:13], v19 offset:36864
ds_read_b64_tr_b16 a[14:15], v19 offset:37632
ds_read_b64_tr_b16 a[88:89], v19 offset:24608
ds_read_b64_tr_b16 a[90:91], v19 offset:25376
ds_read_b64_tr_b16 a[92:93], v19 offset:36896
ds_read_b64_tr_b16 a[94:95], v19 offset:37664
ds_read_b64_tr_b16 a[168:169], v19 offset:24640
ds_read_b64_tr_b16 a[170:171], v19 offset:25408
ds_read_b64_tr_b16 a[172:173], v19 offset:36928
ds_read_b64_tr_b16 a[174:175], v19 offset:37696
s_waitcnt vmcnt(24) lgkmcnt(0)
s_barrier
s_add_u32 m0, s51, 24576
s_add_u32 s49, s50, 5505024
buffer_load_dwordx4 v16, s[76:79], s49 offen nt lds
s_add_u32 m0, s51, 25600
s_add_u32 s49, s50, 5505024
buffer_load_dwordx4 v17, s[76:79], s49 offen nt lds
s_add_u32 m0, s51, 26624
s_add_u32 s49, s50, 5505024
buffer_load_dwordx4 v18, s[76:79], s49 offen nt lds
s_add_u32 m0, s51, 27648
s_add_u32 s49, s50, 5603328
buffer_load_dwordx4 v16, s[76:79], s49 offen nt lds
s_add_u32 m0, s51, 28672
s_add_u32 s49, s50, 5603328
buffer_load_dwordx4 v17, s[76:79], s49 offen nt lds
s_add_u32 m0, s51, 29696
s_add_u32 s49, s50, 5603328
buffer_load_dwordx4 v18, s[76:79], s49 offen nt lds
ds_read_b64_tr_b16 a[16:17], v20 offset:0
ds_read_b64_tr_b16 a[18:19], v20 offset:768
ds_read_b64_tr_b16 a[20:21], v20 offset:12288
ds_read_b64_tr_b16 a[22:23], v20 offset:13056
ds_read_b64_tr_b16 a[96:97], v20 offset:32
ds_read_b64_tr_b16 a[98:99], v20 offset:800
ds_read_b64_tr_b16 a[100:101], v20 offset:12320
ds_read_b64_tr_b16 a[102:103], v20 offset:13088
ds_read_b64_tr_b16 a[176:177], v20 offset:64
ds_read_b64_tr_b16 a[178:179], v20 offset:832
ds_read_b64_tr_b16 a[180:181], v20 offset:12352
ds_read_b64_tr_b16 a[182:183], v20 offset:13120
s_waitcnt vmcnt(24) lgkmcnt(0)
s_barrier
s_add_u32 m0, s51, 49152
s_add_u32 s49, s50, 6291456
buffer_load_dwordx4 v16, s[76:79], s49 offen nt lds
s_add_u32 m0, s51, 50176
s_add_u32 s49, s50, 6291456
buffer_load_dwordx4 v17, s[76:79], s49 offen nt lds
s_add_u32 m0, s51, 51200
s_add_u32 s49, s50, 6291456
buffer_load_dwordx4 v18, s[76:79], s49 offen nt lds
s_add_u32 m0, s51, 52224
s_add_u32 s49, s50, 6389760
buffer_load_dwordx4 v16, s[76:79], s49 offen nt lds
s_add_u32 m0, s51, 53248
s_add_u32 s49, s50, 6389760
buffer_load_dwordx4 v17, s[76:79], s49 offen nt lds
s_add_u32 m0, s51, 54272
s_add_u32 s49, s50, 6389760
buffer_load_dwordx4 v18, s[76:79], s49 offen nt lds
ds_read_b64_tr_b16 a[24:25], v20 offset:24576
ds_read_b64_tr_b16 a[26:27], v20 offset:25344
ds_read_b64_tr_b16 a[28:29], v20 offset:36864
ds_read_b64_tr_b16 a[30:31], v20 offset:37632
ds_read_b64_tr_b16 a[104:105], v20 offset:24608
ds_read_b64_tr_b16 a[106:107], v20 offset:25376
ds_read_b64_tr_b16 a[108:109], v20 offset:36896
ds_read_b64_tr_b16 a[110:111], v20 offset:37664
ds_read_b64_tr_b16 a[184:185], v20 offset:24640
ds_read_b64_tr_b16 a[186:187], v20 offset:25408
ds_read_b64_tr_b16 a[188:189], v20 offset:36928
ds_read_b64_tr_b16 a[190:191], v20 offset:37696
s_waitcnt vmcnt(24) lgkmcnt(0)
s_barrier
s_add_u32 m0, s51, 73728
s_add_u32 s49, s50, 7077888
buffer_load_dwordx4 v16, s[76:79], s49 offen nt lds
s_add_u32 m0, s51, 74752
s_add_u32 s49, s50, 7077888
buffer_load_dwordx4 v17, s[76:79], s49 offen nt lds
s_add_u32 m0, s51, 75776
s_add_u32 s49, s50, 7077888
buffer_load_dwordx4 v18, s[76:79], s49 offen nt lds
s_add_u32 m0, s51, 76800
s_add_u32 s49, s50, 7176192
buffer_load_dwordx4 v16, s[76:79], s49 offen nt lds
s_add_u32 m0, s51, 77824
s_add_u32 s49, s50, 7176192
buffer_load_dwordx4 v17, s[76:79], s49 offen nt lds
s_add_u32 m0, s51, 78848
s_add_u32 s49, s50, 7176192
buffer_load_dwordx4 v18, s[76:79], s49 offen nt lds
ds_read_b64_tr_b16 a[32:33], v21 offset:0
ds_read_b64_tr_b16 a[34:35], v21 offset:768
ds_read_b64_tr_b16 a[36:37], v21 offset:12288
ds_read_b64_tr_b16 a[38:39], v21 offset:13056
ds_read_b64_tr_b16 a[112:113], v21 offset:32
ds_read_b64_tr_b16 a[114:115], v21 offset:800
ds_read_b64_tr_b16 a[116:117], v21 offset:12320
ds_read_b64_tr_b16 a[118:119], v21 offset:13088
ds_read_b64_tr_b16 a[192:193], v21 offset:64
ds_read_b64_tr_b16 a[194:195], v21 offset:832
ds_read_b64_tr_b16 a[196:197], v21 offset:12352
ds_read_b64_tr_b16 a[198:199], v21 offset:13120
s_waitcnt vmcnt(24) lgkmcnt(0)
s_barrier
ds_read_b64_tr_b16 a[40:41], v21 offset:24576
ds_read_b64_tr_b16 a[42:43], v21 offset:25344
ds_read_b64_tr_b16 a[44:45], v21 offset:36864
ds_read_b64_tr_b16 a[46:47], v21 offset:37632
ds_read_b64_tr_b16 a[120:121], v21 offset:24608
ds_read_b64_tr_b16 a[122:123], v21 offset:25376
ds_read_b64_tr_b16 a[124:125], v21 offset:36896
ds_read_b64_tr_b16 a[126:127], v21 offset:37664
ds_read_b64_tr_b16 a[200:201], v21 offset:24640
ds_read_b64_tr_b16 a[202:203], v21 offset:25408
ds_read_b64_tr_b16 a[204:205], v21 offset:36928
ds_read_b64_tr_b16 a[206:207], v21 offset:37696
s_waitcnt vmcnt(18) lgkmcnt(0)
s_barrier
ds_read_b64_tr_b16 a[48:49], v19 offset:0
ds_read_b64_tr_b16 a[50:51], v19 offset:768
ds_read_b64_tr_b16 a[52:53], v19 offset:12288
ds_read_b64_tr_b16 a[54:55], v19 offset:13056
ds_read_b64_tr_b16 a[128:129], v19 offset:32
ds_read_b64_tr_b16 a[130:131], v19 offset:800
ds_read_b64_tr_b16 a[132:133], v19 offset:12320
ds_read_b64_tr_b16 a[134:135], v19 offset:13088
ds_read_b64_tr_b16 a[208:209], v19 offset:64
ds_read_b64_tr_b16 a[210:211], v19 offset:832
)"
R"(ds_read_b64_tr_b16 a[212:213], v19 offset:12352
ds_read_b64_tr_b16 a[214:215], v19 offset:13120
s_waitcnt vmcnt(12) lgkmcnt(0)
s_barrier
ds_read_b64_tr_b16 a[56:57], v19 offset:24576
ds_read_b64_tr_b16 a[58:59], v19 offset:25344
ds_read_b64_tr_b16 a[60:61], v19 offset:36864
ds_read_b64_tr_b16 a[62:63], v19 offset:37632
ds_read_b64_tr_b16 a[136:137], v19 offset:24608
ds_read_b64_tr_b16 a[138:139], v19 offset:25376
ds_read_b64_tr_b16 a[140:141], v19 offset:36896
ds_read_b64_tr_b16 a[142:143], v19 offset:37664
ds_read_b64_tr_b16 a[216:217], v19 offset:24640
ds_read_b64_tr_b16 a[218:219], v19 offset:25408
ds_read_b64_tr_b16 a[220:221], v19 offset:36928
ds_read_b64_tr_b16 a[222:223], v19 offset:37696
s_waitcnt vmcnt(6) lgkmcnt(0)
s_barrier
ds_read_b64_tr_b16 a[64:65], v20 offset:0
ds_read_b64_tr_b16 a[66:67], v20 offset:768
ds_read_b64_tr_b16 a[68:69], v20 offset:12288
ds_read_b64_tr_b16 a[70:71], v20 offset:13056
ds_read_b64_tr_b16 a[144:145], v20 offset:32
ds_read_b64_tr_b16 a[146:147], v20 offset:800
ds_read_b64_tr_b16 a[148:149], v20 offset:12320
ds_read_b64_tr_b16 a[150:151], v20 offset:13088
ds_read_b64_tr_b16 a[224:225], v20 offset:64
ds_read_b64_tr_b16 a[226:227], v20 offset:832
ds_read_b64_tr_b16 a[228:229], v20 offset:12352
ds_read_b64_tr_b16 a[230:231], v20 offset:13120
s_waitcnt vmcnt(0) lgkmcnt(0)
s_barrier
ds_read_b64_tr_b16 a[72:73], v20 offset:24576
ds_read_b64_tr_b16 a[74:75], v20 offset:25344
ds_read_b64_tr_b16 a[76:77], v20 offset:36864
ds_read_b64_tr_b16 a[78:79], v20 offset:37632
ds_read_b64_tr_b16 a[152:153], v20 offset:24608
ds_read_b64_tr_b16 a[154:155], v20 offset:25376
ds_read_b64_tr_b16 a[156:157], v20 offset:36896
ds_read_b64_tr_b16 a[158:159], v20 offset:37664
ds_read_b64_tr_b16 a[232:233], v20 offset:24640
ds_read_b64_tr_b16 a[234:235], v20 offset:25408
ds_read_b64_tr_b16 a[236:237], v20 offset:36928
ds_read_b64_tr_b16 a[238:239], v20 offset:37696
s_waitcnt lgkmcnt(0)
s_barrier
hp_layer_%=:
s_lshl_b32 s47, s21, 6
s_cmp_eq_u32 s46, 0
s_cbranch_scc1 hp_roleq_%=
s_cmp_eq_u32 s46, 3
s_cbranch_scc1 hp_roleo_%=
s_cmp_eq_u32 s46, 4
s_cbranch_scc1 hp_rolet_%=
s_mul_i32 s62, s45, 10240
s_mul_i32 s50, s47, 40960
s_add_u32 s50, s50, s62
s_add_u32 s50, s50, 2539520
s_mov_b32 s61, s62
s_lshl_b32 s48, s47, 7
s_add_u32 s48, s48, 43520
v_mov_b32 v225, s48
s_mov_b32 s48, 2
v_and_b32 v224, 1, v15
v_lshl_add_u32 v226, v224, 7, v225
v_mov_b32 v224, 0
v_cmp_gt_u32 vcc, s48, v224
s_cbranch_vccz hpk_xfp_ok_%=
s_mov_b32 s60, 0
s_nop 2
hpk_xfp_poll_%=:
buffer_load_dword v224, v226, s[40:43], s58 offen sc1
s_waitcnt vmcnt(0)
v_cmp_gt_u32 vcc, s48, v224
s_nop 4
s_cbranch_vccz hpk_xfp_ok_%=
s_sleep 1
s_add_u32 s60, s60, 1
s_cmp_gt_u32 s60, 65536
s_cbranch_scc1 hp_abort_%=
s_and_b32 s59, s60, 63
s_cbranch_scc1 hpk_xfp_poll_%=
v_mov_b32 v4, 1024
buffer_load_dword v4, v4, s[40:43], s58 offen sc1
s_waitcnt vmcnt(0)
v_readfirstlane_b32 s59, v4
s_nop 0
s_cmp_lg_u32 s59, 0
s_nop 2
s_cbranch_scc1 hp_end_%=
s_branch hpk_xfp_poll_%=
hpk_xfp_ok_%=:
v_add_u32 v225, 256, v225
s_add_u32 m0, s61, 0
s_add_u32 s51, s50, 0
buffer_load_dwordx4 v0, s[40:43], s51 offen offset:0 sc1 lds
buffer_load_dwordx4 v0, s[40:43], s51 offen offset:1024 sc1 lds
buffer_load_dwordx4 v0, s[40:43], s51 offen offset:2048 sc1 lds
buffer_load_dwordx4 v0, s[40:43], s51 offen offset:3072 sc1 lds
s_add_u32 m0, s61, 4096
s_add_u32 s51, s50, 4096
buffer_load_dwordx4 v0, s[40:43], s51 offen offset:0 sc1 lds
buffer_load_dwordx4 v0, s[40:43], s51 offen offset:1024 sc1 lds
buffer_load_dwordx4 v0, s[40:43], s51 offen offset:2048 sc1 lds
buffer_load_dwordx4 v0, s[40:43], s51 offen offset:3072 sc1 lds
s_add_u32 m0, s61, 8192
s_add_u32 s51, s50, 8192
buffer_load_dwordx4 v0, s[40:43], s51 offen offset:0 sc1 lds
buffer_load_dwordx4 v0, s[40:43], s51 offen offset:1024 sc1 lds
s_add_u32 s50, s50, 40960
s_add_u32 s61, s61, 40960
s_cmp_ge_u32 s61, 122880
s_cselect_b32 s51, 122880, 0
s_sub_u32 s61, s61, s51
s_add_u32 m0, s61, 0
s_add_u32 s51, s50, 0
buffer_load_dwordx4 v0, s[40:43], s51 offen offset:0 sc1 lds
buffer_load_dwordx4 v0, s[40:43], s51 offen offset:1024 sc1 lds
buffer_load_dwordx4 v0, s[40:43], s51 offen offset:2048 sc1 lds
buffer_load_dwordx4 v0, s[40:43], s51 offen offset:3072 sc1 lds
s_add_u32 m0, s61, 4096
s_add_u32 s51, s50, 4096
buffer_load_dwordx4 v0, s[40:43], s51 offen offset:0 sc1 lds
buffer_load_dwordx4 v0, s[40:43], s51 offen offset:1024 sc1 lds
buffer_load_dwordx4 v0, s[40:43], s51 offen offset:2048 sc1 lds
buffer_load_dwordx4 v0, s[40:43], s51 offen offset:3072 sc1 lds
s_add_u32 m0, s61, 8192
s_add_u32 s51, s50, 8192
buffer_load_dwordx4 v0, s[40:43], s51 offen offset:0 sc1 lds
buffer_load_dwordx4 v0, s[40:43], s51 offen offset:1024 sc1 lds
s_add_u32 s50, s50, 40960
s_add_u32 s61, s61, 40960
s_cmp_ge_u32 s61, 122880
s_cselect_b32 s51, 122880, 0
s_sub_u32 s61, s61, s51
v_mov_b32 v224, 0
v_mov_b32 v226, 1
v_mov_b32 v1, v0
s_mov_b32 s64, 0
s_lshl_b32 s48, s44, 7
s_cmp_eq_u32 s45, 0
s_cselect_b32 s59, 1, 0
v_lshlrev_b32 v3, 7, v15
v_cmp_gt_u32 vcc, s59, v15
v_mov_b32 v4, 2147483632
s_nop 0
v_cndmask_b32 v3, v4, v3, vcc
s_lshr_b32 s49, s47, 1
s_lshl_b32 s49, s49, 7
s_add_u32 s49, s49, 254976
v_mov_b32 v10, s49
v_mov_b32 v9, 0
s_add_u32 s53, s48, 2099200
s_lshl_b32 s54, s47, 7
s_add_u32 s54, s54, 2107392
s_mov_b32 s63, 20365312
s_add_u32 s55, s48, 2375936
s_cmp_eq_u32 s46, 1
s_cselect_b32 s55, s55, 0x40000000
s_cselect_b32 s53, s54, s53
s_cselect_b32 s54, 128, 0
s_cselect_b32 s63, 7782400, s63
s_mul_i32 s49, s44, 12288
s_add_u32 s63, s63, s49
s_mul_i32 s49, s45, 3072
s_add_u32 s63, s63, s49
s_mov_b32 s52, 0x40000000
s_waitcnt vmcnt(10)
s_waitcnt lgkmcnt(0)
s_barrier
s_mov_b32 s65, 0x40000000
s_mov_b32 s56, 0x40000000
ds_read_b128 v[80:83], v1 offset:0
ds_read_b128 v[84:87], v1 offset:20480
ds_read_b128 v[88:91], v1 offset:1024
ds_read_b128 v[92:95], v1 offset:21504
ds_read_b128 v[96:99], v1 offset:2048
hpk_loop_%=:
s_bitcmp1_b32 s47, 5
s_cselect_b32 s48, 8, 0
v_cmp_gt_u32 vcc, s48, v9
s_cbranch_vccz hpk_free_ok_%=
s_mov_b32 s60, 0
s_nop 2
hpk_free_poll_%=:
buffer_load_dword v9, v10, s[40:43], s58 offen sc1
s_waitcnt vmcnt(0)
v_cmp_gt_u32 vcc, s48, v9
s_nop 4
s_cbranch_vccz hpk_free_ok_%=
s_sleep 8
s_add_u32 s60, s60, 1
s_cmp_gt_u32 s60, 65536
s_cbranch_scc1 hp_abort_%=
s_and_b32 s59, s60, 63
s_cbranch_scc1 hpk_free_poll_%=
v_mov_b32 v4, 1024
buffer_load_dword v4, v4, s[40:43], s58 offen sc1
s_waitcnt vmcnt(0)
v_readfirstlane_b32 s59, v4
)"
R"(s_nop 0
s_cmp_lg_u32 s59, 0
s_nop 2
s_cbranch_scc1 hp_end_%=
s_branch hpk_free_poll_%=
hpk_free_ok_%=:
v_add_u32 v10, 128, v10
s_waitcnt lgkmcnt(4)
v_mfma_f32_16x16x32_bf16 v[16:19], a[0:3], v[80:83], 0
v_mfma_f32_16x16x32_bf16 v[24:27], a[80:83], v[80:83], 0
v_mfma_f32_16x16x32_bf16 v[32:35], a[160:163], v[80:83], 0
ds_read_b128 v[80:83], v1 offset:22528
s_waitcnt lgkmcnt(4)
v_mfma_f32_16x16x32_bf16 v[20:23], a[0:3], v[84:87], 0
v_mfma_f32_16x16x32_bf16 v[28:31], a[80:83], v[84:87], 0
v_mfma_f32_16x16x32_bf16 v[36:39], a[160:163], v[84:87], 0
ds_read_b128 v[84:87], v1 offset:3072
s_waitcnt lgkmcnt(4)
v_mfma_f32_16x16x32_bf16 v[16:19], a[4:7], v[88:91], v[16:19]
v_mfma_f32_16x16x32_bf16 v[24:27], a[84:87], v[88:91], v[24:27]
v_mfma_f32_16x16x32_bf16 v[32:35], a[164:167], v[88:91], v[32:35]
ds_read_b128 v[88:91], v1 offset:23552
s_waitcnt lgkmcnt(4)
v_mfma_f32_16x16x32_bf16 v[20:23], a[4:7], v[92:95], v[20:23]
v_cvt_pk_bf16_f32 v104, v40, v41
v_cvt_pk_bf16_f32 v105, v42, v43
v_mfma_f32_16x16x32_bf16 v[28:31], a[84:87], v[92:95], v[28:31]
v_cvt_pk_bf16_f32 v106, v48, v49
v_cvt_pk_bf16_f32 v107, v50, v51
v_mfma_f32_16x16x32_bf16 v[36:39], a[164:167], v[92:95], v[36:39]
ds_read_b128 v[92:95], v1 offset:4096
s_waitcnt lgkmcnt(4)
v_mfma_f32_16x16x32_bf16 v[16:19], a[8:11], v[96:99], v[16:19]
buffer_store_dwordx4 v[104:107], v0, s[40:43], s65 offen offset:0 sc1
v_mfma_f32_16x16x32_bf16 v[24:27], a[88:91], v[96:99], v[24:27]
v_cvt_pk_bf16_f32 v108, v44, v45
v_cvt_pk_bf16_f32 v109, v46, v47
v_mfma_f32_16x16x32_bf16 v[32:35], a[168:171], v[96:99], v[32:35]
v_cvt_pk_bf16_f32 v110, v52, v53
ds_read_b128 v[96:99], v1 offset:24576
s_waitcnt lgkmcnt(4)
v_mfma_f32_16x16x32_bf16 v[20:23], a[8:11], v[80:83], v[20:23]
v_cvt_pk_bf16_f32 v111, v54, v55
v_mfma_f32_16x16x32_bf16 v[28:31], a[88:91], v[80:83], v[28:31]
buffer_store_dwordx4 v[108:111], v0, s[40:43], s65 offen offset:1024 sc1
v_mfma_f32_16x16x32_bf16 v[36:39], a[168:171], v[80:83], v[36:39]
v_cvt_pk_bf16_f32 v112, v56, v57
ds_read_b128 v[80:83], v1 offset:5120
s_waitcnt lgkmcnt(4)
v_mfma_f32_16x16x32_bf16 v[16:19], a[12:15], v[84:87], v[16:19]
v_cvt_pk_bf16_f32 v113, v58, v59
v_cvt_pk_bf16_f32 v114, v60, v61
v_mfma_f32_16x16x32_bf16 v[24:27], a[92:95], v[84:87], v[24:27]
v_cvt_pk_bf16_f32 v115, v62, v63
v_mfma_f32_16x16x32_bf16 v[32:35], a[172:175], v[84:87], v[32:35]
ds_read_b128 v[84:87], v1 offset:25600
s_waitcnt lgkmcnt(4)
v_mfma_f32_16x16x32_bf16 v[20:23], a[12:15], v[88:91], v[20:23]
buffer_store_dwordx4 v[112:115], v0, s[40:43], s65 offen offset:2048 sc1
v_mfma_f32_16x16x32_bf16 v[28:31], a[92:95], v[88:91], v[28:31]
v_mfma_f32_16x16x32_bf16 v[36:39], a[172:175], v[88:91], v[36:39]
ds_read_b128 v[88:91], v1 offset:6144
s_waitcnt lgkmcnt(4)
v_mfma_f32_16x16x32_bf16 v[16:19], a[16:19], v[92:95], v[16:19]
buffer_load_dword v224, v225, s[40:43], s58 offen sc1
v_mfma_f32_16x16x32_bf16 v[24:27], a[96:99], v[92:95], v[24:27]
v_mfma_f32_16x16x32_bf16 v[32:35], a[176:179], v[92:95], v[32:35]
ds_read_b128 v[92:95], v1 offset:26624
s_waitcnt lgkmcnt(4)
v_mfma_f32_16x16x32_bf16 v[20:23], a[16:19], v[96:99], v[20:23]
v_mfma_f32_16x16x32_bf16 v[28:31], a[96:99], v[96:99], v[28:31]
v_mfma_f32_16x16x32_bf16 v[36:39], a[176:179], v[96:99], v[36:39]
ds_read_b128 v[96:99], v1 offset:7168
s_waitcnt lgkmcnt(4)
v_mfma_f32_16x16x32_bf16 v[16:19], a[20:23], v[80:83], v[16:19]
v_mfma_f32_16x16x32_bf16 v[24:27], a[100:103], v[80:83], v[24:27]
v_mfma_f32_16x16x32_bf16 v[32:35], a[180:183], v[80:83], v[32:35]
ds_read_b128 v[80:83], v1 offset:27648
s_waitcnt lgkmcnt(4)
v_mfma_f32_16x16x32_bf16 v[20:23], a[20:23], v[84:87], v[20:23]
v_mfma_f32_16x16x32_bf16 v[28:31], a[100:103], v[84:87], v[28:31]
v_mfma_f32_16x16x32_bf16 v[36:39], a[180:183], v[84:87], v[36:39]
ds_read_b128 v[84:87], v1 offset:8192
s_waitcnt lgkmcnt(4)
v_mfma_f32_16x16x32_bf16 v[16:19], a[24:27], v[88:91], v[16:19]
v_mfma_f32_16x16x32_bf16 v[24:27], a[104:107], v[88:91], v[24:27]
v_mfma_f32_16x16x32_bf16 v[32:35], a[184:187], v[88:91], v[32:35]
ds_read_b128 v[88:91], v1 offset:28672
s_waitcnt lgkmcnt(4)
v_mfma_f32_16x16x32_bf16 v[20:23], a[24:27], v[92:95], v[20:23]
v_mfma_f32_16x16x32_bf16 v[28:31], a[104:107], v[92:95], v[28:31]
v_mfma_f32_16x16x32_bf16 v[36:39], a[184:187], v[92:95], v[36:39]
ds_read_b128 v[92:95], v1 offset:9216
s_waitcnt lgkmcnt(4)
v_mfma_f32_16x16x32_bf16 v[16:19], a[28:31], v[96:99], v[16:19]
v_mfma_f32_16x16x32_bf16 v[24:27], a[108:111], v[96:99], v[24:27]
v_mfma_f32_16x16x32_bf16 v[32:35], a[188:191], v[96:99], v[32:35]
ds_read_b128 v[96:99], v1 offset:29696
s_waitcnt lgkmcnt(4)
v_mfma_f32_16x16x32_bf16 v[20:23], a[28:31], v[80:83], v[20:23]
v_mfma_f32_16x16x32_bf16 v[28:31], a[108:111], v[80:83], v[28:31]
v_mfma_f32_16x16x32_bf16 v[36:39], a[188:191], v[80:83], v[36:39]
ds_read_b128 v[80:83], v1 offset:10240
s_waitcnt lgkmcnt(4)
v_mfma_f32_16x16x32_bf16 v[16:19], a[32:35], v[84:87], v[16:19]
v_mfma_f32_16x16x32_bf16 v[24:27], a[112:115], v[84:87], v[24:27]
v_mfma_f32_16x16x32_bf16 v[32:35], a[192:195], v[84:87], v[32:35]
ds_read_b128 v[84:87], v1 offset:30720
s_waitcnt lgkmcnt(4)
v_mfma_f32_16x16x32_bf16 v[20:23], a[32:35], v[88:91], v[20:23]
v_mfma_f32_16x16x32_bf16 v[28:31], a[112:115], v[88:91], v[28:31]
v_mfma_f32_16x16x32_bf16 v[36:39], a[192:195], v[88:91], v[36:39]
ds_read_b128 v[88:91], v1 offset:11264
s_waitcnt lgkmcnt(4)
v_mfma_f32_16x16x32_bf16 v[16:19], a[36:39], v[92:95], v[16:19]
v_mfma_f32_16x16x32_bf16 v[24:27], a[116:119], v[92:95], v[24:27]
v_mfma_f32_16x16x32_bf16 v[32:35], a[196:199], v[92:95], v[32:35]
ds_read_b128 v[92:95], v1 offset:31744
s_waitcnt lgkmcnt(4)
v_mfma_f32_16x16x32_bf16 v[20:23], a[36:39], v[96:99], v[20:23]
v_mfma_f32_16x16x32_bf16 v[28:31], a[116:119], v[96:99], v[28:31]
v_mfma_f32_16x16x32_bf16 v[36:39], a[196:199], v[96:99], v[36:39]
ds_read_b128 v[96:99], v1 offset:12288
s_waitcnt lgkmcnt(4)
v_mfma_f32_16x16x32_bf16 v[16:19], a[40:43], v[80:83], v[16:19]
v_mfma_f32_16x16x32_bf16 v[24:27], a[120:123], v[80:83], v[24:27]
v_mfma_f32_16x16x32_bf16 v[32:35], a[200:203], v[80:83], v[32:35]
ds_read_b128 v[80:83], v1 offset:32768
s_waitcnt lgkmcnt(4)
v_mfma_f32_16x16x32_bf16 v[20:23], a[40:43], v[84:87], v[20:23]
v_mfma_f32_16x16x32_bf16 v[28:31], a[120:123], v[84:87], v[28:31]
v_mfma_f32_16x16x32_bf16 v[36:39], a[200:203], v[84:87], v[36:39]
ds_read_b128 v[84:87], v1 offset:13312
s_waitcnt lgkmcnt(4)
v_mfma_f32_16x16x32_bf16 v[16:19], a[44:47], v[88:91], v[16:19]
v_mfma_f32_16x16x32_bf16 v[24:27], a[124:127], v[88:91], v[24:27]
v_mfma_f32_16x16x32_bf16 v[32:35], a[204:207], v[88:91], v[32:35]
ds_read_b128 v[88:91], v1 offset:33792
s_waitcnt lgkmcnt(4)
v_mfma_f32_16x16x32_bf16 v[20:23], a[44:47], v[92:95], v[20:23]
v_mfma_f32_16x16x32_bf16 v[28:31], a[124:127], v[92:95], v[28:31]
v_mfma_f32_16x16x32_bf16 v[36:39], a[204:207], v[92:95], v[36:39]
ds_read_b128 v[92:95], v1 offset:14336
s_waitcnt lgkmcnt(4)
v_mfma_f32_16x16x32_bf16 v[16:19], a[48:51], v[96:99], v[16:19]
v_mfma_f32_16x16x32_bf16 v[24:27], a[128:131], v[96:99], v[24:27]
v_mfma_f32_16x16x32_bf16 v[32:35], a[208:211], v[96:99], v[32:35]
ds_read_b128 v[96:99], v1 offset:34816
s_waitcnt lgkmcnt(4)
v_mfma_f32_16x16x32_bf16 v[20:23], a[48:51], v[80:83], v[20:23]
v_mfma_f32_16x16x32_bf16 v[28:31], a[128:131], v[80:83], v[28:31]
v_mfma_f32_16x16x32_bf16 v[36:39], a[208:211], v[80:83], v[36:39]
ds_read_b128 v[80:83], v1 offset:15360
s_waitcnt lgkmcnt(4)
v_mfma_f32_16x16x32_bf16 v[16:19], a[52:55], v[84:87], v[16:19]
v_mfma_f32_16x16x32_bf16 v[24:27], a[132:135], v[84:87], v[24:27]
v_mfma_f32_16x16x32_bf16 v[32:35], a[212:215], v[84:87], v[32:35]
ds_read_b128 v[84:87], v1 offset:35840
s_waitcnt lgkmcnt(4)
v_mfma_f32_16x16x32_bf16 v[20:23], a[52:55], v[88:91], v[20:23]
v_mfma_f32_16x16x32_bf16 v[28:31], a[132:135], v[88:91], v[28:31]
v_mfma_f32_16x16x32_bf16 v[36:39], a[212:215], v[88:91], v[36:39]
ds_read_b128 v[88:91], v1 offset:16384
s_waitcnt lgkmcnt(4)
v_mfma_f32_16x16x32_bf16 v[16:19], a[56:59], v[92:95], v[16:19]
v_mfma_f32_16x16x32_bf16 v[24:27], a[136:139], v[92:95], v[24:27]
v_mfma_f32_16x16x32_bf16 v[32:35], a[216:219], v[92:95], v[32:35]
ds_read_b128 v[92:95], v1 offset:36864
s_waitcnt lgkmcnt(4)
v_mfma_f32_16x16x32_bf16 v[20:23], a[56:59], v[96:99], v[20:23]
v_mfma_f32_16x16x32_bf16 v[28:31], a[136:139], v[96:99], v[28:31]
v_mfma_f32_16x16x32_bf16 v[36:39], a[216:219], v[96:99], v[36:39]
s_waitcnt vmcnt(0)
s_barrier
buffer_atomic_add v226, v3, s[40:43], s56 offen
buffer_atomic_add v226, v3, s[40:43], s52 offen
s_and_b32 s48, s47, 63
s_cmp_lt_u32 s48, 62
s_cselect_b32 s48, 2, 0
v_cmp_gt_u32 vcc, s48, v224
s_cbranch_vccz hpk_xf0_ok_%=
s_mov_b32 s60, 0
s_nop 2
hpk_xf0_poll_%=:
buffer_load_dword v224, v225, s[40:43], s58 offen sc1
s_waitcnt vmcnt(0)
v_cmp_gt_u32 vcc, s48, v224
s_nop 4
s_cbranch_vccz hpk_xf0_ok_%=
s_sleep 2
s_add_u32 s60, s60, 1
s_cmp_gt_u32 s60, 65536
s_cbranch_scc1 hp_abort_%=
s_and_b32 s59, s60, 63
s_cbranch_scc1 hpk_xf0_poll_%=
v_mov_b32 v4, 1024
buffer_load_dword v4, v4, s[40:43], s58 offen sc1
s_waitcnt vmcnt(0)
v_readfirstlane_b32 s59, v4
s_nop 0
)"
R"(s_cmp_lg_u32 s59, 0
s_nop 2
s_cbranch_scc1 hp_end_%=
s_branch hpk_xf0_poll_%=
hpk_xf0_ok_%=:
v_add_u32 v225, 128, v225
ds_read_b128 v[96:99], v1 offset:17408
s_waitcnt lgkmcnt(4)
v_mfma_f32_16x16x32_bf16 v[16:19], a[60:63], v[80:83], v[16:19]
s_add_u32 m0, s61, 0
s_add_u32 s51, s50, 0
buffer_load_dwordx4 v0, s[40:43], s51 offen offset:0 sc1 lds
v_mfma_f32_16x16x32_bf16 v[24:27], a[140:143], v[80:83], v[24:27]
v_mfma_f32_16x16x32_bf16 v[32:35], a[220:223], v[80:83], v[32:35]
ds_read_b128 v[80:83], v1 offset:37888
s_waitcnt lgkmcnt(4)
v_mfma_f32_16x16x32_bf16 v[20:23], a[60:63], v[84:87], v[20:23]
buffer_load_dwordx4 v0, s[40:43], s51 offen offset:1024 sc1 lds
v_mfma_f32_16x16x32_bf16 v[28:31], a[140:143], v[84:87], v[28:31]
v_mfma_f32_16x16x32_bf16 v[36:39], a[220:223], v[84:87], v[36:39]
ds_read_b128 v[84:87], v1 offset:18432
s_waitcnt lgkmcnt(4)
v_mfma_f32_16x16x32_bf16 v[16:19], a[64:67], v[88:91], v[16:19]
buffer_load_dwordx4 v0, s[40:43], s51 offen offset:2048 sc1 lds
v_mfma_f32_16x16x32_bf16 v[24:27], a[144:147], v[88:91], v[24:27]
v_mfma_f32_16x16x32_bf16 v[32:35], a[224:227], v[88:91], v[32:35]
ds_read_b128 v[88:91], v1 offset:38912
s_waitcnt lgkmcnt(4)
v_mfma_f32_16x16x32_bf16 v[20:23], a[64:67], v[92:95], v[20:23]
buffer_load_dwordx4 v0, s[40:43], s51 offen offset:3072 sc1 lds
v_mfma_f32_16x16x32_bf16 v[28:31], a[144:147], v[92:95], v[28:31]
v_mfma_f32_16x16x32_bf16 v[36:39], a[224:227], v[92:95], v[36:39]
ds_read_b128 v[92:95], v1 offset:19456
s_waitcnt lgkmcnt(4)
v_mfma_f32_16x16x32_bf16 v[16:19], a[68:71], v[96:99], v[16:19]
s_add_u32 m0, s61, 4096
s_add_u32 s51, s50, 4096
buffer_load_dwordx4 v0, s[40:43], s51 offen offset:0 sc1 lds
v_mfma_f32_16x16x32_bf16 v[24:27], a[148:151], v[96:99], v[24:27]
v_mfma_f32_16x16x32_bf16 v[32:35], a[228:231], v[96:99], v[32:35]
ds_read_b128 v[96:99], v1 offset:39936
s_waitcnt lgkmcnt(4)
v_mfma_f32_16x16x32_bf16 v[20:23], a[68:71], v[80:83], v[20:23]
buffer_load_dwordx4 v0, s[40:43], s51 offen offset:1024 sc1 lds
v_mfma_f32_16x16x32_bf16 v[28:31], a[148:151], v[80:83], v[28:31]
v_mfma_f32_16x16x32_bf16 v[36:39], a[228:231], v[80:83], v[36:39]
s_add_u32 s64, s64, 40960
s_cmp_ge_u32 s64, 122880
s_cselect_b32 s64, 0, s64
v_add_u32 v1, s64, v0
ds_read_b128 v[80:83], v1 offset:0
s_waitcnt lgkmcnt(4)
v_mfma_f32_16x16x32_bf16 v[16:19], a[72:75], v[84:87], v[16:19]
buffer_load_dwordx4 v0, s[40:43], s51 offen offset:2048 sc1 lds
v_mfma_f32_16x16x32_bf16 v[24:27], a[152:155], v[84:87], v[24:27]
v_mfma_f32_16x16x32_bf16 v[32:35], a[232:235], v[84:87], v[32:35]
ds_read_b128 v[84:87], v1 offset:20480
s_waitcnt lgkmcnt(4)
v_mfma_f32_16x16x32_bf16 v[20:23], a[72:75], v[88:91], v[20:23]
buffer_load_dwordx4 v0, s[40:43], s51 offen offset:3072 sc1 lds
v_mfma_f32_16x16x32_bf16 v[28:31], a[152:155], v[88:91], v[28:31]
v_mfma_f32_16x16x32_bf16 v[36:39], a[232:235], v[88:91], v[36:39]
ds_read_b128 v[88:91], v1 offset:1024
s_waitcnt lgkmcnt(4)
v_mfma_f32_16x16x32_bf16 v[16:19], a[76:79], v[92:95], v[16:19]
s_add_u32 m0, s61, 8192
s_add_u32 s51, s50, 8192
buffer_load_dwordx4 v0, s[40:43], s51 offen offset:0 sc1 lds
v_mfma_f32_16x16x32_bf16 v[24:27], a[156:159], v[92:95], v[24:27]
v_mfma_f32_16x16x32_bf16 v[32:35], a[236:239], v[92:95], v[32:35]
ds_read_b128 v[92:95], v1 offset:21504
s_waitcnt lgkmcnt(4)
v_mfma_f32_16x16x32_bf16 v[20:23], a[76:79], v[96:99], v[20:23]
buffer_load_dwordx4 v0, s[40:43], s51 offen offset:1024 sc1 lds
v_mfma_f32_16x16x32_bf16 v[28:31], a[156:159], v[96:99], v[28:31]
s_add_u32 s50, s50, 40960
s_add_u32 s61, s61, 40960
s_cmp_ge_u32 s61, 122880
s_cselect_b32 s51, 122880, 0
s_sub_u32 s61, s61, s51
v_mfma_f32_16x16x32_bf16 v[36:39], a[236:239], v[96:99], v[36:39]
ds_read_b128 v[96:99], v1 offset:2048
s_mov_b32 s65, s63
s_add_u32 s59, s47, 0
s_and_b32 s59, s59, 63
s_cmp_eq_u32 s59, 31
s_cselect_b32 s59, -31, 1
s_mul_i32 s57, s59, 393216
s_add_u32 s63, s63, s57
s_mov_b32 s52, s53
s_add_u32 s53, s53, s54
s_and_b32 s59, s47, 62
s_cselect_b32 s56, 0x40000000, s55
s_add_u32 s47, s47, 1
s_waitcnt lgkmcnt(4)
v_mfma_f32_16x16x32_bf16 v[40:43], a[0:3], v[80:83], 0
v_mfma_f32_16x16x32_bf16 v[48:51], a[80:83], v[80:83], 0
v_mfma_f32_16x16x32_bf16 v[56:59], a[160:163], v[80:83], 0
ds_read_b128 v[80:83], v1 offset:22528
s_waitcnt lgkmcnt(4)
v_mfma_f32_16x16x32_bf16 v[44:47], a[0:3], v[84:87], 0
v_mfma_f32_16x16x32_bf16 v[52:55], a[80:83], v[84:87], 0
v_mfma_f32_16x16x32_bf16 v[60:63], a[160:163], v[84:87], 0
ds_read_b128 v[84:87], v1 offset:3072
s_waitcnt lgkmcnt(4)
v_mfma_f32_16x16x32_bf16 v[40:43], a[4:7], v[88:91], v[40:43]
v_mfma_f32_16x16x32_bf16 v[48:51], a[84:87], v[88:91], v[48:51]
v_mfma_f32_16x16x32_bf16 v[56:59], a[164:167], v[88:91], v[56:59]
ds_read_b128 v[88:91], v1 offset:23552
s_waitcnt lgkmcnt(4)
v_mfma_f32_16x16x32_bf16 v[44:47], a[4:7], v[92:95], v[44:47]
v_cvt_pk_bf16_f32 v104, v16, v17
v_cvt_pk_bf16_f32 v105, v18, v19
v_mfma_f32_16x16x32_bf16 v[52:55], a[84:87], v[92:95], v[52:55]
v_cvt_pk_bf16_f32 v106, v24, v25
v_cvt_pk_bf16_f32 v107, v26, v27
v_mfma_f32_16x16x32_bf16 v[60:63], a[164:167], v[92:95], v[60:63]
ds_read_b128 v[92:95], v1 offset:4096
s_waitcnt lgkmcnt(4)
v_mfma_f32_16x16x32_bf16 v[40:43], a[8:11], v[96:99], v[40:43]
buffer_store_dwordx4 v[104:107], v0, s[40:43], s65 offen offset:0 sc1
v_mfma_f32_16x16x32_bf16 v[48:51], a[88:91], v[96:99], v[48:51]
v_cvt_pk_bf16_f32 v108, v20, v21
v_cvt_pk_bf16_f32 v109, v22, v23
v_mfma_f32_16x16x32_bf16 v[56:59], a[168:171], v[96:99], v[56:59]
v_cvt_pk_bf16_f32 v110, v28, v29
ds_read_b128 v[96:99], v1 offset:24576
s_waitcnt lgkmcnt(4)
v_mfma_f32_16x16x32_bf16 v[44:47], a[8:11], v[80:83], v[44:47]
v_cvt_pk_bf16_f32 v111, v30, v31
v_mfma_f32_16x16x32_bf16 v[52:55], a[88:91], v[80:83], v[52:55]
buffer_store_dwordx4 v[108:111], v0, s[40:43], s65 offen offset:1024 sc1
v_mfma_f32_16x16x32_bf16 v[60:63], a[168:171], v[80:83], v[60:63]
v_cvt_pk_bf16_f32 v112, v32, v33
ds_read_b128 v[80:83], v1 offset:5120
s_waitcnt lgkmcnt(4)
v_mfma_f32_16x16x32_bf16 v[40:43], a[12:15], v[84:87], v[40:43]
v_cvt_pk_bf16_f32 v113, v34, v35
v_cvt_pk_bf16_f32 v114, v36, v37
v_mfma_f32_16x16x32_bf16 v[48:51], a[92:95], v[84:87], v[48:51]
v_cvt_pk_bf16_f32 v115, v38, v39
v_mfma_f32_16x16x32_bf16 v[56:59], a[172:175], v[84:87], v[56:59]
ds_read_b128 v[84:87], v1 offset:25600
s_waitcnt lgkmcnt(4)
v_mfma_f32_16x16x32_bf16 v[44:47], a[12:15], v[88:91], v[44:47]
buffer_store_dwordx4 v[112:115], v0, s[40:43], s65 offen offset:2048 sc1
v_mfma_f32_16x16x32_bf16 v[52:55], a[92:95], v[88:91], v[52:55]
v_mfma_f32_16x16x32_bf16 v[60:63], a[172:175], v[88:91], v[60:63]
ds_read_b128 v[88:91], v1 offset:6144
s_waitcnt lgkmcnt(4)
v_mfma_f32_16x16x32_bf16 v[40:43], a[16:19], v[92:95], v[40:43]
buffer_load_dword v224, v225, s[40:43], s58 offen sc1
v_mfma_f32_16x16x32_bf16 v[48:51], a[96:99], v[92:95], v[48:51]
v_mfma_f32_16x16x32_bf16 v[56:59], a[176:179], v[92:95], v[56:59]
ds_read_b128 v[92:95], v1 offset:26624
s_waitcnt lgkmcnt(4)
v_mfma_f32_16x16x32_bf16 v[44:47], a[16:19], v[96:99], v[44:47]
buffer_load_dword v9, v10, s[40:43], s58 offen sc1
v_mfma_f32_16x16x32_bf16 v[52:55], a[96:99], v[96:99], v[52:55]
v_mfma_f32_16x16x32_bf16 v[60:63], a[176:179], v[96:99], v[60:63]
ds_read_b128 v[96:99], v1 offset:7168
s_waitcnt lgkmcnt(4)
v_mfma_f32_16x16x32_bf16 v[40:43], a[20:23], v[80:83], v[40:43]
v_mfma_f32_16x16x32_bf16 v[48:51], a[100:103], v[80:83], v[48:51]
v_mfma_f32_16x16x32_bf16 v[56:59], a[180:183], v[80:83], v[56:59]
ds_read_b128 v[80:83], v1 offset:27648
s_waitcnt lgkmcnt(4)
v_mfma_f32_16x16x32_bf16 v[44:47], a[20:23], v[84:87], v[44:47]
v_mfma_f32_16x16x32_bf16 v[52:55], a[100:103], v[84:87], v[52:55]
v_mfma_f32_16x16x32_bf16 v[60:63], a[180:183], v[84:87], v[60:63]
ds_read_b128 v[84:87], v1 offset:8192
s_waitcnt lgkmcnt(4)
v_mfma_f32_16x16x32_bf16 v[40:43], a[24:27], v[88:91], v[40:43]
v_mfma_f32_16x16x32_bf16 v[48:51], a[104:107], v[88:91], v[48:51]
v_mfma_f32_16x16x32_bf16 v[56:59], a[184:187], v[88:91], v[56:59]
ds_read_b128 v[88:91], v1 offset:28672
s_waitcnt lgkmcnt(4)
v_mfma_f32_16x16x32_bf16 v[44:47], a[24:27], v[92:95], v[44:47]
v_mfma_f32_16x16x32_bf16 v[52:55], a[104:107], v[92:95], v[52:55]
v_mfma_f32_16x16x32_bf16 v[60:63], a[184:187], v[92:95], v[60:63]
ds_read_b128 v[92:95], v1 offset:9216
s_waitcnt lgkmcnt(4)
v_mfma_f32_16x16x32_bf16 v[40:43], a[28:31], v[96:99], v[40:43]
v_mfma_f32_16x16x32_bf16 v[48:51], a[108:111], v[96:99], v[48:51]
v_mfma_f32_16x16x32_bf16 v[56:59], a[188:191], v[96:99], v[56:59]
ds_read_b128 v[96:99], v1 offset:29696
s_waitcnt lgkmcnt(4)
v_mfma_f32_16x16x32_bf16 v[44:47], a[28:31], v[80:83], v[44:47]
v_mfma_f32_16x16x32_bf16 v[52:55], a[108:111], v[80:83], v[52:55]
v_mfma_f32_16x16x32_bf16 v[60:63], a[188:191], v[80:83], v[60:63]
ds_read_b128 v[80:83], v1 offset:10240
s_waitcnt lgkmcnt(4)
v_mfma_f32_16x16x32_bf16 v[40:43], a[32:35], v[84:87], v[40:43]
v_mfma_f32_16x16x32_bf16 v[48:51], a[112:115], v[84:87], v[48:51]
v_mfma_f32_16x16x32_bf16 v[56:59], a[192:195], v[84:87], v[56:59]
ds_read_b128 v[84:87], v1 offset:30720
s_waitcnt lgkmcnt(4)
v_mfma_f32_16x16x32_bf16 v[44:47], a[32:35], v[88:91], v[44:47]
v_mfma_f32_16x16x32_bf16 v[52:55], a[112:115], v[88:91], v[52:55]
v_mfma_f32_16x16x32_bf16 v[60:63], a[192:195], v[88:91], v[60:63]
)"
R"(ds_read_b128 v[88:91], v1 offset:11264
s_waitcnt lgkmcnt(4)
v_mfma_f32_16x16x32_bf16 v[40:43], a[36:39], v[92:95], v[40:43]
v_mfma_f32_16x16x32_bf16 v[48:51], a[116:119], v[92:95], v[48:51]
v_mfma_f32_16x16x32_bf16 v[56:59], a[196:199], v[92:95], v[56:59]
ds_read_b128 v[92:95], v1 offset:31744
s_waitcnt lgkmcnt(4)
v_mfma_f32_16x16x32_bf16 v[44:47], a[36:39], v[96:99], v[44:47]
v_mfma_f32_16x16x32_bf16 v[52:55], a[116:119], v[96:99], v[52:55]
v_mfma_f32_16x16x32_bf16 v[60:63], a[196:199], v[96:99], v[60:63]
ds_read_b128 v[96:99], v1 offset:12288
s_waitcnt lgkmcnt(4)
v_mfma_f32_16x16x32_bf16 v[40:43], a[40:43], v[80:83], v[40:43]
v_mfma_f32_16x16x32_bf16 v[48:51], a[120:123], v[80:83], v[48:51]
v_mfma_f32_16x16x32_bf16 v[56:59], a[200:203], v[80:83], v[56:59]
ds_read_b128 v[80:83], v1 offset:32768
s_waitcnt lgkmcnt(4)
v_mfma_f32_16x16x32_bf16 v[44:47], a[40:43], v[84:87], v[44:47]
v_mfma_f32_16x16x32_bf16 v[52:55], a[120:123], v[84:87], v[52:55]
v_mfma_f32_16x16x32_bf16 v[60:63], a[200:203], v[84:87], v[60:63]
ds_read_b128 v[84:87], v1 offset:13312
s_waitcnt lgkmcnt(4)
v_mfma_f32_16x16x32_bf16 v[40:43], a[44:47], v[88:91], v[40:43]
v_mfma_f32_16x16x32_bf16 v[48:51], a[124:127], v[88:91], v[48:51]
v_mfma_f32_16x16x32_bf16 v[56:59], a[204:207], v[88:91], v[56:59]
ds_read_b128 v[88:91], v1 offset:33792
s_waitcnt lgkmcnt(4)
v_mfma_f32_16x16x32_bf16 v[44:47], a[44:47], v[92:95], v[44:47]
v_mfma_f32_16x16x32_bf16 v[52:55], a[124:127], v[92:95], v[52:55]
v_mfma_f32_16x16x32_bf16 v[60:63], a[204:207], v[92:95], v[60:63]
ds_read_b128 v[92:95], v1 offset:14336
s_waitcnt lgkmcnt(4)
v_mfma_f32_16x16x32_bf16 v[40:43], a[48:51], v[96:99], v[40:43]
v_mfma_f32_16x16x32_bf16 v[48:51], a[128:131], v[96:99], v[48:51]
v_mfma_f32_16x16x32_bf16 v[56:59], a[208:211], v[96:99], v[56:59]
ds_read_b128 v[96:99], v1 offset:34816
s_waitcnt lgkmcnt(4)
v_mfma_f32_16x16x32_bf16 v[44:47], a[48:51], v[80:83], v[44:47]
v_mfma_f32_16x16x32_bf16 v[52:55], a[128:131], v[80:83], v[52:55]
v_mfma_f32_16x16x32_bf16 v[60:63], a[208:211], v[80:83], v[60:63]
ds_read_b128 v[80:83], v1 offset:15360
s_waitcnt lgkmcnt(4)
v_mfma_f32_16x16x32_bf16 v[40:43], a[52:55], v[84:87], v[40:43]
v_mfma_f32_16x16x32_bf16 v[48:51], a[132:135], v[84:87], v[48:51]
v_mfma_f32_16x16x32_bf16 v[56:59], a[212:215], v[84:87], v[56:59]
ds_read_b128 v[84:87], v1 offset:35840
s_waitcnt lgkmcnt(4)
v_mfma_f32_16x16x32_bf16 v[44:47], a[52:55], v[88:91], v[44:47]
v_mfma_f32_16x16x32_bf16 v[52:55], a[132:135], v[88:91], v[52:55]
v_mfma_f32_16x16x32_bf16 v[60:63], a[212:215], v[88:91], v[60:63]
ds_read_b128 v[88:91], v1 offset:16384
s_waitcnt lgkmcnt(4)
v_mfma_f32_16x16x32_bf16 v[40:43], a[56:59], v[92:95], v[40:43]
v_mfma_f32_16x16x32_bf16 v[48:51], a[136:139], v[92:95], v[48:51]
v_mfma_f32_16x16x32_bf16 v[56:59], a[216:219], v[92:95], v[56:59]
ds_read_b128 v[92:95], v1 offset:36864
s_waitcnt lgkmcnt(4)
v_mfma_f32_16x16x32_bf16 v[44:47], a[56:59], v[96:99], v[44:47]
v_mfma_f32_16x16x32_bf16 v[52:55], a[136:139], v[96:99], v[52:55]
v_mfma_f32_16x16x32_bf16 v[60:63], a[216:219], v[96:99], v[60:63]
s_waitcnt vmcnt(0)
s_barrier
buffer_atomic_add v226, v3, s[40:43], s56 offen
buffer_atomic_add v226, v3, s[40:43], s52 offen
s_and_b32 s48, s47, 63
s_cmp_lt_u32 s48, 62
s_cselect_b32 s48, 2, 0
v_cmp_gt_u32 vcc, s48, v224
s_cbranch_vccz hpk_xf1_ok_%=
s_mov_b32 s60, 0
s_nop 2
hpk_xf1_poll_%=:
buffer_load_dword v224, v225, s[40:43], s58 offen sc1
s_waitcnt vmcnt(0)
v_cmp_gt_u32 vcc, s48, v224
s_nop 4
s_cbranch_vccz hpk_xf1_ok_%=
s_sleep 2
s_add_u32 s60, s60, 1
s_cmp_gt_u32 s60, 65536
s_cbranch_scc1 hp_abort_%=
s_and_b32 s59, s60, 63
s_cbranch_scc1 hpk_xf1_poll_%=
v_mov_b32 v4, 1024
buffer_load_dword v4, v4, s[40:43], s58 offen sc1
s_waitcnt vmcnt(0)
v_readfirstlane_b32 s59, v4
s_nop 0
s_cmp_lg_u32 s59, 0
s_nop 2
s_cbranch_scc1 hp_end_%=
s_branch hpk_xf1_poll_%=
hpk_xf1_ok_%=:
v_add_u32 v225, 128, v225
ds_read_b128 v[96:99], v1 offset:17408
s_waitcnt lgkmcnt(4)
v_mfma_f32_16x16x32_bf16 v[40:43], a[60:63], v[80:83], v[40:43]
s_add_u32 m0, s61, 0
s_add_u32 s51, s50, 0
buffer_load_dwordx4 v0, s[40:43], s51 offen offset:0 sc1 lds
v_mfma_f32_16x16x32_bf16 v[48:51], a[140:143], v[80:83], v[48:51]
v_mfma_f32_16x16x32_bf16 v[56:59], a[220:223], v[80:83], v[56:59]
ds_read_b128 v[80:83], v1 offset:37888
s_waitcnt lgkmcnt(4)
v_mfma_f32_16x16x32_bf16 v[44:47], a[60:63], v[84:87], v[44:47]
buffer_load_dwordx4 v0, s[40:43], s51 offen offset:1024 sc1 lds
v_mfma_f32_16x16x32_bf16 v[52:55], a[140:143], v[84:87], v[52:55]
v_mfma_f32_16x16x32_bf16 v[60:63], a[220:223], v[84:87], v[60:63]
ds_read_b128 v[84:87], v1 offset:18432
s_waitcnt lgkmcnt(4)
v_mfma_f32_16x16x32_bf16 v[40:43], a[64:67], v[88:91], v[40:43]
buffer_load_dwordx4 v0, s[40:43], s51 offen offset:2048 sc1 lds
v_mfma_f32_16x16x32_bf16 v[48:51], a[144:147], v[88:91], v[48:51]
v_mfma_f32_16x16x32_bf16 v[56:59], a[224:227], v[88:91], v[56:59]
ds_read_b128 v[88:91], v1 offset:38912
s_waitcnt lgkmcnt(4)
v_mfma_f32_16x16x32_bf16 v[44:47], a[64:67], v[92:95], v[44:47]
buffer_load_dwordx4 v0, s[40:43], s51 offen offset:3072 sc1 lds
v_mfma_f32_16x16x32_bf16 v[52:55], a[144:147], v[92:95], v[52:55]
v_mfma_f32_16x16x32_bf16 v[60:63], a[224:227], v[92:95], v[60:63]
ds_read_b128 v[92:95], v1 offset:19456
s_waitcnt lgkmcnt(4)
v_mfma_f32_16x16x32_bf16 v[40:43], a[68:71], v[96:99], v[40:43]
s_add_u32 m0, s61, 4096
s_add_u32 s51, s50, 4096
buffer_load_dwordx4 v0, s[40:43], s51 offen offset:0 sc1 lds
v_mfma_f32_16x16x32_bf16 v[48:51], a[148:151], v[96:99], v[48:51]
v_mfma_f32_16x16x32_bf16 v[56:59], a[228:231], v[96:99], v[56:59]
ds_read_b128 v[96:99], v1 offset:39936
s_waitcnt lgkmcnt(4)
v_mfma_f32_16x16x32_bf16 v[44:47], a[68:71], v[80:83], v[44:47]
buffer_load_dwordx4 v0, s[40:43], s51 offen offset:1024 sc1 lds
v_mfma_f32_16x16x32_bf16 v[52:55], a[148:151], v[80:83], v[52:55]
v_mfma_f32_16x16x32_bf16 v[60:63], a[228:231], v[80:83], v[60:63]
s_add_u32 s64, s64, 40960
s_cmp_ge_u32 s64, 122880
s_cselect_b32 s64, 0, s64
v_add_u32 v1, s64, v0
ds_read_b128 v[80:83], v1 offset:0
s_waitcnt lgkmcnt(4)
v_mfma_f32_16x16x32_bf16 v[40:43], a[72:75], v[84:87], v[40:43]
buffer_load_dwordx4 v0, s[40:43], s51 offen offset:2048 sc1 lds
v_mfma_f32_16x16x32_bf16 v[48:51], a[152:155], v[84:87], v[48:51]
v_mfma_f32_16x16x32_bf16 v[56:59], a[232:235], v[84:87], v[56:59]
ds_read_b128 v[84:87], v1 offset:20480
s_waitcnt lgkmcnt(4)
v_mfma_f32_16x16x32_bf16 v[44:47], a[72:75], v[88:91], v[44:47]
buffer_load_dwordx4 v0, s[40:43], s51 offen offset:3072 sc1 lds
v_mfma_f32_16x16x32_bf16 v[52:55], a[152:155], v[88:91], v[52:55]
v_mfma_f32_16x16x32_bf16 v[60:63], a[232:235], v[88:91], v[60:63]
ds_read_b128 v[88:91], v1 offset:1024
s_waitcnt lgkmcnt(4)
v_mfma_f32_16x16x32_bf16 v[40:43], a[76:79], v[92:95], v[40:43]
s_add_u32 m0, s61, 8192
s_add_u32 s51, s50, 8192
buffer_load_dwordx4 v0, s[40:43], s51 offen offset:0 sc1 lds
v_mfma_f32_16x16x32_bf16 v[48:51], a[156:159], v[92:95], v[48:51]
v_mfma_f32_16x16x32_bf16 v[56:59], a[236:239], v[92:95], v[56:59]
ds_read_b128 v[92:95], v1 offset:21504
s_waitcnt lgkmcnt(4)
v_mfma_f32_16x16x32_bf16 v[44:47], a[76:79], v[96:99], v[44:47]
buffer_load_dwordx4 v0, s[40:43], s51 offen offset:1024 sc1 lds
v_mfma_f32_16x16x32_bf16 v[52:55], a[156:159], v[96:99], v[52:55]
s_add_u32 s50, s50, 40960
s_add_u32 s61, s61, 40960
s_cmp_ge_u32 s61, 122880
s_cselect_b32 s51, 122880, 0
s_sub_u32 s61, s61, s51
v_mfma_f32_16x16x32_bf16 v[60:63], a[236:239], v[96:99], v[60:63]
ds_read_b128 v[96:99], v1 offset:2048
s_mov_b32 s65, s63
s_add_u32 s59, s47, 0
s_and_b32 s59, s59, 63
s_cmp_eq_u32 s59, 31
s_cselect_b32 s59, -31, 1
s_mul_i32 s57, s59, 393216
s_add_u32 s63, s63, s57
s_mov_b32 s52, s53
s_add_u32 s53, s53, s54
s_and_b32 s59, s47, 62
s_cselect_b32 s56, 0x40000000, s55
s_add_u32 s47, s47, 1
s_and_b32 s59, s47, 63
s_cbranch_scc1 hpk_loop_%=
s_waitcnt lgkmcnt(0)
v_cvt_pk_bf16_f32 v104, v40, v41
v_cvt_pk_bf16_f32 v105, v42, v43
v_cvt_pk_bf16_f32 v106, v48, v49
v_cvt_pk_bf16_f32 v107, v50, v51
buffer_store_dwordx4 v[104:107], v0, s[40:43], s65 offen offset:0 sc1
v_cvt_pk_bf16_f32 v108, v44, v45
v_cvt_pk_bf16_f32 v109, v46, v47
v_cvt_pk_bf16_f32 v110, v52, v53
v_cvt_pk_bf16_f32 v111, v54, v55
buffer_store_dwordx4 v[108:111], v0, s[40:43], s65 offen offset:1024 sc1
v_cvt_pk_bf16_f32 v112, v56, v57
v_cvt_pk_bf16_f32 v113, v58, v59
v_cvt_pk_bf16_f32 v114, v60, v61
v_cvt_pk_bf16_f32 v115, v62, v63
buffer_store_dwordx4 v[112:115], v0, s[40:43], s65 offen offset:2048 sc1
)"
R"(s_waitcnt vmcnt(0)
s_waitcnt lgkmcnt(0)
s_barrier
buffer_atomic_add v226, v3, s[40:43], s52 offen
s_nop 1
s_branch hp_ret_%=
hp_roleq_%=:
s_mul_i32 s62, s45, 10240
s_mul_i32 s50, s47, 40960
s_add_u32 s50, s50, s62
s_add_u32 s50, s50, 2539520
s_mov_b32 s61, s62
s_lshl_b32 s48, s47, 7
s_add_u32 s48, s48, 43520
v_mov_b32 v225, s48
s_mov_b32 s48, 2
v_and_b32 v224, 1, v15
v_lshl_add_u32 v226, v224, 7, v225
v_mov_b32 v224, 0
v_cmp_gt_u32 vcc, s48, v224
s_cbranch_vccz hpq_xfp_ok_%=
s_mov_b32 s60, 0
s_nop 2
hpq_xfp_poll_%=:
buffer_load_dword v224, v226, s[40:43], s58 offen sc1
s_waitcnt vmcnt(0)
v_cmp_gt_u32 vcc, s48, v224
s_nop 4
s_cbranch_vccz hpq_xfp_ok_%=
s_sleep 1
s_add_u32 s60, s60, 1
s_cmp_gt_u32 s60, 65536
s_cbranch_scc1 hp_abort_%=
s_and_b32 s59, s60, 63
s_cbranch_scc1 hpq_xfp_poll_%=
v_mov_b32 v4, 1024
buffer_load_dword v4, v4, s[40:43], s58 offen sc1
s_waitcnt vmcnt(0)
v_readfirstlane_b32 s59, v4
s_nop 0
s_cmp_lg_u32 s59, 0
s_nop 2
s_cbranch_scc1 hp_end_%=
s_branch hpq_xfp_poll_%=
hpq_xfp_ok_%=:
v_add_u32 v225, 256, v225
s_add_u32 m0, s61, 0
s_add_u32 s51, s50, 0
buffer_load_dwordx4 v0, s[40:43], s51 offen offset:0 sc1 lds
buffer_load_dwordx4 v0, s[40:43], s51 offen offset:1024 sc1 lds
buffer_load_dwordx4 v0, s[40:43], s51 offen offset:2048 sc1 lds
buffer_load_dwordx4 v0, s[40:43], s51 offen offset:3072 sc1 lds
s_add_u32 m0, s61, 4096
s_add_u32 s51, s50, 4096
buffer_load_dwordx4 v0, s[40:43], s51 offen offset:0 sc1 lds
buffer_load_dwordx4 v0, s[40:43], s51 offen offset:1024 sc1 lds
buffer_load_dwordx4 v0, s[40:43], s51 offen offset:2048 sc1 lds
buffer_load_dwordx4 v0, s[40:43], s51 offen offset:3072 sc1 lds
s_add_u32 m0, s61, 8192
s_add_u32 s51, s50, 8192
buffer_load_dwordx4 v0, s[40:43], s51 offen offset:0 sc1 lds
buffer_load_dwordx4 v0, s[40:43], s51 offen offset:1024 sc1 lds
s_add_u32 s50, s50, 40960
s_add_u32 s61, s61, 40960
s_cmp_ge_u32 s61, 122880
s_cselect_b32 s51, 122880, 0
s_sub_u32 s61, s61, s51
s_add_u32 m0, s61, 0
s_add_u32 s51, s50, 0
buffer_load_dwordx4 v0, s[40:43], s51 offen offset:0 sc1 lds
buffer_load_dwordx4 v0, s[40:43], s51 offen offset:1024 sc1 lds
buffer_load_dwordx4 v0, s[40:43], s51 offen offset:2048 sc1 lds
buffer_load_dwordx4 v0, s[40:43], s51 offen offset:3072 sc1 lds
s_add_u32 m0, s61, 4096
s_add_u32 s51, s50, 4096
buffer_load_dwordx4 v0, s[40:43], s51 offen offset:0 sc1 lds
buffer_load_dwordx4 v0, s[40:43], s51 offen offset:1024 sc1 lds
buffer_load_dwordx4 v0, s[40:43], s51 offen offset:2048 sc1 lds
buffer_load_dwordx4 v0, s[40:43], s51 offen offset:3072 sc1 lds
s_add_u32 m0, s61, 8192
s_add_u32 s51, s50, 8192
buffer_load_dwordx4 v0, s[40:43], s51 offen offset:0 sc1 lds
buffer_load_dwordx4 v0, s[40:43], s51 offen offset:1024 sc1 lds
s_add_u32 s50, s50, 40960
s_add_u32 s61, s61, 40960
s_cmp_ge_u32 s61, 122880
s_cselect_b32 s51, 122880, 0
s_sub_u32 s61, s61, s51
v_mov_b32 v224, 0
v_mov_b32 v226, 1
v_mov_b32 v1, v0
s_mov_b32 s64, 0
s_lshl_b32 s48, s44, 7
s_lshr_b32 s49, s44, 2
s_lshl_b32 s49, s49, 7
v_mov_b32 v3, s49
v_cmp_eq_u32 vcc, 0, v15
v_mov_b32 v4, 2147483632
s_nop 0
v_cndmask_b32 v3, v4, v3, vcc
s_mov_b32 s52, 0x40000000
v_lshrrev_b32 v13, 4, v15
v_and_b32 v14, 15, v15
s_lshl_b32 s49, s45, 11
s_add_u32 s49, s49, 122880
v_add_u32 v6, s49, v0
s_lshr_b32 s49, s45, 1
s_lshl_b32 s49, s49, 11
s_and_b32 s53, s45, 1
s_lshl_b32 s53, s53, 3
s_add_u32 s49, s49, s53
s_add_u32 s49, s49, 131072
v_add_u32 v7, s49, v0
s_lshl_b32 s49, s45, 10
s_add_u32 s49, s49, 122880
v_add_u32 v8, s49, v0
v_mov_b32 v9, 0
s_add_u32 s49, s48, 6144
v_mov_b32 v10, s49
v_and_b32 v11, 7, v15
v_lshlrev_b32 v11, 4, v11
v_lshl_add_u32 v11, v13, 7, v11
s_bfe_u32 s49, s44, 0x20001
s_lshl_b32 s49, s49, 8
v_add_u32 v12, s49, v0
v_cmp_gt_u32 vcc, 16, v15
v_mov_b32 v4, 2147483632
s_nop 0
v_cndmask_b32 v12, v4, v12, vcc
s_lshl_b32 s49, s45, 4
v_add_u32 v14, s49, v14
v_lshlrev_b32 v13, 2, v13
v_sub_u32 v14, v14, v13
v_mov_b32 v4, 0xff800000
v_cmp_gt_i32 vcc, 0, v14
s_nop 1
v_cndmask_b32 v212, 0, v4, vcc
v_cmp_gt_i32 vcc, 1, v14
s_nop 1
v_cndmask_b32 v213, 0, v4, vcc
v_cmp_gt_i32 vcc, 2, v14
s_nop 1
v_cndmask_b32 v214, 0, v4, vcc
v_cmp_gt_i32 vcc, 3, v14
s_nop 1
v_cndmask_b32 v215, 0, v4, vcc
v_cmp_gt_i32 vcc, 16, v14
s_nop 1
v_cndmask_b32 v216, 0, v4, vcc
v_cmp_gt_i32 vcc, 17, v14
s_nop 1
v_cndmask_b32 v217, 0, v4, vcc
v_cmp_gt_i32 vcc, 18, v14
s_nop 1
v_cndmask_b32 v218, 0, v4, vcc
v_cmp_gt_i32 vcc, 19, v14
s_nop 1
v_cndmask_b32 v219, 0, v4, vcc
v_cmp_lt_u32 vcc, 31, v15
s_nop 1
v_cndmask_b32 v220, 0, v4, vcc
v_cndmask_b32 v221, 0, v4, vcc
v_cndmask_b32 v222, 0, v4, vcc
v_cndmask_b32 v223, 0, v4, vcc
s_waitcnt vmcnt(10)
s_waitcnt lgkmcnt(0)
s_barrier
s_mov_b32 s65, s47
s_sub_u32 s63, s47, 2
s_cmp_gt_u32 s45, 1
s_cselect_b32 s30, 0x40000000, 0
s_mul_i32 s34, s44, 3072
s_add_u32 s34, s34, 127139840
s_mul_i32 s35, s63, 1536
s_add_u32 s35, s35, 2157568
s_lshl1_add_u32 s36, s44, s45
s_lshl_b32 s36, s36, 10
s_add_u32 s36, s36, 130351104
s_lshr_b32 s37, s44, 3
s_lshl_b32 s37, s37, 1
s_and_b32 s48, s44, 1
s_add_u32 s37, s37, s48
s_lshl3_add_u32 s37, s45, s37
s_lshl_b32 s37, s37, 10
s_add_u32 s37, s37, 132546560
s_mul_i32 s54, s44, 12288
s_add_u32 s54, s54, 7389184
s_or_b32 s54, s54, s30
s_add_u32 s55, s54, 4096
s_add_u32 s56, s54, 8192
s_lshl_b32 s48, s47, 7
s_add_u32 s48, s48, 10240
v_mov_b32 v227, s48
v_mov_b32 v236, 0
s_mov_b32 s48, 32
v_add_u32 v227, -128, v227
v_mov_b32 v236, 32
s_cmp_gt_u32 s45, 1
s_nop 0
s_cbranch_scc1 hpq_light_%=
)"
R"(ds_read_b128 v[80:83], v1 offset:0
ds_read_b128 v[84:87], v1 offset:20480
ds_read_b128 v[88:91], v1 offset:1024
ds_read_b128 v[92:95], v1 offset:21504
ds_read_b128 v[96:99], v1 offset:2048
hpq_loop_%=:
s_mov_b32 s29, s52
s_waitcnt vmcnt(25)
s_add_u32 s48, s63, 1
s_cmp_ge_i32 s63, s65
s_cselect_b32 s48, s48, 0
s_cselect_b32 s49, s30, 0x40000000
v_cmp_gt_u32 vcc, s48, v9
s_cbranch_vccz hpq_kma0_ok_%=
s_waitcnt vmcnt(0)
buffer_atomic_add v226, v3, s[40:43], s29 offen
s_mov_b32 s29, 0x40000000
v_cmp_gt_u32 vcc, s48, v9
s_cbranch_vccz hpq_kma0_ok_%=
s_mov_b32 s60, 0
s_nop 2
hpq_kma0_poll_%=:
buffer_load_dword v9, v10, s[40:43], s58 offen sc1
s_waitcnt vmcnt(0)
v_cmp_gt_u32 vcc, s48, v9
s_nop 4
s_cbranch_vccz hpq_kma0_ok_%=
s_sleep 2
s_add_u32 s60, s60, 1
s_cmp_gt_u32 s60, 65536
s_cbranch_scc1 hp_abort_%=
s_and_b32 s59, s60, 63
s_cbranch_scc1 hpq_kma0_poll_%=
v_mov_b32 v4, 1024
buffer_load_dword v4, v4, s[40:43], s58 offen sc1
s_waitcnt vmcnt(0)
v_readfirstlane_b32 s59, v4
s_nop 0
s_cmp_lg_u32 s59, 0
s_nop 2
s_cbranch_scc1 hp_end_%=
s_branch hpq_kma0_poll_%=
hpq_kma0_ok_%=:
s_movk_i32 s48, 32
v_cmp_gt_u32 vcc, s48, v236
s_cbranch_vccz hpq_kxa0_ok_%=
s_mov_b32 s60, 0
s_nop 2
hpq_kxa0_poll_%=:
buffer_load_dword v236, v227, s[40:43], s58 offen sc1
s_waitcnt vmcnt(0)
v_cmp_gt_u32 vcc, s48, v236
s_nop 4
s_cbranch_vccz hpq_kxa0_ok_%=
s_sleep 24
s_add_u32 s60, s60, 1
s_cmp_gt_u32 s60, 65536
s_cbranch_scc1 hp_abort_%=
s_and_b32 s59, s60, 63
s_cbranch_scc1 hpq_kxa0_poll_%=
v_mov_b32 v4, 1024
buffer_load_dword v4, v4, s[40:43], s58 offen sc1
s_waitcnt vmcnt(0)
v_readfirstlane_b32 s59, v4
s_nop 0
s_cmp_lg_u32 s59, 0
s_nop 2
s_cbranch_scc1 hp_end_%=
s_branch hpq_kxa0_poll_%=
hpq_kxa0_ok_%=:
v_add_u32 v227, 128, v227
s_or_b32 s57, s34, s49
s_waitcnt lgkmcnt(4)
v_mfma_f32_16x16x32_bf16 v[16:19], a[0:3], v[80:83], 0
s_or_b32 s52, s35, s49
buffer_load_dwordx4 v[164:167], v11, s[40:43], s57 offen sc1
v_mfma_f32_16x16x32_bf16 v[24:27], a[80:83], v[80:83], 0
s_or_b32 s26, s36, s49
buffer_load_dwordx4 v[168:171], v11, s[40:43], s57 offen offset:512 sc1
v_mfma_f32_16x16x32_bf16 v[32:35], a[160:163], v[80:83], 0
s_or_b32 s27, s37, s49
ds_read_b128 v[80:83], v1 offset:22528
s_waitcnt lgkmcnt(4)
v_mfma_f32_16x16x32_bf16 v[20:23], a[0:3], v[84:87], 0
s_add_u32 s38, s63, 0
buffer_load_dwordx4 v[172:175], v11, s[40:43], s57 offen offset:1024 sc1
v_mfma_f32_16x16x32_bf16 v[28:31], a[80:83], v[84:87], 0
s_and_b32 s38, s38, 63
buffer_load_dwordx4 v[176:179], v11, s[40:43], s57 offen offset:1536 sc1
v_mfma_f32_16x16x32_bf16 v[36:39], a[160:163], v[84:87], 0
s_cmp_eq_u32 s38, 31
ds_read_b128 v[84:87], v1 offset:3072
s_waitcnt lgkmcnt(4)
v_mfma_f32_16x16x32_bf16 v[16:19], a[4:7], v[88:91], v[16:19]
s_cselect_b32 s38, -31, 1
buffer_load_dwordx4 v[180:183], v11, s[40:43], s57 offen offset:2048 sc1
v_mfma_f32_16x16x32_bf16 v[24:27], a[84:87], v[88:91], v[24:27]
s_mul_i32 s39, s38, 98304
buffer_load_dwordx4 v[184:187], v11, s[40:43], s57 offen offset:2560 sc1
v_mfma_f32_16x16x32_bf16 v[32:35], a[164:167], v[88:91], v[32:35]
s_add_u32 s34, s34, s39
ds_read_b128 v[88:91], v1 offset:23552
s_waitcnt lgkmcnt(4)
v_mfma_f32_16x16x32_bf16 v[20:23], a[4:7], v[92:95], v[20:23]
s_mul_i32 s39, s38, 65536
buffer_load_dword v236, v227, s[40:43], s58 offen sc1
v_cvt_pk_bf16_f32 v104, v40, v41
v_mfma_f32_16x16x32_bf16 v[28:31], a[84:87], v[92:95], v[28:31]
s_add_u32 s36, s36, s39
s_waitcnt vmcnt(20)
v_swap_b32 v150, v152
v_mfma_f32_16x16x32_bf16 v[36:39], a[164:167], v[92:95], v[36:39]
s_mul_i32 s39, s38, 16384
v_cvt_pk_bf16_f32 v105, v42, v43
ds_read_b128 v[92:95], v1 offset:4096
s_waitcnt lgkmcnt(4)
v_mfma_f32_16x16x32_bf16 v[16:19], a[8:11], v[96:99], v[16:19]
s_add_u32 s37, s37, s39
v_swap_b32 v151, v153
v_mfma_f32_16x16x32_bf16 v[24:27], a[88:91], v[96:99], v[24:27]
s_add_u32 s35, s35, 1536
v_swap_b32 v158, v160
v_mfma_f32_16x16x32_bf16 v[32:35], a[168:171], v[96:99], v[32:35]
s_add_u32 s63, s63, 1
v_cvt_pk_bf16_f32 v106, v48, v49
ds_read_b128 v[96:99], v1 offset:24576
s_waitcnt lgkmcnt(4)
v_mfma_f32_16x16x32_bf16 v[20:23], a[8:11], v[80:83], v[20:23]
v_swap_b32 v159, v161
v_mfma_f32_16x16x32_bf16 v[28:31], a[88:91], v[80:83], v[28:31]
v_cvt_pk_bf16_f32 v107, v50, v51
v_mfma_f32_16x16x32_bf16 v[36:39], a[168:171], v[80:83], v[36:39]
ds_read_b128 v[80:83], v1 offset:5120
s_waitcnt lgkmcnt(4)
v_mfma_f32_16x16x32_bf16 v[16:19], a[12:15], v[84:87], v[16:19]
ds_write_b128 v6, v[104:107] offset:12288
v_mfma_f32_16x16x32_bf16 v[64:67], v[116:119], v[188:191], v[212:215]
v_cvt_pk_bf16_f32 v108, v44, v45
v_cvt_pk_bf16_f32 v109, v46, v47
v_mfma_f32_16x16x32_bf16 v[24:27], a[92:95], v[84:87], v[24:27]
v_cvt_pk_bf16_f32 v110, v52, v53
v_cvt_pk_bf16_f32 v111, v54, v55
v_mfma_f32_16x16x32_bf16 v[68:71], v[120:123], v[188:191], v[216:219]
ds_write_b128 v6, v[108:111] offset:13312
v_mfma_f32_16x16x32_bf16 v[32:35], a[172:175], v[84:87], v[32:35]
v_cvt_pk_bf16_f32 v112, v56, v57
ds_read_b128 v[84:87], v1 offset:25600
s_waitcnt lgkmcnt(6)
v_mfma_f32_16x16x32_bf16 v[20:23], a[12:15], v[88:91], v[20:23]
v_cvt_pk_bf16_f32 v113, v58, v59
v_cvt_pk_bf16_f32 v114, v60, v61
v_mfma_f32_16x16x32_bf16 v[64:67], v[124:127], v[192:195], v[64:67]
v_cvt_pk_bf16_f32 v115, v62, v63
v_mfma_f32_16x16x32_bf16 v[28:31], a[92:95], v[88:91], v[28:31]
ds_write2st64_b64 v7, v[112:113], v[114:115] offset0:24 offset1:26
v_mfma_f32_16x16x32_bf16 v[68:71], v[128:131], v[192:195], v[68:71]
buffer_load_dword v224, v225, s[40:43], s58 offen sc1
v_mfma_f32_16x16x32_bf16 v[36:39], a[172:175], v[88:91], v[36:39]
ds_read_b128 v[88:91], v1 offset:6144
s_waitcnt lgkmcnt(7)
v_mfma_f32_16x16x32_bf16 v[16:19], a[16:19], v[92:95], v[16:19]
v_mfma_f32_16x16x32_bf16 v[64:67], v[132:135], v[196:199], v[64:67]
v_mfma_f32_16x16x32_bf16 v[24:27], a[96:99], v[92:95], v[24:27]
v_mfma_f32_16x16x32_bf16 v[68:71], v[136:139], v[196:199], v[68:71]
v_mfma_f32_16x16x32_bf16 v[32:35], a[176:179], v[92:95], v[32:35]
ds_read_b128 v[92:95], v1 offset:26624
s_waitcnt lgkmcnt(7)
v_mfma_f32_16x16x32_bf16 v[20:23], a[16:19], v[96:99], v[20:23]
v_mfma_f32_16x16x32_bf16 v[64:67], v[140:143], v[200:203], v[64:67]
v_mfma_f32_16x16x32_bf16 v[28:31], a[96:99], v[96:99], v[28:31]
v_mfma_f32_16x16x32_bf16 v[68:71], v[144:147], v[200:203], v[68:71]
v_mfma_f32_16x16x32_bf16 v[36:39], a[176:179], v[96:99], v[36:39]
ds_read_b128 v[96:99], v1 offset:7168
s_waitcnt lgkmcnt(7)
v_mfma_f32_16x16x32_bf16 v[16:19], a[20:23], v[80:83], v[16:19]
v_mfma_f32_16x16x32_bf16 v[64:67], v[148:151], v[204:207], v[64:67]
v_mfma_f32_16x16x32_bf16 v[24:27], a[100:103], v[80:83], v[24:27]
v_mfma_f32_16x16x32_bf16 v[68:71], v[152:155], v[204:207], v[68:71]
v_mfma_f32_16x16x32_bf16 v[32:35], a[180:183], v[80:83], v[32:35]
ds_read_b128 v[80:83], v1 offset:27648
s_waitcnt lgkmcnt(5)
v_mfma_f32_16x16x32_bf16 v[20:23], a[20:23], v[84:87], v[20:23]
v_mfma_f32_16x16x32_bf16 v[64:67], v[156:159], v[208:211], v[64:67]
v_mfma_f32_16x16x32_bf16 v[28:31], a[100:103], v[84:87], v[28:31]
v_mfma_f32_16x16x32_bf16 v[68:71], v[160:163], v[208:211], v[68:71]
v_mfma_f32_16x16x32_bf16 v[36:39], a[180:183], v[84:87], v[36:39]
ds_read_b128 v[84:87], v1 offset:8192
s_waitcnt lgkmcnt(4)
v_mfma_f32_16x16x32_bf16 v[16:19], a[24:27], v[88:91], v[16:19]
v_mfma_f32_16x16x32_bf16 v[24:27], a[104:107], v[88:91], v[24:27]
v_mfma_f32_16x16x32_bf16 v[32:35], a[184:187], v[88:91], v[32:35]
ds_read_b128 v[88:91], v1 offset:28672
s_waitcnt lgkmcnt(4)
v_mfma_f32_16x16x32_bf16 v[20:23], a[24:27], v[92:95], v[20:23]
buffer_load_dword v9, v10, s[40:43], s58 offen sc1
s_waitcnt vmcnt(8)
v_mfma_f32_16x16x32_bf16 v[72:75], v[164:167], v[188:191], v[220:223]
v_mfma_f32_16x16x32_bf16 v[28:31], a[104:107], v[92:95], v[28:31]
s_waitcnt vmcnt(7)
v_mfma_f32_16x16x32_bf16 v[72:75], v[168:171], v[192:195], v[72:75]
)"
R"(v_mfma_f32_16x16x32_bf16 v[36:39], a[184:187], v[92:95], v[36:39]
ds_read_b128 v[92:95], v1 offset:9216
s_waitcnt lgkmcnt(4)
v_mfma_f32_16x16x32_bf16 v[16:19], a[28:31], v[96:99], v[16:19]
buffer_load_dwordx4 v[116:119], v0, s[40:43], s54 offen sc1
s_waitcnt vmcnt(7)
v_mfma_f32_16x16x32_bf16 v[72:75], v[172:175], v[196:199], v[72:75]
v_mfma_f32_16x16x32_bf16 v[24:27], a[108:111], v[96:99], v[24:27]
s_waitcnt vmcnt(6)
v_mfma_f32_16x16x32_bf16 v[72:75], v[176:179], v[200:203], v[72:75]
v_mfma_f32_16x16x32_bf16 v[32:35], a[188:191], v[96:99], v[32:35]
ds_read_b128 v[96:99], v1 offset:29696
s_waitcnt lgkmcnt(4)
v_mfma_f32_16x16x32_bf16 v[20:23], a[28:31], v[80:83], v[20:23]
buffer_load_dwordx4 v[120:123], v0, s[40:43], s54 offen offset:1024 sc1
s_waitcnt vmcnt(6)
v_mfma_f32_16x16x32_bf16 v[72:75], v[180:183], v[204:207], v[72:75]
v_mfma_f32_16x16x32_bf16 v[28:31], a[108:111], v[80:83], v[28:31]
s_waitcnt vmcnt(5)
v_mfma_f32_16x16x32_bf16 v[72:75], v[184:187], v[208:211], v[72:75]
v_mfma_f32_16x16x32_bf16 v[36:39], a[188:191], v[80:83], v[36:39]
ds_read_b128 v[80:83], v1 offset:10240
s_waitcnt lgkmcnt(4)
v_mfma_f32_16x16x32_bf16 v[16:19], a[32:35], v[84:87], v[16:19]
buffer_load_dwordx4 v[124:127], v0, s[40:43], s54 offen offset:3072 sc1
v_mfma_f32_16x16x32_bf16 v[24:27], a[112:115], v[84:87], v[24:27]
v_mfma_f32_16x16x32_bf16 v[32:35], a[192:195], v[84:87], v[32:35]
ds_read_b128 v[84:87], v1 offset:30720
s_waitcnt lgkmcnt(4)
v_mfma_f32_16x16x32_bf16 v[20:23], a[32:35], v[88:91], v[20:23]
v_max3_f32 v76, v64, v65, v66
v_max3_f32 v76, v76, v67, v68
v_mfma_f32_16x16x32_bf16 v[28:31], a[112:115], v[88:91], v[28:31]
v_max3_f32 v76, v76, v69, v70
v_max3_f32 v76, v76, v71, v72
v_mfma_f32_16x16x32_bf16 v[36:39], a[192:195], v[88:91], v[36:39]
v_max3_f32 v76, v76, v73, v74
ds_read_b128 v[88:91], v1 offset:11264
s_waitcnt lgkmcnt(4)
v_mfma_f32_16x16x32_bf16 v[16:19], a[36:39], v[92:95], v[16:19]
buffer_load_dwordx4 v[128:131], v0, s[40:43], s55 offen sc1
v_max_f32 v76, v76, v75
v_mfma_f32_16x16x32_bf16 v[24:27], a[116:119], v[92:95], v[24:27]
v_mov_b32 v77, v76
v_mfma_f32_16x16x32_bf16 v[32:35], a[196:199], v[92:95], v[32:35]
ds_read_b128 v[92:95], v1 offset:31744
s_waitcnt lgkmcnt(4)
v_mfma_f32_16x16x32_bf16 v[20:23], a[36:39], v[96:99], v[20:23]
v_permlane16_swap_b32 v76, v77
v_mfma_f32_16x16x32_bf16 v[28:31], a[116:119], v[96:99], v[28:31]
v_max_f32 v76, v76, v77
v_mov_b32 v77, v76
v_mfma_f32_16x16x32_bf16 v[36:39], a[196:199], v[96:99], v[36:39]
buffer_load_dwordx4 v[132:135], v0, s[40:43], s55 offen offset:2048 sc1
ds_read_b128 v[96:99], v1 offset:12288
s_waitcnt lgkmcnt(4)
v_mfma_f32_16x16x32_bf16 v[16:19], a[40:43], v[80:83], v[16:19]
buffer_load_dwordx4 v[136:139], v0, s[40:43], s55 offen offset:3072 sc1
v_mfma_f32_16x16x32_bf16 v[24:27], a[120:123], v[80:83], v[24:27]
v_permlane32_swap_b32 v76, v77
v_mfma_f32_16x16x32_bf16 v[32:35], a[200:203], v[80:83], v[32:35]
v_max_f32 v76, v76, v77
ds_read_b128 v[80:83], v1 offset:32768
s_waitcnt lgkmcnt(4)
v_mfma_f32_16x16x32_bf16 v[20:23], a[40:43], v[84:87], v[20:23]
v_mul_f32 v76, 0xbdd53b95, v76
v_fmamk_f32 v64, v64, 0x3dd53b95, v76
v_mfma_f32_16x16x32_bf16 v[28:31], a[120:123], v[84:87], v[28:31]
v_fmamk_f32 v65, v65, 0x3dd53b95, v76
v_fmamk_f32 v66, v66, 0x3dd53b95, v76
v_mfma_f32_16x16x32_bf16 v[36:39], a[200:203], v[84:87], v[36:39]
v_fmamk_f32 v67, v67, 0x3dd53b95, v76
ds_read_b128 v[84:87], v1 offset:13312
s_waitcnt lgkmcnt(4)
v_mfma_f32_16x16x32_bf16 v[16:19], a[44:47], v[88:91], v[16:19]
buffer_load_dwordx4 v[140:143], v0, s[40:43], s56 offen offset:1024 sc1
v_fmamk_f32 v68, v68, 0x3dd53b95, v76
v_mfma_f32_16x16x32_bf16 v[24:27], a[124:127], v[88:91], v[24:27]
v_fmamk_f32 v69, v69, 0x3dd53b95, v76
v_fmamk_f32 v70, v70, 0x3dd53b95, v76
v_mfma_f32_16x16x32_bf16 v[32:35], a[204:207], v[88:91], v[32:35]
v_fmamk_f32 v71, v71, 0x3dd53b95, v76
ds_read_b128 v[88:91], v1 offset:33792
s_waitcnt lgkmcnt(4)
v_mfma_f32_16x16x32_bf16 v[20:23], a[44:47], v[92:95], v[20:23]
v_fmamk_f32 v72, v72, 0x3dd53b95, v76
v_fmamk_f32 v73, v73, 0x3dd53b95, v76
v_mfma_f32_16x16x32_bf16 v[28:31], a[124:127], v[92:95], v[28:31]
v_fmamk_f32 v74, v74, 0x3dd53b95, v76
v_fmamk_f32 v75, v75, 0x3dd53b95, v76
v_mfma_f32_16x16x32_bf16 v[36:39], a[204:207], v[92:95], v[36:39]
buffer_load_dwordx4 v[144:147], v0, s[40:43], s56 offen offset:2048 sc1
ds_read_b128 v[92:95], v1 offset:14336
s_waitcnt lgkmcnt(4)
v_mfma_f32_16x16x32_bf16 v[16:19], a[48:51], v[96:99], v[16:19]
buffer_load_dwordx4 v[148:151], v0, s[40:43], s54 offen offset:2048 sc1
v_mfma_f32_16x16x32_bf16 v[24:27], a[128:131], v[96:99], v[24:27]
v_exp_f32 v64, v64
v_mfma_f32_16x16x32_bf16 v[32:35], a[208:211], v[96:99], v[32:35]
ds_read_b128 v[96:99], v1 offset:34816
s_waitcnt lgkmcnt(4)
v_mfma_f32_16x16x32_bf16 v[20:23], a[48:51], v[80:83], v[20:23]
v_exp_f32 v65, v65
v_mfma_f32_16x16x32_bf16 v[28:31], a[128:131], v[80:83], v[28:31]
v_exp_f32 v66, v66
v_mfma_f32_16x16x32_bf16 v[36:39], a[208:211], v[80:83], v[36:39]
buffer_load_dwordx4 v[152:155], v0, s[40:43], s55 offen offset:1024 sc1
ds_read_b128 v[80:83], v1 offset:15360
s_waitcnt lgkmcnt(4)
v_mfma_f32_16x16x32_bf16 v[16:19], a[52:55], v[84:87], v[16:19]
buffer_load_dwordx4 v[156:159], v0, s[40:43], s56 offen sc1
v_mfma_f32_16x16x32_bf16 v[24:27], a[132:135], v[84:87], v[24:27]
v_exp_f32 v67, v67
v_mfma_f32_16x16x32_bf16 v[32:35], a[212:215], v[84:87], v[32:35]
s_add_u32 s38, s63, 0
ds_read_b128 v[84:87], v1 offset:35840
s_waitcnt lgkmcnt(4)
v_mfma_f32_16x16x32_bf16 v[20:23], a[52:55], v[88:91], v[20:23]
s_and_b32 s38, s38, 63
v_exp_f32 v68, v68
v_mfma_f32_16x16x32_bf16 v[28:31], a[132:135], v[88:91], v[28:31]
s_cmp_eq_u32 s38, 31
v_exp_f32 v69, v69
v_mfma_f32_16x16x32_bf16 v[36:39], a[212:215], v[88:91], v[36:39]
s_cselect_b32 s38, -31, 1
buffer_load_dwordx4 v[160:163], v0, s[40:43], s56 offen offset:3072 sc1
ds_read_b128 v[88:91], v1 offset:16384
s_waitcnt lgkmcnt(4)
v_mfma_f32_16x16x32_bf16 v[16:19], a[56:59], v[92:95], v[16:19]
s_mul_i32 s39, s38, 393216
v_exp_f32 v70, v70
v_mfma_f32_16x16x32_bf16 v[24:27], a[136:139], v[92:95], v[24:27]
s_add_u32 s54, s54, s39
v_exp_f32 v71, v71
v_mfma_f32_16x16x32_bf16 v[32:35], a[216:219], v[92:95], v[32:35]
s_add_u32 s55, s55, s39
ds_read_b128 v[92:95], v1 offset:36864
s_waitcnt lgkmcnt(4)
v_mfma_f32_16x16x32_bf16 v[20:23], a[56:59], v[96:99], v[20:23]
s_add_u32 s56, s56, s39
v_exp_f32 v72, v72
v_mfma_f32_16x16x32_bf16 v[28:31], a[136:139], v[96:99], v[28:31]
v_exp_f32 v73, v73
v_mfma_f32_16x16x32_bf16 v[36:39], a[216:219], v[96:99], v[36:39]
s_waitcnt vmcnt(13) lgkmcnt(4)
s_barrier
buffer_atomic_add v226, v3, s[40:43], s29 offen
s_and_b32 s48, s47, 63
s_cmp_lt_u32 s48, 62
s_cselect_b32 s48, 2, 0
v_cmp_gt_u32 vcc, s48, v224
s_cbranch_vccz hpq_xf0_ok_%=
s_mov_b32 s60, 0
s_nop 2
hpq_xf0_poll_%=:
buffer_load_dword v224, v225, s[40:43], s58 offen sc1
s_waitcnt vmcnt(0)
v_cmp_gt_u32 vcc, s48, v224
s_nop 4
s_cbranch_vccz hpq_xf0_ok_%=
s_sleep 2
s_add_u32 s60, s60, 1
s_cmp_gt_u32 s60, 65536
s_cbranch_scc1 hp_abort_%=
s_and_b32 s59, s60, 63
s_cbranch_scc1 hpq_xf0_poll_%=
v_mov_b32 v4, 1024
buffer_load_dword v4, v4, s[40:43], s58 offen sc1
s_waitcnt vmcnt(0)
v_readfirstlane_b32 s59, v4
s_nop 0
s_cmp_lg_u32 s59, 0
s_nop 2
s_cbranch_scc1 hp_end_%=
s_branch hpq_xf0_poll_%=
hpq_xf0_ok_%=:
v_add_u32 v225, 128, v225
ds_read_b128 v[96:99], v1 offset:17408
s_waitcnt lgkmcnt(4)
v_mfma_f32_16x16x32_bf16 v[16:19], a[60:63], v[80:83], v[16:19]
s_add_u32 m0, s61, 0
s_add_u32 s51, s50, 0
buffer_load_dwordx4 v0, s[40:43], s51 offen offset:0 sc1 lds
v_mfma_f32_16x16x32_bf16 v[24:27], a[140:143], v[80:83], v[24:27]
v_exp_f32 v74, v74
v_mfma_f32_16x16x32_bf16 v[32:35], a[220:223], v[80:83], v[32:35]
ds_read_b128 v[188:191], v8 offset:12288
ds_read_b128 v[80:83], v1 offset:37888
s_waitcnt lgkmcnt(5)
v_mfma_f32_16x16x32_bf16 v[20:23], a[60:63], v[84:87], v[20:23]
buffer_load_dwordx4 v0, s[40:43], s51 offen offset:1024 sc1 lds
v_mfma_f32_16x16x32_bf16 v[28:31], a[140:143], v[84:87], v[28:31]
v_exp_f32 v75, v75
v_mfma_f32_16x16x32_bf16 v[36:39], a[220:223], v[84:87], v[36:39]
v_add_f32 v76, v64, v65
ds_read_b128 v[84:87], v1 offset:18432
s_waitcnt lgkmcnt(5)
v_mfma_f32_16x16x32_bf16 v[16:19], a[64:67], v[88:91], v[16:19]
buffer_load_dwordx4 v0, s[40:43], s51 offen offset:2048 sc1 lds
v_mfma_f32_16x16x32_bf16 v[24:27], a[144:147], v[88:91], v[24:27]
)"
R"(v_add_f32 v76, v76, v66
v_add_f32 v76, v76, v67
v_mfma_f32_16x16x32_bf16 v[32:35], a[224:227], v[88:91], v[32:35]
v_add_f32 v76, v76, v68
ds_read_b128 v[88:91], v1 offset:38912
s_waitcnt lgkmcnt(5)
v_mfma_f32_16x16x32_bf16 v[20:23], a[64:67], v[92:95], v[20:23]
buffer_load_dwordx4 v0, s[40:43], s51 offen offset:3072 sc1 lds
v_mfma_f32_16x16x32_bf16 v[28:31], a[144:147], v[92:95], v[28:31]
v_add_f32 v76, v76, v69
v_add_f32 v76, v76, v70
v_mfma_f32_16x16x32_bf16 v[36:39], a[224:227], v[92:95], v[36:39]
v_add_f32 v76, v76, v71
ds_read_b128 v[92:95], v1 offset:19456
s_waitcnt lgkmcnt(5)
v_mfma_f32_16x16x32_bf16 v[16:19], a[68:71], v[96:99], v[16:19]
s_add_u32 m0, s61, 4096
s_add_u32 s51, s50, 4096
buffer_load_dwordx4 v0, s[40:43], s51 offen offset:0 sc1 lds
v_mfma_f32_16x16x32_bf16 v[24:27], a[148:151], v[96:99], v[24:27]
v_add_f32 v76, v76, v72
v_add_f32 v76, v76, v73
v_mfma_f32_16x16x32_bf16 v[32:35], a[228:231], v[96:99], v[32:35]
v_add_f32 v76, v76, v74
ds_read_b128 v[192:195], v8 offset:14336
ds_read_b128 v[196:199], v8 offset:16384
ds_read_b128 v[200:203], v8 offset:18432
ds_read_b128 v[204:207], v8 offset:20480
ds_read_b128 v[208:211], v8 offset:22528
ds_read_b128 v[96:99], v1 offset:39936
s_waitcnt lgkmcnt(9)
v_mfma_f32_16x16x32_bf16 v[20:23], a[68:71], v[80:83], v[20:23]
buffer_load_dwordx4 v0, s[40:43], s51 offen offset:1024 sc1 lds
v_mfma_f32_16x16x32_bf16 v[28:31], a[148:151], v[80:83], v[28:31]
v_add_f32 v76, v76, v75
v_mov_b32 v77, v76
v_mfma_f32_16x16x32_bf16 v[36:39], a[228:231], v[80:83], v[36:39]
s_add_u32 s64, s64, 40960
s_cmp_ge_u32 s64, 122880
s_cselect_b32 s64, 0, s64
v_add_u32 v1, s64, v0
ds_read_b128 v[80:83], v1 offset:0
s_waitcnt lgkmcnt(9)
v_mfma_f32_16x16x32_bf16 v[16:19], a[72:75], v[84:87], v[16:19]
buffer_load_dwordx4 v0, s[40:43], s51 offen offset:2048 sc1 lds
v_mfma_f32_16x16x32_bf16 v[24:27], a[152:155], v[84:87], v[24:27]
v_permlane16_swap_b32 v76, v77
v_mfma_f32_16x16x32_bf16 v[32:35], a[232:235], v[84:87], v[32:35]
v_add_f32 v76, v76, v77
ds_read_b128 v[84:87], v1 offset:20480
s_waitcnt lgkmcnt(9)
v_mfma_f32_16x16x32_bf16 v[20:23], a[72:75], v[88:91], v[20:23]
buffer_load_dwordx4 v0, s[40:43], s51 offen offset:3072 sc1 lds
v_mfma_f32_16x16x32_bf16 v[28:31], a[152:155], v[88:91], v[28:31]
v_mov_b32 v77, v76
v_mfma_f32_16x16x32_bf16 v[36:39], a[232:235], v[88:91], v[36:39]
ds_read_b128 v[88:91], v1 offset:1024
s_waitcnt lgkmcnt(9)
v_mfma_f32_16x16x32_bf16 v[16:19], a[76:79], v[92:95], v[16:19]
s_add_u32 m0, s61, 8192
s_add_u32 s51, s50, 8192
buffer_load_dwordx4 v0, s[40:43], s51 offen offset:0 sc1 lds
v_mfma_f32_16x16x32_bf16 v[24:27], a[156:159], v[92:95], v[24:27]
v_permlane32_swap_b32 v76, v77
v_mfma_f32_16x16x32_bf16 v[32:35], a[236:239], v[92:95], v[32:35]
v_add_f32 v76, v76, v77
ds_read_b128 v[92:95], v1 offset:21504
s_waitcnt lgkmcnt(4)
v_mfma_f32_16x16x32_bf16 v[20:23], a[76:79], v[96:99], v[20:23]
buffer_load_dwordx4 v0, s[40:43], s51 offen offset:1024 sc1 lds
v_mfma_f32_16x16x32_bf16 v[28:31], a[156:159], v[96:99], v[28:31]
s_add_u32 s50, s50, 40960
s_add_u32 s61, s61, 40960
s_cmp_ge_u32 s61, 122880
s_cselect_b32 s51, 122880, 0
s_sub_u32 s61, s61, s51
v_rcp_f32 v76, v76
v_mfma_f32_16x16x32_bf16 v[36:39], a[236:239], v[96:99], v[36:39]
v_mul_f32 v64, v64, v76
ds_read_b128 v[96:99], v1 offset:2048
v_mul_f32 v65, v65, v76
v_mul_f32 v66, v66, v76
v_mul_f32 v67, v67, v76
v_mul_f32 v68, v68, v76
v_mul_f32 v69, v69, v76
v_mul_f32 v70, v70, v76
v_mul_f32 v71, v71, v76
v_mul_f32 v72, v72, v76
v_mul_f32 v73, v73, v76
v_mul_f32 v74, v74, v76
v_mul_f32 v75, v75, v76
v_cvt_pk_bf16_f32 v228, v64, v65
v_cvt_pk_bf16_f32 v229, v66, v67
v_cvt_pk_bf16_f32 v230, v68, v69
v_cvt_pk_bf16_f32 v231, v70, v71
v_cvt_pk_bf16_f32 v232, v72, v73
v_cvt_pk_bf16_f32 v233, v74, v75
v_mov_b32 v234, v232
v_mov_b32 v235, v233
buffer_store_dwordx4 v[228:231], v0, s[40:43], s26 offen sc1
v_permlane16_swap_b32 v232, v234
v_permlane16_swap_b32 v233, v235
buffer_store_dwordx4 v[232:235], v12, s[40:43], s27 offen sc1
s_add_u32 s47, s47, 1
s_mov_b32 s29, s52
s_waitcnt vmcnt(25)
s_add_u32 s48, s63, 1
s_cmp_ge_i32 s63, s65
s_cselect_b32 s48, s48, 0
s_cselect_b32 s49, s30, 0x40000000
v_cmp_gt_u32 vcc, s48, v9
s_cbranch_vccz hpq_kma1_ok_%=
s_waitcnt vmcnt(0)
buffer_atomic_add v226, v3, s[40:43], s29 offen
s_mov_b32 s29, 0x40000000
v_cmp_gt_u32 vcc, s48, v9
s_cbranch_vccz hpq_kma1_ok_%=
s_mov_b32 s60, 0
s_nop 2
hpq_kma1_poll_%=:
buffer_load_dword v9, v10, s[40:43], s58 offen sc1
s_waitcnt vmcnt(0)
v_cmp_gt_u32 vcc, s48, v9
s_nop 4
s_cbranch_vccz hpq_kma1_ok_%=
s_sleep 2
s_add_u32 s60, s60, 1
s_cmp_gt_u32 s60, 65536
s_cbranch_scc1 hp_abort_%=
s_and_b32 s59, s60, 63
s_cbranch_scc1 hpq_kma1_poll_%=
v_mov_b32 v4, 1024
buffer_load_dword v4, v4, s[40:43], s58 offen sc1
s_waitcnt vmcnt(0)
v_readfirstlane_b32 s59, v4
s_nop 0
s_cmp_lg_u32 s59, 0
s_nop 2
s_cbranch_scc1 hp_end_%=
s_branch hpq_kma1_poll_%=
hpq_kma1_ok_%=:
s_movk_i32 s48, 32
v_cmp_gt_u32 vcc, s48, v236
s_cbranch_vccz hpq_kxa1_ok_%=
s_mov_b32 s60, 0
s_nop 2
hpq_kxa1_poll_%=:
buffer_load_dword v236, v227, s[40:43], s58 offen sc1
s_waitcnt vmcnt(0)
v_cmp_gt_u32 vcc, s48, v236
s_nop 4
s_cbranch_vccz hpq_kxa1_ok_%=
s_sleep 24
s_add_u32 s60, s60, 1
s_cmp_gt_u32 s60, 65536
s_cbranch_scc1 hp_abort_%=
s_and_b32 s59, s60, 63
s_cbranch_scc1 hpq_kxa1_poll_%=
v_mov_b32 v4, 1024
buffer_load_dword v4, v4, s[40:43], s58 offen sc1
s_waitcnt vmcnt(0)
v_readfirstlane_b32 s59, v4
s_nop 0
s_cmp_lg_u32 s59, 0
s_nop 2
s_cbranch_scc1 hp_end_%=
s_branch hpq_kxa1_poll_%=
hpq_kxa1_ok_%=:
v_add_u32 v227, 128, v227
s_or_b32 s57, s34, s49
s_waitcnt lgkmcnt(4)
v_mfma_f32_16x16x32_bf16 v[40:43], a[0:3], v[80:83], 0
s_or_b32 s52, s35, s49
buffer_load_dwordx4 v[164:167], v11, s[40:43], s57 offen sc1
v_mfma_f32_16x16x32_bf16 v[48:51], a[80:83], v[80:83], 0
s_or_b32 s26, s36, s49
buffer_load_dwordx4 v[168:171], v11, s[40:43], s57 offen offset:512 sc1
v_mfma_f32_16x16x32_bf16 v[56:59], a[160:163], v[80:83], 0
s_or_b32 s27, s37, s49
ds_read_b128 v[80:83], v1 offset:22528
s_waitcnt lgkmcnt(4)
v_mfma_f32_16x16x32_bf16 v[44:47], a[0:3], v[84:87], 0
s_add_u32 s38, s63, 0
buffer_load_dwordx4 v[172:175], v11, s[40:43], s57 offen offset:1024 sc1
v_mfma_f32_16x16x32_bf16 v[52:55], a[80:83], v[84:87], 0
s_and_b32 s38, s38, 63
buffer_load_dwordx4 v[176:179], v11, s[40:43], s57 offen offset:1536 sc1
v_mfma_f32_16x16x32_bf16 v[60:63], a[160:163], v[84:87], 0
s_cmp_eq_u32 s38, 31
ds_read_b128 v[84:87], v1 offset:3072
s_waitcnt lgkmcnt(4)
v_mfma_f32_16x16x32_bf16 v[40:43], a[4:7], v[88:91], v[40:43]
s_cselect_b32 s38, -31, 1
buffer_load_dwordx4 v[180:183], v11, s[40:43], s57 offen offset:2048 sc1
v_mfma_f32_16x16x32_bf16 v[48:51], a[84:87], v[88:91], v[48:51]
s_mul_i32 s39, s38, 98304
buffer_load_dwordx4 v[184:187], v11, s[40:43], s57 offen offset:2560 sc1
v_mfma_f32_16x16x32_bf16 v[56:59], a[164:167], v[88:91], v[56:59]
s_add_u32 s34, s34, s39
ds_read_b128 v[88:91], v1 offset:23552
)"
R"(s_waitcnt lgkmcnt(4)
v_mfma_f32_16x16x32_bf16 v[44:47], a[4:7], v[92:95], v[44:47]
s_mul_i32 s39, s38, 65536
buffer_load_dword v236, v227, s[40:43], s58 offen sc1
v_cvt_pk_bf16_f32 v104, v16, v17
v_mfma_f32_16x16x32_bf16 v[52:55], a[84:87], v[92:95], v[52:55]
s_add_u32 s36, s36, s39
s_waitcnt vmcnt(20)
v_swap_b32 v150, v152
v_mfma_f32_16x16x32_bf16 v[60:63], a[164:167], v[92:95], v[60:63]
s_mul_i32 s39, s38, 16384
v_cvt_pk_bf16_f32 v105, v18, v19
ds_read_b128 v[92:95], v1 offset:4096
s_waitcnt lgkmcnt(4)
v_mfma_f32_16x16x32_bf16 v[40:43], a[8:11], v[96:99], v[40:43]
s_add_u32 s37, s37, s39
v_swap_b32 v151, v153
v_mfma_f32_16x16x32_bf16 v[48:51], a[88:91], v[96:99], v[48:51]
s_add_u32 s35, s35, 1536
v_swap_b32 v158, v160
v_mfma_f32_16x16x32_bf16 v[56:59], a[168:171], v[96:99], v[56:59]
s_add_u32 s63, s63, 1
v_cvt_pk_bf16_f32 v106, v24, v25
ds_read_b128 v[96:99], v1 offset:24576
s_waitcnt lgkmcnt(4)
v_mfma_f32_16x16x32_bf16 v[44:47], a[8:11], v[80:83], v[44:47]
v_swap_b32 v159, v161
v_mfma_f32_16x16x32_bf16 v[52:55], a[88:91], v[80:83], v[52:55]
v_cvt_pk_bf16_f32 v107, v26, v27
v_mfma_f32_16x16x32_bf16 v[60:63], a[168:171], v[80:83], v[60:63]
ds_read_b128 v[80:83], v1 offset:5120
s_waitcnt lgkmcnt(4)
v_mfma_f32_16x16x32_bf16 v[40:43], a[12:15], v[84:87], v[40:43]
ds_write_b128 v6, v[104:107] offset:0
v_mfma_f32_16x16x32_bf16 v[64:67], v[116:119], v[188:191], v[212:215]
v_cvt_pk_bf16_f32 v108, v20, v21
v_cvt_pk_bf16_f32 v109, v22, v23
v_mfma_f32_16x16x32_bf16 v[48:51], a[92:95], v[84:87], v[48:51]
v_cvt_pk_bf16_f32 v110, v28, v29
v_cvt_pk_bf16_f32 v111, v30, v31
v_mfma_f32_16x16x32_bf16 v[68:71], v[120:123], v[188:191], v[216:219]
ds_write_b128 v6, v[108:111] offset:1024
v_mfma_f32_16x16x32_bf16 v[56:59], a[172:175], v[84:87], v[56:59]
v_cvt_pk_bf16_f32 v112, v32, v33
ds_read_b128 v[84:87], v1 offset:25600
s_waitcnt lgkmcnt(6)
v_mfma_f32_16x16x32_bf16 v[44:47], a[12:15], v[88:91], v[44:47]
v_cvt_pk_bf16_f32 v113, v34, v35
v_cvt_pk_bf16_f32 v114, v36, v37
v_mfma_f32_16x16x32_bf16 v[64:67], v[124:127], v[192:195], v[64:67]
v_cvt_pk_bf16_f32 v115, v38, v39
v_mfma_f32_16x16x32_bf16 v[52:55], a[92:95], v[88:91], v[52:55]
ds_write2st64_b64 v7, v[112:113], v[114:115] offset0:0 offset1:2
v_mfma_f32_16x16x32_bf16 v[68:71], v[128:131], v[192:195], v[68:71]
buffer_load_dword v224, v225, s[40:43], s58 offen sc1
v_mfma_f32_16x16x32_bf16 v[60:63], a[172:175], v[88:91], v[60:63]
ds_read_b128 v[88:91], v1 offset:6144
s_waitcnt lgkmcnt(7)
v_mfma_f32_16x16x32_bf16 v[40:43], a[16:19], v[92:95], v[40:43]
v_mfma_f32_16x16x32_bf16 v[64:67], v[132:135], v[196:199], v[64:67]
v_mfma_f32_16x16x32_bf16 v[48:51], a[96:99], v[92:95], v[48:51]
v_mfma_f32_16x16x32_bf16 v[68:71], v[136:139], v[196:199], v[68:71]
v_mfma_f32_16x16x32_bf16 v[56:59], a[176:179], v[92:95], v[56:59]
ds_read_b128 v[92:95], v1 offset:26624
s_waitcnt lgkmcnt(7)
v_mfma_f32_16x16x32_bf16 v[44:47], a[16:19], v[96:99], v[44:47]
v_mfma_f32_16x16x32_bf16 v[64:67], v[140:143], v[200:203], v[64:67]
v_mfma_f32_16x16x32_bf16 v[52:55], a[96:99], v[96:99], v[52:55]
v_mfma_f32_16x16x32_bf16 v[68:71], v[144:147], v[200:203], v[68:71]
v_mfma_f32_16x16x32_bf16 v[60:63], a[176:179], v[96:99], v[60:63]
ds_read_b128 v[96:99], v1 offset:7168
s_waitcnt lgkmcnt(7)
v_mfma_f32_16x16x32_bf16 v[40:43], a[20:23], v[80:83], v[40:43]
v_mfma_f32_16x16x32_bf16 v[64:67], v[148:151], v[204:207], v[64:67]
v_mfma_f32_16x16x32_bf16 v[48:51], a[100:103], v[80:83], v[48:51]
v_mfma_f32_16x16x32_bf16 v[68:71], v[152:155], v[204:207], v[68:71]
v_mfma_f32_16x16x32_bf16 v[56:59], a[180:183], v[80:83], v[56:59]
ds_read_b128 v[80:83], v1 offset:27648
s_waitcnt lgkmcnt(5)
v_mfma_f32_16x16x32_bf16 v[44:47], a[20:23], v[84:87], v[44:47]
v_mfma_f32_16x16x32_bf16 v[64:67], v[156:159], v[208:211], v[64:67]
v_mfma_f32_16x16x32_bf16 v[52:55], a[100:103], v[84:87], v[52:55]
v_mfma_f32_16x16x32_bf16 v[68:71], v[160:163], v[208:211], v[68:71]
v_mfma_f32_16x16x32_bf16 v[60:63], a[180:183], v[84:87], v[60:63]
ds_read_b128 v[84:87], v1 offset:8192
s_waitcnt lgkmcnt(4)
v_mfma_f32_16x16x32_bf16 v[40:43], a[24:27], v[88:91], v[40:43]
v_mfma_f32_16x16x32_bf16 v[48:51], a[104:107], v[88:91], v[48:51]
v_mfma_f32_16x16x32_bf16 v[56:59], a[184:187], v[88:91], v[56:59]
ds_read_b128 v[88:91], v1 offset:28672
s_waitcnt lgkmcnt(4)
v_mfma_f32_16x16x32_bf16 v[44:47], a[24:27], v[92:95], v[44:47]
buffer_load_dword v9, v10, s[40:43], s58 offen sc1
s_waitcnt vmcnt(8)
v_mfma_f32_16x16x32_bf16 v[72:75], v[164:167], v[188:191], v[220:223]
v_mfma_f32_16x16x32_bf16 v[52:55], a[104:107], v[92:95], v[52:55]
s_waitcnt vmcnt(7)
v_mfma_f32_16x16x32_bf16 v[72:75], v[168:171], v[192:195], v[72:75]
v_mfma_f32_16x16x32_bf16 v[60:63], a[184:187], v[92:95], v[60:63]
ds_read_b128 v[92:95], v1 offset:9216
s_waitcnt lgkmcnt(4)
v_mfma_f32_16x16x32_bf16 v[40:43], a[28:31], v[96:99], v[40:43]
buffer_load_dwordx4 v[116:119], v0, s[40:43], s54 offen sc1
s_waitcnt vmcnt(7)
v_mfma_f32_16x16x32_bf16 v[72:75], v[172:175], v[196:199], v[72:75]
v_mfma_f32_16x16x32_bf16 v[48:51], a[108:111], v[96:99], v[48:51]
s_waitcnt vmcnt(6)
v_mfma_f32_16x16x32_bf16 v[72:75], v[176:179], v[200:203], v[72:75]
v_mfma_f32_16x16x32_bf16 v[56:59], a[188:191], v[96:99], v[56:59]
ds_read_b128 v[96:99], v1 offset:29696
s_waitcnt lgkmcnt(4)
v_mfma_f32_16x16x32_bf16 v[44:47], a[28:31], v[80:83], v[44:47]
buffer_load_dwordx4 v[120:123], v0, s[40:43], s54 offen offset:1024 sc1
s_waitcnt vmcnt(6)
v_mfma_f32_16x16x32_bf16 v[72:75], v[180:183], v[204:207], v[72:75]
v_mfma_f32_16x16x32_bf16 v[52:55], a[108:111], v[80:83], v[52:55]
s_waitcnt vmcnt(5)
v_mfma_f32_16x16x32_bf16 v[72:75], v[184:187], v[208:211], v[72:75]
v_mfma_f32_16x16x32_bf16 v[60:63], a[188:191], v[80:83], v[60:63]
ds_read_b128 v[80:83], v1 offset:10240
s_waitcnt lgkmcnt(4)
v_mfma_f32_16x16x32_bf16 v[40:43], a[32:35], v[84:87], v[40:43]
buffer_load_dwordx4 v[124:127], v0, s[40:43], s54 offen offset:3072 sc1
v_mfma_f32_16x16x32_bf16 v[48:51], a[112:115], v[84:87], v[48:51]
v_mfma_f32_16x16x32_bf16 v[56:59], a[192:195], v[84:87], v[56:59]
ds_read_b128 v[84:87], v1 offset:30720
s_waitcnt lgkmcnt(4)
v_mfma_f32_16x16x32_bf16 v[44:47], a[32:35], v[88:91], v[44:47]
v_max3_f32 v76, v64, v65, v66
v_max3_f32 v76, v76, v67, v68
v_mfma_f32_16x16x32_bf16 v[52:55], a[112:115], v[88:91], v[52:55]
v_max3_f32 v76, v76, v69, v70
v_max3_f32 v76, v76, v71, v72
v_mfma_f32_16x16x32_bf16 v[60:63], a[192:195], v[88:91], v[60:63]
v_max3_f32 v76, v76, v73, v74
ds_read_b128 v[88:91], v1 offset:11264
s_waitcnt lgkmcnt(4)
v_mfma_f32_16x16x32_bf16 v[40:43], a[36:39], v[92:95], v[40:43]
buffer_load_dwordx4 v[128:131], v0, s[40:43], s55 offen sc1
v_max_f32 v76, v76, v75
v_mfma_f32_16x16x32_bf16 v[48:51], a[116:119], v[92:95], v[48:51]
v_mov_b32 v77, v76
v_mfma_f32_16x16x32_bf16 v[56:59], a[196:199], v[92:95], v[56:59]
ds_read_b128 v[92:95], v1 offset:31744
s_waitcnt lgkmcnt(4)
v_mfma_f32_16x16x32_bf16 v[44:47], a[36:39], v[96:99], v[44:47]
v_permlane16_swap_b32 v76, v77
v_mfma_f32_16x16x32_bf16 v[52:55], a[116:119], v[96:99], v[52:55]
v_max_f32 v76, v76, v77
v_mov_b32 v77, v76
v_mfma_f32_16x16x32_bf16 v[60:63], a[196:199], v[96:99], v[60:63]
buffer_load_dwordx4 v[132:135], v0, s[40:43], s55 offen offset:2048 sc1
ds_read_b128 v[96:99], v1 offset:12288
s_waitcnt lgkmcnt(4)
v_mfma_f32_16x16x32_bf16 v[40:43], a[40:43], v[80:83], v[40:43]
buffer_load_dwordx4 v[136:139], v0, s[40:43], s55 offen offset:3072 sc1
v_mfma_f32_16x16x32_bf16 v[48:51], a[120:123], v[80:83], v[48:51]
v_permlane32_swap_b32 v76, v77
v_mfma_f32_16x16x32_bf16 v[56:59], a[200:203], v[80:83], v[56:59]
v_max_f32 v76, v76, v77
ds_read_b128 v[80:83], v1 offset:32768
s_waitcnt lgkmcnt(4)
v_mfma_f32_16x16x32_bf16 v[44:47], a[40:43], v[84:87], v[44:47]
v_mul_f32 v76, 0xbdd53b95, v76
v_fmamk_f32 v64, v64, 0x3dd53b95, v76
v_mfma_f32_16x16x32_bf16 v[52:55], a[120:123], v[84:87], v[52:55]
v_fmamk_f32 v65, v65, 0x3dd53b95, v76
v_fmamk_f32 v66, v66, 0x3dd53b95, v76
v_mfma_f32_16x16x32_bf16 v[60:63], a[200:203], v[84:87], v[60:63]
v_fmamk_f32 v67, v67, 0x3dd53b95, v76
ds_read_b128 v[84:87], v1 offset:13312
s_waitcnt lgkmcnt(4)
v_mfma_f32_16x16x32_bf16 v[40:43], a[44:47], v[88:91], v[40:43]
buffer_load_dwordx4 v[140:143], v0, s[40:43], s56 offen offset:1024 sc1
v_fmamk_f32 v68, v68, 0x3dd53b95, v76
v_mfma_f32_16x16x32_bf16 v[48:51], a[124:127], v[88:91], v[48:51]
v_fmamk_f32 v69, v69, 0x3dd53b95, v76
v_fmamk_f32 v70, v70, 0x3dd53b95, v76
v_mfma_f32_16x16x32_bf16 v[56:59], a[204:207], v[88:91], v[56:59]
v_fmamk_f32 v71, v71, 0x3dd53b95, v76
ds_read_b128 v[88:91], v1 offset:33792
s_waitcnt lgkmcnt(4)
v_mfma_f32_16x16x32_bf16 v[44:47], a[44:47], v[92:95], v[44:47]
v_fmamk_f32 v72, v72, 0x3dd53b95, v76
v_fmamk_f32 v73, v73, 0x3dd53b95, v76
v_mfma_f32_16x16x32_bf16 v[52:55], a[124:127], v[92:95], v[52:55]
v_fmamk_f32 v74, v74, 0x3dd53b95, v76
v_fmamk_f32 v75, v75, 0x3dd53b95, v76
v_mfma_f32_16x16x32_bf16 v[60:63], a[204:207], v[92:95], v[60:63]
buffer_load_dwordx4 v[144:147], v0, s[40:43], s56 offen offset:2048 sc1
ds_read_b128 v[92:95], v1 offset:14336
s_waitcnt lgkmcnt(4)
v_mfma_f32_16x16x32_bf16 v[40:43], a[48:51], v[96:99], v[40:43]
buffer_load_dwordx4 v[148:151], v0, s[40:43], s54 offen offset:2048 sc1
v_mfma_f32_16x16x32_bf16 v[48:51], a[128:131], v[96:99], v[48:51]
v_exp_f32 v64, v64
v_mfma_f32_16x16x32_bf16 v[56:59], a[208:211], v[96:99], v[56:59]
ds_read_b128 v[96:99], v1 offset:34816
s_waitcnt lgkmcnt(4)
v_mfma_f32_16x16x32_bf16 v[44:47], a[48:51], v[80:83], v[44:47]
)"
R"(v_exp_f32 v65, v65
v_mfma_f32_16x16x32_bf16 v[52:55], a[128:131], v[80:83], v[52:55]
v_exp_f32 v66, v66
v_mfma_f32_16x16x32_bf16 v[60:63], a[208:211], v[80:83], v[60:63]
buffer_load_dwordx4 v[152:155], v0, s[40:43], s55 offen offset:1024 sc1
ds_read_b128 v[80:83], v1 offset:15360
s_waitcnt lgkmcnt(4)
v_mfma_f32_16x16x32_bf16 v[40:43], a[52:55], v[84:87], v[40:43]
buffer_load_dwordx4 v[156:159], v0, s[40:43], s56 offen sc1
v_mfma_f32_16x16x32_bf16 v[48:51], a[132:135], v[84:87], v[48:51]
v_exp_f32 v67, v67
v_mfma_f32_16x16x32_bf16 v[56:59], a[212:215], v[84:87], v[56:59]
s_add_u32 s38, s63, 0
ds_read_b128 v[84:87], v1 offset:35840
s_waitcnt lgkmcnt(4)
v_mfma_f32_16x16x32_bf16 v[44:47], a[52:55], v[88:91], v[44:47]
s_and_b32 s38, s38, 63
v_exp_f32 v68, v68
v_mfma_f32_16x16x32_bf16 v[52:55], a[132:135], v[88:91], v[52:55]
s_cmp_eq_u32 s38, 31
v_exp_f32 v69, v69
v_mfma_f32_16x16x32_bf16 v[60:63], a[212:215], v[88:91], v[60:63]
s_cselect_b32 s38, -31, 1
buffer_load_dwordx4 v[160:163], v0, s[40:43], s56 offen offset:3072 sc1
ds_read_b128 v[88:91], v1 offset:16384
s_waitcnt lgkmcnt(4)
v_mfma_f32_16x16x32_bf16 v[40:43], a[56:59], v[92:95], v[40:43]
s_mul_i32 s39, s38, 393216
v_exp_f32 v70, v70
v_mfma_f32_16x16x32_bf16 v[48:51], a[136:139], v[92:95], v[48:51]
s_add_u32 s54, s54, s39
v_exp_f32 v71, v71
v_mfma_f32_16x16x32_bf16 v[56:59], a[216:219], v[92:95], v[56:59]
s_add_u32 s55, s55, s39
ds_read_b128 v[92:95], v1 offset:36864
s_waitcnt lgkmcnt(4)
v_mfma_f32_16x16x32_bf16 v[44:47], a[56:59], v[96:99], v[44:47]
s_add_u32 s56, s56, s39
v_exp_f32 v72, v72
v_mfma_f32_16x16x32_bf16 v[52:55], a[136:139], v[96:99], v[52:55]
v_exp_f32 v73, v73
v_mfma_f32_16x16x32_bf16 v[60:63], a[216:219], v[96:99], v[60:63]
s_waitcnt vmcnt(13) lgkmcnt(4)
s_barrier
buffer_atomic_add v226, v3, s[40:43], s29 offen
s_and_b32 s48, s47, 63
s_cmp_lt_u32 s48, 62
s_cselect_b32 s48, 2, 0
v_cmp_gt_u32 vcc, s48, v224
s_cbranch_vccz hpq_xf1_ok_%=
s_mov_b32 s60, 0
s_nop 2
hpq_xf1_poll_%=:
buffer_load_dword v224, v225, s[40:43], s58 offen sc1
s_waitcnt vmcnt(0)
v_cmp_gt_u32 vcc, s48, v224
s_nop 4
s_cbranch_vccz hpq_xf1_ok_%=
s_sleep 2
s_add_u32 s60, s60, 1
s_cmp_gt_u32 s60, 65536
s_cbranch_scc1 hp_abort_%=
s_and_b32 s59, s60, 63
s_cbranch_scc1 hpq_xf1_poll_%=
v_mov_b32 v4, 1024
buffer_load_dword v4, v4, s[40:43], s58 offen sc1
s_waitcnt vmcnt(0)
v_readfirstlane_b32 s59, v4
s_nop 0
s_cmp_lg_u32 s59, 0
s_nop 2
s_cbranch_scc1 hp_end_%=
s_branch hpq_xf1_poll_%=
hpq_xf1_ok_%=:
v_add_u32 v225, 128, v225
ds_read_b128 v[96:99], v1 offset:17408
s_waitcnt lgkmcnt(4)
v_mfma_f32_16x16x32_bf16 v[40:43], a[60:63], v[80:83], v[40:43]
s_add_u32 m0, s61, 0
s_add_u32 s51, s50, 0
buffer_load_dwordx4 v0, s[40:43], s51 offen offset:0 sc1 lds
v_mfma_f32_16x16x32_bf16 v[48:51], a[140:143], v[80:83], v[48:51]
v_exp_f32 v74, v74
v_mfma_f32_16x16x32_bf16 v[56:59], a[220:223], v[80:83], v[56:59]
ds_read_b128 v[188:191], v8 offset:0
ds_read_b128 v[80:83], v1 offset:37888
s_waitcnt lgkmcnt(5)
v_mfma_f32_16x16x32_bf16 v[44:47], a[60:63], v[84:87], v[44:47]
buffer_load_dwordx4 v0, s[40:43], s51 offen offset:1024 sc1 lds
v_mfma_f32_16x16x32_bf16 v[52:55], a[140:143], v[84:87], v[52:55]
v_exp_f32 v75, v75
v_mfma_f32_16x16x32_bf16 v[60:63], a[220:223], v[84:87], v[60:63]
v_add_f32 v76, v64, v65
ds_read_b128 v[84:87], v1 offset:18432
s_waitcnt lgkmcnt(5)
v_mfma_f32_16x16x32_bf16 v[40:43], a[64:67], v[88:91], v[40:43]
buffer_load_dwordx4 v0, s[40:43], s51 offen offset:2048 sc1 lds
v_mfma_f32_16x16x32_bf16 v[48:51], a[144:147], v[88:91], v[48:51]
v_add_f32 v76, v76, v66
v_add_f32 v76, v76, v67
v_mfma_f32_16x16x32_bf16 v[56:59], a[224:227], v[88:91], v[56:59]
v_add_f32 v76, v76, v68
ds_read_b128 v[88:91], v1 offset:38912
s_waitcnt lgkmcnt(5)
v_mfma_f32_16x16x32_bf16 v[44:47], a[64:67], v[92:95], v[44:47]
buffer_load_dwordx4 v0, s[40:43], s51 offen offset:3072 sc1 lds
v_mfma_f32_16x16x32_bf16 v[52:55], a[144:147], v[92:95], v[52:55]
v_add_f32 v76, v76, v69
v_add_f32 v76, v76, v70
v_mfma_f32_16x16x32_bf16 v[60:63], a[224:227], v[92:95], v[60:63]
v_add_f32 v76, v76, v71
ds_read_b128 v[92:95], v1 offset:19456
s_waitcnt lgkmcnt(5)
v_mfma_f32_16x16x32_bf16 v[40:43], a[68:71], v[96:99], v[40:43]
s_add_u32 m0, s61, 4096
s_add_u32 s51, s50, 4096
buffer_load_dwordx4 v0, s[40:43], s51 offen offset:0 sc1 lds
v_mfma_f32_16x16x32_bf16 v[48:51], a[148:151], v[96:99], v[48:51]
v_add_f32 v76, v76, v72
v_add_f32 v76, v76, v73
v_mfma_f32_16x16x32_bf16 v[56:59], a[228:231], v[96:99], v[56:59]
v_add_f32 v76, v76, v74
ds_read_b128 v[192:195], v8 offset:2048
ds_read_b128 v[196:199], v8 offset:4096
ds_read_b128 v[200:203], v8 offset:6144
ds_read_b128 v[204:207], v8 offset:8192
ds_read_b128 v[208:211], v8 offset:10240
ds_read_b128 v[96:99], v1 offset:39936
s_waitcnt lgkmcnt(9)
v_mfma_f32_16x16x32_bf16 v[44:47], a[68:71], v[80:83], v[44:47]
buffer_load_dwordx4 v0, s[40:43], s51 offen offset:1024 sc1 lds
v_mfma_f32_16x16x32_bf16 v[52:55], a[148:151], v[80:83], v[52:55]
v_add_f32 v76, v76, v75
v_mov_b32 v77, v76
v_mfma_f32_16x16x32_bf16 v[60:63], a[228:231], v[80:83], v[60:63]
s_add_u32 s64, s64, 40960
s_cmp_ge_u32 s64, 122880
s_cselect_b32 s64, 0, s64
v_add_u32 v1, s64, v0
ds_read_b128 v[80:83], v1 offset:0
s_waitcnt lgkmcnt(9)
v_mfma_f32_16x16x32_bf16 v[40:43], a[72:75], v[84:87], v[40:43]
buffer_load_dwordx4 v0, s[40:43], s51 offen offset:2048 sc1 lds
v_mfma_f32_16x16x32_bf16 v[48:51], a[152:155], v[84:87], v[48:51]
v_permlane16_swap_b32 v76, v77
v_mfma_f32_16x16x32_bf16 v[56:59], a[232:235], v[84:87], v[56:59]
v_add_f32 v76, v76, v77
ds_read_b128 v[84:87], v1 offset:20480
s_waitcnt lgkmcnt(9)
v_mfma_f32_16x16x32_bf16 v[44:47], a[72:75], v[88:91], v[44:47]
buffer_load_dwordx4 v0, s[40:43], s51 offen offset:3072 sc1 lds
v_mfma_f32_16x16x32_bf16 v[52:55], a[152:155], v[88:91], v[52:55]
v_mov_b32 v77, v76
v_mfma_f32_16x16x32_bf16 v[60:63], a[232:235], v[88:91], v[60:63]
ds_read_b128 v[88:91], v1 offset:1024
s_waitcnt lgkmcnt(9)
v_mfma_f32_16x16x32_bf16 v[40:43], a[76:79], v[92:95], v[40:43]
s_add_u32 m0, s61, 8192
s_add_u32 s51, s50, 8192
buffer_load_dwordx4 v0, s[40:43], s51 offen offset:0 sc1 lds
v_mfma_f32_16x16x32_bf16 v[48:51], a[156:159], v[92:95], v[48:51]
v_permlane32_swap_b32 v76, v77
v_mfma_f32_16x16x32_bf16 v[56:59], a[236:239], v[92:95], v[56:59]
v_add_f32 v76, v76, v77
ds_read_b128 v[92:95], v1 offset:21504
s_waitcnt lgkmcnt(4)
v_mfma_f32_16x16x32_bf16 v[44:47], a[76:79], v[96:99], v[44:47]
buffer_load_dwordx4 v0, s[40:43], s51 offen offset:1024 sc1 lds
v_mfma_f32_16x16x32_bf16 v[52:55], a[156:159], v[96:99], v[52:55]
s_add_u32 s50, s50, 40960
s_add_u32 s61, s61, 40960
s_cmp_ge_u32 s61, 122880
s_cselect_b32 s51, 122880, 0
s_sub_u32 s61, s61, s51
v_rcp_f32 v76, v76
v_mfma_f32_16x16x32_bf16 v[60:63], a[236:239], v[96:99], v[60:63]
v_mul_f32 v64, v64, v76
ds_read_b128 v[96:99], v1 offset:2048
v_mul_f32 v65, v65, v76
v_mul_f32 v66, v66, v76
v_mul_f32 v67, v67, v76
v_mul_f32 v68, v68, v76
v_mul_f32 v69, v69, v76
v_mul_f32 v70, v70, v76
v_mul_f32 v71, v71, v76
v_mul_f32 v72, v72, v76
v_mul_f32 v73, v73, v76
v_mul_f32 v74, v74, v76
v_mul_f32 v75, v75, v76
v_cvt_pk_bf16_f32 v228, v64, v65
v_cvt_pk_bf16_f32 v229, v66, v67
v_cvt_pk_bf16_f32 v230, v68, v69
v_cvt_pk_bf16_f32 v231, v70, v71
v_cvt_pk_bf16_f32 v232, v72, v73
v_cvt_pk_bf16_f32 v233, v74, v75
v_mov_b32 v234, v232
v_mov_b32 v235, v233
buffer_store_dwordx4 v[228:231], v0, s[40:43], s26 offen sc1
v_permlane16_swap_b32 v232, v234
v_permlane16_swap_b32 v233, v235
)"
R"(buffer_store_dwordx4 v[232:235], v12, s[40:43], s27 offen sc1
s_add_u32 s47, s47, 1
s_and_b32 s59, s47, 63
s_cbranch_scc1 hpq_loop_%=
s_waitcnt lgkmcnt(0)
v_cvt_pk_bf16_f32 v104, v40, v41
v_cvt_pk_bf16_f32 v105, v42, v43
v_cvt_pk_bf16_f32 v106, v48, v49
v_cvt_pk_bf16_f32 v107, v50, v51
ds_write_b128 v6, v[104:107] offset:12288
v_cvt_pk_bf16_f32 v108, v44, v45
v_cvt_pk_bf16_f32 v109, v46, v47
v_cvt_pk_bf16_f32 v110, v52, v53
v_cvt_pk_bf16_f32 v111, v54, v55
ds_write_b128 v6, v[108:111] offset:13312
v_cvt_pk_bf16_f32 v112, v56, v57
v_cvt_pk_bf16_f32 v113, v58, v59
v_cvt_pk_bf16_f32 v114, v60, v61
v_cvt_pk_bf16_f32 v115, v62, v63
ds_write2st64_b64 v7, v[112:113], v[114:115] offset0:24 offset1:26
s_waitcnt lgkmcnt(0)
s_barrier
s_waitcnt vmcnt(13)
s_mov_b32 s29, s52
s_waitcnt vmcnt(25)
s_add_u32 s48, s63, 1
s_cmp_ge_i32 s63, s65
s_cselect_b32 s48, s48, 0
s_cselect_b32 s49, s30, 0x40000000
v_cmp_gt_u32 vcc, s48, v9
s_cbranch_vccz hpq_kme0_ok_%=
s_waitcnt vmcnt(0)
buffer_atomic_add v226, v3, s[40:43], s29 offen
s_mov_b32 s29, 0x40000000
v_cmp_gt_u32 vcc, s48, v9
s_cbranch_vccz hpq_kme0_ok_%=
s_mov_b32 s60, 0
s_nop 2
hpq_kme0_poll_%=:
buffer_load_dword v9, v10, s[40:43], s58 offen sc1
s_waitcnt vmcnt(0)
v_cmp_gt_u32 vcc, s48, v9
s_nop 4
s_cbranch_vccz hpq_kme0_ok_%=
s_sleep 2
s_add_u32 s60, s60, 1
s_cmp_gt_u32 s60, 65536
s_cbranch_scc1 hp_abort_%=
s_and_b32 s59, s60, 63
s_cbranch_scc1 hpq_kme0_poll_%=
v_mov_b32 v4, 1024
buffer_load_dword v4, v4, s[40:43], s58 offen sc1
s_waitcnt vmcnt(0)
v_readfirstlane_b32 s59, v4
s_nop 0
s_cmp_lg_u32 s59, 0
s_nop 2
s_cbranch_scc1 hp_end_%=
s_branch hpq_kme0_poll_%=
hpq_kme0_ok_%=:
s_movk_i32 s48, 32
v_cmp_gt_u32 vcc, s48, v236
s_cbranch_vccz hpq_kxe0_ok_%=
s_mov_b32 s60, 0
s_nop 2
hpq_kxe0_poll_%=:
buffer_load_dword v236, v227, s[40:43], s58 offen sc1
s_waitcnt vmcnt(0)
v_cmp_gt_u32 vcc, s48, v236
s_nop 4
s_cbranch_vccz hpq_kxe0_ok_%=
s_sleep 24
s_add_u32 s60, s60, 1
s_cmp_gt_u32 s60, 65536
s_cbranch_scc1 hp_abort_%=
s_and_b32 s59, s60, 63
s_cbranch_scc1 hpq_kxe0_poll_%=
v_mov_b32 v4, 1024
buffer_load_dword v4, v4, s[40:43], s58 offen sc1
s_waitcnt vmcnt(0)
v_readfirstlane_b32 s59, v4
s_nop 0
s_cmp_lg_u32 s59, 0
s_nop 2
s_cbranch_scc1 hp_end_%=
s_branch hpq_kxe0_poll_%=
hpq_kxe0_ok_%=:
v_add_u32 v227, 128, v227
s_or_b32 s57, s34, s49
buffer_load_dwordx4 v[164:167], v11, s[40:43], s57 offen sc1
buffer_load_dwordx4 v[168:171], v11, s[40:43], s57 offen offset:512 sc1
buffer_load_dwordx4 v[172:175], v11, s[40:43], s57 offen offset:1024 sc1
buffer_load_dwordx4 v[176:179], v11, s[40:43], s57 offen offset:1536 sc1
buffer_load_dwordx4 v[180:183], v11, s[40:43], s57 offen offset:2048 sc1
buffer_load_dwordx4 v[184:187], v11, s[40:43], s57 offen offset:2560 sc1
s_or_b32 s52, s35, s49
s_or_b32 s26, s36, s49
s_or_b32 s27, s37, s49
s_add_u32 s38, s63, 0
s_and_b32 s38, s38, 63
s_cmp_eq_u32 s38, 31
s_cselect_b32 s38, -31, 1
s_mul_i32 s39, s38, 98304
s_add_u32 s34, s34, s39
s_mul_i32 s39, s38, 65536
s_add_u32 s36, s36, s39
s_mul_i32 s39, s38, 16384
s_add_u32 s37, s37, s39
s_add_u32 s35, s35, 1536
s_add_u32 s63, s63, 1
v_swap_b32 v150, v152
v_swap_b32 v151, v153
v_swap_b32 v158, v160
v_swap_b32 v159, v161
v_mfma_f32_16x16x32_bf16 v[64:67], v[116:119], v[188:191], v[212:215]
v_mfma_f32_16x16x32_bf16 v[68:71], v[120:123], v[188:191], v[216:219]
v_mfma_f32_16x16x32_bf16 v[64:67], v[124:127], v[192:195], v[64:67]
v_mfma_f32_16x16x32_bf16 v[68:71], v[128:131], v[192:195], v[68:71]
v_mfma_f32_16x16x32_bf16 v[64:67], v[132:135], v[196:199], v[64:67]
v_mfma_f32_16x16x32_bf16 v[68:71], v[136:139], v[196:199], v[68:71]
v_mfma_f32_16x16x32_bf16 v[64:67], v[140:143], v[200:203], v[64:67]
v_mfma_f32_16x16x32_bf16 v[68:71], v[144:147], v[200:203], v[68:71]
v_mfma_f32_16x16x32_bf16 v[64:67], v[148:151], v[204:207], v[64:67]
v_mfma_f32_16x16x32_bf16 v[68:71], v[152:155], v[204:207], v[68:71]
v_mfma_f32_16x16x32_bf16 v[64:67], v[156:159], v[208:211], v[64:67]
v_mfma_f32_16x16x32_bf16 v[68:71], v[160:163], v[208:211], v[68:71]
s_waitcnt vmcnt(5)
v_mfma_f32_16x16x32_bf16 v[72:75], v[164:167], v[188:191], v[220:223]
s_waitcnt vmcnt(4)
v_mfma_f32_16x16x32_bf16 v[72:75], v[168:171], v[192:195], v[72:75]
s_waitcnt vmcnt(3)
v_mfma_f32_16x16x32_bf16 v[72:75], v[172:175], v[196:199], v[72:75]
s_waitcnt vmcnt(2)
v_mfma_f32_16x16x32_bf16 v[72:75], v[176:179], v[200:203], v[72:75]
s_waitcnt vmcnt(1)
v_mfma_f32_16x16x32_bf16 v[72:75], v[180:183], v[204:207], v[72:75]
s_waitcnt vmcnt(0)
v_mfma_f32_16x16x32_bf16 v[72:75], v[184:187], v[208:211], v[72:75]
buffer_load_dwordx4 v[116:119], v0, s[40:43], s54 offen sc1
buffer_load_dwordx4 v[120:123], v0, s[40:43], s54 offen offset:1024 sc1
buffer_load_dwordx4 v[124:127], v0, s[40:43], s54 offen offset:3072 sc1
buffer_load_dwordx4 v[128:131], v0, s[40:43], s55 offen sc1
buffer_load_dwordx4 v[132:135], v0, s[40:43], s55 offen offset:2048 sc1
buffer_load_dwordx4 v[136:139], v0, s[40:43], s55 offen offset:3072 sc1
buffer_load_dwordx4 v[140:143], v0, s[40:43], s56 offen offset:1024 sc1
buffer_load_dwordx4 v[144:147], v0, s[40:43], s56 offen offset:2048 sc1
buffer_load_dwordx4 v[148:151], v0, s[40:43], s54 offen offset:2048 sc1
buffer_load_dwordx4 v[152:155], v0, s[40:43], s55 offen offset:1024 sc1
buffer_load_dwordx4 v[156:159], v0, s[40:43], s56 offen sc1
buffer_load_dwordx4 v[160:163], v0, s[40:43], s56 offen offset:3072 sc1
s_add_u32 s38, s63, 0
s_and_b32 s38, s38, 63
s_cmp_eq_u32 s38, 31
s_cselect_b32 s38, -31, 1
s_mul_i32 s39, s38, 393216
s_add_u32 s54, s54, s39
s_add_u32 s55, s55, s39
s_add_u32 s56, s56, s39
v_max3_f32 v76, v64, v65, v66
v_max3_f32 v76, v76, v67, v68
v_max3_f32 v76, v76, v69, v70
v_max3_f32 v76, v76, v71, v72
v_max3_f32 v76, v76, v73, v74
v_max_f32 v76, v76, v75
v_mov_b32 v77, v76
s_nop 1
v_permlane16_swap_b32 v76, v77
v_max_f32 v76, v76, v77
v_mov_b32 v77, v76
s_nop 1
v_permlane32_swap_b32 v76, v77
v_max_f32 v76, v76, v77
v_mul_f32 v76, 0xbdd53b95, v76
v_fmamk_f32 v64, v64, 0x3dd53b95, v76
v_fmamk_f32 v65, v65, 0x3dd53b95, v76
v_fmamk_f32 v66, v66, 0x3dd53b95, v76
v_fmamk_f32 v67, v67, 0x3dd53b95, v76
v_fmamk_f32 v68, v68, 0x3dd53b95, v76
v_fmamk_f32 v69, v69, 0x3dd53b95, v76
v_fmamk_f32 v70, v70, 0x3dd53b95, v76
v_fmamk_f32 v71, v71, 0x3dd53b95, v76
v_fmamk_f32 v72, v72, 0x3dd53b95, v76
v_fmamk_f32 v73, v73, 0x3dd53b95, v76
v_fmamk_f32 v74, v74, 0x3dd53b95, v76
v_fmamk_f32 v75, v75, 0x3dd53b95, v76
v_exp_f32 v64, v64
v_exp_f32 v65, v65
v_exp_f32 v66, v66
v_exp_f32 v67, v67
v_exp_f32 v68, v68
v_exp_f32 v69, v69
v_exp_f32 v70, v70
v_exp_f32 v71, v71
v_exp_f32 v72, v72
v_exp_f32 v73, v73
v_exp_f32 v74, v74
v_exp_f32 v75, v75
v_add_f32 v76, v64, v65
v_add_f32 v76, v76, v66
v_add_f32 v76, v76, v67
)"
R"(v_add_f32 v76, v76, v68
v_add_f32 v76, v76, v69
v_add_f32 v76, v76, v70
v_add_f32 v76, v76, v71
v_add_f32 v76, v76, v72
v_add_f32 v76, v76, v73
v_add_f32 v76, v76, v74
v_add_f32 v76, v76, v75
v_mov_b32 v77, v76
s_nop 1
v_permlane16_swap_b32 v76, v77
v_add_f32 v76, v76, v77
v_mov_b32 v77, v76
s_nop 1
v_permlane32_swap_b32 v76, v77
v_add_f32 v76, v76, v77
v_rcp_f32 v76, v76
s_nop 0
v_mul_f32 v64, v64, v76
v_mul_f32 v65, v65, v76
v_mul_f32 v66, v66, v76
v_mul_f32 v67, v67, v76
v_mul_f32 v68, v68, v76
v_mul_f32 v69, v69, v76
v_mul_f32 v70, v70, v76
v_mul_f32 v71, v71, v76
v_mul_f32 v72, v72, v76
v_mul_f32 v73, v73, v76
v_mul_f32 v74, v74, v76
v_mul_f32 v75, v75, v76
v_cvt_pk_bf16_f32 v228, v64, v65
v_cvt_pk_bf16_f32 v229, v66, v67
v_cvt_pk_bf16_f32 v230, v68, v69
v_cvt_pk_bf16_f32 v231, v70, v71
v_cvt_pk_bf16_f32 v232, v72, v73
v_cvt_pk_bf16_f32 v233, v74, v75
v_mov_b32 v234, v232
v_mov_b32 v235, v233
buffer_store_dwordx4 v[228:231], v0, s[40:43], s26 offen sc1
v_permlane16_swap_b32 v232, v234
v_permlane16_swap_b32 v233, v235
buffer_store_dwordx4 v[232:235], v12, s[40:43], s27 offen sc1
s_waitcnt vmcnt(2)
buffer_atomic_add v226, v3, s[40:43], s29 offen
s_mov_b32 s29, s52
s_waitcnt vmcnt(25)
s_add_u32 s48, s63, 1
s_cmp_ge_i32 s63, s65
s_cselect_b32 s48, s48, 0
s_cselect_b32 s49, s30, 0x40000000
v_cmp_gt_u32 vcc, s48, v9
s_cbranch_vccz hpq_kme1_ok_%=
s_waitcnt vmcnt(0)
buffer_atomic_add v226, v3, s[40:43], s29 offen
s_mov_b32 s29, 0x40000000
v_cmp_gt_u32 vcc, s48, v9
s_cbranch_vccz hpq_kme1_ok_%=
s_mov_b32 s60, 0
s_nop 2
hpq_kme1_poll_%=:
buffer_load_dword v9, v10, s[40:43], s58 offen sc1
s_waitcnt vmcnt(0)
v_cmp_gt_u32 vcc, s48, v9
s_nop 4
s_cbranch_vccz hpq_kme1_ok_%=
s_sleep 2
s_add_u32 s60, s60, 1
s_cmp_gt_u32 s60, 65536
s_cbranch_scc1 hp_abort_%=
s_and_b32 s59, s60, 63
s_cbranch_scc1 hpq_kme1_poll_%=
v_mov_b32 v4, 1024
buffer_load_dword v4, v4, s[40:43], s58 offen sc1
s_waitcnt vmcnt(0)
v_readfirstlane_b32 s59, v4
s_nop 0
s_cmp_lg_u32 s59, 0
s_nop 2
s_cbranch_scc1 hp_end_%=
s_branch hpq_kme1_poll_%=
hpq_kme1_ok_%=:
s_or_b32 s57, s34, s49
buffer_load_dwordx4 v[164:167], v11, s[40:43], s57 offen sc1
buffer_load_dwordx4 v[168:171], v11, s[40:43], s57 offen offset:512 sc1
buffer_load_dwordx4 v[172:175], v11, s[40:43], s57 offen offset:1024 sc1
buffer_load_dwordx4 v[176:179], v11, s[40:43], s57 offen offset:1536 sc1
buffer_load_dwordx4 v[180:183], v11, s[40:43], s57 offen offset:2048 sc1
buffer_load_dwordx4 v[184:187], v11, s[40:43], s57 offen offset:2560 sc1
s_or_b32 s52, s35, s49
s_or_b32 s26, s36, s49
s_or_b32 s27, s37, s49
s_add_u32 s38, s63, 0
s_and_b32 s38, s38, 63
s_cmp_eq_u32 s38, 31
s_cselect_b32 s38, -31, 1
s_mul_i32 s39, s38, 98304
s_add_u32 s34, s34, s39
s_mul_i32 s39, s38, 65536
s_add_u32 s36, s36, s39
s_mul_i32 s39, s38, 16384
s_add_u32 s37, s37, s39
s_add_u32 s35, s35, 1536
s_add_u32 s63, s63, 1
ds_read_b128 v[188:191], v8 offset:12288
ds_read_b128 v[192:195], v8 offset:14336
ds_read_b128 v[196:199], v8 offset:16384
ds_read_b128 v[200:203], v8 offset:18432
ds_read_b128 v[204:207], v8 offset:20480
ds_read_b128 v[208:211], v8 offset:22528
v_swap_b32 v150, v152
v_swap_b32 v151, v153
v_swap_b32 v158, v160
v_swap_b32 v159, v161
s_waitcnt lgkmcnt(5)
v_mfma_f32_16x16x32_bf16 v[64:67], v[116:119], v[188:191], v[212:215]
v_mfma_f32_16x16x32_bf16 v[68:71], v[120:123], v[188:191], v[216:219]
s_waitcnt lgkmcnt(4)
v_mfma_f32_16x16x32_bf16 v[64:67], v[124:127], v[192:195], v[64:67]
v_mfma_f32_16x16x32_bf16 v[68:71], v[128:131], v[192:195], v[68:71]
s_waitcnt lgkmcnt(3)
v_mfma_f32_16x16x32_bf16 v[64:67], v[132:135], v[196:199], v[64:67]
v_mfma_f32_16x16x32_bf16 v[68:71], v[136:139], v[196:199], v[68:71]
s_waitcnt lgkmcnt(2)
v_mfma_f32_16x16x32_bf16 v[64:67], v[140:143], v[200:203], v[64:67]
v_mfma_f32_16x16x32_bf16 v[68:71], v[144:147], v[200:203], v[68:71]
s_waitcnt lgkmcnt(1)
v_mfma_f32_16x16x32_bf16 v[64:67], v[148:151], v[204:207], v[64:67]
v_mfma_f32_16x16x32_bf16 v[68:71], v[152:155], v[204:207], v[68:71]
s_waitcnt lgkmcnt(0)
v_mfma_f32_16x16x32_bf16 v[64:67], v[156:159], v[208:211], v[64:67]
v_mfma_f32_16x16x32_bf16 v[68:71], v[160:163], v[208:211], v[68:71]
s_waitcnt vmcnt(5)
v_mfma_f32_16x16x32_bf16 v[72:75], v[164:167], v[188:191], v[220:223]
s_waitcnt vmcnt(4)
v_mfma_f32_16x16x32_bf16 v[72:75], v[168:171], v[192:195], v[72:75]
s_waitcnt vmcnt(3)
v_mfma_f32_16x16x32_bf16 v[72:75], v[172:175], v[196:199], v[72:75]
s_waitcnt vmcnt(2)
v_mfma_f32_16x16x32_bf16 v[72:75], v[176:179], v[200:203], v[72:75]
s_waitcnt vmcnt(1)
v_mfma_f32_16x16x32_bf16 v[72:75], v[180:183], v[204:207], v[72:75]
s_waitcnt vmcnt(0)
v_mfma_f32_16x16x32_bf16 v[72:75], v[184:187], v[208:211], v[72:75]
v_max3_f32 v76, v64, v65, v66
v_max3_f32 v76, v76, v67, v68
v_max3_f32 v76, v76, v69, v70
s_nop 4
v_max3_f32 v76, v76, v71, v72
v_max3_f32 v76, v76, v73, v74
v_max_f32 v76, v76, v75
v_mov_b32 v77, v76
s_nop 1
v_permlane16_swap_b32 v76, v77
v_max_f32 v76, v76, v77
v_mov_b32 v77, v76
s_nop 1
v_permlane32_swap_b32 v76, v77
v_max_f32 v76, v76, v77
v_mul_f32 v76, 0xbdd53b95, v76
v_fmamk_f32 v64, v64, 0x3dd53b95, v76
v_fmamk_f32 v65, v65, 0x3dd53b95, v76
v_fmamk_f32 v66, v66, 0x3dd53b95, v76
v_fmamk_f32 v67, v67, 0x3dd53b95, v76
v_fmamk_f32 v68, v68, 0x3dd53b95, v76
v_fmamk_f32 v69, v69, 0x3dd53b95, v76
v_fmamk_f32 v70, v70, 0x3dd53b95, v76
v_fmamk_f32 v71, v71, 0x3dd53b95, v76
v_fmamk_f32 v72, v72, 0x3dd53b95, v76
v_fmamk_f32 v73, v73, 0x3dd53b95, v76
v_fmamk_f32 v74, v74, 0x3dd53b95, v76
v_fmamk_f32 v75, v75, 0x3dd53b95, v76
v_exp_f32 v64, v64
v_exp_f32 v65, v65
v_exp_f32 v66, v66
v_exp_f32 v67, v67
v_exp_f32 v68, v68
v_exp_f32 v69, v69
v_exp_f32 v70, v70
v_exp_f32 v71, v71
v_exp_f32 v72, v72
v_exp_f32 v73, v73
v_exp_f32 v74, v74
v_exp_f32 v75, v75
v_add_f32 v76, v64, v65
v_add_f32 v76, v76, v66
v_add_f32 v76, v76, v67
v_add_f32 v76, v76, v68
v_add_f32 v76, v76, v69
v_add_f32 v76, v76, v70
v_add_f32 v76, v76, v71
v_add_f32 v76, v76, v72
v_add_f32 v76, v76, v73
v_add_f32 v76, v76, v74
v_add_f32 v76, v76, v75
v_mov_b32 v77, v76
s_nop 1
v_permlane16_swap_b32 v76, v77
v_add_f32 v76, v76, v77
v_mov_b32 v77, v76
s_nop 1
)"
R"(v_permlane32_swap_b32 v76, v77
v_add_f32 v76, v76, v77
v_rcp_f32 v76, v76
s_nop 0
v_mul_f32 v64, v64, v76
v_mul_f32 v65, v65, v76
v_mul_f32 v66, v66, v76
v_mul_f32 v67, v67, v76
v_mul_f32 v68, v68, v76
v_mul_f32 v69, v69, v76
v_mul_f32 v70, v70, v76
v_mul_f32 v71, v71, v76
v_mul_f32 v72, v72, v76
v_mul_f32 v73, v73, v76
v_mul_f32 v74, v74, v76
v_mul_f32 v75, v75, v76
v_cvt_pk_bf16_f32 v228, v64, v65
v_cvt_pk_bf16_f32 v229, v66, v67
v_cvt_pk_bf16_f32 v230, v68, v69
v_cvt_pk_bf16_f32 v231, v70, v71
v_cvt_pk_bf16_f32 v232, v72, v73
v_cvt_pk_bf16_f32 v233, v74, v75
v_mov_b32 v234, v232
v_mov_b32 v235, v233
buffer_store_dwordx4 v[228:231], v0, s[40:43], s26 offen sc1
v_permlane16_swap_b32 v232, v234
v_permlane16_swap_b32 v233, v235
buffer_store_dwordx4 v[232:235], v12, s[40:43], s27 offen sc1
s_waitcnt vmcnt(2)
buffer_atomic_add v226, v3, s[40:43], s29 offen
s_waitcnt vmcnt(0)
buffer_atomic_add v226, v3, s[40:43], s52 offen
s_nop 1
s_branch hp_ret_%=
hpq_light_%=:
ds_read_b128 v[80:83], v1 offset:0
ds_read_b128 v[84:87], v1 offset:20480
ds_read_b128 v[88:91], v1 offset:1024
ds_read_b128 v[92:95], v1 offset:21504
ds_read_b128 v[96:99], v1 offset:2048
hpl_loop_%=:
s_waitcnt lgkmcnt(4)
v_mfma_f32_16x16x32_bf16 v[16:19], a[0:3], v[80:83], 0
v_mfma_f32_16x16x32_bf16 v[24:27], a[80:83], v[80:83], 0
v_mfma_f32_16x16x32_bf16 v[32:35], a[160:163], v[80:83], 0
ds_read_b128 v[80:83], v1 offset:22528
s_waitcnt lgkmcnt(4)
v_mfma_f32_16x16x32_bf16 v[20:23], a[0:3], v[84:87], 0
v_mfma_f32_16x16x32_bf16 v[28:31], a[80:83], v[84:87], 0
v_mfma_f32_16x16x32_bf16 v[36:39], a[160:163], v[84:87], 0
ds_read_b128 v[84:87], v1 offset:3072
s_waitcnt lgkmcnt(4)
v_mfma_f32_16x16x32_bf16 v[16:19], a[4:7], v[88:91], v[16:19]
v_mfma_f32_16x16x32_bf16 v[24:27], a[84:87], v[88:91], v[24:27]
v_mfma_f32_16x16x32_bf16 v[32:35], a[164:167], v[88:91], v[32:35]
ds_read_b128 v[88:91], v1 offset:23552
s_waitcnt lgkmcnt(4)
v_mfma_f32_16x16x32_bf16 v[20:23], a[4:7], v[92:95], v[20:23]
v_cvt_pk_bf16_f32 v104, v40, v41
v_cvt_pk_bf16_f32 v105, v42, v43
v_mfma_f32_16x16x32_bf16 v[28:31], a[84:87], v[92:95], v[28:31]
v_cvt_pk_bf16_f32 v106, v48, v49
v_cvt_pk_bf16_f32 v107, v50, v51
v_mfma_f32_16x16x32_bf16 v[36:39], a[164:167], v[92:95], v[36:39]
ds_read_b128 v[92:95], v1 offset:4096
s_waitcnt lgkmcnt(4)
v_mfma_f32_16x16x32_bf16 v[16:19], a[8:11], v[96:99], v[16:19]
ds_write_b128 v6, v[104:107] offset:12288
v_mfma_f32_16x16x32_bf16 v[24:27], a[88:91], v[96:99], v[24:27]
v_cvt_pk_bf16_f32 v108, v44, v45
v_cvt_pk_bf16_f32 v109, v46, v47
v_mfma_f32_16x16x32_bf16 v[32:35], a[168:171], v[96:99], v[32:35]
v_cvt_pk_bf16_f32 v110, v52, v53
ds_read_b128 v[96:99], v1 offset:24576
s_waitcnt lgkmcnt(5)
v_mfma_f32_16x16x32_bf16 v[20:23], a[8:11], v[80:83], v[20:23]
v_cvt_pk_bf16_f32 v111, v54, v55
v_mfma_f32_16x16x32_bf16 v[28:31], a[88:91], v[80:83], v[28:31]
ds_write_b128 v6, v[108:111] offset:13312
v_mfma_f32_16x16x32_bf16 v[36:39], a[168:171], v[80:83], v[36:39]
v_cvt_pk_bf16_f32 v112, v56, v57
ds_read_b128 v[80:83], v1 offset:5120
s_waitcnt lgkmcnt(6)
v_mfma_f32_16x16x32_bf16 v[16:19], a[12:15], v[84:87], v[16:19]
v_cvt_pk_bf16_f32 v113, v58, v59
v_cvt_pk_bf16_f32 v114, v60, v61
v_mfma_f32_16x16x32_bf16 v[24:27], a[92:95], v[84:87], v[24:27]
v_cvt_pk_bf16_f32 v115, v62, v63
v_mfma_f32_16x16x32_bf16 v[32:35], a[172:175], v[84:87], v[32:35]
ds_read_b128 v[84:87], v1 offset:25600
s_waitcnt lgkmcnt(6)
v_mfma_f32_16x16x32_bf16 v[20:23], a[12:15], v[88:91], v[20:23]
ds_write2st64_b64 v7, v[112:113], v[114:115] offset0:24 offset1:26
v_mfma_f32_16x16x32_bf16 v[28:31], a[92:95], v[88:91], v[28:31]
buffer_load_dword v224, v225, s[40:43], s58 offen sc1
v_mfma_f32_16x16x32_bf16 v[36:39], a[172:175], v[88:91], v[36:39]
ds_read_b128 v[88:91], v1 offset:6144
s_waitcnt lgkmcnt(7)
v_mfma_f32_16x16x32_bf16 v[16:19], a[16:19], v[92:95], v[16:19]
v_mfma_f32_16x16x32_bf16 v[24:27], a[96:99], v[92:95], v[24:27]
v_mfma_f32_16x16x32_bf16 v[32:35], a[176:179], v[92:95], v[32:35]
ds_read_b128 v[92:95], v1 offset:26624
s_waitcnt lgkmcnt(6)
v_mfma_f32_16x16x32_bf16 v[20:23], a[16:19], v[96:99], v[20:23]
v_mfma_f32_16x16x32_bf16 v[28:31], a[96:99], v[96:99], v[28:31]
v_mfma_f32_16x16x32_bf16 v[36:39], a[176:179], v[96:99], v[36:39]
ds_read_b128 v[96:99], v1 offset:7168
s_waitcnt lgkmcnt(5)
v_mfma_f32_16x16x32_bf16 v[16:19], a[20:23], v[80:83], v[16:19]
v_mfma_f32_16x16x32_bf16 v[24:27], a[100:103], v[80:83], v[24:27]
v_mfma_f32_16x16x32_bf16 v[32:35], a[180:183], v[80:83], v[32:35]
ds_read_b128 v[80:83], v1 offset:27648
s_waitcnt lgkmcnt(5)
v_mfma_f32_16x16x32_bf16 v[20:23], a[20:23], v[84:87], v[20:23]
v_mfma_f32_16x16x32_bf16 v[28:31], a[100:103], v[84:87], v[28:31]
v_mfma_f32_16x16x32_bf16 v[36:39], a[180:183], v[84:87], v[36:39]
ds_read_b128 v[84:87], v1 offset:8192
s_waitcnt lgkmcnt(4)
v_mfma_f32_16x16x32_bf16 v[16:19], a[24:27], v[88:91], v[16:19]
v_mfma_f32_16x16x32_bf16 v[24:27], a[104:107], v[88:91], v[24:27]
v_mfma_f32_16x16x32_bf16 v[32:35], a[184:187], v[88:91], v[32:35]
ds_read_b128 v[88:91], v1 offset:28672
s_waitcnt lgkmcnt(4)
v_mfma_f32_16x16x32_bf16 v[20:23], a[24:27], v[92:95], v[20:23]
v_mfma_f32_16x16x32_bf16 v[28:31], a[104:107], v[92:95], v[28:31]
v_mfma_f32_16x16x32_bf16 v[36:39], a[184:187], v[92:95], v[36:39]
ds_read_b128 v[92:95], v1 offset:9216
s_waitcnt lgkmcnt(4)
v_mfma_f32_16x16x32_bf16 v[16:19], a[28:31], v[96:99], v[16:19]
v_mfma_f32_16x16x32_bf16 v[24:27], a[108:111], v[96:99], v[24:27]
v_mfma_f32_16x16x32_bf16 v[32:35], a[188:191], v[96:99], v[32:35]
ds_read_b128 v[96:99], v1 offset:29696
s_waitcnt lgkmcnt(4)
v_mfma_f32_16x16x32_bf16 v[20:23], a[28:31], v[80:83], v[20:23]
v_mfma_f32_16x16x32_bf16 v[28:31], a[108:111], v[80:83], v[28:31]
v_mfma_f32_16x16x32_bf16 v[36:39], a[188:191], v[80:83], v[36:39]
ds_read_b128 v[80:83], v1 offset:10240
s_waitcnt lgkmcnt(4)
v_mfma_f32_16x16x32_bf16 v[16:19], a[32:35], v[84:87], v[16:19]
v_mfma_f32_16x16x32_bf16 v[24:27], a[112:115], v[84:87], v[24:27]
v_mfma_f32_16x16x32_bf16 v[32:35], a[192:195], v[84:87], v[32:35]
ds_read_b128 v[84:87], v1 offset:30720
s_waitcnt lgkmcnt(4)
v_mfma_f32_16x16x32_bf16 v[20:23], a[32:35], v[88:91], v[20:23]
v_mfma_f32_16x16x32_bf16 v[28:31], a[112:115], v[88:91], v[28:31]
v_mfma_f32_16x16x32_bf16 v[36:39], a[192:195], v[88:91], v[36:39]
ds_read_b128 v[88:91], v1 offset:11264
s_waitcnt lgkmcnt(4)
v_mfma_f32_16x16x32_bf16 v[16:19], a[36:39], v[92:95], v[16:19]
v_mfma_f32_16x16x32_bf16 v[24:27], a[116:119], v[92:95], v[24:27]
v_mfma_f32_16x16x32_bf16 v[32:35], a[196:199], v[92:95], v[32:35]
ds_read_b128 v[92:95], v1 offset:31744
s_waitcnt lgkmcnt(4)
v_mfma_f32_16x16x32_bf16 v[20:23], a[36:39], v[96:99], v[20:23]
v_mfma_f32_16x16x32_bf16 v[28:31], a[116:119], v[96:99], v[28:31]
v_mfma_f32_16x16x32_bf16 v[36:39], a[196:199], v[96:99], v[36:39]
ds_read_b128 v[96:99], v1 offset:12288
s_waitcnt lgkmcnt(4)
v_mfma_f32_16x16x32_bf16 v[16:19], a[40:43], v[80:83], v[16:19]
v_mfma_f32_16x16x32_bf16 v[24:27], a[120:123], v[80:83], v[24:27]
v_mfma_f32_16x16x32_bf16 v[32:35], a[200:203], v[80:83], v[32:35]
ds_read_b128 v[80:83], v1 offset:32768
s_waitcnt lgkmcnt(4)
v_mfma_f32_16x16x32_bf16 v[20:23], a[40:43], v[84:87], v[20:23]
v_mfma_f32_16x16x32_bf16 v[28:31], a[120:123], v[84:87], v[28:31]
v_mfma_f32_16x16x32_bf16 v[36:39], a[200:203], v[84:87], v[36:39]
ds_read_b128 v[84:87], v1 offset:13312
s_waitcnt lgkmcnt(4)
v_mfma_f32_16x16x32_bf16 v[16:19], a[44:47], v[88:91], v[16:19]
v_mfma_f32_16x16x32_bf16 v[24:27], a[124:127], v[88:91], v[24:27]
v_mfma_f32_16x16x32_bf16 v[32:35], a[204:207], v[88:91], v[32:35]
ds_read_b128 v[88:91], v1 offset:33792
s_waitcnt lgkmcnt(4)
v_mfma_f32_16x16x32_bf16 v[20:23], a[44:47], v[92:95], v[20:23]
v_mfma_f32_16x16x32_bf16 v[28:31], a[124:127], v[92:95], v[28:31]
v_mfma_f32_16x16x32_bf16 v[36:39], a[204:207], v[92:95], v[36:39]
ds_read_b128 v[92:95], v1 offset:14336
s_waitcnt lgkmcnt(4)
v_mfma_f32_16x16x32_bf16 v[16:19], a[48:51], v[96:99], v[16:19]
v_mfma_f32_16x16x32_bf16 v[24:27], a[128:131], v[96:99], v[24:27]
v_mfma_f32_16x16x32_bf16 v[32:35], a[208:211], v[96:99], v[32:35]
ds_read_b128 v[96:99], v1 offset:34816
s_waitcnt lgkmcnt(4)
v_mfma_f32_16x16x32_bf16 v[20:23], a[48:51], v[80:83], v[20:23]
v_mfma_f32_16x16x32_bf16 v[28:31], a[128:131], v[80:83], v[28:31]
v_mfma_f32_16x16x32_bf16 v[36:39], a[208:211], v[80:83], v[36:39]
ds_read_b128 v[80:83], v1 offset:15360
s_waitcnt lgkmcnt(4)
v_mfma_f32_16x16x32_bf16 v[16:19], a[52:55], v[84:87], v[16:19]
v_mfma_f32_16x16x32_bf16 v[24:27], a[132:135], v[84:87], v[24:27]
v_mfma_f32_16x16x32_bf16 v[32:35], a[212:215], v[84:87], v[32:35]
ds_read_b128 v[84:87], v1 offset:35840
s_waitcnt lgkmcnt(4)
v_mfma_f32_16x16x32_bf16 v[20:23], a[52:55], v[88:91], v[20:23]
v_mfma_f32_16x16x32_bf16 v[28:31], a[132:135], v[88:91], v[28:31]
v_mfma_f32_16x16x32_bf16 v[36:39], a[212:215], v[88:91], v[36:39]
ds_read_b128 v[88:91], v1 offset:16384
s_waitcnt lgkmcnt(4)
v_mfma_f32_16x16x32_bf16 v[16:19], a[56:59], v[92:95], v[16:19]
v_mfma_f32_16x16x32_bf16 v[24:27], a[136:139], v[92:95], v[24:27]
)"
R"(v_mfma_f32_16x16x32_bf16 v[32:35], a[216:219], v[92:95], v[32:35]
ds_read_b128 v[92:95], v1 offset:36864
s_waitcnt lgkmcnt(4)
v_mfma_f32_16x16x32_bf16 v[20:23], a[56:59], v[96:99], v[20:23]
v_mfma_f32_16x16x32_bf16 v[28:31], a[136:139], v[96:99], v[28:31]
v_mfma_f32_16x16x32_bf16 v[36:39], a[216:219], v[96:99], v[36:39]
s_waitcnt vmcnt(0) lgkmcnt(4)
s_barrier
s_and_b32 s48, s47, 63
s_cmp_lt_u32 s48, 62
s_cselect_b32 s48, 2, 0
v_cmp_gt_u32 vcc, s48, v224
s_cbranch_vccz hpl_xf0_ok_%=
s_mov_b32 s60, 0
s_nop 2
hpl_xf0_poll_%=:
buffer_load_dword v224, v225, s[40:43], s58 offen sc1
s_waitcnt vmcnt(0)
v_cmp_gt_u32 vcc, s48, v224
s_nop 4
s_cbranch_vccz hpl_xf0_ok_%=
s_sleep 2
s_add_u32 s60, s60, 1
s_cmp_gt_u32 s60, 65536
s_cbranch_scc1 hp_abort_%=
s_and_b32 s59, s60, 63
s_cbranch_scc1 hpl_xf0_poll_%=
v_mov_b32 v4, 1024
buffer_load_dword v4, v4, s[40:43], s58 offen sc1
s_waitcnt vmcnt(0)
v_readfirstlane_b32 s59, v4
s_nop 0
s_cmp_lg_u32 s59, 0
s_nop 2
s_cbranch_scc1 hp_end_%=
s_branch hpl_xf0_poll_%=
hpl_xf0_ok_%=:
v_add_u32 v225, 128, v225
ds_read_b128 v[96:99], v1 offset:17408
s_waitcnt lgkmcnt(4)
v_mfma_f32_16x16x32_bf16 v[16:19], a[60:63], v[80:83], v[16:19]
s_add_u32 m0, s61, 0
s_add_u32 s51, s50, 0
buffer_load_dwordx4 v0, s[40:43], s51 offen offset:0 sc1 lds
v_mfma_f32_16x16x32_bf16 v[24:27], a[140:143], v[80:83], v[24:27]
v_mfma_f32_16x16x32_bf16 v[32:35], a[220:223], v[80:83], v[32:35]
ds_read_b128 v[80:83], v1 offset:37888
s_waitcnt lgkmcnt(4)
v_mfma_f32_16x16x32_bf16 v[20:23], a[60:63], v[84:87], v[20:23]
buffer_load_dwordx4 v0, s[40:43], s51 offen offset:1024 sc1 lds
v_mfma_f32_16x16x32_bf16 v[28:31], a[140:143], v[84:87], v[28:31]
v_mfma_f32_16x16x32_bf16 v[36:39], a[220:223], v[84:87], v[36:39]
ds_read_b128 v[84:87], v1 offset:18432
s_waitcnt lgkmcnt(4)
v_mfma_f32_16x16x32_bf16 v[16:19], a[64:67], v[88:91], v[16:19]
buffer_load_dwordx4 v0, s[40:43], s51 offen offset:2048 sc1 lds
v_mfma_f32_16x16x32_bf16 v[24:27], a[144:147], v[88:91], v[24:27]
v_mfma_f32_16x16x32_bf16 v[32:35], a[224:227], v[88:91], v[32:35]
ds_read_b128 v[88:91], v1 offset:38912
s_waitcnt lgkmcnt(4)
v_mfma_f32_16x16x32_bf16 v[20:23], a[64:67], v[92:95], v[20:23]
buffer_load_dwordx4 v0, s[40:43], s51 offen offset:3072 sc1 lds
v_mfma_f32_16x16x32_bf16 v[28:31], a[144:147], v[92:95], v[28:31]
v_mfma_f32_16x16x32_bf16 v[36:39], a[224:227], v[92:95], v[36:39]
ds_read_b128 v[92:95], v1 offset:19456
s_waitcnt lgkmcnt(4)
v_mfma_f32_16x16x32_bf16 v[16:19], a[68:71], v[96:99], v[16:19]
s_add_u32 m0, s61, 4096
s_add_u32 s51, s50, 4096
buffer_load_dwordx4 v0, s[40:43], s51 offen offset:0 sc1 lds
v_mfma_f32_16x16x32_bf16 v[24:27], a[148:151], v[96:99], v[24:27]
v_mfma_f32_16x16x32_bf16 v[32:35], a[228:231], v[96:99], v[32:35]
ds_read_b128 v[96:99], v1 offset:39936
s_waitcnt lgkmcnt(4)
v_mfma_f32_16x16x32_bf16 v[20:23], a[68:71], v[80:83], v[20:23]
buffer_load_dwordx4 v0, s[40:43], s51 offen offset:1024 sc1 lds
v_mfma_f32_16x16x32_bf16 v[28:31], a[148:151], v[80:83], v[28:31]
v_mfma_f32_16x16x32_bf16 v[36:39], a[228:231], v[80:83], v[36:39]
s_add_u32 s64, s64, 40960
s_cmp_ge_u32 s64, 122880
s_cselect_b32 s64, 0, s64
v_add_u32 v1, s64, v0
ds_read_b128 v[80:83], v1 offset:0
s_waitcnt lgkmcnt(4)
v_mfma_f32_16x16x32_bf16 v[16:19], a[72:75], v[84:87], v[16:19]
buffer_load_dwordx4 v0, s[40:43], s51 offen offset:2048 sc1 lds
v_mfma_f32_16x16x32_bf16 v[24:27], a[152:155], v[84:87], v[24:27]
v_mfma_f32_16x16x32_bf16 v[32:35], a[232:235], v[84:87], v[32:35]
ds_read_b128 v[84:87], v1 offset:20480
s_waitcnt lgkmcnt(4)
v_mfma_f32_16x16x32_bf16 v[20:23], a[72:75], v[88:91], v[20:23]
buffer_load_dwordx4 v0, s[40:43], s51 offen offset:3072 sc1 lds
v_mfma_f32_16x16x32_bf16 v[28:31], a[152:155], v[88:91], v[28:31]
v_mfma_f32_16x16x32_bf16 v[36:39], a[232:235], v[88:91], v[36:39]
ds_read_b128 v[88:91], v1 offset:1024
s_waitcnt lgkmcnt(4)
v_mfma_f32_16x16x32_bf16 v[16:19], a[76:79], v[92:95], v[16:19]
s_add_u32 m0, s61, 8192
s_add_u32 s51, s50, 8192
buffer_load_dwordx4 v0, s[40:43], s51 offen offset:0 sc1 lds
v_mfma_f32_16x16x32_bf16 v[24:27], a[156:159], v[92:95], v[24:27]
v_mfma_f32_16x16x32_bf16 v[32:35], a[236:239], v[92:95], v[32:35]
ds_read_b128 v[92:95], v1 offset:21504
s_waitcnt lgkmcnt(4)
v_mfma_f32_16x16x32_bf16 v[20:23], a[76:79], v[96:99], v[20:23]
buffer_load_dwordx4 v0, s[40:43], s51 offen offset:1024 sc1 lds
v_mfma_f32_16x16x32_bf16 v[28:31], a[156:159], v[96:99], v[28:31]
s_add_u32 s50, s50, 40960
s_add_u32 s61, s61, 40960
s_cmp_ge_u32 s61, 122880
s_cselect_b32 s51, 122880, 0
s_sub_u32 s61, s61, s51
v_mfma_f32_16x16x32_bf16 v[36:39], a[236:239], v[96:99], v[36:39]
ds_read_b128 v[96:99], v1 offset:2048
s_add_u32 s47, s47, 1
s_waitcnt lgkmcnt(4)
v_mfma_f32_16x16x32_bf16 v[40:43], a[0:3], v[80:83], 0
v_mfma_f32_16x16x32_bf16 v[48:51], a[80:83], v[80:83], 0
v_mfma_f32_16x16x32_bf16 v[56:59], a[160:163], v[80:83], 0
ds_read_b128 v[80:83], v1 offset:22528
s_waitcnt lgkmcnt(4)
v_mfma_f32_16x16x32_bf16 v[44:47], a[0:3], v[84:87], 0
v_mfma_f32_16x16x32_bf16 v[52:55], a[80:83], v[84:87], 0
v_mfma_f32_16x16x32_bf16 v[60:63], a[160:163], v[84:87], 0
ds_read_b128 v[84:87], v1 offset:3072
s_waitcnt lgkmcnt(4)
v_mfma_f32_16x16x32_bf16 v[40:43], a[4:7], v[88:91], v[40:43]
v_mfma_f32_16x16x32_bf16 v[48:51], a[84:87], v[88:91], v[48:51]
v_mfma_f32_16x16x32_bf16 v[56:59], a[164:167], v[88:91], v[56:59]
ds_read_b128 v[88:91], v1 offset:23552
s_waitcnt lgkmcnt(4)
v_mfma_f32_16x16x32_bf16 v[44:47], a[4:7], v[92:95], v[44:47]
v_cvt_pk_bf16_f32 v104, v16, v17
v_cvt_pk_bf16_f32 v105, v18, v19
v_mfma_f32_16x16x32_bf16 v[52:55], a[84:87], v[92:95], v[52:55]
v_cvt_pk_bf16_f32 v106, v24, v25
v_cvt_pk_bf16_f32 v107, v26, v27
v_mfma_f32_16x16x32_bf16 v[60:63], a[164:167], v[92:95], v[60:63]
ds_read_b128 v[92:95], v1 offset:4096
s_waitcnt lgkmcnt(4)
v_mfma_f32_16x16x32_bf16 v[40:43], a[8:11], v[96:99], v[40:43]
ds_write_b128 v6, v[104:107] offset:0
v_mfma_f32_16x16x32_bf16 v[48:51], a[88:91], v[96:99], v[48:51]
v_cvt_pk_bf16_f32 v108, v20, v21
v_cvt_pk_bf16_f32 v109, v22, v23
v_mfma_f32_16x16x32_bf16 v[56:59], a[168:171], v[96:99], v[56:59]
v_cvt_pk_bf16_f32 v110, v28, v29
ds_read_b128 v[96:99], v1 offset:24576
s_waitcnt lgkmcnt(5)
v_mfma_f32_16x16x32_bf16 v[44:47], a[8:11], v[80:83], v[44:47]
v_cvt_pk_bf16_f32 v111, v30, v31
v_mfma_f32_16x16x32_bf16 v[52:55], a[88:91], v[80:83], v[52:55]
ds_write_b128 v6, v[108:111] offset:1024
v_mfma_f32_16x16x32_bf16 v[60:63], a[168:171], v[80:83], v[60:63]
v_cvt_pk_bf16_f32 v112, v32, v33
ds_read_b128 v[80:83], v1 offset:5120
s_waitcnt lgkmcnt(6)
v_mfma_f32_16x16x32_bf16 v[40:43], a[12:15], v[84:87], v[40:43]
v_cvt_pk_bf16_f32 v113, v34, v35
v_cvt_pk_bf16_f32 v114, v36, v37
v_mfma_f32_16x16x32_bf16 v[48:51], a[92:95], v[84:87], v[48:51]
v_cvt_pk_bf16_f32 v115, v38, v39
v_mfma_f32_16x16x32_bf16 v[56:59], a[172:175], v[84:87], v[56:59]
ds_read_b128 v[84:87], v1 offset:25600
s_waitcnt lgkmcnt(6)
v_mfma_f32_16x16x32_bf16 v[44:47], a[12:15], v[88:91], v[44:47]
ds_write2st64_b64 v7, v[112:113], v[114:115] offset0:0 offset1:2
v_mfma_f32_16x16x32_bf16 v[52:55], a[92:95], v[88:91], v[52:55]
buffer_load_dword v224, v225, s[40:43], s58 offen sc1
v_mfma_f32_16x16x32_bf16 v[60:63], a[172:175], v[88:91], v[60:63]
ds_read_b128 v[88:91], v1 offset:6144
s_waitcnt lgkmcnt(7)
v_mfma_f32_16x16x32_bf16 v[40:43], a[16:19], v[92:95], v[40:43]
v_mfma_f32_16x16x32_bf16 v[48:51], a[96:99], v[92:95], v[48:51]
v_mfma_f32_16x16x32_bf16 v[56:59], a[176:179], v[92:95], v[56:59]
ds_read_b128 v[92:95], v1 offset:26624
s_waitcnt lgkmcnt(6)
v_mfma_f32_16x16x32_bf16 v[44:47], a[16:19], v[96:99], v[44:47]
v_mfma_f32_16x16x32_bf16 v[52:55], a[96:99], v[96:99], v[52:55]
v_mfma_f32_16x16x32_bf16 v[60:63], a[176:179], v[96:99], v[60:63]
ds_read_b128 v[96:99], v1 offset:7168
s_waitcnt lgkmcnt(5)
v_mfma_f32_16x16x32_bf16 v[40:43], a[20:23], v[80:83], v[40:43]
v_mfma_f32_16x16x32_bf16 v[48:51], a[100:103], v[80:83], v[48:51]
v_mfma_f32_16x16x32_bf16 v[56:59], a[180:183], v[80:83], v[56:59]
ds_read_b128 v[80:83], v1 offset:27648
s_waitcnt lgkmcnt(5)
v_mfma_f32_16x16x32_bf16 v[44:47], a[20:23], v[84:87], v[44:47]
v_mfma_f32_16x16x32_bf16 v[52:55], a[100:103], v[84:87], v[52:55]
v_mfma_f32_16x16x32_bf16 v[60:63], a[180:183], v[84:87], v[60:63]
ds_read_b128 v[84:87], v1 offset:8192
s_waitcnt lgkmcnt(4)
v_mfma_f32_16x16x32_bf16 v[40:43], a[24:27], v[88:91], v[40:43]
v_mfma_f32_16x16x32_bf16 v[48:51], a[104:107], v[88:91], v[48:51]
v_mfma_f32_16x16x32_bf16 v[56:59], a[184:187], v[88:91], v[56:59]
ds_read_b128 v[88:91], v1 offset:28672
s_waitcnt lgkmcnt(4)
v_mfma_f32_16x16x32_bf16 v[44:47], a[24:27], v[92:95], v[44:47]
v_mfma_f32_16x16x32_bf16 v[52:55], a[104:107], v[92:95], v[52:55]
v_mfma_f32_16x16x32_bf16 v[60:63], a[184:187], v[92:95], v[60:63]
)"
R"(ds_read_b128 v[92:95], v1 offset:9216
s_waitcnt lgkmcnt(4)
v_mfma_f32_16x16x32_bf16 v[40:43], a[28:31], v[96:99], v[40:43]
v_mfma_f32_16x16x32_bf16 v[48:51], a[108:111], v[96:99], v[48:51]
v_mfma_f32_16x16x32_bf16 v[56:59], a[188:191], v[96:99], v[56:59]
ds_read_b128 v[96:99], v1 offset:29696
s_waitcnt lgkmcnt(4)
v_mfma_f32_16x16x32_bf16 v[44:47], a[28:31], v[80:83], v[44:47]
v_mfma_f32_16x16x32_bf16 v[52:55], a[108:111], v[80:83], v[52:55]
v_mfma_f32_16x16x32_bf16 v[60:63], a[188:191], v[80:83], v[60:63]
ds_read_b128 v[80:83], v1 offset:10240
s_waitcnt lgkmcnt(4)
v_mfma_f32_16x16x32_bf16 v[40:43], a[32:35], v[84:87], v[40:43]
v_mfma_f32_16x16x32_bf16 v[48:51], a[112:115], v[84:87], v[48:51]
v_mfma_f32_16x16x32_bf16 v[56:59], a[192:195], v[84:87], v[56:59]
ds_read_b128 v[84:87], v1 offset:30720
s_waitcnt lgkmcnt(4)
v_mfma_f32_16x16x32_bf16 v[44:47], a[32:35], v[88:91], v[44:47]
v_mfma_f32_16x16x32_bf16 v[52:55], a[112:115], v[88:91], v[52:55]
v_mfma_f32_16x16x32_bf16 v[60:63], a[192:195], v[88:91], v[60:63]
ds_read_b128 v[88:91], v1 offset:11264
s_waitcnt lgkmcnt(4)
v_mfma_f32_16x16x32_bf16 v[40:43], a[36:39], v[92:95], v[40:43]
v_mfma_f32_16x16x32_bf16 v[48:51], a[116:119], v[92:95], v[48:51]
v_mfma_f32_16x16x32_bf16 v[56:59], a[196:199], v[92:95], v[56:59]
ds_read_b128 v[92:95], v1 offset:31744
s_waitcnt lgkmcnt(4)
v_mfma_f32_16x16x32_bf16 v[44:47], a[36:39], v[96:99], v[44:47]
v_mfma_f32_16x16x32_bf16 v[52:55], a[116:119], v[96:99], v[52:55]
v_mfma_f32_16x16x32_bf16 v[60:63], a[196:199], v[96:99], v[60:63]
ds_read_b128 v[96:99], v1 offset:12288
s_waitcnt lgkmcnt(4)
v_mfma_f32_16x16x32_bf16 v[40:43], a[40:43], v[80:83], v[40:43]
v_mfma_f32_16x16x32_bf16 v[48:51], a[120:123], v[80:83], v[48:51]
v_mfma_f32_16x16x32_bf16 v[56:59], a[200:203], v[80:83], v[56:59]
ds_read_b128 v[80:83], v1 offset:32768
s_waitcnt lgkmcnt(4)
v_mfma_f32_16x16x32_bf16 v[44:47], a[40:43], v[84:87], v[44:47]
v_mfma_f32_16x16x32_bf16 v[52:55], a[120:123], v[84:87], v[52:55]
v_mfma_f32_16x16x32_bf16 v[60:63], a[200:203], v[84:87], v[60:63]
ds_read_b128 v[84:87], v1 offset:13312
s_waitcnt lgkmcnt(4)
v_mfma_f32_16x16x32_bf16 v[40:43], a[44:47], v[88:91], v[40:43]
v_mfma_f32_16x16x32_bf16 v[48:51], a[124:127], v[88:91], v[48:51]
v_mfma_f32_16x16x32_bf16 v[56:59], a[204:207], v[88:91], v[56:59]
ds_read_b128 v[88:91], v1 offset:33792
s_waitcnt lgkmcnt(4)
v_mfma_f32_16x16x32_bf16 v[44:47], a[44:47], v[92:95], v[44:47]
v_mfma_f32_16x16x32_bf16 v[52:55], a[124:127], v[92:95], v[52:55]
v_mfma_f32_16x16x32_bf16 v[60:63], a[204:207], v[92:95], v[60:63]
ds_read_b128 v[92:95], v1 offset:14336
s_waitcnt lgkmcnt(4)
v_mfma_f32_16x16x32_bf16 v[40:43], a[48:51], v[96:99], v[40:43]
v_mfma_f32_16x16x32_bf16 v[48:51], a[128:131], v[96:99], v[48:51]
v_mfma_f32_16x16x32_bf16 v[56:59], a[208:211], v[96:99], v[56:59]
ds_read_b128 v[96:99], v1 offset:34816
s_waitcnt lgkmcnt(4)
v_mfma_f32_16x16x32_bf16 v[44:47], a[48:51], v[80:83], v[44:47]
v_mfma_f32_16x16x32_bf16 v[52:55], a[128:131], v[80:83], v[52:55]
v_mfma_f32_16x16x32_bf16 v[60:63], a[208:211], v[80:83], v[60:63]
ds_read_b128 v[80:83], v1 offset:15360
s_waitcnt lgkmcnt(4)
v_mfma_f32_16x16x32_bf16 v[40:43], a[52:55], v[84:87], v[40:43]
v_mfma_f32_16x16x32_bf16 v[48:51], a[132:135], v[84:87], v[48:51]
v_mfma_f32_16x16x32_bf16 v[56:59], a[212:215], v[84:87], v[56:59]
ds_read_b128 v[84:87], v1 offset:35840
s_waitcnt lgkmcnt(4)
v_mfma_f32_16x16x32_bf16 v[44:47], a[52:55], v[88:91], v[44:47]
v_mfma_f32_16x16x32_bf16 v[52:55], a[132:135], v[88:91], v[52:55]
v_mfma_f32_16x16x32_bf16 v[60:63], a[212:215], v[88:91], v[60:63]
ds_read_b128 v[88:91], v1 offset:16384
s_waitcnt lgkmcnt(4)
v_mfma_f32_16x16x32_bf16 v[40:43], a[56:59], v[92:95], v[40:43]
v_mfma_f32_16x16x32_bf16 v[48:51], a[136:139], v[92:95], v[48:51]
v_mfma_f32_16x16x32_bf16 v[56:59], a[216:219], v[92:95], v[56:59]
ds_read_b128 v[92:95], v1 offset:36864
s_waitcnt lgkmcnt(4)
v_mfma_f32_16x16x32_bf16 v[44:47], a[56:59], v[96:99], v[44:47]
v_mfma_f32_16x16x32_bf16 v[52:55], a[136:139], v[96:99], v[52:55]
v_mfma_f32_16x16x32_bf16 v[60:63], a[216:219], v[96:99], v[60:63]
s_waitcnt vmcnt(0) lgkmcnt(4)
s_barrier
s_and_b32 s48, s47, 63
s_cmp_lt_u32 s48, 62
s_cselect_b32 s48, 2, 0
v_cmp_gt_u32 vcc, s48, v224
s_cbranch_vccz hpl_xf1_ok_%=
s_mov_b32 s60, 0
s_nop 2
hpl_xf1_poll_%=:
buffer_load_dword v224, v225, s[40:43], s58 offen sc1
s_waitcnt vmcnt(0)
v_cmp_gt_u32 vcc, s48, v224
s_nop 4
s_cbranch_vccz hpl_xf1_ok_%=
s_sleep 2
s_add_u32 s60, s60, 1
s_cmp_gt_u32 s60, 65536
s_cbranch_scc1 hp_abort_%=
s_and_b32 s59, s60, 63
s_cbranch_scc1 hpl_xf1_poll_%=
v_mov_b32 v4, 1024
buffer_load_dword v4, v4, s[40:43], s58 offen sc1
s_waitcnt vmcnt(0)
v_readfirstlane_b32 s59, v4
s_nop 0
s_cmp_lg_u32 s59, 0
s_nop 2
s_cbranch_scc1 hp_end_%=
s_branch hpl_xf1_poll_%=
hpl_xf1_ok_%=:
v_add_u32 v225, 128, v225
ds_read_b128 v[96:99], v1 offset:17408
s_waitcnt lgkmcnt(4)
v_mfma_f32_16x16x32_bf16 v[40:43], a[60:63], v[80:83], v[40:43]
s_add_u32 m0, s61, 0
s_add_u32 s51, s50, 0
buffer_load_dwordx4 v0, s[40:43], s51 offen offset:0 sc1 lds
v_mfma_f32_16x16x32_bf16 v[48:51], a[140:143], v[80:83], v[48:51]
v_mfma_f32_16x16x32_bf16 v[56:59], a[220:223], v[80:83], v[56:59]
ds_read_b128 v[80:83], v1 offset:37888
s_waitcnt lgkmcnt(4)
v_mfma_f32_16x16x32_bf16 v[44:47], a[60:63], v[84:87], v[44:47]
buffer_load_dwordx4 v0, s[40:43], s51 offen offset:1024 sc1 lds
v_mfma_f32_16x16x32_bf16 v[52:55], a[140:143], v[84:87], v[52:55]
v_mfma_f32_16x16x32_bf16 v[60:63], a[220:223], v[84:87], v[60:63]
ds_read_b128 v[84:87], v1 offset:18432
s_waitcnt lgkmcnt(4)
v_mfma_f32_16x16x32_bf16 v[40:43], a[64:67], v[88:91], v[40:43]
buffer_load_dwordx4 v0, s[40:43], s51 offen offset:2048 sc1 lds
v_mfma_f32_16x16x32_bf16 v[48:51], a[144:147], v[88:91], v[48:51]
v_mfma_f32_16x16x32_bf16 v[56:59], a[224:227], v[88:91], v[56:59]
ds_read_b128 v[88:91], v1 offset:38912
s_waitcnt lgkmcnt(4)
v_mfma_f32_16x16x32_bf16 v[44:47], a[64:67], v[92:95], v[44:47]
buffer_load_dwordx4 v0, s[40:43], s51 offen offset:3072 sc1 lds
v_mfma_f32_16x16x32_bf16 v[52:55], a[144:147], v[92:95], v[52:55]
v_mfma_f32_16x16x32_bf16 v[60:63], a[224:227], v[92:95], v[60:63]
ds_read_b128 v[92:95], v1 offset:19456
s_waitcnt lgkmcnt(4)
v_mfma_f32_16x16x32_bf16 v[40:43], a[68:71], v[96:99], v[40:43]
s_add_u32 m0, s61, 4096
s_add_u32 s51, s50, 4096
buffer_load_dwordx4 v0, s[40:43], s51 offen offset:0 sc1 lds
v_mfma_f32_16x16x32_bf16 v[48:51], a[148:151], v[96:99], v[48:51]
v_mfma_f32_16x16x32_bf16 v[56:59], a[228:231], v[96:99], v[56:59]
ds_read_b128 v[96:99], v1 offset:39936
s_waitcnt lgkmcnt(4)
v_mfma_f32_16x16x32_bf16 v[44:47], a[68:71], v[80:83], v[44:47]
buffer_load_dwordx4 v0, s[40:43], s51 offen offset:1024 sc1 lds
v_mfma_f32_16x16x32_bf16 v[52:55], a[148:151], v[80:83], v[52:55]
v_mfma_f32_16x16x32_bf16 v[60:63], a[228:231], v[80:83], v[60:63]
s_add_u32 s64, s64, 40960
s_cmp_ge_u32 s64, 122880
s_cselect_b32 s64, 0, s64
v_add_u32 v1, s64, v0
ds_read_b128 v[80:83], v1 offset:0
s_waitcnt lgkmcnt(4)
v_mfma_f32_16x16x32_bf16 v[40:43], a[72:75], v[84:87], v[40:43]
buffer_load_dwordx4 v0, s[40:43], s51 offen offset:2048 sc1 lds
v_mfma_f32_16x16x32_bf16 v[48:51], a[152:155], v[84:87], v[48:51]
v_mfma_f32_16x16x32_bf16 v[56:59], a[232:235], v[84:87], v[56:59]
ds_read_b128 v[84:87], v1 offset:20480
s_waitcnt lgkmcnt(4)
v_mfma_f32_16x16x32_bf16 v[44:47], a[72:75], v[88:91], v[44:47]
buffer_load_dwordx4 v0, s[40:43], s51 offen offset:3072 sc1 lds
v_mfma_f32_16x16x32_bf16 v[52:55], a[152:155], v[88:91], v[52:55]
v_mfma_f32_16x16x32_bf16 v[60:63], a[232:235], v[88:91], v[60:63]
ds_read_b128 v[88:91], v1 offset:1024
s_waitcnt lgkmcnt(4)
v_mfma_f32_16x16x32_bf16 v[40:43], a[76:79], v[92:95], v[40:43]
s_add_u32 m0, s61, 8192
s_add_u32 s51, s50, 8192
buffer_load_dwordx4 v0, s[40:43], s51 offen offset:0 sc1 lds
v_mfma_f32_16x16x32_bf16 v[48:51], a[156:159], v[92:95], v[48:51]
v_mfma_f32_16x16x32_bf16 v[56:59], a[236:239], v[92:95], v[56:59]
ds_read_b128 v[92:95], v1 offset:21504
s_waitcnt lgkmcnt(4)
v_mfma_f32_16x16x32_bf16 v[44:47], a[76:79], v[96:99], v[44:47]
buffer_load_dwordx4 v0, s[40:43], s51 offen offset:1024 sc1 lds
v_mfma_f32_16x16x32_bf16 v[52:55], a[156:159], v[96:99], v[52:55]
s_add_u32 s50, s50, 40960
s_add_u32 s61, s61, 40960
s_cmp_ge_u32 s61, 122880
s_cselect_b32 s51, 122880, 0
s_sub_u32 s61, s61, s51
v_mfma_f32_16x16x32_bf16 v[60:63], a[236:239], v[96:99], v[60:63]
ds_read_b128 v[96:99], v1 offset:2048
s_add_u32 s47, s47, 1
s_and_b32 s59, s47, 63
s_cbranch_scc1 hpl_loop_%=
s_waitcnt lgkmcnt(0)
v_cvt_pk_bf16_f32 v104, v40, v41
v_cvt_pk_bf16_f32 v105, v42, v43
v_cvt_pk_bf16_f32 v106, v48, v49
v_cvt_pk_bf16_f32 v107, v50, v51
ds_write_b128 v6, v[104:107] offset:12288
v_cvt_pk_bf16_f32 v108, v44, v45
v_cvt_pk_bf16_f32 v109, v46, v47
v_cvt_pk_bf16_f32 v110, v52, v53
)"
R"(v_cvt_pk_bf16_f32 v111, v54, v55
ds_write_b128 v6, v[108:111] offset:13312
v_cvt_pk_bf16_f32 v112, v56, v57
v_cvt_pk_bf16_f32 v113, v58, v59
v_cvt_pk_bf16_f32 v114, v60, v61
v_cvt_pk_bf16_f32 v115, v62, v63
ds_write2st64_b64 v7, v[112:113], v[114:115] offset0:24 offset1:26
s_waitcnt lgkmcnt(0)
s_barrier
s_branch hp_ret_%=
hp_roleo_%=:
v_and_b32 v1, 0x370, v0
v_bfe_u32 v4, v15, 3, 1
v_lshl_add_u32 v1, v4, 15, v1
s_lshr_b32 s48, s44, 1
s_lshl_b32 s48, s48, 10
s_and_b32 s49, s48, 0x2000
s_add_u32 s48, s48, s49
s_and_b32 s49, s44, 1
s_lshl_b32 s49, s49, 7
s_add_u32 s48, s48, s49
v_add_u32 v1, s48, v1
s_mul_i32 s62, s45, 655360
s_add_u32 s62, s62, 32964608
s_mov_b32 s63, s62
s_lshr_b32 s48, s47, 1
s_lshl_b32 s48, s48, 7
s_add_u32 s48, s48, 254976
v_mov_b32 v13, s48
v_mov_b32 v14, 0
s_lshl_b32 s48, s45, 11
v_add_u32 v8, s48, v0
s_lshr_b32 s48, s45, 1
s_lshl_b32 s48, s48, 11
s_and_b32 s49, s45, 1
s_lshl3_add_u32 s48, s49, s48
s_add_u32 s48, s48, 8192
v_add_u32 v9, s48, v0
s_mul_i32 s49, s45, 3072
s_mul_i32 s50, s44, 12288
s_add_u32 s50, s50, 20365312
s_add_u32 s50, s50, s49
s_lshl_b32 s48, s44, 7
s_add_u32 s49, s48, 2048
v_mov_b32 v7, s49
v_mov_b32 v6, 0
s_cmp_eq_u32 s45, 0
s_cselect_b32 s59, 1, 0
v_lshlrev_b32 v2, 7, v15
v_cmp_gt_u32 vcc, s59, v15
v_mov_b32 v4, 2147483632
s_nop 0
v_cndmask_b32 v2, v4, v2, vcc
s_cmp_eq_u32 s45, 0
s_cselect_b32 s59, 8, 0
v_lshlrev_b32 v10, 7, v15
v_cmp_gt_u32 vcc, s59, v15
v_mov_b32 v4, 2147483632
s_nop 0
v_cndmask_b32 v10, v4, v10, vcc
v_mov_b32 v12, 1
s_add_u32 s61, s47, 64
s_waitcnt vmcnt(0)
s_add_u32 s55, s47, 1
s_mov_b32 s64, 0x40000000
s_mov_b32 s65, 0x40000000
v_cmp_gt_u32 vcc, s55, v6
s_cbranch_vccz hpo_v0_ok_%=
s_mov_b32 s60, 0
s_nop 2
hpo_v0_poll_%=:
buffer_load_dword v6, v7, s[40:43], s58 offen sc1
s_waitcnt vmcnt(0)
v_cmp_gt_u32 vcc, s55, v6
s_nop 4
s_cbranch_vccz hpo_v0_ok_%=
s_sleep 1
s_add_u32 s60, s60, 1
s_cmp_gt_u32 s60, 65536
s_cbranch_scc1 hp_abort_%=
s_and_b32 s59, s60, 63
s_cbranch_scc1 hpo_v0_poll_%=
v_mov_b32 v4, 1024
buffer_load_dword v4, v4, s[40:43], s58 offen sc1
s_waitcnt vmcnt(0)
v_readfirstlane_b32 s59, v4
s_nop 0
s_cmp_lg_u32 s59, 0
s_nop 2
s_cbranch_scc1 hp_end_%=
s_branch hpo_v0_poll_%=
hpo_v0_ok_%=:
buffer_load_dwordx4 v[144:147], v0, s[40:43], s50 offen offset:0 sc1
buffer_load_dwordx4 v[148:151], v0, s[40:43], s50 offen offset:1024 sc1
buffer_load_dwordx4 v[152:155], v0, s[40:43], s50 offen offset:2048 sc1
s_waitcnt vmcnt(2)
ds_write_b128 v8, v[144:147] offset:0
s_waitcnt vmcnt(1)
ds_write_b128 v8, v[148:151] offset:1024
s_waitcnt vmcnt(0)
ds_write2st64_b64 v9, v[152:153], v[154:155] offset0:0 offset1:2
s_waitcnt lgkmcnt(0)
s_barrier
ds_read_b128 v[16:19], v0 offset:0
ds_read_b128 v[20:23], v0 offset:1024
ds_read_b128 v[24:27], v0 offset:2048
ds_read_b128 v[28:31], v0 offset:3072
ds_read_b128 v[32:35], v0 offset:4096
ds_read_b128 v[36:39], v0 offset:5120
ds_read_b128 v[40:43], v0 offset:6144
ds_read_b128 v[44:47], v0 offset:7168
ds_read_b128 v[48:51], v0 offset:8192
ds_read_b128 v[52:55], v0 offset:9216
ds_read_b128 v[56:59], v0 offset:10240
ds_read_b128 v[60:63], v0 offset:11264
s_waitcnt lgkmcnt(0)
s_mov_b32 s53, 0x40000000
v_mov_b32 v5, v6
s_nop 1
hpo_loop_%=:
s_bitcmp1_b32 s47, 5
s_cselect_b32 s48, 8, 0
v_cmp_gt_u32 vcc, s48, v14
s_cbranch_vccz hpo_free_ok_%=
s_mov_b32 s60, 0
s_nop 2
hpo_free_poll_%=:
buffer_load_dword v14, v13, s[40:43], s58 offen sc1
s_waitcnt vmcnt(0)
v_cmp_gt_u32 vcc, s48, v14
s_nop 4
s_cbranch_vccz hpo_free_ok_%=
s_sleep 8
s_add_u32 s60, s60, 1
s_cmp_gt_u32 s60, 65536
s_cbranch_scc1 hp_abort_%=
s_and_b32 s59, s60, 63
s_cbranch_scc1 hpo_free_poll_%=
v_mov_b32 v4, 1024
buffer_load_dword v4, v4, s[40:43], s58 offen sc1
s_waitcnt vmcnt(0)
v_readfirstlane_b32 s59, v4
s_nop 0
s_cmp_lg_u32 s59, 0
s_nop 2
s_cbranch_scc1 hp_end_%=
s_branch hpo_free_poll_%=
hpo_free_ok_%=:
v_add_u32 v13, 128, v13
s_cmp_lg_u32 s47, 12
s_cbranch_scc1 hpo_gate_ok_%=
v_add_u32 v3, 4096, v7
v_mov_b32 v11, 0
s_mov_b32 s48, 2
v_cmp_gt_u32 vcc, s48, v11
s_cbranch_vccz hpo_gate_ok_%=
s_mov_b32 s60, 0
s_nop 2
hpo_gate_poll_%=:
buffer_load_dword v11, v3, s[40:43], s58 offen sc1
s_waitcnt vmcnt(0)
v_cmp_gt_u32 vcc, s48, v11
s_nop 4
s_cbranch_vccz hpo_gate_ok_%=
s_sleep 8
s_add_u32 s60, s60, 1
s_cmp_gt_u32 s60, 65536
s_cbranch_scc1 hp_abort_%=
s_and_b32 s59, s60, 63
s_cbranch_scc1 hpo_gate_poll_%=
v_mov_b32 v4, 1024
buffer_load_dword v4, v4, s[40:43], s58 offen sc1
s_waitcnt vmcnt(0)
v_readfirstlane_b32 s59, v4
s_nop 0
s_cmp_lg_u32 s59, 0
s_nop 2
s_cbranch_scc1 hp_end_%=
s_branch hpo_gate_poll_%=
hpo_gate_ok_%=:
s_add_u32 s55, s47, 2
s_min_u32 s55, s55, s61
s_add_u32 s59, s47, 0
s_and_b32 s59, s59, 63
s_cmp_eq_u32 s59, 31
s_cselect_b32 s59, -31, 1
s_mul_i32 s57, s59, 393216
s_add_u32 s50, s50, s57
v_mfma_f32_16x16x32_bf16 v[112:115], v[16:19], a[0:3], 0
buffer_load_dword v6, v7, s[40:43], s58 offen sc1
v_mfma_f32_16x16x32_bf16 v[116:119], v[20:23], a[0:3], 0
v_mfma_f32_16x16x32_bf16 v[112:115], v[24:27], a[4:7], v[112:115]
v_mfma_f32_16x16x32_bf16 v[116:119], v[28:31], a[4:7], v[116:119]
v_mfma_f32_16x16x32_bf16 v[112:115], v[32:35], a[8:11], v[112:115]
v_mfma_f32_16x16x32_bf16 v[116:119], v[36:39], a[8:11], v[116:119]
v_mfma_f32_16x16x32_bf16 v[112:115], v[40:43], a[12:15], v[112:115]
v_cvt_pk_bf16_f32 v140, v120, v121
v_mfma_f32_16x16x32_bf16 v[116:119], v[44:47], a[12:15], v[116:119]
v_cvt_pk_bf16_f32 v141, v122, v123
v_mfma_f32_16x16x32_bf16 v[112:115], v[48:51], a[16:19], v[112:115]
)"
R"(v_cvt_pk_bf16_f32 v142, v124, v125
v_mfma_f32_16x16x32_bf16 v[116:119], v[52:55], a[16:19], v[116:119]
v_cvt_pk_bf16_f32 v143, v126, v127
v_mfma_f32_16x16x32_bf16 v[112:115], v[56:59], a[20:23], v[112:115]
buffer_store_dwordx4 v[140:143], v1, s[40:43], s53 offen sc1
v_mfma_f32_16x16x32_bf16 v[116:119], v[60:63], a[20:23], v[116:119]
s_mov_b32 s52, s63
s_lshr_b32 s59, s52, 8
s_and_b32 s59, s59, 0x2000
s_add_u32 s53, s52, s59
v_mfma_f32_16x16x32_bf16 v[120:123], v[16:19], a[24:27], 0
v_mfma_f32_16x16x32_bf16 v[124:127], v[20:23], a[24:27], 0
v_mfma_f32_16x16x32_bf16 v[120:123], v[24:27], a[28:31], v[120:123]
v_mfma_f32_16x16x32_bf16 v[124:127], v[28:31], a[28:31], v[124:127]
v_mfma_f32_16x16x32_bf16 v[120:123], v[32:35], a[32:35], v[120:123]
v_mfma_f32_16x16x32_bf16 v[124:127], v[36:39], a[32:35], v[124:127]
v_mfma_f32_16x16x32_bf16 v[120:123], v[40:43], a[36:39], v[120:123]
v_cvt_pk_bf16_f32 v136, v112, v113
v_mfma_f32_16x16x32_bf16 v[124:127], v[44:47], a[36:39], v[124:127]
v_cvt_pk_bf16_f32 v137, v114, v115
v_mfma_f32_16x16x32_bf16 v[120:123], v[48:51], a[40:43], v[120:123]
v_cvt_pk_bf16_f32 v138, v116, v117
v_mfma_f32_16x16x32_bf16 v[124:127], v[52:55], a[40:43], v[124:127]
v_cvt_pk_bf16_f32 v139, v118, v119
v_mfma_f32_16x16x32_bf16 v[120:123], v[56:59], a[44:47], v[120:123]
buffer_store_dwordx4 v[136:139], v1, s[40:43], s53 offen sc1
v_mfma_f32_16x16x32_bf16 v[124:127], v[60:63], a[44:47], v[124:127]
s_add_u32 s52, s52, 65536
s_lshr_b32 s59, s52, 8
s_and_b32 s59, s59, 0x2000
s_add_u32 s53, s52, s59
v_mfma_f32_16x16x32_bf16 v[112:115], v[16:19], a[48:51], 0
v_mfma_f32_16x16x32_bf16 v[116:119], v[20:23], a[48:51], 0
v_mfma_f32_16x16x32_bf16 v[112:115], v[24:27], a[52:55], v[112:115]
v_mfma_f32_16x16x32_bf16 v[116:119], v[28:31], a[52:55], v[116:119]
v_mfma_f32_16x16x32_bf16 v[112:115], v[32:35], a[56:59], v[112:115]
v_mfma_f32_16x16x32_bf16 v[116:119], v[36:39], a[56:59], v[116:119]
v_mfma_f32_16x16x32_bf16 v[112:115], v[40:43], a[60:63], v[112:115]
v_cvt_pk_bf16_f32 v140, v120, v121
v_mfma_f32_16x16x32_bf16 v[116:119], v[44:47], a[60:63], v[116:119]
v_cvt_pk_bf16_f32 v141, v122, v123
v_mfma_f32_16x16x32_bf16 v[112:115], v[48:51], a[64:67], v[112:115]
v_cvt_pk_bf16_f32 v142, v124, v125
v_mfma_f32_16x16x32_bf16 v[116:119], v[52:55], a[64:67], v[116:119]
v_cvt_pk_bf16_f32 v143, v126, v127
v_mfma_f32_16x16x32_bf16 v[112:115], v[56:59], a[68:71], v[112:115]
buffer_store_dwordx4 v[140:143], v1, s[40:43], s53 offen sc1
v_mfma_f32_16x16x32_bf16 v[116:119], v[60:63], a[68:71], v[116:119]
s_add_u32 s52, s52, 65536
s_lshr_b32 s59, s52, 8
s_and_b32 s59, s59, 0x2000
s_add_u32 s53, s52, s59
v_cmp_gt_u32 vcc, s55, v5
s_cbranch_vccz hpo_known0_%=
s_waitcnt vmcnt(3)
v_cmp_gt_u32 vcc, s55, v6
hpo_known0_%=:
s_nop 0
s_cmp_eq_u64 vcc, 0
s_cselect_b32 s56, s50, 0x40000000
v_mfma_f32_16x16x32_bf16 v[120:123], v[16:19], a[72:75], 0
buffer_load_dwordx4 v[144:147], v0, s[40:43], s56 offen offset:0 sc1
v_mfma_f32_16x16x32_bf16 v[124:127], v[20:23], a[72:75], 0
buffer_load_dwordx4 v[148:151], v0, s[40:43], s56 offen offset:1024 sc1
v_mfma_f32_16x16x32_bf16 v[120:123], v[24:27], a[76:79], v[120:123]
buffer_load_dwordx4 v[152:155], v0, s[40:43], s56 offen offset:2048 sc1
v_mfma_f32_16x16x32_bf16 v[124:127], v[28:31], a[76:79], v[124:127]
v_mfma_f32_16x16x32_bf16 v[120:123], v[32:35], a[80:83], v[120:123]
v_mfma_f32_16x16x32_bf16 v[124:127], v[36:39], a[80:83], v[124:127]
v_mfma_f32_16x16x32_bf16 v[120:123], v[40:43], a[84:87], v[120:123]
v_cvt_pk_bf16_f32 v136, v112, v113
v_mfma_f32_16x16x32_bf16 v[124:127], v[44:47], a[84:87], v[124:127]
v_cvt_pk_bf16_f32 v137, v114, v115
v_mfma_f32_16x16x32_bf16 v[120:123], v[48:51], a[88:91], v[120:123]
v_cvt_pk_bf16_f32 v138, v116, v117
v_mfma_f32_16x16x32_bf16 v[124:127], v[52:55], a[88:91], v[124:127]
v_cvt_pk_bf16_f32 v139, v118, v119
v_mfma_f32_16x16x32_bf16 v[120:123], v[56:59], a[92:95], v[120:123]
buffer_store_dwordx4 v[136:139], v1, s[40:43], s53 offen sc1
v_mfma_f32_16x16x32_bf16 v[124:127], v[60:63], a[92:95], v[124:127]
s_add_u32 s52, s52, 65536
s_lshr_b32 s59, s52, 8
s_and_b32 s59, s59, 0x2000
s_add_u32 s53, s52, s59
v_mfma_f32_16x16x32_bf16 v[112:115], v[16:19], a[96:99], 0
v_mfma_f32_16x16x32_bf16 v[116:119], v[20:23], a[96:99], 0
v_mfma_f32_16x16x32_bf16 v[112:115], v[24:27], a[100:103], v[112:115]
v_mfma_f32_16x16x32_bf16 v[116:119], v[28:31], a[100:103], v[116:119]
v_mfma_f32_16x16x32_bf16 v[112:115], v[32:35], a[104:107], v[112:115]
v_mfma_f32_16x16x32_bf16 v[116:119], v[36:39], a[104:107], v[116:119]
v_mfma_f32_16x16x32_bf16 v[112:115], v[40:43], a[108:111], v[112:115]
v_cvt_pk_bf16_f32 v140, v120, v121
v_mfma_f32_16x16x32_bf16 v[116:119], v[44:47], a[108:111], v[116:119]
v_cvt_pk_bf16_f32 v141, v122, v123
v_mfma_f32_16x16x32_bf16 v[112:115], v[48:51], a[112:115], v[112:115]
v_cvt_pk_bf16_f32 v142, v124, v125
v_mfma_f32_16x16x32_bf16 v[116:119], v[52:55], a[112:115], v[116:119]
v_cvt_pk_bf16_f32 v143, v126, v127
v_mfma_f32_16x16x32_bf16 v[112:115], v[56:59], a[116:119], v[112:115]
buffer_store_dwordx4 v[140:143], v1, s[40:43], s53 offen sc1
v_mfma_f32_16x16x32_bf16 v[116:119], v[60:63], a[116:119], v[116:119]
s_add_u32 s52, s52, 65536
s_lshr_b32 s59, s52, 8
s_and_b32 s59, s59, 0x2000
s_add_u32 s53, s52, s59
v_mfma_f32_16x16x32_bf16 v[120:123], v[16:19], a[120:123], 0
v_mfma_f32_16x16x32_bf16 v[124:127], v[20:23], a[120:123], 0
v_mfma_f32_16x16x32_bf16 v[120:123], v[24:27], a[124:127], v[120:123]
v_mfma_f32_16x16x32_bf16 v[124:127], v[28:31], a[124:127], v[124:127]
v_mfma_f32_16x16x32_bf16 v[120:123], v[32:35], a[128:131], v[120:123]
v_mfma_f32_16x16x32_bf16 v[124:127], v[36:39], a[128:131], v[124:127]
v_mfma_f32_16x16x32_bf16 v[120:123], v[40:43], a[132:135], v[120:123]
v_cvt_pk_bf16_f32 v136, v112, v113
v_mfma_f32_16x16x32_bf16 v[124:127], v[44:47], a[132:135], v[124:127]
v_cvt_pk_bf16_f32 v137, v114, v115
v_mfma_f32_16x16x32_bf16 v[120:123], v[48:51], a[136:139], v[120:123]
v_cvt_pk_bf16_f32 v138, v116, v117
v_mfma_f32_16x16x32_bf16 v[124:127], v[52:55], a[136:139], v[124:127]
v_cvt_pk_bf16_f32 v139, v118, v119
v_mfma_f32_16x16x32_bf16 v[120:123], v[56:59], a[140:143], v[120:123]
buffer_store_dwordx4 v[136:139], v1, s[40:43], s53 offen sc1
v_mfma_f32_16x16x32_bf16 v[124:127], v[60:63], a[140:143], v[124:127]
s_add_u32 s52, s52, 65536
s_lshr_b32 s59, s52, 8
s_and_b32 s59, s59, 0x2000
s_add_u32 s53, s52, s59
v_mfma_f32_16x16x32_bf16 v[112:115], v[16:19], a[144:147], 0
v_mfma_f32_16x16x32_bf16 v[116:119], v[20:23], a[144:147], 0
v_mfma_f32_16x16x32_bf16 v[112:115], v[24:27], a[148:151], v[112:115]
v_mfma_f32_16x16x32_bf16 v[116:119], v[28:31], a[148:151], v[116:119]
v_mfma_f32_16x16x32_bf16 v[112:115], v[32:35], a[152:155], v[112:115]
v_mfma_f32_16x16x32_bf16 v[116:119], v[36:39], a[152:155], v[116:119]
v_mfma_f32_16x16x32_bf16 v[112:115], v[40:43], a[156:159], v[112:115]
v_cvt_pk_bf16_f32 v140, v120, v121
v_mfma_f32_16x16x32_bf16 v[116:119], v[44:47], a[156:159], v[116:119]
v_cvt_pk_bf16_f32 v141, v122, v123
v_mfma_f32_16x16x32_bf16 v[112:115], v[48:51], a[160:163], v[112:115]
v_cvt_pk_bf16_f32 v142, v124, v125
v_mfma_f32_16x16x32_bf16 v[116:119], v[52:55], a[160:163], v[116:119]
v_cvt_pk_bf16_f32 v143, v126, v127
v_mfma_f32_16x16x32_bf16 v[112:115], v[56:59], a[164:167], v[112:115]
buffer_store_dwordx4 v[140:143], v1, s[40:43], s53 offen sc1
v_mfma_f32_16x16x32_bf16 v[116:119], v[60:63], a[164:167], v[116:119]
s_add_u32 s52, s52, 65536
s_lshr_b32 s59, s52, 8
s_and_b32 s59, s59, 0x2000
s_add_u32 s53, s52, s59
s_waitcnt vmcnt(4)
s_cmp_eq_u32 s56, s50
s_cbranch_scc1 hpo_have0_%=
v_cmp_gt_u32 vcc, s55, v6
s_cbranch_vccz hpo_v10_ok_%=
s_mov_b32 s60, 0
s_nop 2
hpo_v10_poll_%=:
buffer_load_dword v6, v7, s[40:43], s58 offen sc1
s_waitcnt vmcnt(0)
v_cmp_gt_u32 vcc, s55, v6
s_nop 4
s_cbranch_vccz hpo_v10_ok_%=
s_sleep 2
s_add_u32 s60, s60, 1
s_cmp_gt_u32 s60, 65536
s_cbranch_scc1 hp_abort_%=
s_and_b32 s59, s60, 63
s_cbranch_scc1 hpo_v10_poll_%=
v_mov_b32 v4, 1024
buffer_load_dword v4, v4, s[40:43], s58 offen sc1
s_waitcnt vmcnt(0)
v_readfirstlane_b32 s59, v4
s_nop 0
s_cmp_lg_u32 s59, 0
s_nop 2
s_cbranch_scc1 hp_end_%=
s_branch hpo_v10_poll_%=
hpo_v10_ok_%=:
buffer_load_dwordx4 v[144:147], v0, s[40:43], s50 offen offset:0 sc1
buffer_load_dwordx4 v[148:151], v0, s[40:43], s50 offen offset:1024 sc1
buffer_load_dwordx4 v[152:155], v0, s[40:43], s50 offen offset:2048 sc1
s_waitcnt vmcnt(0)
hpo_have0_%=:
v_mov_b32 v5, v6
v_mfma_f32_16x16x32_bf16 v[120:123], v[16:19], a[168:171], 0
ds_write_b128 v8, v[144:147] offset:12288
v_mfma_f32_16x16x32_bf16 v[124:127], v[20:23], a[168:171], 0
ds_write_b128 v8, v[148:151] offset:13312
v_mfma_f32_16x16x32_bf16 v[120:123], v[24:27], a[172:175], v[120:123]
ds_write2st64_b64 v9, v[152:153], v[154:155] offset0:24 offset1:26
v_mfma_f32_16x16x32_bf16 v[124:127], v[28:31], a[172:175], v[124:127]
v_mfma_f32_16x16x32_bf16 v[120:123], v[32:35], a[176:179], v[120:123]
v_mfma_f32_16x16x32_bf16 v[124:127], v[36:39], a[176:179], v[124:127]
v_mfma_f32_16x16x32_bf16 v[120:123], v[40:43], a[180:183], v[120:123]
v_cvt_pk_bf16_f32 v136, v112, v113
v_mfma_f32_16x16x32_bf16 v[124:127], v[44:47], a[180:183], v[124:127]
v_cvt_pk_bf16_f32 v137, v114, v115
v_mfma_f32_16x16x32_bf16 v[120:123], v[48:51], a[184:187], v[120:123]
v_cvt_pk_bf16_f32 v138, v116, v117
v_mfma_f32_16x16x32_bf16 v[124:127], v[52:55], a[184:187], v[124:127]
v_cvt_pk_bf16_f32 v139, v118, v119
v_mfma_f32_16x16x32_bf16 v[120:123], v[56:59], a[188:191], v[120:123]
)"
R"(buffer_store_dwordx4 v[136:139], v1, s[40:43], s53 offen sc1
v_mfma_f32_16x16x32_bf16 v[124:127], v[60:63], a[188:191], v[124:127]
s_add_u32 s52, s52, 65536
s_lshr_b32 s59, s52, 8
s_and_b32 s59, s59, 0x2000
s_add_u32 s53, s52, s59
s_waitcnt lgkmcnt(0)
s_barrier
buffer_atomic_add v12, v10, s[40:43], s65 offen
buffer_atomic_add v12, v2, s[40:43], s64 offen
v_mfma_f32_16x16x32_bf16 v[112:115], v[16:19], a[192:195], 0
ds_read_b128 v[64:67], v0 offset:12288
v_mfma_f32_16x16x32_bf16 v[116:119], v[20:23], a[192:195], 0
ds_read_b128 v[68:71], v0 offset:13312
v_mfma_f32_16x16x32_bf16 v[112:115], v[24:27], a[196:199], v[112:115]
ds_read_b128 v[72:75], v0 offset:14336
v_mfma_f32_16x16x32_bf16 v[116:119], v[28:31], a[196:199], v[116:119]
ds_read_b128 v[76:79], v0 offset:15360
v_mfma_f32_16x16x32_bf16 v[112:115], v[32:35], a[200:203], v[112:115]
ds_read_b128 v[80:83], v0 offset:16384
v_mfma_f32_16x16x32_bf16 v[116:119], v[36:39], a[200:203], v[116:119]
ds_read_b128 v[84:87], v0 offset:17408
v_mfma_f32_16x16x32_bf16 v[112:115], v[40:43], a[204:207], v[112:115]
v_cvt_pk_bf16_f32 v140, v120, v121
v_mfma_f32_16x16x32_bf16 v[116:119], v[44:47], a[204:207], v[116:119]
v_cvt_pk_bf16_f32 v141, v122, v123
v_mfma_f32_16x16x32_bf16 v[112:115], v[48:51], a[208:211], v[112:115]
v_cvt_pk_bf16_f32 v142, v124, v125
v_mfma_f32_16x16x32_bf16 v[116:119], v[52:55], a[208:211], v[116:119]
v_cvt_pk_bf16_f32 v143, v126, v127
v_mfma_f32_16x16x32_bf16 v[112:115], v[56:59], a[212:215], v[112:115]
buffer_store_dwordx4 v[140:143], v1, s[40:43], s53 offen sc1
v_mfma_f32_16x16x32_bf16 v[116:119], v[60:63], a[212:215], v[116:119]
s_add_u32 s52, s52, 65536
s_lshr_b32 s59, s52, 8
s_and_b32 s59, s59, 0x2000
s_add_u32 s53, s52, s59
v_mfma_f32_16x16x32_bf16 v[120:123], v[16:19], a[216:219], 0
ds_read_b128 v[88:91], v0 offset:18432
v_mfma_f32_16x16x32_bf16 v[124:127], v[20:23], a[216:219], 0
ds_read_b128 v[92:95], v0 offset:19456
v_mfma_f32_16x16x32_bf16 v[120:123], v[24:27], a[220:223], v[120:123]
ds_read_b128 v[96:99], v0 offset:20480
v_mfma_f32_16x16x32_bf16 v[124:127], v[28:31], a[220:223], v[124:127]
ds_read_b128 v[100:103], v0 offset:21504
v_mfma_f32_16x16x32_bf16 v[120:123], v[32:35], a[224:227], v[120:123]
ds_read_b128 v[104:107], v0 offset:22528
v_mfma_f32_16x16x32_bf16 v[124:127], v[36:39], a[224:227], v[124:127]
ds_read_b128 v[108:111], v0 offset:23552
v_mfma_f32_16x16x32_bf16 v[120:123], v[40:43], a[228:231], v[120:123]
v_cvt_pk_bf16_f32 v136, v112, v113
v_mfma_f32_16x16x32_bf16 v[124:127], v[44:47], a[228:231], v[124:127]
v_cvt_pk_bf16_f32 v137, v114, v115
v_mfma_f32_16x16x32_bf16 v[120:123], v[48:51], a[232:235], v[120:123]
v_cvt_pk_bf16_f32 v138, v116, v117
v_mfma_f32_16x16x32_bf16 v[124:127], v[52:55], a[232:235], v[124:127]
v_cvt_pk_bf16_f32 v139, v118, v119
v_mfma_f32_16x16x32_bf16 v[120:123], v[56:59], a[236:239], v[120:123]
buffer_store_dwordx4 v[136:139], v1, s[40:43], s53 offen sc1
v_mfma_f32_16x16x32_bf16 v[124:127], v[60:63], a[236:239], v[124:127]
s_add_u32 s52, s52, 65536
s_lshr_b32 s59, s52, 8
s_and_b32 s59, s59, 0x2000
s_add_u32 s53, s52, s59
s_waitcnt lgkmcnt(0)
s_add_u32 s63, s63, 2621440
s_and_b32 s59, s47, 31
s_cmp_eq_u32 s59, 31
s_cselect_b32 s63, s62, s63
s_lshl_b32 s64, s47, 7
s_add_u32 s64, s64, 2124032
s_and_b32 s57, s47, 63
s_lshr_b32 s65, s47, 5
s_add_u32 s65, s65, s57
s_lshl_b32 s65, s65, 10
s_add_u32 s65, s65, 2380032
s_cmp_lt_u32 s57, 2
s_cselect_b32 s65, s65, 0x40000000
s_add_u32 s47, s47, 1
s_add_u32 s55, s47, 2
s_min_u32 s55, s55, s61
s_add_u32 s59, s47, 0
s_and_b32 s59, s59, 63
s_cmp_eq_u32 s59, 31
s_cselect_b32 s59, -31, 1
s_mul_i32 s57, s59, 393216
s_add_u32 s50, s50, s57
v_mfma_f32_16x16x32_bf16 v[112:115], v[64:67], a[0:3], 0
buffer_load_dword v6, v7, s[40:43], s58 offen sc1
v_mfma_f32_16x16x32_bf16 v[116:119], v[68:71], a[0:3], 0
buffer_load_dword v14, v13, s[40:43], s58 offen sc1
v_mfma_f32_16x16x32_bf16 v[112:115], v[72:75], a[4:7], v[112:115]
v_mfma_f32_16x16x32_bf16 v[116:119], v[76:79], a[4:7], v[116:119]
v_mfma_f32_16x16x32_bf16 v[112:115], v[80:83], a[8:11], v[112:115]
v_mfma_f32_16x16x32_bf16 v[116:119], v[84:87], a[8:11], v[116:119]
v_mfma_f32_16x16x32_bf16 v[112:115], v[88:91], a[12:15], v[112:115]
v_cvt_pk_bf16_f32 v140, v120, v121
v_mfma_f32_16x16x32_bf16 v[116:119], v[92:95], a[12:15], v[116:119]
v_cvt_pk_bf16_f32 v141, v122, v123
v_mfma_f32_16x16x32_bf16 v[112:115], v[96:99], a[16:19], v[112:115]
v_cvt_pk_bf16_f32 v142, v124, v125
v_mfma_f32_16x16x32_bf16 v[116:119], v[100:103], a[16:19], v[116:119]
v_cvt_pk_bf16_f32 v143, v126, v127
v_mfma_f32_16x16x32_bf16 v[112:115], v[104:107], a[20:23], v[112:115]
buffer_store_dwordx4 v[140:143], v1, s[40:43], s53 offen sc1
v_mfma_f32_16x16x32_bf16 v[116:119], v[108:111], a[20:23], v[116:119]
s_mov_b32 s52, s63
s_lshr_b32 s59, s52, 8
s_and_b32 s59, s59, 0x2000
s_add_u32 s53, s52, s59
v_mfma_f32_16x16x32_bf16 v[120:123], v[64:67], a[24:27], 0
v_mfma_f32_16x16x32_bf16 v[124:127], v[68:71], a[24:27], 0
v_mfma_f32_16x16x32_bf16 v[120:123], v[72:75], a[28:31], v[120:123]
v_mfma_f32_16x16x32_bf16 v[124:127], v[76:79], a[28:31], v[124:127]
v_mfma_f32_16x16x32_bf16 v[120:123], v[80:83], a[32:35], v[120:123]
v_mfma_f32_16x16x32_bf16 v[124:127], v[84:87], a[32:35], v[124:127]
v_mfma_f32_16x16x32_bf16 v[120:123], v[88:91], a[36:39], v[120:123]
v_cvt_pk_bf16_f32 v136, v112, v113
v_mfma_f32_16x16x32_bf16 v[124:127], v[92:95], a[36:39], v[124:127]
v_cvt_pk_bf16_f32 v137, v114, v115
v_mfma_f32_16x16x32_bf16 v[120:123], v[96:99], a[40:43], v[120:123]
v_cvt_pk_bf16_f32 v138, v116, v117
v_mfma_f32_16x16x32_bf16 v[124:127], v[100:103], a[40:43], v[124:127]
v_cvt_pk_bf16_f32 v139, v118, v119
v_mfma_f32_16x16x32_bf16 v[120:123], v[104:107], a[44:47], v[120:123]
buffer_store_dwordx4 v[136:139], v1, s[40:43], s53 offen sc1
v_mfma_f32_16x16x32_bf16 v[124:127], v[108:111], a[44:47], v[124:127]
s_add_u32 s52, s52, 65536
s_lshr_b32 s59, s52, 8
s_and_b32 s59, s59, 0x2000
s_add_u32 s53, s52, s59
v_mfma_f32_16x16x32_bf16 v[112:115], v[64:67], a[48:51], 0
v_mfma_f32_16x16x32_bf16 v[116:119], v[68:71], a[48:51], 0
v_mfma_f32_16x16x32_bf16 v[112:115], v[72:75], a[52:55], v[112:115]
v_mfma_f32_16x16x32_bf16 v[116:119], v[76:79], a[52:55], v[116:119]
v_mfma_f32_16x16x32_bf16 v[112:115], v[80:83], a[56:59], v[112:115]
v_mfma_f32_16x16x32_bf16 v[116:119], v[84:87], a[56:59], v[116:119]
v_mfma_f32_16x16x32_bf16 v[112:115], v[88:91], a[60:63], v[112:115]
v_cvt_pk_bf16_f32 v140, v120, v121
v_mfma_f32_16x16x32_bf16 v[116:119], v[92:95], a[60:63], v[116:119]
v_cvt_pk_bf16_f32 v141, v122, v123
v_mfma_f32_16x16x32_bf16 v[112:115], v[96:99], a[64:67], v[112:115]
v_cvt_pk_bf16_f32 v142, v124, v125
v_mfma_f32_16x16x32_bf16 v[116:119], v[100:103], a[64:67], v[116:119]
v_cvt_pk_bf16_f32 v143, v126, v127
v_mfma_f32_16x16x32_bf16 v[112:115], v[104:107], a[68:71], v[112:115]
buffer_store_dwordx4 v[140:143], v1, s[40:43], s53 offen sc1
v_mfma_f32_16x16x32_bf16 v[116:119], v[108:111], a[68:71], v[116:119]
s_add_u32 s52, s52, 65536
s_lshr_b32 s59, s52, 8
s_and_b32 s59, s59, 0x2000
s_add_u32 s53, s52, s59
v_cmp_gt_u32 vcc, s55, v5
s_cbranch_vccz hpo_known1_%=
s_waitcnt vmcnt(3)
v_cmp_gt_u32 vcc, s55, v6
hpo_known1_%=:
s_nop 0
s_cmp_eq_u64 vcc, 0
s_cselect_b32 s56, s50, 0x40000000
v_mfma_f32_16x16x32_bf16 v[120:123], v[64:67], a[72:75], 0
buffer_load_dwordx4 v[144:147], v0, s[40:43], s56 offen offset:0 sc1
v_mfma_f32_16x16x32_bf16 v[124:127], v[68:71], a[72:75], 0
buffer_load_dwordx4 v[148:151], v0, s[40:43], s56 offen offset:1024 sc1
v_mfma_f32_16x16x32_bf16 v[120:123], v[72:75], a[76:79], v[120:123]
buffer_load_dwordx4 v[152:155], v0, s[40:43], s56 offen offset:2048 sc1
v_mfma_f32_16x16x32_bf16 v[124:127], v[76:79], a[76:79], v[124:127]
v_mfma_f32_16x16x32_bf16 v[120:123], v[80:83], a[80:83], v[120:123]
v_mfma_f32_16x16x32_bf16 v[124:127], v[84:87], a[80:83], v[124:127]
v_mfma_f32_16x16x32_bf16 v[120:123], v[88:91], a[84:87], v[120:123]
v_cvt_pk_bf16_f32 v136, v112, v113
v_mfma_f32_16x16x32_bf16 v[124:127], v[92:95], a[84:87], v[124:127]
v_cvt_pk_bf16_f32 v137, v114, v115
v_mfma_f32_16x16x32_bf16 v[120:123], v[96:99], a[88:91], v[120:123]
v_cvt_pk_bf16_f32 v138, v116, v117
v_mfma_f32_16x16x32_bf16 v[124:127], v[100:103], a[88:91], v[124:127]
v_cvt_pk_bf16_f32 v139, v118, v119
v_mfma_f32_16x16x32_bf16 v[120:123], v[104:107], a[92:95], v[120:123]
buffer_store_dwordx4 v[136:139], v1, s[40:43], s53 offen sc1
v_mfma_f32_16x16x32_bf16 v[124:127], v[108:111], a[92:95], v[124:127]
s_add_u32 s52, s52, 65536
s_lshr_b32 s59, s52, 8
s_and_b32 s59, s59, 0x2000
s_add_u32 s53, s52, s59
v_mfma_f32_16x16x32_bf16 v[112:115], v[64:67], a[96:99], 0
v_mfma_f32_16x16x32_bf16 v[116:119], v[68:71], a[96:99], 0
v_mfma_f32_16x16x32_bf16 v[112:115], v[72:75], a[100:103], v[112:115]
v_mfma_f32_16x16x32_bf16 v[116:119], v[76:79], a[100:103], v[116:119]
v_mfma_f32_16x16x32_bf16 v[112:115], v[80:83], a[104:107], v[112:115]
v_mfma_f32_16x16x32_bf16 v[116:119], v[84:87], a[104:107], v[116:119]
v_mfma_f32_16x16x32_bf16 v[112:115], v[88:91], a[108:111], v[112:115]
v_cvt_pk_bf16_f32 v140, v120, v121
v_mfma_f32_16x16x32_bf16 v[116:119], v[92:95], a[108:111], v[116:119]
v_cvt_pk_bf16_f32 v141, v122, v123
v_mfma_f32_16x16x32_bf16 v[112:115], v[96:99], a[112:115], v[112:115]
v_cvt_pk_bf16_f32 v142, v124, v125
v_mfma_f32_16x16x32_bf16 v[116:119], v[100:103], a[112:115], v[116:119]
v_cvt_pk_bf16_f32 v143, v126, v127
v_mfma_f32_16x16x32_bf16 v[112:115], v[104:107], a[116:119], v[112:115]
buffer_store_dwordx4 v[140:143], v1, s[40:43], s53 offen sc1
)"
R"(v_mfma_f32_16x16x32_bf16 v[116:119], v[108:111], a[116:119], v[116:119]
s_add_u32 s52, s52, 65536
s_lshr_b32 s59, s52, 8
s_and_b32 s59, s59, 0x2000
s_add_u32 s53, s52, s59
v_mfma_f32_16x16x32_bf16 v[120:123], v[64:67], a[120:123], 0
v_mfma_f32_16x16x32_bf16 v[124:127], v[68:71], a[120:123], 0
v_mfma_f32_16x16x32_bf16 v[120:123], v[72:75], a[124:127], v[120:123]
v_mfma_f32_16x16x32_bf16 v[124:127], v[76:79], a[124:127], v[124:127]
v_mfma_f32_16x16x32_bf16 v[120:123], v[80:83], a[128:131], v[120:123]
v_mfma_f32_16x16x32_bf16 v[124:127], v[84:87], a[128:131], v[124:127]
v_mfma_f32_16x16x32_bf16 v[120:123], v[88:91], a[132:135], v[120:123]
v_cvt_pk_bf16_f32 v136, v112, v113
v_mfma_f32_16x16x32_bf16 v[124:127], v[92:95], a[132:135], v[124:127]
v_cvt_pk_bf16_f32 v137, v114, v115
v_mfma_f32_16x16x32_bf16 v[120:123], v[96:99], a[136:139], v[120:123]
v_cvt_pk_bf16_f32 v138, v116, v117
v_mfma_f32_16x16x32_bf16 v[124:127], v[100:103], a[136:139], v[124:127]
v_cvt_pk_bf16_f32 v139, v118, v119
v_mfma_f32_16x16x32_bf16 v[120:123], v[104:107], a[140:143], v[120:123]
buffer_store_dwordx4 v[136:139], v1, s[40:43], s53 offen sc1
v_mfma_f32_16x16x32_bf16 v[124:127], v[108:111], a[140:143], v[124:127]
s_add_u32 s52, s52, 65536
s_lshr_b32 s59, s52, 8
s_and_b32 s59, s59, 0x2000
s_add_u32 s53, s52, s59
v_mfma_f32_16x16x32_bf16 v[112:115], v[64:67], a[144:147], 0
v_mfma_f32_16x16x32_bf16 v[116:119], v[68:71], a[144:147], 0
v_mfma_f32_16x16x32_bf16 v[112:115], v[72:75], a[148:151], v[112:115]
v_mfma_f32_16x16x32_bf16 v[116:119], v[76:79], a[148:151], v[116:119]
v_mfma_f32_16x16x32_bf16 v[112:115], v[80:83], a[152:155], v[112:115]
v_mfma_f32_16x16x32_bf16 v[116:119], v[84:87], a[152:155], v[116:119]
v_mfma_f32_16x16x32_bf16 v[112:115], v[88:91], a[156:159], v[112:115]
v_cvt_pk_bf16_f32 v140, v120, v121
v_mfma_f32_16x16x32_bf16 v[116:119], v[92:95], a[156:159], v[116:119]
v_cvt_pk_bf16_f32 v141, v122, v123
v_mfma_f32_16x16x32_bf16 v[112:115], v[96:99], a[160:163], v[112:115]
v_cvt_pk_bf16_f32 v142, v124, v125
v_mfma_f32_16x16x32_bf16 v[116:119], v[100:103], a[160:163], v[116:119]
v_cvt_pk_bf16_f32 v143, v126, v127
v_mfma_f32_16x16x32_bf16 v[112:115], v[104:107], a[164:167], v[112:115]
buffer_store_dwordx4 v[140:143], v1, s[40:43], s53 offen sc1
v_mfma_f32_16x16x32_bf16 v[116:119], v[108:111], a[164:167], v[116:119]
s_add_u32 s52, s52, 65536
s_lshr_b32 s59, s52, 8
s_and_b32 s59, s59, 0x2000
s_add_u32 s53, s52, s59
s_waitcnt vmcnt(4)
s_cmp_eq_u32 s56, s50
s_cbranch_scc1 hpo_have1_%=
v_cmp_gt_u32 vcc, s55, v6
s_cbranch_vccz hpo_v11_ok_%=
s_mov_b32 s60, 0
s_nop 2
hpo_v11_poll_%=:
buffer_load_dword v6, v7, s[40:43], s58 offen sc1
s_waitcnt vmcnt(0)
v_cmp_gt_u32 vcc, s55, v6
s_nop 4
s_cbranch_vccz hpo_v11_ok_%=
s_sleep 2
s_add_u32 s60, s60, 1
s_cmp_gt_u32 s60, 65536
s_cbranch_scc1 hp_abort_%=
s_and_b32 s59, s60, 63
s_cbranch_scc1 hpo_v11_poll_%=
v_mov_b32 v4, 1024
buffer_load_dword v4, v4, s[40:43], s58 offen sc1
s_waitcnt vmcnt(0)
v_readfirstlane_b32 s59, v4
s_nop 0
s_cmp_lg_u32 s59, 0
s_nop 2
s_cbranch_scc1 hp_end_%=
s_branch hpo_v11_poll_%=
hpo_v11_ok_%=:
buffer_load_dwordx4 v[144:147], v0, s[40:43], s50 offen offset:0 sc1
buffer_load_dwordx4 v[148:151], v0, s[40:43], s50 offen offset:1024 sc1
buffer_load_dwordx4 v[152:155], v0, s[40:43], s50 offen offset:2048 sc1
s_waitcnt vmcnt(0)
hpo_have1_%=:
v_mov_b32 v5, v6
v_mfma_f32_16x16x32_bf16 v[120:123], v[64:67], a[168:171], 0
ds_write_b128 v8, v[144:147] offset:0
v_mfma_f32_16x16x32_bf16 v[124:127], v[68:71], a[168:171], 0
ds_write_b128 v8, v[148:151] offset:1024
v_mfma_f32_16x16x32_bf16 v[120:123], v[72:75], a[172:175], v[120:123]
ds_write2st64_b64 v9, v[152:153], v[154:155] offset0:0 offset1:2
v_mfma_f32_16x16x32_bf16 v[124:127], v[76:79], a[172:175], v[124:127]
v_mfma_f32_16x16x32_bf16 v[120:123], v[80:83], a[176:179], v[120:123]
v_mfma_f32_16x16x32_bf16 v[124:127], v[84:87], a[176:179], v[124:127]
v_mfma_f32_16x16x32_bf16 v[120:123], v[88:91], a[180:183], v[120:123]
v_cvt_pk_bf16_f32 v136, v112, v113
v_mfma_f32_16x16x32_bf16 v[124:127], v[92:95], a[180:183], v[124:127]
v_cvt_pk_bf16_f32 v137, v114, v115
v_mfma_f32_16x16x32_bf16 v[120:123], v[96:99], a[184:187], v[120:123]
v_cvt_pk_bf16_f32 v138, v116, v117
v_mfma_f32_16x16x32_bf16 v[124:127], v[100:103], a[184:187], v[124:127]
v_cvt_pk_bf16_f32 v139, v118, v119
v_mfma_f32_16x16x32_bf16 v[120:123], v[104:107], a[188:191], v[120:123]
buffer_store_dwordx4 v[136:139], v1, s[40:43], s53 offen sc1
v_mfma_f32_16x16x32_bf16 v[124:127], v[108:111], a[188:191], v[124:127]
s_add_u32 s52, s52, 65536
s_lshr_b32 s59, s52, 8
s_and_b32 s59, s59, 0x2000
s_add_u32 s53, s52, s59
s_waitcnt lgkmcnt(0)
s_barrier
buffer_atomic_add v12, v10, s[40:43], s65 offen
buffer_atomic_add v12, v2, s[40:43], s64 offen
v_mfma_f32_16x16x32_bf16 v[112:115], v[64:67], a[192:195], 0
ds_read_b128 v[16:19], v0 offset:0
v_mfma_f32_16x16x32_bf16 v[116:119], v[68:71], a[192:195], 0
ds_read_b128 v[20:23], v0 offset:1024
v_mfma_f32_16x16x32_bf16 v[112:115], v[72:75], a[196:199], v[112:115]
ds_read_b128 v[24:27], v0 offset:2048
v_mfma_f32_16x16x32_bf16 v[116:119], v[76:79], a[196:199], v[116:119]
ds_read_b128 v[28:31], v0 offset:3072
v_mfma_f32_16x16x32_bf16 v[112:115], v[80:83], a[200:203], v[112:115]
ds_read_b128 v[32:35], v0 offset:4096
v_mfma_f32_16x16x32_bf16 v[116:119], v[84:87], a[200:203], v[116:119]
ds_read_b128 v[36:39], v0 offset:5120
v_mfma_f32_16x16x32_bf16 v[112:115], v[88:91], a[204:207], v[112:115]
v_cvt_pk_bf16_f32 v140, v120, v121
v_mfma_f32_16x16x32_bf16 v[116:119], v[92:95], a[204:207], v[116:119]
v_cvt_pk_bf16_f32 v141, v122, v123
v_mfma_f32_16x16x32_bf16 v[112:115], v[96:99], a[208:211], v[112:115]
v_cvt_pk_bf16_f32 v142, v124, v125
v_mfma_f32_16x16x32_bf16 v[116:119], v[100:103], a[208:211], v[116:119]
v_cvt_pk_bf16_f32 v143, v126, v127
v_mfma_f32_16x16x32_bf16 v[112:115], v[104:107], a[212:215], v[112:115]
buffer_store_dwordx4 v[140:143], v1, s[40:43], s53 offen sc1
v_mfma_f32_16x16x32_bf16 v[116:119], v[108:111], a[212:215], v[116:119]
s_add_u32 s52, s52, 65536
s_lshr_b32 s59, s52, 8
s_and_b32 s59, s59, 0x2000
s_add_u32 s53, s52, s59
v_mfma_f32_16x16x32_bf16 v[120:123], v[64:67], a[216:219], 0
ds_read_b128 v[40:43], v0 offset:6144
v_mfma_f32_16x16x32_bf16 v[124:127], v[68:71], a[216:219], 0
ds_read_b128 v[44:47], v0 offset:7168
v_mfma_f32_16x16x32_bf16 v[120:123], v[72:75], a[220:223], v[120:123]
ds_read_b128 v[48:51], v0 offset:8192
v_mfma_f32_16x16x32_bf16 v[124:127], v[76:79], a[220:223], v[124:127]
ds_read_b128 v[52:55], v0 offset:9216
v_mfma_f32_16x16x32_bf16 v[120:123], v[80:83], a[224:227], v[120:123]
ds_read_b128 v[56:59], v0 offset:10240
v_mfma_f32_16x16x32_bf16 v[124:127], v[84:87], a[224:227], v[124:127]
ds_read_b128 v[60:63], v0 offset:11264
v_mfma_f32_16x16x32_bf16 v[120:123], v[88:91], a[228:231], v[120:123]
v_cvt_pk_bf16_f32 v136, v112, v113
v_mfma_f32_16x16x32_bf16 v[124:127], v[92:95], a[228:231], v[124:127]
v_cvt_pk_bf16_f32 v137, v114, v115
v_mfma_f32_16x16x32_bf16 v[120:123], v[96:99], a[232:235], v[120:123]
v_cvt_pk_bf16_f32 v138, v116, v117
v_mfma_f32_16x16x32_bf16 v[124:127], v[100:103], a[232:235], v[124:127]
v_cvt_pk_bf16_f32 v139, v118, v119
v_mfma_f32_16x16x32_bf16 v[120:123], v[104:107], a[236:239], v[120:123]
buffer_store_dwordx4 v[136:139], v1, s[40:43], s53 offen sc1
v_mfma_f32_16x16x32_bf16 v[124:127], v[108:111], a[236:239], v[124:127]
s_add_u32 s52, s52, 65536
s_lshr_b32 s59, s52, 8
s_and_b32 s59, s59, 0x2000
s_add_u32 s53, s52, s59
s_waitcnt lgkmcnt(0)
s_add_u32 s63, s63, 2621440
s_and_b32 s59, s47, 31
s_cmp_eq_u32 s59, 31
s_cselect_b32 s63, s62, s63
s_lshl_b32 s64, s47, 7
s_add_u32 s64, s64, 2124032
s_and_b32 s57, s47, 63
s_lshr_b32 s65, s47, 5
s_add_u32 s65, s65, s57
s_lshl_b32 s65, s65, 10
s_add_u32 s65, s65, 2380032
s_cmp_lt_u32 s57, 2
s_cselect_b32 s65, s65, 0x40000000
s_add_u32 s47, s47, 1
s_cmp_lg_u32 s57, 63
s_cbranch_scc1 hpo_loop_%=
v_cvt_pk_bf16_f32 v140, v120, v121
v_cvt_pk_bf16_f32 v141, v122, v123
v_cvt_pk_bf16_f32 v142, v124, v125
v_cvt_pk_bf16_f32 v143, v126, v127
buffer_store_dwordx4 v[140:143], v1, s[40:43], s53 offen sc1
s_waitcnt vmcnt(0)
s_barrier
buffer_atomic_add v12, v2, s[40:43], s64 offen
s_nop 1
s_branch hp_ret_%=
hp_rolet_%=:
s_lshr_b32 s61, s44, 2
s_and_b32 s48, s44, 3
s_add_u32 s47, s47, s48
v_and_b32 v4, 0x370, v0
v_bfe_u32 v5, v15, 3, 1
v_lshl_add_u32 v1, v5, 15, v4
v_lshl_add_u32 v2, v5, 12, v4
s_cmp_eq_u32 s45, 0
)"
R"(s_cselect_b32 s59, 1, 0
v_lshlrev_b32 v3, 7, v15
v_cmp_gt_u32 vcc, s59, v15
v_mov_b32 v4, 2147483632
s_nop 0
v_cndmask_b32 v3, v4, v3, vcc
v_mov_b32 v6, 1
s_mul_i32 s48, s45, 20480
v_add_u32 v10, s48, v0
s_add_u32 s88, s45, 0
s_min_u32 s88, s88, 9
s_lshl_b32 s48, s88, 11
v_add_u32 v11, s48, v0
s_cmp_ge_u32 s88, 5
s_cselect_b32 s48, 15, 0
s_add_u32 s88, s88, s48
s_mul_i32 s48, s61, 5
s_add_u32 s88, s88, s48
s_add_u32 s89, s45, 4
s_min_u32 s89, s89, 9
s_lshl_b32 s48, s89, 11
v_add_u32 v12, s48, v0
s_cmp_ge_u32 s89, 5
s_cselect_b32 s48, 15, 0
s_add_u32 s89, s89, s48
s_mul_i32 s48, s61, 5
s_add_u32 s89, s89, s48
s_add_u32 s90, s45, 8
s_min_u32 s90, s90, 9
s_lshl_b32 s48, s90, 11
v_add_u32 v13, s48, v0
s_cmp_ge_u32 s90, 5
s_cselect_b32 s48, 15, 0
s_add_u32 s90, s90, s48
s_mul_i32 s48, s61, 5
s_add_u32 s90, s90, s48
v_lshlrev_b32 v7, 7, v15
s_lshl_b32 s48, s61, 7
v_add_u32 v8, s48, v7
v_cmp_eq_u32 vcc, 8, v15
s_nop 1
v_cndmask_b32 v7, v7, v8, vcc
v_mov_b32 v8, 8
v_mov_b32 v9, 20
v_cndmask_b32 v8, v8, v9, vcc
v_cmp_gt_u32 vcc, 9, v15
s_nop 1
v_cndmask_b32 v8, 0, v8, vcc
v_mov_b32 v4, 2147483632
v_cndmask_b32 v7, v4, v7, vcc
v_mov_b32 v9, 32
v_cmp_eq_u32 vcc, 0, v15
s_nop 1
v_cndmask_b32 v9, 0, v9, vcc
s_mov_b32 s63, s47
s_and_b32 s47, s63, 63
s_lshl_b32 s54, s63, 7
s_add_u32 s54, s54, 2124032
s_cmp_lg_u32 s45, 0
s_cbranch_scc1 hpt_u0_ok_%=
s_mov_b32 s60, 0
hpt_u0_%=:
v_mov_b32 v4, 0
buffer_atomic_add v4, v3, s[40:43], s54 offen sc0
s_waitcnt vmcnt(0)
v_cmp_gt_u32 vcc, v9, v4
s_nop 4
s_cbranch_vccz hpt_u0_ok_%=
s_sleep 6
s_add_u32 s60, s60, 1
s_cmp_gt_u32 s60, 65536
s_cbranch_scc1 hp_abort_%=
s_and_b32 s59, s60, 63
s_cbranch_scc1 hpt_u0_%=
v_mov_b32 v4, 1024
buffer_load_dword v4, v4, s[40:43], s58 offen sc1
s_waitcnt vmcnt(0)
v_readfirstlane_b32 s59, v4
s_nop 0
s_cmp_lg_u32 s59, 0
s_nop 2
s_cbranch_scc1 hp_end_%=
s_branch hpt_u0_%=
hpt_u0_ok_%=:
s_barrier
s_mov_b32 s64, 0
s_add_u32 s50, s47, 0
s_and_b32 s50, s50, 31
s_mul_i32 s50, s50, 80
s_mul_i32 s48, s61, 20
s_add_u32 s50, s50, s48
s_lshl_b32 s50, s50, 15
s_lshl_b32 s48, s45, 12
s_and_b32 s49, s48, 0x2000
s_add_u32 s48, s48, s49
s_add_u32 s50, s50, s48
s_add_u32 s50, s50, 32964608
s_or_b32 s50, s50, s64
s_lshr_b32 s59, s50, 8
s_and_b32 s59, s59, 0x2000
s_add_u32 s30, s50, s59
s_add_u32 s50, s50, 65536
s_lshr_b32 s59, s50, 8
s_and_b32 s59, s59, 0x2000
s_add_u32 s31, s50, s59
s_add_u32 s50, s50, 65536
s_lshr_b32 s59, s50, 8
s_and_b32 s59, s59, 0x2000
s_add_u32 s34, s50, s59
s_add_u32 s50, s50, 65536
s_lshr_b32 s59, s50, 8
s_and_b32 s59, s59, 0x2000
s_add_u32 s35, s50, s59
s_add_u32 s50, s50, 65536
s_lshr_b32 s59, s50, 8
s_and_b32 s59, s59, 0x2000
s_add_u32 s36, s50, s59
s_add_u32 s50, s50, 65536
s_lshr_b32 s59, s50, 8
s_and_b32 s59, s59, 0x2000
s_add_u32 s37, s50, s59
s_add_u32 s50, s50, 65536
s_lshr_b32 s59, s50, 8
s_and_b32 s59, s59, 0x2000
s_add_u32 s38, s50, s59
s_add_u32 s50, s50, 65536
s_lshr_b32 s59, s50, 8
s_and_b32 s59, s59, 0x2000
s_add_u32 s39, s50, s59
s_add_u32 s50, s50, 65536
s_lshr_b32 s59, s50, 8
s_and_b32 s59, s59, 0x2000
s_add_u32 s96, s50, s59
s_add_u32 s50, s50, 65536
s_lshr_b32 s59, s50, 8
s_and_b32 s59, s59, 0x2000
s_add_u32 s97, s50, s59
s_add_u32 s50, s50, 65536
buffer_load_dwordx4 a[0:3], v1, s[40:43], s30 offen sc1
buffer_load_dwordx4 a[4:7], v1, s[40:43], s31 offen sc1
buffer_load_dwordx4 a[8:11], v1, s[40:43], s34 offen sc1
buffer_load_dwordx4 a[12:15], v1, s[40:43], s35 offen sc1
buffer_load_dwordx4 a[16:19], v1, s[40:43], s36 offen sc1
buffer_load_dwordx4 a[20:23], v1, s[40:43], s37 offen sc1
buffer_load_dwordx4 a[24:27], v1, s[40:43], s38 offen sc1
buffer_load_dwordx4 a[28:31], v1, s[40:43], s39 offen sc1
buffer_load_dwordx4 a[32:35], v1, s[40:43], s96 offen sc1
buffer_load_dwordx4 a[36:39], v1, s[40:43], s97 offen sc1
buffer_load_dwordx4 a[40:43], v1, s[40:43], s30 offen offset:128 sc1
buffer_load_dwordx4 a[44:47], v1, s[40:43], s31 offen offset:128 sc1
buffer_load_dwordx4 a[48:51], v1, s[40:43], s34 offen offset:128 sc1
buffer_load_dwordx4 a[52:55], v1, s[40:43], s35 offen offset:128 sc1
buffer_load_dwordx4 a[56:59], v1, s[40:43], s36 offen offset:128 sc1
buffer_load_dwordx4 a[60:63], v1, s[40:43], s37 offen offset:128 sc1
buffer_load_dwordx4 a[64:67], v1, s[40:43], s38 offen offset:128 sc1
buffer_load_dwordx4 a[68:71], v1, s[40:43], s39 offen offset:128 sc1
buffer_load_dwordx4 a[72:75], v1, s[40:43], s96 offen offset:128 sc1
buffer_load_dwordx4 a[76:79], v1, s[40:43], s97 offen offset:128 sc1
buffer_load_dwordx4 a[80:83], v1, s[40:43], s30 offen offset:1024 sc1
buffer_load_dwordx4 a[84:87], v1, s[40:43], s31 offen offset:1024 sc1
buffer_load_dwordx4 a[88:91], v1, s[40:43], s34 offen offset:1024 sc1
buffer_load_dwordx4 a[92:95], v1, s[40:43], s35 offen offset:1024 sc1
buffer_load_dwordx4 a[96:99], v1, s[40:43], s36 offen offset:1024 sc1
buffer_load_dwordx4 a[100:103], v1, s[40:43], s37 offen offset:1024 sc1
buffer_load_dwordx4 a[104:107], v1, s[40:43], s38 offen offset:1024 sc1
buffer_load_dwordx4 a[108:111], v1, s[40:43], s39 offen offset:1024 sc1
buffer_load_dwordx4 a[112:115], v1, s[40:43], s96 offen offset:1024 sc1
buffer_load_dwordx4 a[116:119], v1, s[40:43], s97 offen offset:1024 sc1
buffer_load_dwordx4 a[120:123], v1, s[40:43], s30 offen offset:1152 sc1
buffer_load_dwordx4 a[124:127], v1, s[40:43], s31 offen offset:1152 sc1
buffer_load_dwordx4 a[128:131], v1, s[40:43], s34 offen offset:1152 sc1
buffer_load_dwordx4 a[132:135], v1, s[40:43], s35 offen offset:1152 sc1
buffer_load_dwordx4 a[136:139], v1, s[40:43], s36 offen offset:1152 sc1
buffer_load_dwordx4 a[140:143], v1, s[40:43], s37 offen offset:1152 sc1
buffer_load_dwordx4 a[144:147], v1, s[40:43], s38 offen offset:1152 sc1
buffer_load_dwordx4 a[148:151], v1, s[40:43], s39 offen offset:1152 sc1
buffer_load_dwordx4 a[152:155], v1, s[40:43], s96 offen offset:1152 sc1
buffer_load_dwordx4 a[156:159], v1, s[40:43], s97 offen offset:1152 sc1
buffer_load_dwordx4 a[160:163], v1, s[40:43], s30 offen offset:2048 sc1
buffer_load_dwordx4 a[164:167], v1, s[40:43], s31 offen offset:2048 sc1
buffer_load_dwordx4 a[168:171], v1, s[40:43], s34 offen offset:2048 sc1
buffer_load_dwordx4 a[172:175], v1, s[40:43], s35 offen offset:2048 sc1
buffer_load_dwordx4 a[176:179], v1, s[40:43], s36 offen offset:2048 sc1
buffer_load_dwordx4 a[180:183], v1, s[40:43], s37 offen offset:2048 sc1
buffer_load_dwordx4 a[184:187], v1, s[40:43], s38 offen offset:2048 sc1
buffer_load_dwordx4 a[188:191], v1, s[40:43], s39 offen offset:2048 sc1
buffer_load_dwordx4 a[192:195], v1, s[40:43], s96 offen offset:2048 sc1
buffer_load_dwordx4 a[196:199], v1, s[40:43], s97 offen offset:2048 sc1
buffer_load_dwordx4 a[200:203], v1, s[40:43], s30 offen offset:2176 sc1
buffer_load_dwordx4 a[204:207], v1, s[40:43], s31 offen offset:2176 sc1
buffer_load_dwordx4 a[208:211], v1, s[40:43], s34 offen offset:2176 sc1
buffer_load_dwordx4 a[212:215], v1, s[40:43], s35 offen offset:2176 sc1
buffer_load_dwordx4 a[216:219], v1, s[40:43], s36 offen offset:2176 sc1
buffer_load_dwordx4 a[220:223], v1, s[40:43], s37 offen offset:2176 sc1
buffer_load_dwordx4 a[224:227], v1, s[40:43], s38 offen offset:2176 sc1
buffer_load_dwordx4 a[228:231], v1, s[40:43], s39 offen offset:2176 sc1
buffer_load_dwordx4 a[232:235], v1, s[40:43], s96 offen offset:2176 sc1
buffer_load_dwordx4 a[236:239], v1, s[40:43], s97 offen offset:2176 sc1
buffer_load_dwordx4 a[240:243], v1, s[40:43], s30 offen offset:3072 sc1
buffer_load_dwordx4 a[244:247], v1, s[40:43], s31 offen offset:3072 sc1
)"
R"(buffer_load_dwordx4 a[248:251], v1, s[40:43], s34 offen offset:3072 sc1
s_waitcnt vmcnt(61)
buffer_load_dwordx4 a[252:255], v1, s[40:43], s35 offen offset:3072 sc1
buffer_load_dwordx4 v[192:195], v1, s[40:43], s36 offen offset:3072 sc1
s_waitcnt vmcnt(61)
buffer_load_dwordx4 v[196:199], v1, s[40:43], s37 offen offset:3072 sc1
buffer_load_dwordx4 v[200:203], v1, s[40:43], s38 offen offset:3072 sc1
s_waitcnt vmcnt(61)
buffer_load_dwordx4 v[204:207], v1, s[40:43], s39 offen offset:3072 sc1
buffer_load_dwordx4 v[208:211], v1, s[40:43], s96 offen offset:3072 sc1
s_waitcnt vmcnt(61)
buffer_load_dwordx4 v[212:215], v1, s[40:43], s97 offen offset:3072 sc1
buffer_load_dwordx4 v[216:219], v1, s[40:43], s30 offen offset:3200 sc1
s_waitcnt vmcnt(61)
buffer_load_dwordx4 v[220:223], v1, s[40:43], s31 offen offset:3200 sc1
buffer_load_dwordx4 v[224:227], v1, s[40:43], s34 offen offset:3200 sc1
s_waitcnt vmcnt(61)
buffer_load_dwordx4 v[228:231], v1, s[40:43], s35 offen offset:3200 sc1
buffer_load_dwordx4 v[232:235], v1, s[40:43], s36 offen offset:3200 sc1
s_waitcnt vmcnt(61)
buffer_load_dwordx4 v[236:239], v1, s[40:43], s37 offen offset:3200 sc1
buffer_load_dwordx4 v[240:243], v1, s[40:43], s38 offen offset:3200 sc1
s_waitcnt vmcnt(61)
buffer_load_dwordx4 v[244:247], v1, s[40:43], s39 offen offset:3200 sc1
buffer_load_dwordx4 v[248:251], v1, s[40:43], s96 offen offset:3200 sc1
s_waitcnt vmcnt(61)
buffer_load_dwordx4 v[252:255], v1, s[40:43], s97 offen offset:3200 sc1
hpt_loop_%=:
s_and_b32 s47, s63, 63
s_cmp_lt_u32 s47, 52
s_cselect_b32 s48, 4, 0
s_cselect_b32 s64, 0, 0x40000000
s_add_u32 s54, s63, s48
s_lshl_b32 s54, s54, 7
s_add_u32 s54, s54, 2124032
s_cmp_lg_u32 s45, 0
s_cbranch_scc1 hpt_ux_ok_%=
s_mov_b32 s60, 0
hpt_ux_%=:
v_mov_b32 v4, 0
buffer_atomic_add v4, v3, s[40:43], s54 offen sc0
s_waitcnt vmcnt(0)
v_cmp_gt_u32 vcc, v9, v4
s_nop 4
s_cbranch_vccz hpt_ux_ok_%=
s_sleep 6
s_add_u32 s60, s60, 1
s_cmp_gt_u32 s60, 65536
s_cbranch_scc1 hp_abort_%=
s_and_b32 s59, s60, 63
s_cbranch_scc1 hpt_ux_%=
v_mov_b32 v4, 1024
buffer_load_dword v4, v4, s[40:43], s58 offen sc1
s_waitcnt vmcnt(0)
v_readfirstlane_b32 s59, v4
s_nop 0
s_cmp_lg_u32 s59, 0
s_nop 2
s_cbranch_scc1 hp_end_%=
s_branch hpt_ux_%=
hpt_ux_ok_%=:
s_barrier
s_add_u32 s50, s47, 4
s_and_b32 s50, s50, 31
s_mul_i32 s50, s50, 80
s_mul_i32 s48, s61, 20
s_add_u32 s50, s50, s48
s_lshl_b32 s50, s50, 15
s_lshl_b32 s48, s45, 12
s_and_b32 s49, s48, 0x2000
s_add_u32 s48, s48, s49
s_add_u32 s50, s50, s48
s_add_u32 s50, s50, 32964608
s_or_b32 s50, s50, s64
s_lshr_b32 s59, s50, 8
s_and_b32 s59, s59, 0x2000
s_add_u32 s30, s50, s59
s_add_u32 s50, s50, 65536
s_lshr_b32 s59, s50, 8
s_and_b32 s59, s59, 0x2000
s_add_u32 s31, s50, s59
s_add_u32 s50, s50, 65536
s_lshr_b32 s59, s50, 8
s_and_b32 s59, s59, 0x2000
s_add_u32 s34, s50, s59
s_add_u32 s50, s50, 65536
s_lshr_b32 s59, s50, 8
s_and_b32 s59, s59, 0x2000
s_add_u32 s35, s50, s59
s_add_u32 s50, s50, 65536
s_lshr_b32 s59, s50, 8
s_and_b32 s59, s59, 0x2000
s_add_u32 s36, s50, s59
s_add_u32 s50, s50, 65536
s_lshr_b32 s59, s50, 8
s_and_b32 s59, s59, 0x2000
s_add_u32 s37, s50, s59
s_add_u32 s50, s50, 65536
s_lshr_b32 s59, s50, 8
s_and_b32 s59, s59, 0x2000
s_add_u32 s38, s50, s59
s_add_u32 s50, s50, 65536
s_lshr_b32 s59, s50, 8
s_and_b32 s59, s59, 0x2000
s_add_u32 s39, s50, s59
s_add_u32 s50, s50, 65536
s_lshr_b32 s59, s50, 8
s_and_b32 s59, s59, 0x2000
s_add_u32 s96, s50, s59
s_add_u32 s50, s50, 65536
s_lshr_b32 s59, s50, 8
s_and_b32 s59, s59, 0x2000
s_add_u32 s97, s50, s59
s_add_u32 s50, s50, 65536
s_and_b32 s65, s47, 31
s_mul_i32 s62, s65, 80
s_mul_i32 s48, s61, 20
s_add_u32 s62, s62, s48
s_mul_i32 s54, s63, 1536
s_add_u32 s54, s54, 2157568
s_cmp_lg_u32 s45, 0
s_cbranch_scc1 hpt_pt_ok_%=
s_mov_b32 s60, 0
hpt_pt_%=:
v_mov_b32 v4, 0
buffer_atomic_add v4, v7, s[40:43], s54 offen sc0
s_waitcnt vmcnt(0)
v_cmp_gt_u32 vcc, v8, v4
s_nop 4
s_cbranch_vccz hpt_pt_ok_%=
s_sleep 6
s_add_u32 s60, s60, 1
s_cmp_gt_u32 s60, 65536
s_cbranch_scc1 hp_abort_%=
s_and_b32 s59, s60, 63
s_cbranch_scc1 hpt_pt_%=
v_mov_b32 v4, 1024
buffer_load_dword v4, v4, s[40:43], s58 offen sc1
s_waitcnt vmcnt(0)
v_readfirstlane_b32 s59, v4
s_nop 0
s_cmp_lg_u32 s59, 0
s_nop 2
s_cbranch_scc1 hp_end_%=
s_branch hpt_pt_%=
hpt_pt_ok_%=:
s_barrier
s_lshl_b32 s50, s65, 5
s_lshl_b32 s48, s45, 3
s_add_u32 s50, s50, s48
s_lshl_b32 s50, s50, 11
s_add_u32 s50, s50, 130482176
buffer_load_dwordx4 v[32:35], v0, s[40:43], s50 offen sc1
buffer_load_dwordx4 v[36:39], v0, s[40:43], s50 offen offset:1024 sc1
buffer_load_dwordx4 v[40:43], v0, s[40:43], s50 offen offset:2048 sc1
buffer_load_dwordx4 v[44:47], v0, s[40:43], s50 offen offset:3072 sc1
s_add_u32 s50, s50, 4096
buffer_load_dwordx4 v[48:51], v0, s[40:43], s50 offen sc1
buffer_load_dwordx4 v[52:55], v0, s[40:43], s50 offen offset:1024 sc1
buffer_load_dwordx4 v[56:59], v0, s[40:43], s50 offen offset:2048 sc1
buffer_load_dwordx4 v[60:63], v0, s[40:43], s50 offen offset:3072 sc1
s_add_u32 s50, s50, 4096
buffer_load_dwordx4 v[64:67], v0, s[40:43], s50 offen sc1
buffer_load_dwordx4 v[68:71], v0, s[40:43], s50 offen offset:1024 sc1
buffer_load_dwordx4 v[72:75], v0, s[40:43], s50 offen offset:2048 sc1
buffer_load_dwordx4 v[76:79], v0, s[40:43], s50 offen offset:3072 sc1
s_add_u32 s50, s50, 4096
buffer_load_dwordx4 v[80:83], v0, s[40:43], s50 offen sc1
buffer_load_dwordx4 v[84:87], v0, s[40:43], s50 offen offset:1024 sc1
buffer_load_dwordx4 v[88:91], v0, s[40:43], s50 offen offset:2048 sc1
buffer_load_dwordx4 v[92:95], v0, s[40:43], s50 offen offset:3072 sc1
s_lshl_b32 s50, s65, 14
s_lshl_b32 s48, s45, 11
s_add_u32 s50, s50, s48
s_add_u32 s50, s50, 132579328
s_add_u32 s51, s50, 8192
buffer_load_dwordx4 v[96:99], v0, s[40:43], s50 offen sc1
buffer_load_dwordx4 v[100:103], v0, s[40:43], s51 offen sc1
buffer_load_dwordx4 v[104:107], v0, s[40:43], s50 offen offset:1024 sc1
buffer_load_dwordx4 v[108:111], v0, s[40:43], s51 offen offset:1024 sc1
s_lshl_b32 s50, s62, 12
s_lshl_b32 s48, s45, 10
s_add_u32 s50, s50, s48
s_add_u32 s50, s50, 116850688
s_add_u32 s76, s50, 0
s_add_u32 s77, s50, 8192
s_add_u32 s78, s50, 16384
s_add_u32 s79, s50, 24576
s_add_u32 s80, s50, 32768
s_add_u32 s81, s50, 40960
s_add_u32 s82, s50, 49152
s_add_u32 s83, s50, 57344
s_add_u32 s84, s50, 65536
s_add_u32 s85, s50, 73728
s_mul_i32 s48, s47, 40
s_add_u32 s48, s48, s88
s_lshl_b32 s12, s48, 11
s_add_u32 s15, s12, 133103616
s_mul_i32 s48, s47, 40
s_add_u32 s48, s48, s89
)"
R"(s_lshl_b32 s13, s48, 11
s_add_u32 s16, s13, 133103616
s_mul_i32 s48, s47, 40
s_add_u32 s48, s48, s90
s_lshl_b32 s14, s48, 11
s_add_u32 s17, s14, 133103616
s_waitcnt vmcnt(19)
v_mfma_f32_16x16x32_bf16 v[112:115], a[0:3], v[32:35], 0
s_waitcnt vmcnt(18)
v_mfma_f32_16x16x32_bf16 v[152:155], a[0:3], v[36:39], 0
buffer_load_dwordx4 a[0:3], v2, s[40:43], s76 offen sc1
v_mfma_f32_16x16x32_bf16 v[116:119], a[4:7], v[32:35], 0
v_mfma_f32_16x16x32_bf16 v[156:159], a[4:7], v[36:39], 0
buffer_load_dwordx4 a[4:7], v2, s[40:43], s77 offen sc1
v_mfma_f32_16x16x32_bf16 v[120:123], a[8:11], v[32:35], 0
v_mfma_f32_16x16x32_bf16 v[160:163], a[8:11], v[36:39], 0
buffer_load_dwordx4 a[8:11], v2, s[40:43], s78 offen sc1
v_mfma_f32_16x16x32_bf16 v[124:127], a[12:15], v[32:35], 0
v_mfma_f32_16x16x32_bf16 v[164:167], a[12:15], v[36:39], 0
buffer_load_dwordx4 a[12:15], v2, s[40:43], s79 offen sc1
v_mfma_f32_16x16x32_bf16 v[128:131], a[16:19], v[32:35], 0
v_mfma_f32_16x16x32_bf16 v[168:171], a[16:19], v[36:39], 0
buffer_load_dwordx4 a[16:19], v2, s[40:43], s80 offen sc1
v_mfma_f32_16x16x32_bf16 v[132:135], a[20:23], v[32:35], 0
v_mfma_f32_16x16x32_bf16 v[172:175], a[20:23], v[36:39], 0
buffer_load_dwordx4 a[20:23], v2, s[40:43], s81 offen sc1
v_mfma_f32_16x16x32_bf16 v[136:139], a[24:27], v[32:35], 0
v_mfma_f32_16x16x32_bf16 v[176:179], a[24:27], v[36:39], 0
buffer_load_dwordx4 a[24:27], v2, s[40:43], s82 offen sc1
v_mfma_f32_16x16x32_bf16 v[140:143], a[28:31], v[32:35], 0
v_mfma_f32_16x16x32_bf16 v[180:183], a[28:31], v[36:39], 0
buffer_load_dwordx4 a[28:31], v2, s[40:43], s83 offen sc1
v_mfma_f32_16x16x32_bf16 v[144:147], a[32:35], v[32:35], 0
v_mfma_f32_16x16x32_bf16 v[184:187], a[32:35], v[36:39], 0
buffer_load_dwordx4 a[32:35], v2, s[40:43], s84 offen sc1
v_mfma_f32_16x16x32_bf16 v[148:151], a[36:39], v[32:35], 0
v_mfma_f32_16x16x32_bf16 v[188:191], a[36:39], v[36:39], 0
buffer_load_dwordx4 a[36:39], v2, s[40:43], s85 offen sc1
s_waitcnt vmcnt(27)
v_mfma_f32_16x16x32_bf16 v[112:115], a[40:43], v[40:43], v[112:115]
s_waitcnt vmcnt(26)
v_mfma_f32_16x16x32_bf16 v[152:155], a[40:43], v[44:47], v[152:155]
buffer_load_dwordx4 a[40:43], v2, s[40:43], s76 offen offset:128 sc1
v_mfma_f32_16x16x32_bf16 v[116:119], a[44:47], v[40:43], v[116:119]
v_mfma_f32_16x16x32_bf16 v[156:159], a[44:47], v[44:47], v[156:159]
buffer_load_dwordx4 a[44:47], v2, s[40:43], s77 offen offset:128 sc1
v_mfma_f32_16x16x32_bf16 v[120:123], a[48:51], v[40:43], v[120:123]
v_mfma_f32_16x16x32_bf16 v[160:163], a[48:51], v[44:47], v[160:163]
buffer_load_dwordx4 a[48:51], v2, s[40:43], s78 offen offset:128 sc1
v_mfma_f32_16x16x32_bf16 v[124:127], a[52:55], v[40:43], v[124:127]
v_mfma_f32_16x16x32_bf16 v[164:167], a[52:55], v[44:47], v[164:167]
buffer_load_dwordx4 a[52:55], v2, s[40:43], s79 offen offset:128 sc1
v_mfma_f32_16x16x32_bf16 v[128:131], a[56:59], v[40:43], v[128:131]
v_mfma_f32_16x16x32_bf16 v[168:171], a[56:59], v[44:47], v[168:171]
buffer_load_dwordx4 a[56:59], v2, s[40:43], s80 offen offset:128 sc1
v_mfma_f32_16x16x32_bf16 v[132:135], a[60:63], v[40:43], v[132:135]
v_mfma_f32_16x16x32_bf16 v[172:175], a[60:63], v[44:47], v[172:175]
buffer_load_dwordx4 a[60:63], v2, s[40:43], s81 offen offset:128 sc1
v_mfma_f32_16x16x32_bf16 v[136:139], a[64:67], v[40:43], v[136:139]
v_mfma_f32_16x16x32_bf16 v[176:179], a[64:67], v[44:47], v[176:179]
buffer_load_dwordx4 a[64:67], v2, s[40:43], s82 offen offset:128 sc1
v_mfma_f32_16x16x32_bf16 v[140:143], a[68:71], v[40:43], v[140:143]
v_mfma_f32_16x16x32_bf16 v[180:183], a[68:71], v[44:47], v[180:183]
buffer_load_dwordx4 a[68:71], v2, s[40:43], s83 offen offset:128 sc1
v_mfma_f32_16x16x32_bf16 v[144:147], a[72:75], v[40:43], v[144:147]
v_mfma_f32_16x16x32_bf16 v[184:187], a[72:75], v[44:47], v[184:187]
buffer_load_dwordx4 a[72:75], v2, s[40:43], s84 offen offset:128 sc1
v_mfma_f32_16x16x32_bf16 v[148:151], a[76:79], v[40:43], v[148:151]
v_mfma_f32_16x16x32_bf16 v[188:191], a[76:79], v[44:47], v[188:191]
buffer_load_dwordx4 a[76:79], v2, s[40:43], s85 offen offset:128 sc1
s_waitcnt vmcnt(35)
v_mfma_f32_16x16x32_bf16 v[112:115], a[80:83], v[48:51], v[112:115]
s_waitcnt vmcnt(34)
v_mfma_f32_16x16x32_bf16 v[152:155], a[80:83], v[52:55], v[152:155]
v_mfma_f32_16x16x32_bf16 v[116:119], a[84:87], v[48:51], v[116:119]
v_mfma_f32_16x16x32_bf16 v[156:159], a[84:87], v[52:55], v[156:159]
v_mfma_f32_16x16x32_bf16 v[120:123], a[88:91], v[48:51], v[120:123]
v_mfma_f32_16x16x32_bf16 v[160:163], a[88:91], v[52:55], v[160:163]
v_mfma_f32_16x16x32_bf16 v[124:127], a[92:95], v[48:51], v[124:127]
v_mfma_f32_16x16x32_bf16 v[164:167], a[92:95], v[52:55], v[164:167]
v_mfma_f32_16x16x32_bf16 v[128:131], a[96:99], v[48:51], v[128:131]
v_mfma_f32_16x16x32_bf16 v[168:171], a[96:99], v[52:55], v[168:171]
v_mfma_f32_16x16x32_bf16 v[132:135], a[100:103], v[48:51], v[132:135]
v_mfma_f32_16x16x32_bf16 v[172:175], a[100:103], v[52:55], v[172:175]
v_mfma_f32_16x16x32_bf16 v[136:139], a[104:107], v[48:51], v[136:139]
v_mfma_f32_16x16x32_bf16 v[176:179], a[104:107], v[52:55], v[176:179]
v_mfma_f32_16x16x32_bf16 v[140:143], a[108:111], v[48:51], v[140:143]
v_mfma_f32_16x16x32_bf16 v[180:183], a[108:111], v[52:55], v[180:183]
v_mfma_f32_16x16x32_bf16 v[144:147], a[112:115], v[48:51], v[144:147]
v_mfma_f32_16x16x32_bf16 v[184:187], a[112:115], v[52:55], v[184:187]
v_mfma_f32_16x16x32_bf16 v[148:151], a[116:119], v[48:51], v[148:151]
v_mfma_f32_16x16x32_bf16 v[188:191], a[116:119], v[52:55], v[188:191]
s_waitcnt vmcnt(33)
v_mfma_f32_16x16x32_bf16 v[112:115], a[120:123], v[56:59], v[112:115]
s_waitcnt vmcnt(32)
v_mfma_f32_16x16x32_bf16 v[152:155], a[120:123], v[60:63], v[152:155]
buffer_load_dwordx4 v[32:35], v0, s[40:43], s15 offen sc1
v_mfma_f32_16x16x32_bf16 v[116:119], a[124:127], v[56:59], v[116:119]
v_mfma_f32_16x16x32_bf16 v[156:159], a[124:127], v[60:63], v[156:159]
buffer_load_dwordx4 v[36:39], v0, s[40:43], s15 offen offset:1024 sc1
v_mfma_f32_16x16x32_bf16 v[120:123], a[128:131], v[56:59], v[120:123]
v_mfma_f32_16x16x32_bf16 v[160:163], a[128:131], v[60:63], v[160:163]
buffer_load_dwordx4 v[40:43], v0, s[40:43], s16 offen sc1
v_mfma_f32_16x16x32_bf16 v[124:127], a[132:135], v[56:59], v[124:127]
v_mfma_f32_16x16x32_bf16 v[164:167], a[132:135], v[60:63], v[164:167]
buffer_load_dwordx4 v[44:47], v0, s[40:43], s16 offen offset:1024 sc1
v_mfma_f32_16x16x32_bf16 v[128:131], a[136:139], v[56:59], v[128:131]
v_mfma_f32_16x16x32_bf16 v[168:171], a[136:139], v[60:63], v[168:171]
buffer_load_dwordx4 v[48:51], v0, s[40:43], s17 offen sc1
v_mfma_f32_16x16x32_bf16 v[132:135], a[140:143], v[56:59], v[132:135]
v_mfma_f32_16x16x32_bf16 v[172:175], a[140:143], v[60:63], v[172:175]
buffer_load_dwordx4 v[52:55], v0, s[40:43], s17 offen offset:1024 sc1
v_mfma_f32_16x16x32_bf16 v[136:139], a[144:147], v[56:59], v[136:139]
v_mfma_f32_16x16x32_bf16 v[176:179], a[144:147], v[60:63], v[176:179]
v_mfma_f32_16x16x32_bf16 v[140:143], a[148:151], v[56:59], v[140:143]
v_mfma_f32_16x16x32_bf16 v[180:183], a[148:151], v[60:63], v[180:183]
v_mfma_f32_16x16x32_bf16 v[144:147], a[152:155], v[56:59], v[144:147]
v_mfma_f32_16x16x32_bf16 v[184:187], a[152:155], v[60:63], v[184:187]
v_mfma_f32_16x16x32_bf16 v[148:151], a[156:159], v[56:59], v[148:151]
v_mfma_f32_16x16x32_bf16 v[188:191], a[156:159], v[60:63], v[188:191]
s_waitcnt vmcnt(37)
v_mfma_f32_16x16x32_bf16 v[112:115], a[160:163], v[64:67], v[112:115]
s_waitcnt vmcnt(36)
v_mfma_f32_16x16x32_bf16 v[152:155], a[160:163], v[68:71], v[152:155]
buffer_load_dwordx4 a[80:83], v1, s[40:43], s30 offen offset:1024 sc1
v_mfma_f32_16x16x32_bf16 v[116:119], a[164:167], v[64:67], v[116:119]
v_mfma_f32_16x16x32_bf16 v[156:159], a[164:167], v[68:71], v[156:159]
buffer_load_dwordx4 a[84:87], v1, s[40:43], s31 offen offset:1024 sc1
v_mfma_f32_16x16x32_bf16 v[120:123], a[168:171], v[64:67], v[120:123]
v_mfma_f32_16x16x32_bf16 v[160:163], a[168:171], v[68:71], v[160:163]
buffer_load_dwordx4 a[88:91], v1, s[40:43], s34 offen offset:1024 sc1
v_mfma_f32_16x16x32_bf16 v[124:127], a[172:175], v[64:67], v[124:127]
v_mfma_f32_16x16x32_bf16 v[164:167], a[172:175], v[68:71], v[164:167]
buffer_load_dwordx4 a[92:95], v1, s[40:43], s35 offen offset:1024 sc1
v_mfma_f32_16x16x32_bf16 v[128:131], a[176:179], v[64:67], v[128:131]
v_mfma_f32_16x16x32_bf16 v[168:171], a[176:179], v[68:71], v[168:171]
buffer_load_dwordx4 a[96:99], v1, s[40:43], s36 offen offset:1024 sc1
v_mfma_f32_16x16x32_bf16 v[132:135], a[180:183], v[64:67], v[132:135]
v_mfma_f32_16x16x32_bf16 v[172:175], a[180:183], v[68:71], v[172:175]
buffer_load_dwordx4 a[100:103], v1, s[40:43], s37 offen offset:1024 sc1
v_mfma_f32_16x16x32_bf16 v[136:139], a[184:187], v[64:67], v[136:139]
v_mfma_f32_16x16x32_bf16 v[176:179], a[184:187], v[68:71], v[176:179]
buffer_load_dwordx4 a[104:107], v1, s[40:43], s38 offen offset:1024 sc1
v_mfma_f32_16x16x32_bf16 v[140:143], a[188:191], v[64:67], v[140:143]
v_mfma_f32_16x16x32_bf16 v[180:183], a[188:191], v[68:71], v[180:183]
buffer_load_dwordx4 a[108:111], v1, s[40:43], s39 offen offset:1024 sc1
v_mfma_f32_16x16x32_bf16 v[144:147], a[192:195], v[64:67], v[144:147]
v_mfma_f32_16x16x32_bf16 v[184:187], a[192:195], v[68:71], v[184:187]
buffer_load_dwordx4 a[112:115], v1, s[40:43], s96 offen offset:1024 sc1
v_mfma_f32_16x16x32_bf16 v[148:151], a[196:199], v[64:67], v[148:151]
v_mfma_f32_16x16x32_bf16 v[188:191], a[196:199], v[68:71], v[188:191]
buffer_load_dwordx4 a[116:119], v1, s[40:43], s97 offen offset:1024 sc1
s_waitcnt vmcnt(45)
v_mfma_f32_16x16x32_bf16 v[112:115], a[200:203], v[72:75], v[112:115]
s_waitcnt vmcnt(44)
v_mfma_f32_16x16x32_bf16 v[152:155], a[200:203], v[76:79], v[152:155]
buffer_load_dwordx4 a[120:123], v1, s[40:43], s30 offen offset:1152 sc1
v_mfma_f32_16x16x32_bf16 v[116:119], a[204:207], v[72:75], v[116:119]
v_mfma_f32_16x16x32_bf16 v[156:159], a[204:207], v[76:79], v[156:159]
buffer_load_dwordx4 a[124:127], v1, s[40:43], s31 offen offset:1152 sc1
v_mfma_f32_16x16x32_bf16 v[120:123], a[208:211], v[72:75], v[120:123]
v_mfma_f32_16x16x32_bf16 v[160:163], a[208:211], v[76:79], v[160:163]
buffer_load_dwordx4 a[128:131], v1, s[40:43], s34 offen offset:1152 sc1
v_mfma_f32_16x16x32_bf16 v[124:127], a[212:215], v[72:75], v[124:127]
v_mfma_f32_16x16x32_bf16 v[164:167], a[212:215], v[76:79], v[164:167]
buffer_load_dwordx4 a[132:135], v1, s[40:43], s35 offen offset:1152 sc1
v_mfma_f32_16x16x32_bf16 v[128:131], a[216:219], v[72:75], v[128:131]
v_mfma_f32_16x16x32_bf16 v[168:171], a[216:219], v[76:79], v[168:171]
buffer_load_dwordx4 a[136:139], v1, s[40:43], s36 offen offset:1152 sc1
v_mfma_f32_16x16x32_bf16 v[132:135], a[220:223], v[72:75], v[132:135]
v_mfma_f32_16x16x32_bf16 v[172:175], a[220:223], v[76:79], v[172:175]
buffer_load_dwordx4 a[140:143], v1, s[40:43], s37 offen offset:1152 sc1
v_mfma_f32_16x16x32_bf16 v[136:139], a[224:227], v[72:75], v[136:139]
v_mfma_f32_16x16x32_bf16 v[176:179], a[224:227], v[76:79], v[176:179]
buffer_load_dwordx4 a[144:147], v1, s[40:43], s38 offen offset:1152 sc1
v_mfma_f32_16x16x32_bf16 v[140:143], a[228:231], v[72:75], v[140:143]
v_mfma_f32_16x16x32_bf16 v[180:183], a[228:231], v[76:79], v[180:183]
buffer_load_dwordx4 a[148:151], v1, s[40:43], s39 offen offset:1152 sc1
v_mfma_f32_16x16x32_bf16 v[144:147], a[232:235], v[72:75], v[144:147]
v_mfma_f32_16x16x32_bf16 v[184:187], a[232:235], v[76:79], v[184:187]
buffer_load_dwordx4 a[152:155], v1, s[40:43], s96 offen offset:1152 sc1
v_mfma_f32_16x16x32_bf16 v[148:151], a[236:239], v[72:75], v[148:151]
v_mfma_f32_16x16x32_bf16 v[188:191], a[236:239], v[76:79], v[188:191]
buffer_load_dwordx4 a[156:159], v1, s[40:43], s97 offen offset:1152 sc1
s_waitcnt vmcnt(53)
v_mfma_f32_16x16x32_bf16 v[112:115], a[240:243], v[80:83], v[112:115]
s_waitcnt vmcnt(52)
v_mfma_f32_16x16x32_bf16 v[152:155], a[240:243], v[84:87], v[152:155]
buffer_load_dwordx4 a[160:163], v1, s[40:43], s30 offen offset:2048 sc1
v_mfma_f32_16x16x32_bf16 v[116:119], a[244:247], v[80:83], v[116:119]
v_mfma_f32_16x16x32_bf16 v[156:159], a[244:247], v[84:87], v[156:159]
buffer_load_dwordx4 a[164:167], v1, s[40:43], s31 offen offset:2048 sc1
v_mfma_f32_16x16x32_bf16 v[120:123], a[248:251], v[80:83], v[120:123]
v_mfma_f32_16x16x32_bf16 v[160:163], a[248:251], v[84:87], v[160:163]
buffer_load_dwordx4 a[168:171], v1, s[40:43], s34 offen offset:2048 sc1
v_mfma_f32_16x16x32_bf16 v[124:127], a[252:255], v[80:83], v[124:127]
v_mfma_f32_16x16x32_bf16 v[164:167], a[252:255], v[84:87], v[164:167]
buffer_load_dwordx4 a[172:175], v1, s[40:43], s35 offen offset:2048 sc1
v_mfma_f32_16x16x32_bf16 v[128:131], v[192:195], v[80:83], v[128:131]
v_mfma_f32_16x16x32_bf16 v[168:171], v[192:195], v[84:87], v[168:171]
)"
R"(buffer_load_dwordx4 a[176:179], v1, s[40:43], s36 offen offset:2048 sc1
v_mfma_f32_16x16x32_bf16 v[132:135], v[196:199], v[80:83], v[132:135]
v_mfma_f32_16x16x32_bf16 v[172:175], v[196:199], v[84:87], v[172:175]
buffer_load_dwordx4 a[180:183], v1, s[40:43], s37 offen offset:2048 sc1
v_mfma_f32_16x16x32_bf16 v[136:139], v[200:203], v[80:83], v[136:139]
v_mfma_f32_16x16x32_bf16 v[176:179], v[200:203], v[84:87], v[176:179]
buffer_load_dwordx4 a[184:187], v1, s[40:43], s38 offen offset:2048 sc1
v_mfma_f32_16x16x32_bf16 v[140:143], v[204:207], v[80:83], v[140:143]
v_mfma_f32_16x16x32_bf16 v[180:183], v[204:207], v[84:87], v[180:183]
buffer_load_dwordx4 a[188:191], v1, s[40:43], s39 offen offset:2048 sc1
v_mfma_f32_16x16x32_bf16 v[144:147], v[208:211], v[80:83], v[144:147]
v_mfma_f32_16x16x32_bf16 v[184:187], v[208:211], v[84:87], v[184:187]
buffer_load_dwordx4 a[192:195], v1, s[40:43], s96 offen offset:2048 sc1
v_mfma_f32_16x16x32_bf16 v[148:151], v[212:215], v[80:83], v[148:151]
v_mfma_f32_16x16x32_bf16 v[188:191], v[212:215], v[84:87], v[188:191]
buffer_load_dwordx4 a[196:199], v1, s[40:43], s97 offen offset:2048 sc1
s_waitcnt vmcnt(61)
v_mfma_f32_16x16x32_bf16 v[112:115], v[216:219], v[88:91], v[112:115]
s_waitcnt vmcnt(60)
v_mfma_f32_16x16x32_bf16 v[152:155], v[216:219], v[92:95], v[152:155]
buffer_load_dwordx4 a[200:203], v1, s[40:43], s30 offen offset:2176 sc1
v_mfma_f32_16x16x32_bf16 v[116:119], v[220:223], v[88:91], v[116:119]
v_mfma_f32_16x16x32_bf16 v[156:159], v[220:223], v[92:95], v[156:159]
buffer_load_dwordx4 a[204:207], v1, s[40:43], s31 offen offset:2176 sc1
v_mfma_f32_16x16x32_bf16 v[120:123], v[224:227], v[88:91], v[120:123]
v_mfma_f32_16x16x32_bf16 v[160:163], v[224:227], v[92:95], v[160:163]
buffer_load_dwordx4 a[208:211], v1, s[40:43], s34 offen offset:2176 sc1
s_waitcnt vmcnt(61)
v_mfma_f32_16x16x32_bf16 v[124:127], v[228:231], v[88:91], v[124:127]
v_mfma_f32_16x16x32_bf16 v[164:167], v[228:231], v[92:95], v[164:167]
buffer_load_dwordx4 a[212:215], v1, s[40:43], s35 offen offset:2176 sc1
v_mfma_f32_16x16x32_bf16 v[128:131], v[232:235], v[88:91], v[128:131]
v_mfma_f32_16x16x32_bf16 v[168:171], v[232:235], v[92:95], v[168:171]
buffer_load_dwordx4 a[216:219], v1, s[40:43], s36 offen offset:2176 sc1
s_waitcnt vmcnt(61)
v_mfma_f32_16x16x32_bf16 v[132:135], v[236:239], v[88:91], v[132:135]
v_mfma_f32_16x16x32_bf16 v[172:175], v[236:239], v[92:95], v[172:175]
buffer_load_dwordx4 a[220:223], v1, s[40:43], s37 offen offset:2176 sc1
v_mfma_f32_16x16x32_bf16 v[136:139], v[240:243], v[88:91], v[136:139]
v_mfma_f32_16x16x32_bf16 v[176:179], v[240:243], v[92:95], v[176:179]
buffer_load_dwordx4 a[224:227], v1, s[40:43], s38 offen offset:2176 sc1
s_waitcnt vmcnt(61)
v_mfma_f32_16x16x32_bf16 v[140:143], v[244:247], v[88:91], v[140:143]
v_mfma_f32_16x16x32_bf16 v[180:183], v[244:247], v[92:95], v[180:183]
buffer_load_dwordx4 a[228:231], v1, s[40:43], s39 offen offset:2176 sc1
v_mfma_f32_16x16x32_bf16 v[144:147], v[248:251], v[88:91], v[144:147]
v_mfma_f32_16x16x32_bf16 v[184:187], v[248:251], v[92:95], v[184:187]
buffer_load_dwordx4 a[232:235], v1, s[40:43], s96 offen offset:2176 sc1
s_waitcnt vmcnt(61)
v_mfma_f32_16x16x32_bf16 v[148:151], v[252:255], v[88:91], v[148:151]
v_mfma_f32_16x16x32_bf16 v[188:191], v[252:255], v[92:95], v[188:191]
buffer_load_dwordx4 a[236:239], v1, s[40:43], s97 offen offset:2176 sc1
v_mfma_f32_16x16x32_bf16 v[112:115], a[0:3], v[96:99], v[112:115]
v_mfma_f32_16x16x32_bf16 v[152:155], a[0:3], v[100:103], v[152:155]
v_mfma_f32_16x16x32_bf16 v[116:119], a[4:7], v[96:99], v[116:119]
v_mfma_f32_16x16x32_bf16 v[156:159], a[4:7], v[100:103], v[156:159]
v_mfma_f32_16x16x32_bf16 v[120:123], a[8:11], v[96:99], v[120:123]
v_mfma_f32_16x16x32_bf16 v[160:163], a[8:11], v[100:103], v[160:163]
v_mfma_f32_16x16x32_bf16 v[124:127], a[12:15], v[96:99], v[124:127]
v_mfma_f32_16x16x32_bf16 v[164:167], a[12:15], v[100:103], v[164:167]
s_waitcnt vmcnt(61)
v_mfma_f32_16x16x32_bf16 v[128:131], a[16:19], v[96:99], v[128:131]
v_mfma_f32_16x16x32_bf16 v[168:171], a[16:19], v[100:103], v[168:171]
s_waitcnt vmcnt(60)
v_mfma_f32_16x16x32_bf16 v[132:135], a[20:23], v[96:99], v[132:135]
v_mfma_f32_16x16x32_bf16 v[172:175], a[20:23], v[100:103], v[172:175]
s_waitcnt vmcnt(59)
v_mfma_f32_16x16x32_bf16 v[136:139], a[24:27], v[96:99], v[136:139]
v_mfma_f32_16x16x32_bf16 v[176:179], a[24:27], v[100:103], v[176:179]
s_waitcnt vmcnt(58)
v_mfma_f32_16x16x32_bf16 v[140:143], a[28:31], v[96:99], v[140:143]
v_mfma_f32_16x16x32_bf16 v[180:183], a[28:31], v[100:103], v[180:183]
s_waitcnt vmcnt(57)
v_mfma_f32_16x16x32_bf16 v[144:147], a[32:35], v[96:99], v[144:147]
v_mfma_f32_16x16x32_bf16 v[184:187], a[32:35], v[100:103], v[184:187]
s_waitcnt vmcnt(56)
v_mfma_f32_16x16x32_bf16 v[148:151], a[36:39], v[96:99], v[148:151]
v_mfma_f32_16x16x32_bf16 v[188:191], a[36:39], v[100:103], v[188:191]
s_waitcnt vmcnt(55)
v_mfma_f32_16x16x32_bf16 v[112:115], a[40:43], v[104:107], v[112:115]
v_mfma_f32_16x16x32_bf16 v[152:155], a[40:43], v[108:111], v[152:155]
s_waitcnt vmcnt(54)
v_mfma_f32_16x16x32_bf16 v[116:119], a[44:47], v[104:107], v[116:119]
v_mfma_f32_16x16x32_bf16 v[156:159], a[44:47], v[108:111], v[156:159]
s_waitcnt vmcnt(53)
v_mfma_f32_16x16x32_bf16 v[120:123], a[48:51], v[104:107], v[120:123]
v_mfma_f32_16x16x32_bf16 v[160:163], a[48:51], v[108:111], v[160:163]
s_waitcnt vmcnt(52)
v_mfma_f32_16x16x32_bf16 v[124:127], a[52:55], v[104:107], v[124:127]
v_mfma_f32_16x16x32_bf16 v[164:167], a[52:55], v[108:111], v[164:167]
s_waitcnt vmcnt(51)
v_mfma_f32_16x16x32_bf16 v[128:131], a[56:59], v[104:107], v[128:131]
v_mfma_f32_16x16x32_bf16 v[168:171], a[56:59], v[108:111], v[168:171]
s_waitcnt vmcnt(50)
v_mfma_f32_16x16x32_bf16 v[132:135], a[60:63], v[104:107], v[132:135]
v_mfma_f32_16x16x32_bf16 v[172:175], a[60:63], v[108:111], v[172:175]
s_waitcnt vmcnt(49)
v_mfma_f32_16x16x32_bf16 v[136:139], a[64:67], v[104:107], v[136:139]
v_mfma_f32_16x16x32_bf16 v[176:179], a[64:67], v[108:111], v[176:179]
s_waitcnt vmcnt(48)
v_mfma_f32_16x16x32_bf16 v[140:143], a[68:71], v[104:107], v[140:143]
v_mfma_f32_16x16x32_bf16 v[180:183], a[68:71], v[108:111], v[180:183]
s_waitcnt vmcnt(47)
v_mfma_f32_16x16x32_bf16 v[144:147], a[72:75], v[104:107], v[144:147]
v_mfma_f32_16x16x32_bf16 v[184:187], a[72:75], v[108:111], v[184:187]
s_waitcnt vmcnt(46)
v_mfma_f32_16x16x32_bf16 v[148:151], a[76:79], v[104:107], v[148:151]
v_mfma_f32_16x16x32_bf16 v[188:191], a[76:79], v[108:111], v[188:191]
ds_write_b128 v10, v[112:115] offset:0
ds_write_b128 v10, v[116:119] offset:1024
ds_write_b128 v10, v[120:123] offset:2048
ds_write_b128 v10, v[124:127] offset:3072
ds_write_b128 v10, v[128:131] offset:4096
ds_write_b128 v10, v[132:135] offset:5120
ds_write_b128 v10, v[136:139] offset:6144
ds_write_b128 v10, v[140:143] offset:7168
ds_write_b128 v10, v[144:147] offset:8192
ds_write_b128 v10, v[148:151] offset:9216
ds_write_b128 v10, v[152:155] offset:10240
ds_write_b128 v10, v[156:159] offset:11264
ds_write_b128 v10, v[160:163] offset:12288
ds_write_b128 v10, v[164:167] offset:13312
ds_write_b128 v10, v[168:171] offset:14336
ds_write_b128 v10, v[172:175] offset:15360
s_waitcnt lgkmcnt(14)
ds_write_b128 v10, v[176:179] offset:16384
ds_write_b128 v10, v[180:183] offset:17408
s_waitcnt lgkmcnt(14)
ds_write_b128 v10, v[184:187] offset:18432
ds_write_b128 v10, v[188:191] offset:19456
s_waitcnt lgkmcnt(14)
s_waitcnt lgkmcnt(0)
s_barrier
ds_read_b128 v[112:115], v11 offset:0
ds_read_b128 v[116:119], v11 offset:20480
ds_read_b128 v[120:123], v11 offset:40960
ds_read_b128 v[124:127], v11 offset:61440
ds_read_b128 v[128:131], v11 offset:1024
ds_read_b128 v[132:135], v11 offset:21504
ds_read_b128 v[136:139], v11 offset:41984
ds_read_b128 v[140:143], v11 offset:62464
ds_read_b128 v[144:147], v12 offset:0
ds_read_b128 v[148:151], v12 offset:20480
ds_read_b128 v[152:155], v12 offset:40960
ds_read_b128 v[156:159], v12 offset:61440
ds_read_b128 v[160:163], v12 offset:1024
ds_read_b128 v[164:167], v12 offset:21504
ds_read_b128 v[168:171], v12 offset:41984
ds_read_b128 v[172:175], v12 offset:62464
s_waitcnt lgkmcnt(14)
ds_read_b128 v[176:179], v13 offset:0
ds_read_b128 v[180:183], v13 offset:20480
s_waitcnt lgkmcnt(14)
ds_read_b128 v[184:187], v13 offset:40960
ds_read_b128 v[188:191], v13 offset:61440
s_waitcnt lgkmcnt(14)
ds_read_b128 v[192:195], v13 offset:1024
ds_read_b128 v[196:199], v13 offset:21504
s_waitcnt lgkmcnt(14)
ds_read_b128 v[200:203], v13 offset:41984
ds_read_b128 v[204:207], v13 offset:62464
s_waitcnt lgkmcnt(14)
v_pk_add_f32 v[112:113], v[112:113], v[116:117]
v_pk_add_f32 v[120:121], v[120:121], v[124:125]
s_waitcnt vmcnt(45)
v_pk_add_f32 v[32:33], v[32:33], v[112:113]
v_pk_add_f32 v[32:33], v[32:33], v[120:121]
v_pk_add_f32 v[114:115], v[114:115], v[118:119]
v_pk_add_f32 v[122:123], v[122:123], v[126:127]
v_pk_add_f32 v[34:35], v[34:35], v[114:115]
v_pk_add_f32 v[34:35], v[34:35], v[122:123]
v_pk_add_f32 v[128:129], v[128:129], v[132:133]
v_pk_add_f32 v[136:137], v[136:137], v[140:141]
s_waitcnt vmcnt(44)
v_pk_add_f32 v[36:37], v[36:37], v[128:129]
v_pk_add_f32 v[36:37], v[36:37], v[136:137]
v_pk_add_f32 v[130:131], v[130:131], v[134:135]
v_pk_add_f32 v[138:139], v[138:139], v[142:143]
v_pk_add_f32 v[38:39], v[38:39], v[130:131]
v_pk_add_f32 v[38:39], v[38:39], v[138:139]
s_add_u32 s48, s12, 138346496
s_lshr_b32 s49, s12, 1
s_add_u32 s49, s49, 143589376
v_cvt_pk_bf16_f32 v64, v32, v33
v_cvt_pk_bf16_f32 v65, v34, v35
v_cvt_pk_bf16_f32 v66, v36, v37
v_cvt_pk_bf16_f32 v67, v38, v39
buffer_store_dwordx4 v[64:67], v0, s[40:43], s49 offen sc1
buffer_store_dwordx4 v[32:35], v0, s[40:43], s48 offen sc1
buffer_store_dwordx4 v[36:39], v0, s[40:43], s48 offen offset:1024 sc1
v_pk_add_f32 v[144:145], v[144:145], v[148:149]
s_waitcnt lgkmcnt(12)
v_pk_add_f32 v[152:153], v[152:153], v[156:157]
s_waitcnt vmcnt(46)
v_pk_add_f32 v[40:41], v[40:41], v[144:145]
v_pk_add_f32 v[40:41], v[40:41], v[152:153]
v_pk_add_f32 v[146:147], v[146:147], v[150:151]
v_pk_add_f32 v[154:155], v[154:155], v[158:159]
v_pk_add_f32 v[42:43], v[42:43], v[146:147]
v_pk_add_f32 v[42:43], v[42:43], v[154:155]
)"
R"(s_waitcnt lgkmcnt(10)
v_pk_add_f32 v[160:161], v[160:161], v[164:165]
s_waitcnt lgkmcnt(8)
v_pk_add_f32 v[168:169], v[168:169], v[172:173]
s_waitcnt vmcnt(45)
v_pk_add_f32 v[44:45], v[44:45], v[160:161]
v_pk_add_f32 v[44:45], v[44:45], v[168:169]
v_pk_add_f32 v[162:163], v[162:163], v[166:167]
v_pk_add_f32 v[170:171], v[170:171], v[174:175]
v_pk_add_f32 v[46:47], v[46:47], v[162:163]
v_pk_add_f32 v[46:47], v[46:47], v[170:171]
s_add_u32 s48, s13, 138346496
s_lshr_b32 s49, s13, 1
s_add_u32 s49, s49, 143589376
v_cvt_pk_bf16_f32 v68, v40, v41
v_cvt_pk_bf16_f32 v69, v42, v43
v_cvt_pk_bf16_f32 v70, v44, v45
v_cvt_pk_bf16_f32 v71, v46, v47
buffer_store_dwordx4 v[68:71], v0, s[40:43], s49 offen sc1
buffer_store_dwordx4 v[40:43], v0, s[40:43], s48 offen sc1
buffer_store_dwordx4 v[44:47], v0, s[40:43], s48 offen offset:1024 sc1
s_waitcnt lgkmcnt(6)
v_pk_add_f32 v[176:177], v[176:177], v[180:181]
s_waitcnt lgkmcnt(4)
v_pk_add_f32 v[184:185], v[184:185], v[188:189]
s_waitcnt vmcnt(47)
v_pk_add_f32 v[48:49], v[48:49], v[176:177]
v_pk_add_f32 v[48:49], v[48:49], v[184:185]
v_pk_add_f32 v[178:179], v[178:179], v[182:183]
v_pk_add_f32 v[186:187], v[186:187], v[190:191]
v_pk_add_f32 v[50:51], v[50:51], v[178:179]
v_pk_add_f32 v[50:51], v[50:51], v[186:187]
s_waitcnt lgkmcnt(2)
v_pk_add_f32 v[192:193], v[192:193], v[196:197]
s_waitcnt lgkmcnt(0)
v_pk_add_f32 v[200:201], v[200:201], v[204:205]
s_waitcnt vmcnt(46)
v_pk_add_f32 v[52:53], v[52:53], v[192:193]
v_pk_add_f32 v[52:53], v[52:53], v[200:201]
v_pk_add_f32 v[194:195], v[194:195], v[198:199]
v_pk_add_f32 v[202:203], v[202:203], v[206:207]
v_pk_add_f32 v[54:55], v[54:55], v[194:195]
v_pk_add_f32 v[54:55], v[54:55], v[202:203]
s_add_u32 s48, s14, 138346496
s_lshr_b32 s49, s14, 1
s_add_u32 s49, s49, 143589376
v_cvt_pk_bf16_f32 v72, v48, v49
v_cvt_pk_bf16_f32 v73, v50, v51
v_cvt_pk_bf16_f32 v74, v52, v53
v_cvt_pk_bf16_f32 v75, v54, v55
buffer_store_dwordx4 v[72:75], v0, s[40:43], s49 offen sc1
buffer_store_dwordx4 v[48:51], v0, s[40:43], s48 offen sc1
buffer_store_dwordx4 v[52:55], v0, s[40:43], s48 offen offset:1024 sc1
s_lshr_b32 s54, s63, 1
s_lshl_b32 s54, s54, 7
s_add_u32 s54, s54, 2354176
buffer_load_dwordx4 a[0:3], v1, s[40:43], s30 offen sc1
buffer_load_dwordx4 a[4:7], v1, s[40:43], s31 offen sc1
buffer_load_dwordx4 a[8:11], v1, s[40:43], s34 offen sc1
buffer_load_dwordx4 a[12:15], v1, s[40:43], s35 offen sc1
buffer_load_dwordx4 a[16:19], v1, s[40:43], s36 offen sc1
buffer_load_dwordx4 a[20:23], v1, s[40:43], s37 offen sc1
buffer_load_dwordx4 a[24:27], v1, s[40:43], s38 offen sc1
buffer_load_dwordx4 a[28:31], v1, s[40:43], s39 offen sc1
buffer_load_dwordx4 a[32:35], v1, s[40:43], s96 offen sc1
buffer_load_dwordx4 a[36:39], v1, s[40:43], s97 offen sc1
buffer_load_dwordx4 a[40:43], v1, s[40:43], s30 offen offset:128 sc1
buffer_load_dwordx4 a[44:47], v1, s[40:43], s31 offen offset:128 sc1
buffer_load_dwordx4 a[48:51], v1, s[40:43], s34 offen offset:128 sc1
buffer_load_dwordx4 a[52:55], v1, s[40:43], s35 offen offset:128 sc1
buffer_load_dwordx4 a[56:59], v1, s[40:43], s36 offen offset:128 sc1
buffer_load_dwordx4 a[60:63], v1, s[40:43], s37 offen offset:128 sc1
buffer_load_dwordx4 a[64:67], v1, s[40:43], s38 offen offset:128 sc1
buffer_load_dwordx4 a[68:71], v1, s[40:43], s39 offen offset:128 sc1
buffer_load_dwordx4 a[72:75], v1, s[40:43], s96 offen offset:128 sc1
buffer_load_dwordx4 a[76:79], v1, s[40:43], s97 offen offset:128 sc1
buffer_load_dwordx4 a[240:243], v1, s[40:43], s30 offen offset:3072 sc1
buffer_load_dwordx4 a[244:247], v1, s[40:43], s31 offen offset:3072 sc1
buffer_load_dwordx4 a[248:251], v1, s[40:43], s34 offen offset:3072 sc1
buffer_load_dwordx4 a[252:255], v1, s[40:43], s35 offen offset:3072 sc1
buffer_load_dwordx4 v[192:195], v1, s[40:43], s36 offen offset:3072 sc1
buffer_load_dwordx4 v[196:199], v1, s[40:43], s37 offen offset:3072 sc1
buffer_load_dwordx4 v[200:203], v1, s[40:43], s38 offen offset:3072 sc1
buffer_load_dwordx4 v[204:207], v1, s[40:43], s39 offen offset:3072 sc1
buffer_load_dwordx4 v[208:211], v1, s[40:43], s96 offen offset:3072 sc1
buffer_load_dwordx4 v[212:215], v1, s[40:43], s97 offen offset:3072 sc1
buffer_load_dwordx4 v[216:219], v1, s[40:43], s30 offen offset:3200 sc1
buffer_load_dwordx4 v[220:223], v1, s[40:43], s31 offen offset:3200 sc1
buffer_load_dwordx4 v[224:227], v1, s[40:43], s34 offen offset:3200 sc1
buffer_load_dwordx4 v[228:231], v1, s[40:43], s35 offen offset:3200 sc1
buffer_load_dwordx4 v[232:235], v1, s[40:43], s36 offen offset:3200 sc1
buffer_load_dwordx4 v[236:239], v1, s[40:43], s37 offen offset:3200 sc1
buffer_load_dwordx4 v[240:243], v1, s[40:43], s38 offen offset:3200 sc1
buffer_load_dwordx4 v[244:247], v1, s[40:43], s39 offen offset:3200 sc1
buffer_load_dwordx4 v[248:251], v1, s[40:43], s96 offen offset:3200 sc1
buffer_load_dwordx4 v[252:255], v1, s[40:43], s97 offen offset:3200 sc1
s_waitcnt vmcnt(40)
s_waitcnt lgkmcnt(0)
s_barrier
buffer_atomic_add v6, v3, s[40:43], s54 offen
s_add_u32 s63, s63, 4
s_cmp_lt_u32 s47, 52
s_cbranch_scc1 hpt_loop_%=
s_branch hp_ret_%=
hp_tf_%=:
v_and_b32 v4, 0x370, v0
v_bfe_u32 v5, v15, 3, 1
v_lshl_add_u32 v1, v5, 15, v4
v_lshl_add_u32 v3, v5, 12, v4
s_lshl_b32 s28, s21, 6
s_add_u32 s28, s28, s26
s_lshl_b32 s54, s28, 7
s_add_u32 s54, s54, 2124032
s_cmp_lg_u32 s45, 0
s_cbranch_scc1 hptf_u_ok_%=
v_cmp_eq_u32 vcc, 0, v15
s_nop 1
v_cndmask_b32 v14, 0, 32, vcc
s_mov_b32 s60, 0
hptf_u_%=:
v_mov_b32 v4, 0
buffer_atomic_add v4, v2, s[40:43], s54 offen sc0
s_waitcnt vmcnt(0)
v_cmp_gt_u32 vcc, v14, v4
s_nop 4
s_cbranch_vccz hptf_u_ok_%=
s_sleep 1
s_add_u32 s60, s60, 1
s_cmp_gt_u32 s60, 65536
s_cbranch_scc1 hp_abort_%=
s_and_b32 s59, s60, 63
s_cbranch_scc1 hptf_u_%=
v_mov_b32 v4, 1024
buffer_load_dword v4, v4, s[40:43], s58 offen sc1
s_waitcnt vmcnt(0)
v_readfirstlane_b32 s59, v4
s_nop 0
s_cmp_lg_u32 s59, 0
s_nop 2
s_cbranch_scc1 hp_end_%=
s_branch hptf_u_%=
hptf_u_ok_%=:
s_barrier
s_and_b32 s29, s26, 31
s_mul_i32 s62, s29, 80
s_lshl3_add_u32 s62, s27, s62
s_lshl_b32 s50, s62, 15
s_lshl_b32 s48, s45, 12
s_and_b32 s49, s48, 0x2000
s_add_u32 s48, s48, s49
s_add_u32 s50, s50, s48
s_add_u32 s50, s50, 32964608
s_lshr_b32 s59, s50, 8
s_and_b32 s59, s59, 0x2000
s_add_u32 s51, s50, s59
s_add_u32 s50, s50, 65536
buffer_load_dwordx4 v[128:131], v1, s[40:43], s51 offen sc1
buffer_load_dwordx4 v[144:147], v1, s[40:43], s51 offen offset:128 sc1
buffer_load_dwordx4 v[160:163], v1, s[40:43], s51 offen offset:1024 sc1
buffer_load_dwordx4 v[176:179], v1, s[40:43], s51 offen offset:1152 sc1
buffer_load_dwordx4 v[192:195], v1, s[40:43], s51 offen offset:2048 sc1
buffer_load_dwordx4 v[208:211], v1, s[40:43], s51 offen offset:2176 sc1
buffer_load_dwordx4 v[224:227], v1, s[40:43], s51 offen offset:3072 sc1
buffer_load_dwordx4 v[240:243], v1, s[40:43], s51 offen offset:3200 sc1
s_lshr_b32 s59, s50, 8
s_and_b32 s59, s59, 0x2000
s_add_u32 s51, s50, s59
s_add_u32 s50, s50, 65536
buffer_load_dwordx4 v[132:135], v1, s[40:43], s51 offen sc1
buffer_load_dwordx4 v[148:151], v1, s[40:43], s51 offen offset:128 sc1
buffer_load_dwordx4 v[164:167], v1, s[40:43], s51 offen offset:1024 sc1
buffer_load_dwordx4 v[180:183], v1, s[40:43], s51 offen offset:1152 sc1
buffer_load_dwordx4 v[196:199], v1, s[40:43], s51 offen offset:2048 sc1
buffer_load_dwordx4 v[212:215], v1, s[40:43], s51 offen offset:2176 sc1
buffer_load_dwordx4 v[228:231], v1, s[40:43], s51 offen offset:3072 sc1
buffer_load_dwordx4 v[244:247], v1, s[40:43], s51 offen offset:3200 sc1
s_lshr_b32 s59, s50, 8
s_and_b32 s59, s59, 0x2000
s_add_u32 s51, s50, s59
s_add_u32 s50, s50, 65536
buffer_load_dwordx4 v[136:139], v1, s[40:43], s51 offen sc1
buffer_load_dwordx4 v[152:155], v1, s[40:43], s51 offen offset:128 sc1
buffer_load_dwordx4 v[168:171], v1, s[40:43], s51 offen offset:1024 sc1
buffer_load_dwordx4 v[184:187], v1, s[40:43], s51 offen offset:1152 sc1
buffer_load_dwordx4 v[200:203], v1, s[40:43], s51 offen offset:2048 sc1
buffer_load_dwordx4 v[216:219], v1, s[40:43], s51 offen offset:2176 sc1
buffer_load_dwordx4 v[232:235], v1, s[40:43], s51 offen offset:3072 sc1
buffer_load_dwordx4 v[248:251], v1, s[40:43], s51 offen offset:3200 sc1
s_lshr_b32 s59, s50, 8
s_and_b32 s59, s59, 0x2000
s_add_u32 s51, s50, s59
s_add_u32 s50, s50, 65536
buffer_load_dwordx4 v[140:143], v1, s[40:43], s51 offen sc1
buffer_load_dwordx4 v[156:159], v1, s[40:43], s51 offen offset:128 sc1
buffer_load_dwordx4 v[172:175], v1, s[40:43], s51 offen offset:1024 sc1
buffer_load_dwordx4 v[188:191], v1, s[40:43], s51 offen offset:1152 sc1
buffer_load_dwordx4 v[204:207], v1, s[40:43], s51 offen offset:2048 sc1
buffer_load_dwordx4 v[220:223], v1, s[40:43], s51 offen offset:2176 sc1
buffer_load_dwordx4 v[236:239], v1, s[40:43], s51 offen offset:3072 sc1
buffer_load_dwordx4 v[252:255], v1, s[40:43], s51 offen offset:3200 sc1
)"
R"(s_lshr_b32 s48, s45, 1
s_lshl1_add_u32 s48, s26, s48
s_mul_i32 s48, s48, 20
s_lshl1_add_u32 s48, s27, s48
s_and_b32 s49, s45, 1
s_add_u32 s48, s48, s49
s_lshl_b32 s30, s48, 11
s_add_u32 s48, s30, 133103616
buffer_load_dwordx4 a[248:251], v0, s[40:43], s48 offen sc1
buffer_load_dwordx4 a[252:255], v0, s[40:43], s48 offen offset:1024 sc1
v_lshlrev_b32 v6, 7, v15
s_mul_i32 s54, s28, 1536
s_add_u32 s54, s54, 2157568
v_mov_b32 v7, 20
v_cmp_gt_u32 vcc, 8, v15
s_nop 1
v_cndmask_b32 v7, v7, 8, vcc
v_cmp_gt_u32 vcc, 12, v15
s_nop 1
v_cndmask_b32 v7, 0, v7, vcc
v_mov_b32 v4, 2147483632
v_cndmask_b32 v6, v4, v6, vcc
s_lshl_b32 s48, s45, 13
v_add_u32 v8, s48, v0
s_lshl_b32 s48, s45, 11
v_add_u32 v9, s48, v0
s_lshl_b32 s50, s29, 5
s_lshl3_add_u32 s50, s45, s50
s_lshl_b32 s50, s50, 11
s_add_u32 s50, s50, 130482176
s_lshl_b32 s52, s29, 14
s_lshl_b32 s48, s45, 11
s_add_u32 s52, s52, s48
s_add_u32 s52, s52, 132579328
s_add_u32 s53, s52, 8192
s_lshl_b32 s51, s62, 12
s_lshl_b32 s48, s45, 10
s_add_u32 s51, s51, s48
s_add_u32 s51, s51, 116850688
s_waitcnt vmcnt(0)
s_cmp_lg_u32 s45, 0
s_cbranch_scc1 hptf_p_ok_%=
s_mov_b32 s60, 0
hptf_p_%=:
v_mov_b32 v4, 0
buffer_atomic_add v4, v6, s[40:43], s54 offen sc0
s_waitcnt vmcnt(0)
v_cmp_gt_u32 vcc, v7, v4
s_nop 4
s_cbranch_vccz hptf_p_ok_%=
s_sleep 2
s_add_u32 s60, s60, 1
s_cmp_gt_u32 s60, 65536
s_cbranch_scc1 hp_abort_%=
s_and_b32 s59, s60, 63
s_cbranch_scc1 hptf_p_%=
v_mov_b32 v4, 1024
buffer_load_dword v4, v4, s[40:43], s58 offen sc1
s_waitcnt vmcnt(0)
v_readfirstlane_b32 s59, v4
s_nop 0
s_cmp_lg_u32 s59, 0
s_nop 2
s_cbranch_scc1 hp_end_%=
s_branch hptf_p_%=
hptf_p_ok_%=:
s_barrier
buffer_load_dwordx4 v[16:19], v0, s[40:43], s50 offen sc1
buffer_load_dwordx4 v[20:23], v0, s[40:43], s50 offen offset:1024 sc1
buffer_load_dwordx4 v[24:27], v0, s[40:43], s50 offen offset:2048 sc1
buffer_load_dwordx4 v[28:31], v0, s[40:43], s50 offen offset:3072 sc1
s_add_u32 s50, s50, 4096
buffer_load_dwordx4 v[32:35], v0, s[40:43], s50 offen sc1
buffer_load_dwordx4 v[36:39], v0, s[40:43], s50 offen offset:1024 sc1
buffer_load_dwordx4 v[40:43], v0, s[40:43], s50 offen offset:2048 sc1
buffer_load_dwordx4 v[44:47], v0, s[40:43], s50 offen offset:3072 sc1
s_add_u32 s50, s50, 4096
buffer_load_dwordx4 v[48:51], v0, s[40:43], s50 offen sc1
buffer_load_dwordx4 v[52:55], v0, s[40:43], s50 offen offset:1024 sc1
buffer_load_dwordx4 v[56:59], v0, s[40:43], s50 offen offset:2048 sc1
buffer_load_dwordx4 v[60:63], v0, s[40:43], s50 offen offset:3072 sc1
s_add_u32 s50, s50, 4096
buffer_load_dwordx4 v[64:67], v0, s[40:43], s50 offen sc1
buffer_load_dwordx4 v[68:71], v0, s[40:43], s50 offen offset:1024 sc1
buffer_load_dwordx4 v[72:75], v0, s[40:43], s50 offen offset:2048 sc1
buffer_load_dwordx4 v[76:79], v0, s[40:43], s50 offen offset:3072 sc1
buffer_load_dwordx4 v[80:83], v0, s[40:43], s52 offen sc1
buffer_load_dwordx4 v[84:87], v0, s[40:43], s53 offen sc1
buffer_load_dwordx4 v[88:91], v0, s[40:43], s52 offen offset:1024 sc1
buffer_load_dwordx4 v[92:95], v0, s[40:43], s53 offen offset:1024 sc1
s_waitcnt vmcnt(19)
v_mfma_f32_16x16x32_bf16 v[96:99], v[128:131], v[16:19], 0
s_waitcnt vmcnt(18)
v_mfma_f32_16x16x32_bf16 v[112:115], v[128:131], v[20:23], 0
s_add_u32 s48, s51, 0
buffer_load_dwordx4 v[128:131], v3, s[40:43], s48 offen sc1
v_mfma_f32_16x16x32_bf16 v[100:103], v[132:135], v[16:19], 0
v_mfma_f32_16x16x32_bf16 v[116:119], v[132:135], v[20:23], 0
s_add_u32 s48, s51, 8192
buffer_load_dwordx4 v[132:135], v3, s[40:43], s48 offen sc1
v_mfma_f32_16x16x32_bf16 v[104:107], v[136:139], v[16:19], 0
v_mfma_f32_16x16x32_bf16 v[120:123], v[136:139], v[20:23], 0
s_add_u32 s48, s51, 16384
buffer_load_dwordx4 v[136:139], v3, s[40:43], s48 offen sc1
v_mfma_f32_16x16x32_bf16 v[108:111], v[140:143], v[16:19], 0
v_mfma_f32_16x16x32_bf16 v[124:127], v[140:143], v[20:23], 0
s_add_u32 s48, s51, 24576
buffer_load_dwordx4 v[140:143], v3, s[40:43], s48 offen sc1
s_waitcnt vmcnt(21)
v_mfma_f32_16x16x32_bf16 v[96:99], v[144:147], v[24:27], v[96:99]
s_waitcnt vmcnt(20)
v_mfma_f32_16x16x32_bf16 v[112:115], v[144:147], v[28:31], v[112:115]
s_add_u32 s48, s51, 0
buffer_load_dwordx4 v[144:147], v3, s[40:43], s48 offen offset:128 sc1
v_mfma_f32_16x16x32_bf16 v[100:103], v[148:151], v[24:27], v[100:103]
v_mfma_f32_16x16x32_bf16 v[116:119], v[148:151], v[28:31], v[116:119]
s_add_u32 s48, s51, 8192
buffer_load_dwordx4 v[148:151], v3, s[40:43], s48 offen offset:128 sc1
v_mfma_f32_16x16x32_bf16 v[104:107], v[152:155], v[24:27], v[104:107]
v_mfma_f32_16x16x32_bf16 v[120:123], v[152:155], v[28:31], v[120:123]
s_add_u32 s48, s51, 16384
buffer_load_dwordx4 v[152:155], v3, s[40:43], s48 offen offset:128 sc1
v_mfma_f32_16x16x32_bf16 v[108:111], v[156:159], v[24:27], v[108:111]
v_mfma_f32_16x16x32_bf16 v[124:127], v[156:159], v[28:31], v[124:127]
s_add_u32 s48, s51, 24576
buffer_load_dwordx4 v[156:159], v3, s[40:43], s48 offen offset:128 sc1
s_waitcnt vmcnt(23)
v_mfma_f32_16x16x32_bf16 v[96:99], v[160:163], v[32:35], v[96:99]
s_waitcnt vmcnt(22)
v_mfma_f32_16x16x32_bf16 v[112:115], v[160:163], v[36:39], v[112:115]
v_mfma_f32_16x16x32_bf16 v[100:103], v[164:167], v[32:35], v[100:103]
v_mfma_f32_16x16x32_bf16 v[116:119], v[164:167], v[36:39], v[116:119]
v_mfma_f32_16x16x32_bf16 v[104:107], v[168:171], v[32:35], v[104:107]
v_mfma_f32_16x16x32_bf16 v[120:123], v[168:171], v[36:39], v[120:123]
v_mfma_f32_16x16x32_bf16 v[108:111], v[172:175], v[32:35], v[108:111]
v_mfma_f32_16x16x32_bf16 v[124:127], v[172:175], v[36:39], v[124:127]
s_waitcnt vmcnt(21)
v_mfma_f32_16x16x32_bf16 v[96:99], v[176:179], v[40:43], v[96:99]
s_waitcnt vmcnt(20)
v_mfma_f32_16x16x32_bf16 v[112:115], v[176:179], v[44:47], v[112:115]
v_mfma_f32_16x16x32_bf16 v[100:103], v[180:183], v[40:43], v[100:103]
v_mfma_f32_16x16x32_bf16 v[116:119], v[180:183], v[44:47], v[116:119]
v_mfma_f32_16x16x32_bf16 v[104:107], v[184:187], v[40:43], v[104:107]
v_mfma_f32_16x16x32_bf16 v[120:123], v[184:187], v[44:47], v[120:123]
v_mfma_f32_16x16x32_bf16 v[108:111], v[188:191], v[40:43], v[108:111]
v_mfma_f32_16x16x32_bf16 v[124:127], v[188:191], v[44:47], v[124:127]
s_waitcnt vmcnt(19)
v_mfma_f32_16x16x32_bf16 v[96:99], v[192:195], v[48:51], v[96:99]
s_waitcnt vmcnt(18)
v_mfma_f32_16x16x32_bf16 v[112:115], v[192:195], v[52:55], v[112:115]
v_mfma_f32_16x16x32_bf16 v[100:103], v[196:199], v[48:51], v[100:103]
v_mfma_f32_16x16x32_bf16 v[116:119], v[196:199], v[52:55], v[116:119]
v_mfma_f32_16x16x32_bf16 v[104:107], v[200:203], v[48:51], v[104:107]
v_mfma_f32_16x16x32_bf16 v[120:123], v[200:203], v[52:55], v[120:123]
v_mfma_f32_16x16x32_bf16 v[108:111], v[204:207], v[48:51], v[108:111]
v_mfma_f32_16x16x32_bf16 v[124:127], v[204:207], v[52:55], v[124:127]
s_waitcnt vmcnt(17)
v_mfma_f32_16x16x32_bf16 v[96:99], v[208:211], v[56:59], v[96:99]
s_waitcnt vmcnt(16)
v_mfma_f32_16x16x32_bf16 v[112:115], v[208:211], v[60:63], v[112:115]
v_mfma_f32_16x16x32_bf16 v[100:103], v[212:215], v[56:59], v[100:103]
v_mfma_f32_16x16x32_bf16 v[116:119], v[212:215], v[60:63], v[116:119]
v_mfma_f32_16x16x32_bf16 v[104:107], v[216:219], v[56:59], v[104:107]
v_mfma_f32_16x16x32_bf16 v[120:123], v[216:219], v[60:63], v[120:123]
v_mfma_f32_16x16x32_bf16 v[108:111], v[220:223], v[56:59], v[108:111]
v_mfma_f32_16x16x32_bf16 v[124:127], v[220:223], v[60:63], v[124:127]
s_waitcnt vmcnt(15)
v_mfma_f32_16x16x32_bf16 v[96:99], v[224:227], v[64:67], v[96:99]
s_waitcnt vmcnt(14)
v_mfma_f32_16x16x32_bf16 v[112:115], v[224:227], v[68:71], v[112:115]
v_mfma_f32_16x16x32_bf16 v[100:103], v[228:231], v[64:67], v[100:103]
v_mfma_f32_16x16x32_bf16 v[116:119], v[228:231], v[68:71], v[116:119]
v_mfma_f32_16x16x32_bf16 v[104:107], v[232:235], v[64:67], v[104:107]
v_mfma_f32_16x16x32_bf16 v[120:123], v[232:235], v[68:71], v[120:123]
v_mfma_f32_16x16x32_bf16 v[108:111], v[236:239], v[64:67], v[108:111]
v_mfma_f32_16x16x32_bf16 v[124:127], v[236:239], v[68:71], v[124:127]
s_waitcnt vmcnt(13)
v_mfma_f32_16x16x32_bf16 v[96:99], v[240:243], v[72:75], v[96:99]
s_waitcnt vmcnt(12)
v_mfma_f32_16x16x32_bf16 v[112:115], v[240:243], v[76:79], v[112:115]
v_mfma_f32_16x16x32_bf16 v[100:103], v[244:247], v[72:75], v[100:103]
v_mfma_f32_16x16x32_bf16 v[116:119], v[244:247], v[76:79], v[116:119]
v_mfma_f32_16x16x32_bf16 v[104:107], v[248:251], v[72:75], v[104:107]
v_mfma_f32_16x16x32_bf16 v[120:123], v[248:251], v[76:79], v[120:123]
v_mfma_f32_16x16x32_bf16 v[108:111], v[252:255], v[72:75], v[108:111]
v_mfma_f32_16x16x32_bf16 v[124:127], v[252:255], v[76:79], v[124:127]
s_waitcnt vmcnt(7)
v_mfma_f32_16x16x32_bf16 v[96:99], v[128:131], v[80:83], v[96:99]
v_mfma_f32_16x16x32_bf16 v[112:115], v[128:131], v[84:87], v[112:115]
s_waitcnt vmcnt(6)
v_mfma_f32_16x16x32_bf16 v[100:103], v[132:135], v[80:83], v[100:103]
v_mfma_f32_16x16x32_bf16 v[116:119], v[132:135], v[84:87], v[116:119]
s_waitcnt vmcnt(5)
v_mfma_f32_16x16x32_bf16 v[104:107], v[136:139], v[80:83], v[104:107]
v_mfma_f32_16x16x32_bf16 v[120:123], v[136:139], v[84:87], v[120:123]
s_waitcnt vmcnt(4)
v_mfma_f32_16x16x32_bf16 v[108:111], v[140:143], v[80:83], v[108:111]
v_mfma_f32_16x16x32_bf16 v[124:127], v[140:143], v[84:87], v[124:127]
s_waitcnt vmcnt(3)
v_mfma_f32_16x16x32_bf16 v[96:99], v[144:147], v[88:91], v[96:99]
)"
R"(v_mfma_f32_16x16x32_bf16 v[112:115], v[144:147], v[92:95], v[112:115]
s_waitcnt vmcnt(2)
v_mfma_f32_16x16x32_bf16 v[100:103], v[148:151], v[88:91], v[100:103]
v_mfma_f32_16x16x32_bf16 v[116:119], v[148:151], v[92:95], v[116:119]
s_waitcnt vmcnt(1)
v_mfma_f32_16x16x32_bf16 v[104:107], v[152:155], v[88:91], v[104:107]
v_mfma_f32_16x16x32_bf16 v[120:123], v[152:155], v[92:95], v[120:123]
s_waitcnt vmcnt(0)
v_mfma_f32_16x16x32_bf16 v[108:111], v[156:159], v[88:91], v[108:111]
v_mfma_f32_16x16x32_bf16 v[124:127], v[156:159], v[92:95], v[124:127]
ds_write_b128 v8, v[96:99] offset:0
ds_write_b128 v8, v[100:103] offset:1024
s_nop 1
ds_write_b128 v8, v[104:107] offset:2048
s_nop 1
ds_write_b128 v8, v[108:111] offset:3072
ds_write_b128 v8, v[112:115] offset:4096
ds_write_b128 v8, v[116:119] offset:5120
ds_write_b128 v8, v[120:123] offset:6144
ds_write_b128 v8, v[124:127] offset:7168
v_accvgpr_read_b32 v16, a248
v_accvgpr_read_b32 v17, a249
v_accvgpr_read_b32 v18, a250
v_accvgpr_read_b32 v19, a251
v_accvgpr_read_b32 v20, a252
v_accvgpr_read_b32 v21, a253
v_accvgpr_read_b32 v22, a254
v_accvgpr_read_b32 v23, a255
s_waitcnt lgkmcnt(0)
s_barrier
ds_read_b128 v[128:131], v9 offset:0
ds_read_b128 v[132:135], v9 offset:8192
ds_read_b128 v[136:139], v9 offset:16384
ds_read_b128 v[140:143], v9 offset:24576
ds_read_b128 v[144:147], v9 offset:1024
ds_read_b128 v[148:151], v9 offset:9216
ds_read_b128 v[152:155], v9 offset:17408
ds_read_b128 v[156:159], v9 offset:25600
s_waitcnt lgkmcnt(6)
v_pk_add_f32 v[128:129], v[128:129], v[132:133]
s_waitcnt lgkmcnt(4)
v_pk_add_f32 v[136:137], v[136:137], v[140:141]
v_pk_add_f32 v[16:17], v[16:17], v[128:129]
v_pk_add_f32 v[16:17], v[16:17], v[136:137]
v_pk_add_f32 v[130:131], v[130:131], v[134:135]
v_pk_add_f32 v[138:139], v[138:139], v[142:143]
v_pk_add_f32 v[18:19], v[18:19], v[130:131]
v_pk_add_f32 v[18:19], v[18:19], v[138:139]
s_waitcnt lgkmcnt(2)
v_pk_add_f32 v[144:145], v[144:145], v[148:149]
s_waitcnt lgkmcnt(0)
v_pk_add_f32 v[152:153], v[152:153], v[156:157]
v_pk_add_f32 v[20:21], v[20:21], v[144:145]
v_pk_add_f32 v[20:21], v[20:21], v[152:153]
v_pk_add_f32 v[146:147], v[146:147], v[150:151]
v_pk_add_f32 v[154:155], v[154:155], v[158:159]
v_pk_add_f32 v[22:23], v[22:23], v[146:147]
v_pk_add_f32 v[22:23], v[22:23], v[154:155]
s_add_u32 s48, s30, 138346496
s_lshr_b32 s49, s30, 1
s_add_u32 s49, s49, 143589376
v_cvt_pk_bf16_f32 v24, v16, v17
v_cvt_pk_bf16_f32 v25, v18, v19
v_cvt_pk_bf16_f32 v26, v20, v21
v_cvt_pk_bf16_f32 v27, v22, v23
buffer_store_dwordx4 v[24:27], v0, s[40:43], s49 offen sc1
buffer_store_dwordx4 v[16:19], v0, s[40:43], s48 offen sc1
buffer_store_dwordx4 v[20:23], v0, s[40:43], s48 offen offset:1024 sc1
s_and_b32 s54, s26, 7
s_lshl3_add_u32 s54, s21, s54
s_lshl_b32 s54, s54, 7
s_add_u32 s54, s54, 2372608
s_branch hp_done_%=
hp_bf_%=:
s_and_b32 s29, s45, 1
s_lshr_b32 s30, s45, 1
s_lshl1_add_u32 s28, s26, s29
s_mul_i32 s48, s29, 7168
s_mul_i32 s49, s30, 15360
s_add_u32 s61, s48, s49
s_mul_i32 s50, s27, 307200
s_add_u32 s50, s50, s48
s_mul_i32 s48, s30, 153600
s_add_u32 s50, s50, s48
s_add_u32 s50, s50, 146210816
s_add_u32 m0, s61, 0
s_add_u32 s51, s50, 0
buffer_load_dwordx4 v0, s[40:43], s51 offen offset:0 sc1 lds
buffer_load_dwordx4 v0, s[40:43], s51 offen offset:1024 sc1 lds
buffer_load_dwordx4 v0, s[40:43], s51 offen offset:2048 sc1 lds
buffer_load_dwordx4 v0, s[40:43], s51 offen offset:3072 sc1 lds
s_add_u32 m0, s61, 4096
s_add_u32 s51, s50, 4096
buffer_load_dwordx4 v0, s[40:43], s51 offen offset:0 sc1 lds
buffer_load_dwordx4 v0, s[40:43], s51 offen offset:1024 sc1 lds
buffer_load_dwordx4 v0, s[40:43], s51 offen offset:2048 sc1 lds
buffer_load_dwordx4 v0, s[40:43], s51 offen offset:3072 sc1 lds
s_add_u32 m0, s61, 30720
s_add_u32 s51, s50, 15360
buffer_load_dwordx4 v0, s[40:43], s51 offen offset:0 sc1 lds
buffer_load_dwordx4 v0, s[40:43], s51 offen offset:1024 sc1 lds
buffer_load_dwordx4 v0, s[40:43], s51 offen offset:2048 sc1 lds
buffer_load_dwordx4 v0, s[40:43], s51 offen offset:3072 sc1 lds
s_add_u32 m0, s61, 34816
s_add_u32 s51, s50, 19456
buffer_load_dwordx4 v0, s[40:43], s51 offen offset:0 sc1 lds
buffer_load_dwordx4 v0, s[40:43], s51 offen offset:1024 sc1 lds
buffer_load_dwordx4 v0, s[40:43], s51 offen offset:2048 sc1 lds
buffer_load_dwordx4 v0, s[40:43], s51 offen offset:3072 sc1 lds
s_add_u32 m0, s61, 61440
s_add_u32 s51, s50, 30720
buffer_load_dwordx4 v0, s[40:43], s51 offen offset:0 sc1 lds
buffer_load_dwordx4 v0, s[40:43], s51 offen offset:1024 sc1 lds
buffer_load_dwordx4 v0, s[40:43], s51 offen offset:2048 sc1 lds
buffer_load_dwordx4 v0, s[40:43], s51 offen offset:3072 sc1 lds
s_add_u32 m0, s61, 65536
s_add_u32 s51, s50, 34816
buffer_load_dwordx4 v0, s[40:43], s51 offen offset:0 sc1 lds
buffer_load_dwordx4 v0, s[40:43], s51 offen offset:1024 sc1 lds
buffer_load_dwordx4 v0, s[40:43], s51 offen offset:2048 sc1 lds
buffer_load_dwordx4 v0, s[40:43], s51 offen offset:3072 sc1 lds
s_add_u32 m0, s61, 92160
s_add_u32 s51, s50, 46080
buffer_load_dwordx4 v0, s[40:43], s51 offen offset:0 sc1 lds
buffer_load_dwordx4 v0, s[40:43], s51 offen offset:1024 sc1 lds
buffer_load_dwordx4 v0, s[40:43], s51 offen offset:2048 sc1 lds
buffer_load_dwordx4 v0, s[40:43], s51 offen offset:3072 sc1 lds
s_add_u32 m0, s61, 96256
s_add_u32 s51, s50, 50176
buffer_load_dwordx4 v0, s[40:43], s51 offen offset:0 sc1 lds
buffer_load_dwordx4 v0, s[40:43], s51 offen offset:1024 sc1 lds
buffer_load_dwordx4 v0, s[40:43], s51 offen offset:2048 sc1 lds
buffer_load_dwordx4 v0, s[40:43], s51 offen offset:3072 sc1 lds
s_add_u32 m0, s61, 122880
s_add_u32 s51, s50, 61440
buffer_load_dwordx4 v0, s[40:43], s51 offen offset:0 sc1 lds
buffer_load_dwordx4 v0, s[40:43], s51 offen offset:1024 sc1 lds
buffer_load_dwordx4 v0, s[40:43], s51 offen offset:2048 sc1 lds
buffer_load_dwordx4 v0, s[40:43], s51 offen offset:3072 sc1 lds
s_add_u32 m0, s61, 126976
s_add_u32 s51, s50, 65536
buffer_load_dwordx4 v0, s[40:43], s51 offen offset:0 sc1 lds
buffer_load_dwordx4 v0, s[40:43], s51 offen offset:1024 sc1 lds
buffer_load_dwordx4 v0, s[40:43], s51 offen offset:2048 sc1 lds
buffer_load_dwordx4 v0, s[40:43], s51 offen offset:3072 sc1 lds
v_lshlrev_b32 v4, 7, v15
s_mul_i32 s48, s29, 29696
s_add_u32 s48, s48, s50
s_add_u32 s48, s48, 76800
buffer_load_dword v16, v4, s[40:43], s48 offen sc1
s_add_u32 s48, s48, 8192
buffer_load_dword v17, v4, s[40:43], s48 offen sc1
s_add_u32 s48, s48, 8192
buffer_load_dword v18, v4, s[40:43], s48 offen sc1
s_add_u32 s48, s48, 8192
buffer_load_dword v19, v4, s[40:43], s48 offen sc1
s_add_u32 s48, s48, 8192
buffer_load_dword v20, v4, s[40:43], s48 offen sc1
s_add_u32 s48, s48, 8192
v_mov_b32 v188, 0x3f803f80
v_mov_b32 v189, 0x3f803f80
v_mov_b32 v190, 0x3f803f80
v_mov_b32 v191, 0x3f803f80
v_add_u32 v3, s49, v0
v_add_u32 v6, 61440, v3
v_add_u32 v7, 122880, v3
v_and_b32 v4, 48, v15
s_mul_i32 s52, s27, 960
s_cmp_eq_u32 s30, 0
s_cselect_b32 s48, 320, 0
s_add_u32 s52, s52, s48
s_add_u32 s52, s52, 148668416
s_and_b32 s31, s26, 7
s_lshl3_add_u32 s54, s21, s31
s_lshl_b32 s54, s54, 7
s_add_u32 s54, s54, 2372608
s_waitcnt vmcnt(0)
s_cmp_lg_u32 s45, 0
s_cbranch_scc1 hpbf_dep_ok_%=
v_cmp_eq_u32 vcc, 0, v15
s_nop 1
v_cndmask_b32 v14, 0, 10, vcc
s_mov_b32 s60, 0
hpbf_dep_%=:
v_mov_b32 v5, 0
buffer_atomic_add v5, v2, s[40:43], s54 offen sc0
s_waitcnt vmcnt(0)
v_cmp_gt_u32 vcc, v14, v5
s_nop 4
s_cbranch_vccz hpbf_dep_ok_%=
s_sleep 1
s_add_u32 s60, s60, 1
s_cmp_gt_u32 s60, 65536
s_cbranch_scc1 hp_abort_%=
s_and_b32 s59, s60, 63
s_cbranch_scc1 hpbf_dep_%=
v_mov_b32 v5, 1024
buffer_load_dword v5, v5, s[40:43], s58 offen sc1
s_waitcnt vmcnt(0)
v_readfirstlane_b32 s59, v5
)"
R"(s_nop 0
s_cmp_lg_u32 s59, 0
s_nop 2
s_cbranch_scc1 hp_end_%=
s_branch hpbf_dep_%=
hpbf_dep_ok_%=:
s_barrier
s_mul_i32 s48, s28, 20480
s_mul_i32 s49, s30, 10240
s_add_u32 s48, s48, s49
s_add_u32 s48, s48, 143589376
buffer_load_dwordx4 v[16:19], v0, s[40:43], s48 offen sc1
buffer_load_dwordx4 v[20:23], v0, s[40:43], s48 offen offset:1024 sc1
buffer_load_dwordx4 v[24:27], v0, s[40:43], s48 offen offset:2048 sc1
buffer_load_dwordx4 v[28:31], v0, s[40:43], s48 offen offset:3072 sc1
s_add_u32 s48, s48, 4096
buffer_load_dwordx4 v[32:35], v0, s[40:43], s48 offen sc1
buffer_load_dwordx4 v[36:39], v0, s[40:43], s48 offen offset:1024 sc1
buffer_load_dwordx4 v[40:43], v0, s[40:43], s48 offen offset:2048 sc1
buffer_load_dwordx4 v[44:47], v0, s[40:43], s48 offen offset:3072 sc1
s_add_u32 s48, s48, 4096
buffer_load_dwordx4 v[48:51], v0, s[40:43], s48 offen sc1
buffer_load_dwordx4 v[52:55], v0, s[40:43], s48 offen offset:1024 sc1
buffer_load_dwordx4 v[56:59], v4, s[40:43], s52 offen sc1
buffer_load_dwordx4 v[60:63], v4, s[40:43], s52 offen offset:64 sc1
buffer_load_dwordx4 v[64:67], v4, s[40:43], s52 offen offset:128 sc1
buffer_load_dwordx4 v[68:71], v4, s[40:43], s52 offen offset:192 sc1
buffer_load_dwordx4 v[72:75], v4, s[40:43], s52 offen offset:256 sc1
buffer_load_dwordx4 v[76:79], v4, s[40:43], s52 offen offset:320 sc1
buffer_load_dwordx4 v[80:83], v4, s[40:43], s52 offen offset:384 sc1
buffer_load_dwordx4 v[84:87], v4, s[40:43], s52 offen offset:448 sc1
buffer_load_dwordx4 v[88:91], v4, s[40:43], s52 offen offset:512 sc1
buffer_load_dwordx4 v[92:95], v4, s[40:43], s52 offen offset:576 sc1
ds_read_b128 v[164:167], v3 offset:0
ds_read_b128 v[168:171], v3 offset:1024
ds_read_b128 v[172:175], v3 offset:2048
ds_read_b128 v[176:179], v3 offset:3072
ds_read_b128 v[180:183], v3 offset:4096
s_waitcnt vmcnt(19) lgkmcnt(4)
v_mfma_f32_16x16x32_bf16 v[96:99], v[164:167], v[16:19], 0
ds_read_b128 v[164:167], v3 offset:5120
s_waitcnt lgkmcnt(4)
v_mfma_f32_16x16x32_bf16 v[100:103], v[168:171], v[16:19], 0
ds_read_b128 v[168:171], v3 offset:6144
s_waitcnt lgkmcnt(4)
v_mfma_f32_16x16x32_bf16 v[104:107], v[172:175], v[16:19], 0
ds_read_b128 v[172:175], v3 offset:7168
s_waitcnt lgkmcnt(4)
v_mfma_f32_16x16x32_bf16 v[108:111], v[176:179], v[16:19], 0
ds_read_b128 v[176:179], v3 offset:8192
s_waitcnt lgkmcnt(4)
v_mfma_f32_16x16x32_bf16 v[112:115], v[180:183], v[16:19], 0
ds_read_b128 v[180:183], v3 offset:9216
s_waitcnt lgkmcnt(4)
v_mfma_f32_16x16x32_bf16 v[116:119], v[164:167], v[16:19], 0
ds_read_b128 v[164:167], v3 offset:10240
s_waitcnt lgkmcnt(4)
v_mfma_f32_16x16x32_bf16 v[120:123], v[168:171], v[16:19], 0
ds_read_b128 v[168:171], v3 offset:11264
s_waitcnt lgkmcnt(4)
v_mfma_f32_16x16x32_bf16 v[124:127], v[172:175], v[16:19], 0
ds_read_b128 v[172:175], v3 offset:12288
v_mfma_f32_16x16x32_bf16 v[156:159], v[188:191], v[16:19], 0
s_waitcnt lgkmcnt(4)
v_mfma_f32_16x16x32_bf16 v[128:131], v[176:179], v[16:19], 0
ds_read_b128 v[176:179], v3 offset:13312
s_waitcnt lgkmcnt(4)
v_mfma_f32_16x16x32_bf16 v[132:135], v[180:183], v[16:19], 0
ds_read_b128 v[180:183], v3 offset:14336
s_waitcnt lgkmcnt(4)
v_mfma_f32_16x16x32_bf16 v[136:139], v[164:167], v[16:19], 0
ds_read_b128 v[164:167], v3 offset:30720
s_waitcnt lgkmcnt(4)
v_mfma_f32_16x16x32_bf16 v[140:143], v[168:171], v[16:19], 0
ds_read_b128 v[168:171], v3 offset:31744
s_waitcnt lgkmcnt(4)
v_mfma_f32_16x16x32_bf16 v[144:147], v[172:175], v[16:19], 0
ds_read_b128 v[172:175], v3 offset:32768
s_waitcnt lgkmcnt(4)
v_mfma_f32_16x16x32_bf16 v[148:151], v[176:179], v[16:19], 0
ds_read_b128 v[176:179], v3 offset:33792
s_waitcnt lgkmcnt(4)
v_mfma_f32_16x16x32_bf16 v[152:155], v[180:183], v[16:19], 0
ds_read_b128 v[180:183], v3 offset:34816
v_mfma_f32_16x16x32_bf16 v[160:163], v[16:19], v[16:19], 0
s_waitcnt vmcnt(18) lgkmcnt(4)
v_mfma_f32_16x16x32_bf16 v[96:99], v[164:167], v[20:23], v[96:99]
s_barrier
ds_read_b128 v[164:167], v3 offset:35840
s_add_u32 m0, s61, 0
s_add_u32 s51, s50, 76800
buffer_load_dwordx4 v0, s[40:43], s51 offen offset:0 sc1 lds
s_waitcnt lgkmcnt(4)
v_mfma_f32_16x16x32_bf16 v[100:103], v[168:171], v[20:23], v[100:103]
ds_read_b128 v[168:171], v3 offset:36864
buffer_load_dwordx4 v0, s[40:43], s51 offen offset:1024 sc1 lds
s_waitcnt lgkmcnt(4)
v_mfma_f32_16x16x32_bf16 v[104:107], v[172:175], v[20:23], v[104:107]
ds_read_b128 v[172:175], v3 offset:37888
buffer_load_dwordx4 v0, s[40:43], s51 offen offset:2048 sc1 lds
s_waitcnt lgkmcnt(4)
v_mfma_f32_16x16x32_bf16 v[108:111], v[176:179], v[20:23], v[108:111]
ds_read_b128 v[176:179], v3 offset:38912
buffer_load_dwordx4 v0, s[40:43], s51 offen offset:3072 sc1 lds
s_waitcnt lgkmcnt(4)
v_mfma_f32_16x16x32_bf16 v[112:115], v[180:183], v[20:23], v[112:115]
ds_read_b128 v[180:183], v3 offset:39936
s_add_u32 m0, s61, 4096
s_add_u32 s51, s50, 80896
buffer_load_dwordx4 v0, s[40:43], s51 offen offset:0 sc1 lds
s_waitcnt lgkmcnt(4)
v_mfma_f32_16x16x32_bf16 v[116:119], v[164:167], v[20:23], v[116:119]
ds_read_b128 v[164:167], v3 offset:40960
buffer_load_dwordx4 v0, s[40:43], s51 offen offset:1024 sc1 lds
s_waitcnt lgkmcnt(4)
v_mfma_f32_16x16x32_bf16 v[120:123], v[168:171], v[20:23], v[120:123]
ds_read_b128 v[168:171], v3 offset:41984
buffer_load_dwordx4 v0, s[40:43], s51 offen offset:2048 sc1 lds
s_waitcnt lgkmcnt(4)
v_mfma_f32_16x16x32_bf16 v[124:127], v[172:175], v[20:23], v[124:127]
ds_read_b128 v[172:175], v3 offset:43008
buffer_load_dwordx4 v0, s[40:43], s51 offen offset:3072 sc1 lds
v_mfma_f32_16x16x32_bf16 v[156:159], v[188:191], v[20:23], v[156:159]
s_waitcnt lgkmcnt(4)
v_mfma_f32_16x16x32_bf16 v[128:131], v[176:179], v[20:23], v[128:131]
ds_read_b128 v[176:179], v3 offset:44032
s_waitcnt lgkmcnt(4)
v_mfma_f32_16x16x32_bf16 v[132:135], v[180:183], v[20:23], v[132:135]
ds_read_b128 v[180:183], v3 offset:45056
s_waitcnt lgkmcnt(4)
v_mfma_f32_16x16x32_bf16 v[136:139], v[164:167], v[20:23], v[136:139]
ds_read_b128 v[164:167], v6 offset:0
s_waitcnt lgkmcnt(4)
v_mfma_f32_16x16x32_bf16 v[140:143], v[168:171], v[20:23], v[140:143]
ds_read_b128 v[168:171], v6 offset:1024
s_waitcnt lgkmcnt(4)
v_mfma_f32_16x16x32_bf16 v[144:147], v[172:175], v[20:23], v[144:147]
ds_read_b128 v[172:175], v6 offset:2048
s_waitcnt lgkmcnt(4)
v_mfma_f32_16x16x32_bf16 v[148:151], v[176:179], v[20:23], v[148:151]
ds_read_b128 v[176:179], v6 offset:3072
s_waitcnt lgkmcnt(4)
v_mfma_f32_16x16x32_bf16 v[152:155], v[180:183], v[20:23], v[152:155]
ds_read_b128 v[180:183], v6 offset:4096
v_mfma_f32_16x16x32_bf16 v[160:163], v[20:23], v[20:23], v[160:163]
s_waitcnt vmcnt(25) lgkmcnt(4)
v_mfma_f32_16x16x32_bf16 v[96:99], v[164:167], v[24:27], v[96:99]
s_barrier
ds_read_b128 v[164:167], v6 offset:5120
s_add_u32 m0, s61, 30720
s_add_u32 s51, s50, 92160
buffer_load_dwordx4 v0, s[40:43], s51 offen offset:0 sc1 lds
s_waitcnt lgkmcnt(4)
v_mfma_f32_16x16x32_bf16 v[100:103], v[168:171], v[24:27], v[100:103]
ds_read_b128 v[168:171], v6 offset:6144
buffer_load_dwordx4 v0, s[40:43], s51 offen offset:1024 sc1 lds
s_waitcnt lgkmcnt(4)
v_mfma_f32_16x16x32_bf16 v[104:107], v[172:175], v[24:27], v[104:107]
ds_read_b128 v[172:175], v6 offset:7168
buffer_load_dwordx4 v0, s[40:43], s51 offen offset:2048 sc1 lds
s_waitcnt lgkmcnt(4)
v_mfma_f32_16x16x32_bf16 v[108:111], v[176:179], v[24:27], v[108:111]
ds_read_b128 v[176:179], v6 offset:8192
buffer_load_dwordx4 v0, s[40:43], s51 offen offset:3072 sc1 lds
s_waitcnt lgkmcnt(4)
v_mfma_f32_16x16x32_bf16 v[112:115], v[180:183], v[24:27], v[112:115]
ds_read_b128 v[180:183], v6 offset:9216
s_add_u32 m0, s61, 34816
s_add_u32 s51, s50, 96256
buffer_load_dwordx4 v0, s[40:43], s51 offen offset:0 sc1 lds
s_waitcnt lgkmcnt(4)
v_mfma_f32_16x16x32_bf16 v[116:119], v[164:167], v[24:27], v[116:119]
ds_read_b128 v[164:167], v6 offset:10240
buffer_load_dwordx4 v0, s[40:43], s51 offen offset:1024 sc1 lds
s_waitcnt lgkmcnt(4)
v_mfma_f32_16x16x32_bf16 v[120:123], v[168:171], v[24:27], v[120:123]
ds_read_b128 v[168:171], v6 offset:11264
buffer_load_dwordx4 v0, s[40:43], s51 offen offset:2048 sc1 lds
s_waitcnt lgkmcnt(4)
v_mfma_f32_16x16x32_bf16 v[124:127], v[172:175], v[24:27], v[124:127]
ds_read_b128 v[172:175], v6 offset:12288
buffer_load_dwordx4 v0, s[40:43], s51 offen offset:3072 sc1 lds
v_mfma_f32_16x16x32_bf16 v[156:159], v[188:191], v[24:27], v[156:159]
s_waitcnt lgkmcnt(4)
v_mfma_f32_16x16x32_bf16 v[128:131], v[176:179], v[24:27], v[128:131]
ds_read_b128 v[176:179], v6 offset:13312
s_waitcnt lgkmcnt(4)
v_mfma_f32_16x16x32_bf16 v[132:135], v[180:183], v[24:27], v[132:135]
ds_read_b128 v[180:183], v6 offset:14336
s_waitcnt lgkmcnt(4)
v_mfma_f32_16x16x32_bf16 v[136:139], v[164:167], v[24:27], v[136:139]
ds_read_b128 v[164:167], v6 offset:30720
s_waitcnt lgkmcnt(4)
v_mfma_f32_16x16x32_bf16 v[140:143], v[168:171], v[24:27], v[140:143]
ds_read_b128 v[168:171], v6 offset:31744
s_waitcnt lgkmcnt(4)
v_mfma_f32_16x16x32_bf16 v[144:147], v[172:175], v[24:27], v[144:147]
ds_read_b128 v[172:175], v6 offset:32768
s_waitcnt lgkmcnt(4)
v_mfma_f32_16x16x32_bf16 v[148:151], v[176:179], v[24:27], v[148:151]
)"
R"(ds_read_b128 v[176:179], v6 offset:33792
s_waitcnt lgkmcnt(4)
v_mfma_f32_16x16x32_bf16 v[152:155], v[180:183], v[24:27], v[152:155]
ds_read_b128 v[180:183], v6 offset:34816
v_mfma_f32_16x16x32_bf16 v[160:163], v[24:27], v[24:27], v[160:163]
s_waitcnt vmcnt(32) lgkmcnt(4)
v_mfma_f32_16x16x32_bf16 v[96:99], v[164:167], v[28:31], v[96:99]
s_barrier
ds_read_b128 v[164:167], v6 offset:35840
s_add_u32 m0, s61, 61440
s_add_u32 s51, s50, 107520
buffer_load_dwordx4 v0, s[40:43], s51 offen offset:0 sc1 lds
s_waitcnt lgkmcnt(4)
v_mfma_f32_16x16x32_bf16 v[100:103], v[168:171], v[28:31], v[100:103]
ds_read_b128 v[168:171], v6 offset:36864
buffer_load_dwordx4 v0, s[40:43], s51 offen offset:1024 sc1 lds
s_waitcnt lgkmcnt(4)
v_mfma_f32_16x16x32_bf16 v[104:107], v[172:175], v[28:31], v[104:107]
ds_read_b128 v[172:175], v6 offset:37888
buffer_load_dwordx4 v0, s[40:43], s51 offen offset:2048 sc1 lds
s_waitcnt lgkmcnt(4)
v_mfma_f32_16x16x32_bf16 v[108:111], v[176:179], v[28:31], v[108:111]
ds_read_b128 v[176:179], v6 offset:38912
buffer_load_dwordx4 v0, s[40:43], s51 offen offset:3072 sc1 lds
s_waitcnt lgkmcnt(4)
v_mfma_f32_16x16x32_bf16 v[112:115], v[180:183], v[28:31], v[112:115]
ds_read_b128 v[180:183], v6 offset:39936
s_add_u32 m0, s61, 65536
s_add_u32 s51, s50, 111616
buffer_load_dwordx4 v0, s[40:43], s51 offen offset:0 sc1 lds
s_waitcnt lgkmcnt(4)
v_mfma_f32_16x16x32_bf16 v[116:119], v[164:167], v[28:31], v[116:119]
ds_read_b128 v[164:167], v6 offset:40960
buffer_load_dwordx4 v0, s[40:43], s51 offen offset:1024 sc1 lds
s_waitcnt lgkmcnt(4)
v_mfma_f32_16x16x32_bf16 v[120:123], v[168:171], v[28:31], v[120:123]
ds_read_b128 v[168:171], v6 offset:41984
buffer_load_dwordx4 v0, s[40:43], s51 offen offset:2048 sc1 lds
s_waitcnt lgkmcnt(4)
v_mfma_f32_16x16x32_bf16 v[124:127], v[172:175], v[28:31], v[124:127]
ds_read_b128 v[172:175], v6 offset:43008
buffer_load_dwordx4 v0, s[40:43], s51 offen offset:3072 sc1 lds
v_mfma_f32_16x16x32_bf16 v[156:159], v[188:191], v[28:31], v[156:159]
s_waitcnt lgkmcnt(4)
v_mfma_f32_16x16x32_bf16 v[128:131], v[176:179], v[28:31], v[128:131]
ds_read_b128 v[176:179], v6 offset:44032
s_waitcnt lgkmcnt(4)
v_mfma_f32_16x16x32_bf16 v[132:135], v[180:183], v[28:31], v[132:135]
ds_read_b128 v[180:183], v6 offset:45056
s_waitcnt lgkmcnt(4)
v_mfma_f32_16x16x32_bf16 v[136:139], v[164:167], v[28:31], v[136:139]
ds_read_b128 v[164:167], v7 offset:0
s_waitcnt lgkmcnt(4)
v_mfma_f32_16x16x32_bf16 v[140:143], v[168:171], v[28:31], v[140:143]
ds_read_b128 v[168:171], v7 offset:1024
s_waitcnt lgkmcnt(4)
v_mfma_f32_16x16x32_bf16 v[144:147], v[172:175], v[28:31], v[144:147]
ds_read_b128 v[172:175], v7 offset:2048
s_waitcnt lgkmcnt(4)
v_mfma_f32_16x16x32_bf16 v[148:151], v[176:179], v[28:31], v[148:151]
ds_read_b128 v[176:179], v7 offset:3072
s_waitcnt lgkmcnt(4)
v_mfma_f32_16x16x32_bf16 v[152:155], v[180:183], v[28:31], v[152:155]
ds_read_b128 v[180:183], v7 offset:4096
v_mfma_f32_16x16x32_bf16 v[160:163], v[28:31], v[28:31], v[160:163]
s_waitcnt vmcnt(39) lgkmcnt(4)
v_mfma_f32_16x16x32_bf16 v[96:99], v[164:167], v[32:35], v[96:99]
s_barrier
ds_read_b128 v[164:167], v7 offset:5120
s_add_u32 m0, s61, 92160
s_add_u32 s51, s50, 122880
buffer_load_dwordx4 v0, s[40:43], s51 offen offset:0 sc1 lds
s_waitcnt lgkmcnt(4)
v_mfma_f32_16x16x32_bf16 v[100:103], v[168:171], v[32:35], v[100:103]
ds_read_b128 v[168:171], v7 offset:6144
buffer_load_dwordx4 v0, s[40:43], s51 offen offset:1024 sc1 lds
s_waitcnt lgkmcnt(4)
v_mfma_f32_16x16x32_bf16 v[104:107], v[172:175], v[32:35], v[104:107]
ds_read_b128 v[172:175], v7 offset:7168
buffer_load_dwordx4 v0, s[40:43], s51 offen offset:2048 sc1 lds
s_waitcnt lgkmcnt(4)
v_mfma_f32_16x16x32_bf16 v[108:111], v[176:179], v[32:35], v[108:111]
ds_read_b128 v[176:179], v7 offset:8192
buffer_load_dwordx4 v0, s[40:43], s51 offen offset:3072 sc1 lds
s_waitcnt lgkmcnt(4)
v_mfma_f32_16x16x32_bf16 v[112:115], v[180:183], v[32:35], v[112:115]
ds_read_b128 v[180:183], v7 offset:9216
s_add_u32 m0, s61, 96256
s_add_u32 s51, s50, 126976
buffer_load_dwordx4 v0, s[40:43], s51 offen offset:0 sc1 lds
s_waitcnt lgkmcnt(4)
v_mfma_f32_16x16x32_bf16 v[116:119], v[164:167], v[32:35], v[116:119]
ds_read_b128 v[164:167], v7 offset:10240
buffer_load_dwordx4 v0, s[40:43], s51 offen offset:1024 sc1 lds
s_waitcnt lgkmcnt(4)
v_mfma_f32_16x16x32_bf16 v[120:123], v[168:171], v[32:35], v[120:123]
ds_read_b128 v[168:171], v7 offset:11264
buffer_load_dwordx4 v0, s[40:43], s51 offen offset:2048 sc1 lds
s_waitcnt lgkmcnt(4)
v_mfma_f32_16x16x32_bf16 v[124:127], v[172:175], v[32:35], v[124:127]
ds_read_b128 v[172:175], v7 offset:12288
buffer_load_dwordx4 v0, s[40:43], s51 offen offset:3072 sc1 lds
v_mfma_f32_16x16x32_bf16 v[156:159], v[188:191], v[32:35], v[156:159]
s_waitcnt lgkmcnt(4)
v_mfma_f32_16x16x32_bf16 v[128:131], v[176:179], v[32:35], v[128:131]
ds_read_b128 v[176:179], v7 offset:13312
s_waitcnt lgkmcnt(4)
v_mfma_f32_16x16x32_bf16 v[132:135], v[180:183], v[32:35], v[132:135]
ds_read_b128 v[180:183], v7 offset:14336
s_waitcnt lgkmcnt(4)
v_mfma_f32_16x16x32_bf16 v[136:139], v[164:167], v[32:35], v[136:139]
s_waitcnt vmcnt(24)
s_barrier
ds_read_b128 v[164:167], v3 offset:0
s_waitcnt lgkmcnt(4)
v_mfma_f32_16x16x32_bf16 v[140:143], v[168:171], v[32:35], v[140:143]
ds_read_b128 v[168:171], v3 offset:1024
s_waitcnt lgkmcnt(4)
v_mfma_f32_16x16x32_bf16 v[144:147], v[172:175], v[32:35], v[144:147]
ds_read_b128 v[172:175], v3 offset:2048
s_waitcnt lgkmcnt(4)
v_mfma_f32_16x16x32_bf16 v[148:151], v[176:179], v[32:35], v[148:151]
ds_read_b128 v[176:179], v3 offset:3072
s_waitcnt lgkmcnt(4)
v_mfma_f32_16x16x32_bf16 v[152:155], v[180:183], v[32:35], v[152:155]
ds_read_b128 v[180:183], v3 offset:4096
v_mfma_f32_16x16x32_bf16 v[160:163], v[32:35], v[32:35], v[160:163]
s_waitcnt lgkmcnt(4)
v_mfma_f32_16x16x32_bf16 v[96:99], v[164:167], v[36:39], v[96:99]
s_barrier
ds_read_b128 v[164:167], v3 offset:5120
s_add_u32 m0, s61, 122880
s_add_u32 s51, s50, 138240
buffer_load_dwordx4 v0, s[40:43], s51 offen offset:0 sc1 lds
s_waitcnt lgkmcnt(4)
v_mfma_f32_16x16x32_bf16 v[100:103], v[168:171], v[36:39], v[100:103]
ds_read_b128 v[168:171], v3 offset:6144
buffer_load_dwordx4 v0, s[40:43], s51 offen offset:1024 sc1 lds
s_waitcnt lgkmcnt(4)
v_mfma_f32_16x16x32_bf16 v[104:107], v[172:175], v[36:39], v[104:107]
ds_read_b128 v[172:175], v3 offset:7168
buffer_load_dwordx4 v0, s[40:43], s51 offen offset:2048 sc1 lds
s_waitcnt lgkmcnt(4)
v_mfma_f32_16x16x32_bf16 v[108:111], v[176:179], v[36:39], v[108:111]
ds_read_b128 v[176:179], v3 offset:8192
buffer_load_dwordx4 v0, s[40:43], s51 offen offset:3072 sc1 lds
s_waitcnt lgkmcnt(4)
v_mfma_f32_16x16x32_bf16 v[112:115], v[180:183], v[36:39], v[112:115]
ds_read_b128 v[180:183], v3 offset:9216
s_add_u32 m0, s61, 126976
s_add_u32 s51, s50, 142336
buffer_load_dwordx4 v0, s[40:43], s51 offen offset:0 sc1 lds
s_waitcnt lgkmcnt(4)
v_mfma_f32_16x16x32_bf16 v[116:119], v[164:167], v[36:39], v[116:119]
ds_read_b128 v[164:167], v3 offset:10240
buffer_load_dwordx4 v0, s[40:43], s51 offen offset:1024 sc1 lds
s_waitcnt lgkmcnt(4)
v_mfma_f32_16x16x32_bf16 v[120:123], v[168:171], v[36:39], v[120:123]
ds_read_b128 v[168:171], v3 offset:11264
buffer_load_dwordx4 v0, s[40:43], s51 offen offset:2048 sc1 lds
s_waitcnt lgkmcnt(4)
v_mfma_f32_16x16x32_bf16 v[124:127], v[172:175], v[36:39], v[124:127]
ds_read_b128 v[172:175], v3 offset:12288
buffer_load_dwordx4 v0, s[40:43], s51 offen offset:3072 sc1 lds
v_mfma_f32_16x16x32_bf16 v[156:159], v[188:191], v[36:39], v[156:159]
s_waitcnt lgkmcnt(4)
v_mfma_f32_16x16x32_bf16 v[128:131], v[176:179], v[36:39], v[128:131]
ds_read_b128 v[176:179], v3 offset:13312
s_waitcnt lgkmcnt(4)
v_mfma_f32_16x16x32_bf16 v[132:135], v[180:183], v[36:39], v[132:135]
ds_read_b128 v[180:183], v3 offset:14336
s_waitcnt lgkmcnt(4)
v_mfma_f32_16x16x32_bf16 v[136:139], v[164:167], v[36:39], v[136:139]
s_waitcnt vmcnt(24)
s_barrier
ds_read_b128 v[164:167], v3 offset:30720
s_waitcnt lgkmcnt(4)
v_mfma_f32_16x16x32_bf16 v[140:143], v[168:171], v[36:39], v[140:143]
ds_read_b128 v[168:171], v3 offset:31744
s_waitcnt lgkmcnt(4)
v_mfma_f32_16x16x32_bf16 v[144:147], v[172:175], v[36:39], v[144:147]
ds_read_b128 v[172:175], v3 offset:32768
s_waitcnt lgkmcnt(4)
v_mfma_f32_16x16x32_bf16 v[148:151], v[176:179], v[36:39], v[148:151]
ds_read_b128 v[176:179], v3 offset:33792
s_waitcnt lgkmcnt(4)
v_mfma_f32_16x16x32_bf16 v[152:155], v[180:183], v[36:39], v[152:155]
ds_read_b128 v[180:183], v3 offset:34816
v_mfma_f32_16x16x32_bf16 v[160:163], v[36:39], v[36:39], v[160:163]
s_waitcnt lgkmcnt(4)
v_mfma_f32_16x16x32_bf16 v[96:99], v[164:167], v[40:43], v[96:99]
ds_read_b128 v[164:167], v3 offset:35840
s_waitcnt lgkmcnt(4)
v_mfma_f32_16x16x32_bf16 v[100:103], v[168:171], v[40:43], v[100:103]
ds_read_b128 v[168:171], v3 offset:36864
s_waitcnt lgkmcnt(4)
v_mfma_f32_16x16x32_bf16 v[104:107], v[172:175], v[40:43], v[104:107]
ds_read_b128 v[172:175], v3 offset:37888
s_waitcnt lgkmcnt(4)
v_mfma_f32_16x16x32_bf16 v[108:111], v[176:179], v[40:43], v[108:111]
)"
R"(ds_read_b128 v[176:179], v3 offset:38912
s_waitcnt lgkmcnt(4)
v_mfma_f32_16x16x32_bf16 v[112:115], v[180:183], v[40:43], v[112:115]
ds_read_b128 v[180:183], v3 offset:39936
s_waitcnt lgkmcnt(4)
v_mfma_f32_16x16x32_bf16 v[116:119], v[164:167], v[40:43], v[116:119]
ds_read_b128 v[164:167], v3 offset:40960
s_waitcnt lgkmcnt(4)
v_mfma_f32_16x16x32_bf16 v[120:123], v[168:171], v[40:43], v[120:123]
ds_read_b128 v[168:171], v3 offset:41984
s_waitcnt lgkmcnt(4)
v_mfma_f32_16x16x32_bf16 v[124:127], v[172:175], v[40:43], v[124:127]
ds_read_b128 v[172:175], v3 offset:43008
v_mfma_f32_16x16x32_bf16 v[156:159], v[188:191], v[40:43], v[156:159]
s_waitcnt lgkmcnt(4)
v_mfma_f32_16x16x32_bf16 v[128:131], v[176:179], v[40:43], v[128:131]
ds_read_b128 v[176:179], v3 offset:44032
s_waitcnt lgkmcnt(4)
v_mfma_f32_16x16x32_bf16 v[132:135], v[180:183], v[40:43], v[132:135]
ds_read_b128 v[180:183], v3 offset:45056
s_waitcnt lgkmcnt(4)
v_mfma_f32_16x16x32_bf16 v[136:139], v[164:167], v[40:43], v[136:139]
s_waitcnt vmcnt(16)
s_barrier
ds_read_b128 v[164:167], v6 offset:0
s_waitcnt lgkmcnt(4)
v_mfma_f32_16x16x32_bf16 v[140:143], v[168:171], v[40:43], v[140:143]
ds_read_b128 v[168:171], v6 offset:1024
s_waitcnt lgkmcnt(4)
v_mfma_f32_16x16x32_bf16 v[144:147], v[172:175], v[40:43], v[144:147]
ds_read_b128 v[172:175], v6 offset:2048
s_waitcnt lgkmcnt(4)
v_mfma_f32_16x16x32_bf16 v[148:151], v[176:179], v[40:43], v[148:151]
ds_read_b128 v[176:179], v6 offset:3072
s_waitcnt lgkmcnt(4)
v_mfma_f32_16x16x32_bf16 v[152:155], v[180:183], v[40:43], v[152:155]
ds_read_b128 v[180:183], v6 offset:4096
v_mfma_f32_16x16x32_bf16 v[160:163], v[40:43], v[40:43], v[160:163]
s_waitcnt lgkmcnt(4)
v_mfma_f32_16x16x32_bf16 v[96:99], v[164:167], v[44:47], v[96:99]
ds_read_b128 v[164:167], v6 offset:5120
s_waitcnt lgkmcnt(4)
v_mfma_f32_16x16x32_bf16 v[100:103], v[168:171], v[44:47], v[100:103]
ds_read_b128 v[168:171], v6 offset:6144
s_waitcnt lgkmcnt(4)
v_mfma_f32_16x16x32_bf16 v[104:107], v[172:175], v[44:47], v[104:107]
ds_read_b128 v[172:175], v6 offset:7168
s_waitcnt lgkmcnt(4)
v_mfma_f32_16x16x32_bf16 v[108:111], v[176:179], v[44:47], v[108:111]
ds_read_b128 v[176:179], v6 offset:8192
s_waitcnt lgkmcnt(4)
v_mfma_f32_16x16x32_bf16 v[112:115], v[180:183], v[44:47], v[112:115]
ds_read_b128 v[180:183], v6 offset:9216
s_waitcnt lgkmcnt(4)
v_mfma_f32_16x16x32_bf16 v[116:119], v[164:167], v[44:47], v[116:119]
ds_read_b128 v[164:167], v6 offset:10240
s_waitcnt lgkmcnt(4)
v_mfma_f32_16x16x32_bf16 v[120:123], v[168:171], v[44:47], v[120:123]
ds_read_b128 v[168:171], v6 offset:11264
s_waitcnt lgkmcnt(4)
v_mfma_f32_16x16x32_bf16 v[124:127], v[172:175], v[44:47], v[124:127]
ds_read_b128 v[172:175], v6 offset:12288
v_mfma_f32_16x16x32_bf16 v[156:159], v[188:191], v[44:47], v[156:159]
s_waitcnt lgkmcnt(4)
v_mfma_f32_16x16x32_bf16 v[128:131], v[176:179], v[44:47], v[128:131]
ds_read_b128 v[176:179], v6 offset:13312
s_waitcnt lgkmcnt(4)
v_mfma_f32_16x16x32_bf16 v[132:135], v[180:183], v[44:47], v[132:135]
ds_read_b128 v[180:183], v6 offset:14336
s_waitcnt lgkmcnt(4)
v_mfma_f32_16x16x32_bf16 v[136:139], v[164:167], v[44:47], v[136:139]
s_waitcnt vmcnt(8)
s_barrier
ds_read_b128 v[164:167], v6 offset:30720
s_waitcnt lgkmcnt(4)
v_mfma_f32_16x16x32_bf16 v[140:143], v[168:171], v[44:47], v[140:143]
ds_read_b128 v[168:171], v6 offset:31744
s_waitcnt lgkmcnt(4)
v_mfma_f32_16x16x32_bf16 v[144:147], v[172:175], v[44:47], v[144:147]
ds_read_b128 v[172:175], v6 offset:32768
s_waitcnt lgkmcnt(4)
v_mfma_f32_16x16x32_bf16 v[148:151], v[176:179], v[44:47], v[148:151]
ds_read_b128 v[176:179], v6 offset:33792
s_waitcnt lgkmcnt(4)
v_mfma_f32_16x16x32_bf16 v[152:155], v[180:183], v[44:47], v[152:155]
ds_read_b128 v[180:183], v6 offset:34816
v_mfma_f32_16x16x32_bf16 v[160:163], v[44:47], v[44:47], v[160:163]
s_waitcnt lgkmcnt(4)
v_mfma_f32_16x16x32_bf16 v[96:99], v[164:167], v[48:51], v[96:99]
ds_read_b128 v[164:167], v6 offset:35840
s_waitcnt lgkmcnt(4)
v_mfma_f32_16x16x32_bf16 v[100:103], v[168:171], v[48:51], v[100:103]
ds_read_b128 v[168:171], v6 offset:36864
s_waitcnt lgkmcnt(4)
v_mfma_f32_16x16x32_bf16 v[104:107], v[172:175], v[48:51], v[104:107]
ds_read_b128 v[172:175], v6 offset:37888
s_waitcnt lgkmcnt(4)
v_mfma_f32_16x16x32_bf16 v[108:111], v[176:179], v[48:51], v[108:111]
ds_read_b128 v[176:179], v6 offset:38912
s_waitcnt lgkmcnt(4)
v_mfma_f32_16x16x32_bf16 v[112:115], v[180:183], v[48:51], v[112:115]
ds_read_b128 v[180:183], v6 offset:39936
s_waitcnt lgkmcnt(4)
v_mfma_f32_16x16x32_bf16 v[116:119], v[164:167], v[48:51], v[116:119]
ds_read_b128 v[164:167], v6 offset:40960
s_waitcnt lgkmcnt(4)
v_mfma_f32_16x16x32_bf16 v[120:123], v[168:171], v[48:51], v[120:123]
ds_read_b128 v[168:171], v6 offset:41984
s_waitcnt lgkmcnt(4)
v_mfma_f32_16x16x32_bf16 v[124:127], v[172:175], v[48:51], v[124:127]
ds_read_b128 v[172:175], v6 offset:43008
v_mfma_f32_16x16x32_bf16 v[156:159], v[188:191], v[48:51], v[156:159]
s_waitcnt lgkmcnt(4)
v_mfma_f32_16x16x32_bf16 v[128:131], v[176:179], v[48:51], v[128:131]
ds_read_b128 v[176:179], v6 offset:44032
s_waitcnt lgkmcnt(4)
v_mfma_f32_16x16x32_bf16 v[132:135], v[180:183], v[48:51], v[132:135]
ds_read_b128 v[180:183], v6 offset:45056
s_waitcnt lgkmcnt(4)
v_mfma_f32_16x16x32_bf16 v[136:139], v[164:167], v[48:51], v[136:139]
s_waitcnt vmcnt(0)
s_barrier
ds_read_b128 v[164:167], v7 offset:0
s_waitcnt lgkmcnt(4)
v_mfma_f32_16x16x32_bf16 v[140:143], v[168:171], v[48:51], v[140:143]
ds_read_b128 v[168:171], v7 offset:1024
s_waitcnt lgkmcnt(4)
v_mfma_f32_16x16x32_bf16 v[144:147], v[172:175], v[48:51], v[144:147]
ds_read_b128 v[172:175], v7 offset:2048
s_waitcnt lgkmcnt(4)
v_mfma_f32_16x16x32_bf16 v[148:151], v[176:179], v[48:51], v[148:151]
ds_read_b128 v[176:179], v7 offset:3072
s_waitcnt lgkmcnt(4)
v_mfma_f32_16x16x32_bf16 v[152:155], v[180:183], v[48:51], v[152:155]
ds_read_b128 v[180:183], v7 offset:4096
v_mfma_f32_16x16x32_bf16 v[160:163], v[48:51], v[48:51], v[160:163]
s_waitcnt lgkmcnt(4)
v_mfma_f32_16x16x32_bf16 v[96:99], v[164:167], v[52:55], v[96:99]
ds_read_b128 v[164:167], v7 offset:5120
s_waitcnt lgkmcnt(4)
v_mfma_f32_16x16x32_bf16 v[100:103], v[168:171], v[52:55], v[100:103]
ds_read_b128 v[168:171], v7 offset:6144
s_waitcnt lgkmcnt(4)
v_mfma_f32_16x16x32_bf16 v[104:107], v[172:175], v[52:55], v[104:107]
ds_read_b128 v[172:175], v7 offset:7168
s_waitcnt lgkmcnt(4)
v_mfma_f32_16x16x32_bf16 v[108:111], v[176:179], v[52:55], v[108:111]
ds_read_b128 v[176:179], v7 offset:8192
s_waitcnt lgkmcnt(4)
v_mfma_f32_16x16x32_bf16 v[112:115], v[180:183], v[52:55], v[112:115]
ds_read_b128 v[180:183], v7 offset:9216
s_waitcnt lgkmcnt(4)
v_mfma_f32_16x16x32_bf16 v[116:119], v[164:167], v[52:55], v[116:119]
ds_read_b128 v[164:167], v7 offset:10240
s_waitcnt lgkmcnt(4)
v_mfma_f32_16x16x32_bf16 v[120:123], v[168:171], v[52:55], v[120:123]
ds_read_b128 v[168:171], v7 offset:11264
s_waitcnt lgkmcnt(4)
v_mfma_f32_16x16x32_bf16 v[124:127], v[172:175], v[52:55], v[124:127]
ds_read_b128 v[172:175], v7 offset:12288
v_mfma_f32_16x16x32_bf16 v[156:159], v[188:191], v[52:55], v[156:159]
s_waitcnt lgkmcnt(4)
v_mfma_f32_16x16x32_bf16 v[128:131], v[176:179], v[52:55], v[128:131]
ds_read_b128 v[176:179], v7 offset:13312
s_waitcnt lgkmcnt(4)
v_mfma_f32_16x16x32_bf16 v[132:135], v[180:183], v[52:55], v[132:135]
ds_read_b128 v[180:183], v7 offset:14336
s_waitcnt lgkmcnt(4)
v_mfma_f32_16x16x32_bf16 v[136:139], v[164:167], v[52:55], v[136:139]
s_waitcnt lgkmcnt(3)
v_mfma_f32_16x16x32_bf16 v[140:143], v[168:171], v[52:55], v[140:143]
s_waitcnt lgkmcnt(2)
v_mfma_f32_16x16x32_bf16 v[144:147], v[172:175], v[52:55], v[144:147]
s_waitcnt lgkmcnt(1)
v_mfma_f32_16x16x32_bf16 v[148:151], v[176:179], v[52:55], v[148:151]
s_waitcnt lgkmcnt(0)
v_mfma_f32_16x16x32_bf16 v[152:155], v[180:183], v[52:55], v[152:155]
v_mfma_f32_16x16x32_bf16 v[160:163], v[52:55], v[52:55], v[160:163]
s_mul_i32 s48, s45, 12288
s_add_u32 s48, s48, 32768
v_add_u32 v6, s48, v0
s_xor_b32 s48, s45, 2
s_mul_i32 s48, s48, 12288
s_add_u32 s48, s48, 32768
v_add_u32 v7, s48, v0
s_add_u32 s48, s31, 26
s_lshl_b32 s48, s48, 5
s_lshl2_add_u32 s48, s27, s48
s_add_u32 s48, s48, s45
s_mul_i32 s34, s48, 6144
s_add_u32 s34, s34, 154738688
s_lshl3_add_u32 s48, s21, s27
s_lshl_b32 s48, s48, 7
s_add_u32 s36, s48, 267264
v_lshlrev_b32 v12, 11, v15
v_add_u32 v12, s36, v12
s_cmp_eq_u32 s31, 7
s_cselect_b32 s48, 4, 0
v_mov_b32 v13, s48
s_lshr_b32 s48, s31, 1
)"
R"(v_mov_b32 v4, s48
s_and_b32 s37, s31, 1
v_cmp_eq_u32 vcc, s37, v15
s_nop 1
v_cndmask_b32 v13, v13, v4, vcc
v_cmp_gt_u32 vcc, 3, v15
s_nop 1
v_cndmask_b32 v13, 0, v13, vcc
v_mov_b32 v4, 2147483632
v_cndmask_b32 v12, v4, v12, vcc
v_mov_b32 v14, 0
buffer_atomic_add v14, v12, s[40:43], s58 offen sc0
s_cmp_eq_u32 s30, 0
s_nop 0
s_cbranch_scc1 hpbf_kv_%=
ds_write_b128 v6, v[116:119] offset:0
ds_write_b128 v6, v[120:123] offset:1024
ds_write_b128 v6, v[124:127] offset:2048
ds_write_b128 v6, v[128:131] offset:3072
ds_write_b128 v6, v[132:135] offset:4096
ds_write_b128 v6, v[136:139] offset:5120
ds_write_b128 v6, v[140:143] offset:6144
ds_write_b128 v6, v[144:147] offset:7168
ds_write_b128 v6, v[148:151] offset:8192
ds_write_b128 v6, v[152:155] offset:9216
ds_write_b128 v6, v[156:159] offset:10240
ds_write_b128 v6, v[160:163] offset:11264
s_waitcnt lgkmcnt(0)
s_barrier
ds_read_b128 v[16:19], v7 offset:0
ds_read_b128 v[20:23], v7 offset:1024
ds_read_b128 v[24:27], v7 offset:2048
ds_read_b128 v[28:31], v7 offset:3072
ds_read_b128 v[32:35], v7 offset:4096
ds_read_b128 v[36:39], v7 offset:5120
ds_read_b128 v[40:43], v7 offset:6144
s_waitcnt lgkmcnt(6)
v_pk_add_f32 v[96:97], v[96:97], v[16:17]
v_pk_add_f32 v[98:99], v[98:99], v[18:19]
s_waitcnt lgkmcnt(5)
v_pk_add_f32 v[100:101], v[100:101], v[20:21]
v_pk_add_f32 v[102:103], v[102:103], v[22:23]
s_waitcnt lgkmcnt(4)
v_pk_add_f32 v[104:105], v[104:105], v[24:25]
v_pk_add_f32 v[106:107], v[106:107], v[26:27]
s_waitcnt lgkmcnt(3)
v_pk_add_f32 v[108:109], v[108:109], v[28:29]
v_pk_add_f32 v[110:111], v[110:111], v[30:31]
s_waitcnt lgkmcnt(2)
v_pk_add_f32 v[112:113], v[112:113], v[32:33]
v_pk_add_f32 v[114:115], v[114:115], v[34:35]
s_waitcnt lgkmcnt(1)
v_pk_add_f32 v[156:157], v[156:157], v[36:37]
v_pk_add_f32 v[158:159], v[158:159], v[38:39]
s_waitcnt lgkmcnt(0)
v_pk_add_f32 v[160:161], v[160:161], v[40:41]
v_pk_add_f32 v[162:163], v[162:163], v[42:43]
v_and_b32 v4, 1, v15
v_cmp_eq_u32 vcc, 1, v4
s_nop 1
v_cndmask_b32 v160, v160, v161, vcc
v_cndmask_b32 v162, v162, v163, vcc
v_and_b32 v4, 2, v15
v_cmp_eq_u32 vcc, 2, v4
s_nop 1
v_cndmask_b32 v160, v160, v162, vcc
v_bfe_u32 v4, v15, 2, 2
v_lshrrev_b32 v5, 4, v15
v_cmp_eq_u32 vcc, v4, v5
s_nop 1
v_cndmask_b32 v160, 0, v160, vcc
v_mov_b32 v161, v160
s_nop 1
v_permlane16_swap_b32 v160, v161
v_add_f32 v160, v160, v161
v_mov_b32 v161, v160
s_nop 1
v_permlane32_swap_b32 v160, v161
v_add_f32 v160, v160, v161
v_mul_f32 v8, 0x3acccccd, v156
v_mul_f32 v160, 0x3acccccd, v160
v_fma_f32 v160, -v8, v8, v160
v_max_f32 v160, 0, v160
v_add_f32 v160, 0x3727c5ac, v160
v_rsq_f32 v10, v160
v_sub_f32 v8, 0, v8
v_pk_fma_f32 v[96:97], v[56:57], v[8:9], v[96:97] op_sel_hi:[1,0,1]
v_pk_mul_f32 v[96:97], v[96:97], v[10:11] op_sel_hi:[1,0]
v_pk_fma_f32 v[98:99], v[58:59], v[8:9], v[98:99] op_sel_hi:[1,0,1]
v_pk_mul_f32 v[98:99], v[98:99], v[10:11] op_sel_hi:[1,0]
v_pk_fma_f32 v[100:101], v[60:61], v[8:9], v[100:101] op_sel_hi:[1,0,1]
v_pk_mul_f32 v[100:101], v[100:101], v[10:11] op_sel_hi:[1,0]
v_pk_fma_f32 v[102:103], v[62:63], v[8:9], v[102:103] op_sel_hi:[1,0,1]
v_pk_mul_f32 v[102:103], v[102:103], v[10:11] op_sel_hi:[1,0]
v_pk_fma_f32 v[104:105], v[64:65], v[8:9], v[104:105] op_sel_hi:[1,0,1]
v_pk_mul_f32 v[104:105], v[104:105], v[10:11] op_sel_hi:[1,0]
v_pk_fma_f32 v[106:107], v[66:67], v[8:9], v[106:107] op_sel_hi:[1,0,1]
v_pk_mul_f32 v[106:107], v[106:107], v[10:11] op_sel_hi:[1,0]
v_pk_fma_f32 v[108:109], v[68:69], v[8:9], v[108:109] op_sel_hi:[1,0,1]
v_pk_mul_f32 v[108:109], v[108:109], v[10:11] op_sel_hi:[1,0]
v_pk_fma_f32 v[110:111], v[70:71], v[8:9], v[110:111] op_sel_hi:[1,0,1]
v_pk_mul_f32 v[110:111], v[110:111], v[10:11] op_sel_hi:[1,0]
v_pk_fma_f32 v[112:113], v[72:73], v[8:9], v[112:113] op_sel_hi:[1,0,1]
v_pk_mul_f32 v[112:113], v[112:113], v[10:11] op_sel_hi:[1,0]
v_pk_fma_f32 v[114:115], v[74:75], v[8:9], v[114:115] op_sel_hi:[1,0,1]
v_pk_mul_f32 v[114:115], v[114:115], v[10:11] op_sel_hi:[1,0]
s_waitcnt vmcnt(0)
v_cmp_gt_u32 vcc, v13, v14
s_nop 0
s_cmp_eq_u64 vcc, 0
s_cselect_b32 s35, 0, 0x40000000
s_add_u32 s48, s34, 0
s_cmp_ge_u32 s31, 2
s_cselect_b32 s48, s48, 0x40000000
s_or_b32 s48, s48, s35
s_add_u32 s49, s48, 4096
buffer_load_dwordx4 v[192:195], v0, s[40:43], s48 offen sc1
buffer_load_dwordx4 v[196:199], v0, s[40:43], s48 offen offset:1024 sc1
buffer_load_dwordx4 v[200:203], v0, s[40:43], s48 offen offset:2048 sc1
buffer_load_dwordx4 v[204:207], v0, s[40:43], s48 offen offset:3072 sc1
buffer_load_dwordx4 v[208:211], v0, s[40:43], s49 offen sc1
buffer_load_dwordx4 v[212:215], v0, s[40:43], s49 offen offset:1024 sc1
s_add_u32 s48, s34, 196608
s_cmp_eq_u32 s31, 7
s_cselect_b32 s48, s48, 0x40000000
s_or_b32 s48, s48, s35
s_add_u32 s49, s48, 4096
buffer_load_dwordx4 v[56:59], v0, s[40:43], s48 offen sc1
buffer_load_dwordx4 v[60:63], v0, s[40:43], s48 offen offset:1024 sc1
buffer_load_dwordx4 v[64:67], v0, s[40:43], s48 offen offset:2048 sc1
buffer_load_dwordx4 v[68:71], v0, s[40:43], s48 offen offset:3072 sc1
buffer_load_dwordx4 v[72:75], v0, s[40:43], s49 offen sc1
buffer_load_dwordx4 v[76:79], v0, s[40:43], s49 offen offset:1024 sc1
s_add_u32 s48, s34, 589824
s_cmp_eq_u32 s31, 7
s_cselect_b32 s48, s48, 0x40000000
s_or_b32 s48, s48, s35
s_add_u32 s49, s48, 4096
buffer_load_dwordx4 v[164:167], v0, s[40:43], s48 offen sc1
buffer_load_dwordx4 v[168:171], v0, s[40:43], s48 offen offset:1024 sc1
buffer_load_dwordx4 v[172:175], v0, s[40:43], s48 offen offset:2048 sc1
buffer_load_dwordx4 v[176:179], v0, s[40:43], s48 offen offset:3072 sc1
buffer_load_dwordx4 v[180:183], v0, s[40:43], s49 offen sc1
buffer_load_dwordx4 v[184:187], v0, s[40:43], s49 offen offset:1024 sc1
v_max3_f32 v8, v96, v97, v98
v_max3_f32 v8, v8, v99, v100
v_max3_f32 v8, v8, v101, v102
v_max3_f32 v8, v8, v103, v104
v_max3_f32 v8, v8, v105, v106
v_max3_f32 v8, v8, v107, v108
v_max3_f32 v8, v8, v109, v110
v_max3_f32 v8, v8, v111, v112
v_max3_f32 v8, v8, v113, v114
v_max_f32 v8, v8, v115
v_min3_f32 v9, v96, v97, v98
v_min3_f32 v9, v9, v99, v100
v_min3_f32 v9, v9, v101, v102
v_min3_f32 v9, v9, v103, v104
v_min3_f32 v9, v9, v105, v106
v_min3_f32 v9, v9, v107, v108
v_min3_f32 v9, v9, v109, v110
v_min3_f32 v9, v9, v111, v112
v_min3_f32 v9, v9, v113, v114
v_min_f32 v9, v9, v115
v_mul_f32 v8, 0xbfb8aa3b, v8
v_mul_f32 v10, 0x3fb8aa3b, v9
v_pk_fma_f32 v[16:17], v[96:97], s[22:23], v[8:9] op_sel_hi:[1,1,0]
v_pk_fma_f32 v[36:37], v[96:97], s[24:25], v[10:11] op_sel_hi:[1,1,0]
v_pk_fma_f32 v[18:19], v[98:99], s[22:23], v[8:9] op_sel_hi:[1,1,0]
v_pk_fma_f32 v[38:39], v[98:99], s[24:25], v[10:11] op_sel_hi:[1,1,0]
v_pk_fma_f32 v[20:21], v[100:101], s[22:23], v[8:9] op_sel_hi:[1,1,0]
v_pk_fma_f32 v[40:41], v[100:101], s[24:25], v[10:11] op_sel_hi:[1,1,0]
v_pk_fma_f32 v[22:23], v[102:103], s[22:23], v[8:9] op_sel_hi:[1,1,0]
v_pk_fma_f32 v[42:43], v[102:103], s[24:25], v[10:11] op_sel_hi:[1,1,0]
v_pk_fma_f32 v[24:25], v[104:105], s[22:23], v[8:9] op_sel_hi:[1,1,0]
v_pk_fma_f32 v[44:45], v[104:105], s[24:25], v[10:11] op_sel_hi:[1,1,0]
v_pk_fma_f32 v[26:27], v[106:107], s[22:23], v[8:9] op_sel_hi:[1,1,0]
v_pk_fma_f32 v[46:47], v[106:107], s[24:25], v[10:11] op_sel_hi:[1,1,0]
v_pk_fma_f32 v[28:29], v[108:109], s[22:23], v[8:9] op_sel_hi:[1,1,0]
v_pk_fma_f32 v[48:49], v[108:109], s[24:25], v[10:11] op_sel_hi:[1,1,0]
v_pk_fma_f32 v[30:31], v[110:111], s[22:23], v[8:9] op_sel_hi:[1,1,0]
v_pk_fma_f32 v[50:51], v[110:111], s[24:25], v[10:11] op_sel_hi:[1,1,0]
v_pk_fma_f32 v[32:33], v[112:113], s[22:23], v[8:9] op_sel_hi:[1,1,0]
v_pk_fma_f32 v[52:53], v[112:113], s[24:25], v[10:11] op_sel_hi:[1,1,0]
v_pk_fma_f32 v[34:35], v[114:115], s[22:23], v[8:9] op_sel_hi:[1,1,0]
v_pk_fma_f32 v[54:55], v[114:115], s[24:25], v[10:11] op_sel_hi:[1,1,0]
v_exp_f32 v16, v16
v_exp_f32 v17, v17
v_exp_f32 v18, v18
v_exp_f32 v19, v19
v_exp_f32 v20, v20
v_exp_f32 v21, v21
v_exp_f32 v22, v22
v_exp_f32 v23, v23
v_exp_f32 v24, v24
v_exp_f32 v25, v25
v_exp_f32 v26, v26
v_exp_f32 v27, v27
v_exp_f32 v28, v28
v_exp_f32 v29, v29
)"
R"(v_exp_f32 v30, v30
v_exp_f32 v31, v31
v_exp_f32 v32, v32
v_exp_f32 v33, v33
v_exp_f32 v34, v34
v_exp_f32 v35, v35
v_exp_f32 v36, v36
v_exp_f32 v37, v37
v_exp_f32 v38, v38
v_exp_f32 v39, v39
v_exp_f32 v40, v40
v_exp_f32 v41, v41
v_exp_f32 v42, v42
v_exp_f32 v43, v43
v_exp_f32 v44, v44
v_exp_f32 v45, v45
v_exp_f32 v46, v46
v_exp_f32 v47, v47
v_exp_f32 v48, v48
v_exp_f32 v49, v49
v_exp_f32 v50, v50
v_exp_f32 v51, v51
v_exp_f32 v52, v52
v_exp_f32 v53, v53
v_exp_f32 v54, v54
v_exp_f32 v55, v55
v_pk_add_f32 v[8:9], v[16:17], v[18:19]
v_pk_add_f32 v[8:9], v[8:9], v[20:21]
v_pk_add_f32 v[8:9], v[8:9], v[22:23]
v_pk_add_f32 v[8:9], v[8:9], v[24:25]
v_pk_add_f32 v[8:9], v[8:9], v[26:27]
v_pk_add_f32 v[8:9], v[8:9], v[28:29]
v_pk_add_f32 v[8:9], v[8:9], v[30:31]
v_pk_add_f32 v[8:9], v[8:9], v[32:33]
v_pk_add_f32 v[8:9], v[8:9], v[34:35]
v_add_f32 v8, v8, v9
v_pk_add_f32 v[10:11], v[36:37], v[38:39]
v_pk_add_f32 v[10:11], v[10:11], v[40:41]
v_pk_add_f32 v[10:11], v[10:11], v[42:43]
v_pk_add_f32 v[10:11], v[10:11], v[44:45]
v_pk_add_f32 v[10:11], v[10:11], v[46:47]
v_pk_add_f32 v[10:11], v[10:11], v[48:49]
v_pk_add_f32 v[10:11], v[10:11], v[50:51]
v_pk_add_f32 v[10:11], v[10:11], v[52:53]
v_pk_add_f32 v[10:11], v[10:11], v[54:55]
v_add_f32 v10, v10, v11
v_rcp_f32 v8, v8
v_rcp_f32 v10, v10
v_pk_mul_f32 v[16:17], v[16:17], v[8:9] op_sel_hi:[1,0]
v_pk_mul_f32 v[18:19], v[18:19], v[8:9] op_sel_hi:[1,0]
v_pk_mul_f32 v[20:21], v[20:21], v[8:9] op_sel_hi:[1,0]
v_pk_mul_f32 v[22:23], v[22:23], v[8:9] op_sel_hi:[1,0]
v_pk_mul_f32 v[24:25], v[24:25], v[8:9] op_sel_hi:[1,0]
v_pk_mul_f32 v[26:27], v[26:27], v[8:9] op_sel_hi:[1,0]
v_pk_mul_f32 v[28:29], v[28:29], v[8:9] op_sel_hi:[1,0]
v_pk_mul_f32 v[30:31], v[30:31], v[8:9] op_sel_hi:[1,0]
v_pk_mul_f32 v[32:33], v[32:33], v[8:9] op_sel_hi:[1,0]
v_pk_mul_f32 v[34:35], v[34:35], v[8:9] op_sel_hi:[1,0]
v_pk_mul_f32 v[36:37], v[36:37], v[10:11] op_sel_hi:[1,0]
v_pk_mul_f32 v[38:39], v[38:39], v[10:11] op_sel_hi:[1,0]
v_pk_mul_f32 v[40:41], v[40:41], v[10:11] op_sel_hi:[1,0]
v_pk_mul_f32 v[42:43], v[42:43], v[10:11] op_sel_hi:[1,0]
v_pk_mul_f32 v[44:45], v[44:45], v[10:11] op_sel_hi:[1,0]
v_pk_mul_f32 v[46:47], v[46:47], v[10:11] op_sel_hi:[1,0]
v_pk_mul_f32 v[48:49], v[48:49], v[10:11] op_sel_hi:[1,0]
v_pk_mul_f32 v[50:51], v[50:51], v[10:11] op_sel_hi:[1,0]
v_pk_mul_f32 v[52:53], v[52:53], v[10:11] op_sel_hi:[1,0]
v_pk_mul_f32 v[54:55], v[54:55], v[10:11] op_sel_hi:[1,0]
v_cvt_pk_bf16_f32 v216, v16, v17
v_cvt_pk_bf16_f32 v217, v18, v19
v_cvt_pk_bf16_f32 v218, v20, v21
v_cvt_pk_bf16_f32 v219, v22, v23
v_cvt_pk_bf16_f32 v220, v24, v25
v_cvt_pk_bf16_f32 v221, v26, v27
v_cvt_pk_bf16_f32 v222, v28, v29
v_cvt_pk_bf16_f32 v223, v30, v31
v_cvt_pk_bf16_f32 v224, v32, v33
v_cvt_pk_bf16_f32 v225, v34, v35
v_cvt_pk_bf16_f32 v226, v36, v37
v_cvt_pk_bf16_f32 v227, v38, v39
v_cvt_pk_bf16_f32 v228, v40, v41
v_cvt_pk_bf16_f32 v229, v42, v43
v_cvt_pk_bf16_f32 v230, v44, v45
v_cvt_pk_bf16_f32 v231, v46, v47
v_cvt_pk_bf16_f32 v232, v48, v49
v_cvt_pk_bf16_f32 v233, v50, v51
v_cvt_pk_bf16_f32 v234, v52, v53
v_cvt_pk_bf16_f32 v235, v54, v55
s_lshl3_add_u32 s48, s28, s27
s_mul_i32 s48, s48, 5120
s_add_u32 s48, s48, 149495808
s_add_u32 s49, s48, 4096
buffer_store_dwordx4 v[216:219], v0, s[40:43], s48 offen sc1
buffer_store_dwordx4 v[220:223], v0, s[40:43], s48 offen offset:1024 sc1
buffer_store_dwordx4 v[224:227], v0, s[40:43], s48 offen offset:2048 sc1
buffer_store_dwordx4 v[228:231], v0, s[40:43], s48 offen offset:3072 sc1
buffer_store_dwordx4 v[232:235], v0, s[40:43], s49 offen sc1
s_nop 1
s_branch hpbf_join_%=
hpbf_kv_%=:
ds_write_b128 v6, v[96:99] offset:0
ds_write_b128 v6, v[100:103] offset:1024
ds_write_b128 v6, v[104:107] offset:2048
ds_write_b128 v6, v[108:111] offset:3072
ds_write_b128 v6, v[112:115] offset:4096
ds_write_b128 v6, v[156:159] offset:5120
ds_write_b128 v6, v[160:163] offset:6144
s_waitcnt lgkmcnt(0)
s_barrier
ds_read_b128 v[16:19], v7 offset:0
ds_read_b128 v[20:23], v7 offset:1024
ds_read_b128 v[24:27], v7 offset:2048
ds_read_b128 v[28:31], v7 offset:3072
ds_read_b128 v[32:35], v7 offset:4096
ds_read_b128 v[36:39], v7 offset:5120
ds_read_b128 v[40:43], v7 offset:6144
ds_read_b128 v[44:47], v7 offset:7168
ds_read_b128 v[48:51], v7 offset:8192
ds_read_b128 v[52:55], v7 offset:9216
ds_read_b128 v[164:167], v7 offset:10240
ds_read_b128 v[168:171], v7 offset:11264
s_waitcnt lgkmcnt(11)
v_pk_add_f32 v[116:117], v[116:117], v[16:17]
v_pk_add_f32 v[118:119], v[118:119], v[18:19]
s_waitcnt lgkmcnt(10)
v_pk_add_f32 v[120:121], v[120:121], v[20:21]
v_pk_add_f32 v[122:123], v[122:123], v[22:23]
s_waitcnt lgkmcnt(9)
v_pk_add_f32 v[124:125], v[124:125], v[24:25]
v_pk_add_f32 v[126:127], v[126:127], v[26:27]
s_waitcnt lgkmcnt(8)
v_pk_add_f32 v[128:129], v[128:129], v[28:29]
v_pk_add_f32 v[130:131], v[130:131], v[30:31]
s_waitcnt lgkmcnt(7)
v_pk_add_f32 v[132:133], v[132:133], v[32:33]
v_pk_add_f32 v[134:135], v[134:135], v[34:35]
s_waitcnt lgkmcnt(6)
v_pk_add_f32 v[136:137], v[136:137], v[36:37]
v_pk_add_f32 v[138:139], v[138:139], v[38:39]
s_waitcnt lgkmcnt(5)
v_pk_add_f32 v[140:141], v[140:141], v[40:41]
v_pk_add_f32 v[142:143], v[142:143], v[42:43]
s_waitcnt lgkmcnt(4)
v_pk_add_f32 v[144:145], v[144:145], v[44:45]
v_pk_add_f32 v[146:147], v[146:147], v[46:47]
s_waitcnt lgkmcnt(3)
v_pk_add_f32 v[148:149], v[148:149], v[48:49]
v_pk_add_f32 v[150:151], v[150:151], v[50:51]
s_waitcnt lgkmcnt(2)
v_pk_add_f32 v[152:153], v[152:153], v[52:53]
v_pk_add_f32 v[154:155], v[154:155], v[54:55]
s_waitcnt lgkmcnt(1)
v_pk_add_f32 v[156:157], v[156:157], v[164:165]
v_pk_add_f32 v[158:159], v[158:159], v[166:167]
s_waitcnt lgkmcnt(0)
v_pk_add_f32 v[160:161], v[160:161], v[168:169]
v_pk_add_f32 v[162:163], v[162:163], v[170:171]
v_and_b32 v4, 1, v15
v_cmp_eq_u32 vcc, 1, v4
s_nop 1
v_cndmask_b32 v160, v160, v161, vcc
v_cndmask_b32 v162, v162, v163, vcc
v_and_b32 v4, 2, v15
v_cmp_eq_u32 vcc, 2, v4
s_nop 1
v_cndmask_b32 v160, v160, v162, vcc
v_bfe_u32 v4, v15, 2, 2
v_lshrrev_b32 v5, 4, v15
v_cmp_eq_u32 vcc, v4, v5
s_nop 1
v_cndmask_b32 v160, 0, v160, vcc
v_mov_b32 v161, v160
s_nop 1
v_permlane16_swap_b32 v160, v161
v_add_f32 v160, v160, v161
v_mov_b32 v161, v160
s_nop 1
v_permlane32_swap_b32 v160, v161
v_add_f32 v160, v160, v161
v_mul_f32 v8, 0x3acccccd, v156
v_mul_f32 v160, 0x3acccccd, v160
v_fma_f32 v160, -v8, v8, v160
v_max_f32 v160, 0, v160
v_add_f32 v160, 0x3727c5ac, v160
v_rsq_f32 v10, v160
v_sub_f32 v8, 0, v8
v_pk_fma_f32 v[116:117], v[56:57], v[8:9], v[116:117] op_sel_hi:[1,0,1]
v_pk_mul_f32 v[116:117], v[116:117], v[10:11] op_sel_hi:[1,0]
v_pk_fma_f32 v[118:119], v[58:59], v[8:9], v[118:119] op_sel_hi:[1,0,1]
v_pk_mul_f32 v[118:119], v[118:119], v[10:11] op_sel_hi:[1,0]
v_pk_fma_f32 v[120:121], v[60:61], v[8:9], v[120:121] op_sel_hi:[1,0,1]
v_pk_mul_f32 v[120:121], v[120:121], v[10:11] op_sel_hi:[1,0]
v_pk_fma_f32 v[122:123], v[62:63], v[8:9], v[122:123] op_sel_hi:[1,0,1]
v_pk_mul_f32 v[122:123], v[122:123], v[10:11] op_sel_hi:[1,0]
v_pk_fma_f32 v[124:125], v[64:65], v[8:9], v[124:125] op_sel_hi:[1,0,1]
v_pk_mul_f32 v[124:125], v[124:125], v[10:11] op_sel_hi:[1,0]
v_pk_fma_f32 v[126:127], v[66:67], v[8:9], v[126:127] op_sel_hi:[1,0,1]
v_pk_mul_f32 v[126:127], v[126:127], v[10:11] op_sel_hi:[1,0]
v_pk_fma_f32 v[128:129], v[68:69], v[8:9], v[128:129] op_sel_hi:[1,0,1]
v_pk_mul_f32 v[128:129], v[128:129], v[10:11] op_sel_hi:[1,0]
)"
R"(v_pk_fma_f32 v[130:131], v[70:71], v[8:9], v[130:131] op_sel_hi:[1,0,1]
v_pk_mul_f32 v[130:131], v[130:131], v[10:11] op_sel_hi:[1,0]
v_pk_fma_f32 v[132:133], v[72:73], v[8:9], v[132:133] op_sel_hi:[1,0,1]
v_pk_mul_f32 v[132:133], v[132:133], v[10:11] op_sel_hi:[1,0]
v_pk_fma_f32 v[134:135], v[74:75], v[8:9], v[134:135] op_sel_hi:[1,0,1]
v_pk_mul_f32 v[134:135], v[134:135], v[10:11] op_sel_hi:[1,0]
v_pk_fma_f32 v[136:137], v[76:77], v[8:9], v[136:137] op_sel_hi:[1,0,1]
v_pk_mul_f32 v[136:137], v[136:137], v[10:11] op_sel_hi:[1,0]
v_pk_fma_f32 v[138:139], v[78:79], v[8:9], v[138:139] op_sel_hi:[1,0,1]
v_pk_mul_f32 v[138:139], v[138:139], v[10:11] op_sel_hi:[1,0]
v_pk_fma_f32 v[140:141], v[80:81], v[8:9], v[140:141] op_sel_hi:[1,0,1]
v_pk_mul_f32 v[140:141], v[140:141], v[10:11] op_sel_hi:[1,0]
v_pk_fma_f32 v[142:143], v[82:83], v[8:9], v[142:143] op_sel_hi:[1,0,1]
v_pk_mul_f32 v[142:143], v[142:143], v[10:11] op_sel_hi:[1,0]
v_pk_fma_f32 v[144:145], v[84:85], v[8:9], v[144:145] op_sel_hi:[1,0,1]
v_pk_mul_f32 v[144:145], v[144:145], v[10:11] op_sel_hi:[1,0]
v_pk_fma_f32 v[146:147], v[86:87], v[8:9], v[146:147] op_sel_hi:[1,0,1]
v_pk_mul_f32 v[146:147], v[146:147], v[10:11] op_sel_hi:[1,0]
v_pk_fma_f32 v[148:149], v[88:89], v[8:9], v[148:149] op_sel_hi:[1,0,1]
v_pk_mul_f32 v[148:149], v[148:149], v[10:11] op_sel_hi:[1,0]
v_pk_fma_f32 v[150:151], v[90:91], v[8:9], v[150:151] op_sel_hi:[1,0,1]
v_pk_mul_f32 v[150:151], v[150:151], v[10:11] op_sel_hi:[1,0]
v_pk_fma_f32 v[152:153], v[92:93], v[8:9], v[152:153] op_sel_hi:[1,0,1]
v_pk_mul_f32 v[152:153], v[152:153], v[10:11] op_sel_hi:[1,0]
v_pk_fma_f32 v[154:155], v[94:95], v[8:9], v[154:155] op_sel_hi:[1,0,1]
v_pk_mul_f32 v[154:155], v[154:155], v[10:11] op_sel_hi:[1,0]
s_waitcnt vmcnt(0)
v_cmp_gt_u32 vcc, v13, v14
s_nop 0
s_cmp_eq_u64 vcc, 0
s_cselect_b32 s35, 0, 0x40000000
s_add_u32 s48, s34, 0
s_cmp_ge_u32 s31, 2
s_cselect_b32 s48, s48, 0x40000000
s_or_b32 s48, s48, s35
s_add_u32 s49, s48, 4096
buffer_load_dwordx4 v[192:195], v0, s[40:43], s48 offen sc1
buffer_load_dwordx4 v[196:199], v0, s[40:43], s48 offen offset:1024 sc1
buffer_load_dwordx4 v[200:203], v0, s[40:43], s48 offen offset:2048 sc1
buffer_load_dwordx4 v[204:207], v0, s[40:43], s48 offen offset:3072 sc1
buffer_load_dwordx4 v[208:211], v0, s[40:43], s49 offen sc1
buffer_load_dwordx4 v[212:215], v0, s[40:43], s49 offen offset:1024 sc1
s_add_u32 s48, s34, 196608
s_cmp_eq_u32 s31, 7
s_cselect_b32 s48, s48, 0x40000000
s_or_b32 s48, s48, s35
s_add_u32 s49, s48, 4096
buffer_load_dwordx4 v[56:59], v0, s[40:43], s48 offen sc1
buffer_load_dwordx4 v[60:63], v0, s[40:43], s48 offen offset:1024 sc1
buffer_load_dwordx4 v[64:67], v0, s[40:43], s48 offen offset:2048 sc1
buffer_load_dwordx4 v[68:71], v0, s[40:43], s48 offen offset:3072 sc1
buffer_load_dwordx4 v[72:75], v0, s[40:43], s49 offen sc1
buffer_load_dwordx4 v[76:79], v0, s[40:43], s49 offen offset:1024 sc1
s_add_u32 s48, s34, 589824
s_cmp_eq_u32 s31, 7
s_cselect_b32 s48, s48, 0x40000000
s_or_b32 s48, s48, s35
s_add_u32 s49, s48, 4096
buffer_load_dwordx4 v[164:167], v0, s[40:43], s48 offen sc1
buffer_load_dwordx4 v[168:171], v0, s[40:43], s48 offen offset:1024 sc1
buffer_load_dwordx4 v[172:175], v0, s[40:43], s48 offen offset:2048 sc1
buffer_load_dwordx4 v[176:179], v0, s[40:43], s48 offen offset:3072 sc1
buffer_load_dwordx4 v[180:183], v0, s[40:43], s49 offen sc1
buffer_load_dwordx4 v[184:187], v0, s[40:43], s49 offen offset:1024 sc1
v_max3_f32 v8, v116, v117, v118
v_max3_f32 v8, v8, v119, v120
v_max3_f32 v8, v8, v121, v122
v_max3_f32 v8, v8, v123, v124
v_max3_f32 v8, v8, v125, v126
v_max3_f32 v8, v8, v127, v128
v_max3_f32 v8, v8, v129, v130
v_max3_f32 v8, v8, v131, v132
v_max3_f32 v8, v8, v133, v134
v_max_f32 v8, v8, v135
v_min3_f32 v9, v116, v117, v118
v_min3_f32 v9, v9, v119, v120
v_min3_f32 v9, v9, v121, v122
v_min3_f32 v9, v9, v123, v124
v_min3_f32 v9, v9, v125, v126
v_min3_f32 v9, v9, v127, v128
v_min3_f32 v9, v9, v129, v130
v_min3_f32 v9, v9, v131, v132
v_min3_f32 v9, v9, v133, v134
v_min_f32 v9, v9, v135
v_mul_f32 v8, 0xbfb8aa3b, v8
v_mul_f32 v10, 0x3fb8aa3b, v9
v_pk_fma_f32 v[16:17], v[116:117], s[22:23], v[8:9] op_sel_hi:[1,1,0]
v_pk_fma_f32 v[36:37], v[116:117], s[24:25], v[10:11] op_sel_hi:[1,1,0]
v_pk_fma_f32 v[18:19], v[118:119], s[22:23], v[8:9] op_sel_hi:[1,1,0]
v_pk_fma_f32 v[38:39], v[118:119], s[24:25], v[10:11] op_sel_hi:[1,1,0]
v_pk_fma_f32 v[20:21], v[120:121], s[22:23], v[8:9] op_sel_hi:[1,1,0]
v_pk_fma_f32 v[40:41], v[120:121], s[24:25], v[10:11] op_sel_hi:[1,1,0]
v_pk_fma_f32 v[22:23], v[122:123], s[22:23], v[8:9] op_sel_hi:[1,1,0]
v_pk_fma_f32 v[42:43], v[122:123], s[24:25], v[10:11] op_sel_hi:[1,1,0]
v_pk_fma_f32 v[24:25], v[124:125], s[22:23], v[8:9] op_sel_hi:[1,1,0]
v_pk_fma_f32 v[44:45], v[124:125], s[24:25], v[10:11] op_sel_hi:[1,1,0]
v_pk_fma_f32 v[26:27], v[126:127], s[22:23], v[8:9] op_sel_hi:[1,1,0]
v_pk_fma_f32 v[46:47], v[126:127], s[24:25], v[10:11] op_sel_hi:[1,1,0]
v_pk_fma_f32 v[28:29], v[128:129], s[22:23], v[8:9] op_sel_hi:[1,1,0]
v_pk_fma_f32 v[48:49], v[128:129], s[24:25], v[10:11] op_sel_hi:[1,1,0]
v_pk_fma_f32 v[30:31], v[130:131], s[22:23], v[8:9] op_sel_hi:[1,1,0]
v_pk_fma_f32 v[50:51], v[130:131], s[24:25], v[10:11] op_sel_hi:[1,1,0]
v_pk_fma_f32 v[32:33], v[132:133], s[22:23], v[8:9] op_sel_hi:[1,1,0]
v_pk_fma_f32 v[52:53], v[132:133], s[24:25], v[10:11] op_sel_hi:[1,1,0]
v_pk_fma_f32 v[34:35], v[134:135], s[22:23], v[8:9] op_sel_hi:[1,1,0]
v_pk_fma_f32 v[54:55], v[134:135], s[24:25], v[10:11] op_sel_hi:[1,1,0]
v_exp_f32 v16, v16
v_exp_f32 v17, v17
v_exp_f32 v18, v18
v_exp_f32 v19, v19
v_exp_f32 v20, v20
v_exp_f32 v21, v21
v_exp_f32 v22, v22
v_exp_f32 v23, v23
v_exp_f32 v24, v24
v_exp_f32 v25, v25
v_exp_f32 v26, v26
v_exp_f32 v27, v27
v_exp_f32 v28, v28
v_exp_f32 v29, v29
v_exp_f32 v30, v30
v_exp_f32 v31, v31
v_exp_f32 v32, v32
v_exp_f32 v33, v33
v_exp_f32 v34, v34
v_exp_f32 v35, v35
v_exp_f32 v36, v36
v_exp_f32 v37, v37
v_exp_f32 v38, v38
v_exp_f32 v39, v39
v_exp_f32 v40, v40
v_exp_f32 v41, v41
v_exp_f32 v42, v42
v_exp_f32 v43, v43
v_exp_f32 v44, v44
v_exp_f32 v45, v45
v_exp_f32 v46, v46
v_exp_f32 v47, v47
v_exp_f32 v48, v48
v_exp_f32 v49, v49
v_exp_f32 v50, v50
v_exp_f32 v51, v51
v_exp_f32 v52, v52
v_exp_f32 v53, v53
v_exp_f32 v54, v54
v_exp_f32 v55, v55
v_pk_add_f32 v[8:9], v[16:17], v[18:19]
v_pk_add_f32 v[8:9], v[8:9], v[20:21]
v_pk_add_f32 v[8:9], v[8:9], v[22:23]
v_pk_add_f32 v[8:9], v[8:9], v[24:25]
v_pk_add_f32 v[8:9], v[8:9], v[26:27]
v_pk_add_f32 v[8:9], v[8:9], v[28:29]
v_pk_add_f32 v[8:9], v[8:9], v[30:31]
v_pk_add_f32 v[8:9], v[8:9], v[32:33]
v_pk_add_f32 v[8:9], v[8:9], v[34:35]
v_add_f32 v8, v8, v9
v_pk_add_f32 v[10:11], v[36:37], v[38:39]
v_pk_add_f32 v[10:11], v[10:11], v[40:41]
v_pk_add_f32 v[10:11], v[10:11], v[42:43]
v_pk_add_f32 v[10:11], v[10:11], v[44:45]
v_pk_add_f32 v[10:11], v[10:11], v[46:47]
v_pk_add_f32 v[10:11], v[10:11], v[48:49]
v_pk_add_f32 v[10:11], v[10:11], v[50:51]
v_pk_add_f32 v[10:11], v[10:11], v[52:53]
v_pk_add_f32 v[10:11], v[10:11], v[54:55]
v_add_f32 v10, v10, v11
v_rcp_f32 v8, v8
v_rcp_f32 v10, v10
v_pk_mul_f32 v[16:17], v[16:17], v[8:9] op_sel_hi:[1,0]
v_pk_mul_f32 v[18:19], v[18:19], v[8:9] op_sel_hi:[1,0]
v_pk_mul_f32 v[20:21], v[20:21], v[8:9] op_sel_hi:[1,0]
v_pk_mul_f32 v[22:23], v[22:23], v[8:9] op_sel_hi:[1,0]
v_pk_mul_f32 v[24:25], v[24:25], v[8:9] op_sel_hi:[1,0]
v_pk_mul_f32 v[26:27], v[26:27], v[8:9] op_sel_hi:[1,0]
v_pk_mul_f32 v[28:29], v[28:29], v[8:9] op_sel_hi:[1,0]
v_pk_mul_f32 v[30:31], v[30:31], v[8:9] op_sel_hi:[1,0]
v_pk_mul_f32 v[32:33], v[32:33], v[8:9] op_sel_hi:[1,0]
v_pk_mul_f32 v[34:35], v[34:35], v[8:9] op_sel_hi:[1,0]
v_pk_mul_f32 v[36:37], v[36:37], v[10:11] op_sel_hi:[1,0]
v_pk_mul_f32 v[38:39], v[38:39], v[10:11] op_sel_hi:[1,0]
v_pk_mul_f32 v[40:41], v[40:41], v[10:11] op_sel_hi:[1,0]
v_pk_mul_f32 v[42:43], v[42:43], v[10:11] op_sel_hi:[1,0]
v_pk_mul_f32 v[44:45], v[44:45], v[10:11] op_sel_hi:[1,0]
v_pk_mul_f32 v[46:47], v[46:47], v[10:11] op_sel_hi:[1,0]
v_pk_mul_f32 v[48:49], v[48:49], v[10:11] op_sel_hi:[1,0]
v_pk_mul_f32 v[50:51], v[50:51], v[10:11] op_sel_hi:[1,0]
v_pk_mul_f32 v[52:53], v[52:53], v[10:11] op_sel_hi:[1,0]
v_pk_mul_f32 v[54:55], v[54:55], v[10:11] op_sel_hi:[1,0]
v_cvt_pk_bf16_f32 v216, v16, v17
v_cvt_pk_bf16_f32 v217, v18, v19
v_cvt_pk_bf16_f32 v218, v20, v21
v_cvt_pk_bf16_f32 v219, v22, v23
v_cvt_pk_bf16_f32 v220, v24, v25
v_cvt_pk_bf16_f32 v221, v26, v27
v_cvt_pk_bf16_f32 v222, v28, v29
v_cvt_pk_bf16_f32 v223, v30, v31
v_cvt_pk_bf16_f32 v224, v32, v33
v_cvt_pk_bf16_f32 v225, v34, v35
v_cvt_pk_bf16_f32 v226, v36, v37
v_cvt_pk_bf16_f32 v227, v38, v39
)"
R"(v_cvt_pk_bf16_f32 v228, v40, v41
v_cvt_pk_bf16_f32 v229, v42, v43
v_cvt_pk_bf16_f32 v230, v44, v45
v_cvt_pk_bf16_f32 v231, v46, v47
v_cvt_pk_bf16_f32 v232, v48, v49
v_cvt_pk_bf16_f32 v233, v50, v51
v_cvt_pk_bf16_f32 v234, v52, v53
v_cvt_pk_bf16_f32 v235, v54, v55
s_lshl_b32 s48, s29, 4
v_mov_b32 v236, 0
v_mov_b32 v237, 0
v_mov_b32 v238, 0
v_mov_b32 v239, 0
v_cvt_pk_bf16_f32 v240, v136, v137
v_cvt_pk_bf16_f32 v241, v138, v139
v_cvt_pk_bf16_f32 v242, v140, v141
v_cvt_pk_bf16_f32 v243, v142, v143
v_cvt_pk_bf16_f32 v244, v144, v145
v_cvt_pk_bf16_f32 v245, v146, v147
v_cvt_pk_bf16_f32 v246, v148, v149
v_cvt_pk_bf16_f32 v247, v150, v151
v_cvt_pk_bf16_f32 v248, v152, v153
v_cvt_pk_bf16_f32 v249, v154, v155
v_mov_b32 v250, 0x3f80
v_mov_b32 v251, 0
v_and_b32 v4, 15, v15
v_lshrrev_b32 v5, 4, v15
v_lshl_add_u32 v4, v5, 5, v4
v_add_u32 v4, s48, v4
v_mul_u32_u24 v4, 176, v4
ds_write_b128 v4, v[216:219] offset:0
ds_write_b128 v4, v[220:223] offset:16
ds_write_b128 v4, v[224:227] offset:32
ds_write_b128 v4, v[228:231] offset:48
ds_write_b128 v4, v[232:235] offset:64
ds_write_b128 v4, v[236:239] offset:80
ds_write_b128 v4, v[240:243] offset:96
ds_write_b128 v4, v[244:247] offset:112
ds_write_b128 v4, v[248:251] offset:128
ds_write_b128 v4, v[236:239] offset:144
hpbf_join_%=:
s_cmp_eq_u32 s35, 0
s_cbranch_scc1 hpbf_have_%=
s_mov_b32 s60, 0
hpbf_prev_%=:
v_mov_b32 v4, 0
buffer_atomic_add v4, v12, s[40:43], s58 offen sc0
s_waitcnt vmcnt(0)
v_cmp_gt_u32 vcc, v13, v4
s_nop 4
s_cbranch_vccz hpbf_prev_ok_%=
s_sleep 1
s_add_u32 s60, s60, 1
s_cmp_gt_u32 s60, 65536
s_cbranch_scc1 hp_abort_%=
s_and_b32 s59, s60, 63
s_cbranch_scc1 hpbf_prev_%=
v_mov_b32 v4, 1024
buffer_load_dword v4, v4, s[40:43], s58 offen sc1
s_waitcnt vmcnt(0)
v_readfirstlane_b32 s59, v4
s_nop 0
s_cmp_lg_u32 s59, 0
s_nop 2
s_cbranch_scc1 hp_end_%=
s_branch hpbf_prev_%=
hpbf_prev_ok_%=:
s_mov_b32 s35, 0
s_add_u32 s48, s34, 0
s_cmp_ge_u32 s31, 2
s_cselect_b32 s48, s48, 0x40000000
s_or_b32 s48, s48, s35
s_add_u32 s49, s48, 4096
buffer_load_dwordx4 v[192:195], v0, s[40:43], s48 offen sc1
buffer_load_dwordx4 v[196:199], v0, s[40:43], s48 offen offset:1024 sc1
buffer_load_dwordx4 v[200:203], v0, s[40:43], s48 offen offset:2048 sc1
buffer_load_dwordx4 v[204:207], v0, s[40:43], s48 offen offset:3072 sc1
buffer_load_dwordx4 v[208:211], v0, s[40:43], s49 offen sc1
buffer_load_dwordx4 v[212:215], v0, s[40:43], s49 offen offset:1024 sc1
s_add_u32 s48, s34, 196608
s_cmp_eq_u32 s31, 7
s_cselect_b32 s48, s48, 0x40000000
s_or_b32 s48, s48, s35
s_add_u32 s49, s48, 4096
buffer_load_dwordx4 v[56:59], v0, s[40:43], s48 offen sc1
buffer_load_dwordx4 v[60:63], v0, s[40:43], s48 offen offset:1024 sc1
buffer_load_dwordx4 v[64:67], v0, s[40:43], s48 offen offset:2048 sc1
buffer_load_dwordx4 v[68:71], v0, s[40:43], s48 offen offset:3072 sc1
buffer_load_dwordx4 v[72:75], v0, s[40:43], s49 offen sc1
buffer_load_dwordx4 v[76:79], v0, s[40:43], s49 offen offset:1024 sc1
s_add_u32 s48, s34, 589824
s_cmp_eq_u32 s31, 7
s_cselect_b32 s48, s48, 0x40000000
s_or_b32 s48, s48, s35
s_add_u32 s49, s48, 4096
buffer_load_dwordx4 v[164:167], v0, s[40:43], s48 offen sc1
buffer_load_dwordx4 v[168:171], v0, s[40:43], s48 offen offset:1024 sc1
buffer_load_dwordx4 v[172:175], v0, s[40:43], s48 offen offset:2048 sc1
buffer_load_dwordx4 v[176:179], v0, s[40:43], s48 offen offset:3072 sc1
buffer_load_dwordx4 v[180:183], v0, s[40:43], s49 offen sc1
buffer_load_dwordx4 v[184:187], v0, s[40:43], s49 offen offset:1024 sc1
hpbf_have_%=:
v_lshrrev_b32 v5, 2, v15
s_lshl_b32 s48, s45, 5
v_add_u32 v5, s48, v5
v_mul_u32_u24 v5, 176, v5
v_and_b32 v6, 3, v15
v_lshl_add_u32 v6, v6, 3, v5
s_waitcnt lgkmcnt(0)
s_barrier
ds_read_b64_tr_b16 v[96:97], v6 offset:0
ds_read_b64_tr_b16 v[98:99], v6 offset:2816
ds_read_b64_tr_b16 v[100:101], v6 offset:32
ds_read_b64_tr_b16 v[102:103], v6 offset:2848
ds_read_b64_tr_b16 v[104:105], v6 offset:64
ds_read_b64_tr_b16 v[106:107], v6 offset:2880
ds_read_b64_tr_b16 v[108:109], v6 offset:96
ds_read_b64_tr_b16 v[110:111], v6 offset:2912
ds_read_b64_tr_b16 v[112:113], v6 offset:128
ds_read_b64_tr_b16 v[114:115], v6 offset:2944
s_waitcnt lgkmcnt(2)
v_mfma_f32_16x16x32_bf16 v[120:123], v[96:99], v[108:111], 0
s_waitcnt lgkmcnt(0)
v_mfma_f32_16x16x32_bf16 v[124:127], v[96:99], v[112:115], 0
v_mfma_f32_16x16x32_bf16 v[128:131], v[100:103], v[108:111], 0
v_mfma_f32_16x16x32_bf16 v[132:135], v[100:103], v[112:115], 0
v_mfma_f32_16x16x32_bf16 v[136:139], v[104:107], v[108:111], 0
v_mfma_f32_16x16x32_bf16 v[140:143], v[104:107], v[112:115], 0
s_waitcnt vmcnt(0)
v_pk_add_f32 v[192:193], v[192:193], v[56:57]
v_pk_add_f32 v[194:195], v[194:195], v[58:59]
v_pk_add_f32 v[196:197], v[196:197], v[60:61]
v_pk_add_f32 v[198:199], v[198:199], v[62:63]
v_pk_add_f32 v[200:201], v[200:201], v[64:65]
v_pk_add_f32 v[202:203], v[202:203], v[66:67]
v_pk_add_f32 v[204:205], v[204:205], v[68:69]
v_pk_add_f32 v[206:207], v[206:207], v[70:71]
v_pk_add_f32 v[208:209], v[208:209], v[72:73]
v_pk_add_f32 v[210:211], v[210:211], v[74:75]
v_pk_add_f32 v[212:213], v[212:213], v[76:77]
v_pk_add_f32 v[214:215], v[214:215], v[78:79]
v_pk_add_f32 v[120:121], v[120:121], v[164:165]
v_pk_add_f32 v[122:123], v[122:123], v[166:167]
v_pk_add_f32 v[124:125], v[124:125], v[168:169]
v_pk_add_f32 v[126:127], v[126:127], v[170:171]
v_pk_add_f32 v[128:129], v[128:129], v[172:173]
v_pk_add_f32 v[130:131], v[130:131], v[174:175]
v_pk_add_f32 v[132:133], v[132:133], v[176:177]
v_pk_add_f32 v[134:135], v[134:135], v[178:179]
v_pk_add_f32 v[136:137], v[136:137], v[180:181]
v_pk_add_f32 v[138:139], v[138:139], v[182:183]
v_pk_add_f32 v[140:141], v[140:141], v[184:185]
v_pk_add_f32 v[142:143], v[142:143], v[186:187]
v_pk_add_f32 v[192:193], v[192:193], v[120:121]
v_pk_add_f32 v[194:195], v[194:195], v[122:123]
v_pk_add_f32 v[196:197], v[196:197], v[124:125]
v_pk_add_f32 v[198:199], v[198:199], v[126:127]
v_pk_add_f32 v[200:201], v[200:201], v[128:129]
v_pk_add_f32 v[202:203], v[202:203], v[130:131]
v_pk_add_f32 v[204:205], v[204:205], v[132:133]
v_pk_add_f32 v[206:207], v[206:207], v[134:135]
v_pk_add_f32 v[208:209], v[208:209], v[136:137]
v_pk_add_f32 v[210:211], v[210:211], v[138:139]
v_pk_add_f32 v[212:213], v[212:213], v[140:141]
v_pk_add_f32 v[214:215], v[214:215], v[142:143]
s_cmp_eq_u32 s31, 7
s_nop 0
s_cbranch_scc1 hpbf_fin_%=
s_add_u32 s48, s34, 393216
s_add_u32 s49, s48, 4096
buffer_store_dwordx4 v[192:195], v0, s[40:43], s48 offen sc1
buffer_store_dwordx4 v[196:199], v0, s[40:43], s48 offen offset:1024 sc1
buffer_store_dwordx4 v[200:203], v0, s[40:43], s48 offen offset:2048 sc1
buffer_store_dwordx4 v[204:207], v0, s[40:43], s48 offen offset:3072 sc1
buffer_store_dwordx4 v[208:211], v0, s[40:43], s49 offen sc1
buffer_store_dwordx4 v[212:215], v0, s[40:43], s49 offen offset:1024 sc1
s_lshl_b32 s54, s37, 11
s_add_u32 s54, s54, s36
s_add_u32 s54, s54, s58
s_branch hp_done_%=
hpbf_fin_%=:
v_bfe_u32 v4, v15, 2, 2
v_mul_u32_u24 v4, 1280, v4
v_lshrrev_b32 v5, 5, v15
v_lshl_add_u32 v4, v5, 8, v4
v_and_b32 v5, 3, v15
v_lshl_add_u32 v4, v5, 4, v4
v_bfe_u32 v5, v15, 4, 1
v_lshl_add_u32 v8, v5, 3, v4
v_and_b32 v4, 8, v15
v_cmp_eq_u32 vcc, 0, v4
v_mov_b32 v9, v8
v_mov_b32 v4, 2147483632
v_cndmask_b32 v9, v4, v9, vcc
v_cmp_gt_u32 vcc, 32, v15
v_mov_b32 v10, v8
v_mov_b32 v4, 2147483632
v_cndmask_b32 v10, v4, v10, vcc
v_mov_b32 v11, v9
v_mov_b32 v4, 2147483632
)"
R"(v_cndmask_b32 v11, v4, v11, vcc
s_mul_i32 s48, s27, 7680
s_lshl_b32 s49, s45, 6
s_add_u32 s48, s48, s49
s_add_u32 s48, s48, 162013184
s_add_u32 s49, s48, 5120
v_cvt_pk_bf16_f32 v96, v192, v193
v_cvt_pk_bf16_f32 v97, v194, v195
buffer_store_dwordx2 v[96:97], v8, s[40:43], s48 offen sc1
v_cvt_pk_bf16_f32 v98, v196, v197
v_cvt_pk_bf16_f32 v99, v198, v199
buffer_store_dwordx2 v[98:99], v9, s[40:43], s49 offen sc1
v_cvt_pk_bf16_f32 v100, v200, v201
v_cvt_pk_bf16_f32 v101, v202, v203
buffer_store_dwordx2 v[100:101], v8, s[40:43], s48 offen offset:512 sc1
v_cvt_pk_bf16_f32 v102, v204, v205
v_cvt_pk_bf16_f32 v103, v206, v207
buffer_store_dwordx2 v[102:103], v9, s[40:43], s49 offen offset:512 sc1
v_cvt_pk_bf16_f32 v104, v208, v209
v_cvt_pk_bf16_f32 v105, v210, v211
buffer_store_dwordx2 v[104:105], v10, s[40:43], s48 offen offset:1024 sc1
v_cvt_pk_bf16_f32 v106, v212, v213
v_cvt_pk_bf16_f32 v107, v214, v215
buffer_store_dwordx2 v[106:107], v11, s[40:43], s49 offen offset:1024 sc1
s_lshl_b32 s54, s21, 3
s_lshl_b32 s54, s54, 7
s_add_u32 s54, s54, 2370560
s_mov_b32 s55, 8
s_branch hp_donen_%=
hp_b1_%=:
s_and_b32 s26, s20, 7
s_lshr_b32 s27, s20, 3
s_lshl2_add_u32 s28, s27, s45
s_lshl_b32 s48, s45, 3
s_min_u32 s48, s48, 22
s_lshl_b32 s61, s48, 10
s_mul_i32 s50, s26, 307200
s_add_u32 s50, s50, s61
s_add_u32 s50, s50, 146210816
s_add_u32 m0, s61, 0
s_add_u32 s51, s50, 0
buffer_load_dwordx4 v0, s[40:43], s51 offen offset:0 sc1 lds
buffer_load_dwordx4 v0, s[40:43], s51 offen offset:1024 sc1 lds
buffer_load_dwordx4 v0, s[40:43], s51 offen offset:2048 sc1 lds
buffer_load_dwordx4 v0, s[40:43], s51 offen offset:3072 sc1 lds
s_add_u32 m0, s61, 4096
s_add_u32 s51, s50, 4096
buffer_load_dwordx4 v0, s[40:43], s51 offen offset:0 sc1 lds
buffer_load_dwordx4 v0, s[40:43], s51 offen offset:1024 sc1 lds
buffer_load_dwordx4 v0, s[40:43], s51 offen offset:2048 sc1 lds
buffer_load_dwordx4 v0, s[40:43], s51 offen offset:3072 sc1 lds
s_add_u32 m0, s61, 30720
s_add_u32 s51, s50, 30720
buffer_load_dwordx4 v0, s[40:43], s51 offen offset:0 sc1 lds
buffer_load_dwordx4 v0, s[40:43], s51 offen offset:1024 sc1 lds
buffer_load_dwordx4 v0, s[40:43], s51 offen offset:2048 sc1 lds
buffer_load_dwordx4 v0, s[40:43], s51 offen offset:3072 sc1 lds
s_add_u32 m0, s61, 34816
s_add_u32 s51, s50, 34816
buffer_load_dwordx4 v0, s[40:43], s51 offen offset:0 sc1 lds
buffer_load_dwordx4 v0, s[40:43], s51 offen offset:1024 sc1 lds
buffer_load_dwordx4 v0, s[40:43], s51 offen offset:2048 sc1 lds
buffer_load_dwordx4 v0, s[40:43], s51 offen offset:3072 sc1 lds
s_add_u32 m0, s61, 61440
s_add_u32 s51, s50, 61440
buffer_load_dwordx4 v0, s[40:43], s51 offen offset:0 sc1 lds
buffer_load_dwordx4 v0, s[40:43], s51 offen offset:1024 sc1 lds
buffer_load_dwordx4 v0, s[40:43], s51 offen offset:2048 sc1 lds
buffer_load_dwordx4 v0, s[40:43], s51 offen offset:3072 sc1 lds
s_add_u32 m0, s61, 65536
s_add_u32 s51, s50, 65536
buffer_load_dwordx4 v0, s[40:43], s51 offen offset:0 sc1 lds
buffer_load_dwordx4 v0, s[40:43], s51 offen offset:1024 sc1 lds
buffer_load_dwordx4 v0, s[40:43], s51 offen offset:2048 sc1 lds
buffer_load_dwordx4 v0, s[40:43], s51 offen offset:3072 sc1 lds
s_add_u32 m0, s61, 92160
s_add_u32 s51, s50, 92160
buffer_load_dwordx4 v0, s[40:43], s51 offen offset:0 sc1 lds
buffer_load_dwordx4 v0, s[40:43], s51 offen offset:1024 sc1 lds
buffer_load_dwordx4 v0, s[40:43], s51 offen offset:2048 sc1 lds
buffer_load_dwordx4 v0, s[40:43], s51 offen offset:3072 sc1 lds
s_add_u32 m0, s61, 96256
s_add_u32 s51, s50, 96256
buffer_load_dwordx4 v0, s[40:43], s51 offen offset:0 sc1 lds
buffer_load_dwordx4 v0, s[40:43], s51 offen offset:1024 sc1 lds
buffer_load_dwordx4 v0, s[40:43], s51 offen offset:2048 sc1 lds
buffer_load_dwordx4 v0, s[40:43], s51 offen offset:3072 sc1 lds
s_add_u32 m0, s61, 122880
s_add_u32 s51, s50, 122880
buffer_load_dwordx4 v0, s[40:43], s51 offen offset:0 sc1 lds
buffer_load_dwordx4 v0, s[40:43], s51 offen offset:1024 sc1 lds
buffer_load_dwordx4 v0, s[40:43], s51 offen offset:2048 sc1 lds
buffer_load_dwordx4 v0, s[40:43], s51 offen offset:3072 sc1 lds
s_add_u32 m0, s61, 126976
s_add_u32 s51, s50, 126976
buffer_load_dwordx4 v0, s[40:43], s51 offen offset:0 sc1 lds
buffer_load_dwordx4 v0, s[40:43], s51 offen offset:1024 sc1 lds
buffer_load_dwordx4 v0, s[40:43], s51 offen offset:2048 sc1 lds
buffer_load_dwordx4 v0, s[40:43], s51 offen offset:3072 sc1 lds
v_mov_b32 v188, 0x3f803f80
v_mov_b32 v189, 0x3f803f80
v_mov_b32 v190, 0x3f803f80
v_mov_b32 v191, 0x3f803f80
v_add_u32 v6, 61440, v0
v_add_u32 v7, 122880, v0
s_lshl_b32 s54, s21, 5
s_add_u32 s54, s54, s27
s_lshl_b32 s54, s54, 7
s_add_u32 s54, s54, 2354176
s_waitcnt vmcnt(0)
s_cmp_lg_u32 s45, 0
s_cbranch_scc1 hpb1_dep_ok_%=
v_cmp_eq_u32 vcc, 0, v15
s_nop 1
v_cndmask_b32 v14, 0, 8, vcc
s_mov_b32 s60, 0
hpb1_dep_%=:
v_mov_b32 v4, 0
buffer_atomic_add v4, v2, s[40:43], s54 offen sc0
s_waitcnt vmcnt(0)
v_cmp_gt_u32 vcc, v14, v4
s_nop 4
s_cbranch_vccz hpb1_dep_ok_%=
s_sleep 1
s_add_u32 s60, s60, 1
s_cmp_gt_u32 s60, 65536
s_cbranch_scc1 hp_abort_%=
s_and_b32 s59, s60, 63
s_cbranch_scc1 hpb1_dep_%=
v_mov_b32 v4, 1024
buffer_load_dword v4, v4, s[40:43], s58 offen sc1
s_waitcnt vmcnt(0)
v_readfirstlane_b32 s59, v4
s_nop 0
s_cmp_lg_u32 s59, 0
s_nop 2
s_cbranch_scc1 hp_end_%=
s_branch hpb1_dep_%=
hpb1_dep_ok_%=:
s_barrier
s_mul_i32 s52, s28, 20480
s_add_u32 s52, s52, 143589376
buffer_load_dwordx4 v[16:19], v0, s[40:43], s52 offen sc1
buffer_load_dwordx4 v[20:23], v0, s[40:43], s52 offen offset:1024 sc1
buffer_load_dwordx4 v[24:27], v0, s[40:43], s52 offen offset:2048 sc1
buffer_load_dwordx4 v[28:31], v0, s[40:43], s52 offen offset:3072 sc1
s_add_u32 s52, s52, 4096
buffer_load_dwordx4 v[32:35], v0, s[40:43], s52 offen sc1
buffer_load_dwordx4 v[36:39], v0, s[40:43], s52 offen offset:1024 sc1
buffer_load_dwordx4 v[40:43], v0, s[40:43], s52 offen offset:2048 sc1
buffer_load_dwordx4 v[44:47], v0, s[40:43], s52 offen offset:3072 sc1
s_add_u32 s52, s52, 4096
buffer_load_dwordx4 v[48:51], v0, s[40:43], s52 offen sc1
buffer_load_dwordx4 v[52:55], v0, s[40:43], s52 offen offset:1024 sc1
buffer_load_dwordx4 v[56:59], v0, s[40:43], s52 offen offset:2048 sc1
buffer_load_dwordx4 v[60:63], v0, s[40:43], s52 offen offset:3072 sc1
s_add_u32 s52, s52, 4096
buffer_load_dwordx4 v[64:67], v0, s[40:43], s52 offen sc1
buffer_load_dwordx4 v[68:71], v0, s[40:43], s52 offen offset:1024 sc1
buffer_load_dwordx4 v[72:75], v0, s[40:43], s52 offen offset:2048 sc1
buffer_load_dwordx4 v[76:79], v0, s[40:43], s52 offen offset:3072 sc1
s_add_u32 s52, s52, 4096
buffer_load_dwordx4 v[80:83], v0, s[40:43], s52 offen sc1
buffer_load_dwordx4 v[84:87], v0, s[40:43], s52 offen offset:1024 sc1
buffer_load_dwordx4 v[88:91], v0, s[40:43], s52 offen offset:2048 sc1
buffer_load_dwordx4 v[92:95], v0, s[40:43], s52 offen offset:3072 sc1
ds_read_b128 v[164:167], v0 offset:0
ds_read_b128 v[168:171], v0 offset:1024
ds_read_b128 v[172:175], v0 offset:2048
ds_read_b128 v[176:179], v0 offset:3072
ds_read_b128 v[180:183], v0 offset:4096
ds_read_b128 v[184:187], v0 offset:5120
s_waitcnt vmcnt(19) lgkmcnt(5)
v_mfma_f32_16x16x32_bf16 v[96:99], v[164:167], v[16:19], 0
ds_read_b128 v[164:167], v0 offset:6144
s_waitcnt lgkmcnt(5)
v_mfma_f32_16x16x32_bf16 v[100:103], v[168:171], v[16:19], 0
ds_read_b128 v[168:171], v0 offset:7168
s_waitcnt lgkmcnt(5)
v_mfma_f32_16x16x32_bf16 v[104:107], v[172:175], v[16:19], 0
ds_read_b128 v[172:175], v0 offset:8192
s_waitcnt lgkmcnt(5)
v_mfma_f32_16x16x32_bf16 v[108:111], v[176:179], v[16:19], 0
ds_read_b128 v[176:179], v0 offset:9216
s_waitcnt lgkmcnt(5)
v_mfma_f32_16x16x32_bf16 v[112:115], v[180:183], v[16:19], 0
ds_read_b128 v[180:183], v0 offset:10240
s_waitcnt lgkmcnt(5)
v_mfma_f32_16x16x32_bf16 v[116:119], v[184:187], v[16:19], 0
ds_read_b128 v[184:187], v0 offset:11264
s_waitcnt lgkmcnt(5)
v_mfma_f32_16x16x32_bf16 v[120:123], v[164:167], v[16:19], 0
ds_read_b128 v[164:167], v0 offset:12288
s_waitcnt lgkmcnt(5)
v_mfma_f32_16x16x32_bf16 v[124:127], v[168:171], v[16:19], 0
ds_read_b128 v[168:171], v0 offset:13312
v_mfma_f32_16x16x32_bf16 v[156:159], v[188:191], v[16:19], 0
s_waitcnt lgkmcnt(5)
v_mfma_f32_16x16x32_bf16 v[128:131], v[172:175], v[16:19], 0
ds_read_b128 v[172:175], v0 offset:14336
)"
R"(s_waitcnt lgkmcnt(5)
v_mfma_f32_16x16x32_bf16 v[132:135], v[176:179], v[16:19], 0
ds_read_b128 v[176:179], v0 offset:15360
s_waitcnt lgkmcnt(5)
v_mfma_f32_16x16x32_bf16 v[136:139], v[180:183], v[16:19], 0
ds_read_b128 v[180:183], v0 offset:16384
s_waitcnt lgkmcnt(5)
v_mfma_f32_16x16x32_bf16 v[140:143], v[184:187], v[16:19], 0
ds_read_b128 v[184:187], v0 offset:17408
s_waitcnt lgkmcnt(5)
v_mfma_f32_16x16x32_bf16 v[144:147], v[164:167], v[16:19], 0
ds_read_b128 v[164:167], v0 offset:18432
s_waitcnt lgkmcnt(5)
v_mfma_f32_16x16x32_bf16 v[148:151], v[168:171], v[16:19], 0
ds_read_b128 v[168:171], v0 offset:19456
s_waitcnt lgkmcnt(5)
v_mfma_f32_16x16x32_bf16 v[152:155], v[172:175], v[16:19], 0
ds_read_b128 v[172:175], v0 offset:20480
v_mfma_f32_16x16x32_bf16 v[160:163], v[16:19], v[16:19], 0
s_waitcnt vmcnt(18) lgkmcnt(5)
v_mfma_f32_16x16x32_bf16 v[96:99], v[176:179], v[20:23], v[96:99]
ds_read_b128 v[176:179], v0 offset:21504
s_waitcnt lgkmcnt(5)
v_mfma_f32_16x16x32_bf16 v[100:103], v[180:183], v[20:23], v[100:103]
ds_read_b128 v[180:183], v0 offset:22528
s_waitcnt lgkmcnt(5)
v_mfma_f32_16x16x32_bf16 v[104:107], v[184:187], v[20:23], v[104:107]
ds_read_b128 v[184:187], v0 offset:23552
s_waitcnt lgkmcnt(5)
v_mfma_f32_16x16x32_bf16 v[108:111], v[164:167], v[20:23], v[108:111]
ds_read_b128 v[164:167], v0 offset:24576
s_waitcnt lgkmcnt(5)
v_mfma_f32_16x16x32_bf16 v[112:115], v[168:171], v[20:23], v[112:115]
ds_read_b128 v[168:171], v0 offset:25600
s_waitcnt lgkmcnt(5)
v_mfma_f32_16x16x32_bf16 v[116:119], v[172:175], v[20:23], v[116:119]
ds_read_b128 v[172:175], v0 offset:26624
s_waitcnt lgkmcnt(5)
v_mfma_f32_16x16x32_bf16 v[120:123], v[176:179], v[20:23], v[120:123]
ds_read_b128 v[176:179], v0 offset:27648
s_waitcnt lgkmcnt(5)
v_mfma_f32_16x16x32_bf16 v[124:127], v[180:183], v[20:23], v[124:127]
ds_read_b128 v[180:183], v0 offset:28672
v_mfma_f32_16x16x32_bf16 v[156:159], v[188:191], v[20:23], v[156:159]
s_waitcnt lgkmcnt(5)
v_mfma_f32_16x16x32_bf16 v[128:131], v[184:187], v[20:23], v[128:131]
ds_read_b128 v[184:187], v0 offset:29696
s_waitcnt lgkmcnt(5)
v_mfma_f32_16x16x32_bf16 v[132:135], v[164:167], v[20:23], v[132:135]
ds_read_b128 v[164:167], v0 offset:30720
s_waitcnt lgkmcnt(5)
v_mfma_f32_16x16x32_bf16 v[136:139], v[168:171], v[20:23], v[136:139]
ds_read_b128 v[168:171], v0 offset:31744
s_waitcnt lgkmcnt(5)
v_mfma_f32_16x16x32_bf16 v[140:143], v[172:175], v[20:23], v[140:143]
ds_read_b128 v[172:175], v0 offset:32768
s_waitcnt lgkmcnt(5)
v_mfma_f32_16x16x32_bf16 v[144:147], v[176:179], v[20:23], v[144:147]
ds_read_b128 v[176:179], v0 offset:33792
s_waitcnt lgkmcnt(5)
v_mfma_f32_16x16x32_bf16 v[148:151], v[180:183], v[20:23], v[148:151]
ds_read_b128 v[180:183], v0 offset:34816
s_waitcnt lgkmcnt(5)
v_mfma_f32_16x16x32_bf16 v[152:155], v[184:187], v[20:23], v[152:155]
ds_read_b128 v[184:187], v0 offset:35840
v_mfma_f32_16x16x32_bf16 v[160:163], v[20:23], v[20:23], v[160:163]
s_waitcnt vmcnt(17) lgkmcnt(5)
v_mfma_f32_16x16x32_bf16 v[96:99], v[164:167], v[24:27], v[96:99]
s_barrier
ds_read_b128 v[164:167], v0 offset:36864
s_waitcnt lgkmcnt(5)
v_mfma_f32_16x16x32_bf16 v[100:103], v[168:171], v[24:27], v[100:103]
ds_read_b128 v[168:171], v0 offset:37888
s_add_u32 m0, s61, 0
s_add_u32 s51, s50, 153600
buffer_load_dwordx4 v0, s[40:43], s51 offen offset:0 sc1 lds
s_waitcnt lgkmcnt(5)
v_mfma_f32_16x16x32_bf16 v[104:107], v[172:175], v[24:27], v[104:107]
ds_read_b128 v[172:175], v0 offset:38912
s_waitcnt lgkmcnt(5)
v_mfma_f32_16x16x32_bf16 v[108:111], v[176:179], v[24:27], v[108:111]
ds_read_b128 v[176:179], v0 offset:39936
s_waitcnt lgkmcnt(5)
v_mfma_f32_16x16x32_bf16 v[112:115], v[180:183], v[24:27], v[112:115]
ds_read_b128 v[180:183], v0 offset:40960
buffer_load_dwordx4 v0, s[40:43], s51 offen offset:1024 sc1 lds
s_waitcnt lgkmcnt(5)
v_mfma_f32_16x16x32_bf16 v[116:119], v[184:187], v[24:27], v[116:119]
ds_read_b128 v[184:187], v0 offset:41984
s_waitcnt lgkmcnt(5)
v_mfma_f32_16x16x32_bf16 v[120:123], v[164:167], v[24:27], v[120:123]
ds_read_b128 v[164:167], v0 offset:43008
s_waitcnt lgkmcnt(5)
v_mfma_f32_16x16x32_bf16 v[124:127], v[168:171], v[24:27], v[124:127]
ds_read_b128 v[168:171], v0 offset:44032
buffer_load_dwordx4 v0, s[40:43], s51 offen offset:2048 sc1 lds
v_mfma_f32_16x16x32_bf16 v[156:159], v[188:191], v[24:27], v[156:159]
s_waitcnt lgkmcnt(5)
v_mfma_f32_16x16x32_bf16 v[128:131], v[172:175], v[24:27], v[128:131]
ds_read_b128 v[172:175], v0 offset:45056
s_waitcnt lgkmcnt(5)
v_mfma_f32_16x16x32_bf16 v[132:135], v[176:179], v[24:27], v[132:135]
ds_read_b128 v[176:179], v0 offset:46080
s_waitcnt lgkmcnt(5)
v_mfma_f32_16x16x32_bf16 v[136:139], v[180:183], v[24:27], v[136:139]
ds_read_b128 v[180:183], v0 offset:47104
buffer_load_dwordx4 v0, s[40:43], s51 offen offset:3072 sc1 lds
s_waitcnt lgkmcnt(5)
v_mfma_f32_16x16x32_bf16 v[140:143], v[184:187], v[24:27], v[140:143]
ds_read_b128 v[184:187], v0 offset:48128
s_waitcnt lgkmcnt(5)
v_mfma_f32_16x16x32_bf16 v[144:147], v[164:167], v[24:27], v[144:147]
ds_read_b128 v[164:167], v0 offset:49152
s_waitcnt lgkmcnt(5)
v_mfma_f32_16x16x32_bf16 v[148:151], v[168:171], v[24:27], v[148:151]
ds_read_b128 v[168:171], v0 offset:50176
s_add_u32 m0, s61, 4096
s_add_u32 s51, s50, 157696
buffer_load_dwordx4 v0, s[40:43], s51 offen offset:0 sc1 lds
s_waitcnt lgkmcnt(5)
v_mfma_f32_16x16x32_bf16 v[152:155], v[172:175], v[24:27], v[152:155]
ds_read_b128 v[172:175], v0 offset:51200
v_mfma_f32_16x16x32_bf16 v[160:163], v[24:27], v[24:27], v[160:163]
s_waitcnt vmcnt(21) lgkmcnt(5)
v_mfma_f32_16x16x32_bf16 v[96:99], v[176:179], v[28:31], v[96:99]
ds_read_b128 v[176:179], v0 offset:52224
s_waitcnt lgkmcnt(5)
v_mfma_f32_16x16x32_bf16 v[100:103], v[180:183], v[28:31], v[100:103]
ds_read_b128 v[180:183], v0 offset:53248
buffer_load_dwordx4 v0, s[40:43], s51 offen offset:1024 sc1 lds
s_waitcnt lgkmcnt(5)
v_mfma_f32_16x16x32_bf16 v[104:107], v[184:187], v[28:31], v[104:107]
ds_read_b128 v[184:187], v0 offset:54272
s_waitcnt lgkmcnt(5)
v_mfma_f32_16x16x32_bf16 v[108:111], v[164:167], v[28:31], v[108:111]
ds_read_b128 v[164:167], v0 offset:55296
s_waitcnt lgkmcnt(5)
v_mfma_f32_16x16x32_bf16 v[112:115], v[168:171], v[28:31], v[112:115]
ds_read_b128 v[168:171], v0 offset:56320
buffer_load_dwordx4 v0, s[40:43], s51 offen offset:2048 sc1 lds
s_waitcnt lgkmcnt(5)
v_mfma_f32_16x16x32_bf16 v[116:119], v[172:175], v[28:31], v[116:119]
ds_read_b128 v[172:175], v0 offset:57344
s_waitcnt lgkmcnt(5)
v_mfma_f32_16x16x32_bf16 v[120:123], v[176:179], v[28:31], v[120:123]
ds_read_b128 v[176:179], v0 offset:58368
s_waitcnt lgkmcnt(5)
v_mfma_f32_16x16x32_bf16 v[124:127], v[180:183], v[28:31], v[124:127]
ds_read_b128 v[180:183], v0 offset:59392
buffer_load_dwordx4 v0, s[40:43], s51 offen offset:3072 sc1 lds
v_mfma_f32_16x16x32_bf16 v[156:159], v[188:191], v[28:31], v[156:159]
s_waitcnt lgkmcnt(5)
v_mfma_f32_16x16x32_bf16 v[128:131], v[184:187], v[28:31], v[128:131]
ds_read_b128 v[184:187], v0 offset:60416
s_waitcnt lgkmcnt(5)
v_mfma_f32_16x16x32_bf16 v[132:135], v[164:167], v[28:31], v[132:135]
ds_read_b128 v[164:167], v6 offset:0
s_waitcnt lgkmcnt(5)
v_mfma_f32_16x16x32_bf16 v[136:139], v[168:171], v[28:31], v[136:139]
ds_read_b128 v[168:171], v6 offset:1024
s_waitcnt lgkmcnt(5)
v_mfma_f32_16x16x32_bf16 v[140:143], v[172:175], v[28:31], v[140:143]
ds_read_b128 v[172:175], v6 offset:2048
s_waitcnt lgkmcnt(5)
v_mfma_f32_16x16x32_bf16 v[144:147], v[176:179], v[28:31], v[144:147]
ds_read_b128 v[176:179], v6 offset:3072
s_waitcnt lgkmcnt(5)
v_mfma_f32_16x16x32_bf16 v[148:151], v[180:183], v[28:31], v[148:151]
ds_read_b128 v[180:183], v6 offset:4096
s_waitcnt lgkmcnt(5)
v_mfma_f32_16x16x32_bf16 v[152:155], v[184:187], v[28:31], v[152:155]
ds_read_b128 v[184:187], v6 offset:5120
v_mfma_f32_16x16x32_bf16 v[160:163], v[28:31], v[28:31], v[160:163]
s_waitcnt vmcnt(23) lgkmcnt(5)
v_mfma_f32_16x16x32_bf16 v[96:99], v[164:167], v[32:35], v[96:99]
s_barrier
ds_read_b128 v[164:167], v6 offset:6144
s_waitcnt lgkmcnt(5)
v_mfma_f32_16x16x32_bf16 v[100:103], v[168:171], v[32:35], v[100:103]
ds_read_b128 v[168:171], v6 offset:7168
s_add_u32 m0, s61, 30720
s_add_u32 s51, s50, 184320
buffer_load_dwordx4 v0, s[40:43], s51 offen offset:0 sc1 lds
s_waitcnt lgkmcnt(5)
v_mfma_f32_16x16x32_bf16 v[104:107], v[172:175], v[32:35], v[104:107]
ds_read_b128 v[172:175], v6 offset:8192
s_waitcnt lgkmcnt(5)
v_mfma_f32_16x16x32_bf16 v[108:111], v[176:179], v[32:35], v[108:111]
ds_read_b128 v[176:179], v6 offset:9216
s_waitcnt lgkmcnt(5)
v_mfma_f32_16x16x32_bf16 v[112:115], v[180:183], v[32:35], v[112:115]
ds_read_b128 v[180:183], v6 offset:10240
buffer_load_dwordx4 v0, s[40:43], s51 offen offset:1024 sc1 lds
s_waitcnt lgkmcnt(5)
v_mfma_f32_16x16x32_bf16 v[116:119], v[184:187], v[32:35], v[116:119]
ds_read_b128 v[184:187], v6 offset:11264
s_waitcnt lgkmcnt(5)
v_mfma_f32_16x16x32_bf16 v[120:123], v[164:167], v[32:35], v[120:123]
ds_read_b128 v[164:167], v6 offset:12288
s_waitcnt lgkmcnt(5)
)"
R"(v_mfma_f32_16x16x32_bf16 v[124:127], v[168:171], v[32:35], v[124:127]
ds_read_b128 v[168:171], v6 offset:13312
buffer_load_dwordx4 v0, s[40:43], s51 offen offset:2048 sc1 lds
v_mfma_f32_16x16x32_bf16 v[156:159], v[188:191], v[32:35], v[156:159]
s_waitcnt lgkmcnt(5)
v_mfma_f32_16x16x32_bf16 v[128:131], v[172:175], v[32:35], v[128:131]
ds_read_b128 v[172:175], v6 offset:14336
s_waitcnt lgkmcnt(5)
v_mfma_f32_16x16x32_bf16 v[132:135], v[176:179], v[32:35], v[132:135]
ds_read_b128 v[176:179], v6 offset:15360
s_waitcnt lgkmcnt(5)
v_mfma_f32_16x16x32_bf16 v[136:139], v[180:183], v[32:35], v[136:139]
ds_read_b128 v[180:183], v6 offset:16384
buffer_load_dwordx4 v0, s[40:43], s51 offen offset:3072 sc1 lds
s_waitcnt lgkmcnt(5)
v_mfma_f32_16x16x32_bf16 v[140:143], v[184:187], v[32:35], v[140:143]
ds_read_b128 v[184:187], v6 offset:17408
s_waitcnt lgkmcnt(5)
v_mfma_f32_16x16x32_bf16 v[144:147], v[164:167], v[32:35], v[144:147]
ds_read_b128 v[164:167], v6 offset:18432
s_waitcnt lgkmcnt(5)
v_mfma_f32_16x16x32_bf16 v[148:151], v[168:171], v[32:35], v[148:151]
ds_read_b128 v[168:171], v6 offset:19456
s_add_u32 m0, s61, 34816
s_add_u32 s51, s50, 188416
buffer_load_dwordx4 v0, s[40:43], s51 offen offset:0 sc1 lds
s_waitcnt lgkmcnt(5)
v_mfma_f32_16x16x32_bf16 v[152:155], v[172:175], v[32:35], v[152:155]
ds_read_b128 v[172:175], v6 offset:20480
v_mfma_f32_16x16x32_bf16 v[160:163], v[32:35], v[32:35], v[160:163]
s_waitcnt vmcnt(27) lgkmcnt(5)
v_mfma_f32_16x16x32_bf16 v[96:99], v[176:179], v[36:39], v[96:99]
ds_read_b128 v[176:179], v6 offset:21504
s_waitcnt lgkmcnt(5)
v_mfma_f32_16x16x32_bf16 v[100:103], v[180:183], v[36:39], v[100:103]
ds_read_b128 v[180:183], v6 offset:22528
buffer_load_dwordx4 v0, s[40:43], s51 offen offset:1024 sc1 lds
s_waitcnt lgkmcnt(5)
v_mfma_f32_16x16x32_bf16 v[104:107], v[184:187], v[36:39], v[104:107]
ds_read_b128 v[184:187], v6 offset:23552
s_waitcnt lgkmcnt(5)
v_mfma_f32_16x16x32_bf16 v[108:111], v[164:167], v[36:39], v[108:111]
ds_read_b128 v[164:167], v6 offset:24576
s_waitcnt lgkmcnt(5)
v_mfma_f32_16x16x32_bf16 v[112:115], v[168:171], v[36:39], v[112:115]
ds_read_b128 v[168:171], v6 offset:25600
buffer_load_dwordx4 v0, s[40:43], s51 offen offset:2048 sc1 lds
s_waitcnt lgkmcnt(5)
v_mfma_f32_16x16x32_bf16 v[116:119], v[172:175], v[36:39], v[116:119]
ds_read_b128 v[172:175], v6 offset:26624
s_waitcnt lgkmcnt(5)
v_mfma_f32_16x16x32_bf16 v[120:123], v[176:179], v[36:39], v[120:123]
ds_read_b128 v[176:179], v6 offset:27648
s_waitcnt lgkmcnt(5)
v_mfma_f32_16x16x32_bf16 v[124:127], v[180:183], v[36:39], v[124:127]
ds_read_b128 v[180:183], v6 offset:28672
buffer_load_dwordx4 v0, s[40:43], s51 offen offset:3072 sc1 lds
v_mfma_f32_16x16x32_bf16 v[156:159], v[188:191], v[36:39], v[156:159]
s_waitcnt lgkmcnt(5)
v_mfma_f32_16x16x32_bf16 v[128:131], v[184:187], v[36:39], v[128:131]
ds_read_b128 v[184:187], v6 offset:29696
s_waitcnt lgkmcnt(5)
v_mfma_f32_16x16x32_bf16 v[132:135], v[164:167], v[36:39], v[132:135]
ds_read_b128 v[164:167], v6 offset:30720
s_waitcnt lgkmcnt(5)
v_mfma_f32_16x16x32_bf16 v[136:139], v[168:171], v[36:39], v[136:139]
ds_read_b128 v[168:171], v6 offset:31744
s_waitcnt lgkmcnt(5)
v_mfma_f32_16x16x32_bf16 v[140:143], v[172:175], v[36:39], v[140:143]
ds_read_b128 v[172:175], v6 offset:32768
s_waitcnt lgkmcnt(5)
v_mfma_f32_16x16x32_bf16 v[144:147], v[176:179], v[36:39], v[144:147]
ds_read_b128 v[176:179], v6 offset:33792
s_waitcnt lgkmcnt(5)
v_mfma_f32_16x16x32_bf16 v[148:151], v[180:183], v[36:39], v[148:151]
ds_read_b128 v[180:183], v6 offset:34816
s_waitcnt lgkmcnt(5)
v_mfma_f32_16x16x32_bf16 v[152:155], v[184:187], v[36:39], v[152:155]
ds_read_b128 v[184:187], v6 offset:35840
v_mfma_f32_16x16x32_bf16 v[160:163], v[36:39], v[36:39], v[160:163]
s_waitcnt vmcnt(29) lgkmcnt(5)
v_mfma_f32_16x16x32_bf16 v[96:99], v[164:167], v[40:43], v[96:99]
s_barrier
ds_read_b128 v[164:167], v6 offset:36864
s_waitcnt lgkmcnt(5)
v_mfma_f32_16x16x32_bf16 v[100:103], v[168:171], v[40:43], v[100:103]
ds_read_b128 v[168:171], v6 offset:37888
s_add_u32 m0, s61, 61440
s_add_u32 s51, s50, 215040
buffer_load_dwordx4 v0, s[40:43], s51 offen offset:0 sc1 lds
s_waitcnt lgkmcnt(5)
v_mfma_f32_16x16x32_bf16 v[104:107], v[172:175], v[40:43], v[104:107]
ds_read_b128 v[172:175], v6 offset:38912
s_waitcnt lgkmcnt(5)
v_mfma_f32_16x16x32_bf16 v[108:111], v[176:179], v[40:43], v[108:111]
ds_read_b128 v[176:179], v6 offset:39936
s_waitcnt lgkmcnt(5)
v_mfma_f32_16x16x32_bf16 v[112:115], v[180:183], v[40:43], v[112:115]
ds_read_b128 v[180:183], v6 offset:40960
buffer_load_dwordx4 v0, s[40:43], s51 offen offset:1024 sc1 lds
s_waitcnt lgkmcnt(5)
v_mfma_f32_16x16x32_bf16 v[116:119], v[184:187], v[40:43], v[116:119]
ds_read_b128 v[184:187], v6 offset:41984
s_waitcnt lgkmcnt(5)
v_mfma_f32_16x16x32_bf16 v[120:123], v[164:167], v[40:43], v[120:123]
ds_read_b128 v[164:167], v6 offset:43008
s_waitcnt lgkmcnt(5)
v_mfma_f32_16x16x32_bf16 v[124:127], v[168:171], v[40:43], v[124:127]
ds_read_b128 v[168:171], v6 offset:44032
buffer_load_dwordx4 v0, s[40:43], s51 offen offset:2048 sc1 lds
v_mfma_f32_16x16x32_bf16 v[156:159], v[188:191], v[40:43], v[156:159]
s_waitcnt lgkmcnt(5)
v_mfma_f32_16x16x32_bf16 v[128:131], v[172:175], v[40:43], v[128:131]
ds_read_b128 v[172:175], v6 offset:45056
s_waitcnt lgkmcnt(5)
v_mfma_f32_16x16x32_bf16 v[132:135], v[176:179], v[40:43], v[132:135]
ds_read_b128 v[176:179], v6 offset:46080
s_waitcnt lgkmcnt(5)
v_mfma_f32_16x16x32_bf16 v[136:139], v[180:183], v[40:43], v[136:139]
ds_read_b128 v[180:183], v6 offset:47104
buffer_load_dwordx4 v0, s[40:43], s51 offen offset:3072 sc1 lds
s_waitcnt lgkmcnt(5)
v_mfma_f32_16x16x32_bf16 v[140:143], v[184:187], v[40:43], v[140:143]
ds_read_b128 v[184:187], v6 offset:48128
s_waitcnt lgkmcnt(5)
v_mfma_f32_16x16x32_bf16 v[144:147], v[164:167], v[40:43], v[144:147]
ds_read_b128 v[164:167], v6 offset:49152
s_waitcnt lgkmcnt(5)
v_mfma_f32_16x16x32_bf16 v[148:151], v[168:171], v[40:43], v[148:151]
ds_read_b128 v[168:171], v6 offset:50176
s_add_u32 m0, s61, 65536
s_add_u32 s51, s50, 219136
buffer_load_dwordx4 v0, s[40:43], s51 offen offset:0 sc1 lds
s_waitcnt lgkmcnt(5)
v_mfma_f32_16x16x32_bf16 v[152:155], v[172:175], v[40:43], v[152:155]
ds_read_b128 v[172:175], v6 offset:51200
v_mfma_f32_16x16x32_bf16 v[160:163], v[40:43], v[40:43], v[160:163]
s_waitcnt vmcnt(33) lgkmcnt(5)
v_mfma_f32_16x16x32_bf16 v[96:99], v[176:179], v[44:47], v[96:99]
ds_read_b128 v[176:179], v6 offset:52224
s_waitcnt lgkmcnt(5)
v_mfma_f32_16x16x32_bf16 v[100:103], v[180:183], v[44:47], v[100:103]
ds_read_b128 v[180:183], v6 offset:53248
buffer_load_dwordx4 v0, s[40:43], s51 offen offset:1024 sc1 lds
s_waitcnt lgkmcnt(5)
v_mfma_f32_16x16x32_bf16 v[104:107], v[184:187], v[44:47], v[104:107]
ds_read_b128 v[184:187], v6 offset:54272
s_waitcnt lgkmcnt(5)
v_mfma_f32_16x16x32_bf16 v[108:111], v[164:167], v[44:47], v[108:111]
ds_read_b128 v[164:167], v6 offset:55296
s_waitcnt lgkmcnt(5)
v_mfma_f32_16x16x32_bf16 v[112:115], v[168:171], v[44:47], v[112:115]
ds_read_b128 v[168:171], v6 offset:56320
buffer_load_dwordx4 v0, s[40:43], s51 offen offset:2048 sc1 lds
s_waitcnt lgkmcnt(5)
v_mfma_f32_16x16x32_bf16 v[116:119], v[172:175], v[44:47], v[116:119]
ds_read_b128 v[172:175], v6 offset:57344
s_waitcnt lgkmcnt(5)
v_mfma_f32_16x16x32_bf16 v[120:123], v[176:179], v[44:47], v[120:123]
ds_read_b128 v[176:179], v6 offset:58368
s_waitcnt lgkmcnt(5)
v_mfma_f32_16x16x32_bf16 v[124:127], v[180:183], v[44:47], v[124:127]
ds_read_b128 v[180:183], v6 offset:59392
buffer_load_dwordx4 v0, s[40:43], s51 offen offset:3072 sc1 lds
v_mfma_f32_16x16x32_bf16 v[156:159], v[188:191], v[44:47], v[156:159]
s_waitcnt lgkmcnt(5)
v_mfma_f32_16x16x32_bf16 v[128:131], v[184:187], v[44:47], v[128:131]
ds_read_b128 v[184:187], v6 offset:60416
s_waitcnt lgkmcnt(5)
v_mfma_f32_16x16x32_bf16 v[132:135], v[164:167], v[44:47], v[132:135]
ds_read_b128 v[164:167], v7 offset:0
s_waitcnt lgkmcnt(5)
v_mfma_f32_16x16x32_bf16 v[136:139], v[168:171], v[44:47], v[136:139]
ds_read_b128 v[168:171], v7 offset:1024
s_waitcnt lgkmcnt(5)
v_mfma_f32_16x16x32_bf16 v[140:143], v[172:175], v[44:47], v[140:143]
ds_read_b128 v[172:175], v7 offset:2048
s_waitcnt lgkmcnt(5)
v_mfma_f32_16x16x32_bf16 v[144:147], v[176:179], v[44:47], v[144:147]
ds_read_b128 v[176:179], v7 offset:3072
s_waitcnt lgkmcnt(5)
v_mfma_f32_16x16x32_bf16 v[148:151], v[180:183], v[44:47], v[148:151]
ds_read_b128 v[180:183], v7 offset:4096
s_waitcnt lgkmcnt(5)
v_mfma_f32_16x16x32_bf16 v[152:155], v[184:187], v[44:47], v[152:155]
ds_read_b128 v[184:187], v7 offset:5120
v_mfma_f32_16x16x32_bf16 v[160:163], v[44:47], v[44:47], v[160:163]
s_waitcnt vmcnt(35) lgkmcnt(5)
v_mfma_f32_16x16x32_bf16 v[96:99], v[164:167], v[48:51], v[96:99]
s_barrier
ds_read_b128 v[164:167], v7 offset:6144
s_waitcnt lgkmcnt(5)
v_mfma_f32_16x16x32_bf16 v[100:103], v[168:171], v[48:51], v[100:103]
ds_read_b128 v[168:171], v7 offset:7168
s_add_u32 m0, s61, 92160
s_add_u32 s51, s50, 245760
buffer_load_dwordx4 v0, s[40:43], s51 offen offset:0 sc1 lds
s_waitcnt lgkmcnt(5)
v_mfma_f32_16x16x32_bf16 v[104:107], v[172:175], v[48:51], v[104:107]
ds_read_b128 v[172:175], v7 offset:8192
)"
R"(s_waitcnt lgkmcnt(5)
v_mfma_f32_16x16x32_bf16 v[108:111], v[176:179], v[48:51], v[108:111]
ds_read_b128 v[176:179], v7 offset:9216
s_waitcnt lgkmcnt(5)
v_mfma_f32_16x16x32_bf16 v[112:115], v[180:183], v[48:51], v[112:115]
ds_read_b128 v[180:183], v7 offset:10240
buffer_load_dwordx4 v0, s[40:43], s51 offen offset:1024 sc1 lds
s_waitcnt lgkmcnt(5)
v_mfma_f32_16x16x32_bf16 v[116:119], v[184:187], v[48:51], v[116:119]
ds_read_b128 v[184:187], v7 offset:11264
s_waitcnt lgkmcnt(5)
v_mfma_f32_16x16x32_bf16 v[120:123], v[164:167], v[48:51], v[120:123]
ds_read_b128 v[164:167], v7 offset:12288
s_waitcnt lgkmcnt(5)
v_mfma_f32_16x16x32_bf16 v[124:127], v[168:171], v[48:51], v[124:127]
ds_read_b128 v[168:171], v7 offset:13312
buffer_load_dwordx4 v0, s[40:43], s51 offen offset:2048 sc1 lds
v_mfma_f32_16x16x32_bf16 v[156:159], v[188:191], v[48:51], v[156:159]
s_waitcnt lgkmcnt(5)
v_mfma_f32_16x16x32_bf16 v[128:131], v[172:175], v[48:51], v[128:131]
ds_read_b128 v[172:175], v7 offset:14336
s_waitcnt lgkmcnt(5)
v_mfma_f32_16x16x32_bf16 v[132:135], v[176:179], v[48:51], v[132:135]
ds_read_b128 v[176:179], v7 offset:15360
s_waitcnt lgkmcnt(5)
v_mfma_f32_16x16x32_bf16 v[136:139], v[180:183], v[48:51], v[136:139]
ds_read_b128 v[180:183], v7 offset:16384
buffer_load_dwordx4 v0, s[40:43], s51 offen offset:3072 sc1 lds
s_waitcnt lgkmcnt(5)
v_mfma_f32_16x16x32_bf16 v[140:143], v[184:187], v[48:51], v[140:143]
ds_read_b128 v[184:187], v7 offset:17408
s_waitcnt lgkmcnt(5)
v_mfma_f32_16x16x32_bf16 v[144:147], v[164:167], v[48:51], v[144:147]
ds_read_b128 v[164:167], v7 offset:18432
s_waitcnt lgkmcnt(5)
v_mfma_f32_16x16x32_bf16 v[148:151], v[168:171], v[48:51], v[148:151]
ds_read_b128 v[168:171], v7 offset:19456
s_add_u32 m0, s61, 96256
s_add_u32 s51, s50, 249856
buffer_load_dwordx4 v0, s[40:43], s51 offen offset:0 sc1 lds
s_waitcnt lgkmcnt(5)
v_mfma_f32_16x16x32_bf16 v[152:155], v[172:175], v[48:51], v[152:155]
ds_read_b128 v[172:175], v7 offset:20480
v_mfma_f32_16x16x32_bf16 v[160:163], v[48:51], v[48:51], v[160:163]
s_waitcnt vmcnt(39) lgkmcnt(5)
v_mfma_f32_16x16x32_bf16 v[96:99], v[176:179], v[52:55], v[96:99]
ds_read_b128 v[176:179], v7 offset:21504
s_waitcnt lgkmcnt(5)
v_mfma_f32_16x16x32_bf16 v[100:103], v[180:183], v[52:55], v[100:103]
ds_read_b128 v[180:183], v7 offset:22528
buffer_load_dwordx4 v0, s[40:43], s51 offen offset:1024 sc1 lds
s_waitcnt lgkmcnt(5)
v_mfma_f32_16x16x32_bf16 v[104:107], v[184:187], v[52:55], v[104:107]
ds_read_b128 v[184:187], v7 offset:23552
s_waitcnt lgkmcnt(5)
v_mfma_f32_16x16x32_bf16 v[108:111], v[164:167], v[52:55], v[108:111]
ds_read_b128 v[164:167], v7 offset:24576
s_waitcnt lgkmcnt(5)
v_mfma_f32_16x16x32_bf16 v[112:115], v[168:171], v[52:55], v[112:115]
ds_read_b128 v[168:171], v7 offset:25600
buffer_load_dwordx4 v0, s[40:43], s51 offen offset:2048 sc1 lds
s_waitcnt lgkmcnt(5)
v_mfma_f32_16x16x32_bf16 v[116:119], v[172:175], v[52:55], v[116:119]
ds_read_b128 v[172:175], v7 offset:26624
s_waitcnt lgkmcnt(5)
v_mfma_f32_16x16x32_bf16 v[120:123], v[176:179], v[52:55], v[120:123]
ds_read_b128 v[176:179], v7 offset:27648
s_waitcnt lgkmcnt(5)
v_mfma_f32_16x16x32_bf16 v[124:127], v[180:183], v[52:55], v[124:127]
ds_read_b128 v[180:183], v7 offset:28672
buffer_load_dwordx4 v0, s[40:43], s51 offen offset:3072 sc1 lds
v_mfma_f32_16x16x32_bf16 v[156:159], v[188:191], v[52:55], v[156:159]
s_waitcnt lgkmcnt(5)
v_mfma_f32_16x16x32_bf16 v[128:131], v[184:187], v[52:55], v[128:131]
ds_read_b128 v[184:187], v7 offset:29696
s_waitcnt lgkmcnt(5)
v_mfma_f32_16x16x32_bf16 v[132:135], v[164:167], v[52:55], v[132:135]
s_waitcnt vmcnt(16)
s_barrier
ds_read_b128 v[164:167], v0 offset:0
s_waitcnt lgkmcnt(5)
v_mfma_f32_16x16x32_bf16 v[136:139], v[168:171], v[52:55], v[136:139]
ds_read_b128 v[168:171], v0 offset:1024
s_waitcnt lgkmcnt(5)
v_mfma_f32_16x16x32_bf16 v[140:143], v[172:175], v[52:55], v[140:143]
ds_read_b128 v[172:175], v0 offset:2048
s_waitcnt lgkmcnt(5)
v_mfma_f32_16x16x32_bf16 v[144:147], v[176:179], v[52:55], v[144:147]
ds_read_b128 v[176:179], v0 offset:3072
s_waitcnt lgkmcnt(5)
v_mfma_f32_16x16x32_bf16 v[148:151], v[180:183], v[52:55], v[148:151]
ds_read_b128 v[180:183], v0 offset:4096
s_waitcnt lgkmcnt(5)
v_mfma_f32_16x16x32_bf16 v[152:155], v[184:187], v[52:55], v[152:155]
ds_read_b128 v[184:187], v0 offset:5120
v_mfma_f32_16x16x32_bf16 v[160:163], v[52:55], v[52:55], v[160:163]
s_waitcnt lgkmcnt(5)
v_mfma_f32_16x16x32_bf16 v[96:99], v[164:167], v[56:59], v[96:99]
s_barrier
ds_read_b128 v[164:167], v0 offset:6144
s_waitcnt lgkmcnt(5)
v_mfma_f32_16x16x32_bf16 v[100:103], v[168:171], v[56:59], v[100:103]
ds_read_b128 v[168:171], v0 offset:7168
s_add_u32 m0, s61, 122880
s_add_u32 s51, s50, 276480
buffer_load_dwordx4 v0, s[40:43], s51 offen offset:0 sc1 lds
s_waitcnt lgkmcnt(5)
v_mfma_f32_16x16x32_bf16 v[104:107], v[172:175], v[56:59], v[104:107]
ds_read_b128 v[172:175], v0 offset:8192
s_waitcnt lgkmcnt(5)
v_mfma_f32_16x16x32_bf16 v[108:111], v[176:179], v[56:59], v[108:111]
ds_read_b128 v[176:179], v0 offset:9216
s_waitcnt lgkmcnt(5)
v_mfma_f32_16x16x32_bf16 v[112:115], v[180:183], v[56:59], v[112:115]
ds_read_b128 v[180:183], v0 offset:10240
buffer_load_dwordx4 v0, s[40:43], s51 offen offset:1024 sc1 lds
s_waitcnt lgkmcnt(5)
v_mfma_f32_16x16x32_bf16 v[116:119], v[184:187], v[56:59], v[116:119]
ds_read_b128 v[184:187], v0 offset:11264
s_waitcnt lgkmcnt(5)
v_mfma_f32_16x16x32_bf16 v[120:123], v[164:167], v[56:59], v[120:123]
ds_read_b128 v[164:167], v0 offset:12288
s_waitcnt lgkmcnt(5)
v_mfma_f32_16x16x32_bf16 v[124:127], v[168:171], v[56:59], v[124:127]
ds_read_b128 v[168:171], v0 offset:13312
buffer_load_dwordx4 v0, s[40:43], s51 offen offset:2048 sc1 lds
v_mfma_f32_16x16x32_bf16 v[156:159], v[188:191], v[56:59], v[156:159]
s_waitcnt lgkmcnt(5)
v_mfma_f32_16x16x32_bf16 v[128:131], v[172:175], v[56:59], v[128:131]
ds_read_b128 v[172:175], v0 offset:14336
s_waitcnt lgkmcnt(5)
v_mfma_f32_16x16x32_bf16 v[132:135], v[176:179], v[56:59], v[132:135]
ds_read_b128 v[176:179], v0 offset:15360
s_waitcnt lgkmcnt(5)
v_mfma_f32_16x16x32_bf16 v[136:139], v[180:183], v[56:59], v[136:139]
ds_read_b128 v[180:183], v0 offset:16384
buffer_load_dwordx4 v0, s[40:43], s51 offen offset:3072 sc1 lds
s_waitcnt lgkmcnt(5)
v_mfma_f32_16x16x32_bf16 v[140:143], v[184:187], v[56:59], v[140:143]
ds_read_b128 v[184:187], v0 offset:17408
s_waitcnt lgkmcnt(5)
v_mfma_f32_16x16x32_bf16 v[144:147], v[164:167], v[56:59], v[144:147]
ds_read_b128 v[164:167], v0 offset:18432
s_waitcnt lgkmcnt(5)
v_mfma_f32_16x16x32_bf16 v[148:151], v[168:171], v[56:59], v[148:151]
ds_read_b128 v[168:171], v0 offset:19456
s_add_u32 m0, s61, 126976
s_add_u32 s51, s50, 280576
buffer_load_dwordx4 v0, s[40:43], s51 offen offset:0 sc1 lds
s_waitcnt lgkmcnt(5)
v_mfma_f32_16x16x32_bf16 v[152:155], v[172:175], v[56:59], v[152:155]
ds_read_b128 v[172:175], v0 offset:20480
v_mfma_f32_16x16x32_bf16 v[160:163], v[56:59], v[56:59], v[160:163]
s_waitcnt lgkmcnt(5)
v_mfma_f32_16x16x32_bf16 v[96:99], v[176:179], v[60:63], v[96:99]
ds_read_b128 v[176:179], v0 offset:21504
s_waitcnt lgkmcnt(5)
v_mfma_f32_16x16x32_bf16 v[100:103], v[180:183], v[60:63], v[100:103]
ds_read_b128 v[180:183], v0 offset:22528
buffer_load_dwordx4 v0, s[40:43], s51 offen offset:1024 sc1 lds
s_waitcnt lgkmcnt(5)
v_mfma_f32_16x16x32_bf16 v[104:107], v[184:187], v[60:63], v[104:107]
ds_read_b128 v[184:187], v0 offset:23552
s_waitcnt lgkmcnt(5)
v_mfma_f32_16x16x32_bf16 v[108:111], v[164:167], v[60:63], v[108:111]
ds_read_b128 v[164:167], v0 offset:24576
s_waitcnt lgkmcnt(5)
v_mfma_f32_16x16x32_bf16 v[112:115], v[168:171], v[60:63], v[112:115]
ds_read_b128 v[168:171], v0 offset:25600
buffer_load_dwordx4 v0, s[40:43], s51 offen offset:2048 sc1 lds
s_waitcnt lgkmcnt(5)
v_mfma_f32_16x16x32_bf16 v[116:119], v[172:175], v[60:63], v[116:119]
ds_read_b128 v[172:175], v0 offset:26624
s_waitcnt lgkmcnt(5)
v_mfma_f32_16x16x32_bf16 v[120:123], v[176:179], v[60:63], v[120:123]
ds_read_b128 v[176:179], v0 offset:27648
s_waitcnt lgkmcnt(5)
v_mfma_f32_16x16x32_bf16 v[124:127], v[180:183], v[60:63], v[124:127]
ds_read_b128 v[180:183], v0 offset:28672
buffer_load_dwordx4 v0, s[40:43], s51 offen offset:3072 sc1 lds
v_mfma_f32_16x16x32_bf16 v[156:159], v[188:191], v[60:63], v[156:159]
s_waitcnt lgkmcnt(5)
v_mfma_f32_16x16x32_bf16 v[128:131], v[184:187], v[60:63], v[128:131]
ds_read_b128 v[184:187], v0 offset:29696
s_waitcnt lgkmcnt(5)
v_mfma_f32_16x16x32_bf16 v[132:135], v[164:167], v[60:63], v[132:135]
s_waitcnt vmcnt(16)
s_barrier
ds_read_b128 v[164:167], v0 offset:30720
s_waitcnt lgkmcnt(5)
v_mfma_f32_16x16x32_bf16 v[136:139], v[168:171], v[60:63], v[136:139]
ds_read_b128 v[168:171], v0 offset:31744
s_waitcnt lgkmcnt(5)
v_mfma_f32_16x16x32_bf16 v[140:143], v[172:175], v[60:63], v[140:143]
ds_read_b128 v[172:175], v0 offset:32768
s_waitcnt lgkmcnt(5)
v_mfma_f32_16x16x32_bf16 v[144:147], v[176:179], v[60:63], v[144:147]
ds_read_b128 v[176:179], v0 offset:33792
s_waitcnt lgkmcnt(5)
v_mfma_f32_16x16x32_bf16 v[148:151], v[180:183], v[60:63], v[148:151]
)"
R"(ds_read_b128 v[180:183], v0 offset:34816
s_waitcnt lgkmcnt(5)
v_mfma_f32_16x16x32_bf16 v[152:155], v[184:187], v[60:63], v[152:155]
ds_read_b128 v[184:187], v0 offset:35840
v_mfma_f32_16x16x32_bf16 v[160:163], v[60:63], v[60:63], v[160:163]
s_waitcnt lgkmcnt(5)
v_mfma_f32_16x16x32_bf16 v[96:99], v[164:167], v[64:67], v[96:99]
ds_read_b128 v[164:167], v0 offset:36864
s_waitcnt lgkmcnt(5)
v_mfma_f32_16x16x32_bf16 v[100:103], v[168:171], v[64:67], v[100:103]
ds_read_b128 v[168:171], v0 offset:37888
s_waitcnt lgkmcnt(5)
v_mfma_f32_16x16x32_bf16 v[104:107], v[172:175], v[64:67], v[104:107]
ds_read_b128 v[172:175], v0 offset:38912
s_waitcnt lgkmcnt(5)
v_mfma_f32_16x16x32_bf16 v[108:111], v[176:179], v[64:67], v[108:111]
ds_read_b128 v[176:179], v0 offset:39936
s_waitcnt lgkmcnt(5)
v_mfma_f32_16x16x32_bf16 v[112:115], v[180:183], v[64:67], v[112:115]
ds_read_b128 v[180:183], v0 offset:40960
s_waitcnt lgkmcnt(5)
v_mfma_f32_16x16x32_bf16 v[116:119], v[184:187], v[64:67], v[116:119]
ds_read_b128 v[184:187], v0 offset:41984
s_waitcnt lgkmcnt(5)
v_mfma_f32_16x16x32_bf16 v[120:123], v[164:167], v[64:67], v[120:123]
ds_read_b128 v[164:167], v0 offset:43008
s_waitcnt lgkmcnt(5)
v_mfma_f32_16x16x32_bf16 v[124:127], v[168:171], v[64:67], v[124:127]
ds_read_b128 v[168:171], v0 offset:44032
v_mfma_f32_16x16x32_bf16 v[156:159], v[188:191], v[64:67], v[156:159]
s_waitcnt lgkmcnt(5)
v_mfma_f32_16x16x32_bf16 v[128:131], v[172:175], v[64:67], v[128:131]
ds_read_b128 v[172:175], v0 offset:45056
s_waitcnt lgkmcnt(5)
v_mfma_f32_16x16x32_bf16 v[132:135], v[176:179], v[64:67], v[132:135]
ds_read_b128 v[176:179], v0 offset:46080
s_waitcnt lgkmcnt(5)
v_mfma_f32_16x16x32_bf16 v[136:139], v[180:183], v[64:67], v[136:139]
ds_read_b128 v[180:183], v0 offset:47104
s_waitcnt lgkmcnt(5)
v_mfma_f32_16x16x32_bf16 v[140:143], v[184:187], v[64:67], v[140:143]
ds_read_b128 v[184:187], v0 offset:48128
s_waitcnt lgkmcnt(5)
v_mfma_f32_16x16x32_bf16 v[144:147], v[164:167], v[64:67], v[144:147]
ds_read_b128 v[164:167], v0 offset:49152
s_waitcnt lgkmcnt(5)
v_mfma_f32_16x16x32_bf16 v[148:151], v[168:171], v[64:67], v[148:151]
ds_read_b128 v[168:171], v0 offset:50176
s_waitcnt lgkmcnt(5)
v_mfma_f32_16x16x32_bf16 v[152:155], v[172:175], v[64:67], v[152:155]
ds_read_b128 v[172:175], v0 offset:51200
v_mfma_f32_16x16x32_bf16 v[160:163], v[64:67], v[64:67], v[160:163]
s_waitcnt lgkmcnt(5)
v_mfma_f32_16x16x32_bf16 v[96:99], v[176:179], v[68:71], v[96:99]
ds_read_b128 v[176:179], v0 offset:52224
s_waitcnt lgkmcnt(5)
v_mfma_f32_16x16x32_bf16 v[100:103], v[180:183], v[68:71], v[100:103]
ds_read_b128 v[180:183], v0 offset:53248
s_waitcnt lgkmcnt(5)
v_mfma_f32_16x16x32_bf16 v[104:107], v[184:187], v[68:71], v[104:107]
ds_read_b128 v[184:187], v0 offset:54272
s_waitcnt lgkmcnt(5)
v_mfma_f32_16x16x32_bf16 v[108:111], v[164:167], v[68:71], v[108:111]
ds_read_b128 v[164:167], v0 offset:55296
s_waitcnt lgkmcnt(5)
v_mfma_f32_16x16x32_bf16 v[112:115], v[168:171], v[68:71], v[112:115]
ds_read_b128 v[168:171], v0 offset:56320
s_waitcnt lgkmcnt(5)
v_mfma_f32_16x16x32_bf16 v[116:119], v[172:175], v[68:71], v[116:119]
ds_read_b128 v[172:175], v0 offset:57344
s_waitcnt lgkmcnt(5)
v_mfma_f32_16x16x32_bf16 v[120:123], v[176:179], v[68:71], v[120:123]
ds_read_b128 v[176:179], v0 offset:58368
s_waitcnt lgkmcnt(5)
v_mfma_f32_16x16x32_bf16 v[124:127], v[180:183], v[68:71], v[124:127]
ds_read_b128 v[180:183], v0 offset:59392
v_mfma_f32_16x16x32_bf16 v[156:159], v[188:191], v[68:71], v[156:159]
s_waitcnt lgkmcnt(5)
v_mfma_f32_16x16x32_bf16 v[128:131], v[184:187], v[68:71], v[128:131]
ds_read_b128 v[184:187], v0 offset:60416
s_waitcnt lgkmcnt(5)
v_mfma_f32_16x16x32_bf16 v[132:135], v[164:167], v[68:71], v[132:135]
s_waitcnt vmcnt(16)
s_barrier
ds_read_b128 v[164:167], v6 offset:0
s_waitcnt lgkmcnt(5)
v_mfma_f32_16x16x32_bf16 v[136:139], v[168:171], v[68:71], v[136:139]
ds_read_b128 v[168:171], v6 offset:1024
s_waitcnt lgkmcnt(5)
v_mfma_f32_16x16x32_bf16 v[140:143], v[172:175], v[68:71], v[140:143]
ds_read_b128 v[172:175], v6 offset:2048
s_waitcnt lgkmcnt(5)
v_mfma_f32_16x16x32_bf16 v[144:147], v[176:179], v[68:71], v[144:147]
ds_read_b128 v[176:179], v6 offset:3072
s_waitcnt lgkmcnt(5)
v_mfma_f32_16x16x32_bf16 v[148:151], v[180:183], v[68:71], v[148:151]
ds_read_b128 v[180:183], v6 offset:4096
s_waitcnt lgkmcnt(5)
v_mfma_f32_16x16x32_bf16 v[152:155], v[184:187], v[68:71], v[152:155]
ds_read_b128 v[184:187], v6 offset:5120
v_mfma_f32_16x16x32_bf16 v[160:163], v[68:71], v[68:71], v[160:163]
s_waitcnt lgkmcnt(5)
v_mfma_f32_16x16x32_bf16 v[96:99], v[164:167], v[72:75], v[96:99]
ds_read_b128 v[164:167], v6 offset:6144
s_waitcnt lgkmcnt(5)
v_mfma_f32_16x16x32_bf16 v[100:103], v[168:171], v[72:75], v[100:103]
ds_read_b128 v[168:171], v6 offset:7168
s_waitcnt lgkmcnt(5)
v_mfma_f32_16x16x32_bf16 v[104:107], v[172:175], v[72:75], v[104:107]
ds_read_b128 v[172:175], v6 offset:8192
s_waitcnt lgkmcnt(5)
v_mfma_f32_16x16x32_bf16 v[108:111], v[176:179], v[72:75], v[108:111]
ds_read_b128 v[176:179], v6 offset:9216
s_waitcnt lgkmcnt(5)
v_mfma_f32_16x16x32_bf16 v[112:115], v[180:183], v[72:75], v[112:115]
ds_read_b128 v[180:183], v6 offset:10240
s_waitcnt lgkmcnt(5)
v_mfma_f32_16x16x32_bf16 v[116:119], v[184:187], v[72:75], v[116:119]
ds_read_b128 v[184:187], v6 offset:11264
s_waitcnt lgkmcnt(5)
v_mfma_f32_16x16x32_bf16 v[120:123], v[164:167], v[72:75], v[120:123]
ds_read_b128 v[164:167], v6 offset:12288
s_waitcnt lgkmcnt(5)
v_mfma_f32_16x16x32_bf16 v[124:127], v[168:171], v[72:75], v[124:127]
ds_read_b128 v[168:171], v6 offset:13312
v_mfma_f32_16x16x32_bf16 v[156:159], v[188:191], v[72:75], v[156:159]
s_waitcnt lgkmcnt(5)
v_mfma_f32_16x16x32_bf16 v[128:131], v[172:175], v[72:75], v[128:131]
ds_read_b128 v[172:175], v6 offset:14336
s_waitcnt lgkmcnt(5)
v_mfma_f32_16x16x32_bf16 v[132:135], v[176:179], v[72:75], v[132:135]
ds_read_b128 v[176:179], v6 offset:15360
s_waitcnt lgkmcnt(5)
v_mfma_f32_16x16x32_bf16 v[136:139], v[180:183], v[72:75], v[136:139]
ds_read_b128 v[180:183], v6 offset:16384
s_waitcnt lgkmcnt(5)
v_mfma_f32_16x16x32_bf16 v[140:143], v[184:187], v[72:75], v[140:143]
ds_read_b128 v[184:187], v6 offset:17408
s_waitcnt lgkmcnt(5)
v_mfma_f32_16x16x32_bf16 v[144:147], v[164:167], v[72:75], v[144:147]
ds_read_b128 v[164:167], v6 offset:18432
s_waitcnt lgkmcnt(5)
v_mfma_f32_16x16x32_bf16 v[148:151], v[168:171], v[72:75], v[148:151]
ds_read_b128 v[168:171], v6 offset:19456
s_waitcnt lgkmcnt(5)
v_mfma_f32_16x16x32_bf16 v[152:155], v[172:175], v[72:75], v[152:155]
ds_read_b128 v[172:175], v6 offset:20480
v_mfma_f32_16x16x32_bf16 v[160:163], v[72:75], v[72:75], v[160:163]
s_waitcnt lgkmcnt(5)
v_mfma_f32_16x16x32_bf16 v[96:99], v[176:179], v[76:79], v[96:99]
ds_read_b128 v[176:179], v6 offset:21504
s_waitcnt lgkmcnt(5)
v_mfma_f32_16x16x32_bf16 v[100:103], v[180:183], v[76:79], v[100:103]
ds_read_b128 v[180:183], v6 offset:22528
s_waitcnt lgkmcnt(5)
v_mfma_f32_16x16x32_bf16 v[104:107], v[184:187], v[76:79], v[104:107]
ds_read_b128 v[184:187], v6 offset:23552
s_waitcnt lgkmcnt(5)
v_mfma_f32_16x16x32_bf16 v[108:111], v[164:167], v[76:79], v[108:111]
ds_read_b128 v[164:167], v6 offset:24576
s_waitcnt lgkmcnt(5)
v_mfma_f32_16x16x32_bf16 v[112:115], v[168:171], v[76:79], v[112:115]
ds_read_b128 v[168:171], v6 offset:25600
s_waitcnt lgkmcnt(5)
v_mfma_f32_16x16x32_bf16 v[116:119], v[172:175], v[76:79], v[116:119]
ds_read_b128 v[172:175], v6 offset:26624
s_waitcnt lgkmcnt(5)
v_mfma_f32_16x16x32_bf16 v[120:123], v[176:179], v[76:79], v[120:123]
ds_read_b128 v[176:179], v6 offset:27648
s_waitcnt lgkmcnt(5)
v_mfma_f32_16x16x32_bf16 v[124:127], v[180:183], v[76:79], v[124:127]
ds_read_b128 v[180:183], v6 offset:28672
v_mfma_f32_16x16x32_bf16 v[156:159], v[188:191], v[76:79], v[156:159]
s_waitcnt lgkmcnt(5)
v_mfma_f32_16x16x32_bf16 v[128:131], v[184:187], v[76:79], v[128:131]
ds_read_b128 v[184:187], v6 offset:29696
s_waitcnt lgkmcnt(5)
v_mfma_f32_16x16x32_bf16 v[132:135], v[164:167], v[76:79], v[132:135]
s_waitcnt vmcnt(8)
s_barrier
ds_read_b128 v[164:167], v6 offset:30720
s_waitcnt lgkmcnt(5)
v_mfma_f32_16x16x32_bf16 v[136:139], v[168:171], v[76:79], v[136:139]
ds_read_b128 v[168:171], v6 offset:31744
s_waitcnt lgkmcnt(5)
v_mfma_f32_16x16x32_bf16 v[140:143], v[172:175], v[76:79], v[140:143]
ds_read_b128 v[172:175], v6 offset:32768
s_waitcnt lgkmcnt(5)
v_mfma_f32_16x16x32_bf16 v[144:147], v[176:179], v[76:79], v[144:147]
ds_read_b128 v[176:179], v6 offset:33792
s_waitcnt lgkmcnt(5)
v_mfma_f32_16x16x32_bf16 v[148:151], v[180:183], v[76:79], v[148:151]
ds_read_b128 v[180:183], v6 offset:34816
s_waitcnt lgkmcnt(5)
v_mfma_f32_16x16x32_bf16 v[152:155], v[184:187], v[76:79], v[152:155]
ds_read_b128 v[184:187], v6 offset:35840
v_mfma_f32_16x16x32_bf16 v[160:163], v[76:79], v[76:79], v[160:163]
s_waitcnt lgkmcnt(5)
v_mfma_f32_16x16x32_bf16 v[96:99], v[164:167], v[80:83], v[96:99]
ds_read_b128 v[164:167], v6 offset:36864
)"
R"(v_and_b32 v4, 48, v15
s_mul_i32 s52, s26, 960
s_add_u32 s52, s52, 148668416
s_waitcnt lgkmcnt(5)
v_mfma_f32_16x16x32_bf16 v[100:103], v[168:171], v[80:83], v[100:103]
ds_read_b128 v[168:171], v6 offset:37888
buffer_load_dwordx4 v[16:19], v4, s[40:43], s52 offen sc1
s_waitcnt lgkmcnt(5)
v_mfma_f32_16x16x32_bf16 v[104:107], v[172:175], v[80:83], v[104:107]
ds_read_b128 v[172:175], v6 offset:38912
buffer_load_dwordx4 v[20:23], v4, s[40:43], s52 offen offset:64 sc1
s_waitcnt lgkmcnt(5)
v_mfma_f32_16x16x32_bf16 v[108:111], v[176:179], v[80:83], v[108:111]
ds_read_b128 v[176:179], v6 offset:39936
buffer_load_dwordx4 v[24:27], v4, s[40:43], s52 offen offset:128 sc1
s_waitcnt lgkmcnt(5)
v_mfma_f32_16x16x32_bf16 v[112:115], v[180:183], v[80:83], v[112:115]
ds_read_b128 v[180:183], v6 offset:40960
buffer_load_dwordx4 v[28:31], v4, s[40:43], s52 offen offset:192 sc1
s_waitcnt lgkmcnt(5)
v_mfma_f32_16x16x32_bf16 v[116:119], v[184:187], v[80:83], v[116:119]
ds_read_b128 v[184:187], v6 offset:41984
buffer_load_dwordx4 v[32:35], v4, s[40:43], s52 offen offset:256 sc1
s_waitcnt lgkmcnt(5)
v_mfma_f32_16x16x32_bf16 v[120:123], v[164:167], v[80:83], v[120:123]
ds_read_b128 v[164:167], v6 offset:43008
buffer_load_dwordx4 v[36:39], v4, s[40:43], s52 offen offset:320 sc1
s_waitcnt lgkmcnt(5)
v_mfma_f32_16x16x32_bf16 v[124:127], v[168:171], v[80:83], v[124:127]
ds_read_b128 v[168:171], v6 offset:44032
v_mfma_f32_16x16x32_bf16 v[156:159], v[188:191], v[80:83], v[156:159]
buffer_load_dwordx4 v[40:43], v4, s[40:43], s52 offen offset:384 sc1
s_waitcnt lgkmcnt(5)
v_mfma_f32_16x16x32_bf16 v[128:131], v[172:175], v[80:83], v[128:131]
ds_read_b128 v[172:175], v6 offset:45056
buffer_load_dwordx4 v[44:47], v4, s[40:43], s52 offen offset:448 sc1
s_waitcnt lgkmcnt(5)
v_mfma_f32_16x16x32_bf16 v[132:135], v[176:179], v[80:83], v[132:135]
ds_read_b128 v[176:179], v6 offset:46080
buffer_load_dwordx4 v[48:51], v4, s[40:43], s52 offen offset:512 sc1
s_waitcnt lgkmcnt(5)
v_mfma_f32_16x16x32_bf16 v[136:139], v[180:183], v[80:83], v[136:139]
ds_read_b128 v[180:183], v6 offset:47104
buffer_load_dwordx4 v[52:55], v4, s[40:43], s52 offen offset:576 sc1
s_waitcnt lgkmcnt(5)
v_mfma_f32_16x16x32_bf16 v[140:143], v[184:187], v[80:83], v[140:143]
ds_read_b128 v[184:187], v6 offset:48128
buffer_load_dwordx4 v[56:59], v4, s[40:43], s52 offen offset:640 sc1
s_waitcnt lgkmcnt(5)
v_mfma_f32_16x16x32_bf16 v[144:147], v[164:167], v[80:83], v[144:147]
ds_read_b128 v[164:167], v6 offset:49152
buffer_load_dwordx4 v[60:63], v4, s[40:43], s52 offen offset:704 sc1
s_waitcnt lgkmcnt(5)
v_mfma_f32_16x16x32_bf16 v[148:151], v[168:171], v[80:83], v[148:151]
ds_read_b128 v[168:171], v6 offset:50176
buffer_load_dwordx4 v[64:67], v4, s[40:43], s52 offen offset:768 sc1
s_waitcnt lgkmcnt(5)
v_mfma_f32_16x16x32_bf16 v[152:155], v[172:175], v[80:83], v[152:155]
ds_read_b128 v[172:175], v6 offset:51200
v_mfma_f32_16x16x32_bf16 v[160:163], v[80:83], v[80:83], v[160:163]
buffer_load_dwordx4 v[68:71], v4, s[40:43], s52 offen offset:832 sc1
s_waitcnt lgkmcnt(5)
v_mfma_f32_16x16x32_bf16 v[96:99], v[176:179], v[84:87], v[96:99]
ds_read_b128 v[176:179], v6 offset:52224
buffer_load_dwordx4 v[72:75], v4, s[40:43], s52 offen offset:896 sc1
s_waitcnt lgkmcnt(5)
v_mfma_f32_16x16x32_bf16 v[100:103], v[180:183], v[84:87], v[100:103]
ds_read_b128 v[180:183], v6 offset:53248
s_waitcnt lgkmcnt(5)
v_mfma_f32_16x16x32_bf16 v[104:107], v[184:187], v[84:87], v[104:107]
ds_read_b128 v[184:187], v6 offset:54272
s_waitcnt lgkmcnt(5)
v_mfma_f32_16x16x32_bf16 v[108:111], v[164:167], v[84:87], v[108:111]
ds_read_b128 v[164:167], v6 offset:55296
s_waitcnt lgkmcnt(5)
v_mfma_f32_16x16x32_bf16 v[112:115], v[168:171], v[84:87], v[112:115]
ds_read_b128 v[168:171], v6 offset:56320
s_waitcnt lgkmcnt(5)
v_mfma_f32_16x16x32_bf16 v[116:119], v[172:175], v[84:87], v[116:119]
ds_read_b128 v[172:175], v6 offset:57344
s_waitcnt lgkmcnt(5)
v_mfma_f32_16x16x32_bf16 v[120:123], v[176:179], v[84:87], v[120:123]
ds_read_b128 v[176:179], v6 offset:58368
s_waitcnt lgkmcnt(5)
v_mfma_f32_16x16x32_bf16 v[124:127], v[180:183], v[84:87], v[124:127]
ds_read_b128 v[180:183], v6 offset:59392
v_mfma_f32_16x16x32_bf16 v[156:159], v[188:191], v[84:87], v[156:159]
s_waitcnt lgkmcnt(5)
v_mfma_f32_16x16x32_bf16 v[128:131], v[184:187], v[84:87], v[128:131]
ds_read_b128 v[184:187], v6 offset:60416
s_waitcnt lgkmcnt(5)
v_mfma_f32_16x16x32_bf16 v[132:135], v[164:167], v[84:87], v[132:135]
s_waitcnt vmcnt(0)
s_barrier
ds_read_b128 v[164:167], v7 offset:0
s_waitcnt lgkmcnt(5)
v_mfma_f32_16x16x32_bf16 v[136:139], v[168:171], v[84:87], v[136:139]
ds_read_b128 v[168:171], v7 offset:1024
s_waitcnt lgkmcnt(5)
v_mfma_f32_16x16x32_bf16 v[140:143], v[172:175], v[84:87], v[140:143]
ds_read_b128 v[172:175], v7 offset:2048
s_waitcnt lgkmcnt(5)
v_mfma_f32_16x16x32_bf16 v[144:147], v[176:179], v[84:87], v[144:147]
ds_read_b128 v[176:179], v7 offset:3072
s_waitcnt lgkmcnt(5)
v_mfma_f32_16x16x32_bf16 v[148:151], v[180:183], v[84:87], v[148:151]
ds_read_b128 v[180:183], v7 offset:4096
s_waitcnt lgkmcnt(5)
v_mfma_f32_16x16x32_bf16 v[152:155], v[184:187], v[84:87], v[152:155]
ds_read_b128 v[184:187], v7 offset:5120
v_mfma_f32_16x16x32_bf16 v[160:163], v[84:87], v[84:87], v[160:163]
s_waitcnt lgkmcnt(5)
v_mfma_f32_16x16x32_bf16 v[96:99], v[164:167], v[88:91], v[96:99]
ds_read_b128 v[164:167], v7 offset:6144
s_waitcnt lgkmcnt(5)
v_mfma_f32_16x16x32_bf16 v[100:103], v[168:171], v[88:91], v[100:103]
ds_read_b128 v[168:171], v7 offset:7168
s_waitcnt lgkmcnt(5)
v_mfma_f32_16x16x32_bf16 v[104:107], v[172:175], v[88:91], v[104:107]
ds_read_b128 v[172:175], v7 offset:8192
s_waitcnt lgkmcnt(5)
v_mfma_f32_16x16x32_bf16 v[108:111], v[176:179], v[88:91], v[108:111]
ds_read_b128 v[176:179], v7 offset:9216
s_waitcnt lgkmcnt(5)
v_mfma_f32_16x16x32_bf16 v[112:115], v[180:183], v[88:91], v[112:115]
ds_read_b128 v[180:183], v7 offset:10240
s_waitcnt lgkmcnt(5)
v_mfma_f32_16x16x32_bf16 v[116:119], v[184:187], v[88:91], v[116:119]
ds_read_b128 v[184:187], v7 offset:11264
s_waitcnt lgkmcnt(5)
v_mfma_f32_16x16x32_bf16 v[120:123], v[164:167], v[88:91], v[120:123]
ds_read_b128 v[164:167], v7 offset:12288
s_waitcnt lgkmcnt(5)
v_mfma_f32_16x16x32_bf16 v[124:127], v[168:171], v[88:91], v[124:127]
ds_read_b128 v[168:171], v7 offset:13312
v_mfma_f32_16x16x32_bf16 v[156:159], v[188:191], v[88:91], v[156:159]
s_waitcnt lgkmcnt(5)
v_mfma_f32_16x16x32_bf16 v[128:131], v[172:175], v[88:91], v[128:131]
ds_read_b128 v[172:175], v7 offset:14336
s_waitcnt lgkmcnt(5)
v_mfma_f32_16x16x32_bf16 v[132:135], v[176:179], v[88:91], v[132:135]
ds_read_b128 v[176:179], v7 offset:15360
s_waitcnt lgkmcnt(5)
v_mfma_f32_16x16x32_bf16 v[136:139], v[180:183], v[88:91], v[136:139]
ds_read_b128 v[180:183], v7 offset:16384
s_waitcnt lgkmcnt(5)
v_mfma_f32_16x16x32_bf16 v[140:143], v[184:187], v[88:91], v[140:143]
ds_read_b128 v[184:187], v7 offset:17408
s_waitcnt lgkmcnt(5)
v_mfma_f32_16x16x32_bf16 v[144:147], v[164:167], v[88:91], v[144:147]
ds_read_b128 v[164:167], v7 offset:18432
s_waitcnt lgkmcnt(5)
v_mfma_f32_16x16x32_bf16 v[148:151], v[168:171], v[88:91], v[148:151]
ds_read_b128 v[168:171], v7 offset:19456
s_waitcnt lgkmcnt(5)
v_mfma_f32_16x16x32_bf16 v[152:155], v[172:175], v[88:91], v[152:155]
ds_read_b128 v[172:175], v7 offset:20480
v_mfma_f32_16x16x32_bf16 v[160:163], v[88:91], v[88:91], v[160:163]
s_waitcnt lgkmcnt(5)
v_mfma_f32_16x16x32_bf16 v[96:99], v[176:179], v[92:95], v[96:99]
ds_read_b128 v[176:179], v7 offset:21504
s_waitcnt lgkmcnt(5)
v_mfma_f32_16x16x32_bf16 v[100:103], v[180:183], v[92:95], v[100:103]
ds_read_b128 v[180:183], v7 offset:22528
s_waitcnt lgkmcnt(5)
v_mfma_f32_16x16x32_bf16 v[104:107], v[184:187], v[92:95], v[104:107]
ds_read_b128 v[184:187], v7 offset:23552
s_waitcnt lgkmcnt(5)
v_mfma_f32_16x16x32_bf16 v[108:111], v[164:167], v[92:95], v[108:111]
ds_read_b128 v[164:167], v7 offset:24576
s_waitcnt lgkmcnt(5)
v_mfma_f32_16x16x32_bf16 v[112:115], v[168:171], v[92:95], v[112:115]
ds_read_b128 v[168:171], v7 offset:25600
s_waitcnt lgkmcnt(5)
v_mfma_f32_16x16x32_bf16 v[116:119], v[172:175], v[92:95], v[116:119]
ds_read_b128 v[172:175], v7 offset:26624
s_waitcnt lgkmcnt(5)
v_mfma_f32_16x16x32_bf16 v[120:123], v[176:179], v[92:95], v[120:123]
ds_read_b128 v[176:179], v7 offset:27648
s_waitcnt lgkmcnt(5)
v_mfma_f32_16x16x32_bf16 v[124:127], v[180:183], v[92:95], v[124:127]
ds_read_b128 v[180:183], v7 offset:28672
v_mfma_f32_16x16x32_bf16 v[156:159], v[188:191], v[92:95], v[156:159]
s_waitcnt lgkmcnt(5)
v_mfma_f32_16x16x32_bf16 v[128:131], v[184:187], v[92:95], v[128:131]
ds_read_b128 v[184:187], v7 offset:29696
s_waitcnt lgkmcnt(5)
v_mfma_f32_16x16x32_bf16 v[132:135], v[164:167], v[92:95], v[132:135]
s_waitcnt lgkmcnt(4)
v_mfma_f32_16x16x32_bf16 v[136:139], v[168:171], v[92:95], v[136:139]
s_waitcnt lgkmcnt(3)
v_mfma_f32_16x16x32_bf16 v[140:143], v[172:175], v[92:95], v[140:143]
s_waitcnt lgkmcnt(2)
v_mfma_f32_16x16x32_bf16 v[144:147], v[176:179], v[92:95], v[144:147]
s_waitcnt lgkmcnt(1)
v_mfma_f32_16x16x32_bf16 v[148:151], v[180:183], v[92:95], v[148:151]
s_waitcnt lgkmcnt(0)
v_mfma_f32_16x16x32_bf16 v[152:155], v[184:187], v[92:95], v[152:155]
v_mfma_f32_16x16x32_bf16 v[160:163], v[92:95], v[92:95], v[160:163]
v_and_b32 v4, 1, v15
)"
R"(v_cmp_eq_u32 vcc, 1, v4
s_nop 5
v_cndmask_b32 v160, v160, v161, vcc
v_cndmask_b32 v162, v162, v163, vcc
v_and_b32 v4, 2, v15
v_cmp_eq_u32 vcc, 2, v4
s_nop 1
v_cndmask_b32 v160, v160, v162, vcc
v_bfe_u32 v4, v15, 2, 2
v_lshrrev_b32 v5, 4, v15
v_cmp_eq_u32 vcc, v4, v5
s_nop 1
v_cndmask_b32 v160, 0, v160, vcc
v_mov_b32 v161, v160
s_nop 1
v_permlane16_swap_b32 v160, v161
v_add_f32 v160, v160, v161
v_mov_b32 v161, v160
s_nop 1
v_permlane32_swap_b32 v160, v161
v_add_f32 v160, v160, v161
v_mul_f32 v8, 0x3acccccd, v156
v_mul_f32 v160, 0x3acccccd, v160
v_fma_f32 v160, -v8, v8, v160
v_max_f32 v160, 0, v160
v_add_f32 v160, 0x3727c5ac, v160
v_rsq_f32 v10, v160
v_sub_f32 v8, 0, v8
v_pk_fma_f32 v[96:97], v[16:17], v[8:9], v[96:97] op_sel_hi:[1,0,1]
v_pk_mul_f32 v[96:97], v[96:97], v[10:11] op_sel_hi:[1,0]
v_pk_fma_f32 v[98:99], v[18:19], v[8:9], v[98:99] op_sel_hi:[1,0,1]
v_pk_mul_f32 v[98:99], v[98:99], v[10:11] op_sel_hi:[1,0]
v_pk_fma_f32 v[100:101], v[20:21], v[8:9], v[100:101] op_sel_hi:[1,0,1]
v_pk_mul_f32 v[100:101], v[100:101], v[10:11] op_sel_hi:[1,0]
v_pk_fma_f32 v[102:103], v[22:23], v[8:9], v[102:103] op_sel_hi:[1,0,1]
v_pk_mul_f32 v[102:103], v[102:103], v[10:11] op_sel_hi:[1,0]
v_pk_fma_f32 v[104:105], v[24:25], v[8:9], v[104:105] op_sel_hi:[1,0,1]
v_pk_mul_f32 v[104:105], v[104:105], v[10:11] op_sel_hi:[1,0]
v_pk_fma_f32 v[106:107], v[26:27], v[8:9], v[106:107] op_sel_hi:[1,0,1]
v_pk_mul_f32 v[106:107], v[106:107], v[10:11] op_sel_hi:[1,0]
v_pk_fma_f32 v[108:109], v[28:29], v[8:9], v[108:109] op_sel_hi:[1,0,1]
v_pk_mul_f32 v[108:109], v[108:109], v[10:11] op_sel_hi:[1,0]
v_pk_fma_f32 v[110:111], v[30:31], v[8:9], v[110:111] op_sel_hi:[1,0,1]
v_pk_mul_f32 v[110:111], v[110:111], v[10:11] op_sel_hi:[1,0]
v_pk_fma_f32 v[112:113], v[32:33], v[8:9], v[112:113] op_sel_hi:[1,0,1]
v_pk_mul_f32 v[112:113], v[112:113], v[10:11] op_sel_hi:[1,0]
v_pk_fma_f32 v[114:115], v[34:35], v[8:9], v[114:115] op_sel_hi:[1,0,1]
v_pk_mul_f32 v[114:115], v[114:115], v[10:11] op_sel_hi:[1,0]
v_pk_fma_f32 v[116:117], v[36:37], v[8:9], v[116:117] op_sel_hi:[1,0,1]
v_pk_mul_f32 v[116:117], v[116:117], v[10:11] op_sel_hi:[1,0]
v_pk_fma_f32 v[118:119], v[38:39], v[8:9], v[118:119] op_sel_hi:[1,0,1]
v_pk_mul_f32 v[118:119], v[118:119], v[10:11] op_sel_hi:[1,0]
v_pk_fma_f32 v[120:121], v[40:41], v[8:9], v[120:121] op_sel_hi:[1,0,1]
v_pk_mul_f32 v[120:121], v[120:121], v[10:11] op_sel_hi:[1,0]
v_pk_fma_f32 v[122:123], v[42:43], v[8:9], v[122:123] op_sel_hi:[1,0,1]
v_pk_mul_f32 v[122:123], v[122:123], v[10:11] op_sel_hi:[1,0]
v_pk_fma_f32 v[124:125], v[44:45], v[8:9], v[124:125] op_sel_hi:[1,0,1]
v_pk_mul_f32 v[124:125], v[124:125], v[10:11] op_sel_hi:[1,0]
v_pk_fma_f32 v[126:127], v[46:47], v[8:9], v[126:127] op_sel_hi:[1,0,1]
v_pk_mul_f32 v[126:127], v[126:127], v[10:11] op_sel_hi:[1,0]
v_pk_fma_f32 v[128:129], v[48:49], v[8:9], v[128:129] op_sel_hi:[1,0,1]
v_pk_mul_f32 v[128:129], v[128:129], v[10:11] op_sel_hi:[1,0]
v_pk_fma_f32 v[130:131], v[50:51], v[8:9], v[130:131] op_sel_hi:[1,0,1]
v_pk_mul_f32 v[130:131], v[130:131], v[10:11] op_sel_hi:[1,0]
v_pk_fma_f32 v[132:133], v[52:53], v[8:9], v[132:133] op_sel_hi:[1,0,1]
v_pk_mul_f32 v[132:133], v[132:133], v[10:11] op_sel_hi:[1,0]
v_pk_fma_f32 v[134:135], v[54:55], v[8:9], v[134:135] op_sel_hi:[1,0,1]
v_pk_mul_f32 v[134:135], v[134:135], v[10:11] op_sel_hi:[1,0]
v_pk_fma_f32 v[136:137], v[56:57], v[8:9], v[136:137] op_sel_hi:[1,0,1]
v_pk_mul_f32 v[136:137], v[136:137], v[10:11] op_sel_hi:[1,0]
v_pk_fma_f32 v[138:139], v[58:59], v[8:9], v[138:139] op_sel_hi:[1,0,1]
v_pk_mul_f32 v[138:139], v[138:139], v[10:11] op_sel_hi:[1,0]
v_pk_fma_f32 v[140:141], v[60:61], v[8:9], v[140:141] op_sel_hi:[1,0,1]
v_pk_mul_f32 v[140:141], v[140:141], v[10:11] op_sel_hi:[1,0]
v_pk_fma_f32 v[142:143], v[62:63], v[8:9], v[142:143] op_sel_hi:[1,0,1]
v_pk_mul_f32 v[142:143], v[142:143], v[10:11] op_sel_hi:[1,0]
v_pk_fma_f32 v[144:145], v[64:65], v[8:9], v[144:145] op_sel_hi:[1,0,1]
v_pk_mul_f32 v[144:145], v[144:145], v[10:11] op_sel_hi:[1,0]
v_pk_fma_f32 v[146:147], v[66:67], v[8:9], v[146:147] op_sel_hi:[1,0,1]
v_pk_mul_f32 v[146:147], v[146:147], v[10:11] op_sel_hi:[1,0]
v_pk_fma_f32 v[148:149], v[68:69], v[8:9], v[148:149] op_sel_hi:[1,0,1]
v_pk_mul_f32 v[148:149], v[148:149], v[10:11] op_sel_hi:[1,0]
v_pk_fma_f32 v[150:151], v[70:71], v[8:9], v[150:151] op_sel_hi:[1,0,1]
v_pk_mul_f32 v[150:151], v[150:151], v[10:11] op_sel_hi:[1,0]
v_pk_fma_f32 v[152:153], v[72:73], v[8:9], v[152:153] op_sel_hi:[1,0,1]
v_pk_mul_f32 v[152:153], v[152:153], v[10:11] op_sel_hi:[1,0]
v_pk_fma_f32 v[154:155], v[74:75], v[8:9], v[154:155] op_sel_hi:[1,0,1]
v_pk_mul_f32 v[154:155], v[154:155], v[10:11] op_sel_hi:[1,0]
v_max3_f32 v8, v116, v117, v118
v_max3_f32 v8, v8, v119, v120
v_max3_f32 v8, v8, v121, v122
v_max3_f32 v8, v8, v123, v124
v_max3_f32 v8, v8, v125, v126
v_max3_f32 v8, v8, v127, v128
v_max3_f32 v8, v8, v129, v130
v_max3_f32 v8, v8, v131, v132
v_max3_f32 v8, v8, v133, v134
v_max_f32 v8, v8, v135
v_min3_f32 v9, v116, v117, v118
v_min3_f32 v9, v9, v119, v120
v_min3_f32 v9, v9, v121, v122
v_min3_f32 v9, v9, v123, v124
v_min3_f32 v9, v9, v125, v126
v_min3_f32 v9, v9, v127, v128
v_min3_f32 v9, v9, v129, v130
v_min3_f32 v9, v9, v131, v132
v_min3_f32 v9, v9, v133, v134
v_min_f32 v9, v9, v135
v_mul_f32 v8, 0xbfb8aa3b, v8
v_mul_f32 v10, 0x3fb8aa3b, v9
v_pk_fma_f32 v[16:17], v[116:117], s[22:23], v[8:9] op_sel_hi:[1,1,0]
v_pk_fma_f32 v[36:37], v[116:117], s[24:25], v[10:11] op_sel_hi:[1,1,0]
v_pk_fma_f32 v[18:19], v[118:119], s[22:23], v[8:9] op_sel_hi:[1,1,0]
v_pk_fma_f32 v[38:39], v[118:119], s[24:25], v[10:11] op_sel_hi:[1,1,0]
v_pk_fma_f32 v[20:21], v[120:121], s[22:23], v[8:9] op_sel_hi:[1,1,0]
v_pk_fma_f32 v[40:41], v[120:121], s[24:25], v[10:11] op_sel_hi:[1,1,0]
v_pk_fma_f32 v[22:23], v[122:123], s[22:23], v[8:9] op_sel_hi:[1,1,0]
v_pk_fma_f32 v[42:43], v[122:123], s[24:25], v[10:11] op_sel_hi:[1,1,0]
v_pk_fma_f32 v[24:25], v[124:125], s[22:23], v[8:9] op_sel_hi:[1,1,0]
v_pk_fma_f32 v[44:45], v[124:125], s[24:25], v[10:11] op_sel_hi:[1,1,0]
v_pk_fma_f32 v[26:27], v[126:127], s[22:23], v[8:9] op_sel_hi:[1,1,0]
v_pk_fma_f32 v[46:47], v[126:127], s[24:25], v[10:11] op_sel_hi:[1,1,0]
v_pk_fma_f32 v[28:29], v[128:129], s[22:23], v[8:9] op_sel_hi:[1,1,0]
v_pk_fma_f32 v[48:49], v[128:129], s[24:25], v[10:11] op_sel_hi:[1,1,0]
v_pk_fma_f32 v[30:31], v[130:131], s[22:23], v[8:9] op_sel_hi:[1,1,0]
v_pk_fma_f32 v[50:51], v[130:131], s[24:25], v[10:11] op_sel_hi:[1,1,0]
v_pk_fma_f32 v[32:33], v[132:133], s[22:23], v[8:9] op_sel_hi:[1,1,0]
v_pk_fma_f32 v[52:53], v[132:133], s[24:25], v[10:11] op_sel_hi:[1,1,0]
v_pk_fma_f32 v[34:35], v[134:135], s[22:23], v[8:9] op_sel_hi:[1,1,0]
v_pk_fma_f32 v[54:55], v[134:135], s[24:25], v[10:11] op_sel_hi:[1,1,0]
v_exp_f32 v16, v16
v_exp_f32 v17, v17
v_exp_f32 v18, v18
v_exp_f32 v19, v19
v_exp_f32 v20, v20
v_exp_f32 v21, v21
v_exp_f32 v22, v22
v_exp_f32 v23, v23
v_exp_f32 v24, v24
v_exp_f32 v25, v25
v_exp_f32 v26, v26
v_exp_f32 v27, v27
v_exp_f32 v28, v28
v_exp_f32 v29, v29
v_exp_f32 v30, v30
v_exp_f32 v31, v31
v_exp_f32 v32, v32
v_exp_f32 v33, v33
v_exp_f32 v34, v34
v_exp_f32 v35, v35
v_exp_f32 v36, v36
v_exp_f32 v37, v37
v_exp_f32 v38, v38
v_exp_f32 v39, v39
v_exp_f32 v40, v40
v_exp_f32 v41, v41
v_exp_f32 v42, v42
v_exp_f32 v43, v43
v_exp_f32 v44, v44
v_exp_f32 v45, v45
v_exp_f32 v46, v46
v_exp_f32 v47, v47
v_exp_f32 v48, v48
v_exp_f32 v49, v49
v_exp_f32 v50, v50
v_exp_f32 v51, v51
v_exp_f32 v52, v52
v_exp_f32 v53, v53
v_exp_f32 v54, v54
v_exp_f32 v55, v55
v_pk_add_f32 v[8:9], v[16:17], v[18:19]
v_pk_add_f32 v[8:9], v[8:9], v[20:21]
v_pk_add_f32 v[8:9], v[8:9], v[22:23]
v_pk_add_f32 v[8:9], v[8:9], v[24:25]
v_pk_add_f32 v[8:9], v[8:9], v[26:27]
v_pk_add_f32 v[8:9], v[8:9], v[28:29]
v_pk_add_f32 v[8:9], v[8:9], v[30:31]
v_pk_add_f32 v[8:9], v[8:9], v[32:33]
v_pk_add_f32 v[8:9], v[8:9], v[34:35]
v_add_f32 v8, v8, v9
v_pk_add_f32 v[10:11], v[36:37], v[38:39]
v_pk_add_f32 v[10:11], v[10:11], v[40:41]
v_pk_add_f32 v[10:11], v[10:11], v[42:43]
v_pk_add_f32 v[10:11], v[10:11], v[44:45]
v_pk_add_f32 v[10:11], v[10:11], v[46:47]
v_pk_add_f32 v[10:11], v[10:11], v[48:49]
v_pk_add_f32 v[10:11], v[10:11], v[50:51]
v_pk_add_f32 v[10:11], v[10:11], v[52:53]
v_pk_add_f32 v[10:11], v[10:11], v[54:55]
v_add_f32 v10, v10, v11
v_rcp_f32 v8, v8
v_rcp_f32 v10, v10
v_pk_mul_f32 v[16:17], v[16:17], v[8:9] op_sel_hi:[1,0]
v_pk_mul_f32 v[18:19], v[18:19], v[8:9] op_sel_hi:[1,0]
v_pk_mul_f32 v[20:21], v[20:21], v[8:9] op_sel_hi:[1,0]
v_pk_mul_f32 v[22:23], v[22:23], v[8:9] op_sel_hi:[1,0]
v_pk_mul_f32 v[24:25], v[24:25], v[8:9] op_sel_hi:[1,0]
v_pk_mul_f32 v[26:27], v[26:27], v[8:9] op_sel_hi:[1,0]
v_pk_mul_f32 v[28:29], v[28:29], v[8:9] op_sel_hi:[1,0]
v_pk_mul_f32 v[30:31], v[30:31], v[8:9] op_sel_hi:[1,0]
)"
R"(v_pk_mul_f32 v[32:33], v[32:33], v[8:9] op_sel_hi:[1,0]
v_pk_mul_f32 v[34:35], v[34:35], v[8:9] op_sel_hi:[1,0]
v_pk_mul_f32 v[36:37], v[36:37], v[10:11] op_sel_hi:[1,0]
v_pk_mul_f32 v[38:39], v[38:39], v[10:11] op_sel_hi:[1,0]
v_pk_mul_f32 v[40:41], v[40:41], v[10:11] op_sel_hi:[1,0]
v_pk_mul_f32 v[42:43], v[42:43], v[10:11] op_sel_hi:[1,0]
v_pk_mul_f32 v[44:45], v[44:45], v[10:11] op_sel_hi:[1,0]
v_pk_mul_f32 v[46:47], v[46:47], v[10:11] op_sel_hi:[1,0]
v_pk_mul_f32 v[48:49], v[48:49], v[10:11] op_sel_hi:[1,0]
v_pk_mul_f32 v[50:51], v[50:51], v[10:11] op_sel_hi:[1,0]
v_pk_mul_f32 v[52:53], v[52:53], v[10:11] op_sel_hi:[1,0]
v_pk_mul_f32 v[54:55], v[54:55], v[10:11] op_sel_hi:[1,0]
v_cvt_pk_bf16_f32 v56, v16, v17
v_cvt_pk_bf16_f32 v57, v18, v19
v_cvt_pk_bf16_f32 v58, v20, v21
v_cvt_pk_bf16_f32 v59, v22, v23
v_cvt_pk_bf16_f32 v60, v24, v25
v_cvt_pk_bf16_f32 v61, v26, v27
v_cvt_pk_bf16_f32 v62, v28, v29
v_cvt_pk_bf16_f32 v63, v30, v31
v_cvt_pk_bf16_f32 v64, v32, v33
v_cvt_pk_bf16_f32 v65, v34, v35
v_cvt_pk_bf16_f32 v66, v36, v37
v_cvt_pk_bf16_f32 v67, v38, v39
v_cvt_pk_bf16_f32 v68, v40, v41
v_cvt_pk_bf16_f32 v69, v42, v43
v_cvt_pk_bf16_f32 v70, v44, v45
v_cvt_pk_bf16_f32 v71, v46, v47
v_cvt_pk_bf16_f32 v72, v48, v49
v_cvt_pk_bf16_f32 v73, v50, v51
v_cvt_pk_bf16_f32 v74, v52, v53
v_cvt_pk_bf16_f32 v75, v54, v55
s_lshl_b32 s48, s45, 4
v_mov_b32 v76, 0
v_mov_b32 v77, 0
v_mov_b32 v78, 0
v_mov_b32 v79, 0
v_cvt_pk_bf16_f32 v80, v136, v137
v_cvt_pk_bf16_f32 v81, v138, v139
v_cvt_pk_bf16_f32 v82, v140, v141
v_cvt_pk_bf16_f32 v83, v142, v143
v_cvt_pk_bf16_f32 v84, v144, v145
v_cvt_pk_bf16_f32 v85, v146, v147
v_cvt_pk_bf16_f32 v86, v148, v149
v_cvt_pk_bf16_f32 v87, v150, v151
v_cvt_pk_bf16_f32 v88, v152, v153
v_cvt_pk_bf16_f32 v89, v154, v155
v_mov_b32 v90, 0x3f80
v_mov_b32 v91, 0
v_and_b32 v4, 15, v15
v_lshrrev_b32 v5, 4, v15
v_lshl_add_u32 v4, v5, 6, v4
v_add_u32 v4, s48, v4
v_mul_u32_u24 v4, 176, v4
ds_write_b128 v4, v[56:59] offset:0
ds_write_b128 v4, v[60:63] offset:16
ds_write_b128 v4, v[64:67] offset:32
ds_write_b128 v4, v[68:71] offset:48
ds_write_b128 v4, v[72:75] offset:64
ds_write_b128 v4, v[76:79] offset:80
ds_write_b128 v4, v[80:83] offset:96
ds_write_b128 v4, v[84:87] offset:112
ds_write_b128 v4, v[88:91] offset:128
ds_write_b128 v4, v[76:79] offset:144
v_lshrrev_b32 v5, 2, v15
s_lshl_b32 s48, s45, 6
v_add_u32 v5, s48, v5
v_mul_u32_u24 v5, 176, v5
v_and_b32 v6, 3, v15
v_lshl_add_u32 v6, v6, 3, v5
s_waitcnt lgkmcnt(0)
s_barrier
ds_read_b64_tr_b16 v[116:117], v6 offset:0
ds_read_b64_tr_b16 v[118:119], v6 offset:2816
ds_read_b64_tr_b16 v[120:121], v6 offset:32
ds_read_b64_tr_b16 v[122:123], v6 offset:2848
ds_read_b64_tr_b16 v[124:125], v6 offset:64
ds_read_b64_tr_b16 v[126:127], v6 offset:2880
ds_read_b64_tr_b16 v[128:129], v6 offset:96
ds_read_b64_tr_b16 v[130:131], v6 offset:2912
ds_read_b64_tr_b16 v[132:133], v6 offset:128
ds_read_b64_tr_b16 v[134:135], v6 offset:2944
ds_read_b64_tr_b16 v[136:137], v6 offset:5632
ds_read_b64_tr_b16 v[138:139], v6 offset:8448
ds_read_b64_tr_b16 v[140:141], v6 offset:5664
ds_read_b64_tr_b16 v[142:143], v6 offset:8480
ds_read_b64_tr_b16 v[144:145], v6 offset:5696
ds_read_b64_tr_b16 v[146:147], v6 offset:8512
s_waitcnt lgkmcnt(14)
ds_read_b64_tr_b16 v[148:149], v6 offset:5728
ds_read_b64_tr_b16 v[150:151], v6 offset:8544
s_waitcnt lgkmcnt(14)
ds_read_b64_tr_b16 v[152:153], v6 offset:5760
ds_read_b64_tr_b16 v[154:155], v6 offset:8576
s_waitcnt lgkmcnt(14)
s_waitcnt lgkmcnt(12)
v_mfma_f32_16x16x32_bf16 v[192:195], v[116:119], v[128:131], 0
s_waitcnt lgkmcnt(10)
v_mfma_f32_16x16x32_bf16 v[196:199], v[116:119], v[132:135], 0
v_mfma_f32_16x16x32_bf16 v[200:203], v[120:123], v[128:131], 0
v_mfma_f32_16x16x32_bf16 v[204:207], v[120:123], v[132:135], 0
v_mfma_f32_16x16x32_bf16 v[208:211], v[124:127], v[128:131], 0
v_mfma_f32_16x16x32_bf16 v[212:215], v[124:127], v[132:135], 0
s_waitcnt lgkmcnt(2)
v_mfma_f32_16x16x32_bf16 v[192:195], v[136:139], v[148:151], v[192:195]
s_waitcnt lgkmcnt(0)
v_mfma_f32_16x16x32_bf16 v[196:199], v[136:139], v[152:155], v[196:199]
v_mfma_f32_16x16x32_bf16 v[200:203], v[140:143], v[148:151], v[200:203]
v_mfma_f32_16x16x32_bf16 v[204:207], v[140:143], v[152:155], v[204:207]
v_mfma_f32_16x16x32_bf16 v[208:211], v[144:147], v[148:151], v[208:211]
v_mfma_f32_16x16x32_bf16 v[212:215], v[144:147], v[152:155], v[212:215]
s_lshl_b32 s48, s27, 5
s_lshl2_add_u32 s48, s26, s48
s_add_u32 s48, s48, s45
s_mul_i32 s48, s48, 6144
s_add_u32 s48, s48, 154738688
s_add_u32 s49, s48, 4096
buffer_store_dwordx4 v[192:195], v0, s[40:43], s48 offen sc1
buffer_store_dwordx4 v[196:199], v0, s[40:43], s48 offen offset:1024 sc1
buffer_store_dwordx4 v[200:203], v0, s[40:43], s48 offen offset:2048 sc1
buffer_store_dwordx4 v[204:207], v0, s[40:43], s48 offen offset:3072 sc1
buffer_store_dwordx4 v[208:211], v0, s[40:43], s49 offen sc1
buffer_store_dwordx4 v[212:215], v0, s[40:43], s49 offen offset:1024 sc1
v_max3_f32 v8, v96, v97, v98
v_max3_f32 v8, v8, v99, v100
v_max3_f32 v8, v8, v101, v102
v_max3_f32 v8, v8, v103, v104
v_max3_f32 v8, v8, v105, v106
v_max3_f32 v8, v8, v107, v108
v_max3_f32 v8, v8, v109, v110
v_max3_f32 v8, v8, v111, v112
v_max3_f32 v8, v8, v113, v114
v_max_f32 v8, v8, v115
v_min3_f32 v9, v96, v97, v98
v_min3_f32 v9, v9, v99, v100
v_min3_f32 v9, v9, v101, v102
v_min3_f32 v9, v9, v103, v104
v_min3_f32 v9, v9, v105, v106
v_min3_f32 v9, v9, v107, v108
v_min3_f32 v9, v9, v109, v110
v_min3_f32 v9, v9, v111, v112
v_min3_f32 v9, v9, v113, v114
v_min_f32 v9, v9, v115
v_mul_f32 v8, 0xbfb8aa3b, v8
v_mul_f32 v10, 0x3fb8aa3b, v9
v_pk_fma_f32 v[16:17], v[96:97], s[22:23], v[8:9] op_sel_hi:[1,1,0]
v_pk_fma_f32 v[36:37], v[96:97], s[24:25], v[10:11] op_sel_hi:[1,1,0]
v_pk_fma_f32 v[18:19], v[98:99], s[22:23], v[8:9] op_sel_hi:[1,1,0]
v_pk_fma_f32 v[38:39], v[98:99], s[24:25], v[10:11] op_sel_hi:[1,1,0]
v_pk_fma_f32 v[20:21], v[100:101], s[22:23], v[8:9] op_sel_hi:[1,1,0]
v_pk_fma_f32 v[40:41], v[100:101], s[24:25], v[10:11] op_sel_hi:[1,1,0]
v_pk_fma_f32 v[22:23], v[102:103], s[22:23], v[8:9] op_sel_hi:[1,1,0]
v_pk_fma_f32 v[42:43], v[102:103], s[24:25], v[10:11] op_sel_hi:[1,1,0]
v_pk_fma_f32 v[24:25], v[104:105], s[22:23], v[8:9] op_sel_hi:[1,1,0]
v_pk_fma_f32 v[44:45], v[104:105], s[24:25], v[10:11] op_sel_hi:[1,1,0]
v_pk_fma_f32 v[26:27], v[106:107], s[22:23], v[8:9] op_sel_hi:[1,1,0]
v_pk_fma_f32 v[46:47], v[106:107], s[24:25], v[10:11] op_sel_hi:[1,1,0]
v_pk_fma_f32 v[28:29], v[108:109], s[22:23], v[8:9] op_sel_hi:[1,1,0]
v_pk_fma_f32 v[48:49], v[108:109], s[24:25], v[10:11] op_sel_hi:[1,1,0]
v_pk_fma_f32 v[30:31], v[110:111], s[22:23], v[8:9] op_sel_hi:[1,1,0]
v_pk_fma_f32 v[50:51], v[110:111], s[24:25], v[10:11] op_sel_hi:[1,1,0]
v_pk_fma_f32 v[32:33], v[112:113], s[22:23], v[8:9] op_sel_hi:[1,1,0]
v_pk_fma_f32 v[52:53], v[112:113], s[24:25], v[10:11] op_sel_hi:[1,1,0]
v_pk_fma_f32 v[34:35], v[114:115], s[22:23], v[8:9] op_sel_hi:[1,1,0]
v_pk_fma_f32 v[54:55], v[114:115], s[24:25], v[10:11] op_sel_hi:[1,1,0]
v_exp_f32 v16, v16
v_exp_f32 v17, v17
v_exp_f32 v18, v18
v_exp_f32 v19, v19
v_exp_f32 v20, v20
v_exp_f32 v21, v21
v_exp_f32 v22, v22
v_exp_f32 v23, v23
v_exp_f32 v24, v24
v_exp_f32 v25, v25
v_exp_f32 v26, v26
v_exp_f32 v27, v27
v_exp_f32 v28, v28
v_exp_f32 v29, v29
v_exp_f32 v30, v30
v_exp_f32 v31, v31
v_exp_f32 v32, v32
v_exp_f32 v33, v33
v_exp_f32 v34, v34
v_exp_f32 v35, v35
v_exp_f32 v36, v36
v_exp_f32 v37, v37
v_exp_f32 v38, v38
v_exp_f32 v39, v39
v_exp_f32 v40, v40
v_exp_f32 v41, v41
v_exp_f32 v42, v42
v_exp_f32 v43, v43
v_exp_f32 v44, v44
v_exp_f32 v45, v45
v_exp_f32 v46, v46
v_exp_f32 v47, v47
v_exp_f32 v48, v48
v_exp_f32 v49, v49
v_exp_f32 v50, v50
)"
R"(v_exp_f32 v51, v51
v_exp_f32 v52, v52
v_exp_f32 v53, v53
v_exp_f32 v54, v54
v_exp_f32 v55, v55
v_pk_add_f32 v[8:9], v[16:17], v[18:19]
v_pk_add_f32 v[8:9], v[8:9], v[20:21]
v_pk_add_f32 v[8:9], v[8:9], v[22:23]
v_pk_add_f32 v[8:9], v[8:9], v[24:25]
v_pk_add_f32 v[8:9], v[8:9], v[26:27]
v_pk_add_f32 v[8:9], v[8:9], v[28:29]
v_pk_add_f32 v[8:9], v[8:9], v[30:31]
v_pk_add_f32 v[8:9], v[8:9], v[32:33]
v_pk_add_f32 v[8:9], v[8:9], v[34:35]
v_add_f32 v8, v8, v9
v_pk_add_f32 v[10:11], v[36:37], v[38:39]
v_pk_add_f32 v[10:11], v[10:11], v[40:41]
v_pk_add_f32 v[10:11], v[10:11], v[42:43]
v_pk_add_f32 v[10:11], v[10:11], v[44:45]
v_pk_add_f32 v[10:11], v[10:11], v[46:47]
v_pk_add_f32 v[10:11], v[10:11], v[48:49]
v_pk_add_f32 v[10:11], v[10:11], v[50:51]
v_pk_add_f32 v[10:11], v[10:11], v[52:53]
v_pk_add_f32 v[10:11], v[10:11], v[54:55]
v_add_f32 v10, v10, v11
v_rcp_f32 v8, v8
v_rcp_f32 v10, v10
v_pk_mul_f32 v[16:17], v[16:17], v[8:9] op_sel_hi:[1,0]
v_pk_mul_f32 v[18:19], v[18:19], v[8:9] op_sel_hi:[1,0]
v_pk_mul_f32 v[20:21], v[20:21], v[8:9] op_sel_hi:[1,0]
v_pk_mul_f32 v[22:23], v[22:23], v[8:9] op_sel_hi:[1,0]
v_pk_mul_f32 v[24:25], v[24:25], v[8:9] op_sel_hi:[1,0]
v_pk_mul_f32 v[26:27], v[26:27], v[8:9] op_sel_hi:[1,0]
v_pk_mul_f32 v[28:29], v[28:29], v[8:9] op_sel_hi:[1,0]
v_pk_mul_f32 v[30:31], v[30:31], v[8:9] op_sel_hi:[1,0]
v_pk_mul_f32 v[32:33], v[32:33], v[8:9] op_sel_hi:[1,0]
v_pk_mul_f32 v[34:35], v[34:35], v[8:9] op_sel_hi:[1,0]
v_pk_mul_f32 v[36:37], v[36:37], v[10:11] op_sel_hi:[1,0]
v_pk_mul_f32 v[38:39], v[38:39], v[10:11] op_sel_hi:[1,0]
v_pk_mul_f32 v[40:41], v[40:41], v[10:11] op_sel_hi:[1,0]
v_pk_mul_f32 v[42:43], v[42:43], v[10:11] op_sel_hi:[1,0]
v_pk_mul_f32 v[44:45], v[44:45], v[10:11] op_sel_hi:[1,0]
v_pk_mul_f32 v[46:47], v[46:47], v[10:11] op_sel_hi:[1,0]
v_pk_mul_f32 v[48:49], v[48:49], v[10:11] op_sel_hi:[1,0]
v_pk_mul_f32 v[50:51], v[50:51], v[10:11] op_sel_hi:[1,0]
v_pk_mul_f32 v[52:53], v[52:53], v[10:11] op_sel_hi:[1,0]
v_pk_mul_f32 v[54:55], v[54:55], v[10:11] op_sel_hi:[1,0]
v_cvt_pk_bf16_f32 v56, v16, v17
v_cvt_pk_bf16_f32 v57, v18, v19
v_cvt_pk_bf16_f32 v58, v20, v21
v_cvt_pk_bf16_f32 v59, v22, v23
v_cvt_pk_bf16_f32 v60, v24, v25
v_cvt_pk_bf16_f32 v61, v26, v27
v_cvt_pk_bf16_f32 v62, v28, v29
v_cvt_pk_bf16_f32 v63, v30, v31
v_cvt_pk_bf16_f32 v64, v32, v33
v_cvt_pk_bf16_f32 v65, v34, v35
v_cvt_pk_bf16_f32 v66, v36, v37
v_cvt_pk_bf16_f32 v67, v38, v39
v_cvt_pk_bf16_f32 v68, v40, v41
v_cvt_pk_bf16_f32 v69, v42, v43
v_cvt_pk_bf16_f32 v70, v44, v45
v_cvt_pk_bf16_f32 v71, v46, v47
v_cvt_pk_bf16_f32 v72, v48, v49
v_cvt_pk_bf16_f32 v73, v50, v51
v_cvt_pk_bf16_f32 v74, v52, v53
v_cvt_pk_bf16_f32 v75, v54, v55
s_lshl3_add_u32 s48, s28, s26
s_mul_i32 s48, s48, 5120
s_add_u32 s48, s48, 149495808
s_add_u32 s49, s48, 4096
buffer_store_dwordx4 v[56:59], v0, s[40:43], s48 offen sc1
buffer_store_dwordx4 v[60:63], v0, s[40:43], s48 offen offset:1024 sc1
buffer_store_dwordx4 v[64:67], v0, s[40:43], s48 offen offset:2048 sc1
buffer_store_dwordx4 v[68:71], v0, s[40:43], s48 offen offset:3072 sc1
buffer_store_dwordx4 v[72:75], v0, s[40:43], s49 offen sc1
s_lshl3_add_u32 s54, s21, s26
s_lshl_b32 s54, s54, 7
s_add_u32 s54, s54, 2362368
s_branch hp_done_%=
hp_rd_%=:
s_sub_u32 s26, s20, 368
s_lshr_b32 s27, s26, 2
s_lshl3_add_u32 s28, s21, s27
s_lshl_b32 s54, s28, 7
s_add_u32 s54, s54, 2362368
s_cmp_lg_u32 s45, 0
s_cbranch_scc1 hprd_dep_ok_%=
v_cmp_eq_u32 vcc, 0, v15
s_nop 1
v_cndmask_b32 v14, 0, 28, vcc
s_mov_b32 s60, 0
hprd_dep_%=:
v_mov_b32 v4, 0
buffer_atomic_add v4, v2, s[40:43], s54 offen sc0
s_waitcnt vmcnt(0)
v_cmp_gt_u32 vcc, v14, v4
s_nop 4
s_cbranch_vccz hprd_dep_ok_%=
s_sleep 1
s_add_u32 s60, s60, 1
s_cmp_gt_u32 s60, 65536
s_cbranch_scc1 hp_abort_%=
s_and_b32 s59, s60, 63
s_cbranch_scc1 hprd_dep_%=
v_mov_b32 v4, 1024
buffer_load_dword v4, v4, s[40:43], s58 offen sc1
s_waitcnt vmcnt(0)
v_readfirstlane_b32 s59, v4
s_nop 0
s_cmp_lg_u32 s59, 0
s_nop 2
s_cbranch_scc1 hp_end_%=
s_branch hprd_dep_%=
hprd_dep_ok_%=:
s_barrier
s_mul_i32 s48, s45, 224
s_add_u32 s48, s48, s26
s_mul_i32 s48, s48, 6144
s_add_u32 s48, s48, 154738688
s_add_u32 s49, s48, 4096
buffer_load_dwordx4 v[16:19], v0, s[40:43], s48 offen sc1
buffer_load_dwordx4 v[20:23], v0, s[40:43], s48 offen offset:1024 sc1
buffer_load_dwordx4 v[24:27], v0, s[40:43], s48 offen offset:2048 sc1
buffer_load_dwordx4 v[28:31], v0, s[40:43], s48 offen offset:3072 sc1
buffer_load_dwordx4 v[32:35], v0, s[40:43], s49 offen sc1
buffer_load_dwordx4 v[36:39], v0, s[40:43], s49 offen offset:1024 sc1
s_add_u32 s48, s48, 196608
s_add_u32 s49, s48, 4096
buffer_load_dwordx4 v[40:43], v0, s[40:43], s48 offen sc1
buffer_load_dwordx4 v[44:47], v0, s[40:43], s48 offen offset:1024 sc1
buffer_load_dwordx4 v[48:51], v0, s[40:43], s48 offen offset:2048 sc1
buffer_load_dwordx4 v[52:55], v0, s[40:43], s48 offen offset:3072 sc1
buffer_load_dwordx4 v[56:59], v0, s[40:43], s49 offen sc1
buffer_load_dwordx4 v[60:63], v0, s[40:43], s49 offen offset:1024 sc1
s_add_u32 s48, s48, 196608
s_add_u32 s49, s48, 4096
buffer_load_dwordx4 v[64:67], v0, s[40:43], s48 offen sc1
buffer_load_dwordx4 v[68:71], v0, s[40:43], s48 offen offset:1024 sc1
buffer_load_dwordx4 v[72:75], v0, s[40:43], s48 offen offset:2048 sc1
buffer_load_dwordx4 v[76:79], v0, s[40:43], s48 offen offset:3072 sc1
buffer_load_dwordx4 v[80:83], v0, s[40:43], s49 offen sc1
buffer_load_dwordx4 v[84:87], v0, s[40:43], s49 offen offset:1024 sc1
s_add_u32 s48, s48, 196608
s_add_u32 s49, s48, 4096
buffer_load_dwordx4 v[88:91], v0, s[40:43], s48 offen sc1
buffer_load_dwordx4 v[92:95], v0, s[40:43], s48 offen offset:1024 sc1
buffer_load_dwordx4 v[96:99], v0, s[40:43], s48 offen offset:2048 sc1
buffer_load_dwordx4 v[100:103], v0, s[40:43], s48 offen offset:3072 sc1
buffer_load_dwordx4 v[104:107], v0, s[40:43], s49 offen sc1
buffer_load_dwordx4 v[108:111], v0, s[40:43], s49 offen offset:1024 sc1
s_add_u32 s48, s48, 196608
s_add_u32 s49, s48, 4096
buffer_load_dwordx4 v[112:115], v0, s[40:43], s48 offen sc1
buffer_load_dwordx4 v[116:119], v0, s[40:43], s48 offen offset:1024 sc1
buffer_load_dwordx4 v[120:123], v0, s[40:43], s48 offen offset:2048 sc1
buffer_load_dwordx4 v[124:127], v0, s[40:43], s48 offen offset:3072 sc1
buffer_load_dwordx4 v[128:131], v0, s[40:43], s49 offen sc1
buffer_load_dwordx4 v[132:135], v0, s[40:43], s49 offen offset:1024 sc1
s_add_u32 s48, s48, 196608
s_add_u32 s49, s48, 4096
buffer_load_dwordx4 v[136:139], v0, s[40:43], s48 offen sc1
buffer_load_dwordx4 v[140:143], v0, s[40:43], s48 offen offset:1024 sc1
buffer_load_dwordx4 v[144:147], v0, s[40:43], s48 offen offset:2048 sc1
buffer_load_dwordx4 v[148:151], v0, s[40:43], s48 offen offset:3072 sc1
buffer_load_dwordx4 v[152:155], v0, s[40:43], s49 offen sc1
buffer_load_dwordx4 v[156:159], v0, s[40:43], s49 offen offset:1024 sc1
s_add_u32 s48, s48, 196608
s_add_u32 s49, s48, 4096
buffer_load_dwordx4 v[160:163], v0, s[40:43], s48 offen sc1
buffer_load_dwordx4 v[164:167], v0, s[40:43], s48 offen offset:1024 sc1
buffer_load_dwordx4 v[168:171], v0, s[40:43], s48 offen offset:2048 sc1
buffer_load_dwordx4 v[172:175], v0, s[40:43], s48 offen offset:3072 sc1
buffer_load_dwordx4 v[176:179], v0, s[40:43], s49 offen sc1
buffer_load_dwordx4 v[180:183], v0, s[40:43], s49 offen offset:1024 sc1
s_add_u32 s48, s48, 196608
s_mul_i32 s48, s45, 6144
v_add_u32 v6, s48, v0
s_waitcnt vmcnt(35)
v_pk_add_f32 v[16:17], v[16:17], v[40:41]
v_pk_add_f32 v[18:19], v[18:19], v[42:43]
s_waitcnt vmcnt(34)
v_pk_add_f32 v[20:21], v[20:21], v[44:45]
v_pk_add_f32 v[22:23], v[22:23], v[46:47]
s_waitcnt vmcnt(33)
v_pk_add_f32 v[24:25], v[24:25], v[48:49]
v_pk_add_f32 v[26:27], v[26:27], v[50:51]
s_waitcnt vmcnt(32)
v_pk_add_f32 v[28:29], v[28:29], v[52:53]
v_pk_add_f32 v[30:31], v[30:31], v[54:55]
s_waitcnt vmcnt(31)
v_pk_add_f32 v[32:33], v[32:33], v[56:57]
v_pk_add_f32 v[34:35], v[34:35], v[58:59]
s_waitcnt vmcnt(30)
v_pk_add_f32 v[36:37], v[36:37], v[60:61]
v_pk_add_f32 v[38:39], v[38:39], v[62:63]
s_waitcnt vmcnt(29)
v_pk_add_f32 v[16:17], v[16:17], v[64:65]
v_pk_add_f32 v[18:19], v[18:19], v[66:67]
s_waitcnt vmcnt(28)
)"
R"(v_pk_add_f32 v[20:21], v[20:21], v[68:69]
v_pk_add_f32 v[22:23], v[22:23], v[70:71]
s_waitcnt vmcnt(27)
v_pk_add_f32 v[24:25], v[24:25], v[72:73]
v_pk_add_f32 v[26:27], v[26:27], v[74:75]
s_waitcnt vmcnt(26)
v_pk_add_f32 v[28:29], v[28:29], v[76:77]
v_pk_add_f32 v[30:31], v[30:31], v[78:79]
s_waitcnt vmcnt(25)
v_pk_add_f32 v[32:33], v[32:33], v[80:81]
v_pk_add_f32 v[34:35], v[34:35], v[82:83]
s_waitcnt vmcnt(24)
v_pk_add_f32 v[36:37], v[36:37], v[84:85]
v_pk_add_f32 v[38:39], v[38:39], v[86:87]
s_waitcnt vmcnt(23)
v_pk_add_f32 v[16:17], v[16:17], v[88:89]
v_pk_add_f32 v[18:19], v[18:19], v[90:91]
s_waitcnt vmcnt(22)
v_pk_add_f32 v[20:21], v[20:21], v[92:93]
v_pk_add_f32 v[22:23], v[22:23], v[94:95]
s_waitcnt vmcnt(21)
v_pk_add_f32 v[24:25], v[24:25], v[96:97]
v_pk_add_f32 v[26:27], v[26:27], v[98:99]
s_waitcnt vmcnt(20)
v_pk_add_f32 v[28:29], v[28:29], v[100:101]
v_pk_add_f32 v[30:31], v[30:31], v[102:103]
s_waitcnt vmcnt(19)
v_pk_add_f32 v[32:33], v[32:33], v[104:105]
v_pk_add_f32 v[34:35], v[34:35], v[106:107]
s_waitcnt vmcnt(18)
v_pk_add_f32 v[36:37], v[36:37], v[108:109]
v_pk_add_f32 v[38:39], v[38:39], v[110:111]
s_waitcnt vmcnt(17)
v_pk_add_f32 v[16:17], v[16:17], v[112:113]
v_pk_add_f32 v[18:19], v[18:19], v[114:115]
s_waitcnt vmcnt(16)
v_pk_add_f32 v[20:21], v[20:21], v[116:117]
v_pk_add_f32 v[22:23], v[22:23], v[118:119]
s_waitcnt vmcnt(15)
v_pk_add_f32 v[24:25], v[24:25], v[120:121]
v_pk_add_f32 v[26:27], v[26:27], v[122:123]
s_waitcnt vmcnt(14)
v_pk_add_f32 v[28:29], v[28:29], v[124:125]
v_pk_add_f32 v[30:31], v[30:31], v[126:127]
s_waitcnt vmcnt(13)
v_pk_add_f32 v[32:33], v[32:33], v[128:129]
v_pk_add_f32 v[34:35], v[34:35], v[130:131]
s_waitcnt vmcnt(12)
v_pk_add_f32 v[36:37], v[36:37], v[132:133]
v_pk_add_f32 v[38:39], v[38:39], v[134:135]
s_waitcnt vmcnt(11)
v_pk_add_f32 v[16:17], v[16:17], v[136:137]
v_pk_add_f32 v[18:19], v[18:19], v[138:139]
s_waitcnt vmcnt(10)
v_pk_add_f32 v[20:21], v[20:21], v[140:141]
v_pk_add_f32 v[22:23], v[22:23], v[142:143]
s_waitcnt vmcnt(9)
v_pk_add_f32 v[24:25], v[24:25], v[144:145]
v_pk_add_f32 v[26:27], v[26:27], v[146:147]
s_waitcnt vmcnt(8)
v_pk_add_f32 v[28:29], v[28:29], v[148:149]
v_pk_add_f32 v[30:31], v[30:31], v[150:151]
s_waitcnt vmcnt(7)
v_pk_add_f32 v[32:33], v[32:33], v[152:153]
v_pk_add_f32 v[34:35], v[34:35], v[154:155]
s_waitcnt vmcnt(6)
v_pk_add_f32 v[36:37], v[36:37], v[156:157]
v_pk_add_f32 v[38:39], v[38:39], v[158:159]
s_waitcnt vmcnt(5)
v_pk_add_f32 v[16:17], v[16:17], v[160:161]
v_pk_add_f32 v[18:19], v[18:19], v[162:163]
s_waitcnt vmcnt(4)
v_pk_add_f32 v[20:21], v[20:21], v[164:165]
v_pk_add_f32 v[22:23], v[22:23], v[166:167]
s_waitcnt vmcnt(3)
v_pk_add_f32 v[24:25], v[24:25], v[168:169]
v_pk_add_f32 v[26:27], v[26:27], v[170:171]
s_waitcnt vmcnt(2)
v_pk_add_f32 v[28:29], v[28:29], v[172:173]
v_pk_add_f32 v[30:31], v[30:31], v[174:175]
s_waitcnt vmcnt(1)
v_pk_add_f32 v[32:33], v[32:33], v[176:177]
v_pk_add_f32 v[34:35], v[34:35], v[178:179]
s_waitcnt vmcnt(0)
v_pk_add_f32 v[36:37], v[36:37], v[180:181]
v_pk_add_f32 v[38:39], v[38:39], v[182:183]
ds_write_b128 v6, v[16:19] offset:0
ds_write_b128 v6, v[20:23] offset:1024
ds_write_b128 v6, v[24:27] offset:2048
ds_write_b128 v6, v[28:31] offset:3072
ds_write_b128 v6, v[32:35] offset:4096
ds_write_b128 v6, v[36:39] offset:5120
s_waitcnt lgkmcnt(0)
s_barrier
s_mul_i32 s50, s26, 6144
s_add_u32 s50, s50, 161816576
s_add_u32 s48, s45, 0
s_min_u32 s48, s48, 5
s_lshl_b32 s49, s48, 10
v_add_u32 v6, s49, v0
ds_read_b128 v[16:19], v6 offset:0
ds_read_b128 v[20:23], v6 offset:6144
ds_read_b128 v[24:27], v6 offset:12288
ds_read_b128 v[28:31], v6 offset:18432
s_waitcnt lgkmcnt(2)
v_pk_add_f32 v[16:17], v[16:17], v[20:21]
s_waitcnt lgkmcnt(0)
v_pk_add_f32 v[24:25], v[24:25], v[28:29]
v_pk_add_f32 v[16:17], v[16:17], v[24:25]
v_pk_add_f32 v[18:19], v[18:19], v[22:23]
v_pk_add_f32 v[26:27], v[26:27], v[30:31]
v_pk_add_f32 v[18:19], v[18:19], v[26:27]
buffer_store_dwordx4 v[16:19], v6, s[40:43], s50 offen sc1
s_add_u32 s48, s45, 4
s_min_u32 s48, s48, 5
s_lshl_b32 s49, s48, 10
v_add_u32 v7, s49, v0
ds_read_b128 v[48:51], v7 offset:0
ds_read_b128 v[52:55], v7 offset:6144
ds_read_b128 v[56:59], v7 offset:12288
ds_read_b128 v[60:63], v7 offset:18432
s_waitcnt lgkmcnt(2)
v_pk_add_f32 v[48:49], v[48:49], v[52:53]
s_waitcnt lgkmcnt(0)
v_pk_add_f32 v[56:57], v[56:57], v[60:61]
v_pk_add_f32 v[48:49], v[48:49], v[56:57]
v_pk_add_f32 v[50:51], v[50:51], v[54:55]
v_pk_add_f32 v[58:59], v[58:59], v[62:63]
v_pk_add_f32 v[50:51], v[50:51], v[58:59]
buffer_store_dwordx4 v[48:51], v7, s[40:43], s50 offen sc1
s_lshl_b32 s54, s28, 7
s_add_u32 s54, s54, 2368512
s_branch hp_done_%=
hp_b2_%=:
s_sub_u32 s26, s20, 400
s_and_b32 s29, s26, 1
s_lshr_b32 s26, s26, 1
s_and_b32 s30, s45, 1
s_lshr_b32 s31, s45, 1
s_lshl1_add_u32 s31, s29, s31
s_lshl2_add_u32 s50, s29, s45
s_mul_i32 s50, s50, 102400
s_add_u32 s50, s50, 148676608
s_mul_i32 s61, s45, 12288
s_add_u32 s61, s61, 102656
s_add_u32 m0, s61, 0
s_add_u32 s51, s50, 90112
buffer_load_dwordx4 v0, s[40:43], s51 offen offset:0 sc1 lds
buffer_load_dwordx4 v0, s[40:43], s51 offen offset:1024 sc1 lds
buffer_load_dwordx4 v0, s[40:43], s51 offen offset:2048 sc1 lds
buffer_load_dwordx4 v0, s[40:43], s51 offen offset:3072 sc1 lds
s_add_u32 m0, s61, 4096
s_add_u32 s51, s50, 94208
buffer_load_dwordx4 v0, s[40:43], s51 offen offset:0 sc1 lds
buffer_load_dwordx4 v0, s[40:43], s51 offen offset:1024 sc1 lds
buffer_load_dwordx4 v0, s[40:43], s51 offen offset:2048 sc1 lds
buffer_load_dwordx4 v0, s[40:43], s51 offen offset:3072 sc1 lds
s_add_u32 m0, s61, 8192
s_add_u32 s51, s50, 98304
buffer_load_dwordx4 v0, s[40:43], s51 offen offset:0 sc1 lds
buffer_load_dwordx4 v0, s[40:43], s51 offen offset:1024 sc1 lds
buffer_load_dwordx4 v0, s[40:43], s51 offen offset:2048 sc1 lds
buffer_load_dwordx4 v0, s[40:43], s51 offen offset:3072 sc1 lds
buffer_load_dwordx4 v[128:131], v0, s[40:43], s50 offen sc1
buffer_load_dwordx4 v[132:135], v0, s[40:43], s50 offen offset:1024 sc1
buffer_load_dwordx4 v[136:139], v0, s[40:43], s50 offen offset:2048 sc1
buffer_load_dwordx4 v[140:143], v0, s[40:43], s50 offen offset:3072 sc1
s_add_u32 s50, s50, 4096
buffer_load_dwordx4 v[144:147], v0, s[40:43], s50 offen sc1
buffer_load_dwordx4 v[148:151], v0, s[40:43], s50 offen offset:1024 sc1
buffer_load_dwordx4 v[152:155], v0, s[40:43], s50 offen offset:2048 sc1
buffer_load_dwordx4 v[156:159], v0, s[40:43], s50 offen offset:3072 sc1
s_add_u32 s50, s50, 4096
buffer_load_dwordx4 v[160:163], v0, s[40:43], s50 offen sc1
buffer_load_dwordx4 v[164:167], v0, s[40:43], s50 offen offset:1024 sc1
buffer_load_dwordx4 v[168:171], v0, s[40:43], s50 offen offset:2048 sc1
buffer_load_dwordx4 v[172:175], v0, s[40:43], s50 offen offset:3072 sc1
s_add_u32 s50, s50, 4096
buffer_load_dwordx4 v[176:179], v0, s[40:43], s50 offen sc1
buffer_load_dwordx4 v[180:183], v0, s[40:43], s50 offen offset:1024 sc1
buffer_load_dwordx4 v[184:187], v0, s[40:43], s50 offen offset:2048 sc1
buffer_load_dwordx4 v[188:191], v0, s[40:43], s50 offen offset:3072 sc1
s_add_u32 s50, s50, 4096
buffer_load_dwordx4 v[192:195], v0, s[40:43], s50 offen sc1
buffer_load_dwordx4 v[196:199], v0, s[40:43], s50 offen offset:1024 sc1
buffer_load_dwordx4 v[200:203], v0, s[40:43], s50 offen offset:2048 sc1
buffer_load_dwordx4 v[204:207], v0, s[40:43], s50 offen offset:3072 sc1
s_add_u32 s50, s50, 4096
buffer_load_dwordx4 v[208:211], v0, s[40:43], s50 offen sc1
buffer_load_dwordx4 v[212:215], v0, s[40:43], s50 offen offset:1024 sc1
buffer_load_dwordx4 v[216:219], v0, s[40:43], s50 offen offset:2048 sc1
buffer_load_dwordx4 v[220:223], v0, s[40:43], s50 offen offset:3072 sc1
s_add_u32 s50, s50, 4096
buffer_load_dwordx4 v[224:227], v0, s[40:43], s50 offen sc1
buffer_load_dwordx4 v[228:231], v0, s[40:43], s50 offen offset:1024 sc1
buffer_load_dwordx4 v[232:235], v0, s[40:43], s50 offen offset:2048 sc1
buffer_load_dwordx4 v[236:239], v0, s[40:43], s50 offen offset:3072 sc1
s_add_u32 s50, s50, 4096
buffer_load_dwordx4 v[240:243], v0, s[40:43], s50 offen sc1
buffer_load_dwordx4 v[244:247], v0, s[40:43], s50 offen offset:1024 sc1
)"
R"(buffer_load_dwordx4 v[248:251], v0, s[40:43], s50 offen offset:2048 sc1
buffer_load_dwordx4 v[252:255], v0, s[40:43], s50 offen offset:3072 sc1
v_lshlrev_b32 v8, 7, v15
s_waitcnt vmcnt(0)
s_cmp_eq_u32 s21, 0
s_cbranch_scc1 hpb2_warm_%=
s_add_u32 s50, s50, 4096
buffer_load_dwordx4 a[0:3], v0, s[40:43], s50 offen sc1
buffer_load_dwordx4 a[4:7], v0, s[40:43], s50 offen offset:1024 sc1
buffer_load_dwordx4 a[8:11], v0, s[40:43], s50 offen offset:2048 sc1
buffer_load_dwordx4 a[12:15], v0, s[40:43], s50 offen offset:3072 sc1
s_add_u32 s50, s50, 4096
buffer_load_dwordx4 a[16:19], v0, s[40:43], s50 offen sc1
buffer_load_dwordx4 a[20:23], v0, s[40:43], s50 offen offset:1024 sc1
buffer_load_dwordx4 a[24:27], v0, s[40:43], s50 offen offset:2048 sc1
buffer_load_dwordx4 a[28:31], v0, s[40:43], s50 offen offset:3072 sc1
s_add_u32 s50, s50, 4096
buffer_load_dwordx4 a[32:35], v0, s[40:43], s50 offen sc1
buffer_load_dwordx4 a[36:39], v0, s[40:43], s50 offen offset:1024 sc1
buffer_load_dwordx4 a[40:43], v0, s[40:43], s50 offen offset:2048 sc1
buffer_load_dwordx4 a[44:47], v0, s[40:43], s50 offen offset:3072 sc1
s_add_u32 s50, s50, 4096
buffer_load_dwordx4 a[48:51], v0, s[40:43], s50 offen sc1
buffer_load_dwordx4 a[52:55], v0, s[40:43], s50 offen offset:1024 sc1
buffer_load_dwordx4 a[56:59], v0, s[40:43], s50 offen offset:2048 sc1
buffer_load_dwordx4 a[60:63], v0, s[40:43], s50 offen offset:3072 sc1
s_add_u32 s50, s50, 4096
buffer_load_dwordx4 a[64:67], v0, s[40:43], s50 offen sc1
buffer_load_dwordx4 a[68:71], v0, s[40:43], s50 offen offset:1024 sc1
buffer_load_dwordx4 a[72:75], v0, s[40:43], s50 offen offset:2048 sc1
buffer_load_dwordx4 a[76:79], v0, s[40:43], s50 offen offset:3072 sc1
s_add_u32 s50, s50, 4096
buffer_load_dwordx4 a[80:83], v0, s[40:43], s50 offen sc1
buffer_load_dwordx4 a[84:87], v0, s[40:43], s50 offen offset:1024 sc1
buffer_load_dwordx4 a[88:91], v0, s[40:43], s50 offen offset:2048 sc1
buffer_load_dwordx4 a[92:95], v0, s[40:43], s50 offen offset:3072 sc1
s_add_u32 s50, s50, 4096
buffer_load_dwordx4 a[96:99], v0, s[40:43], s50 offen sc1
buffer_load_dwordx4 a[100:103], v0, s[40:43], s50 offen offset:1024 sc1
buffer_load_dwordx4 a[104:107], v0, s[40:43], s50 offen offset:2048 sc1
buffer_load_dwordx4 a[108:111], v0, s[40:43], s50 offen offset:3072 sc1
s_add_u32 s50, s50, 4096
buffer_load_dwordx4 a[112:115], v0, s[40:43], s50 offen sc1
buffer_load_dwordx4 a[116:119], v0, s[40:43], s50 offen offset:1024 sc1
buffer_load_dwordx4 a[120:123], v0, s[40:43], s50 offen offset:2048 sc1
buffer_load_dwordx4 a[124:127], v0, s[40:43], s50 offen offset:3072 sc1
s_add_u32 s50, s50, 4096
buffer_load_dwordx4 a[128:131], v0, s[40:43], s50 offen sc1
buffer_load_dwordx4 a[132:135], v0, s[40:43], s50 offen offset:1024 sc1
buffer_load_dwordx4 a[136:139], v0, s[40:43], s50 offen offset:2048 sc1
buffer_load_dwordx4 a[140:143], v0, s[40:43], s50 offen offset:3072 sc1
s_add_u32 s50, s50, 4096
buffer_load_dwordx4 a[144:147], v0, s[40:43], s50 offen sc1
buffer_load_dwordx4 a[148:151], v0, s[40:43], s50 offen offset:1024 sc1
buffer_load_dwordx4 a[152:155], v0, s[40:43], s50 offen offset:2048 sc1
buffer_load_dwordx4 a[156:159], v0, s[40:43], s50 offen offset:3072 sc1
s_add_u32 s50, s50, 4096
buffer_load_dwordx4 a[160:163], v0, s[40:43], s50 offen sc1
buffer_load_dwordx4 a[164:167], v0, s[40:43], s50 offen offset:1024 sc1
buffer_load_dwordx4 a[168:171], v0, s[40:43], s50 offen offset:2048 sc1
buffer_load_dwordx4 a[172:175], v0, s[40:43], s50 offen offset:3072 sc1
s_add_u32 s50, s50, 4096
buffer_load_dwordx4 a[176:179], v0, s[40:43], s50 offen sc1
buffer_load_dwordx4 a[180:183], v0, s[40:43], s50 offen offset:1024 sc1
buffer_load_dwordx4 a[184:187], v0, s[40:43], s50 offen offset:2048 sc1
buffer_load_dwordx4 a[188:191], v0, s[40:43], s50 offen offset:3072 sc1
s_add_u32 s50, s50, 4096
buffer_load_dwordx4 a[192:195], v0, s[40:43], s50 offen sc1
buffer_load_dwordx4 a[196:199], v0, s[40:43], s50 offen offset:1024 sc1
buffer_load_dwordx4 a[200:203], v0, s[40:43], s50 offen offset:2048 sc1
buffer_load_dwordx4 a[204:207], v0, s[40:43], s50 offen offset:3072 sc1
s_add_u32 s50, s50, 4096
buffer_load_dwordx4 a[208:211], v0, s[40:43], s50 offen sc1
buffer_load_dwordx4 a[212:215], v0, s[40:43], s50 offen offset:1024 sc1
buffer_load_dwordx4 a[216:219], v0, s[40:43], s50 offen offset:2048 sc1
buffer_load_dwordx4 a[220:223], v0, s[40:43], s50 offen offset:3072 sc1
s_waitcnt vmcnt(0)
s_nop 0
s_branch hpb2_pre_%=
hpb2_warm_%=:
s_add_u32 s48, s50, 4096
buffer_load_dword v24, v8, s[40:43], s48 offen sc1
s_add_u32 s48, s48, 8192
buffer_load_dword v25, v8, s[40:43], s48 offen sc1
s_add_u32 s48, s48, 8192
buffer_load_dword v26, v8, s[40:43], s48 offen sc1
s_add_u32 s48, s48, 8192
buffer_load_dword v27, v8, s[40:43], s48 offen sc1
s_add_u32 s48, s48, 8192
buffer_load_dword v28, v8, s[40:43], s48 offen sc1
s_add_u32 s48, s48, 8192
buffer_load_dword v29, v8, s[40:43], s48 offen sc1
s_add_u32 s48, s48, 8192
buffer_load_dword v30, v8, s[40:43], s48 offen sc1
s_add_u32 s48, s48, 8192
s_waitcnt vmcnt(0)
hpb2_pre_%=:
v_mov_b32 v16, 0
v_lshlrev_b32 v4, 2, v15
ds_write_b32 v4, v16 offset:61440
s_mul_i32 s27, s45, 15360
v_add_u32 v6, s27, v0
v_and_b32 v7, 0xf0, v0
v_add_u32 v7, s27, v7
v_mov_b32 v9, 61440
v_mov_b32 v3, 256
v_bfe_u32 v4, v15, 2, 2
v_lshrrev_b32 v5, 4, v15
v_cmp_eq_u32 vcc, v4, v5
s_nop 1
v_cndmask_b32 v7, v9, v7, vcc
v_cndmask_b32 v3, 0, v3, vcc
s_mul_i32 s48, s45, 5120
s_add_u32 s48, s48, 61696
v_add_u32 v9, s48, v0
s_lshl3_add_u32 s28, s26, s45
s_mul_i32 s28, s28, 10240
s_lshl1_add_u32 s34, s26, s30
s_lshl2_add_u32 s34, s34, s31
s_mul_i32 s34, s34, 10240
s_cmp_ge_u32 s26, 56
s_cbranch_scc1 hpb2_cold_%=
v_and_b32 v10, 7, v15
v_lshlrev_b32 v10, 7, v10
s_lshl_b32 s48, s21, 10
s_add_u32 s48, s48, 265216
v_add_u32 v10, s48, v10
v_mov_b32 v11, 0
s_mov_b32 s48, 28
v_cmp_gt_u32 vcc, s48, v11
s_cbranch_vccz hpb2_early_ok_%=
s_mov_b32 s60, 0
s_nop 2
hpb2_early_poll_%=:
buffer_load_dword v11, v10, s[40:43], s58 offen sc1
s_waitcnt vmcnt(0)
v_cmp_gt_u32 vcc, s48, v11
s_nop 4
s_cbranch_vccz hpb2_early_ok_%=
s_sleep 20
s_add_u32 s60, s60, 1
s_cmp_gt_u32 s60, 65536
s_cbranch_scc1 hp_abort_%=
s_and_b32 s59, s60, 63
s_cbranch_scc1 hpb2_early_poll_%=
v_mov_b32 v4, 1024
buffer_load_dword v4, v4, s[40:43], s58 offen sc1
s_waitcnt vmcnt(0)
v_readfirstlane_b32 s59, v4
s_nop 0
s_cmp_lg_u32 s59, 0
s_nop 2
s_cbranch_scc1 hp_end_%=
s_branch hpb2_early_poll_%=
hpb2_early_ok_%=:
s_add_u32 s48, s28, 149495808
buffer_load_dword v24, v8, s[40:43], s48 offen sc1
s_add_u32 s48, s28, 149497856
buffer_load_dword v25, v8, s[40:43], s48 offen sc1
s_add_u32 s48, s28, 149536768
buffer_load_dword v26, v8, s[40:43], s48 offen sc1
s_add_u32 s48, s28, 149538816
buffer_load_dword v27, v8, s[40:43], s48 offen sc1
s_add_u32 s48, s34, 138346496
buffer_load_dword v28, v8, s[40:43], s48 offen sc1
s_add_u32 s48, s34, 138348544
buffer_load_dword v29, v8, s[40:43], s48 offen sc1
s_waitcnt vmcnt(0)
hpb2_cold_%=:
v_and_b32 v8, 0x1f0, v0
v_lshrrev_b32 v4, 5, v15
v_lshl_add_u32 v8, v4, 10, v8
s_and_b32 s54, s20, 7
s_lshl3_add_u32 s54, s21, s54
s_lshl_b32 s54, s54, 7
s_add_u32 s54, s54, 2370560
s_waitcnt lgkmcnt(0)
s_cmp_lg_u32 s45, 0
s_cbranch_scc1 hpb2_dep_ok_%=
v_cmp_eq_u32 vcc, 0, v15
s_nop 1
v_cndmask_b32 v14, 0, 8, vcc
s_mov_b32 s60, 0
hpb2_dep_%=:
v_mov_b32 v4, 0
buffer_atomic_add v4, v2, s[40:43], s54 offen sc0
s_waitcnt vmcnt(0)
v_cmp_gt_u32 vcc, v14, v4
s_nop 4
s_cbranch_vccz hpb2_dep_ok_%=
s_sleep 1
s_add_u32 s60, s60, 1
s_cmp_gt_u32 s60, 65536
s_cbranch_scc1 hp_abort_%=
s_and_b32 s59, s60, 63
s_cbranch_scc1 hpb2_dep_%=
v_mov_b32 v4, 1024
buffer_load_dword v4, v4, s[40:43], s58 offen sc1
s_waitcnt vmcnt(0)
v_readfirstlane_b32 s59, v4
)"
R"(s_nop 0
s_cmp_lg_u32 s59, 0
s_nop 2
s_cbranch_scc1 hp_end_%=
s_branch hpb2_dep_%=
hpb2_dep_ok_%=:
s_barrier
s_add_u32 s48, s27, 162013184
buffer_load_dwordx4 v[56:59], v0, s[40:43], s48 offen sc1
buffer_load_dwordx4 v[60:63], v0, s[40:43], s48 offen offset:1024 sc1
buffer_load_dwordx4 v[64:67], v0, s[40:43], s48 offen offset:2048 sc1
buffer_load_dwordx4 v[68:71], v0, s[40:43], s48 offen offset:3072 sc1
s_add_u32 s48, s48, 4096
buffer_load_dwordx4 v[72:75], v0, s[40:43], s48 offen sc1
buffer_load_dwordx4 v[76:79], v0, s[40:43], s48 offen offset:1024 sc1
buffer_load_dwordx4 v[80:83], v0, s[40:43], s48 offen offset:2048 sc1
buffer_load_dwordx4 v[84:87], v0, s[40:43], s48 offen offset:3072 sc1
s_add_u32 s48, s48, 4096
buffer_load_dwordx4 v[88:91], v0, s[40:43], s48 offen sc1
buffer_load_dwordx4 v[92:95], v0, s[40:43], s48 offen offset:1024 sc1
buffer_load_dwordx4 v[96:99], v0, s[40:43], s48 offen offset:2048 sc1
buffer_load_dwordx4 v[100:103], v0, s[40:43], s48 offen offset:3072 sc1
s_add_u32 s48, s48, 4096
buffer_load_dwordx4 v[104:107], v0, s[40:43], s48 offen sc1
buffer_load_dwordx4 v[108:111], v0, s[40:43], s48 offen offset:1024 sc1
buffer_load_dwordx4 v[112:115], v0, s[40:43], s48 offen offset:2048 sc1
s_add_u32 s48, s28, 149495808
s_add_u32 s49, s48, 4096
buffer_load_dwordx4 v[16:19], v0, s[40:43], s48 offen sc1
buffer_load_dwordx4 v[20:23], v0, s[40:43], s48 offen offset:1024 sc1
buffer_load_dwordx4 v[24:27], v0, s[40:43], s48 offen offset:2048 sc1
buffer_load_dwordx4 v[28:31], v0, s[40:43], s48 offen offset:3072 sc1
buffer_load_dwordx4 v[32:35], v0, s[40:43], s49 offen sc1
s_add_u32 s48, s28, 149500928
s_add_u32 s49, s48, 4096
buffer_load_dwordx4 v[36:39], v0, s[40:43], s48 offen sc1
buffer_load_dwordx4 v[40:43], v0, s[40:43], s48 offen offset:1024 sc1
buffer_load_dwordx4 v[44:47], v0, s[40:43], s48 offen offset:2048 sc1
buffer_load_dwordx4 v[48:51], v0, s[40:43], s48 offen offset:3072 sc1
buffer_load_dwordx4 v[52:55], v0, s[40:43], s49 offen sc1
s_waitcnt vmcnt(24)
ds_write_b128 v6, v[56:59] offset:0
s_waitcnt vmcnt(23)
ds_write_b128 v6, v[60:63] offset:1024
s_waitcnt vmcnt(22)
ds_write_b128 v6, v[64:67] offset:2048
s_waitcnt vmcnt(21)
ds_write_b128 v6, v[68:71] offset:3072
s_waitcnt vmcnt(20)
ds_write_b128 v6, v[72:75] offset:4096
s_waitcnt vmcnt(19)
ds_write_b128 v6, v[76:79] offset:5120
s_waitcnt vmcnt(18)
ds_write_b128 v6, v[80:83] offset:6144
s_waitcnt vmcnt(17)
ds_write_b128 v6, v[84:87] offset:7168
s_waitcnt vmcnt(16)
ds_write_b128 v6, v[88:91] offset:8192
s_waitcnt vmcnt(15)
ds_write_b128 v6, v[92:95] offset:9216
s_waitcnt vmcnt(14)
ds_write_b128 v6, v[96:99] offset:10240
s_waitcnt vmcnt(13)
ds_write_b128 v6, v[100:103] offset:11264
s_waitcnt vmcnt(12)
ds_write_b128 v6, v[104:107] offset:12288
s_waitcnt vmcnt(11)
ds_write_b128 v6, v[108:111] offset:13312
s_waitcnt vmcnt(10)
ds_write_b128 v6, v[112:115] offset:14336
v_mov_b32 v6, v7
s_waitcnt lgkmcnt(0)
ds_read_b128 v[80:83], v7
v_add_u32 v7, v7, v3
ds_read_b128 v[84:87], v7
v_add_u32 v7, v7, v3
ds_read_b128 v[88:91], v7
v_add_u32 v7, v7, v3
ds_read_b128 v[92:95], v7
v_add_u32 v7, v7, v3
ds_read_b128 v[96:99], v7
v_add_u32 v7, v7, v3
s_waitcnt vmcnt(9) lgkmcnt(4)
v_mfma_f32_16x16x32_bf16 v[56:59], v[80:83], v[16:19], 0
ds_read_b128 v[80:83], v7
v_add_u32 v7, v7, v3
s_waitcnt vmcnt(8) lgkmcnt(4)
v_mfma_f32_16x16x32_bf16 v[56:59], v[84:87], v[20:23], v[56:59]
ds_read_b128 v[84:87], v7
v_add_u32 v7, v7, v3
s_waitcnt vmcnt(7) lgkmcnt(4)
v_mfma_f32_16x16x32_bf16 v[56:59], v[88:91], v[24:27], v[56:59]
ds_read_b128 v[88:91], v7
v_add_u32 v7, v7, v3
s_waitcnt vmcnt(6) lgkmcnt(4)
v_mfma_f32_16x16x32_bf16 v[56:59], v[92:95], v[28:31], v[56:59]
ds_read_b128 v[92:95], v7
v_add_u32 v7, v7, v3
s_waitcnt vmcnt(5) lgkmcnt(4)
v_mfma_f32_16x16x32_bf16 v[56:59], v[96:99], v[32:35], v[56:59]
ds_read_b128 v[96:99], v7
v_add_u32 v7, v7, v3
s_waitcnt lgkmcnt(4)
v_mfma_f32_16x16x32_bf16 v[60:63], v[80:83], v[16:19], 0
ds_read_b128 v[80:83], v7
v_add_u32 v7, v7, v3
s_waitcnt lgkmcnt(4)
v_mfma_f32_16x16x32_bf16 v[60:63], v[84:87], v[20:23], v[60:63]
ds_read_b128 v[84:87], v7
v_add_u32 v7, v7, v3
s_waitcnt lgkmcnt(4)
v_mfma_f32_16x16x32_bf16 v[60:63], v[88:91], v[24:27], v[60:63]
ds_read_b128 v[88:91], v7
v_add_u32 v7, v7, v3
s_waitcnt lgkmcnt(4)
v_mfma_f32_16x16x32_bf16 v[60:63], v[92:95], v[28:31], v[60:63]
ds_read_b128 v[92:95], v7
v_add_u32 v7, v7, v3
s_waitcnt lgkmcnt(4)
v_mfma_f32_16x16x32_bf16 v[60:63], v[96:99], v[32:35], v[60:63]
ds_read_b128 v[96:99], v7
v_add_u32 v7, v7, v3
s_waitcnt lgkmcnt(4)
v_mfma_f32_16x16x32_bf16 v[64:67], v[80:83], v[16:19], 0
ds_read_b128 v[80:83], v7
v_add_u32 v7, v7, v3
s_waitcnt lgkmcnt(4)
v_mfma_f32_16x16x32_bf16 v[64:67], v[84:87], v[20:23], v[64:67]
ds_read_b128 v[84:87], v7
v_add_u32 v7, v7, v3
s_waitcnt lgkmcnt(4)
v_mfma_f32_16x16x32_bf16 v[64:67], v[88:91], v[24:27], v[64:67]
ds_read_b128 v[88:91], v7
v_add_u32 v7, v7, v3
s_waitcnt lgkmcnt(4)
v_mfma_f32_16x16x32_bf16 v[64:67], v[92:95], v[28:31], v[64:67]
ds_read_b128 v[92:95], v7
v_add_u32 v7, v7, v3
s_waitcnt lgkmcnt(4)
v_mfma_f32_16x16x32_bf16 v[64:67], v[96:99], v[32:35], v[64:67]
ds_read_b128 v[96:99], v7
v_add_u32 v7, v7, v3
s_waitcnt lgkmcnt(4)
v_mfma_f32_16x16x32_bf16 v[68:71], v[80:83], v[16:19], 0
ds_read_b128 v[80:83], v7
v_add_u32 v7, v7, v3
s_waitcnt lgkmcnt(4)
v_mfma_f32_16x16x32_bf16 v[68:71], v[84:87], v[20:23], v[68:71]
ds_read_b128 v[84:87], v7
v_add_u32 v7, v7, v3
s_waitcnt lgkmcnt(4)
v_mfma_f32_16x16x32_bf16 v[68:71], v[88:91], v[24:27], v[68:71]
ds_read_b128 v[88:91], v7
v_add_u32 v7, v7, v3
s_waitcnt lgkmcnt(4)
v_mfma_f32_16x16x32_bf16 v[68:71], v[92:95], v[28:31], v[68:71]
ds_read_b128 v[92:95], v7
v_add_u32 v7, v7, v3
s_waitcnt lgkmcnt(4)
v_mfma_f32_16x16x32_bf16 v[68:71], v[96:99], v[32:35], v[68:71]
ds_read_b128 v[96:99], v7
v_add_u32 v7, v7, v3
s_waitcnt lgkmcnt(4)
v_mfma_f32_16x16x32_bf16 v[72:75], v[80:83], v[16:19], 0
ds_read_b128 v[80:83], v7
v_add_u32 v7, v7, v3
s_waitcnt lgkmcnt(4)
v_mfma_f32_16x16x32_bf16 v[72:75], v[84:87], v[20:23], v[72:75]
ds_read_b128 v[84:87], v7
v_add_u32 v7, v7, v3
s_waitcnt lgkmcnt(4)
v_mfma_f32_16x16x32_bf16 v[72:75], v[88:91], v[24:27], v[72:75]
ds_read_b128 v[88:91], v7
v_add_u32 v7, v7, v3
s_waitcnt lgkmcnt(4)
v_mfma_f32_16x16x32_bf16 v[72:75], v[92:95], v[28:31], v[72:75]
ds_read_b128 v[92:95], v7
v_add_u32 v7, v7, v3
s_waitcnt lgkmcnt(4)
v_mfma_f32_16x16x32_bf16 v[72:75], v[96:99], v[32:35], v[72:75]
ds_read_b128 v[96:99], v7
v_add_u32 v7, v7, v3
s_waitcnt lgkmcnt(4)
v_mfma_f32_16x16x32_bf16 v[76:79], v[80:83], v[16:19], 0
ds_read_b128 v[80:83], v7
v_add_u32 v7, v7, v3
s_waitcnt lgkmcnt(4)
v_mfma_f32_16x16x32_bf16 v[76:79], v[84:87], v[20:23], v[76:79]
ds_read_b128 v[84:87], v7
v_add_u32 v7, v7, v3
s_waitcnt lgkmcnt(4)
v_mfma_f32_16x16x32_bf16 v[76:79], v[88:91], v[24:27], v[76:79]
ds_read_b128 v[88:91], v7
v_add_u32 v7, v7, v3
s_waitcnt lgkmcnt(4)
v_mfma_f32_16x16x32_bf16 v[76:79], v[92:95], v[28:31], v[76:79]
ds_read_b128 v[92:95], v7
v_add_u32 v7, v7, v3
s_waitcnt lgkmcnt(4)
v_mfma_f32_16x16x32_bf16 v[76:79], v[96:99], v[32:35], v[76:79]
)"
R"(ds_read_b128 v[96:99], v7
v_add_u32 v7, v7, v3
s_add_u32 s48, s28, 149536768
s_add_u32 s49, s48, 4096
buffer_load_dwordx4 v[16:19], v0, s[40:43], s48 offen sc1
buffer_load_dwordx4 v[20:23], v0, s[40:43], s48 offen offset:1024 sc1
buffer_load_dwordx4 v[24:27], v0, s[40:43], s48 offen offset:2048 sc1
buffer_load_dwordx4 v[28:31], v0, s[40:43], s48 offen offset:3072 sc1
buffer_load_dwordx4 v[32:35], v0, s[40:43], s49 offen sc1
v_add_f32 v76, 0x358637bd, v76
v_rcp_f32 v76, v76
s_nop 0
v_pk_mul_f32 v[56:57], v[56:57], v[76:77] op_sel_hi:[1,0]
v_pk_mul_f32 v[58:59], v[58:59], v[76:77] op_sel_hi:[1,0]
v_pk_mul_f32 v[60:61], v[60:61], v[76:77] op_sel_hi:[1,0]
v_pk_mul_f32 v[62:63], v[62:63], v[76:77] op_sel_hi:[1,0]
v_pk_mul_f32 v[64:65], v[64:65], v[76:77] op_sel_hi:[1,0]
v_pk_mul_f32 v[66:67], v[66:67], v[76:77] op_sel_hi:[1,0]
v_pk_mul_f32 v[68:69], v[68:69], v[76:77] op_sel_hi:[1,0]
v_pk_mul_f32 v[70:71], v[70:71], v[76:77] op_sel_hi:[1,0]
v_pk_mul_f32 v[72:73], v[72:73], v[76:77] op_sel_hi:[1,0]
v_pk_mul_f32 v[74:75], v[74:75], v[76:77] op_sel_hi:[1,0]
v_cvt_pk_bf16_f32 v100, v56, v57
v_cvt_pk_bf16_f32 v101, v58, v59
v_cvt_pk_bf16_f32 v102, v60, v61
v_cvt_pk_bf16_f32 v103, v62, v63
v_cvt_pk_bf16_f32 v104, v64, v65
v_cvt_pk_bf16_f32 v105, v66, v67
v_cvt_pk_bf16_f32 v106, v68, v69
v_cvt_pk_bf16_f32 v107, v70, v71
v_cvt_pk_bf16_f32 v108, v72, v73
v_cvt_pk_bf16_f32 v109, v74, v75
s_waitcnt vmcnt(9) lgkmcnt(4)
v_mfma_f32_16x16x32_bf16 v[56:59], v[80:83], v[36:39], 0
ds_read_b128 v[80:83], v7
v_add_u32 v7, v7, v3
s_waitcnt vmcnt(8) lgkmcnt(4)
v_mfma_f32_16x16x32_bf16 v[56:59], v[84:87], v[40:43], v[56:59]
ds_read_b128 v[84:87], v7
v_add_u32 v7, v7, v3
s_waitcnt vmcnt(7) lgkmcnt(4)
v_mfma_f32_16x16x32_bf16 v[56:59], v[88:91], v[44:47], v[56:59]
ds_read_b128 v[88:91], v7
v_add_u32 v7, v7, v3
s_waitcnt vmcnt(6) lgkmcnt(4)
v_mfma_f32_16x16x32_bf16 v[56:59], v[92:95], v[48:51], v[56:59]
ds_read_b128 v[92:95], v7
v_add_u32 v7, v7, v3
s_waitcnt vmcnt(5) lgkmcnt(4)
v_mfma_f32_16x16x32_bf16 v[56:59], v[96:99], v[52:55], v[56:59]
ds_read_b128 v[96:99], v7
v_add_u32 v7, v7, v3
s_waitcnt lgkmcnt(4)
v_mfma_f32_16x16x32_bf16 v[60:63], v[80:83], v[36:39], 0
ds_read_b128 v[80:83], v7
v_add_u32 v7, v7, v3
s_waitcnt lgkmcnt(4)
v_mfma_f32_16x16x32_bf16 v[60:63], v[84:87], v[40:43], v[60:63]
ds_read_b128 v[84:87], v7
v_add_u32 v7, v7, v3
s_waitcnt lgkmcnt(4)
v_mfma_f32_16x16x32_bf16 v[60:63], v[88:91], v[44:47], v[60:63]
ds_read_b128 v[88:91], v7
v_add_u32 v7, v7, v3
s_waitcnt lgkmcnt(4)
v_mfma_f32_16x16x32_bf16 v[60:63], v[92:95], v[48:51], v[60:63]
ds_read_b128 v[92:95], v7
v_add_u32 v7, v7, v3
s_waitcnt lgkmcnt(4)
v_mfma_f32_16x16x32_bf16 v[60:63], v[96:99], v[52:55], v[60:63]
ds_read_b128 v[96:99], v7
v_add_u32 v7, v7, v3
s_waitcnt lgkmcnt(4)
v_mfma_f32_16x16x32_bf16 v[64:67], v[80:83], v[36:39], 0
ds_read_b128 v[80:83], v7
v_add_u32 v7, v7, v3
s_waitcnt lgkmcnt(4)
v_mfma_f32_16x16x32_bf16 v[64:67], v[84:87], v[40:43], v[64:67]
ds_read_b128 v[84:87], v7
v_add_u32 v7, v7, v3
s_waitcnt lgkmcnt(4)
v_mfma_f32_16x16x32_bf16 v[64:67], v[88:91], v[44:47], v[64:67]
ds_read_b128 v[88:91], v7
v_add_u32 v7, v7, v3
s_waitcnt lgkmcnt(4)
v_mfma_f32_16x16x32_bf16 v[64:67], v[92:95], v[48:51], v[64:67]
ds_read_b128 v[92:95], v7
v_add_u32 v7, v7, v3
s_waitcnt lgkmcnt(4)
v_mfma_f32_16x16x32_bf16 v[64:67], v[96:99], v[52:55], v[64:67]
ds_read_b128 v[96:99], v7
v_add_u32 v7, v7, v3
s_waitcnt lgkmcnt(4)
v_mfma_f32_16x16x32_bf16 v[68:71], v[80:83], v[36:39], 0
ds_read_b128 v[80:83], v7
v_add_u32 v7, v7, v3
s_waitcnt lgkmcnt(4)
v_mfma_f32_16x16x32_bf16 v[68:71], v[84:87], v[40:43], v[68:71]
ds_read_b128 v[84:87], v7
v_add_u32 v7, v7, v3
s_waitcnt lgkmcnt(4)
v_mfma_f32_16x16x32_bf16 v[68:71], v[88:91], v[44:47], v[68:71]
ds_read_b128 v[88:91], v7
v_add_u32 v7, v7, v3
s_waitcnt lgkmcnt(4)
v_mfma_f32_16x16x32_bf16 v[68:71], v[92:95], v[48:51], v[68:71]
ds_read_b128 v[92:95], v7
v_add_u32 v7, v7, v3
s_waitcnt lgkmcnt(4)
v_mfma_f32_16x16x32_bf16 v[68:71], v[96:99], v[52:55], v[68:71]
ds_read_b128 v[96:99], v7
v_add_u32 v7, v7, v3
s_waitcnt lgkmcnt(4)
v_mfma_f32_16x16x32_bf16 v[72:75], v[80:83], v[36:39], 0
ds_read_b128 v[80:83], v7
v_add_u32 v7, v7, v3
s_waitcnt lgkmcnt(4)
v_mfma_f32_16x16x32_bf16 v[72:75], v[84:87], v[40:43], v[72:75]
ds_read_b128 v[84:87], v7
v_add_u32 v7, v7, v3
s_waitcnt lgkmcnt(4)
v_mfma_f32_16x16x32_bf16 v[72:75], v[88:91], v[44:47], v[72:75]
ds_read_b128 v[88:91], v7
v_add_u32 v7, v7, v3
s_waitcnt lgkmcnt(4)
v_mfma_f32_16x16x32_bf16 v[72:75], v[92:95], v[48:51], v[72:75]
ds_read_b128 v[92:95], v7
v_add_u32 v7, v7, v3
s_waitcnt lgkmcnt(4)
v_mfma_f32_16x16x32_bf16 v[72:75], v[96:99], v[52:55], v[72:75]
ds_read_b128 v[96:99], v7
v_mov_b32 v7, v6
s_waitcnt lgkmcnt(4)
v_mfma_f32_16x16x32_bf16 v[76:79], v[80:83], v[36:39], 0
ds_read_b128 v[80:83], v7
v_add_u32 v7, v7, v3
s_waitcnt lgkmcnt(4)
v_mfma_f32_16x16x32_bf16 v[76:79], v[84:87], v[40:43], v[76:79]
ds_read_b128 v[84:87], v7
v_add_u32 v7, v7, v3
s_waitcnt lgkmcnt(4)
v_mfma_f32_16x16x32_bf16 v[76:79], v[88:91], v[44:47], v[76:79]
ds_read_b128 v[88:91], v7
v_add_u32 v7, v7, v3
s_waitcnt lgkmcnt(4)
v_mfma_f32_16x16x32_bf16 v[76:79], v[92:95], v[48:51], v[76:79]
ds_read_b128 v[92:95], v7
v_add_u32 v7, v7, v3
s_waitcnt lgkmcnt(4)
v_mfma_f32_16x16x32_bf16 v[76:79], v[96:99], v[52:55], v[76:79]
ds_read_b128 v[96:99], v7
v_add_u32 v7, v7, v3
s_add_u32 s48, s28, 149541888
s_add_u32 s49, s48, 4096
buffer_load_dwordx4 v[36:39], v0, s[40:43], s48 offen sc1
buffer_load_dwordx4 v[40:43], v0, s[40:43], s48 offen offset:1024 sc1
buffer_load_dwordx4 v[44:47], v0, s[40:43], s48 offen offset:2048 sc1
buffer_load_dwordx4 v[48:51], v0, s[40:43], s48 offen offset:3072 sc1
buffer_load_dwordx4 v[52:55], v0, s[40:43], s49 offen sc1
v_add_f32 v76, 0x358637bd, v76
v_rcp_f32 v76, v76
s_nop 0
v_pk_mul_f32 v[56:57], v[56:57], v[76:77] op_sel_hi:[1,0]
v_pk_mul_f32 v[58:59], v[58:59], v[76:77] op_sel_hi:[1,0]
v_pk_mul_f32 v[60:61], v[60:61], v[76:77] op_sel_hi:[1,0]
v_pk_mul_f32 v[62:63], v[62:63], v[76:77] op_sel_hi:[1,0]
v_pk_mul_f32 v[64:65], v[64:65], v[76:77] op_sel_hi:[1,0]
v_pk_mul_f32 v[66:67], v[66:67], v[76:77] op_sel_hi:[1,0]
v_pk_mul_f32 v[68:69], v[68:69], v[76:77] op_sel_hi:[1,0]
v_pk_mul_f32 v[70:71], v[70:71], v[76:77] op_sel_hi:[1,0]
v_pk_mul_f32 v[72:73], v[72:73], v[76:77] op_sel_hi:[1,0]
v_pk_mul_f32 v[74:75], v[74:75], v[76:77] op_sel_hi:[1,0]
v_cvt_pk_bf16_f32 v110, v56, v57
v_cvt_pk_bf16_f32 v111, v58, v59
v_cvt_pk_bf16_f32 v112, v60, v61
v_cvt_pk_bf16_f32 v113, v62, v63
v_cvt_pk_bf16_f32 v114, v64, v65
v_cvt_pk_bf16_f32 v115, v66, v67
v_cvt_pk_bf16_f32 v116, v68, v69
v_cvt_pk_bf16_f32 v117, v70, v71
v_cvt_pk_bf16_f32 v118, v72, v73
v_cvt_pk_bf16_f32 v119, v74, v75
ds_write_b128 v9, v[100:103] offset:0
ds_write_b128 v9, v[104:107] offset:1024
ds_write_b128 v9, v[108:111] offset:2048
ds_write_b128 v9, v[112:115] offset:3072
ds_write_b128 v9, v[116:119] offset:4096
s_waitcnt vmcnt(9) lgkmcnt(9)
v_mfma_f32_16x16x32_bf16 v[56:59], v[80:83], v[16:19], 0
ds_read_b128 v[80:83], v7
v_add_u32 v7, v7, v3
s_waitcnt vmcnt(8) lgkmcnt(9)
v_mfma_f32_16x16x32_bf16 v[56:59], v[84:87], v[20:23], v[56:59]
ds_read_b128 v[84:87], v7
v_add_u32 v7, v7, v3
s_waitcnt vmcnt(7) lgkmcnt(9)
v_mfma_f32_16x16x32_bf16 v[56:59], v[88:91], v[24:27], v[56:59]
ds_read_b128 v[88:91], v7
v_add_u32 v7, v7, v3
s_waitcnt vmcnt(6) lgkmcnt(9)
)"
R"(v_mfma_f32_16x16x32_bf16 v[56:59], v[92:95], v[28:31], v[56:59]
ds_read_b128 v[92:95], v7
v_add_u32 v7, v7, v3
s_waitcnt vmcnt(5) lgkmcnt(9)
v_mfma_f32_16x16x32_bf16 v[56:59], v[96:99], v[32:35], v[56:59]
ds_read_b128 v[96:99], v7
v_add_u32 v7, v7, v3
s_waitcnt lgkmcnt(4)
v_mfma_f32_16x16x32_bf16 v[60:63], v[80:83], v[16:19], 0
ds_read_b128 v[80:83], v7
v_add_u32 v7, v7, v3
s_waitcnt lgkmcnt(4)
v_mfma_f32_16x16x32_bf16 v[60:63], v[84:87], v[20:23], v[60:63]
ds_read_b128 v[84:87], v7
v_add_u32 v7, v7, v3
s_waitcnt lgkmcnt(4)
v_mfma_f32_16x16x32_bf16 v[60:63], v[88:91], v[24:27], v[60:63]
ds_read_b128 v[88:91], v7
v_add_u32 v7, v7, v3
s_waitcnt lgkmcnt(4)
v_mfma_f32_16x16x32_bf16 v[60:63], v[92:95], v[28:31], v[60:63]
ds_read_b128 v[92:95], v7
v_add_u32 v7, v7, v3
s_waitcnt lgkmcnt(4)
v_mfma_f32_16x16x32_bf16 v[60:63], v[96:99], v[32:35], v[60:63]
ds_read_b128 v[96:99], v7
v_add_u32 v7, v7, v3
s_waitcnt lgkmcnt(4)
v_mfma_f32_16x16x32_bf16 v[64:67], v[80:83], v[16:19], 0
ds_read_b128 v[80:83], v7
v_add_u32 v7, v7, v3
s_waitcnt lgkmcnt(4)
v_mfma_f32_16x16x32_bf16 v[64:67], v[84:87], v[20:23], v[64:67]
ds_read_b128 v[84:87], v7
v_add_u32 v7, v7, v3
s_waitcnt lgkmcnt(4)
v_mfma_f32_16x16x32_bf16 v[64:67], v[88:91], v[24:27], v[64:67]
ds_read_b128 v[88:91], v7
v_add_u32 v7, v7, v3
s_waitcnt lgkmcnt(4)
v_mfma_f32_16x16x32_bf16 v[64:67], v[92:95], v[28:31], v[64:67]
ds_read_b128 v[92:95], v7
v_add_u32 v7, v7, v3
s_waitcnt lgkmcnt(4)
v_mfma_f32_16x16x32_bf16 v[64:67], v[96:99], v[32:35], v[64:67]
ds_read_b128 v[96:99], v7
v_add_u32 v7, v7, v3
s_waitcnt lgkmcnt(4)
v_mfma_f32_16x16x32_bf16 v[68:71], v[80:83], v[16:19], 0
ds_read_b128 v[80:83], v7
v_add_u32 v7, v7, v3
s_waitcnt lgkmcnt(4)
v_mfma_f32_16x16x32_bf16 v[68:71], v[84:87], v[20:23], v[68:71]
ds_read_b128 v[84:87], v7
v_add_u32 v7, v7, v3
s_waitcnt lgkmcnt(4)
v_mfma_f32_16x16x32_bf16 v[68:71], v[88:91], v[24:27], v[68:71]
ds_read_b128 v[88:91], v7
v_add_u32 v7, v7, v3
s_waitcnt lgkmcnt(4)
v_mfma_f32_16x16x32_bf16 v[68:71], v[92:95], v[28:31], v[68:71]
ds_read_b128 v[92:95], v7
v_add_u32 v7, v7, v3
s_waitcnt lgkmcnt(4)
v_mfma_f32_16x16x32_bf16 v[68:71], v[96:99], v[32:35], v[68:71]
ds_read_b128 v[96:99], v7
v_add_u32 v7, v7, v3
s_waitcnt lgkmcnt(4)
v_mfma_f32_16x16x32_bf16 v[72:75], v[80:83], v[16:19], 0
ds_read_b128 v[80:83], v7
v_add_u32 v7, v7, v3
s_waitcnt lgkmcnt(4)
v_mfma_f32_16x16x32_bf16 v[72:75], v[84:87], v[20:23], v[72:75]
ds_read_b128 v[84:87], v7
v_add_u32 v7, v7, v3
s_waitcnt lgkmcnt(4)
v_mfma_f32_16x16x32_bf16 v[72:75], v[88:91], v[24:27], v[72:75]
ds_read_b128 v[88:91], v7
v_add_u32 v7, v7, v3
s_waitcnt lgkmcnt(4)
v_mfma_f32_16x16x32_bf16 v[72:75], v[92:95], v[28:31], v[72:75]
ds_read_b128 v[92:95], v7
v_add_u32 v7, v7, v3
s_waitcnt lgkmcnt(4)
v_mfma_f32_16x16x32_bf16 v[72:75], v[96:99], v[32:35], v[72:75]
ds_read_b128 v[96:99], v7
v_add_u32 v7, v7, v3
s_waitcnt lgkmcnt(4)
v_mfma_f32_16x16x32_bf16 v[76:79], v[80:83], v[16:19], 0
ds_read_b128 v[80:83], v7
v_add_u32 v7, v7, v3
s_waitcnt lgkmcnt(4)
v_mfma_f32_16x16x32_bf16 v[76:79], v[84:87], v[20:23], v[76:79]
ds_read_b128 v[84:87], v7
v_add_u32 v7, v7, v3
s_waitcnt lgkmcnt(4)
v_mfma_f32_16x16x32_bf16 v[76:79], v[88:91], v[24:27], v[76:79]
ds_read_b128 v[88:91], v7
v_add_u32 v7, v7, v3
s_waitcnt lgkmcnt(4)
v_mfma_f32_16x16x32_bf16 v[76:79], v[92:95], v[28:31], v[76:79]
ds_read_b128 v[92:95], v7
v_add_u32 v7, v7, v3
s_waitcnt lgkmcnt(4)
v_mfma_f32_16x16x32_bf16 v[76:79], v[96:99], v[32:35], v[76:79]
ds_read_b128 v[96:99], v7
v_add_u32 v7, v7, v3
s_nop 5
v_add_f32 v76, 0x358637bd, v76
v_rcp_f32 v76, v76
s_nop 0
v_pk_mul_f32 v[56:57], v[56:57], v[76:77] op_sel_hi:[1,0]
v_pk_mul_f32 v[58:59], v[58:59], v[76:77] op_sel_hi:[1,0]
v_pk_mul_f32 v[60:61], v[60:61], v[76:77] op_sel_hi:[1,0]
v_pk_mul_f32 v[62:63], v[62:63], v[76:77] op_sel_hi:[1,0]
v_pk_mul_f32 v[64:65], v[64:65], v[76:77] op_sel_hi:[1,0]
v_pk_mul_f32 v[66:67], v[66:67], v[76:77] op_sel_hi:[1,0]
v_pk_mul_f32 v[68:69], v[68:69], v[76:77] op_sel_hi:[1,0]
v_pk_mul_f32 v[70:71], v[70:71], v[76:77] op_sel_hi:[1,0]
v_pk_mul_f32 v[72:73], v[72:73], v[76:77] op_sel_hi:[1,0]
v_pk_mul_f32 v[74:75], v[74:75], v[76:77] op_sel_hi:[1,0]
v_cvt_pk_bf16_f32 v100, v56, v57
v_cvt_pk_bf16_f32 v101, v58, v59
v_cvt_pk_bf16_f32 v102, v60, v61
v_cvt_pk_bf16_f32 v103, v62, v63
v_cvt_pk_bf16_f32 v104, v64, v65
v_cvt_pk_bf16_f32 v105, v66, v67
v_cvt_pk_bf16_f32 v106, v68, v69
v_cvt_pk_bf16_f32 v107, v70, v71
v_cvt_pk_bf16_f32 v108, v72, v73
v_cvt_pk_bf16_f32 v109, v74, v75
s_waitcnt vmcnt(4) lgkmcnt(4)
v_mfma_f32_16x16x32_bf16 v[56:59], v[80:83], v[36:39], 0
ds_read_b128 v[80:83], v7
v_add_u32 v7, v7, v3
s_waitcnt vmcnt(3) lgkmcnt(4)
v_mfma_f32_16x16x32_bf16 v[56:59], v[84:87], v[40:43], v[56:59]
ds_read_b128 v[84:87], v7
v_add_u32 v7, v7, v3
s_waitcnt vmcnt(2) lgkmcnt(4)
v_mfma_f32_16x16x32_bf16 v[56:59], v[88:91], v[44:47], v[56:59]
ds_read_b128 v[88:91], v7
v_add_u32 v7, v7, v3
s_waitcnt vmcnt(1) lgkmcnt(4)
v_mfma_f32_16x16x32_bf16 v[56:59], v[92:95], v[48:51], v[56:59]
ds_read_b128 v[92:95], v7
v_add_u32 v7, v7, v3
s_waitcnt vmcnt(0) lgkmcnt(4)
v_mfma_f32_16x16x32_bf16 v[56:59], v[96:99], v[52:55], v[56:59]
ds_read_b128 v[96:99], v7
v_add_u32 v7, v7, v3
s_waitcnt lgkmcnt(4)
v_mfma_f32_16x16x32_bf16 v[60:63], v[80:83], v[36:39], 0
ds_read_b128 v[80:83], v7
v_add_u32 v7, v7, v3
s_waitcnt lgkmcnt(4)
v_mfma_f32_16x16x32_bf16 v[60:63], v[84:87], v[40:43], v[60:63]
ds_read_b128 v[84:87], v7
v_add_u32 v7, v7, v3
s_waitcnt lgkmcnt(4)
v_mfma_f32_16x16x32_bf16 v[60:63], v[88:91], v[44:47], v[60:63]
ds_read_b128 v[88:91], v7
v_add_u32 v7, v7, v3
s_waitcnt lgkmcnt(4)
v_mfma_f32_16x16x32_bf16 v[60:63], v[92:95], v[48:51], v[60:63]
ds_read_b128 v[92:95], v7
v_add_u32 v7, v7, v3
s_waitcnt lgkmcnt(4)
v_mfma_f32_16x16x32_bf16 v[60:63], v[96:99], v[52:55], v[60:63]
ds_read_b128 v[96:99], v7
v_add_u32 v7, v7, v3
s_waitcnt lgkmcnt(4)
v_mfma_f32_16x16x32_bf16 v[64:67], v[80:83], v[36:39], 0
ds_read_b128 v[80:83], v7
v_add_u32 v7, v7, v3
s_waitcnt lgkmcnt(4)
v_mfma_f32_16x16x32_bf16 v[64:67], v[84:87], v[40:43], v[64:67]
ds_read_b128 v[84:87], v7
v_add_u32 v7, v7, v3
s_waitcnt lgkmcnt(4)
v_mfma_f32_16x16x32_bf16 v[64:67], v[88:91], v[44:47], v[64:67]
ds_read_b128 v[88:91], v7
v_add_u32 v7, v7, v3
s_waitcnt lgkmcnt(4)
v_mfma_f32_16x16x32_bf16 v[64:67], v[92:95], v[48:51], v[64:67]
ds_read_b128 v[92:95], v7
v_add_u32 v7, v7, v3
s_waitcnt lgkmcnt(4)
v_mfma_f32_16x16x32_bf16 v[64:67], v[96:99], v[52:55], v[64:67]
ds_read_b128 v[96:99], v7
v_add_u32 v7, v7, v3
s_waitcnt lgkmcnt(4)
v_mfma_f32_16x16x32_bf16 v[68:71], v[80:83], v[36:39], 0
ds_read_b128 v[80:83], v7
v_add_u32 v7, v7, v3
s_waitcnt lgkmcnt(4)
v_mfma_f32_16x16x32_bf16 v[68:71], v[84:87], v[40:43], v[68:71]
ds_read_b128 v[84:87], v7
v_add_u32 v7, v7, v3
s_waitcnt lgkmcnt(4)
)"
R"(v_mfma_f32_16x16x32_bf16 v[68:71], v[88:91], v[44:47], v[68:71]
ds_read_b128 v[88:91], v7
v_add_u32 v7, v7, v3
s_waitcnt lgkmcnt(4)
v_mfma_f32_16x16x32_bf16 v[68:71], v[92:95], v[48:51], v[68:71]
ds_read_b128 v[92:95], v7
v_add_u32 v7, v7, v3
s_waitcnt lgkmcnt(4)
v_mfma_f32_16x16x32_bf16 v[68:71], v[96:99], v[52:55], v[68:71]
ds_read_b128 v[96:99], v7
v_add_u32 v7, v7, v3
s_waitcnt lgkmcnt(4)
v_mfma_f32_16x16x32_bf16 v[72:75], v[80:83], v[36:39], 0
ds_read_b128 v[80:83], v7
v_add_u32 v7, v7, v3
s_waitcnt lgkmcnt(4)
v_mfma_f32_16x16x32_bf16 v[72:75], v[84:87], v[40:43], v[72:75]
ds_read_b128 v[84:87], v7
v_add_u32 v7, v7, v3
s_waitcnt lgkmcnt(4)
v_mfma_f32_16x16x32_bf16 v[72:75], v[88:91], v[44:47], v[72:75]
ds_read_b128 v[88:91], v7
v_add_u32 v7, v7, v3
s_waitcnt lgkmcnt(4)
v_mfma_f32_16x16x32_bf16 v[72:75], v[92:95], v[48:51], v[72:75]
ds_read_b128 v[92:95], v7
v_add_u32 v7, v7, v3
s_waitcnt lgkmcnt(4)
v_mfma_f32_16x16x32_bf16 v[72:75], v[96:99], v[52:55], v[72:75]
ds_read_b128 v[96:99], v7
v_add_u32 v7, v7, v3
s_waitcnt lgkmcnt(4)
v_mfma_f32_16x16x32_bf16 v[76:79], v[80:83], v[36:39], 0
s_waitcnt lgkmcnt(3)
v_mfma_f32_16x16x32_bf16 v[76:79], v[84:87], v[40:43], v[76:79]
s_waitcnt lgkmcnt(2)
v_mfma_f32_16x16x32_bf16 v[76:79], v[88:91], v[44:47], v[76:79]
s_waitcnt lgkmcnt(1)
v_mfma_f32_16x16x32_bf16 v[76:79], v[92:95], v[48:51], v[76:79]
s_waitcnt lgkmcnt(0)
v_mfma_f32_16x16x32_bf16 v[76:79], v[96:99], v[52:55], v[76:79]
s_nop 7
v_add_f32 v76, 0x358637bd, v76
v_rcp_f32 v76, v76
s_nop 0
v_pk_mul_f32 v[56:57], v[56:57], v[76:77] op_sel_hi:[1,0]
v_pk_mul_f32 v[58:59], v[58:59], v[76:77] op_sel_hi:[1,0]
v_pk_mul_f32 v[60:61], v[60:61], v[76:77] op_sel_hi:[1,0]
v_pk_mul_f32 v[62:63], v[62:63], v[76:77] op_sel_hi:[1,0]
v_pk_mul_f32 v[64:65], v[64:65], v[76:77] op_sel_hi:[1,0]
v_pk_mul_f32 v[66:67], v[66:67], v[76:77] op_sel_hi:[1,0]
v_pk_mul_f32 v[68:69], v[68:69], v[76:77] op_sel_hi:[1,0]
v_pk_mul_f32 v[70:71], v[70:71], v[76:77] op_sel_hi:[1,0]
v_pk_mul_f32 v[72:73], v[72:73], v[76:77] op_sel_hi:[1,0]
v_pk_mul_f32 v[74:75], v[74:75], v[76:77] op_sel_hi:[1,0]
v_cvt_pk_bf16_f32 v110, v56, v57
v_cvt_pk_bf16_f32 v111, v58, v59
v_cvt_pk_bf16_f32 v112, v60, v61
v_cvt_pk_bf16_f32 v113, v62, v63
v_cvt_pk_bf16_f32 v114, v64, v65
v_cvt_pk_bf16_f32 v115, v66, v67
v_cvt_pk_bf16_f32 v116, v68, v69
v_cvt_pk_bf16_f32 v117, v70, v71
v_cvt_pk_bf16_f32 v118, v72, v73
v_cvt_pk_bf16_f32 v119, v74, v75
ds_write_b128 v9, v[100:103] offset:20480
ds_write_b128 v9, v[104:107] offset:21504
ds_write_b128 v9, v[108:111] offset:22528
ds_write_b128 v9, v[112:115] offset:23552
ds_write_b128 v9, v[116:119] offset:24576
s_mul_i32 s48, s30, 30720
s_add_u32 s48, s48, 61696
v_add_u32 v9, s48, v0
s_mul_i32 s48, s30, -10240
s_add_u32 s48, s48, 82176
v_add_u32 v7, s48, v0
v_add_u32 v6, s61, v0
s_waitcnt lgkmcnt(0)
s_barrier
s_cmp_eq_u32 s21, 0
s_cbranch_scc1 hpb2_mid_%=
ds_read_b128 v[96:99], v9 offset:0
ds_read_b128 v[100:103], v7 offset:0
ds_read_b128 v[104:107], v9 offset:1024
ds_read_b128 v[108:111], v7 offset:1024
ds_read_b128 v[112:115], v9 offset:2048
ds_read_b128 v[116:119], v7 offset:2048
s_waitcnt lgkmcnt(5)
v_mfma_f32_16x16x32_bf16 v[16:19], v[128:131], v[96:99], 0
s_waitcnt lgkmcnt(4)
v_mfma_f32_16x16x32_bf16 v[56:59], v[128:131], v[100:103], 0
ds_read_b128 v[120:123], v9 offset:3072
v_mfma_f32_16x16x32_bf16 v[20:23], v[132:135], v[96:99], 0
v_mfma_f32_16x16x32_bf16 v[60:63], v[132:135], v[100:103], 0
v_mfma_f32_16x16x32_bf16 v[24:27], v[136:139], v[96:99], 0
v_mfma_f32_16x16x32_bf16 v[64:67], v[136:139], v[100:103], 0
v_mfma_f32_16x16x32_bf16 v[28:31], v[140:143], v[96:99], 0
v_mfma_f32_16x16x32_bf16 v[68:71], v[140:143], v[100:103], 0
v_mfma_f32_16x16x32_bf16 v[32:35], v[144:147], v[96:99], 0
v_mfma_f32_16x16x32_bf16 v[72:75], v[144:147], v[100:103], 0
v_mfma_f32_16x16x32_bf16 v[36:39], v[148:151], v[96:99], 0
v_mfma_f32_16x16x32_bf16 v[76:79], v[148:151], v[100:103], 0
ds_read_b128 v[124:127], v7 offset:3072
v_mfma_f32_16x16x32_bf16 v[40:43], v[152:155], v[96:99], 0
v_mfma_f32_16x16x32_bf16 v[80:83], v[152:155], v[100:103], 0
v_mfma_f32_16x16x32_bf16 v[44:47], v[156:159], v[96:99], 0
v_mfma_f32_16x16x32_bf16 v[84:87], v[156:159], v[100:103], 0
v_mfma_f32_16x16x32_bf16 v[48:51], v[160:163], v[96:99], 0
v_mfma_f32_16x16x32_bf16 v[88:91], v[160:163], v[100:103], 0
v_mfma_f32_16x16x32_bf16 v[52:55], v[164:167], v[96:99], 0
v_mfma_f32_16x16x32_bf16 v[92:95], v[164:167], v[100:103], 0
s_waitcnt lgkmcnt(5)
v_mfma_f32_16x16x32_bf16 v[16:19], v[168:171], v[104:107], v[16:19]
s_waitcnt lgkmcnt(4)
v_mfma_f32_16x16x32_bf16 v[56:59], v[168:171], v[108:111], v[56:59]
ds_read_b128 v[96:99], v9 offset:4096
v_mfma_f32_16x16x32_bf16 v[20:23], v[172:175], v[104:107], v[20:23]
v_mfma_f32_16x16x32_bf16 v[60:63], v[172:175], v[108:111], v[60:63]
v_mfma_f32_16x16x32_bf16 v[24:27], v[176:179], v[104:107], v[24:27]
v_mfma_f32_16x16x32_bf16 v[64:67], v[176:179], v[108:111], v[64:67]
v_mfma_f32_16x16x32_bf16 v[28:31], v[180:183], v[104:107], v[28:31]
v_mfma_f32_16x16x32_bf16 v[68:71], v[180:183], v[108:111], v[68:71]
v_mfma_f32_16x16x32_bf16 v[32:35], v[184:187], v[104:107], v[32:35]
v_mfma_f32_16x16x32_bf16 v[72:75], v[184:187], v[108:111], v[72:75]
v_mfma_f32_16x16x32_bf16 v[36:39], v[188:191], v[104:107], v[36:39]
v_mfma_f32_16x16x32_bf16 v[76:79], v[188:191], v[108:111], v[76:79]
ds_read_b128 v[100:103], v7 offset:4096
v_mfma_f32_16x16x32_bf16 v[40:43], v[192:195], v[104:107], v[40:43]
v_mfma_f32_16x16x32_bf16 v[80:83], v[192:195], v[108:111], v[80:83]
v_mfma_f32_16x16x32_bf16 v[44:47], v[196:199], v[104:107], v[44:47]
v_mfma_f32_16x16x32_bf16 v[84:87], v[196:199], v[108:111], v[84:87]
v_mfma_f32_16x16x32_bf16 v[48:51], v[200:203], v[104:107], v[48:51]
v_mfma_f32_16x16x32_bf16 v[88:91], v[200:203], v[108:111], v[88:91]
v_mfma_f32_16x16x32_bf16 v[52:55], v[204:207], v[104:107], v[52:55]
v_mfma_f32_16x16x32_bf16 v[92:95], v[204:207], v[108:111], v[92:95]
s_waitcnt lgkmcnt(5)
v_mfma_f32_16x16x32_bf16 v[16:19], v[208:211], v[112:115], v[16:19]
s_waitcnt lgkmcnt(4)
v_mfma_f32_16x16x32_bf16 v[56:59], v[208:211], v[116:119], v[56:59]
ds_read_b128 v[104:107], v9 offset:5120
v_mfma_f32_16x16x32_bf16 v[20:23], v[212:215], v[112:115], v[20:23]
v_mfma_f32_16x16x32_bf16 v[60:63], v[212:215], v[116:119], v[60:63]
v_mfma_f32_16x16x32_bf16 v[24:27], v[216:219], v[112:115], v[24:27]
v_mfma_f32_16x16x32_bf16 v[64:67], v[216:219], v[116:119], v[64:67]
v_mfma_f32_16x16x32_bf16 v[28:31], v[220:223], v[112:115], v[28:31]
v_mfma_f32_16x16x32_bf16 v[68:71], v[220:223], v[116:119], v[68:71]
v_mfma_f32_16x16x32_bf16 v[32:35], v[224:227], v[112:115], v[32:35]
v_mfma_f32_16x16x32_bf16 v[72:75], v[224:227], v[116:119], v[72:75]
v_mfma_f32_16x16x32_bf16 v[36:39], v[228:231], v[112:115], v[36:39]
v_mfma_f32_16x16x32_bf16 v[76:79], v[228:231], v[116:119], v[76:79]
ds_read_b128 v[108:111], v7 offset:5120
v_mfma_f32_16x16x32_bf16 v[40:43], v[232:235], v[112:115], v[40:43]
v_mfma_f32_16x16x32_bf16 v[80:83], v[232:235], v[116:119], v[80:83]
v_mfma_f32_16x16x32_bf16 v[44:47], v[236:239], v[112:115], v[44:47]
v_mfma_f32_16x16x32_bf16 v[84:87], v[236:239], v[116:119], v[84:87]
v_mfma_f32_16x16x32_bf16 v[48:51], v[240:243], v[112:115], v[48:51]
v_mfma_f32_16x16x32_bf16 v[88:91], v[240:243], v[116:119], v[88:91]
v_mfma_f32_16x16x32_bf16 v[52:55], v[244:247], v[112:115], v[52:55]
v_mfma_f32_16x16x32_bf16 v[92:95], v[244:247], v[116:119], v[92:95]
s_waitcnt lgkmcnt(5)
v_mfma_f32_16x16x32_bf16 v[16:19], v[248:251], v[120:123], v[16:19]
s_waitcnt lgkmcnt(4)
v_mfma_f32_16x16x32_bf16 v[56:59], v[248:251], v[124:127], v[56:59]
ds_read_b128 v[112:115], v9 offset:6144
v_mfma_f32_16x16x32_bf16 v[20:23], v[252:255], v[120:123], v[20:23]
v_mfma_f32_16x16x32_bf16 v[60:63], v[252:255], v[124:127], v[60:63]
v_mfma_f32_16x16x32_bf16 v[24:27], a[0:3], v[120:123], v[24:27]
v_mfma_f32_16x16x32_bf16 v[64:67], a[0:3], v[124:127], v[64:67]
v_mfma_f32_16x16x32_bf16 v[28:31], a[4:7], v[120:123], v[28:31]
v_mfma_f32_16x16x32_bf16 v[68:71], a[4:7], v[124:127], v[68:71]
v_mfma_f32_16x16x32_bf16 v[32:35], a[8:11], v[120:123], v[32:35]
v_mfma_f32_16x16x32_bf16 v[72:75], a[8:11], v[124:127], v[72:75]
v_mfma_f32_16x16x32_bf16 v[36:39], a[12:15], v[120:123], v[36:39]
v_mfma_f32_16x16x32_bf16 v[76:79], a[12:15], v[124:127], v[76:79]
ds_read_b128 v[116:119], v7 offset:6144
v_mfma_f32_16x16x32_bf16 v[40:43], a[16:19], v[120:123], v[40:43]
v_mfma_f32_16x16x32_bf16 v[80:83], a[16:19], v[124:127], v[80:83]
v_mfma_f32_16x16x32_bf16 v[44:47], a[20:23], v[120:123], v[44:47]
v_mfma_f32_16x16x32_bf16 v[84:87], a[20:23], v[124:127], v[84:87]
v_mfma_f32_16x16x32_bf16 v[48:51], a[24:27], v[120:123], v[48:51]
v_mfma_f32_16x16x32_bf16 v[88:91], a[24:27], v[124:127], v[88:91]
v_mfma_f32_16x16x32_bf16 v[52:55], a[28:31], v[120:123], v[52:55]
v_mfma_f32_16x16x32_bf16 v[92:95], a[28:31], v[124:127], v[92:95]
s_waitcnt lgkmcnt(5)
v_mfma_f32_16x16x32_bf16 v[16:19], a[32:35], v[96:99], v[16:19]
s_waitcnt lgkmcnt(4)
v_mfma_f32_16x16x32_bf16 v[56:59], a[32:35], v[100:103], v[56:59]
ds_read_b128 v[120:123], v9 offset:7168
v_mfma_f32_16x16x32_bf16 v[20:23], a[36:39], v[96:99], v[20:23]
v_mfma_f32_16x16x32_bf16 v[60:63], a[36:39], v[100:103], v[60:63]
v_mfma_f32_16x16x32_bf16 v[24:27], a[40:43], v[96:99], v[24:27]
v_mfma_f32_16x16x32_bf16 v[64:67], a[40:43], v[100:103], v[64:67]
v_mfma_f32_16x16x32_bf16 v[28:31], a[44:47], v[96:99], v[28:31]
v_mfma_f32_16x16x32_bf16 v[68:71], a[44:47], v[100:103], v[68:71]
v_mfma_f32_16x16x32_bf16 v[32:35], a[48:51], v[96:99], v[32:35]
v_mfma_f32_16x16x32_bf16 v[72:75], a[48:51], v[100:103], v[72:75]
v_mfma_f32_16x16x32_bf16 v[36:39], a[52:55], v[96:99], v[36:39]
v_mfma_f32_16x16x32_bf16 v[76:79], a[52:55], v[100:103], v[76:79]
ds_read_b128 v[124:127], v7 offset:7168
v_mfma_f32_16x16x32_bf16 v[40:43], a[56:59], v[96:99], v[40:43]
)"
R"(v_mfma_f32_16x16x32_bf16 v[80:83], a[56:59], v[100:103], v[80:83]
v_mfma_f32_16x16x32_bf16 v[44:47], a[60:63], v[96:99], v[44:47]
v_mfma_f32_16x16x32_bf16 v[84:87], a[60:63], v[100:103], v[84:87]
v_mfma_f32_16x16x32_bf16 v[48:51], a[64:67], v[96:99], v[48:51]
v_mfma_f32_16x16x32_bf16 v[88:91], a[64:67], v[100:103], v[88:91]
v_mfma_f32_16x16x32_bf16 v[52:55], a[68:71], v[96:99], v[52:55]
v_mfma_f32_16x16x32_bf16 v[92:95], a[68:71], v[100:103], v[92:95]
s_waitcnt lgkmcnt(5)
v_mfma_f32_16x16x32_bf16 v[16:19], a[72:75], v[104:107], v[16:19]
s_waitcnt lgkmcnt(4)
v_mfma_f32_16x16x32_bf16 v[56:59], a[72:75], v[108:111], v[56:59]
ds_read_b128 v[96:99], v9 offset:8192
v_mfma_f32_16x16x32_bf16 v[20:23], a[76:79], v[104:107], v[20:23]
v_mfma_f32_16x16x32_bf16 v[60:63], a[76:79], v[108:111], v[60:63]
v_mfma_f32_16x16x32_bf16 v[24:27], a[80:83], v[104:107], v[24:27]
v_mfma_f32_16x16x32_bf16 v[64:67], a[80:83], v[108:111], v[64:67]
v_mfma_f32_16x16x32_bf16 v[28:31], a[84:87], v[104:107], v[28:31]
v_mfma_f32_16x16x32_bf16 v[68:71], a[84:87], v[108:111], v[68:71]
v_mfma_f32_16x16x32_bf16 v[32:35], a[88:91], v[104:107], v[32:35]
v_mfma_f32_16x16x32_bf16 v[72:75], a[88:91], v[108:111], v[72:75]
v_mfma_f32_16x16x32_bf16 v[36:39], a[92:95], v[104:107], v[36:39]
v_mfma_f32_16x16x32_bf16 v[76:79], a[92:95], v[108:111], v[76:79]
ds_read_b128 v[100:103], v7 offset:8192
v_mfma_f32_16x16x32_bf16 v[40:43], a[96:99], v[104:107], v[40:43]
v_mfma_f32_16x16x32_bf16 v[80:83], a[96:99], v[108:111], v[80:83]
ds_read_b128 v[224:227], v6 offset:0
v_mfma_f32_16x16x32_bf16 v[44:47], a[100:103], v[104:107], v[44:47]
v_mfma_f32_16x16x32_bf16 v[84:87], a[100:103], v[108:111], v[84:87]
ds_read_b128 v[228:231], v6 offset:1024
v_mfma_f32_16x16x32_bf16 v[48:51], a[104:107], v[104:107], v[48:51]
v_mfma_f32_16x16x32_bf16 v[88:91], a[104:107], v[108:111], v[88:91]
ds_read_b128 v[232:235], v6 offset:2048
v_mfma_f32_16x16x32_bf16 v[52:55], a[108:111], v[104:107], v[52:55]
v_mfma_f32_16x16x32_bf16 v[92:95], a[108:111], v[108:111], v[92:95]
ds_read_b128 v[236:239], v6 offset:3072
s_waitcnt lgkmcnt(9)
v_mfma_f32_16x16x32_bf16 v[16:19], a[112:115], v[112:115], v[16:19]
s_waitcnt lgkmcnt(8)
v_mfma_f32_16x16x32_bf16 v[56:59], a[112:115], v[116:119], v[56:59]
ds_read_b128 v[240:243], v6 offset:4096
ds_read_b128 v[104:107], v9 offset:9216
v_mfma_f32_16x16x32_bf16 v[20:23], a[116:119], v[112:115], v[20:23]
v_mfma_f32_16x16x32_bf16 v[60:63], a[116:119], v[116:119], v[60:63]
ds_read_b128 v[244:247], v6 offset:5120
v_mfma_f32_16x16x32_bf16 v[24:27], a[120:123], v[112:115], v[24:27]
v_mfma_f32_16x16x32_bf16 v[64:67], a[120:123], v[116:119], v[64:67]
ds_read_b128 v[248:251], v6 offset:6144
v_mfma_f32_16x16x32_bf16 v[28:31], a[124:127], v[112:115], v[28:31]
v_mfma_f32_16x16x32_bf16 v[68:71], a[124:127], v[116:119], v[68:71]
ds_read_b128 v[252:255], v6 offset:7168
v_mfma_f32_16x16x32_bf16 v[32:35], a[128:131], v[112:115], v[32:35]
v_mfma_f32_16x16x32_bf16 v[72:75], a[128:131], v[116:119], v[72:75]
ds_read_b128 v[128:131], v6 offset:8192
v_mfma_f32_16x16x32_bf16 v[36:39], a[132:135], v[112:115], v[36:39]
v_mfma_f32_16x16x32_bf16 v[76:79], a[132:135], v[116:119], v[76:79]
ds_read_b128 v[132:135], v6 offset:9216
ds_read_b128 v[108:111], v7 offset:9216
s_waitcnt lgkmcnt(14)
v_mfma_f32_16x16x32_bf16 v[40:43], a[136:139], v[112:115], v[40:43]
v_mfma_f32_16x16x32_bf16 v[80:83], a[136:139], v[116:119], v[80:83]
ds_read_b128 v[136:139], v6 offset:10240
v_mfma_f32_16x16x32_bf16 v[44:47], a[140:143], v[112:115], v[44:47]
v_mfma_f32_16x16x32_bf16 v[84:87], a[140:143], v[116:119], v[84:87]
ds_read_b128 v[140:143], v6 offset:11264
s_waitcnt lgkmcnt(14)
v_mfma_f32_16x16x32_bf16 v[48:51], a[144:147], v[112:115], v[48:51]
v_mfma_f32_16x16x32_bf16 v[88:91], a[144:147], v[116:119], v[88:91]
v_mfma_f32_16x16x32_bf16 v[52:55], a[148:151], v[112:115], v[52:55]
v_mfma_f32_16x16x32_bf16 v[92:95], a[148:151], v[116:119], v[92:95]
v_mfma_f32_16x16x32_bf16 v[16:19], a[152:155], v[120:123], v[16:19]
v_mfma_f32_16x16x32_bf16 v[56:59], a[152:155], v[124:127], v[56:59]
v_mfma_f32_16x16x32_bf16 v[20:23], a[156:159], v[120:123], v[20:23]
v_mfma_f32_16x16x32_bf16 v[60:63], a[156:159], v[124:127], v[60:63]
v_mfma_f32_16x16x32_bf16 v[24:27], a[160:163], v[120:123], v[24:27]
v_mfma_f32_16x16x32_bf16 v[64:67], a[160:163], v[124:127], v[64:67]
s_add_u32 s48, s34, 138346496
buffer_load_dwordx4 v[160:163], v8, s[40:43], s48 offen sc1
v_mfma_f32_16x16x32_bf16 v[28:31], a[164:167], v[120:123], v[28:31]
v_mfma_f32_16x16x32_bf16 v[68:71], a[164:167], v[124:127], v[68:71]
s_add_u32 s48, s34, 138346496
buffer_load_dwordx4 v[164:167], v8, s[40:43], s48 offen offset:512 sc1
v_mfma_f32_16x16x32_bf16 v[32:35], a[168:171], v[120:123], v[32:35]
v_mfma_f32_16x16x32_bf16 v[72:75], a[168:171], v[124:127], v[72:75]
s_add_u32 s48, s34, 138348544
buffer_load_dwordx4 v[168:171], v8, s[40:43], s48 offen sc1
v_mfma_f32_16x16x32_bf16 v[36:39], a[172:175], v[120:123], v[36:39]
v_mfma_f32_16x16x32_bf16 v[76:79], a[172:175], v[124:127], v[76:79]
s_add_u32 s48, s34, 138348544
buffer_load_dwordx4 v[172:175], v8, s[40:43], s48 offen offset:512 sc1
v_mfma_f32_16x16x32_bf16 v[40:43], a[176:179], v[120:123], v[40:43]
v_mfma_f32_16x16x32_bf16 v[80:83], a[176:179], v[124:127], v[80:83]
s_add_u32 s48, s34, 138350592
buffer_load_dwordx4 v[176:179], v8, s[40:43], s48 offen sc1
v_mfma_f32_16x16x32_bf16 v[44:47], a[180:183], v[120:123], v[44:47]
v_mfma_f32_16x16x32_bf16 v[84:87], a[180:183], v[124:127], v[84:87]
s_add_u32 s48, s34, 138350592
buffer_load_dwordx4 v[180:183], v8, s[40:43], s48 offen offset:512 sc1
v_mfma_f32_16x16x32_bf16 v[48:51], a[184:187], v[120:123], v[48:51]
v_mfma_f32_16x16x32_bf16 v[88:91], a[184:187], v[124:127], v[88:91]
s_add_u32 s48, s34, 138352640
buffer_load_dwordx4 v[184:187], v8, s[40:43], s48 offen sc1
v_mfma_f32_16x16x32_bf16 v[52:55], a[188:191], v[120:123], v[52:55]
v_mfma_f32_16x16x32_bf16 v[92:95], a[188:191], v[124:127], v[92:95]
s_add_u32 s48, s34, 138352640
buffer_load_dwordx4 v[188:191], v8, s[40:43], s48 offen offset:512 sc1
v_mfma_f32_16x16x32_bf16 v[16:19], a[192:195], v[96:99], v[16:19]
v_mfma_f32_16x16x32_bf16 v[56:59], a[192:195], v[100:103], v[56:59]
s_add_u32 s48, s34, 138354688
buffer_load_dwordx4 v[192:195], v8, s[40:43], s48 offen sc1
v_mfma_f32_16x16x32_bf16 v[20:23], a[196:199], v[96:99], v[20:23]
v_mfma_f32_16x16x32_bf16 v[60:63], a[196:199], v[100:103], v[60:63]
s_add_u32 s48, s34, 138354688
buffer_load_dwordx4 v[196:199], v8, s[40:43], s48 offen offset:512 sc1
v_mfma_f32_16x16x32_bf16 v[24:27], a[200:203], v[96:99], v[24:27]
v_mfma_f32_16x16x32_bf16 v[64:67], a[200:203], v[100:103], v[64:67]
v_mfma_f32_16x16x32_bf16 v[28:31], a[204:207], v[96:99], v[28:31]
v_mfma_f32_16x16x32_bf16 v[68:71], a[204:207], v[100:103], v[68:71]
v_mfma_f32_16x16x32_bf16 v[32:35], a[208:211], v[96:99], v[32:35]
v_mfma_f32_16x16x32_bf16 v[72:75], a[208:211], v[100:103], v[72:75]
v_mfma_f32_16x16x32_bf16 v[36:39], a[212:215], v[96:99], v[36:39]
v_mfma_f32_16x16x32_bf16 v[76:79], a[212:215], v[100:103], v[76:79]
v_mfma_f32_16x16x32_bf16 v[40:43], a[216:219], v[96:99], v[40:43]
v_mfma_f32_16x16x32_bf16 v[80:83], a[216:219], v[100:103], v[80:83]
v_mfma_f32_16x16x32_bf16 v[44:47], a[220:223], v[96:99], v[44:47]
v_mfma_f32_16x16x32_bf16 v[84:87], a[220:223], v[100:103], v[84:87]
s_waitcnt lgkmcnt(13)
v_mfma_f32_16x16x32_bf16 v[48:51], v[224:227], v[96:99], v[48:51]
v_mfma_f32_16x16x32_bf16 v[88:91], v[224:227], v[100:103], v[88:91]
s_waitcnt lgkmcnt(12)
v_mfma_f32_16x16x32_bf16 v[52:55], v[228:231], v[96:99], v[52:55]
v_mfma_f32_16x16x32_bf16 v[92:95], v[228:231], v[100:103], v[92:95]
s_waitcnt lgkmcnt(8)
v_mfma_f32_16x16x32_bf16 v[16:19], v[232:235], v[104:107], v[16:19]
s_waitcnt lgkmcnt(2)
v_mfma_f32_16x16x32_bf16 v[56:59], v[232:235], v[108:111], v[56:59]
v_mfma_f32_16x16x32_bf16 v[20:23], v[236:239], v[104:107], v[20:23]
v_mfma_f32_16x16x32_bf16 v[60:63], v[236:239], v[108:111], v[60:63]
v_mfma_f32_16x16x32_bf16 v[24:27], v[240:243], v[104:107], v[24:27]
v_mfma_f32_16x16x32_bf16 v[64:67], v[240:243], v[108:111], v[64:67]
v_mfma_f32_16x16x32_bf16 v[28:31], v[244:247], v[104:107], v[28:31]
v_mfma_f32_16x16x32_bf16 v[68:71], v[244:247], v[108:111], v[68:71]
v_mfma_f32_16x16x32_bf16 v[32:35], v[248:251], v[104:107], v[32:35]
v_mfma_f32_16x16x32_bf16 v[72:75], v[248:251], v[108:111], v[72:75]
v_mfma_f32_16x16x32_bf16 v[36:39], v[252:255], v[104:107], v[36:39]
v_mfma_f32_16x16x32_bf16 v[76:79], v[252:255], v[108:111], v[76:79]
v_mfma_f32_16x16x32_bf16 v[40:43], v[128:131], v[104:107], v[40:43]
v_mfma_f32_16x16x32_bf16 v[80:83], v[128:131], v[108:111], v[80:83]
v_mfma_f32_16x16x32_bf16 v[44:47], v[132:135], v[104:107], v[44:47]
v_mfma_f32_16x16x32_bf16 v[84:87], v[132:135], v[108:111], v[84:87]
s_waitcnt lgkmcnt(1)
v_mfma_f32_16x16x32_bf16 v[48:51], v[136:139], v[104:107], v[48:51]
v_mfma_f32_16x16x32_bf16 v[88:91], v[136:139], v[108:111], v[88:91]
s_waitcnt lgkmcnt(0)
v_mfma_f32_16x16x32_bf16 v[52:55], v[140:143], v[104:107], v[52:55]
v_mfma_f32_16x16x32_bf16 v[92:95], v[140:143], v[108:111], v[92:95]
s_mul_i32 s48, s45, 10240
v_add_u32 v6, s48, v0
s_xor_b32 s48, s45, 1
s_mul_i32 s48, s48, 10240
v_add_u32 v7, s48, v0
ds_write_b128 v6, v[56:59] offset:0
ds_write_b128 v6, v[60:63] offset:1024
ds_write_b128 v6, v[64:67] offset:2048
ds_write_b128 v6, v[68:71] offset:3072
ds_write_b128 v6, v[72:75] offset:4096
ds_write_b128 v6, v[76:79] offset:5120
ds_write_b128 v6, v[80:83] offset:6144
ds_write_b128 v6, v[84:87] offset:7168
ds_write_b128 v6, v[88:91] offset:8192
ds_write_b128 v6, v[92:95] offset:9216
s_waitcnt vmcnt(9)
v_pk_add_f32 v[160:161], v[160:161], v[16:17]
v_pk_add_f32 v[162:163], v[162:163], v[20:21]
s_waitcnt vmcnt(8)
v_pk_add_f32 v[164:165], v[164:165], v[18:19]
v_pk_add_f32 v[166:167], v[166:167], v[22:23]
s_waitcnt vmcnt(7)
v_pk_add_f32 v[168:169], v[168:169], v[24:25]
v_pk_add_f32 v[170:171], v[170:171], v[28:29]
s_waitcnt vmcnt(6)
v_pk_add_f32 v[172:173], v[172:173], v[26:27]
v_pk_add_f32 v[174:175], v[174:175], v[30:31]
s_waitcnt vmcnt(5)
v_pk_add_f32 v[176:177], v[176:177], v[32:33]
v_pk_add_f32 v[178:179], v[178:179], v[36:37]
s_waitcnt vmcnt(4)
v_pk_add_f32 v[180:181], v[180:181], v[34:35]
v_pk_add_f32 v[182:183], v[182:183], v[38:39]
s_waitcnt vmcnt(3)
v_pk_add_f32 v[184:185], v[184:185], v[40:41]
v_pk_add_f32 v[186:187], v[186:187], v[44:45]
s_waitcnt vmcnt(2)
v_pk_add_f32 v[188:189], v[188:189], v[42:43]
v_pk_add_f32 v[190:191], v[190:191], v[46:47]
s_waitcnt vmcnt(1)
v_pk_add_f32 v[192:193], v[192:193], v[48:49]
v_pk_add_f32 v[194:195], v[194:195], v[52:53]
s_waitcnt vmcnt(0)
v_pk_add_f32 v[196:197], v[196:197], v[50:51]
v_pk_add_f32 v[198:199], v[198:199], v[54:55]
)"
R"(s_waitcnt lgkmcnt(0)
s_barrier
ds_read_b128 v[16:19], v7 offset:0
ds_read_b128 v[20:23], v7 offset:1024
ds_read_b128 v[24:27], v7 offset:2048
ds_read_b128 v[28:31], v7 offset:3072
ds_read_b128 v[32:35], v7 offset:4096
ds_read_b128 v[36:39], v7 offset:5120
ds_read_b128 v[40:43], v7 offset:6144
ds_read_b128 v[44:47], v7 offset:7168
ds_read_b128 v[48:51], v7 offset:8192
ds_read_b128 v[52:55], v7 offset:9216
s_waitcnt lgkmcnt(9)
v_pk_add_f32 v[160:161], v[160:161], v[16:17]
s_waitcnt lgkmcnt(8)
v_pk_add_f32 v[162:163], v[162:163], v[20:21]
v_pk_add_f32 v[164:165], v[164:165], v[18:19]
v_pk_add_f32 v[166:167], v[166:167], v[22:23]
s_waitcnt lgkmcnt(7)
v_pk_add_f32 v[168:169], v[168:169], v[24:25]
s_waitcnt lgkmcnt(6)
v_pk_add_f32 v[170:171], v[170:171], v[28:29]
v_pk_add_f32 v[172:173], v[172:173], v[26:27]
v_pk_add_f32 v[174:175], v[174:175], v[30:31]
s_waitcnt lgkmcnt(5)
v_pk_add_f32 v[176:177], v[176:177], v[32:33]
s_waitcnt lgkmcnt(4)
v_pk_add_f32 v[178:179], v[178:179], v[36:37]
v_pk_add_f32 v[180:181], v[180:181], v[34:35]
v_pk_add_f32 v[182:183], v[182:183], v[38:39]
s_waitcnt lgkmcnt(3)
v_pk_add_f32 v[184:185], v[184:185], v[40:41]
s_waitcnt lgkmcnt(2)
v_pk_add_f32 v[186:187], v[186:187], v[44:45]
v_pk_add_f32 v[188:189], v[188:189], v[42:43]
v_pk_add_f32 v[190:191], v[190:191], v[46:47]
s_waitcnt lgkmcnt(1)
v_pk_add_f32 v[192:193], v[192:193], v[48:49]
s_waitcnt lgkmcnt(0)
v_pk_add_f32 v[194:195], v[194:195], v[52:53]
v_pk_add_f32 v[196:197], v[196:197], v[50:51]
v_pk_add_f32 v[198:199], v[198:199], v[54:55]
s_mov_b32 s36, s66
s_mov_b32 s37, s67
s_mov_b32 s38, 2621440
s_mov_b32 s39, 0x20000
v_and_b32 v4, 15, v15
v_mul_u32_u24 v4, 1280, v4
v_and_b32 v5, 48, v15
v_add_u32 v4, v4, v5
s_lshl1_add_u32 s48, s26, s30
s_mul_i32 s48, s48, 20480
s_mul_i32 s49, s31, 320
s_add_u32 s48, s48, s49
v_cvt_pk_bf16_f32 v16, v160, v161
v_cvt_pk_bf16_f32 v17, v162, v163
v_cvt_pk_bf16_f32 v18, v164, v165
v_cvt_pk_bf16_f32 v19, v166, v167
v_cvt_pk_bf16_f32 v20, v168, v169
v_cvt_pk_bf16_f32 v21, v170, v171
v_cvt_pk_bf16_f32 v22, v172, v173
v_cvt_pk_bf16_f32 v23, v174, v175
v_cvt_pk_bf16_f32 v24, v176, v177
v_cvt_pk_bf16_f32 v25, v178, v179
v_cvt_pk_bf16_f32 v26, v180, v181
v_cvt_pk_bf16_f32 v27, v182, v183
v_cvt_pk_bf16_f32 v28, v184, v185
v_cvt_pk_bf16_f32 v29, v186, v187
v_cvt_pk_bf16_f32 v30, v188, v189
v_cvt_pk_bf16_f32 v31, v190, v191
v_cvt_pk_bf16_f32 v32, v192, v193
v_cvt_pk_bf16_f32 v33, v194, v195
v_cvt_pk_bf16_f32 v34, v196, v197
v_cvt_pk_bf16_f32 v35, v198, v199
v_permlane16_swap_b32 v16, v18
v_permlane16_swap_b32 v17, v19
buffer_store_dwordx4 v[16:19], v4, s[36:39], s48 offen offset:0
v_permlane16_swap_b32 v20, v22
v_permlane16_swap_b32 v21, v23
buffer_store_dwordx4 v[20:23], v4, s[36:39], s48 offen offset:64
v_permlane16_swap_b32 v24, v26
v_permlane16_swap_b32 v25, v27
buffer_store_dwordx4 v[24:27], v4, s[36:39], s48 offen offset:128
v_permlane16_swap_b32 v28, v30
v_permlane16_swap_b32 v29, v31
buffer_store_dwordx4 v[28:31], v4, s[36:39], s48 offen offset:192
v_permlane16_swap_b32 v32, v34
v_permlane16_swap_b32 v33, v35
buffer_store_dwordx4 v[32:35], v4, s[36:39], s48 offen offset:256
s_mov_b32 s54, 0x40000000
s_nop 0
s_branch hp_done_%=
hpb2_mid_%=:
ds_read_b128 v[96:99], v9 offset:0
ds_read_b128 v[100:103], v7 offset:0
ds_read_b128 v[104:107], v9 offset:1024
ds_read_b128 v[108:111], v7 offset:1024
ds_read_b128 v[112:115], v9 offset:2048
ds_read_b128 v[116:119], v7 offset:2048
s_waitcnt lgkmcnt(5)
v_mfma_f32_16x16x32_bf16 v[16:19], v[128:131], v[96:99], 0
s_waitcnt lgkmcnt(4)
v_mfma_f32_16x16x32_bf16 v[56:59], v[128:131], v[100:103], 0
s_add_u32 s50, s50, 4096
buffer_load_dwordx4 v[128:131], v0, s[40:43], s50 offen sc1
ds_read_b128 v[120:123], v9 offset:3072
v_mfma_f32_16x16x32_bf16 v[20:23], v[132:135], v[96:99], 0
v_mfma_f32_16x16x32_bf16 v[60:63], v[132:135], v[100:103], 0
buffer_load_dwordx4 v[132:135], v0, s[40:43], s50 offen offset:1024 sc1
v_mfma_f32_16x16x32_bf16 v[24:27], v[136:139], v[96:99], 0
v_mfma_f32_16x16x32_bf16 v[64:67], v[136:139], v[100:103], 0
buffer_load_dwordx4 v[136:139], v0, s[40:43], s50 offen offset:2048 sc1
v_mfma_f32_16x16x32_bf16 v[28:31], v[140:143], v[96:99], 0
v_mfma_f32_16x16x32_bf16 v[68:71], v[140:143], v[100:103], 0
buffer_load_dwordx4 v[140:143], v0, s[40:43], s50 offen offset:3072 sc1
v_mfma_f32_16x16x32_bf16 v[32:35], v[144:147], v[96:99], 0
v_mfma_f32_16x16x32_bf16 v[72:75], v[144:147], v[100:103], 0
s_add_u32 s50, s50, 4096
buffer_load_dwordx4 v[144:147], v0, s[40:43], s50 offen sc1
v_mfma_f32_16x16x32_bf16 v[36:39], v[148:151], v[96:99], 0
v_mfma_f32_16x16x32_bf16 v[76:79], v[148:151], v[100:103], 0
buffer_load_dwordx4 v[148:151], v0, s[40:43], s50 offen offset:1024 sc1
ds_read_b128 v[124:127], v7 offset:3072
v_mfma_f32_16x16x32_bf16 v[40:43], v[152:155], v[96:99], 0
v_mfma_f32_16x16x32_bf16 v[80:83], v[152:155], v[100:103], 0
buffer_load_dwordx4 v[152:155], v0, s[40:43], s50 offen offset:2048 sc1
v_mfma_f32_16x16x32_bf16 v[44:47], v[156:159], v[96:99], 0
v_mfma_f32_16x16x32_bf16 v[84:87], v[156:159], v[100:103], 0
buffer_load_dwordx4 v[156:159], v0, s[40:43], s50 offen offset:3072 sc1
v_mfma_f32_16x16x32_bf16 v[48:51], v[160:163], v[96:99], 0
v_mfma_f32_16x16x32_bf16 v[88:91], v[160:163], v[100:103], 0
s_add_u32 s50, s50, 4096
buffer_load_dwordx4 v[160:163], v0, s[40:43], s50 offen sc1
v_mfma_f32_16x16x32_bf16 v[52:55], v[164:167], v[96:99], 0
v_mfma_f32_16x16x32_bf16 v[92:95], v[164:167], v[100:103], 0
buffer_load_dwordx4 v[164:167], v0, s[40:43], s50 offen offset:1024 sc1
s_waitcnt lgkmcnt(5)
v_mfma_f32_16x16x32_bf16 v[16:19], v[168:171], v[104:107], v[16:19]
s_waitcnt lgkmcnt(4)
v_mfma_f32_16x16x32_bf16 v[56:59], v[168:171], v[108:111], v[56:59]
buffer_load_dwordx4 v[168:171], v0, s[40:43], s50 offen offset:2048 sc1
ds_read_b128 v[96:99], v9 offset:4096
v_mfma_f32_16x16x32_bf16 v[20:23], v[172:175], v[104:107], v[20:23]
v_mfma_f32_16x16x32_bf16 v[60:63], v[172:175], v[108:111], v[60:63]
buffer_load_dwordx4 v[172:175], v0, s[40:43], s50 offen offset:3072 sc1
v_mfma_f32_16x16x32_bf16 v[24:27], v[176:179], v[104:107], v[24:27]
v_mfma_f32_16x16x32_bf16 v[64:67], v[176:179], v[108:111], v[64:67]
s_add_u32 s50, s50, 4096
buffer_load_dwordx4 v[176:179], v0, s[40:43], s50 offen sc1
v_mfma_f32_16x16x32_bf16 v[28:31], v[180:183], v[104:107], v[28:31]
v_mfma_f32_16x16x32_bf16 v[68:71], v[180:183], v[108:111], v[68:71]
buffer_load_dwordx4 v[180:183], v0, s[40:43], s50 offen offset:1024 sc1
v_mfma_f32_16x16x32_bf16 v[32:35], v[184:187], v[104:107], v[32:35]
v_mfma_f32_16x16x32_bf16 v[72:75], v[184:187], v[108:111], v[72:75]
buffer_load_dwordx4 v[184:187], v0, s[40:43], s50 offen offset:2048 sc1
v_mfma_f32_16x16x32_bf16 v[36:39], v[188:191], v[104:107], v[36:39]
v_mfma_f32_16x16x32_bf16 v[76:79], v[188:191], v[108:111], v[76:79]
buffer_load_dwordx4 v[188:191], v0, s[40:43], s50 offen offset:3072 sc1
ds_read_b128 v[100:103], v7 offset:4096
v_mfma_f32_16x16x32_bf16 v[40:43], v[192:195], v[104:107], v[40:43]
v_mfma_f32_16x16x32_bf16 v[80:83], v[192:195], v[108:111], v[80:83]
s_add_u32 s50, s50, 4096
buffer_load_dwordx4 v[192:195], v0, s[40:43], s50 offen sc1
v_mfma_f32_16x16x32_bf16 v[44:47], v[196:199], v[104:107], v[44:47]
v_mfma_f32_16x16x32_bf16 v[84:87], v[196:199], v[108:111], v[84:87]
buffer_load_dwordx4 v[196:199], v0, s[40:43], s50 offen offset:1024 sc1
v_mfma_f32_16x16x32_bf16 v[48:51], v[200:203], v[104:107], v[48:51]
v_mfma_f32_16x16x32_bf16 v[88:91], v[200:203], v[108:111], v[88:91]
buffer_load_dwordx4 v[200:203], v0, s[40:43], s50 offen offset:2048 sc1
v_mfma_f32_16x16x32_bf16 v[52:55], v[204:207], v[104:107], v[52:55]
v_mfma_f32_16x16x32_bf16 v[92:95], v[204:207], v[108:111], v[92:95]
buffer_load_dwordx4 v[204:207], v0, s[40:43], s50 offen offset:3072 sc1
s_waitcnt lgkmcnt(5)
v_mfma_f32_16x16x32_bf16 v[16:19], v[208:211], v[112:115], v[16:19]
s_waitcnt lgkmcnt(4)
v_mfma_f32_16x16x32_bf16 v[56:59], v[208:211], v[116:119], v[56:59]
s_add_u32 s50, s50, 4096
buffer_load_dwordx4 v[208:211], v0, s[40:43], s50 offen sc1
ds_read_b128 v[104:107], v9 offset:5120
v_mfma_f32_16x16x32_bf16 v[20:23], v[212:215], v[112:115], v[20:23]
v_mfma_f32_16x16x32_bf16 v[60:63], v[212:215], v[116:119], v[60:63]
buffer_load_dwordx4 v[212:215], v0, s[40:43], s50 offen offset:1024 sc1
v_mfma_f32_16x16x32_bf16 v[24:27], v[216:219], v[112:115], v[24:27]
v_mfma_f32_16x16x32_bf16 v[64:67], v[216:219], v[116:119], v[64:67]
buffer_load_dwordx4 v[216:219], v0, s[40:43], s50 offen offset:2048 sc1
v_mfma_f32_16x16x32_bf16 v[28:31], v[220:223], v[112:115], v[28:31]
v_mfma_f32_16x16x32_bf16 v[68:71], v[220:223], v[116:119], v[68:71]
buffer_load_dwordx4 v[220:223], v0, s[40:43], s50 offen offset:3072 sc1
v_mfma_f32_16x16x32_bf16 v[32:35], v[224:227], v[112:115], v[32:35]
v_mfma_f32_16x16x32_bf16 v[72:75], v[224:227], v[116:119], v[72:75]
s_add_u32 s50, s50, 4096
buffer_load_dwordx4 v[224:227], v0, s[40:43], s50 offen sc1
v_mfma_f32_16x16x32_bf16 v[36:39], v[228:231], v[112:115], v[36:39]
v_mfma_f32_16x16x32_bf16 v[76:79], v[228:231], v[116:119], v[76:79]
buffer_load_dwordx4 v[228:231], v0, s[40:43], s50 offen offset:1024 sc1
ds_read_b128 v[108:111], v7 offset:5120
v_mfma_f32_16x16x32_bf16 v[40:43], v[232:235], v[112:115], v[40:43]
v_mfma_f32_16x16x32_bf16 v[80:83], v[232:235], v[116:119], v[80:83]
buffer_load_dwordx4 v[232:235], v0, s[40:43], s50 offen offset:2048 sc1
v_mfma_f32_16x16x32_bf16 v[44:47], v[236:239], v[112:115], v[44:47]
)"
R"(v_mfma_f32_16x16x32_bf16 v[84:87], v[236:239], v[116:119], v[84:87]
buffer_load_dwordx4 v[236:239], v0, s[40:43], s50 offen offset:3072 sc1
v_mfma_f32_16x16x32_bf16 v[48:51], v[240:243], v[112:115], v[48:51]
v_mfma_f32_16x16x32_bf16 v[88:91], v[240:243], v[116:119], v[88:91]
s_add_u32 s50, s50, 4096
buffer_load_dwordx4 v[240:243], v0, s[40:43], s50 offen sc1
v_mfma_f32_16x16x32_bf16 v[52:55], v[244:247], v[112:115], v[52:55]
v_mfma_f32_16x16x32_bf16 v[92:95], v[244:247], v[116:119], v[92:95]
buffer_load_dwordx4 v[244:247], v0, s[40:43], s50 offen offset:1024 sc1
s_waitcnt lgkmcnt(5)
v_mfma_f32_16x16x32_bf16 v[16:19], v[248:251], v[120:123], v[16:19]
s_waitcnt lgkmcnt(4)
v_mfma_f32_16x16x32_bf16 v[56:59], v[248:251], v[124:127], v[56:59]
buffer_load_dwordx4 v[248:251], v0, s[40:43], s50 offen offset:2048 sc1
ds_read_b128 v[112:115], v9 offset:6144
v_mfma_f32_16x16x32_bf16 v[20:23], v[252:255], v[120:123], v[20:23]
v_mfma_f32_16x16x32_bf16 v[60:63], v[252:255], v[124:127], v[60:63]
buffer_load_dwordx4 v[252:255], v0, s[40:43], s50 offen offset:3072 sc1
s_waitcnt vmcnt(31)
v_mfma_f32_16x16x32_bf16 v[24:27], v[128:131], v[120:123], v[24:27]
v_mfma_f32_16x16x32_bf16 v[64:67], v[128:131], v[124:127], v[64:67]
s_add_u32 s50, s50, 4096
buffer_load_dwordx4 v[128:131], v0, s[40:43], s50 offen sc1
s_waitcnt vmcnt(31)
v_mfma_f32_16x16x32_bf16 v[28:31], v[132:135], v[120:123], v[28:31]
v_mfma_f32_16x16x32_bf16 v[68:71], v[132:135], v[124:127], v[68:71]
buffer_load_dwordx4 v[132:135], v0, s[40:43], s50 offen offset:1024 sc1
s_waitcnt vmcnt(31)
v_mfma_f32_16x16x32_bf16 v[32:35], v[136:139], v[120:123], v[32:35]
v_mfma_f32_16x16x32_bf16 v[72:75], v[136:139], v[124:127], v[72:75]
buffer_load_dwordx4 v[136:139], v0, s[40:43], s50 offen offset:2048 sc1
s_waitcnt vmcnt(31)
v_mfma_f32_16x16x32_bf16 v[36:39], v[140:143], v[120:123], v[36:39]
v_mfma_f32_16x16x32_bf16 v[76:79], v[140:143], v[124:127], v[76:79]
buffer_load_dwordx4 v[140:143], v0, s[40:43], s50 offen offset:3072 sc1
ds_read_b128 v[116:119], v7 offset:6144
s_waitcnt vmcnt(31)
v_mfma_f32_16x16x32_bf16 v[40:43], v[144:147], v[120:123], v[40:43]
v_mfma_f32_16x16x32_bf16 v[80:83], v[144:147], v[124:127], v[80:83]
s_add_u32 s50, s50, 4096
buffer_load_dwordx4 v[144:147], v0, s[40:43], s50 offen sc1
s_waitcnt vmcnt(31)
v_mfma_f32_16x16x32_bf16 v[44:47], v[148:151], v[120:123], v[44:47]
v_mfma_f32_16x16x32_bf16 v[84:87], v[148:151], v[124:127], v[84:87]
buffer_load_dwordx4 v[148:151], v0, s[40:43], s50 offen offset:1024 sc1
s_waitcnt vmcnt(31)
v_mfma_f32_16x16x32_bf16 v[48:51], v[152:155], v[120:123], v[48:51]
v_mfma_f32_16x16x32_bf16 v[88:91], v[152:155], v[124:127], v[88:91]
buffer_load_dwordx4 v[152:155], v0, s[40:43], s50 offen offset:2048 sc1
s_waitcnt vmcnt(31)
v_mfma_f32_16x16x32_bf16 v[52:55], v[156:159], v[120:123], v[52:55]
v_mfma_f32_16x16x32_bf16 v[92:95], v[156:159], v[124:127], v[92:95]
buffer_load_dwordx4 v[156:159], v0, s[40:43], s50 offen offset:3072 sc1
s_waitcnt vmcnt(31) lgkmcnt(5)
v_mfma_f32_16x16x32_bf16 v[16:19], v[160:163], v[96:99], v[16:19]
s_waitcnt lgkmcnt(4)
v_mfma_f32_16x16x32_bf16 v[56:59], v[160:163], v[100:103], v[56:59]
s_add_u32 s50, s50, 4096
buffer_load_dwordx4 v[160:163], v0, s[40:43], s50 offen sc1
ds_read_b128 v[120:123], v9 offset:7168
s_waitcnt vmcnt(31)
v_mfma_f32_16x16x32_bf16 v[20:23], v[164:167], v[96:99], v[20:23]
v_mfma_f32_16x16x32_bf16 v[60:63], v[164:167], v[100:103], v[60:63]
buffer_load_dwordx4 v[164:167], v0, s[40:43], s50 offen offset:1024 sc1
s_waitcnt vmcnt(31)
v_mfma_f32_16x16x32_bf16 v[24:27], v[168:171], v[96:99], v[24:27]
v_mfma_f32_16x16x32_bf16 v[64:67], v[168:171], v[100:103], v[64:67]
buffer_load_dwordx4 v[168:171], v0, s[40:43], s50 offen offset:2048 sc1
s_waitcnt vmcnt(31)
v_mfma_f32_16x16x32_bf16 v[28:31], v[172:175], v[96:99], v[28:31]
v_mfma_f32_16x16x32_bf16 v[68:71], v[172:175], v[100:103], v[68:71]
buffer_load_dwordx4 v[172:175], v0, s[40:43], s50 offen offset:3072 sc1
s_waitcnt vmcnt(31)
v_mfma_f32_16x16x32_bf16 v[32:35], v[176:179], v[96:99], v[32:35]
v_mfma_f32_16x16x32_bf16 v[72:75], v[176:179], v[100:103], v[72:75]
s_add_u32 s50, s50, 4096
buffer_load_dwordx4 v[176:179], v0, s[40:43], s50 offen sc1
s_waitcnt vmcnt(31)
v_mfma_f32_16x16x32_bf16 v[36:39], v[180:183], v[96:99], v[36:39]
v_mfma_f32_16x16x32_bf16 v[76:79], v[180:183], v[100:103], v[76:79]
buffer_load_dwordx4 v[180:183], v0, s[40:43], s50 offen offset:1024 sc1
ds_read_b128 v[124:127], v7 offset:7168
s_waitcnt vmcnt(31)
v_mfma_f32_16x16x32_bf16 v[40:43], v[184:187], v[96:99], v[40:43]
v_mfma_f32_16x16x32_bf16 v[80:83], v[184:187], v[100:103], v[80:83]
buffer_load_dwordx4 v[184:187], v0, s[40:43], s50 offen offset:2048 sc1
s_waitcnt vmcnt(31)
v_mfma_f32_16x16x32_bf16 v[44:47], v[188:191], v[96:99], v[44:47]
v_mfma_f32_16x16x32_bf16 v[84:87], v[188:191], v[100:103], v[84:87]
buffer_load_dwordx4 v[188:191], v0, s[40:43], s50 offen offset:3072 sc1
s_waitcnt vmcnt(31)
v_mfma_f32_16x16x32_bf16 v[48:51], v[192:195], v[96:99], v[48:51]
v_mfma_f32_16x16x32_bf16 v[88:91], v[192:195], v[100:103], v[88:91]
s_add_u32 s50, s50, 4096
buffer_load_dwordx4 v[192:195], v0, s[40:43], s50 offen sc1
s_waitcnt vmcnt(31)
v_mfma_f32_16x16x32_bf16 v[52:55], v[196:199], v[96:99], v[52:55]
v_mfma_f32_16x16x32_bf16 v[92:95], v[196:199], v[100:103], v[92:95]
buffer_load_dwordx4 v[196:199], v0, s[40:43], s50 offen offset:1024 sc1
s_waitcnt vmcnt(31) lgkmcnt(5)
v_mfma_f32_16x16x32_bf16 v[16:19], v[200:203], v[104:107], v[16:19]
s_waitcnt lgkmcnt(4)
v_mfma_f32_16x16x32_bf16 v[56:59], v[200:203], v[108:111], v[56:59]
buffer_load_dwordx4 v[200:203], v0, s[40:43], s50 offen offset:2048 sc1
ds_read_b128 v[96:99], v9 offset:8192
s_waitcnt vmcnt(31)
v_mfma_f32_16x16x32_bf16 v[20:23], v[204:207], v[104:107], v[20:23]
v_mfma_f32_16x16x32_bf16 v[60:63], v[204:207], v[108:111], v[60:63]
buffer_load_dwordx4 v[204:207], v0, s[40:43], s50 offen offset:3072 sc1
s_waitcnt vmcnt(31)
v_mfma_f32_16x16x32_bf16 v[24:27], v[208:211], v[104:107], v[24:27]
v_mfma_f32_16x16x32_bf16 v[64:67], v[208:211], v[108:111], v[64:67]
s_add_u32 s50, s50, 4096
buffer_load_dwordx4 v[208:211], v0, s[40:43], s50 offen sc1
s_waitcnt vmcnt(31)
v_mfma_f32_16x16x32_bf16 v[28:31], v[212:215], v[104:107], v[28:31]
v_mfma_f32_16x16x32_bf16 v[68:71], v[212:215], v[108:111], v[68:71]
buffer_load_dwordx4 v[212:215], v0, s[40:43], s50 offen offset:1024 sc1
s_waitcnt vmcnt(31)
v_mfma_f32_16x16x32_bf16 v[32:35], v[216:219], v[104:107], v[32:35]
v_mfma_f32_16x16x32_bf16 v[72:75], v[216:219], v[108:111], v[72:75]
buffer_load_dwordx4 v[216:219], v0, s[40:43], s50 offen offset:2048 sc1
s_waitcnt vmcnt(31)
v_mfma_f32_16x16x32_bf16 v[36:39], v[220:223], v[104:107], v[36:39]
v_mfma_f32_16x16x32_bf16 v[76:79], v[220:223], v[108:111], v[76:79]
buffer_load_dwordx4 v[220:223], v0, s[40:43], s50 offen offset:3072 sc1
ds_read_b128 v[100:103], v7 offset:8192
s_waitcnt vmcnt(31)
v_mfma_f32_16x16x32_bf16 v[40:43], v[224:227], v[104:107], v[40:43]
v_mfma_f32_16x16x32_bf16 v[80:83], v[224:227], v[108:111], v[80:83]
ds_read_b128 v[224:227], v6 offset:0
s_waitcnt vmcnt(30)
v_mfma_f32_16x16x32_bf16 v[44:47], v[228:231], v[104:107], v[44:47]
v_mfma_f32_16x16x32_bf16 v[84:87], v[228:231], v[108:111], v[84:87]
ds_read_b128 v[228:231], v6 offset:1024
s_waitcnt vmcnt(29)
v_mfma_f32_16x16x32_bf16 v[48:51], v[232:235], v[104:107], v[48:51]
v_mfma_f32_16x16x32_bf16 v[88:91], v[232:235], v[108:111], v[88:91]
ds_read_b128 v[232:235], v6 offset:2048
s_waitcnt vmcnt(28)
v_mfma_f32_16x16x32_bf16 v[52:55], v[236:239], v[104:107], v[52:55]
v_mfma_f32_16x16x32_bf16 v[92:95], v[236:239], v[108:111], v[92:95]
ds_read_b128 v[236:239], v6 offset:3072
s_waitcnt vmcnt(27) lgkmcnt(9)
v_mfma_f32_16x16x32_bf16 v[16:19], v[240:243], v[112:115], v[16:19]
s_waitcnt lgkmcnt(8)
v_mfma_f32_16x16x32_bf16 v[56:59], v[240:243], v[116:119], v[56:59]
ds_read_b128 v[240:243], v6 offset:4096
ds_read_b128 v[104:107], v9 offset:9216
s_waitcnt vmcnt(26)
v_mfma_f32_16x16x32_bf16 v[20:23], v[244:247], v[112:115], v[20:23]
v_mfma_f32_16x16x32_bf16 v[60:63], v[244:247], v[116:119], v[60:63]
ds_read_b128 v[244:247], v6 offset:5120
s_waitcnt vmcnt(25)
v_mfma_f32_16x16x32_bf16 v[24:27], v[248:251], v[112:115], v[24:27]
v_mfma_f32_16x16x32_bf16 v[64:67], v[248:251], v[116:119], v[64:67]
ds_read_b128 v[248:251], v6 offset:6144
s_waitcnt vmcnt(24)
v_mfma_f32_16x16x32_bf16 v[28:31], v[252:255], v[112:115], v[28:31]
v_mfma_f32_16x16x32_bf16 v[68:71], v[252:255], v[116:119], v[68:71]
ds_read_b128 v[252:255], v6 offset:7168
s_waitcnt vmcnt(23)
v_mfma_f32_16x16x32_bf16 v[32:35], v[128:131], v[112:115], v[32:35]
v_mfma_f32_16x16x32_bf16 v[72:75], v[128:131], v[116:119], v[72:75]
ds_read_b128 v[128:131], v6 offset:8192
s_waitcnt vmcnt(22)
v_mfma_f32_16x16x32_bf16 v[36:39], v[132:135], v[112:115], v[36:39]
v_mfma_f32_16x16x32_bf16 v[76:79], v[132:135], v[116:119], v[76:79]
ds_read_b128 v[132:135], v6 offset:9216
ds_read_b128 v[108:111], v7 offset:9216
s_waitcnt lgkmcnt(14)
s_waitcnt vmcnt(21)
v_mfma_f32_16x16x32_bf16 v[40:43], v[136:139], v[112:115], v[40:43]
v_mfma_f32_16x16x32_bf16 v[80:83], v[136:139], v[116:119], v[80:83]
ds_read_b128 v[136:139], v6 offset:10240
s_waitcnt vmcnt(20)
v_mfma_f32_16x16x32_bf16 v[44:47], v[140:143], v[112:115], v[44:47]
v_mfma_f32_16x16x32_bf16 v[84:87], v[140:143], v[116:119], v[84:87]
ds_read_b128 v[140:143], v6 offset:11264
s_waitcnt lgkmcnt(14)
s_waitcnt vmcnt(19)
v_mfma_f32_16x16x32_bf16 v[48:51], v[144:147], v[112:115], v[48:51]
v_mfma_f32_16x16x32_bf16 v[88:91], v[144:147], v[116:119], v[88:91]
s_waitcnt vmcnt(18)
v_mfma_f32_16x16x32_bf16 v[52:55], v[148:151], v[112:115], v[52:55]
v_mfma_f32_16x16x32_bf16 v[92:95], v[148:151], v[116:119], v[92:95]
s_waitcnt vmcnt(17)
v_mfma_f32_16x16x32_bf16 v[16:19], v[152:155], v[120:123], v[16:19]
v_mfma_f32_16x16x32_bf16 v[56:59], v[152:155], v[124:127], v[56:59]
s_waitcnt vmcnt(16)
v_mfma_f32_16x16x32_bf16 v[20:23], v[156:159], v[120:123], v[20:23]
v_mfma_f32_16x16x32_bf16 v[60:63], v[156:159], v[124:127], v[60:63]
s_waitcnt vmcnt(15)
v_mfma_f32_16x16x32_bf16 v[24:27], v[160:163], v[120:123], v[24:27]
v_mfma_f32_16x16x32_bf16 v[64:67], v[160:163], v[124:127], v[64:67]
s_add_u32 s48, s34, 138346496
buffer_load_dwordx4 v[160:163], v8, s[40:43], s48 offen sc1
s_waitcnt vmcnt(15)
v_mfma_f32_16x16x32_bf16 v[28:31], v[164:167], v[120:123], v[28:31]
v_mfma_f32_16x16x32_bf16 v[68:71], v[164:167], v[124:127], v[68:71]
)"
R"(s_add_u32 s48, s34, 138346496
buffer_load_dwordx4 v[164:167], v8, s[40:43], s48 offen offset:512 sc1
s_waitcnt vmcnt(15)
v_mfma_f32_16x16x32_bf16 v[32:35], v[168:171], v[120:123], v[32:35]
v_mfma_f32_16x16x32_bf16 v[72:75], v[168:171], v[124:127], v[72:75]
s_add_u32 s48, s34, 138348544
buffer_load_dwordx4 v[168:171], v8, s[40:43], s48 offen sc1
s_waitcnt vmcnt(15)
v_mfma_f32_16x16x32_bf16 v[36:39], v[172:175], v[120:123], v[36:39]
v_mfma_f32_16x16x32_bf16 v[76:79], v[172:175], v[124:127], v[76:79]
s_add_u32 s48, s34, 138348544
buffer_load_dwordx4 v[172:175], v8, s[40:43], s48 offen offset:512 sc1
s_waitcnt vmcnt(15)
v_mfma_f32_16x16x32_bf16 v[40:43], v[176:179], v[120:123], v[40:43]
v_mfma_f32_16x16x32_bf16 v[80:83], v[176:179], v[124:127], v[80:83]
s_add_u32 s48, s34, 138350592
buffer_load_dwordx4 v[176:179], v8, s[40:43], s48 offen sc1
s_waitcnt vmcnt(15)
v_mfma_f32_16x16x32_bf16 v[44:47], v[180:183], v[120:123], v[44:47]
v_mfma_f32_16x16x32_bf16 v[84:87], v[180:183], v[124:127], v[84:87]
s_add_u32 s48, s34, 138350592
buffer_load_dwordx4 v[180:183], v8, s[40:43], s48 offen offset:512 sc1
s_waitcnt vmcnt(15)
v_mfma_f32_16x16x32_bf16 v[48:51], v[184:187], v[120:123], v[48:51]
v_mfma_f32_16x16x32_bf16 v[88:91], v[184:187], v[124:127], v[88:91]
s_add_u32 s48, s34, 138352640
buffer_load_dwordx4 v[184:187], v8, s[40:43], s48 offen sc1
s_waitcnt vmcnt(15)
v_mfma_f32_16x16x32_bf16 v[52:55], v[188:191], v[120:123], v[52:55]
v_mfma_f32_16x16x32_bf16 v[92:95], v[188:191], v[124:127], v[92:95]
s_add_u32 s48, s34, 138352640
buffer_load_dwordx4 v[188:191], v8, s[40:43], s48 offen offset:512 sc1
s_waitcnt vmcnt(15)
v_mfma_f32_16x16x32_bf16 v[16:19], v[192:195], v[96:99], v[16:19]
v_mfma_f32_16x16x32_bf16 v[56:59], v[192:195], v[100:103], v[56:59]
s_add_u32 s48, s34, 138354688
buffer_load_dwordx4 v[192:195], v8, s[40:43], s48 offen sc1
s_waitcnt vmcnt(15)
v_mfma_f32_16x16x32_bf16 v[20:23], v[196:199], v[96:99], v[20:23]
v_mfma_f32_16x16x32_bf16 v[60:63], v[196:199], v[100:103], v[60:63]
s_add_u32 s48, s34, 138354688
buffer_load_dwordx4 v[196:199], v8, s[40:43], s48 offen offset:512 sc1
s_waitcnt vmcnt(15)
v_mfma_f32_16x16x32_bf16 v[24:27], v[200:203], v[96:99], v[24:27]
v_mfma_f32_16x16x32_bf16 v[64:67], v[200:203], v[100:103], v[64:67]
s_waitcnt vmcnt(14)
v_mfma_f32_16x16x32_bf16 v[28:31], v[204:207], v[96:99], v[28:31]
v_mfma_f32_16x16x32_bf16 v[68:71], v[204:207], v[100:103], v[68:71]
s_waitcnt vmcnt(13)
v_mfma_f32_16x16x32_bf16 v[32:35], v[208:211], v[96:99], v[32:35]
v_mfma_f32_16x16x32_bf16 v[72:75], v[208:211], v[100:103], v[72:75]
s_waitcnt vmcnt(12)
v_mfma_f32_16x16x32_bf16 v[36:39], v[212:215], v[96:99], v[36:39]
v_mfma_f32_16x16x32_bf16 v[76:79], v[212:215], v[100:103], v[76:79]
s_waitcnt vmcnt(11)
v_mfma_f32_16x16x32_bf16 v[40:43], v[216:219], v[96:99], v[40:43]
v_mfma_f32_16x16x32_bf16 v[80:83], v[216:219], v[100:103], v[80:83]
s_waitcnt vmcnt(10)
v_mfma_f32_16x16x32_bf16 v[44:47], v[220:223], v[96:99], v[44:47]
v_mfma_f32_16x16x32_bf16 v[84:87], v[220:223], v[100:103], v[84:87]
s_waitcnt lgkmcnt(13)
v_mfma_f32_16x16x32_bf16 v[48:51], v[224:227], v[96:99], v[48:51]
v_mfma_f32_16x16x32_bf16 v[88:91], v[224:227], v[100:103], v[88:91]
s_waitcnt lgkmcnt(12)
v_mfma_f32_16x16x32_bf16 v[52:55], v[228:231], v[96:99], v[52:55]
v_mfma_f32_16x16x32_bf16 v[92:95], v[228:231], v[100:103], v[92:95]
s_waitcnt lgkmcnt(8)
v_mfma_f32_16x16x32_bf16 v[16:19], v[232:235], v[104:107], v[16:19]
s_waitcnt lgkmcnt(2)
v_mfma_f32_16x16x32_bf16 v[56:59], v[232:235], v[108:111], v[56:59]
v_mfma_f32_16x16x32_bf16 v[20:23], v[236:239], v[104:107], v[20:23]
v_mfma_f32_16x16x32_bf16 v[60:63], v[236:239], v[108:111], v[60:63]
v_mfma_f32_16x16x32_bf16 v[24:27], v[240:243], v[104:107], v[24:27]
v_mfma_f32_16x16x32_bf16 v[64:67], v[240:243], v[108:111], v[64:67]
v_mfma_f32_16x16x32_bf16 v[28:31], v[244:247], v[104:107], v[28:31]
v_mfma_f32_16x16x32_bf16 v[68:71], v[244:247], v[108:111], v[68:71]
v_mfma_f32_16x16x32_bf16 v[32:35], v[248:251], v[104:107], v[32:35]
v_mfma_f32_16x16x32_bf16 v[72:75], v[248:251], v[108:111], v[72:75]
v_mfma_f32_16x16x32_bf16 v[36:39], v[252:255], v[104:107], v[36:39]
v_mfma_f32_16x16x32_bf16 v[76:79], v[252:255], v[108:111], v[76:79]
v_mfma_f32_16x16x32_bf16 v[40:43], v[128:131], v[104:107], v[40:43]
v_mfma_f32_16x16x32_bf16 v[80:83], v[128:131], v[108:111], v[80:83]
v_mfma_f32_16x16x32_bf16 v[44:47], v[132:135], v[104:107], v[44:47]
v_mfma_f32_16x16x32_bf16 v[84:87], v[132:135], v[108:111], v[84:87]
s_waitcnt lgkmcnt(1)
v_mfma_f32_16x16x32_bf16 v[48:51], v[136:139], v[104:107], v[48:51]
v_mfma_f32_16x16x32_bf16 v[88:91], v[136:139], v[108:111], v[88:91]
s_waitcnt lgkmcnt(0)
v_mfma_f32_16x16x32_bf16 v[52:55], v[140:143], v[104:107], v[52:55]
v_mfma_f32_16x16x32_bf16 v[92:95], v[140:143], v[108:111], v[92:95]
s_mul_i32 s48, s45, 10240
v_add_u32 v6, s48, v0
s_xor_b32 s48, s45, 1
s_mul_i32 s48, s48, 10240
v_add_u32 v7, s48, v0
ds_write_b128 v6, v[56:59] offset:0
ds_write_b128 v6, v[60:63] offset:1024
ds_write_b128 v6, v[64:67] offset:2048
ds_write_b128 v6, v[68:71] offset:3072
ds_write_b128 v6, v[72:75] offset:4096
ds_write_b128 v6, v[76:79] offset:5120
ds_write_b128 v6, v[80:83] offset:6144
ds_write_b128 v6, v[84:87] offset:7168
ds_write_b128 v6, v[88:91] offset:8192
ds_write_b128 v6, v[92:95] offset:9216
s_waitcnt vmcnt(9)
v_pk_add_f32 v[160:161], v[160:161], v[16:17]
v_pk_add_f32 v[162:163], v[162:163], v[20:21]
s_waitcnt vmcnt(8)
v_pk_add_f32 v[164:165], v[164:165], v[18:19]
v_pk_add_f32 v[166:167], v[166:167], v[22:23]
s_waitcnt vmcnt(7)
v_pk_add_f32 v[168:169], v[168:169], v[24:25]
v_pk_add_f32 v[170:171], v[170:171], v[28:29]
s_waitcnt vmcnt(6)
v_pk_add_f32 v[172:173], v[172:173], v[26:27]
v_pk_add_f32 v[174:175], v[174:175], v[30:31]
s_waitcnt vmcnt(5)
v_pk_add_f32 v[176:177], v[176:177], v[32:33]
v_pk_add_f32 v[178:179], v[178:179], v[36:37]
s_waitcnt vmcnt(4)
v_pk_add_f32 v[180:181], v[180:181], v[34:35]
v_pk_add_f32 v[182:183], v[182:183], v[38:39]
s_waitcnt vmcnt(3)
v_pk_add_f32 v[184:185], v[184:185], v[40:41]
v_pk_add_f32 v[186:187], v[186:187], v[44:45]
s_waitcnt vmcnt(2)
v_pk_add_f32 v[188:189], v[188:189], v[42:43]
v_pk_add_f32 v[190:191], v[190:191], v[46:47]
s_waitcnt vmcnt(1)
v_pk_add_f32 v[192:193], v[192:193], v[48:49]
v_pk_add_f32 v[194:195], v[194:195], v[52:53]
s_waitcnt vmcnt(0)
v_pk_add_f32 v[196:197], v[196:197], v[50:51]
v_pk_add_f32 v[198:199], v[198:199], v[54:55]
s_waitcnt lgkmcnt(0)
s_barrier
ds_read_b128 v[16:19], v7 offset:0
ds_read_b128 v[20:23], v7 offset:1024
ds_read_b128 v[24:27], v7 offset:2048
ds_read_b128 v[28:31], v7 offset:3072
ds_read_b128 v[32:35], v7 offset:4096
ds_read_b128 v[36:39], v7 offset:5120
ds_read_b128 v[40:43], v7 offset:6144
ds_read_b128 v[44:47], v7 offset:7168
ds_read_b128 v[48:51], v7 offset:8192
ds_read_b128 v[52:55], v7 offset:9216
s_waitcnt lgkmcnt(9)
v_pk_add_f32 v[160:161], v[160:161], v[16:17]
s_waitcnt lgkmcnt(8)
v_pk_add_f32 v[162:163], v[162:163], v[20:21]
v_pk_add_f32 v[164:165], v[164:165], v[18:19]
v_pk_add_f32 v[166:167], v[166:167], v[22:23]
s_waitcnt lgkmcnt(7)
v_pk_add_f32 v[168:169], v[168:169], v[24:25]
s_waitcnt lgkmcnt(6)
v_pk_add_f32 v[170:171], v[170:171], v[28:29]
v_pk_add_f32 v[172:173], v[172:173], v[26:27]
v_pk_add_f32 v[174:175], v[174:175], v[30:31]
s_waitcnt lgkmcnt(5)
v_pk_add_f32 v[176:177], v[176:177], v[32:33]
s_waitcnt lgkmcnt(4)
v_pk_add_f32 v[178:179], v[178:179], v[36:37]
v_pk_add_f32 v[180:181], v[180:181], v[34:35]
v_pk_add_f32 v[182:183], v[182:183], v[38:39]
s_waitcnt lgkmcnt(3)
v_pk_add_f32 v[184:185], v[184:185], v[40:41]
s_waitcnt lgkmcnt(2)
v_pk_add_f32 v[186:187], v[186:187], v[44:45]
v_pk_add_f32 v[188:189], v[188:189], v[42:43]
v_pk_add_f32 v[190:191], v[190:191], v[46:47]
s_waitcnt lgkmcnt(1)
v_pk_add_f32 v[192:193], v[192:193], v[48:49]
s_waitcnt lgkmcnt(0)
v_pk_add_f32 v[194:195], v[194:195], v[52:53]
v_pk_add_f32 v[196:197], v[196:197], v[50:51]
v_pk_add_f32 v[198:199], v[198:199], v[54:55]
v_pk_add_f32 v[12:13], v[160:161], v[162:163]
v_pk_add_f32 v[12:13], v[12:13], v[164:165]
v_pk_add_f32 v[12:13], v[12:13], v[166:167]
v_pk_add_f32 v[12:13], v[12:13], v[168:169]
v_pk_add_f32 v[12:13], v[12:13], v[170:171]
v_pk_add_f32 v[12:13], v[12:13], v[172:173]
v_pk_add_f32 v[12:13], v[12:13], v[174:175]
v_pk_add_f32 v[12:13], v[12:13], v[176:177]
v_pk_add_f32 v[12:13], v[12:13], v[178:179]
v_pk_add_f32 v[12:13], v[12:13], v[180:181]
v_pk_add_f32 v[12:13], v[12:13], v[182:183]
v_pk_add_f32 v[12:13], v[12:13], v[184:185]
v_pk_add_f32 v[12:13], v[12:13], v[186:187]
v_pk_add_f32 v[12:13], v[12:13], v[188:189]
v_pk_add_f32 v[12:13], v[12:13], v[190:191]
v_pk_add_f32 v[12:13], v[12:13], v[192:193]
v_pk_add_f32 v[12:13], v[12:13], v[194:195]
v_pk_add_f32 v[12:13], v[12:13], v[196:197]
v_pk_add_f32 v[12:13], v[12:13], v[198:199]
v_add_f32 v10, v12, v13
v_mov_b32 v12, v10
s_nop 1
v_permlane16_swap_b32 v10, v12
)"
R"(v_add_f32 v10, v10, v12
v_mov_b32 v12, v10
s_nop 1
v_permlane32_swap_b32 v10, v12
v_add_f32 v10, v10, v12
v_mul_f32 v10, 0xbbcccccd, v10
v_pk_add_f32 v[16:17], v[160:161], v[10:11] op_sel_hi:[1,0]
v_pk_add_f32 v[18:19], v[162:163], v[10:11] op_sel_hi:[1,0]
v_pk_add_f32 v[20:21], v[164:165], v[10:11] op_sel_hi:[1,0]
v_pk_add_f32 v[22:23], v[166:167], v[10:11] op_sel_hi:[1,0]
v_pk_add_f32 v[24:25], v[168:169], v[10:11] op_sel_hi:[1,0]
v_pk_add_f32 v[26:27], v[170:171], v[10:11] op_sel_hi:[1,0]
v_pk_add_f32 v[28:29], v[172:173], v[10:11] op_sel_hi:[1,0]
v_pk_add_f32 v[30:31], v[174:175], v[10:11] op_sel_hi:[1,0]
v_pk_add_f32 v[32:33], v[176:177], v[10:11] op_sel_hi:[1,0]
v_pk_add_f32 v[34:35], v[178:179], v[10:11] op_sel_hi:[1,0]
v_pk_add_f32 v[36:37], v[180:181], v[10:11] op_sel_hi:[1,0]
v_pk_add_f32 v[38:39], v[182:183], v[10:11] op_sel_hi:[1,0]
v_pk_add_f32 v[40:41], v[184:185], v[10:11] op_sel_hi:[1,0]
v_pk_add_f32 v[42:43], v[186:187], v[10:11] op_sel_hi:[1,0]
v_pk_add_f32 v[44:45], v[188:189], v[10:11] op_sel_hi:[1,0]
v_pk_add_f32 v[46:47], v[190:191], v[10:11] op_sel_hi:[1,0]
v_pk_add_f32 v[48:49], v[192:193], v[10:11] op_sel_hi:[1,0]
v_pk_add_f32 v[50:51], v[194:195], v[10:11] op_sel_hi:[1,0]
v_pk_add_f32 v[52:53], v[196:197], v[10:11] op_sel_hi:[1,0]
v_pk_add_f32 v[54:55], v[198:199], v[10:11] op_sel_hi:[1,0]
v_pk_mul_f32 v[12:13], v[16:17], v[16:17]
v_pk_fma_f32 v[12:13], v[18:19], v[18:19], v[12:13]
v_pk_fma_f32 v[12:13], v[20:21], v[20:21], v[12:13]
v_pk_fma_f32 v[12:13], v[22:23], v[22:23], v[12:13]
v_pk_fma_f32 v[12:13], v[24:25], v[24:25], v[12:13]
v_pk_fma_f32 v[12:13], v[26:27], v[26:27], v[12:13]
v_pk_fma_f32 v[12:13], v[28:29], v[28:29], v[12:13]
v_pk_fma_f32 v[12:13], v[30:31], v[30:31], v[12:13]
v_pk_fma_f32 v[12:13], v[32:33], v[32:33], v[12:13]
v_pk_fma_f32 v[12:13], v[34:35], v[34:35], v[12:13]
v_pk_fma_f32 v[12:13], v[36:37], v[36:37], v[12:13]
v_pk_fma_f32 v[12:13], v[38:39], v[38:39], v[12:13]
v_pk_fma_f32 v[12:13], v[40:41], v[40:41], v[12:13]
v_pk_fma_f32 v[12:13], v[42:43], v[42:43], v[12:13]
v_pk_fma_f32 v[12:13], v[44:45], v[44:45], v[12:13]
v_pk_fma_f32 v[12:13], v[46:47], v[46:47], v[12:13]
v_pk_fma_f32 v[12:13], v[48:49], v[48:49], v[12:13]
v_pk_fma_f32 v[12:13], v[50:51], v[50:51], v[12:13]
v_pk_fma_f32 v[12:13], v[52:53], v[52:53], v[12:13]
v_pk_fma_f32 v[12:13], v[54:55], v[54:55], v[12:13]
v_add_f32 v11, v12, v13
v_mov_b32 v12, v11
s_nop 1
v_permlane16_swap_b32 v11, v12
v_add_f32 v11, v11, v12
v_mov_b32 v12, v11
s_nop 1
v_permlane32_swap_b32 v11, v12
v_add_f32 v11, v11, v12
v_min_u32 v10, 0xfffffffe, v10
v_min_u32 v11, 0xfffffffe, v11
s_lshl1_add_u32 s48, s26, s30
s_lshl_b32 s48, s48, 9
s_and_b32 s49, s48, 0xffffe000
s_add_u32 s48, s48, s49
s_add_u32 s35, s48, 1441792
v_lshlrev_b32 v6, 3, v15
v_mov_b32 v7, v6
v_lshrrev_b32 v4, 4, v15
v_cmp_eq_u32 vcc, s31, v4
v_mov_b32 v4, 2147483632
s_nop 0
v_cndmask_b32 v7, v4, v7, vcc
buffer_store_dwordx2 v[10:11], v7, s[40:43], s35 offen sc1
s_add_u32 s48, s34, 133103616
buffer_store_dwordx4 v[160:163], v8, s[40:43], s48 offen sc1
buffer_store_dwordx4 v[164:167], v8, s[40:43], s48 offen offset:512 sc1
s_add_u32 s48, s34, 133105664
buffer_store_dwordx4 v[168:171], v8, s[40:43], s48 offen sc1
buffer_store_dwordx4 v[172:175], v8, s[40:43], s48 offen offset:512 sc1
s_add_u32 s48, s34, 133107712
buffer_store_dwordx4 v[176:179], v8, s[40:43], s48 offen sc1
buffer_store_dwordx4 v[180:183], v8, s[40:43], s48 offen offset:512 sc1
s_add_u32 s48, s34, 133109760
buffer_store_dwordx4 v[184:187], v8, s[40:43], s48 offen sc1
buffer_store_dwordx4 v[188:191], v8, s[40:43], s48 offen offset:512 sc1
s_add_u32 s48, s34, 133111808
buffer_store_dwordx4 v[192:195], v8, s[40:43], s48 offen sc1
buffer_store_dwordx4 v[196:199], v8, s[40:43], s48 offen offset:512 sc1
s_mov_b32 s60, 0
hpb2_ln_%=:
buffer_load_dwordx2 v[96:97], v6, s[40:43], s35 offen sc1
s_waitcnt vmcnt(0)
v_max_u32 v4, v96, v97
v_cmp_eq_u32 vcc, -1, v4
s_nop 4
s_cbranch_vccz hpb2_ln_ok_%=
s_sleep 1
s_add_u32 s60, s60, 1
s_cmp_gt_u32 s60, 65536
s_cbranch_scc1 hp_abort_%=
s_and_b32 s59, s60, 63
s_cbranch_scc1 hpb2_ln_%=
v_mov_b32 v4, 1024
buffer_load_dword v4, v4, s[40:43], s58 offen sc1
s_waitcnt vmcnt(0)
v_readfirstlane_b32 s59, v4
s_nop 0
s_cmp_lg_u32 s59, 0
s_nop 2
s_cbranch_scc1 hp_end_%=
s_branch hpb2_ln_%=
hpb2_ln_ok_%=:
v_mov_b32 v12, v96
v_mov_b32 v13, v12
s_nop 1
v_permlane16_swap_b32 v12, v13
v_add_f32 v12, v12, v13
v_mov_b32 v13, v12
s_nop 1
v_permlane32_swap_b32 v12, v13
v_add_f32 v12, v12, v13
v_mul_f32 v12, 0.25, v12
v_sub_f32 v96, v96, v12
v_mul_f32 v96, v96, v96
v_fmamk_f32 v96, v96, 0x43200000, v97
v_mov_b32 v97, v96
s_nop 1
v_permlane16_swap_b32 v96, v97
v_add_f32 v96, v96, v97
v_mov_b32 v97, v96
s_nop 1
v_permlane32_swap_b32 v96, v97
v_add_f32 v96, v96, v97
v_mov_b32 v97, 0x3727c5ac
v_fmamk_f32 v96, v96, 0x3acccccd, v97
v_rsq_f32 v96, v96
v_sub_f32 v12, v12, v10
v_mul_f32 v12, v12, v96
v_pk_fma_f32 v[16:17], v[16:17], v[96:97], v[12:13] op_sel_hi:[1,0,0]
v_pk_fma_f32 v[18:19], v[18:19], v[96:97], v[12:13] op_sel_hi:[1,0,0]
v_pk_fma_f32 v[20:21], v[20:21], v[96:97], v[12:13] op_sel_hi:[1,0,0]
v_pk_fma_f32 v[22:23], v[22:23], v[96:97], v[12:13] op_sel_hi:[1,0,0]
v_pk_fma_f32 v[24:25], v[24:25], v[96:97], v[12:13] op_sel_hi:[1,0,0]
v_pk_fma_f32 v[26:27], v[26:27], v[96:97], v[12:13] op_sel_hi:[1,0,0]
v_pk_fma_f32 v[28:29], v[28:29], v[96:97], v[12:13] op_sel_hi:[1,0,0]
v_pk_fma_f32 v[30:31], v[30:31], v[96:97], v[12:13] op_sel_hi:[1,0,0]
v_pk_fma_f32 v[32:33], v[32:33], v[96:97], v[12:13] op_sel_hi:[1,0,0]
v_pk_fma_f32 v[34:35], v[34:35], v[96:97], v[12:13] op_sel_hi:[1,0,0]
v_pk_fma_f32 v[36:37], v[36:37], v[96:97], v[12:13] op_sel_hi:[1,0,0]
v_pk_fma_f32 v[38:39], v[38:39], v[96:97], v[12:13] op_sel_hi:[1,0,0]
v_pk_fma_f32 v[40:41], v[40:41], v[96:97], v[12:13] op_sel_hi:[1,0,0]
v_pk_fma_f32 v[42:43], v[42:43], v[96:97], v[12:13] op_sel_hi:[1,0,0]
v_pk_fma_f32 v[44:45], v[44:45], v[96:97], v[12:13] op_sel_hi:[1,0,0]
v_pk_fma_f32 v[46:47], v[46:47], v[96:97], v[12:13] op_sel_hi:[1,0,0]
v_pk_fma_f32 v[48:49], v[48:49], v[96:97], v[12:13] op_sel_hi:[1,0,0]
v_pk_fma_f32 v[50:51], v[50:51], v[96:97], v[12:13] op_sel_hi:[1,0,0]
v_pk_fma_f32 v[52:53], v[52:53], v[96:97], v[12:13] op_sel_hi:[1,0,0]
v_pk_fma_f32 v[54:55], v[54:55], v[96:97], v[12:13] op_sel_hi:[1,0,0]
s_lshr_b32 s48, s34, 1
s_add_u32 s48, s48, 5160960
s_add_u32 s49, s48, 4096
v_cvt_pk_bf16_f32 v56, v16, v17
v_cvt_pk_bf16_f32 v57, v20, v21
v_cvt_pk_bf16_f32 v58, v18, v19
v_cvt_pk_bf16_f32 v59, v22, v23
buffer_store_dwordx4 v[56:59], v0, s[40:43], s48 offen sc1
v_cvt_pk_bf16_f32 v60, v24, v25
v_cvt_pk_bf16_f32 v61, v28, v29
v_cvt_pk_bf16_f32 v62, v26, v27
v_cvt_pk_bf16_f32 v63, v30, v31
buffer_store_dwordx4 v[60:63], v0, s[40:43], s48 offen offset:1024 sc1
v_cvt_pk_bf16_f32 v64, v32, v33
v_cvt_pk_bf16_f32 v65, v36, v37
v_cvt_pk_bf16_f32 v66, v34, v35
v_cvt_pk_bf16_f32 v67, v38, v39
buffer_store_dwordx4 v[64:67], v0, s[40:43], s48 offen offset:2048 sc1
v_cvt_pk_bf16_f32 v68, v40, v41
v_cvt_pk_bf16_f32 v69, v44, v45
v_cvt_pk_bf16_f32 v70, v42, v43
v_cvt_pk_bf16_f32 v71, v46, v47
buffer_store_dwordx4 v[68:71], v0, s[40:43], s48 offen offset:3072 sc1
v_cvt_pk_bf16_f32 v72, v48, v49
v_cvt_pk_bf16_f32 v73, v52, v53
v_cvt_pk_bf16_f32 v74, v50, v51
v_cvt_pk_bf16_f32 v75, v54, v55
buffer_store_dwordx4 v[72:75], v0, s[40:43], s49 offen sc1
s_add_u32 s54, s26, 64
s_lshl_b32 s54, s54, 7
s_add_u32 s54, s54, 2140672
hp_done_%=:
s_mov_b32 s55, 1
hp_donen_%=:
s_cmp_eq_u32 s45, 0
s_cselect_b32 s59, s55, 0
v_lshlrev_b32 v2, 7, v15
v_cmp_gt_u32 vcc, s59, v15
v_mov_b32 v4, 2147483632
s_nop 0
v_cndmask_b32 v2, v4, v2, vcc
v_mov_b32 v1, 1
s_waitcnt vmcnt(0)
s_waitcnt lgkmcnt(0)
s_barrier
)"
R"(buffer_atomic_add v1, v2, s[40:43], s54 offen
s_cmp_ge_u32 s20, 400
s_nop 0
s_cbranch_scc1 hp_over_%=
s_branch hp_next_%=
hp_ret_%=:
s_waitcnt vmcnt(0)
s_waitcnt lgkmcnt(0)
s_barrier
hp_next_%=:
v_mov_b32 v3, 155648
s_cmp_lg_u32 s45, 0
s_nop 0
s_cbranch_scc1 hp_tk_%=
s_cmp_eq_u32 s45, 0
s_cselect_b32 s59, 1, 0
v_lshlrev_b32 v2, 7, v15
v_cmp_gt_u32 vcc, s59, v15
v_mov_b32 v4, 2147483632
s_nop 0
v_cndmask_b32 v2, v4, v2, vcc
v_mov_b32 v1, 1
s_lshl_b32 s54, s21, 7
s_add_u32 s54, s54, 2374656
buffer_atomic_add v1, v2, s[40:43], s54 offen sc0
s_waitcnt vmcnt(0)
v_readfirstlane_b32 s48, v1
s_nop 1
v_mov_b32 v1, s48
ds_write_b32 v3, v1
s_nop 0
hp_tk_%=:
s_waitcnt lgkmcnt(0)
s_barrier
ds_read_b32 v1, v3
s_waitcnt lgkmcnt(0)
v_readfirstlane_b32 s20, v1
s_nop 0
s_cmp_lt_u32 s20, 224
s_nop 2
s_cbranch_scc1 hp_b1_%=
s_cmp_ge_u32 s20, 400
s_cbranch_scc1 hp_hi_%=
s_sub_u32 s48, s20, 368
s_cmp_lt_u32 s48, 32
s_cbranch_scc1 hp_rd_%=
s_cmp_ge_u32 s20, 400
s_cselect_b32 s48, 32, 0
s_sub_u32 s27, s20, s48
s_sub_u32 s27, s27, 224
s_mul_i32 s26, s27, 3641
s_lshr_b32 s26, s26, 16
s_mul_i32 s48, s26, 18
s_sub_u32 s27, s27, s48
s_lshl_b32 s26, s26, 2
s_lshr_b32 s26, 1985229328, s26
s_and_b32 s26, s26, 7
s_add_u32 s26, s26, 56
s_cmp_lt_u32 s27, 10
s_cbranch_scc1 hp_tf_%=
s_sub_u32 s27, s27, 10
s_branch hp_bf_%=
hp_hi_%=:
s_cmp_lt_u32 s20, 528
s_cbranch_scc1 hp_b2_%=
hp_over_%=:
s_add_u32 s21, s21, 1
s_cmp_lt_u32 s21, 2
s_cbranch_scc1 hp_layer_%=
s_branch hp_end_%=
hp_abort_%=:
v_mov_b32 v4, 1
v_mov_b32 v5, 1024
buffer_store_dword v4, v5, s[40:43], s58 offen sc1
hp_end_%=:
s_endpgm
)"
    :: ROLE_INPUTS : ROLE_CLOBBERS);
    __builtin_unreachable();
}

__global__ void __launch_bounds__(256, 1) mega(char* ws, u16* out, const u16* Wq, const u16* Wk, const u16* Wv, const u16* Wo, const u16* wqkv, const u16* wphi, const u16* wo, Tune tn) {
    extern __shared__ __attribute__((aligned(16))) char smem[];
    LAS char* lds = (LAS char*)smem;
    rsrc_t r = __builtin_amdgcn_make_buffer_rsrc(ws, 0, WS_BYTES, 0x00020000);
    const int w = __builtin_amdgcn_readfirstlane(threadIdx.x >> 6);
    const unsigned xcc = xcc_id();
    if (threadIdx.x == 0) {
        const unsigned nearg = ld4(r, 0, OFF_FLAGS + FL_NEAR) & 1u;
        const unsigned pos = ((((xcc >> 2) ^ nearg) & 1u) << 2) | (xcc & 3u);
        unsigned role = pos * 32 + ((blockIdx.x >> 3) & 31);
        unsigned* claim = (unsigned*)(ws + OFF_FLAGS + FL_CLAIM);
#pragma unroll 1
        for (int n = 0; n < 256; ++n, role = (role + 1) & 255)
            if (__hip_atomic_exchange(claim + role, 1u, __ATOMIC_RELAXED, __HIP_MEMORY_SCOPE_AGENT) == 0u) break;
        *(LAS unsigned*)lds = role;
    }
    __syncthreads();
    const int role = __builtin_amdgcn_readfirstlane(*(LAS unsigned*)lds);
    __syncthreads();
    const int pos = role >> 5, idx = role & 31;
    if (pos >= 4 && idx < 24) {
#pragma unroll 1
        for (int j = idx; j < 30; j += 24) prep_layer_b(wqkv, wphi, wo, r, lds, (j >> 1) * 8 + (pos - 4) * 2 + (j & 1), w, threadIdx.x & 63);
        drain();
        bar();
    }
    const unsigned lo = (unsigned)(uintptr_t)ws, hi = (unsigned)((uintptr_t)ws >> 32) & 0xffffu;
    const int bid = pos < 4 ? (pos * 8 + (idx & 7)) * 4 + (idx >> 3) : ((pos - 4) * 8 + (idx & 7)) * 4;
    if (pos < 4 && idx < 8) role_head(lo, hi, pos * 8 + idx, w, xcc & 3, tn, Wq, Wk, 0);
    else if (pos < 4 && idx < 28) role_col(lo, hi, pos * 20 + idx - 8, w, xcc & 3, tn, Wo, Wv, bid);
    else {
        tn.p[0] = (int)plo(out);
        tn.p[1] = (int)phi(out);
        const int kind = pos < 4 ? 4 : idx >> 3, id = pos < 4 ? pos * 4 + idx - 28 : (pos - 4) * 8 + (idx & 7);
        role_helper(lo, hi, id, w, kind, tn, kind == 0 ? Wq : (kind == 1 ? Wk : (kind == 2 ? Wv : Wo)), Wv, bid);
    }
}

extern "C" void kernel_launch(void* const* d_in, const int* in_sizes, int n_in, void* d_out, int out_size, void* d_ws, size_t ws_size, hipStream_t stream) {
    const u16* x = (const u16*)d_in[0];
    const u16* mem0 = (const u16*)d_in[1];
    const u16 *Wq = (const u16*)d_in[2], *Wk = (const u16*)d_in[3], *Wv = (const u16*)d_in[4], *Wo = (const u16*)d_in[5];
    const u16 *wqkv = (const u16*)d_in[8], *wphi = (const u16*)d_in[9], *wo = (const u16*)d_in[10];
    char* ws = (char*)(((uintptr_t)d_ws + 0x1FFFFF) & ~(uintptr_t)0x1FFFFF);
    (void)hipFuncSetAttribute((const void*)mega, hipFuncAttributeMaxDynamicSharedMemorySize, LDS_BYTES);
    Tune tn = {{51,123,30,72,22,80,110,40}};
    prep<<<PB_END, 256, 0, stream>>>(x, mem0, ws);
    mega<<<256, 256, LDS_BYTES, stream>>>(ws, (u16*)d_out, Wq, Wk, Wv, Wo, wqkv, wphi, wo, tn);
}
```
